```python
import torch

def _softmax(x, axis=-1):
    return torch.softmax(x, dim=axis)
def _gelu(x):
    return torch.nn.functional.gelu(x, approximate="tanh")
def _silu(x):
    return torch.nn.functional.silu(x)
def _sigmoid(x):
    return torch.sigmoid(x)
def _layernorm(x, axis=-1, eps=1e-5):
    m = x.mean(dim=axis, keepdim=True)
    v = x.var(dim=axis, keepdim=True, unbiased=False)
    return (x - m) / torch.sqrt(v + eps)
def _scatter_add(tgt, idx, src, axis=0):
    return tgt.clone().index_add_(axis, idx, src)
def _segment_sum(src, seg, n):
    out = torch.zeros((n,) + src.shape[1:], dtype=src.dtype)
    return out.index_add_(0, seg, src)
def _pad(x, pw):
    flat = tuple(v for pair in reversed(pw) for v in pair)
    return torch.nn.functional.pad(x, flat)

kernel_name = 'interleave_layers__softmax_full_mha__moe_arch_mod__prompt'

def setup_inputs():

    _w = (lambda a: (lambda t: t.to(torch.bfloat16) if t.is_floating_point() else t)(torch.as_tensor(a)))
    def _A_setup():
        B, S, D = 1, 2048, 1024
        return {
            "x": _w((lambda *s: torch.randn(*s, dtype=torch.bfloat16) * 0.05)(B, S, D)),
            "wqkv": _w((lambda *s: torch.randn(*s, dtype=torch.bfloat16) * 0.05)(D, 3 * D)),
            "wo": _w((lambda *s: torch.randn(*s, dtype=torch.bfloat16) * 0.05)(D, D)),
        }
    _A_in = _A_setup()

    def _B_setup():
        B, S, D, F, C = 1, 2048, 1024, 3584, 1
        return {
            "x": _w((lambda *s: torch.randn(*s, dtype=torch.bfloat16) * 0.05)(B, S, D)),
            "w_r": _w((lambda *s: torch.randn(*s, dtype=torch.bfloat16) * 0.05)(D)),
            "w_gate": _w((lambda *s: torch.randn(*s, dtype=torch.bfloat16) * 0.05)(D, F)),
            "w_up": _w((lambda *s: torch.randn(*s, dtype=torch.bfloat16) * 0.05)(D, F)),
            "w_down": _w((lambda *s: torch.randn(*s, dtype=torch.bfloat16) * 0.05)(F, D)),
        }
    _B_in = _B_setup()

    return {
        "A_x": _A_in["x"],
        "A_wqkv": _A_in["wqkv"],
        "A_wo": _A_in["wo"],
        "B_x": _B_in["x"],
        "B_w_r": _B_in["w_r"],
        "B_w_gate": _B_in["w_gate"],
        "B_w_up": _B_in["w_up"],
        "B_w_down": _B_in["w_down"],
    }

def reference(A_x, A_wqkv, A_wo, B_x, B_w_r, B_w_gate, B_w_up, B_w_down):
    def _A_ref(x, wqkv, wo):
        B, S, D = x.shape
        H = 32; Dh = D // H
        qkv = (lambda x, *d: x.permute(*d))((x @ wqkv).reshape(B, S, 3, H, Dh), 2, 0, 3, 1, 4)
        q, k, v = qkv[0], qkv[1], qkv[2]
        scale = Dh ** -0.5
        scores = torch.einsum("bhqd,bhkd->bhqk", q, k) * scale
        p = _softmax(scores, axis=-1)
        o = torch.einsum("bhqk,bhkd->bhqd", p, v)
        return (lambda x, *d: x.permute(*d))(o, 0, 2, 1, 3).reshape(B, S, D) @ wo

    def _B_ref(x, w_r, w_gate, w_up, w_down):
        B, S, D = x.shape
        C = 1
        r = x @ w_r
        idx = torch.argsort(-r, axis=-1)[:, :C]
        idx_e = (lambda x, n, axis: torch.repeat_interleave(x, n, dim=axis))(idx.reshape(B, C, 1), D, axis=2)
        x_sel = (lambda x, i, axis: torch.take_along_dim(x, i, dim=axis))(x, idx_e, axis=1)
        g = _silu(x_sel @ w_gate)
        u = x_sel @ w_up
        y = (g * u) @ w_down
        r_sel = _sigmoid((lambda x, i, axis: torch.take_along_dim(x, i, dim=axis))(r, idx, axis=1))
        y = y * r_sel.reshape(B, C, 1)
        b_off = (torch.arange(B) * S).reshape(B, 1)
        flat_idx = (idx + b_off).reshape(B * C)
        zero = (x * 0.0).reshape(B * S, D)
        upd = _scatter_add(zero, flat_idx, y.reshape(B * C, D), axis=0)
        return x + upd.reshape(B, S, D)

    x = A_x
    x = x + _A_ref(_layernorm(x, axis=-1), A_wqkv, A_wo)
    x = x + _B_ref(_layernorm(x, axis=-1), B_w_r, B_w_gate, B_w_up, B_w_down)
    x = x + _A_ref(_layernorm(x, axis=-1), A_wqkv, A_wo)
    x = x + _B_ref(_layernorm(x, axis=-1), B_w_r, B_w_gate, B_w_up, B_w_down)
    return x
```

```cpp
#include <hip/hip_runtime.h>
#include <hip/hip_bf16.h>
#include <stdint.h>
#include <type_traits>
#include <utility>

typedef unsigned short u16;
typedef short s16x4 __attribute__((ext_vector_type(4)));
typedef short s16x8 __attribute__((ext_vector_type(8)));
typedef float f32x16 __attribute__((ext_vector_type(16)));
typedef float f32x4 __attribute__((ext_vector_type(4)));
typedef unsigned int u32x4 __attribute__((ext_vector_type(4)));
typedef unsigned int u32x2 __attribute__((ext_vector_type(2)));
typedef __bf16 bf16x8 __attribute__((ext_vector_type(8)));
typedef __bf16 bf16x16 __attribute__((ext_vector_type(16)));

__device__ __forceinline__ int fresh_lane() {
  int l;
  asm volatile("v_mbcnt_lo_u32_b32 %0, -1, 0\n\tv_mbcnt_hi_u32_b32 %0, -1, %0" : "=v"(l));
  return l;
}
#define STAMP(k, i)
#define STAMPV(k, i, v)
#define CT0()
#define CT1(x)
#define LOGDEF
#define LOG()
#define LOGAT(k)
#define lk_set(k)
#define LOGOUT(slot)
#define TE(grp, k)
#define TEARG
#define TEPASS
constexpr int S_ = 2048, D_ = 1024, H_ = 32, DH_ = 32, F_ = 3584;

__device__ __forceinline__ int kpos(int r) { return (r & ~12) | ((r & 4) << 1) | ((r & 8) >> 1); }
__device__ __forceinline__ float bf2f(u16 v) { return __uint_as_float(((unsigned)v) << 16); }
__device__ __forceinline__ u16 f2bf(float f) {
  __hip_bfloat16 b = __float2bfloat16(f);
  return *reinterpret_cast<u16*>(&b);
}
typedef float f32x2 __attribute__((ext_vector_type(2)));
typedef __bf16 bf16x2 __attribute__((ext_vector_type(2)));
__device__ __forceinline__ unsigned pack2(float lo, float hi) {
  f32x2 v = {lo, hi};
  return __builtin_bit_cast(unsigned, __builtin_convertvector(v, bf16x2));
}
__device__ __forceinline__ float rbf(float f) { return __uint_as_float(pack2(f, f) << 16); }
__device__ __forceinline__ void rbf2(float& a, float& b) {
  const unsigned p = pack2(a, b);
  a = __uint_as_float(p << 16); b = __uint_as_float(p & 0xffff0000u);
}

template <int CTRL, int RMASK>
__device__ __forceinline__ float dpp_add(float v) {
  const int t = __builtin_amdgcn_update_dpp(0, __float_as_int(v), CTRL, RMASK, 0xf, false);
  return v + __int_as_float(t);
}
__device__ __forceinline__ float sum_hi3(float v) {
  v += __int_as_float(__builtin_amdgcn_update_dpp(0, __float_as_int(v), 0x128, 0xf, 0xf, false));
  const auto a = __builtin_amdgcn_permlane16_swap(__float_as_uint(v), __float_as_uint(v), false, false);
  v = __uint_as_float(a[0]) + __uint_as_float(a[1]);
  const auto b = __builtin_amdgcn_permlane32_swap(__float_as_uint(v), __float_as_uint(v), false, false);
  return __uint_as_float(b[0]) + __uint_as_float(b[1]);
}
__device__ __forceinline__ float wave_sum(float v) {
  v = dpp_add<0x111, 0xf>(v);
  v = dpp_add<0x112, 0xf>(v);
  v = dpp_add<0x114, 0xf>(v);
  v = dpp_add<0x118, 0xf>(v);
  v = dpp_add<0x142, 0xa>(v);
  v = dpp_add<0x143, 0xc>(v);
  return __int_as_float(__builtin_amdgcn_readlane(__float_as_int(v), 63));
}

template <int CTRL, int RMASK>
__device__ __forceinline__ unsigned dpp_umax(unsigned v) {
  const unsigned t = (unsigned)__builtin_amdgcn_update_dpp(0, (int)v, CTRL, RMASK, 0xf, false);
  return v > t ? v : t;
}
__device__ __forceinline__ unsigned wave_umax(unsigned v) {
  v = dpp_umax<0x111, 0xf>(v); v = dpp_umax<0x112, 0xf>(v); v = dpp_umax<0x114, 0xf>(v); v = dpp_umax<0x118, 0xf>(v);
  v = dpp_umax<0x142, 0xa>(v); v = dpp_umax<0x143, 0xc>(v);
  return (unsigned)__builtin_amdgcn_readlane((int)v, 63);
}

#define NREP 32
constexpr unsigned SENT = 0xFFFFFFFFu;
constexpr int GRAN_N = 256 * NREP;
constexpr int Y_N = F_ / 16 * D_;
constexpr int X2_N = D_ / 2;
constexpr int XS_N = 128;
constexpr int P4_N = 3 * D_ * 16;
constexpr int PA_N = H_ * 8 * 64;
constexpr int HAND_N = GRAN_N + Y_N + X2_N + XS_N + P4_N + PA_N;
constexpr int MD_O = 16 * 16 * 128 * 4;
constexpr int PK_O = MD_O;
constexpr int PP_O = PK_O + 128 * NREP;
constexpr int Y3_O = PP_O + H_ * S_;
constexpr int PS_N = Y3_O + Y_N;
constexpr int GR_ROW = 64, GR_COL = GR_ROW + 4 * S_, GR_KV = GR_COL + 2 * 16 * 16 * 192;
#define RO 16
#define RK 16
#define RX 1
constexpr int GR_O = GR_KV + 2 * 16 * 32 * RK, GR_X = GR_O + 2 * 16 * 64 * RO, GR_N = GR_X + 2 * 16 * 32 * RX;
constexpr int GR_F = GR_N + 256 * 64;
constexpr int STAT_N = 3 * S_;

__device__ __forceinline__ void st_ag(unsigned* p, unsigned v) { __hip_atomic_store(p, v, __ATOMIC_RELAXED, __HIP_MEMORY_SCOPE_AGENT); }
__device__ __forceinline__ unsigned ld_ag(const unsigned* p) { return __hip_atomic_load(p, __ATOMIC_RELAXED, __HIP_MEMORY_SCOPE_AGENT); }
__device__ __forceinline__ double ld_ag(const double* p) { return __hip_atomic_load(p, __ATOMIC_RELAXED, __HIP_MEMORY_SCOPE_AGENT); }
__device__ __forceinline__ unsigned f2word(float f) { const unsigned u = __float_as_uint(f); return u < 0xFFFFFFFEu ? u : 0xFFFFFFFEu; }
constexpr int SPIN_MAX = 1 << 18;
__device__ __forceinline__ unsigned umax(unsigned a, unsigned b) { return a > b ? a : b; }
__device__ __forceinline__ bool none(bool p) { return __builtin_amdgcn_ballot_w64(p) == 0ull; }

__device__ __forceinline__ void ln_from_sums(double s1, double s2, float& mean, float& var, float& mb, float& den) {
  const double md = s1 * (1.0 / 1024.0);
  const double vd = fma(-md, md, s2 * (1.0 / 1024.0));
  mean = (float)md; var = fmaxf((float)vd, 0.f);
  mb = rbf(mean);
  den = rbf(__builtin_amdgcn_sqrtf(rbf(rbf(var) + 1e-5f)));
}
__device__ __forceinline__ float frcp(float x) { return __builtin_amdgcn_rcpf(x); }
__device__ __forceinline__ void ln_apply2(float& a, float& b, float mb, float den, float rin) {
  a -= mb; b -= mb;
  rbf2(a, b);
  float qa = a * rin, qb = b * rin;
  qa = fmaf(fmaf(-qa, den, a), rin, qa);
  qb = fmaf(fmaf(-qb, den, b), rin, qb);
  rbf2(qa, qb);
  a = qa; b = qb;
}

#define CODE_ADDR(SYM, out64)                                                                                          \
  do {                                                                                                                 \
    unsigned lo_, hi_;                                                                                                 \
    asm volatile("s_getpc_b64 s[90:91]\n\ts_add_u32 s90, s90, " SYM "@rel32@lo+4\n\ts_addc_u32 s91, s91, " SYM          \
                 "@rel32@hi+12\n\ts_mov_b32 %0, s90\n\ts_mov_b32 %1, s91"                                             \
                 : "=s"(lo_), "=s"(hi_) :: "s90", "s91", "scc");                                                        \
    out64 = ((unsigned long long)hi_ << 32) | lo_;                                                                     \
  } while (0)

#define QNX 4
constexpr int QMX = 32 / QNX, QNG = 16 / QNX, QMG = 8 / QNG;

__device__ __forceinline__ unsigned touch(const void* p) { return *reinterpret_cast<const unsigned*>(p); }

__device__ __forceinline__ unsigned touch_code(unsigned long long lo, unsigned long long hi, int pc, int lane) {
  const unsigned long long p = lo + (unsigned)(pc * 8192 + lane * 128);
  return p + 4 <= hi ? *reinterpret_cast<__attribute__((address_space(1))) const unsigned*>(p) : 0u;
}

template <bool SC = false>
__device__ __forceinline__ void glds16s(const void* sbase, unsigned voff, unsigned lds_dst) {
  unsigned keep;
  if (SC)
    asm volatile("s_mov_b32 %0, m0\n\ts_mov_b32 m0, %3\n\ts_nop 2\n\tglobal_load_lds_dwordx4 %1, %2 sc1\n\ts_mov_b32 m0, %0"
                 : "=&s"(keep) : "v"(voff), "s"(sbase), "s"(lds_dst) : "memory");
  else
  asm volatile("s_mov_b32 %0, m0\n\ts_mov_b32 m0, %3\n\ts_nop 2\n\tglobal_load_lds_dwordx4 %1, %2\n\ts_mov_b32 m0, %0"
               : "=&s"(keep) : "v"(voff), "s"(sbase), "s"(lds_dst) : "memory");
}
template <bool SC>
__device__ __forceinline__ void glds16x4(const void* sbase, unsigned v0, unsigned v1, unsigned v2, unsigned v3, unsigned lds_dst) {
  unsigned keep;
  if (SC)
    asm volatile("s_mov_b32 %0, m0\n\ts_mov_b32 m0, %6\n\ts_nop 2\n\tglobal_load_lds_dwordx4 %1, %5 sc1\n\tglobal_load_lds_dwordx4 %2, %5 offset:1024 sc1\n\t"
                 "global_load_lds_dwordx4 %3, %5 offset:2048 sc1\n\tglobal_load_lds_dwordx4 %4, %5 offset:3072 sc1\n\ts_mov_b32 m0, %0"
                 : "=&s"(keep) : "v"(v0), "v"(v1), "v"(v2), "v"(v3), "s"(sbase), "s"(__builtin_amdgcn_readfirstlane(lds_dst)) : "memory");
  else
    asm volatile("s_mov_b32 %0, m0\n\ts_mov_b32 m0, %6\n\ts_nop 2\n\tglobal_load_lds_dwordx4 %1, %5\n\tglobal_load_lds_dwordx4 %2, %5 offset:1024\n\t"
                 "global_load_lds_dwordx4 %3, %5 offset:2048\n\tglobal_load_lds_dwordx4 %4, %5 offset:3072\n\ts_mov_b32 m0, %0"
                 : "=&s"(keep) : "v"(v0), "v"(v1), "v"(v2), "v"(v3), "s"(sbase), "s"(__builtin_amdgcn_readfirstlane(lds_dst)) : "memory");
}
constexpr float QSCALE = 0.17677669529663687f * 1.4426950408889634f;

__device__ __forceinline__ unsigned min4(u32x4 v) { const unsigned a = v[0] < v[1] ? v[0] : v[1], b = v[2] < v[3] ? v[2] : v[3]; return a < b ? a : b; }
#define LDS_AT(T, a) ((__attribute__((address_space(3))) T*)(uintptr_t)(a))
__device__ __forceinline__ u32x4 lds_rd4(unsigned a) { return *LDS_AT(const volatile u32x4, a); }
__device__ __forceinline__ void lds_wr(unsigned a, unsigned v) { *LDS_AT(volatile unsigned, a) = v; }
__device__ __forceinline__ void split3(float v, unsigned& p0, unsigned& p1, unsigned& p2) {
  p0 = pack2(v, v) & 0xffffu; v -= __uint_as_float(p0 << 16);
  p1 = pack2(v, v) & 0xffffu; v -= __uint_as_float(p1 << 16);
  p2 = pack2(v, v) & 0xffffu;
}

#define PIN() __builtin_amdgcn_sched_barrier(0)
template <int B, class F, int... I>
__device__ __forceinline__ void static_for_(F&& f, std::integer_sequence<int, I...>) { (f(std::integral_constant<int, B + I>{}), ...); }
template <int B, int E, class F>
__device__ __forceinline__ void static_for(F&& f) { static_for_<B>(f, std::make_integer_sequence<int, E - B>{}); }

#define FCW 0
#define FCPRE 50
#define FCSLP 2
#define SIGAT 24
#define XPRIO 1
#define OPD 6
#define LAUX 16
#define CSLP 2
#define EPRIO 3
#define OPRIO 1
#define XSLP 1
#define ROT 0
#define CSL1 8
#define XAUX 16
__device__ __forceinline__ void oproj_tail(const bool RES2, const int tid, const unsigned lbase, const unsigned flg, const unsigned scr, const unsigned fcm, const int tmi, const int tni, const int idx,
                                           unsigned* osig, unsigned* xsig, const unsigned tag,
                                           const u16* A, u16* out,
                                           const u16* res, const u16* __restrict__ w_r, double* stats,
                                           const double* stats_in, unsigned* hand, u32x4 (&xres)[2] TEARG) {
  constexpr int ldw = D_, PD = OPD;
  const int lane = tid & 63, wid = __builtin_amdgcn_readfirstlane(tid >> 6);
  const int m0 = tmi * 128, n0 = tni * 64;
  const int dk = RES2 ? 7 : 3;
  if (wid == 0) STAMP(dk, 0);
  TE(1, 3);
  asm volatile("" : "+s"(A) :: "memory");
  PIN();
  const int row = m0 + (wid & 3) * 32 + (wid >> 2) * 16 + (lane >> 3), col = n0 + (lane & 7) * 8;
  const unsigned tb = lbase + 147456;
  typedef __attribute__((address_space(1))) double* gdp;
  typedef __attribute__((address_space(1))) unsigned* gup;
  unsigned tr, oad;
  gdp pst;
  gup pps;
  int xo;
  auto setup = [&]() __attribute__((always_inline)) {
    tr = tb + (row - m0) * 128 + (((lane ^ (lane >> 3)) & 7) << 4);
    pst = (gdp)(stats + ((lane & 3) == 3 ? 0 : (lane & 3)) * S_ + row + 8 * ((lane >> 2) & 1));
    pps = (gup)(hand + 2 * HAND_N + ((tmi * 16 + tni) * 128 + row - m0 + 8 * ((lane >> 2) & 1)) * 4 + (lane & 3));
    xo = (row * ldw + col) * 2;
    asm volatile("" : "+v"(tr), "+v"(pst), "+v"(pps), "+v"(xo));
    PIN();
  };
  {
    oad = lane == 0 ? flg + 24 : flg + 64 + lane * 4;
    gup ps = (gup)(osig + lane * 64);
    asm volatile("" : "+v"(oad), "+v"(ps));
    asm volatile("s_waitcnt vmcnt(1)" ::: "memory");
    TE(2, 1);
    const unsigned old = __hip_atomic_fetch_add(LDS_AT(unsigned, oad), 1u, __ATOMIC_RELAXED, __HIP_MEMORY_SCOPE_WORKGROUP);
    if (__builtin_amdgcn_readfirstlane(old) == 7u && lane < RO) __hip_atomic_store(ps, tag, __ATOMIC_RELAXED, __HIP_MEMORY_SCOPE_AGENT);
    PIN();
    TE(2, 2);
    if (wid == 0) STAMP(dk, 12);
  }
  auto announce = [&]() __attribute__((always_inline)) {
    asm volatile("" ::: "memory");
    PIN();
    __builtin_amdgcn_s_barrier();
    PIN();
    if (wid == 0) STAMP(dk, 1);
    TE(2, 3); TE(4, 0); TE(7, 1);
  };
  float rf[2][8];
  auto unpack = [&](const u32x4& r0, const u32x4& r1) __attribute__((always_inline)) {
#pragma unroll
    for (int e = 0; e < 4; ++e) {
      rf[0][2 * e] = __uint_as_float(r0[e] << 16); rf[0][2 * e + 1] = __uint_as_float(r0[e] & 0xffff0000u);
      rf[1][2 * e] = __uint_as_float(r1[e] << 16); rf[1][2 * e + 1] = __uint_as_float(r1[e] & 0xffff0000u);
    }
  };
  u32x4 rwr, yv[2];
  if (wid < 4) {
    const __amdgpu_buffer_rsrc_t rsa = __builtin_amdgcn_make_buffer_rsrc(const_cast<u16*>(A), 0, S_ * D_ * 2, 0x00020000);
    int sa0 = (tmi * 4 + wid) * 65536, va0 = (lane >> 4) * 512 + (lane & 15) * 16;
    asm volatile("" : "+v"(va0));
    asm volatile("" : "+s"(sa0));
    const int rot = ROT ? tni & 7 : 0;
    auto lda = [&](int i, int h) { return __builtin_amdgcn_raw_buffer_load_b128(rsa, va0 + (i & 1) * 2048 + h * 256, sa0 + (((i >> 2) + rot) & 7) * 8192 + (i & 2) * 2048, LAUX); };
    unsigned wad[4], tw[4];
    f32x4 acc[2][4];
    const int k = (lane >> 5) * 16 + ((lane >> 2) & 7);
#pragma unroll
    for (int j = 0; j < 4; ++j) {
      wad[j] = lbase + k * 128 + (((2 * j) ^ (k & 6)) << 4) + 8 * (lane & 3) + rot * 16384;
      acc[0][j] = acc[1][j] = f32x4{0.f, 0.f, 0.f, 0.f};
      tw[j] = tb + wid * 4096 + (lane & 15) * 128 + (((2 * j + (lane >> 5)) ^ (lane & 7)) << 4) + ((lane >> 4) & 1) * 8;
      asm volatile("" : "+v"(wad[j]), "+v"(tw[j]), "+v"(acc[0][j]), "+v"(acc[1][j]));
    }
    auto ldw_ = [&](int i, int j) {
      const unsigned a = wad[j] + (i & 3) * 4096;
      s16x4 lo = __builtin_amdgcn_ds_read_tr16_b64_v4i16(LDS_AT(s16x4, a));
      s16x4 h4 = __builtin_amdgcn_ds_read_tr16_b64_v4i16(LDS_AT(s16x4, a + 1024));
      return (s16x8)__builtin_shufflevector(lo, h4, 0, 1, 2, 3, 4, 5, 6, 7);
    };
    setup();
    announce();
    u32x4 af[PD][2];
    s16x8 wf[2][4];
#pragma unroll
    for (int i = 0; i < PD; ++i) { af[i][0] = lda(i, 0); af[i][1] = lda(i, 1); }
    PIN();
    __builtin_amdgcn_s_setprio(OPRIO);
#pragma unroll 1
    for (int spin = 0; spin < SPIN_MAX; ++spin)
      if (__builtin_amdgcn_readfirstlane(*LDS_AT(const volatile unsigned, flg)) >= 40u) break;
    asm volatile("" : "+v"(va0) :: "memory");
    PIN();
#pragma unroll
    for (int j = 0; j < 4; ++j) wf[0][j] = ldw_(0, j);
#pragma unroll
    for (int i = 0; i < 32; ++i) {
      if ((i & 3) == 3 && i < 31) {
        const unsigned dw = ((i >> 2) + rot) == 7 ? -7u * 16384u : 16384u;
#pragma unroll
        for (int j = 0; j < 4; ++j) { wad[j] += dw; asm volatile("" : "+v"(wad[j])); }
      }
      if (i == 32 - PD) { rwr = *reinterpret_cast<const u32x4*>(w_r + col); PIN(); }
      if (i == SIGAT) { __hip_atomic_fetch_add(LDS_AT(unsigned, oad + 4), 1u, __ATOMIC_RELAXED, __HIP_MEMORY_SCOPE_WORKGROUP); PIN(); }
#pragma unroll
      for (int j = 0; j < 4; ++j) {
        if (i + 1 < 32) wf[(i + 1) & 1][j] = ldw_(i + 1, j);
        PIN();
#pragma unroll
        for (int h = 0; h < 2; ++h) {
          asm volatile("v_mfma_f32_16x16x32_bf16 %0, %1, %2, %0" : "+v"(acc[h][j]) : "v"(wf[i & 1][j]), "v"(af[i % PD][h]));
          PIN();
          if (j == 3 && i + PD < 32) { af[i % PD][h] = lda(i + PD, h); PIN(); }
        }
      }
    }
    asm volatile("s_nop 3" : "+v"(acc[0][0]), "+v"(acc[1][0]));
    TE(4, 1); TE(7, 2);
    if (wid == 0) STAMP(dk, 17);
    if (wid == 1 && !RES2) STAMP(4, 0);
#pragma unroll
    for (int j = 0; j < 4; ++j)
#pragma unroll
      for (int h = 0; h < 2; ++h) {
        *LDS_AT(u32x2, tw[j] + h * 2048) = u32x2{pack2(acc[h][j][0], acc[h][j][1]), pack2(acc[h][j][2], acc[h][j][3])};
        PIN();
      }
    __builtin_amdgcn_s_setprio(XPRIO);
    asm volatile("s_waitcnt lgkmcnt(0)" ::: "memory");
    PIN();
    __builtin_amdgcn_s_barrier();
    asm volatile("" ::: "memory");
    PIN();
    TE(4, 2); TE(5, 0);
    const u32x4 r0 = lds_rd4(scr + wid * 2048 + lane * 16), r1 = lds_rd4(scr + wid * 2048 + 1024 + lane * 16);
#pragma unroll
    for (int p = 0; p < 2; ++p) yv[p] = lds_rd4(tr + p * 1024);
    unpack(r0, r1);
  } else {
    u32x4 rres[4];
    double is1[4], is2[4];
#pragma unroll
    for (int p = 0; p < 4; ++p)
      rres[p] = __builtin_amdgcn_raw_buffer_load_b128(__builtin_amdgcn_make_buffer_rsrc(const_cast<u16*>(res), 0, S_ * D_ * 2, 0x00020000), ((row - 16 + 8 * p) * ldw + col) * 2, 0, 16);
    rwr = *reinterpret_cast<const u32x4*>(w_r + col);
#pragma unroll
    for (int p = 0; p < 4; ++p) { is1[p] = ld_ag(stats_in + row - 16 + 8 * p); is2[p] = ld_ag(stats_in + S_ + row - 16 + 8 * p); }
    PIN();
    setup();
    announce();
    if (RES2) {
      const __amdgpu_buffer_rsrc_t rsh = __builtin_amdgcn_make_buffer_rsrc(hand, 0, HAND_N * 4, 0x00020000);
      const int dr = idx - (row - 16);
      const int qo = (dr & ~24) == 0 ? (GRAN_N + Y_N) * 4 + col * 2 : HAND_N * 4;
      const u32x4 qq = __builtin_amdgcn_raw_buffer_load_b128(rsh, qo, 0, 16);
      PIN();
#pragma unroll
      for (int p = 0; p < 4; ++p) {
        float mean, var, mb, den;
        ln_from_sums(is1[p], is2[p], mean, var, mb, den);
        const float rin = frcp(den);
#pragma unroll
        for (int e = 0; e < 4; ++e) {
          const float xl = __uint_as_float(rres[p][e] << 16), xh = __uint_as_float(rres[p][e] & 0xffff0000u);
          float hl = xl, hh = xh;
          ln_apply2(hl, hh, mb, den, rin);
          rres[p][e] = pack2(xl + hl, xh + hh);
        }
        asm volatile("" : "+v"(rres[p]));
      }
      PIN();
#pragma unroll
      for (int p = 0; p < 4; ++p)
#pragma unroll
        for (int e = 0; e < 4; ++e) rres[p][e] = dr == 8 * p ? qq[e] : rres[p][e];
    }
    *LDS_AT(u32x4, scr + (wid - 4) * 2048 + lane * 16) = rres[0];
    *LDS_AT(u32x4, scr + (wid - 4) * 2048 + 1024 + lane * 16) = rres[1];
    unpack(rres[2], rres[3]);
#pragma unroll
    for (int e = 0; e < 8; ++e) asm volatile("" : "+v"(rf[0][e]), "+v"(rf[1][e]));
    TE(4, 1); TE(7, 2);
    asm volatile("" ::: "memory");
    __builtin_amdgcn_s_waitcnt(0x0070);
    __builtin_amdgcn_s_setprio(XPRIO);
    PIN();
    __builtin_amdgcn_s_barrier();
    asm volatile("" ::: "memory");
    PIN();
    TE(4, 2); TE(5, 0);
#pragma unroll
    for (int p = 0; p < 2; ++p) yv[p] = lds_rd4(tr + p * 1024);
  }
  {
    const __amdgpu_buffer_rsrc_t rso = __builtin_amdgcn_make_buffer_rsrc(out, 0, S_ * D_ * 2, 0x00020000);
    unsigned fm = 0u;
    auto xst = [&](int p) __attribute__((always_inline)) {
      const unsigned r = row + 8 * p;
      __builtin_amdgcn_raw_buffer_store_b128(xres[p], rso, !RES2 || r == (fm & 0xffffu) || r == (fm >> 16) ? xo + p * (8 * ldw * 2) : S_ * D_ * 2, 0, XAUX);
      PIN();
    };
#pragma unroll
    for (int p = 0; p < 2; ++p) {
      if (p == 0) { fm = *LDS_AT(const volatile unsigned, fcm); PIN(); }
#pragma unroll
      for (int e = 0; e < 4; ++e)
        xres[p][e] = pack2(__uint_as_float(yv[p][e] << 16) + rf[p][2 * e], __uint_as_float(yv[p][e] & 0xffff0000u) + rf[p][2 * e + 1]);
      xst(p);
    }
    __builtin_amdgcn_s_setprio(0);
    PIN();
    TE(4, 3); TE(7, 3);
    float f[2][3];
#pragma unroll
    for (int p = 0; p < 2; ++p) {
      f[p][0] = f[p][1] = f[p][2] = 0.f;
#pragma unroll
      for (int e = 0; e < 4; ++e) {
        const unsigned xu = xres[p][e], wu = rwr[e];
        const bf16x2 x = __builtin_bit_cast(bf16x2, xu);
        f[p][0] = __builtin_amdgcn_fdot2_f32_bf16(x, __builtin_bit_cast(bf16x2, 0x3f803f80u), f[p][0], false);
        f[p][1] = __builtin_amdgcn_fdot2_f32_bf16(x, x, f[p][1], false);
        f[p][2] = __builtin_amdgcn_fdot2_f32_bf16(x, __builtin_bit_cast(bf16x2, wu), f[p][2], false);
      }
    }
    auto addx = [&](float keep, float give, auto ctl) {
      return keep + __int_as_float(__builtin_amdgcn_update_dpp(0, __float_as_int(give), decltype(ctl)::value, 0xf, 0xf, false));
    };
    const bool b2 = lane & 4, b1 = lane & 2, z0 = (lane & 3) == 0, z1 = (lane & 3) == 1;
    float g[3];
#pragma unroll
    for (int c = 0; c < 3; ++c) g[c] = addx(b2 ? f[1][c] : f[0][c], b2 ? f[0][c] : f[1][c], std::integral_constant<int, 0x141>{});
    const float u1 = addx(b1 ? g[2] : g[0], b1 ? g[0] : g[2], std::integral_constant<int, 0x4e>{});
    const float u2 = addx(g[1], g[1], std::integral_constant<int, 0x4e>{});
    const float sv = addx(z1 ? u2 : u1, z0 ? u2 : u1, std::integral_constant<int, 0xb1>{});
    if (wid == 0) STAMP(dk, 14);
    if (RES2) {
      if ((lane & 3) != 3) __hip_atomic_store(pps, f2word(sv), __ATOMIC_RELAXED, __HIP_MEMORY_SCOPE_AGENT);
      PIN();
      if (__builtin_expect(__builtin_amdgcn_readfirstlane(fm) == SENT, 0)) {
#pragma unroll 1
        for (int i = 0; i < 4 * SPIN_MAX && __builtin_amdgcn_readfirstlane(fm) == SENT; ++i) fm = *LDS_AT(const volatile unsigned, fcm);
        xst(0); xst(1);
      }
      __hip_atomic_fetch_add(LDS_AT(unsigned, oad), 1u, __ATOMIC_RELAXED, __HIP_MEMORY_SCOPE_WORKGROUP);
      TE(5, 1); TE(5, 2);
    } else {
      if ((lane & 3) != 3) __builtin_amdgcn_global_atomic_fadd_f64(pst, (double)sv);
      PIN();
      TE(5, 1);
      asm volatile("s_waitcnt vmcnt(1)" ::: "memory");
      TE(5, 2);
      const unsigned old = __hip_atomic_fetch_add(LDS_AT(unsigned, oad), 1u, __ATOMIC_RELAXED, __HIP_MEMORY_SCOPE_WORKGROUP);
      if (__builtin_amdgcn_readfirstlane(old) == 15u && lane < RX) st_ag(xsig + lane * 32, tag);
      if (__builtin_amdgcn_readfirstlane(old) == 15u) STAMP(4, 1);
    }
    PIN();
  }
}

#define MFMA32(a, b, c) __builtin_amdgcn_mfma_f32_32x32x16_bf16(a, b, c, 0, 0, 0)
#define MFMA16(a, b, c) __builtin_amdgcn_mfma_f32_16x16x32_bf16(a, b, c, 0, 0, 0)

#define PLO(x) __builtin_bit_cast(bf16x16, __builtin_shufflevector(x, x, 0, 1, 2, 3, 4, 5, 6, 7))
#define PHI(x) __builtin_bit_cast(bf16x16, __builtin_shufflevector(x, x, 8, 9, 10, 11, 12, 13, 14, 15))
#define SMF32(a, b, c) __builtin_amdgcn_smfmac_f32_32x32x32_bf16(__builtin_bit_cast(bf16x8, a), b, c, sidx, 0, 0)
#define SMF16(a, b, c) __builtin_amdgcn_smfmac_f32_16x16x64_bf16(__builtin_bit_cast(bf16x8, a), b, c, sidx, 0, 0)

__device__ __forceinline__ void patch_kv(unsigned tab, int r, s16x8& k0, s16x8& k1, s16x8& v0, s16x8& v1, bool on = true) {
  const int lane = fresh_lane(), hi = lane >> 5;
  if (on && (lane & 31) == kpos(r)) {
    k0 = *LDS_AT(const s16x8, tab + hi * 16); k1 = *LDS_AT(const s16x8, tab + 32 + hi * 16);
  }
  const short vv = *LDS_AT(const short, tab + 128 + (lane & 31) * 2);
  const int js = (on && hi == ((r >> 2) & 1)) ? (r >> 4) * 8 + ((r >> 3) & 1) * 4 + (r & 3) : -1;
#pragma unroll
  for (int e = 0; e < 8; ++e) { v0[e] = js == e ? vv : v0[e]; v1[e] = js == 8 + e ? vv : v1[e]; }
}

__device__ __forceinline__ void attn_safe(const u16* KV, int tile0, s16x8 q0, s16x8 q1,
                                       f32x16& o, float& l_out, int ptile, int prow, unsigned ptab) {
  const int lane = fresh_lane();
  float m = -INFINITY, l = 0.f;
  const int sidx = 0xDDDDDDDD;
  const __amdgpu_buffer_rsrc_t rs = __builtin_amdgcn_make_buffer_rsrc(const_cast<u16*>(KV), 0, 2 * S_ * D_ * 2, 0x00020000);
#pragma unroll
  for (int e = 0; e < 16; ++e) o[e] = 0.f;
  for (int tt = 0; tt < 64; ++tt) {
    const int t = tile0 + tt, off = t * 2048 + lane * 16;
    s16x8 k0 = __builtin_bit_cast(s16x8, __builtin_amdgcn_raw_buffer_load_b128(rs, off, 0, 16));
    s16x8 k1 = __builtin_bit_cast(s16x8, __builtin_amdgcn_raw_buffer_load_b128(rs, off + 1024, 0, 16));
    s16x8 v0 = __builtin_bit_cast(s16x8, __builtin_amdgcn_raw_buffer_load_b128(rs, off + S_ * D_ * 2, 0, 16));
    s16x8 v1 = __builtin_bit_cast(s16x8, __builtin_amdgcn_raw_buffer_load_b128(rs, off + S_ * D_ * 2 + 1024, 0, 16));
    if (t == ptile) patch_kv(ptab, prow, k0, k1, v0, v1);
    f32x16 sc;
#pragma unroll
    for (int e = 0; e < 16; ++e) sc[e] = 0.f;
    sc = MFMA32(k0, q0, sc); sc = MFMA32(k1, q1, sc);
    float mt = sc[0];
#pragma unroll
    for (int e = 1; e < 16; ++e) mt = fmaxf(mt, sc[e]);
    mt = fmaxf(mt, __shfl_xor(mt, 32, 64));
    const float mn = fmaxf(m, mt);
    const float alpha = __builtin_amdgcn_exp2f(m - mn);
    m = mn;
#pragma unroll
    for (int e = 0; e < 16; ++e) sc[e] = __builtin_amdgcn_exp2f(sc[e] - mn);
    float ls = 0.f;
#pragma unroll
    for (int e = 0; e < 16; ++e) ls += __uint_as_float(__float_as_uint(sc[e]) & 0xffff0000u);
    l = l * alpha + ls;
#pragma unroll
    for (int e = 0; e < 16; ++e) o[e] *= alpha;
    o = SMF32(v0, PLO(sc), o); o = SMF32(v1, PHI(sc), o);
  }
  l_out = l + __shfl_xor(l, 32, 64);
}

template <int CTRL, int RMASK>
__device__ __forceinline__ float dpp_fmax(float v) {
  return fmaxf(v, __int_as_float(__builtin_amdgcn_update_dpp(__float_as_int(v), __float_as_int(v), CTRL, RMASK, 0xf, false)));
}
__device__ __forceinline__ float wave_fmax(float v) {
  v = dpp_fmax<0x111, 0xf>(v); v = dpp_fmax<0x112, 0xf>(v); v = dpp_fmax<0x114, 0xf>(v); v = dpp_fmax<0x118, 0xf>(v);
  v = dpp_fmax<0x142, 0xa>(v); v = dpp_fmax<0x143, 0xc>(v);
  return __int_as_float(__builtin_amdgcn_readlane(__float_as_int(v), 63));
}
__device__ __forceinline__ float sum_hi5(float v) {
  v = sum_hi3(v);
  v += __int_as_float(__builtin_amdgcn_update_dpp(0, __float_as_int(v), 0x124, 0xf, 0xf, false));
  v += __int_as_float(__builtin_amdgcn_update_dpp(0, __float_as_int(v), 0x122, 0xf, 0xf, false));
  return v;
}

#define PSLP 8
#define WSLP 3
#define PRE 28
#define PRE1 8
#define PRE5 8
#define W3SLP 2
#define FSTART 10
#define BAL1 false
#define FPRE0 20
#define FPRE 16
#define FSLP 4
#define OAUX 16
#define BALO 1
#define OMX 4
#define T0 2u
#define TS 2u
#define TW 10
#define HW 2
#define HP1 1
#define POLLAT 11
#define X2AT 2
#define HPRIO 3
#define SSLP 1
#define SPRE 12
#define QOV 2
#define FLAT 4
#define WGAPS {2, 6, 10, 14, 18, 21, 25, 29}
#define KVS 4
#define CKAT 3
#define VEARLY 2
#define GNS 12
#define GNS2 20
#define PNS 12
#define GEV 4
#define G0 5u
#define GS 2u
#define IAT 1u
#define WDAT 10u
#define W3AT 11u
#define WOAT 12u
#define YSLP 2
#define ISLP 8
#define X3SLP 1
#define RPRE 8
#define RSLP 2
#define KSLP 2
#define UAT 13u
#define UHW 0
#define JDL 4000u
#define PPSLP 2
#define PKPRE 24
#define PKSLP 4
#define XPRE 8
#define RPRE2 8
#define XRSLP 2
#define MPRE 20
#define KEARLY 0
#define REFINE 1
#define FPRE2 24
#define OSLP 5
#define YD1 150
#define YD2 180
#define KDT 200
#define XDT 65
#define R2PRIO 0
#define LNPRIO 1
#define K6 2
#define MPR2 8
#define MSLP 4
#define BARG 1
struct Args {
  long long lay;
  const u16* a; u16* kv; u16* o; u16* xo; double* so;
  const u16* W; u16* wc;
  const u16 *wg, *wu, *wd, *wo, *w_r;
  unsigned* gran; float* csf; double* stats; unsigned* hand; u16* out;
};
typedef __attribute__((address_space(4))) const Args* ArgP;
#define WCAT 3
#define CAP0 1
#define PRE4 50
#define PRE7 20
#define UARR 12u
#define WMAP 0x32100333
#define SMAP 0
#define STCH 6
#define KPOLL 10
#define TPRIO 2
#define PG1 6
#define PG2 12
#define TSLP 9
__device__ __forceinline__ bool slow_cols(const u16* W, int tni) { return ((((unsigned)(uintptr_t)W >> 7) + tni) & 7) == 3; }
#define ARGS_HERE                                                                                                      \
  ArgP ap_ = ap; asm volatile("" : "+s"(ap_));                                                                          \
  const u16* A = ap_->a; const u16* __restrict__ W = ap_->W; u16* KV = ap_->kv; u16* Of = ap_->o;        \
  const u16* __restrict__ wg = ap_->wg; const u16* __restrict__ wu = ap_->wu; const u16* __restrict__ wd = ap_->wd;       \
  const u16* __restrict__ wo = ap_->wo; const u16* __restrict__ w_r = ap_->w_r; unsigned* gran = ap_->gran;              \
  float* csf = ap_->csf; const double* stats = ap_->stats; double* stats_out = ap_->so; unsigned* hand = ap_->hand; \
  u16* xout = ap_->xo; u16* __restrict__ out = ap_->out
#define AGAIN "s_getpc_b64 s[90:91]\n\ts_add_u32 s90, s90, layers_kernel@rel32@lo+4\n\ts_addc_u32 s91, s91, layers_kernel@rel32@hi+12\n\ts_setpc_b64 s[90:91]"
#define KEPT(c)                                                                                                        \
  c "{v[104:107]}"(gu[0]), c "{v[108:111]}"(gu[1]), c "{v[112:115]}"(gu[2]), c "{v[116:119]}"(gu[3]), c "{v[120:123]}"(gu[4]),      \
  c "{v[124:127]}"(gu[5]), c "{v[128:131]}"(gu[6]), c "{v[132:135]}"(gu[7]), c "{v[136:139]}"(gu[8]), c "{v[140:143]}"(gu[9]),      \
  c "{v[144:147]}"(gu[10]), c "{v[148:151]}"(gu[11]), c "{v[152:155]}"(gu[12]), c "{v[156:159]}"(gu[13]), c "{v[160:163]}"(gu[14]), \
  c "{v[164:167]}"(gu[15])
#define LAYER_HERE ArgP apl_ = ap; asm volatile("" : "+s"(apl_)); const bool LAY2 = apl_->lay != 0
__device__ __forceinline__ void layer_body(const int wid, const int bid, const unsigned lbase, const ArgP ap, const unsigned long long t_entry) {
  constexpr int K = 1024, NKT = 16, NST = 3, ABYTES = 16384, WBYTES = 24576, STB = ABYTES + WBYTES, ldw = 3 * D_;
  constexpr int TABA = NST * STB, TABW = TABA + 2048, ALPHA = TABW + 3072, CRED = ALPHA + 512;
  constexpr int ASB = 16384, FLG = 8 * ASB + 256;
  constexpr int DUM = FLG + 64, TAB = DUM + 256, RED = TAB + 512, SACT = RED + 4096, RED2 = SACT + 1024, PL = RED2 + 2048, XML = PL + 512, UGL = XML + 4096, SPM = UGL + 1024, QOFF = 9 * ASB;
  static_assert(SPM + 2048 <= QOFF && CRED <= 8 * ASB && RED + 8192 <= QOFF && RED % 16 == 0, "lds");
#define XPERM 1
  const int xcd = XPERM == 1 ? (bid & 1) * 4 + ((bid >> 1) & 3) : (XPERM == 2 ? (~bid & 1) * 4 + ((bid >> 1) & 3) : bid & 7), loc = bid >> 3;
  const int tmi = (xcd / QNG) * QMX + loc / QNX, tni = (xcd % QNG) * QNX + loc % QNX;
  const int m0 = tmi * 128, n0 = tni * 64;
  const int omi = (xcd / (OMX / 2)) * OMX + loc / (32 / OMX), oni = (xcd % (OMX / 2)) * (32 / OMX) + loc % (32 / OMX);
  constexpr unsigned VOFF = (unsigned)S_ * D_ * 2;
  auto all_ge = [&](unsigned w, unsigned n) { return ((w + (128u - n) * 0x01010101u) & 0x80808080u) == 0x80808080u; };
  if (wid >= 8) {
    u32x4 gu[16];
    LAYER_HERE;
    constexpr int dk = 6;
    const int hw = wid - 8, b = bid;
    int nb = 0;
    constexpr int lim = NKT - 1;
    auto tick_nw = [&]() {
      PIN();
      if (nb < lim) { __builtin_amdgcn_s_barrier(); ++nb; } else __builtin_amdgcn_s_sleep(2);
      asm volatile("" ::: "memory");
      PIN();
    };
    auto tick = [&]() {
      asm volatile("s_waitcnt lgkmcnt(0)" ::: "memory");
      tick_nw();
    };
    unsigned best, tag, rmd;
    float cs, alpha, m, wsum;
    u32x2 xq;
    s16x8 sl[8];
    const int kcap = CAP0 + (tmi & 7);
    if (!LAY2) {
      ARGS_HERE;
      const int lane = fresh_lane(), t = hw * 64 + lane;
      const unsigned ep = ld_ag(gran);
      const int srow = m0 + tni * 8 + hw * 2;
      u32x4 xa[2][2], wv[8];
      const int wr_ = (3 - ((unsigned)(uintptr_t)W >> 7) - (tni & ~3)) & 7;
      const bool cpy = hw == 3 && wr_ < 4 && wr_ != (tni & 3), wld = hw < 3 ? wr_ != (tni & 3) : cpy;
      const int wcn = cpy ? (tni & ~3) + wr_ : tni, wcp = cpy ? (tni - wr_ - 1) & 3 : hw;
      const int wof = ((tmi * 64 + (lane >> 3)) * ldw + wcp * D_ + wcn * 64 + (lane & 7) * 8) * 2;
      auto ld_w = [&]() {
        const __amdgpu_buffer_rsrc_t rw = __builtin_amdgcn_make_buffer_rsrc(const_cast<u16*>(W), 0, wld ? D_ * ldw * 2 : 0, 0x00020000);
#pragma unroll
        for (int r = 0; r < 8; ++r) wv[r] = __builtin_amdgcn_raw_buffer_load_b128(rw, wof, r * 8 * ldw * 2, 0);
        PIN();
      };
      auto wcopy = [&]() __attribute__((always_inline)) {
        if (cpy) {
          const __amdgpu_buffer_rsrc_t rw = __builtin_amdgcn_make_buffer_rsrc(ap_->wc, 0, D_ * ldw * 2, 0x00020000);
#pragma unroll
          for (int r = 0; r < 8; ++r) __builtin_amdgcn_raw_buffer_store_b128(wv[r], rw, wof + r * 8 * ldw * 2, 0, 16);
        }
        PIN();
      };
      const int wat = ((hw == 3 ? SMAP : WMAP) >> (4 * (tmi & 7))) & 3;
      unsigned ps = 0u;
      auto stouch = [&](int at) __attribute__((always_inline)) {
        if (at == STCH && cpy) ps = __builtin_amdgcn_raw_buffer_load_b32(__builtin_amdgcn_make_buffer_rsrc(const_cast<u16*>(W), 0, D_ * ldw * 2, 0x00020000),
                                                                        (((tmi ^ 8) * 64 + lane) * ldw + wcp * D_ + wcn * 64) * 2, 0, 0);
        PIN();
      };
      auto ld_x = [&](int h) {
#pragma unroll
        for (int r = 0; r < 2; ++r) xa[r][h] = *reinterpret_cast<const u32x4*>(A + (size_t)(srow + r) * D_ + (h ^ (tmi >> 3)) * 512 + lane * 8);
        PIN();
      };
      ld_x(0);
      if (X2AT == 0) ld_x(1);
      if (wat == 0) ld_w();
      unsigned pc = 0u;
      if (hw == 3) {
        unsigned long long a0, a1;
        CODE_ADDR("layers_kernel", a0);
        CODE_ADDR("end_marker_kernel", a1);
        pc = touch_code(a0, a1, loc, lane);
      }
      if (hw == 2) st_ag(gran + GR_N + bid * 64 + lane, lane + 1u);
      {
        const int gi = bid * 256 + t;
        if (gi < GRAN_N / 4) __builtin_amdgcn_raw_buffer_store_b128(u32x4{SENT, SENT, SENT, SENT}, __builtin_amdgcn_make_buffer_rsrc(hand, 0, GRAN_N * 4, 0x00020000), gi * 16, 0, 16);
        if (t < 24) __hip_atomic_store(stats_out + (t >> 3) * S_ + m0 + tni * 8 + (t & 7), 0.0, __ATOMIC_RELAXED, __HIP_MEMORY_SCOPE_AGENT);
      }
      tick();
      if (wat == 1) ld_w();
      if (X2AT == 1) ld_x(1);
      tick();
      if (wat == 2) ld_w();
      if (WCAT == 2) wcopy();
      if (X2AT == 2) ld_x(1);
      asm volatile("" :: "v"(ep));
      tick();
      if (wat == 3) ld_w();
      if (WCAT == 3) wcopy();
      if (X2AT == 3) ld_x(1);
      stouch(3);
#pragma unroll
      for (int i = 0; i < HW; ++i) { tick(); stouch(4 + i); }
      __builtin_amdgcn_s_setprio(HP1);
      PIN();
      tag = ep + 1u;
      const __amdgpu_buffer_rsrc_t rsc = __builtin_amdgcn_make_buffer_rsrc(gran, 0, GR_N * 4, 0x00020000);
      {
        u32x4 od;
#pragma unroll
        for (int q = 0; q < 4; ++q) od[q] = ((lane & 7) >> 1) == q ? ((lane & 1) ? 0x3f800000u : 0x00003f80u) : 0u;
        f32x4 d[2] = {{0.f, 0.f, 0.f, 0.f}, {0.f, 0.f, 0.f, 0.f}};
#pragma unroll
        for (int r = 0; r < 8; ++r) d[r & 1] = __builtin_amdgcn_mfma_f32_16x16x32_bf16(__builtin_bit_cast(s16x8, od), __builtin_bit_cast(s16x8, wv[r]), d[r & 1], 0, 0, 0);
        const float c0 = dpp_add<0x128, 0xf>(lane >= 32 ? d[0][2] + d[1][2] : d[0][0] + d[1][0]), c1 = dpp_add<0x128, 0xf>(lane >= 32 ? d[0][3] + d[1][3] : d[0][1] + d[1][1]);
        const int col = wcp * 64 + (lane & 7) * 8 + ((lane >> 4) & 1) * 4 + (lane >> 5) * 2;
        asm volatile("s_waitcnt vmcnt(0)" ::: "memory");
        __builtin_amdgcn_raw_buffer_store_b128(u32x4{__float_as_uint(c0), tag, __float_as_uint(c1), tag}, rsc,
                                               (lane & 8) || !wld ? GR_N * 4 : (GR_COL + ((wcn * 16 + tmi) * 192 + col) * 2) * 4, 0, 16);
      }
      asm volatile("" :: "v"(pc));
      tick_nw();
      stouch(6);
      const int tc = t < 192 ? t : 0;
      {
        float xv[2][16], mean[2];
#pragma unroll
        for (int r = 0; r < 2; ++r) {
          float sm = 0.f;
#pragma unroll
          for (int i = 0; i < 4; ++i) {
            xv[r][2 * i] = __uint_as_float(xa[r][0][i] << 16); xv[r][2 * i + 1] = __uint_as_float(xa[r][0][i] & 0xffff0000u);
            xv[r][8 + 2 * i] = __uint_as_float(xa[r][1][i] << 16); xv[r][8 + 2 * i + 1] = __uint_as_float(xa[r][1][i] & 0xffff0000u);
          }
#pragma unroll
          for (int i = 0; i < 16; ++i) sm += xv[r][i];
          mean[r] = wave_sum(sm) * (1.0f / 1024.0f);
          tick();
          stouch(7 + r);
        }
        float vr[2];
#pragma unroll
        for (int r = 0; r < 2; ++r) {
          float v = 0.f;
#pragma unroll
          for (int i = 0; i < 16; ++i) { const float d = xv[r][i] - mean[r]; v += d * d; }
          vr[r] = wave_sum(v) * (1.0f / 1024.0f);
          if (r == 0) tick();
        }
        const float vv = lane & 2 ? vr[1] : vr[0], mm = lane & 2 ? mean[1] : mean[0];
        const float rin = 1.0f / rbf(sqrtf(rbf(rbf(vv) + 1e-5f)));
        if (lane < 4) __builtin_amdgcn_raw_buffer_store_b64(u32x2{__float_as_uint((lane & 1) ? rbf(mm) : rin), tag}, rsc, (GR_ROW + srow * 4 + lane * 2) * 4, 0, 16);
      }
      tick();
      while (nb < POLLAT) tick();
      asm volatile("" :: "v"(ps));
#pragma unroll 1
      for (int spin = 0;; ++spin) {
        u32x2 csv[16];
        unsigned lo = tag, hi = tag;
        auto use = [&](int i0, int i1) __attribute__((always_inline)) {
#pragma unroll
          for (int i = i0; i < i1; i += 2) {
            lo = min(min(lo, csv[i][1]), csv[i + 1][1]); hi = max(max(hi, csv[i][1]), csv[i + 1][1]);
            cs += __uint_as_float(csv[i][0]); cs += __uint_as_float(csv[i + 1][0]);
          }
          asm volatile("" : "+v"(lo), "+v"(hi), "+v"(cs));
          PIN();
        };
        cs = 0.f;
#pragma unroll
        for (int i = 0; i < 16; ++i) {
          csv[i] = __builtin_amdgcn_raw_buffer_load_b64(rsc, (GR_COL + (tni * 3072 + tc) * 2) * 4 + i * 1536, 0, 16);
          if (i == PG1 - 1 || i == PG2 - 1) tick();
        }
        const u32x4 rp = __builtin_amdgcn_raw_buffer_load_b128(rsc, (GR_ROW + (m0 + (t & 127)) * 4) * 4, 0, 16);
        PIN();
        use(0, PG1);
        tick();
        use(PG1, PG2);
        tick();
        use(PG2, 16);
        const unsigned bad = (min(min(lo, rp[1]), rp[3]) ^ tag) | (max(max(hi, rp[1]), rp[3]) ^ tag);
        alpha = __uint_as_float(rp[0]); m = __uint_as_float(rp[2]);
        if (none(bad != 0u) || spin >= SPIN_MAX) break;
      }
      if (tmi == 0 && t < 192) st_ag(reinterpret_cast<unsigned*>(csf) + (t >> 6) * 1024 + n0 + (t & 63), __float_as_uint(cs));
      best = rmd = 0u;
      wsum = 0.f;
      asm volatile("" : "=v"(xq), KEPT("="));
#pragma unroll
      for (int i = 0; i < 8; ++i) asm volatile("" : "=v"(sl[i]));
    } else {
      asm volatile("; kept registers" : KEPT("="));
      PIN();
      ARGS_HERE;
      const int lane = fresh_lane(), t = hw * 64 + lane;
      best = rmd = 0u;
      tag = __builtin_amdgcn_readfirstlane(*LDS_AT(const volatile unsigned, lbase + FLG + 20));
#pragma unroll
      for (int i = 0; i < 8; ++i) asm volatile("" : "=v"(sl[i]));
      unsigned sad;
      auto tickc = [&]() __attribute__((always_inline)) {
        tick();
        if (nb == kcap + 1) {
#pragma unroll
          for (int i = 0; i < 8; ++i) {
            const unsigned a = (sad ^ ((i >> 2) * 64) ^ (((i >> 1) & 1) * 16)) + (i & 1) * 12288;
            sl[i] = __builtin_shufflevector(__builtin_amdgcn_ds_read_tr16_b64_v4i16(LDS_AT(s16x4, a)), __builtin_amdgcn_ds_read_tr16_b64_v4i16(LDS_AT(s16x4, a + 1536)), 0, 1, 2, 3, 4, 5, 6, 7);
          }
        }
        PIN();
      };
      static_assert(CAP0 + 8 <= KPOLL, "the stage is taken before that");
      if (hw == 1) STAMP(4, 4);
      if (hw == 0) {
#pragma unroll 1
        for (int spin = 0; spin < SPIN_MAX; ++spin) {
          if (none(ld_ag(gran + GR_X + (tmi * RX + tni % RX) * 32 + 16 + (lane & 15)) != tag)) break;
          __builtin_amdgcn_s_sleep(1);
        }
      }
      if (hw == 0) STAMP(4, 5);
      asm volatile("" ::: "memory");
      PIN();
      __builtin_amdgcn_s_barrier();
      PIN();
      if (hw == 1) STAMP(4, 6);
      {
        const int ln = fresh_lane(), g4 = ln >> 4, tp = ln & 3, k = 8 * g4 + ((ln & 15) >> 2);
        sad = lbase + (kcap % NST) * STB + ABYTES + k * 384 + (((2 * (tp >> 1)) ^ (4 * ((k >> 1) & 1)) ^ (g4 & 1)) << 4) + 8 * (tp & 1) + (hw % 3) * 128;
      }
      wsum = __uint_as_float(__builtin_amdgcn_readfirstlane(*LDS_AT(const volatile unsigned, lbase + FLG + 60)));
      asm volatile("" : "+v"(sad));
      PIN();
#pragma unroll 1
      for (int spin = 0; spin < SPIN_MAX; ++spin) {
        const unsigned v = ld_ag(gran + GR_X + (tmi * RX + tni % RX) * 32 + (lane & 15));
        tickc();
        if (none(v != tag)) break;
      }
      asm volatile("" ::: "memory");
      PIN();
      const int row = m0 + (t & 127);
      const double s1 = ld_ag(stats + row), s2 = ld_ag(stats + S_ + row), sw = ld_ag(stats + 2 * S_ + row);
      cs = __uint_as_float(ld_ag(reinterpret_cast<const unsigned*>(csf) + (t < 192 ? (t >> 6) : 0) * 1024 + n0 + (t & 63)));
      tickc();
      float mean, var, mb, den;
      ln_from_sums(s1, s2, mean, var, mb, den);
      const float rin = frcp(den), g1 = 1.0f + rin;
      const float mb2 = rbf(mean + (mean - mb) * rin);
      const float den2 = rbf(__builtin_amdgcn_sqrtf(rbf(rbf(var * g1 * g1) + 1e-5f)));
      alpha = g1 * frcp(den2); m = (mb * rin + mb2) * frcp(g1);
      const __amdgpu_buffer_rsrc_t rsg = __builtin_amdgcn_make_buffer_rsrc(hand, 0, GRAN_N * 4, 0x00020000);
      if (tmi == tni && hw >= 2) {
        const float r = (float)fma(-(double)mb, (double)wsum, sw) * rin;
        const unsigned rb = pack2(r, r);
        unsigned rkey = (rb ^ ((unsigned)((int)rb >> 31) | 0x80000000u)) & 0xffff0000u;
        if (r != r) rkey = 0u;
        rkey = wave_umax(rkey | (unsigned)(2047 - row));
        const unsigned md = __builtin_amdgcn_readlane(f2word(__uint_as_float((__float_as_uint(mb) >> 16) | (__float_as_uint(den) & 0xffff0000u))), (2047 - (int)(rkey & 0xffffu)) & 63);
        __builtin_amdgcn_raw_buffer_store_b64(u32x2{rkey, md}, rsg, lane < NREP ? lane * 256 + (tmi * 2 + hw - 2) * 8 : GRAN_N * 4, 0, 16);
      }
      while (nb < KPOLL) tickc();
#pragma unroll 1
      for (int spin = 0; spin < SPIN_MAX; ++spin) {
        const u32x2 v = __builtin_amdgcn_raw_buffer_load_b64(rsg, (bid % NREP) * 256 + (lane & 31) * 8, 0, 16);
        tick(); tick();
        if (none(umax(v[0], v[1]) == SENT)) {
          best = wave_umax(v[0]);
          rmd = __builtin_amdgcn_readlane(v[1], ((2047 - (int)(best & 0xffffu)) >> 6) & 31);
          break;
        }
      }
      const int ix = (2047 - (int)(best & 0xffffu)) & 2047;
      if (row == ix) alpha = 0.f;
      xq = __builtin_amdgcn_raw_buffer_load_b64(__builtin_amdgcn_make_buffer_rsrc(const_cast<u16*>(A), 0, S_ * D_ * 2, 0x00020000), (ix * D_ + hw * 256 + (lane & 3) * 64 + (lane >> 2) * 4) * 2, 0, 16);
      PIN();
    }
    tag = __builtin_amdgcn_readfirstlane(tag);
    wsum = __uint_as_float(__builtin_amdgcn_readfirstlane(__float_as_uint(wsum)));
    const int lane = fresh_lane(), t = hw * 64 + lane;
    {
      unsigned c0, c1, c2, a0, a1, a2;
      split3(cs, c0, c1, c2);
      split3(-m, a0, a1, a2);
      u32x4 tw = {c0 | (c1 << 16), c0 | (c2 << 16), c1 | (c0 << 16), 0u}, ta = {a0 | (a0 << 16), a1 | (a0 << 16), a1 | (a2 << 16), 0u};
      asm volatile("" : "+v"(tw), "+v"(ta), "+v"(alpha));
      __builtin_amdgcn_s_setprio(TPRIO);
      if (nb < lim) {
        while (nb < lim) tick();
        __builtin_amdgcn_s_sleep(TSLP);
      }
      if (t < 192) *LDS_AT(u32x4, lbase + TABW + t * 16) = tw;
      if (t < 128) {
        *LDS_AT(u32x4, lbase + TABA + t * 16) = ta;
        *LDS_AT(float, lbase + ALPHA + t * 4) = alpha;
      }
    }
    if (t >= 192 && t < 196) *LDS_AT(u32x4, lbase + CRED + ((t >> 1) & 1) * 512 + (t & 1) * 128) = u32x4{0u, 0u, 0u, 0u};
    asm volatile("s_waitcnt lgkmcnt(0)" ::: "memory");
    PIN();
    __builtin_amdgcn_s_barrier();
    PIN();
    *LDS_AT(u32x4, lbase + XML + t * 16) = u32x4{SENT, SENT, SENT, SENT};
    *LDS_AT(u32x4, lbase + SPM + (t & 127) * 16) = u32x4{SENT, SENT, SENT, SENT};
    if (hw == 0) {
      unsigned v;
      asm volatile("v_mov_b32 %0, 0\n\tv_writelane_b32 %0, 2, 0\n\tv_writelane_b32 %0, %1, 5\n\tv_writelane_b32 %0, %2, 7\n\tv_writelane_b32 %0, %3, 9"
                   : "=&v"(v) : "s"(tag), "s"(best), "s"(rmd));
      lds_wr(lbase + FLG + lane * 4, v);
    }
    const int idx = LAY2 ? (2047 - (int)(best & 0xffffu)) & 2047 : -64;
    const bool p1blk = (oni & 7) != 7, fast = tmi < 8, own = fast && (loc & 1);
    const int fg = xcd * 28 + (p1blk ? loc - (loc >> 3) : 0), ow = (xcd & 3) * 16 + (loc >> 1);
    static_assert(OMX == 4, "oni % 8 = loc % 8");
#define GLD(a, b) "global_load_dwordx4 v[" #a ":" #b "], %0, off\n\tv_lshl_add_u64 %0, %0, 0, %1\n\t"
#define GLD4(...) asm volatile(__VA_ARGS__ : "+v"(p) : "s"((unsigned long long)(F_ * 2)), KEPT() : "memory")
    auto gptr = [&](const u16* wg, const u16* wu) {
      const int ln = fresh_lane();
      return ((ln & 2) ? wu : wg) + (size_t)(hw * 256 + (ln >> 2) * 4) * F_ + fg * 16 + (ln & 1) * 8;
    };
    auto gather = [&](const u16*& p, int e) __attribute__((always_inline)) {
      if (e == 0) GLD4(GLD(104, 107) GLD(108, 111) GLD(112, 115) GLD(116, 119));
      else if (e == 1) GLD4(GLD(120, 123) GLD(124, 127) GLD(128, 131) GLD(132, 135));
      else if (e == 2) GLD4(GLD(136, 139) GLD(140, 143) GLD(144, 147) GLD(148, 151));
      else GLD4(GLD(152, 155) GLD(156, 159) GLD(160, 163) GLD(164, 167));
      p += 60 * F_;
    };
    auto dmask = [&](int q) { const int ln = fresh_lane(); return ((ln >> 1) & 1) == q ? ((ln & 1) ? 0xffff0000u : 0x0000ffffu) : 0u; };
    unsigned dm0, dm1, dad;
    auto gu_setup = [&]() __attribute__((always_inline)) {
      const int ln = fresh_lane();
      dm0 = dmask(0); dm1 = dmask(1);
      dad = lbase + SACT + ((ln & 3) * 8 + (ln >> 5) * 4 + ((ln >> 4) & 1)) * 16 + hw * 4;
      asm volatile("" : "+v"(dm0), "+v"(dm1), "+v"(dad));
    };
    auto gate_up = [&](u32x2 x, unsigned md) __attribute__((always_inline)) {
      const unsigned rin = __builtin_amdgcn_readfirstlane(__float_as_uint(frcp(__uint_as_float(md & 0xffff0000u))));
      asm volatile("s_getpc_b64 s[90:91]\n\ts_add_u32 s90, s90, gate_up_code@rel32@lo+4\n\ts_addc_u32 s91, s91, gate_up_code@rel32@hi+12\n\ts_swappc_b64 s[92:93], s[90:91]"
                   : "+{v[100:101]}"(x) : "{s84}"(md << 16), "{s85}"(md & 0xffff0000u), "{s86}"(rin), "{v99}"(dad), "{v102}"(dm0), "{v103}"(dm1), KEPT()
                   : "v84", "v85", "v86", "v87", "v88", "v89", "v90", "v91", "v92", "v93", "v94", "v95", "v96", "v97", "s88", "s89", "s90", "s91", "s92", "s93", "scc", "memory");
    };
    auto act_diag = [&](int so = 0) __attribute__((always_inline)) {
      const int ln = fresh_lane();
      const f32x4 g4 = *LDS_AT(const volatile f32x4, lbase + SACT + so + (ln >> 2) * 16), u4 = *LDS_AT(const volatile f32x4, lbase + SACT + so + 256 + (ln >> 2) * 16);
      float gv = (g4[0] + g4[1]) + (g4[2] + g4[3]), uv = (u4[0] + u4[1]) + (u4[2] + u4[3]);
      rbf2(gv, uv);
      const float av = rbf(gv * frcp(1.0f + __expf(-gv))) * uv;
      const unsigned ap2 = pack2(av, av);
      return __builtin_bit_cast(s16x4, u32x2{ap2 & dm0, ap2 & dm1});
    };
    auto down = [&](const s16x4& a, const u32x2 (&w)[16]) __attribute__((always_inline)) {
      f32x4 d[2] = {{0.f, 0.f, 0.f, 0.f}, {0.f, 0.f, 0.f, 0.f}};
      static_for<0, 16>([&](auto fc) __attribute__((always_inline)) {
        constexpr int f = decltype(fc)::value;
        d[f & 1] = __builtin_amdgcn_mfma_f32_4x4x4bf16_1k(a, __builtin_bit_cast(s16x4, w[f]), d[f & 1], 4, f, 0);
      });
      return u32x4{f2word(d[0][0] + d[1][0]), f2word(d[0][1] + d[1][1]), f2word(d[0][2] + d[1][2]), f2word(d[0][3] + d[1][3])};
    };
    auto fold16 = [&](f32x2 y0, f32x2 y1) __attribute__((always_inline)) {
      const auto q0 = __builtin_amdgcn_permlane32_swap(__float_as_uint(y0[0]), __float_as_uint(y1[0]), false, false);
      const auto q1 = __builtin_amdgcn_permlane32_swap(__float_as_uint(y0[1]), __float_as_uint(y1[1]), false, false);
      const auto q = __builtin_amdgcn_permlane16_swap(__float_as_uint(__uint_as_float(q0[0]) + __uint_as_float(q0[1])),
                                                      __float_as_uint(__uint_as_float(q1[0]) + __uint_as_float(q1[1])), false, false);
      return __uint_as_float(q[0]) + __uint_as_float(q[1]);
    };
    auto fold8 = [&](f32x2 y0, f32x2 y1) __attribute__((always_inline)) { return dpp_add<0x128, 0xf>(fold16(y0, y1)); };
    auto fold4 = [&](f32x2 y0, f32x2 y1) __attribute__((always_inline)) { return dpp_add<0x124, 0xf>(fold8(y0, y1)); };
    auto y_sum = [&](const __amdgpu_buffer_rsrc_t rs, auto nbc) __attribute__((always_inline)) {
      constexpr int NB = decltype(nbc)::value, N = 14 / NB;
      const int ln = fresh_lane(), src = (GRAN_N + (ln >> 2) * D_ + ow * 16 + (ln & 3) * 4) * 4;
      f32x2 y0 = {0.f, 0.f}, y1 = {0.f, 0.f};
#pragma unroll 1
      for (int bt = 0; bt < NB; ++bt) {
        f32x2 z0, z1;
#pragma unroll 1
        for (int spin = 0; spin < SPIN_MAX; ++spin) {
          u32x4 v[N];
#pragma unroll
          for (int i = 0; i < N; ++i) v[i] = __builtin_amdgcn_raw_buffer_load_b128(rs, src, (bt * N + i) * (16 * D_ * 4), 16);
          z0 = y0; z1 = y1;
#pragma unroll
          for (int i = 0; i < N; ++i) {
            z0 += f32x2{__uint_as_float(v[i][0]), __uint_as_float(v[i][1])};
            z1 += f32x2{__uint_as_float(v[i][2]), __uint_as_float(v[i][3])};
          }
          const float chk = (z0[0] + z0[1]) + (z1[0] + z1[1]);
          if (none(chk != chk)) break;
          unsigned mx = 0u;
#pragma unroll
          for (int i = 0; i < N; ++i) mx = umax(umax(umax(mx, v[i][0]), v[i][1]), umax(v[i][2], v[i][3]));
          if (none(mx == SENT)) break;
          __builtin_amdgcn_s_sleep(YSLP);
          asm volatile("" ::: "memory");
        }
        y0 = z0; y1 = z1;
      }
      return fold4(y0, y1);
    };
    [&]() __attribute__((always_inline)) {
    ARGS_HERE;
    const unsigned l16 = lane * 16;
    unsigned* kvflag = gran + GR_KV + ((LAY2 ? 16 : 0) + tni) * (32 * RK);
    auto wait_sig = [&](int sh) {
#pragma unroll 1
      for (int i = 0; i < 4 * SPIN_MAX; ++i) {
        if (((__builtin_amdgcn_readfirstlane(*LDS_AT(const volatile unsigned, lbase + FLG + 32)) >> sh) & 7u) >= 4u) break;
        __builtin_amdgcn_s_sleep(SSLP);
      }
      asm volatile("" ::: "memory");
      PIN();
    };
    PIN();
    asm volatile("s_waitcnt lgkmcnt(0)" ::: "memory");
    if (!LAY2) asm volatile("s_waitcnt vmcnt(0)" ::: "memory");
    __builtin_amdgcn_s_barrier();
    PIN();
    __builtin_amdgcn_s_setprio(HPRIO);
    if (hw < 3) {
      __builtin_amdgcn_s_sleep(SPRE);
      wait_sig(0);
      __builtin_amdgcn_raw_buffer_store_b32(tag, __builtin_amdgcn_make_buffer_rsrc(kvflag, 0, hw == 0 ? RK * 128 : 0, 0x00020000), lane * 128 + tmi * 4, 0, 16);
      if (hw == 0) STAMP(LAY2 ? 5 : 1, 16);
    }
    if (hw == 3) {
      __builtin_amdgcn_s_sleep(KVS);
      const unsigned kv0 = ld_ag(kvflag + (tmi % RK) * 32 + (lane & 15));
      PIN();
      wait_sig(16);
      const char* cnt_tab = reinterpret_cast<const char*>(gran + GR_N + bid * 64);
      const unsigned vwo = (unsigned)((lane >> 3) * D_ + oni * 64 + ((lane & 7) ^ ((lane >> 3) & 6)) * 8) * 2u;
      constexpr unsigned vs = 8u * D_ * 2u - 1024u;
      unsigned seen = (unsigned)__ballot(kv0 == tag);
      const char* sp = nullptr;
      u32x2 pw = {0u, 0u};
      const int nsp = LAY2 ? PNS : -1;
#pragma unroll 1
      for (int ns = 2; ns < 38; ++ns) {
        if (ns == nsp) {
          gu_setup();
          gate_up(xq, rmd);
          PIN();
          *LDS_AT(volatile unsigned char, lbase + FLG + 7) = (unsigned char)0x7f;
        }
        if (!(ns & 1) && ns < 32) {
          const int jq = (ns >> 1) - 1, pn = jq < 7 ? (tmi & 8) + jq + (jq >= (tmi & 7)) : ((tmi & 8) ^ 8) + jq - 7;
#pragma unroll 1
          for (int i = 0; i < SPIN_MAX && !((seen >> pn) & 1u); ++i) seen = (unsigned)__ballot(ld_ag(kvflag + (tmi % RK) * 32 + (lane & 15)) == tag);
          sp = reinterpret_cast<const char*>(KV) + (size_t)(tni * 128 + pn * 4) * 2048;
        }
        const unsigned add = ns < 8 ? 0x80808080u : (128u - (ns < 9 ? 1u : (unsigned)(ns - 7) >> 1)) * 0x01010101u;
#pragma unroll 1
        for (int i = 0; i < 4 * SPIN_MAX; ++i) {
          if (((__builtin_amdgcn_readfirstlane(pw[0]) + add) & (__builtin_amdgcn_readfirstlane(pw[1]) + add) & 0x80808080u) == 0x80808080u) break;
          __builtin_amdgcn_s_sleep(W3SLP);
          pw = *LDS_AT(const volatile u32x2, lbase + FLG + 8);
        }
        asm volatile("" ::: "memory");
        if (ns < 32) {
          if (ns == 31) glds16s<true>(csf + 4096 + tni * 128, l16, lbase + UGL);
#define NJ 4
#pragma unroll
          for (int j = 0; j < NJ; ++j)
            glds16x4<true>(sp + (j >> 1) * 131072u + (j & 1) * 4096u, l16, l16, l16 + (VOFF - 2048u), l16 + (VOFF - 2048u), lbase + (ns & 7) * ASB + j * 4096);
          unsigned keep;
          asm volatile("s_mov_b32 %0, m0\n\ts_mov_b32 m0, %3\n\ts_nop 2\n\tglobal_load_lds_dword %1, %2 sc1\n\ts_mov_b32 m0, %0"
                       : "=&s"(keep) : "v"(0u), "s"(cnt_tab + ns * 4), "s"(lbase + FLG - 252) : "memory");
          sp += 2048;
        } else {
          const char* wp = reinterpret_cast<const char*>(wo) + (size_t)(ns & 7) * (128 * D_ * 2);
#pragma unroll
          for (int j = 0; j < 4; ++j)
            glds16x4<false>(wp + j * (32u * D_ * 2u), vwo, vwo + vs, vwo + 2 * vs, vwo + 3 * vs, lbase + (ns & 7) * ASB + j * 4096);
        }
      }
      asm volatile("s_waitcnt vmcnt(0)" ::: "memory");
      return;
    }
    if (!LAY2) {
      auto until = [&](unsigned n) {
        while ((__builtin_amdgcn_readfirstlane(*LDS_AT(const volatile unsigned, lbase + FLG + 8)) & 255u) < n) __builtin_amdgcn_s_sleep(16);
        asm volatile("" ::: "memory");
        PIN();
      };
      if (hw == 0) {
        const __amdgpu_buffer_rsrc_t rsx = __builtin_amdgcn_make_buffer_rsrc((ap_ + 1)->xo, 0, S_ * D_ * 2, 0x00020000);
        until(IAT);
#pragma unroll 1
        for (int i = 0; i < 16; ++i) {
          __builtin_amdgcn_raw_buffer_store_b128(u32x4{SENT, SENT, SENT, SENT}, rsx, ((omi * 128 + i * 8 + (lane >> 3)) * D_ + oni * 64 + (lane & 7) * 8) * 2, 0, 16);
          __builtin_amdgcn_s_sleep(ISLP);
        }
      } else if (hw == 2) {
        const __amdgpu_buffer_rsrc_t rsi = __builtin_amdgcn_make_buffer_rsrc(hand + GRAN_N, 0, (2 * HAND_N + PS_N - GRAN_N) * 4, 0x00020000);
        until(IAT);
#pragma unroll 1
        for (int i = 0; i < ((2 * HAND_N + PS_N - GRAN_N) / 4 + 16383) / 16384; ++i) {
          __builtin_amdgcn_raw_buffer_store_b128(u32x4{SENT, SENT, SENT, SENT}, rsi, ((i * 256 + bid) * 64 + lane) * 16, 0, 16);
          __builtin_amdgcn_s_sleep(ISLP);
        }
      }
      const int ng = loc & 15, fc = xcd * 2 + (loc >> 4);
      const size_t og = (size_t)((fg - xcd * 28) / 7 * 256 + lane) * F_ + (xcd * 7 + fg % 7) * 64;
      const u16* pd = wd + (size_t)(fc * 224 + (lane < 56 ? lane : 0)) * D_ + ng * 64;
      const u16* p = gptr(wg, wu);
#pragma unroll 1
      for (unsigned n = T0; n < T0 + 13u; ++n) {
        until(n);
        if (hw == 1) {
          const unsigned i = n - T0;
          const u16* q = i < 8u ? ((i & 1u) ? wu : wg) + og + (size_t)(i >> 1) * (64 * F_) : pd + (size_t)((i - 8u - (i > TW)) & 3u) * (56 * D_);
          if (i == TW) {
            q = wo + (size_t)(768 + (loc / (32 / OMX)) * (256 / OMX) + (lane & (256 / OMX - 1) & 63)) * D_ + oni * 64;
            unsigned long long a0, a1;
            CODE_ADDR("layers_kernel", a0); CODE_ADDR("end_marker_kernel", a1);
            if (loc < 8) { const unsigned p1 = touch_code(a0, a1, 3 + loc, lane); asm volatile("" :: "v"(p1)); }
          }
          const unsigned p0 = touch(q);
          asm volatile("" :: "v"(p0));
        }
        if (n >= G0 && (n - G0) % GS == 0u && (n - G0) / GS < 4u) gather(p, (n - G0) / GS);
        if (n == UAT && hw == UHW) {
          u32x4 a[4][2], c[2];
#pragma unroll
          for (int i = 0; i < 4; ++i)
#pragma unroll
            for (int h = 0; h < 2; ++h) a[i][h] = *reinterpret_cast<const u32x4*>(wo + (size_t)(tni * 64 + tmi * 4 + i) * D_ + h * 512 + lane * 8);
#pragma unroll
          for (int h = 0; h < 2; ++h) c[h] = *reinterpret_cast<const u32x4*>(w_r + h * 512 + lane * 8);
          auto dot = [&](unsigned x, unsigned y, float z) { return __builtin_amdgcn_fdot2_f32_bf16(__builtin_bit_cast(bf16x2, x), __builtin_bit_cast(bf16x2, y), z, false); };
          PIN();
          __builtin_amdgcn_s_setprio(0);
#pragma unroll 1
          for (int i = 0; i < SPIN_MAX; ++i) {
            if (__builtin_amdgcn_readfirstlane(*LDS_AT(const volatile unsigned, lbase + FLG + 16)) >= UARR) break;
            __builtin_amdgcn_s_sleep(4);
          }
          PIN();
          float wm = 0.f;
#pragma unroll
          for (int e = 0; e < 8; ++e) wm = dot(c[e >> 2][e & 3], 0x3f803f80u, wm);
          wm = wave_sum(wm);
          lds_wr(lbase + FLG + 60, __float_as_uint(wm));
          wm *= -1.0f / 1024.0f;
          f32x2 u[2], g[2];
#pragma unroll
          for (int i = 0; i < 4; ++i) {
            float p1 = 0.f, pw = 0.f, p2 = 0.f;
#pragma unroll
            for (int e = 0; e < 8; ++e) {
              const unsigned x = a[i][e >> 2][e & 3];
              p1 = dot(x, 0x3f803f80u, p1); pw = dot(x, c[e >> 2][e & 3], pw); p2 = dot(x, x, p2);
            }
            u[i >> 1][i & 1] = fmaf(p1, wm, pw); g[i >> 1][i & 1] = p2;
          }
          float us = fold4(u[0], u[1]), gs = fold4(g[0], g[1]);
          us = dpp_add<0x121, 0xf>(dpp_add<0x122, 0xf>(us)); gs = dpp_add<0x121, 0xf>(dpp_add<0x122, 0xf>(gs));
          __builtin_amdgcn_raw_buffer_store_b32(__float_as_uint((lane & 1) ? __builtin_amdgcn_sqrtf(gs) * (1.0f / 32.0f) : us),
              __builtin_amdgcn_make_buffer_rsrc(csf + 4096 + tni * 128, 0, 512, 0x00020000), (lane & 14) ? 512 : ((lane & 1) * 64 + tmi * 4 + (lane >> 4)) * 4, 0, 16);
        }
      }
      if (hw == 2 && bid == 0) {
        const __amdgpu_buffer_rsrc_t rsc = __builtin_amdgcn_make_buffer_rsrc(gran, 0, GR_N * 4, 0x00020000);
#pragma unroll 1
        for (int spin = 0; spin < SPIN_MAX; ++spin) {
          bool ok = true;
#pragma unroll
          for (int i = 0; i < 4; ++i) ok &= __builtin_amdgcn_raw_buffer_load_b32(rsc, (GR_ROW + (i * 64 + lane) * 32 + 1) * 4, 0, 16) == tag;
          if (__all(ok)) break;
          __builtin_amdgcn_s_sleep(8);
          asm volatile("" ::: "memory");
        }
        if (lane == 0) st_ag(gran, tag);
      }
      return;
    }
    unsigned* X2 = hand + GRAN_N + Y_N;
    unsigned* XS = X2 + X2_N;
    unsigned* P4 = XS + XS_N;
    unsigned* PA = P4 + P4_N;
    const __amdgpu_buffer_rsrc_t rsh = __builtin_amdgcn_make_buffer_rsrc(hand, 0, HAND_N * 4, 0x00020000);
    auto signal = [&](unsigned ph) {
      asm volatile("" ::: "memory");
      PIN();
      *LDS_AT(volatile unsigned char, lbase + FLG + 4 + hw) = (unsigned char)ph;
    };
    auto wait_all = [&](unsigned ph) {
#pragma unroll 1
      for (int i = 0; i < SPIN_MAX; ++i) {
        if (all_ge(__builtin_amdgcn_readfirstlane(*LDS_AT(const volatile unsigned, lbase + FLG + 4)), ph)) break;
        __builtin_amdgcn_s_sleep(WSLP);
      }
      asm volatile("" ::: "memory");
      PIN();
    };
    gu_setup();
    gate_up(xq, rmd);
    signal(hw == 2 ? 2u : 0x7fu);
    if (wid == 8) STAMP(dk, 2);
    if (hw < 2) {
#pragma unroll 1
      for (int i = 0; i < SPIN_MAX; ++i) {
        if (__builtin_amdgcn_readfirstlane(*LDS_AT(const volatile unsigned, lbase + FLG)) >= FSTART) break;
        __builtin_amdgcn_s_sleep(WSLP);
      }
      asm volatile("" ::: "memory");
      PIN();
      if (wid == 8) STAMP(dk, 10);
      u32x2 w[16];
      const __amdgpu_buffer_rsrc_t rd_ = __builtin_amdgcn_make_buffer_rsrc(const_cast<u16*>(wd), 0, D_ * F_ * 2, 0x00020000);
      auto ldw = [&](int c) __attribute__((always_inline)) {
#pragma unroll
        for (int f = 0; f < 16; ++f) w[f] = __builtin_amdgcn_raw_buffer_load_b64(rd_, lane * 8 + c * 512 + (f & 1) * 2048, (fg * 16 + (f & ~1)) * (D_ * 2) + hw * 1024, 0);
        PIN();
      };
      ldw(0);
      const unsigned pt = __builtin_amdgcn_raw_buffer_load_b32(rd_, (lane >> 2) * (D_ * 2) + (lane & 3) * 128 + 512, fg * 16 * (D_ * 2) + hw * 1024, 0);
      int yo = p1blk ? (GRAN_N + fg * D_ + hw * 512) * 4 + lane * 16 : HAND_N * 4;
      asm volatile("" : "+v"(yo));
      wait_all(2u);
      if (wid == 8) STAMP(dk, 3);
      const s16x4 a = act_diag();
      const u32x4 y0 = down(a, w);
      PIN();
      ldw(1);
      __builtin_amdgcn_raw_buffer_store_b128(y0, rsh, yo, 0, 16);
      PIN();
      __builtin_amdgcn_raw_buffer_store_b128(down(a, w), rsh, yo + 1024, 0, 16);
      asm volatile("" :: "v"(pt));
      if (wid == 8) STAMP(dk, 4);
    }
    PIN();
    [&]() __attribute__((always_inline)) {
    const int lane = fresh_lane();
    const float rmb = __uint_as_float(rmd << 16);
    if (hw == 2) {
      wait_all(2u);
      if (own) {
        const float rden = __uint_as_float(rmd & 0xffff0000u), rrin = frcp(rden);
        const int col = ow * 16 + (lane & 3) * 4 + (lane >> 4);
        const unsigned xo2 = ld_ag(reinterpret_cast<const unsigned*>(A + (size_t)idx * D_ + (col & ~1)));
        __builtin_amdgcn_s_sleep(PRE1);
        const float y = y_sum(rsh, std::integral_constant<int, 2>{});
        const unsigned rk = best >> 16;
        const float rv = __uint_as_float(((rk & 0x8000u) ? (rk & 0x7fffu) : (~rk & 0xffffu)) << 16);
        const float rs = rbf(frcp(1.0f + __expf(-rv)));
        const float x1v = __uint_as_float((col & 1) ? xo2 & 0xffff0000u : xo2 << 16);
        float h0 = x1v, h1 = x1v;
        ln_apply2(h0, h1, rmb, rden, rrin);
        const float x2v = rbf(x1v + rbf(h0 + rbf(rbf(y) * rs)));
        const auto pr = __builtin_amdgcn_permlane16_swap(__float_as_uint(x2v), __float_as_uint(x2v), false, false);
        __builtin_amdgcn_raw_buffer_store_b32(f2word(__uint_as_float((pr[0] >> 16) | pr[1])), rsh, (lane & 28) ? HAND_N * 4 : (int)(X2 - hand) * 4 + col * 2, 0, 16);
        const float d = x2v - rmb;
        const float sd = wave_sum(d), sq = wave_sum(d * d);
        if (lane < 2) st_ag(XS + ow * 2 + lane, f2word(lane ? sq : sd));
        STAMP(dk, 5);
      }
    }
    {
      const int ks = kcap ^ (tmi & 8), g4 = lane >> 4;
      const int src = (int)(X2 - hand) * 4 + ks * 128 + g4 * 16;
      int dst = (lane & 12) ? HAND_N * 4 : (int)((P4 - hand) + (tni * 16 + ks) * 192 + hw * 64 + (lane & 2) * 16 + (lane & 1) * 8 + (g4 >> 1) * 16 + (g4 & 1) * 4) * 4;
      asm volatile("" : "+v"(dst));
      u32x4 x0, x1;
      if (!(hw == 2 && own)) __builtin_amdgcn_s_sleep(PRE4);
#pragma unroll 1
      for (int spin = 0; spin < SPIN_MAX; ++spin) {
        x0 = __builtin_amdgcn_raw_buffer_load_b128(rsh, src, 0, 16); x1 = __builtin_amdgcn_raw_buffer_load_b128(rsh, src + 64, 0, 16);
        if (none(umax(umax(umax(x0[0], x0[1]), umax(x0[2], x0[3])), umax(umax(x1[0], x1[1]), umax(x1[2], x1[3]))) == SENT)) break;
        __builtin_amdgcn_s_sleep(PSLP);
        asm volatile("" ::: "memory");
      }
      f32x4 d[4];
#pragma unroll
      for (int i = 0; i < 4; ++i) d[i] = MFMA16(sl[2 * i], __builtin_bit_cast(s16x8, x0), (f32x4{0.f, 0.f, 0.f, 0.f}));
#pragma unroll
      for (int i = 0; i < 4; ++i) d[i] = MFMA16(sl[2 * i + 1], __builtin_bit_cast(s16x8, x1), d[i]);
      u32x4 o;
#pragma unroll
      for (int e = 0; e < 4; ++e) o[e] = f2word((lane & 2) ? ((lane & 1) ? d[3][e] : d[2][e]) : ((lane & 1) ? d[1][e] : d[0][e]));
      __builtin_amdgcn_raw_buffer_store_b128(o, rsh, dst, 0, 16);
      PIN();
      if (wid == 8) STAMP(dk, 6);
    }
    if (hw == 2) {
      const int col = (lane & 15) * 4 + (lane >> 4), src = (int)((P4 - hand) + (tni * 16 + (lane >> 4)) * 192 + (lane & 15) * 4) * 4;
      const __amdgpu_buffer_rsrc_t rsq = __builtin_amdgcn_make_buffer_rsrc(hand, 0, fast ? HAND_N * 4 : 0, 0x00020000);
      float cs[3], val[3];
#pragma unroll
      for (int p = 0; p < 3; ++p) cs[p] = __uint_as_float(ld_ag(reinterpret_cast<const unsigned*>(csf) + p * 1024 + n0 + col));
      u32x2 xs;
      __builtin_amdgcn_s_sleep(PRE7);
#pragma unroll 1
      for (int spin = 0; spin < SPIN_MAX; ++spin) {
        u32x4 v[3][4];
        xs = __builtin_amdgcn_raw_buffer_load_b64(rsh, (int)((XS - hand) + lane * 2) * 4, 0, 16);
#pragma unroll
        for (int p = 2; p >= 0; --p)
#pragma unroll
          for (int i = 0; i < 4; ++i) v[p][i] = __builtin_amdgcn_raw_buffer_load_b128(p ? rsh : rsq, src + p * 256, i * (4 * 192 * 4), 16);
        auto part = [&](int p) __attribute__((always_inline)) {
          float z[4];
#pragma unroll
          for (int e = 0; e < 4; ++e) {
            z[e] = (__uint_as_float(v[p][0][e]) + __uint_as_float(v[p][1][e])) + (__uint_as_float(v[p][2][e]) + __uint_as_float(v[p][3][e]));
            asm volatile("" : "+v"(z[e]));
          }
          val[p] = fold16(f32x2{z[0], z[1]}, f32x2{z[2], z[3]});
        };
        part(2); part(1);
        val[0] = 0.f;
        if (fast) part(0);
        const float chk = (val[0] + val[1]) + (val[2] + __uint_as_float(xs[0])) + __uint_as_float(xs[1]);
        if (none(chk != chk)) break;
        unsigned mx = umax(xs[0], xs[1]);
#pragma unroll
        for (int i = 0; i < 12; ++i) mx = umax(umax(umax(mx, v[i >> 2][i & 3][0]), v[i >> 2][i & 3][1]), umax(v[i >> 2][i & 3][2], v[i >> 2][i & 3][3]));
        if (none(mx == SENT)) break;
        __builtin_amdgcn_s_sleep(PSLP);
        asm volatile("" ::: "memory");
      }
      const auto sw = __builtin_amdgcn_permlane32_swap(xs[0], xs[1], false, false);
      float sm = __uint_as_float(sw[0]) + __uint_as_float(sw[1]);
      sm = dpp_add<0x142, 0xa>(dpp_add<0x118, 0xf>(dpp_add<0x114, 0xf>(dpp_add<0x112, 0xf>(dpp_add<0x111, 0xf>(sm)))));
      const float dm = __int_as_float(__builtin_amdgcn_readlane(__float_as_int(sm), 31)) * (0.25f / 1024.0f);
      const float var = fmaxf(__int_as_float(__builtin_amdgcn_readlane(__float_as_int(sm), 63)) * (0.25f / 1024.0f) - dm * dm, 0.f);
      const float mb = rbf(rmb + dm);
      const float rin = frcp(rbf(__builtin_amdgcn_sqrtf(rbf(rbf(var) + 1e-5f))));
      const unsigned kv = pack2((val[1] - mb * cs[1]) * rin, (val[2] - mb * cs[2]) * rin);
      *LDS_AT(u16, lbase + TAB + 128 + kpos(col) * 2) = (u16)kv;
      *LDS_AT(u16, lbase + TAB + 256 + col * 2) = (u16)(kv >> 16);
      if (fast) *LDS_AT(u16, lbase + TAB + kpos(col) * 2) = f2bf((val[0] - mb * cs[0]) * rin * QSCALE);
      signal(7u);
      STAMP(dk, 7);
      if (own) {
#pragma unroll
        for (int i = 0; i < 14; ++i)
          __builtin_amdgcn_raw_buffer_store_b128(u32x4{SENT, SENT, SENT, SENT}, rsh, (GRAN_N + (i * 16 + (lane >> 2)) * D_ + ow * 16 + (lane & 3) * 4) * 4, 0, 16);
      }
      return;
    }
    if (!fast) return;
    const int head = tni * 2 + hw;
    const unsigned tb = lbase + TAB + hw * 64;
    {
      const __amdgpu_buffer_rsrc_t rs = __builtin_amdgcn_make_buffer_rsrc(KV, 0, 2 * S_ * D_ * 2, 0x00020000);
      const int src = (head * 64 + tmi * 4) * 2048 + lane * 16;
      auto ld = [&](int i, unsigned off) { return __builtin_bit_cast(s16x8, __builtin_amdgcn_raw_buffer_load_b128(rs, src + (i & 3) * 1024, (i >> 3) * 65536 + ((i >> 2) & 1) * 4096 + off, 16)); };
#pragma unroll 1
      for (int i = 0; i < SPIN_MAX; ++i)
        if (__builtin_amdgcn_readfirstlane(ld_ag(kvflag + (tmi % RK) * 32 + tmi + 8)) == tag) break;
      asm volatile("" ::: "memory");
      s16x8 f[16];
#pragma unroll
      for (int i = 0; i < 16; ++i) f[i] = ld(i, 0u);
      PIN();
      const bool on = ((lane ^ (lane >> 4)) & 1) == 0;
      const unsigned pl = lbase + RED + hw * 1024;
      unsigned pw = pl + (lane >> 4) * 64 + (lane & 15) * 2, pr = pl + (on ? (lane >> 5) * 16 : 512), qr = on ? tb + (lane >> 5) * 16 : pl + 512;
      const int gone = (((idx >> 7) & 7) == tmi) ? ((idx >> 10) * 2 + (kpos(idx & 31) >> 4)) * 64 + ((idx >> 5) & 3) * 16 + (kpos(idx & 31) & 15) : -1;
      asm volatile("" : "+v"(pw), "+v"(pr), "+v"(qr));
      *LDS_AT(u32x2, pl + 512 + lane * 8) = u32x2{0u, 0u};
      wait_all(7u);
      u32x4 q[2];
#pragma unroll
      for (int sx = 0; sx < 2; ++sx) {
        q[sx] = *LDS_AT(const u32x4, qr + sx * 32);
      }
      float sc[4];
#pragma unroll
      for (int i = 0; i < 8; ++i) {
        f32x4 d = MFMA16(__builtin_bit_cast(s16x8, q[0]), f[2 * i], (f32x4{0.f, 0.f, 0.f, 0.f}));
        d = MFMA16(__builtin_bit_cast(s16x8, q[1]), f[2 * i + 1], d);
        PIN();
        f[2 * i] = ld(2 * i, VOFF); f[2 * i + 1] = ld(2 * i + 1, VOFF);
        PIN();
#pragma unroll
        for (int r = 0; r < 2; ++r) sc[(i >> 2) * 2 + r] = (i & 3) == 0 || (lane >> 4) == (i & 3) ? d[r] : sc[(i >> 2) * 2 + r];
      }
      if (gone >= 0) {
#pragma unroll
        for (int r = 0; r < 4; ++r) sc[r] = r * 64 + lane == gone ? -INFINITY : sc[r];
      }
      const float mx = wave_fmax(fmaxf(fmaxf(sc[0], sc[1]), fmaxf(sc[2], sc[3])));
      const unsigned p0 = pack2(__builtin_amdgcn_exp2f(sc[0] - mx), __builtin_amdgcn_exp2f(sc[1] - mx)), p1 = pack2(__builtin_amdgcn_exp2f(sc[2] - mx), __builtin_amdgcn_exp2f(sc[3] - mx));
      *LDS_AT(u16, pw) = (u16)p0; *LDS_AT(u16, pw + 32) = (u16)(p0 >> 16);
      *LDS_AT(u16, pw + 256) = (u16)p1; *LDS_AT(u16, pw + 288) = (u16)(p1 >> 16);
      asm volatile("" ::: "memory");
      const float ls = wave_sum((__uint_as_float(p0 << 16) + __uint_as_float(p0 & 0xffff0000u)) + (__uint_as_float(p1 << 16) + __uint_as_float(p1 & 0xffff0000u)));
      f32x4 o = {0.f, 0.f, 0.f, 0.f};
#pragma unroll
      for (int i = 0; i < 16; ++i) o = MFMA16(*LDS_AT(const s16x8, pr + i * 32), f[i], o);
      int pv = __float_as_int((lane & 16) ? o[1] : o[0]);
      asm volatile("s_nop 0\n\tv_writelane_b32 %0, %1, 32\n\tv_writelane_b32 %0, %2, 33" : "+v"(pv) : "s"(ls), "s"(mx));
      __builtin_amdgcn_raw_buffer_store_b32(f2word(__int_as_float(pv)), rsh, lane < 34 ? (int)((PA - hand) + (head * 8 + tmi) * 64 + lane) * 4 : HAND_N * 4, 0, 16);
    }
    if (wid == 8) STAMP(dk, 8);
    if (tmi != ((idx >> 7) & 7)) return;
    {
      const int src = (int)((PA - hand) + head * 512 + (lane < 34 ? lane : 0)) * 4;
      const unsigned ta = tb + (lane & 31) * 2;
      const float so = wave_sum(lane < 32 ? bf2f(*LDS_AT(const u16, ta)) * bf2f(*LDS_AT(const u16, ta + 128)) : 0.f);
      const float vo = lane < 32 ? bf2f(*LDS_AT(const u16, ta + 256)) : 1.0f;
      unsigned v[8];
      __builtin_amdgcn_s_sleep(PRE5);
#pragma unroll 1
      for (int spin = 0; spin < SPIN_MAX; ++spin) {
        unsigned mx = 0u;
#pragma unroll
        for (int i = 0; i < 8; ++i) v[i] = __builtin_amdgcn_raw_buffer_load_b32(rsh, src + i * 256, 0, 16);
#pragma unroll
        for (int i = 0; i < 8; ++i) mx = umax(mx, v[i]);
        if (none(mx == SENT)) break;
        __builtin_amdgcn_s_sleep(PSLP);
        asm volatile("" ::: "memory");
      }
      float m8[8], mx = so;
#pragma unroll
      for (int i = 0; i < 8; ++i) { m8[i] = __int_as_float(__builtin_amdgcn_readlane((int)v[i], 33)); mx = fmaxf(mx, m8[i]); }
      float acc = __builtin_amdgcn_exp2f(so - mx) * vo;
#pragma unroll
      for (int i = 0; i < 8; ++i) acc += __builtin_amdgcn_exp2f(m8[i] - mx) * __uint_as_float(v[i]);
      const float l = __int_as_float(__builtin_amdgcn_readlane(__float_as_int(acc), 32));
      const float ov = acc / l;
      const float o1 = __int_as_float(__builtin_amdgcn_update_dpp(0, __float_as_int(ov), 0x55, 0xf, 0xf, false));
      const float o2 = __int_as_float(__builtin_amdgcn_update_dpp(0, __float_as_int(ov), 0xaa, 0xf, 0xf, false));
      const float o3 = __int_as_float(__builtin_amdgcn_update_dpp(0, __float_as_int(ov), 0xff, 0xf, 0xf, false));
      const __amdgpu_buffer_rsrc_t rso = __builtin_amdgcn_make_buffer_rsrc(Of, 0, S_ * D_ * 2, 0x00020000);
      if (lane < 32 && (lane & 3) == 0)
        __builtin_amdgcn_raw_buffer_store_b64(u32x2{pack2(ov, o1), pack2(o2, o3)}, rso,
            (((idx >> 5) * 32 + head) * 1024 + (lane >> 4) * 512 + (((lane >> 2) & 1) * 32 + (idx & 31)) * 8 + ((lane >> 3) & 1) * 4) * 2, 0, 16);
      asm volatile("s_waitcnt vmcnt(0)" ::: "memory");
      if (lane < RO) st_ag(gran + GR_O + ((16 + (idx >> 7)) * RO + lane) * 64 + 16 + head, tag);
      STAMP(dk, 9);
    }
    }();
    }();
    __builtin_amdgcn_s_setprio(0);
    asm volatile("s_waitcnt vmcnt(0) lgkmcnt(0)" ::: "memory");
    PIN();
    ARGS_HERE;
    u32x2 w[16];
    auto ld_wd = [&]() __attribute__((always_inline)) {
      const __amdgpu_buffer_rsrc_t rd_ = __builtin_amdgcn_make_buffer_rsrc(const_cast<u16*>(wd), 0, D_ * F_ * 2, 0x00020000);
#pragma unroll
      for (int f = 0; f < 16; ++f) w[f] = __builtin_amdgcn_raw_buffer_load_b64(rd_, t * 8 + (f & 1) * 2048, (fg * 16 + (f & ~1)) * (D_ * 2), 0);
      PIN();
    };
    float fnum = 0.f, fvar = 0.f;
    u32x2 wq = {0u, 0u};
    const int rrow = omi * 128 + (oni >> 3) * 64 + (hw & 1) * 32;
    if (LAY2) {
      ld_wd();
      wq = *reinterpret_cast<const u32x2*>(w_r + hw * 256 + (lane & 3) * 64 + (lane >> 2) * 4);
      if (!p1blk) {
        const int fr = rrow + (lane & 7) * 4 + (lane >> 4);
        const double s1 = ld_ag(stats + fr), s2 = ld_ag(stats + S_ + fr), sw = ld_ag(stats + 2 * S_ + fr);
        lds_wr(lbase + SPM + 132, __float_as_uint(wsum));
        float mean, var, mb, den;
        ln_from_sums(s1, s2, mean, var, mb, den);
        const float g1 = 1.0f + frcp(den);
        fnum = fr == idx ? __uint_as_float(0x7fc00000u) : fmaf(-mean, wsum, (float)sw) * g1;
        fvar = var * g1 * g1;
      }
      asm volatile("" : "+v"(fnum), "+v"(fvar));
      PIN();
    }
    if (hw == 3) {

      const int nfl = (LAY2 && (idx >> 7) == omi) ? 48 : 16;
      auto pfl = (__attribute__((address_space(1))) const unsigned*)(gran + GR_O + (((LAY2 ? 16 : 0) + omi) * RO + oni % RO) * 64 + (lane < nfl ? lane : 0));
      constexpr unsigned vs = 8u * D_ * 2u - 1024u;
      unsigned vw0 = (unsigned)((lane >> 3) * D_ + oni * 64 + ((lane & 7) ^ ((lane >> 3) & 6)) * 8) * 2u, vw1 = vw0 + vs, vw2 = vw0 + 2 * vs, vw3 = vw0 + 3 * vs;
      asm volatile("" : "+v"(pfl), "+v"(vw0), "+v"(vw1), "+v"(vw2), "+v"(vw3));
      auto wait_arr = [&](unsigned n) {
#pragma unroll 1
        for (int i = 0; i < 4 * SPIN_MAX; ++i) {
          if (__builtin_amdgcn_readfirstlane(*LDS_AT(const volatile unsigned, lbase + FLG + 16)) >= n) break;
          __builtin_amdgcn_s_sleep(1);
        }
        asm volatile("" ::: "memory");
        PIN();
      };
      wait_arr(12u);
      unsigned fl;
      asm volatile("global_load_dword %0, %1, off sc1" : "=v"(fl) : "v"(pfl) : "memory");
#pragma unroll 1
      for (int i = 0; i < 4 * SPIN_MAX; ++i) {
        if (__builtin_amdgcn_readfirstlane(*LDS_AT(const volatile unsigned, lbase + FLG + 24)) >= 8u) break;
        __builtin_amdgcn_s_sleep(1);
      }
      asm volatile("" ::: "memory");
      PIN();
#pragma unroll
      for (int j = 24; j < 32; ++j)
        glds16x4<false>(reinterpret_cast<const char*>(wo) + j * (32u * D_ * 2u), vw0, vw1, vw2, vw3, lbase + j * 4096);
      unsigned keep;
      asm volatile("s_mov_b32 %0, m0\n\ts_mov_b32 m0, %3\n\ts_nop 2\n\tglobal_load_lds_dword %1, %2 sc1\n\ts_mov_b32 m0, %0"
                   : "=&s"(keep) : "v"(0u), "s"(reinterpret_cast<const char*>(gran + GR_N + bid * 64 + 39)), "s"(lbase + FLG - 252) : "memory");
      asm volatile("s_waitcnt vmcnt(33)" : "+v"(fl) :: "memory");
      if (__builtin_amdgcn_ballot_w64(fl != tag)) {
        __builtin_amdgcn_s_sleep(CSL1);
#pragma unroll 1
        for (int spin = 0; spin < SPIN_MAX; ++spin) {
          if (!__builtin_amdgcn_ballot_w64(__hip_atomic_load(pfl, __ATOMIC_RELAXED, __HIP_MEMORY_SCOPE_AGENT) != tag)) break;
          __builtin_amdgcn_s_sleep(CSLP);
        }
      }
      asm volatile("" ::: "memory");
      PIN();
    }
    u32x4 pv[4];
    asm volatile("" : "=v"(pv[0]), "=v"(pv[1]), "=v"(pv[2]), "=v"(pv[3]));
    if (LAY2 && !p1blk && hw < 2) {
      const __amdgpu_buffer_rsrc_t rsp = __builtin_amdgcn_make_buffer_rsrc(hand + 2 * HAND_N + PP_O, 0, H_ * S_ * 4, 0x00020000);
      const int src = ((lane >> 3) * S_ + rrow + (lane & 7) * 4) * 4;
#pragma unroll 1
      for (int i = 0; i < 4 * SPIN_MAX; ++i) {
        if (__builtin_amdgcn_readfirstlane(*LDS_AT(const volatile unsigned, lbase + FLG + 24)) >= 8u) break;
        __builtin_amdgcn_s_sleep(2);
      }
#pragma unroll 1
      for (int spin = 0; spin < SPIN_MAX; ++spin) {
        asm volatile("" ::: "memory");
        unsigned mx = 0u;
#pragma unroll
        for (int i = 0; i < 4; ++i) pv[i] = __builtin_amdgcn_raw_buffer_load_b128(rsp, src, i * (8 * S_ * 4), 16);
#pragma unroll
        for (int i = 0; i < 4; ++i) mx = umax(umax(umax(mx, pv[i][0]), pv[i][1]), umax(pv[i][2], pv[i][3]));
        if (none(mx == SENT)) break;
        __builtin_amdgcn_s_sleep(PPSLP);
      }
    }
    asm volatile("s_waitcnt lgkmcnt(0)" ::: "memory");
    __builtin_amdgcn_s_barrier();
    if (!LAY2) {
      __builtin_amdgcn_s_barrier();
      if (hw == 3) {
        const u16* p = gptr(wg, wu);
#pragma unroll
        for (int e = 0; e < 4; ++e) gather(p, e);
        asm volatile("s_waitcnt vmcnt(0)" ::: "memory");
        STAMP(4, 2);
      }
      __builtin_amdgcn_s_barrier();
      PIN();
      if (hw == 1) STAMP(4, 3);
      __builtin_amdgcn_raw_buffer_store_b32(tag, __builtin_amdgcn_make_buffer_rsrc(gran + GR_X + omi * RX * 32, 0, hw == 0 ? RX * 128 : 0, 0x00020000), lane * 128 + oni * 4, 0, 16);
      asm volatile(AGAIN "\ngate_up_code:\n\t"
                 "v_lshlrev_b32 v84, 16, v100\n\tv_and_b32 v85, 0xffff0000, v100\n\tv_lshlrev_b32 v86, 16, v101\n\tv_and_b32 v87, 0xffff0000, v101\n\t"
                 "v_subrev_f32 v84, s84, v84\n\tv_subrev_f32 v85, s84, v85\n\tv_subrev_f32 v86, s84, v86\n\tv_subrev_f32 v87, s84, v87\n\t"
                 "v_cvt_pk_bf16_f32 v85, v84, v85\n\tv_cvt_pk_bf16_f32 v87, v86, v87\n\ts_mov_b32 s88, 0x01000100\n\ts_mov_b32 s89, 0x03020302\n\t"
                 "v_lshlrev_b32 v84, 16, v85\n\tv_and_b32 v85, 0xffff0000, v85\n\tv_lshlrev_b32 v86, 16, v87\n\tv_and_b32 v87, 0xffff0000, v87\n\t"
                 "v_mul_f32 v88, s86, v84\n\tv_mul_f32 v89, s86, v85\n\tv_mul_f32 v90, s86, v86\n\tv_mul_f32 v91, s86, v87\n\tv_fma_f32 v84, -v88, s85, v84\n\t"
                 "v_fma_f32 v85, -v89, s85, v85\n\tv_fma_f32 v86, -v90, s85, v86\n\tv_fma_f32 v87, -v91, s85, v87\n\tv_fmac_f32 v88, s86, v84\n\t"
                 "v_fmac_f32 v89, s86, v85\n\tv_fmac_f32 v90, s86, v86\n\tv_fmac_f32 v91, s86, v87\n\tv_cvt_pk_bf16_f32 v88, v88, v89\n\t"
                 "v_cvt_pk_bf16_f32 v90, v90, v91\n\tv_perm_b32 v92, v88, v88, s88\n\tv_perm_b32 v93, v88, v88, s89\n\tv_perm_b32 v94, v90, v90, s88\n\t"
                 "v_perm_b32 v95, v90, v90, s89\n\ts_nop 0\n\tv_and_b32_dpp v96, v92, v102 quad_perm:[0,0,0,0] row_mask:0xf bank_mask:0xf\n\t"
                 "v_and_b32_dpp v97, v92, v103 quad_perm:[0,0,0,0] row_mask:0xf bank_mask:0xf\n\t"
                 "v_and_b32_dpp v100, v93, v102 quad_perm:[0,0,0,0] row_mask:0xf bank_mask:0xf\n\t"
                 "v_and_b32_dpp v101, v93, v103 quad_perm:[0,0,0,0] row_mask:0xf bank_mask:0xf\n\tv_mfma_f32_4x4x4_16b_bf16 v[84:87], v[96:97], v[104:105], 0\n\t"
                 "v_mfma_f32_4x4x4_16b_bf16 v[88:91], v[96:97], v[106:107], 0\n\tv_and_b32_dpp v96, v94, v102 quad_perm:[0,0,0,0] row_mask:0xf bank_mask:0xf\n\t"
                 "v_and_b32_dpp v97, v94, v103 quad_perm:[0,0,0,0] row_mask:0xf bank_mask:0xf\n\t"
                 "v_mfma_f32_4x4x4_16b_bf16 v[84:87], v[100:101], v[108:109], v[84:87]\n\tv_mfma_f32_4x4x4_16b_bf16 v[88:91], v[100:101], v[110:111], v[88:91]\n\t"
                 "v_and_b32_dpp v100, v95, v102 quad_perm:[0,0,0,0] row_mask:0xf bank_mask:0xf\n\t"
                 "v_and_b32_dpp v101, v95, v103 quad_perm:[0,0,0,0] row_mask:0xf bank_mask:0xf\n\tv_mfma_f32_4x4x4_16b_bf16 v[84:87], v[96:97], v[112:113], v[84:87]\n\t"
                 "v_mfma_f32_4x4x4_16b_bf16 v[88:91], v[96:97], v[114:115], v[88:91]\n\tv_and_b32_dpp v96, v92, v102 quad_perm:[1,1,1,1] row_mask:0xf bank_mask:0xf\n\t"
                 "v_and_b32_dpp v97, v92, v103 quad_perm:[1,1,1,1] row_mask:0xf bank_mask:0xf\n\t"
                 "v_mfma_f32_4x4x4_16b_bf16 v[84:87], v[100:101], v[116:117], v[84:87]\n\tv_mfma_f32_4x4x4_16b_bf16 v[88:91], v[100:101], v[118:119], v[88:91]\n\t"
                 "v_and_b32_dpp v100, v93, v102 quad_perm:[1,1,1,1] row_mask:0xf bank_mask:0xf\n\t"
                 "v_and_b32_dpp v101, v93, v103 quad_perm:[1,1,1,1] row_mask:0xf bank_mask:0xf\n\tv_mfma_f32_4x4x4_16b_bf16 v[84:87], v[96:97], v[120:121], v[84:87]\n\t"
                 "v_mfma_f32_4x4x4_16b_bf16 v[88:91], v[96:97], v[122:123], v[88:91]\n\tv_and_b32_dpp v96, v94, v102 quad_perm:[1,1,1,1] row_mask:0xf bank_mask:0xf\n\t"
                 "v_and_b32_dpp v97, v94, v103 quad_perm:[1,1,1,1] row_mask:0xf bank_mask:0xf\n\t"
                 "v_mfma_f32_4x4x4_16b_bf16 v[84:87], v[100:101], v[124:125], v[84:87]\n\tv_mfma_f32_4x4x4_16b_bf16 v[88:91], v[100:101], v[126:127], v[88:91]\n\t"
                 "v_and_b32_dpp v100, v95, v102 quad_perm:[1,1,1,1] row_mask:0xf bank_mask:0xf\n\t"
                 "v_and_b32_dpp v101, v95, v103 quad_perm:[1,1,1,1] row_mask:0xf bank_mask:0xf\n\tv_mfma_f32_4x4x4_16b_bf16 v[84:87], v[96:97], v[128:129], v[84:87]\n\t"
                 "v_mfma_f32_4x4x4_16b_bf16 v[88:91], v[96:97], v[130:131], v[88:91]\n\tv_and_b32_dpp v96, v92, v102 quad_perm:[2,2,2,2] row_mask:0xf bank_mask:0xf\n\t"
                 "v_and_b32_dpp v97, v92, v103 quad_perm:[2,2,2,2] row_mask:0xf bank_mask:0xf\n\t"
                 "v_mfma_f32_4x4x4_16b_bf16 v[84:87], v[100:101], v[132:133], v[84:87]\n\tv_mfma_f32_4x4x4_16b_bf16 v[88:91], v[100:101], v[134:135], v[88:91]\n\t"
                 "v_and_b32_dpp v100, v93, v102 quad_perm:[2,2,2,2] row_mask:0xf bank_mask:0xf\n\t"
                 "v_and_b32_dpp v101, v93, v103 quad_perm:[2,2,2,2] row_mask:0xf bank_mask:0xf\n\tv_mfma_f32_4x4x4_16b_bf16 v[84:87], v[96:97], v[136:137], v[84:87]\n\t"
                 "v_mfma_f32_4x4x4_16b_bf16 v[88:91], v[96:97], v[138:139], v[88:91]\n\tv_and_b32_dpp v96, v94, v102 quad_perm:[2,2,2,2] row_mask:0xf bank_mask:0xf\n\t"
                 "v_and_b32_dpp v97, v94, v103 quad_perm:[2,2,2,2] row_mask:0xf bank_mask:0xf\n\t"
                 "v_mfma_f32_4x4x4_16b_bf16 v[84:87], v[100:101], v[140:141], v[84:87]\n\tv_mfma_f32_4x4x4_16b_bf16 v[88:91], v[100:101], v[142:143], v[88:91]\n\t"
                 "v_and_b32_dpp v100, v95, v102 quad_perm:[2,2,2,2] row_mask:0xf bank_mask:0xf\n\t"
                 "v_and_b32_dpp v101, v95, v103 quad_perm:[2,2,2,2] row_mask:0xf bank_mask:0xf\n\tv_mfma_f32_4x4x4_16b_bf16 v[84:87], v[96:97], v[144:145], v[84:87]\n\t"
                 "v_mfma_f32_4x4x4_16b_bf16 v[88:91], v[96:97], v[146:147], v[88:91]\n\tv_and_b32_dpp v96, v92, v102 quad_perm:[3,3,3,3] row_mask:0xf bank_mask:0xf\n\t"
                 "v_and_b32_dpp v97, v92, v103 quad_perm:[3,3,3,3] row_mask:0xf bank_mask:0xf\n\t"
                 "v_mfma_f32_4x4x4_16b_bf16 v[84:87], v[100:101], v[148:149], v[84:87]\n\tv_mfma_f32_4x4x4_16b_bf16 v[88:91], v[100:101], v[150:151], v[88:91]\n\t"
                 "v_and_b32_dpp v100, v93, v102 quad_perm:[3,3,3,3] row_mask:0xf bank_mask:0xf\n\t"
                 "v_and_b32_dpp v101, v93, v103 quad_perm:[3,3,3,3] row_mask:0xf bank_mask:0xf\n\tv_mfma_f32_4x4x4_16b_bf16 v[84:87], v[96:97], v[152:153], v[84:87]\n\t"
                 "v_mfma_f32_4x4x4_16b_bf16 v[88:91], v[96:97], v[154:155], v[88:91]\n\tv_and_b32_dpp v96, v94, v102 quad_perm:[3,3,3,3] row_mask:0xf bank_mask:0xf\n\t"
                 "v_and_b32_dpp v97, v94, v103 quad_perm:[3,3,3,3] row_mask:0xf bank_mask:0xf\n\t"
                 "v_mfma_f32_4x4x4_16b_bf16 v[84:87], v[100:101], v[156:157], v[84:87]\n\tv_mfma_f32_4x4x4_16b_bf16 v[88:91], v[100:101], v[158:159], v[88:91]\n\t"
                 "v_and_b32_dpp v100, v95, v102 quad_perm:[3,3,3,3] row_mask:0xf bank_mask:0xf\n\t"
                 "v_and_b32_dpp v101, v95, v103 quad_perm:[3,3,3,3] row_mask:0xf bank_mask:0xf\n\tv_mfma_f32_4x4x4_16b_bf16 v[84:87], v[96:97], v[160:161], v[84:87]\n\t"
                 "v_mfma_f32_4x4x4_16b_bf16 v[88:91], v[96:97], v[162:163], v[88:91]\n\ts_nop 1\n\t"
                 "v_mfma_f32_4x4x4_16b_bf16 v[84:87], v[100:101], v[164:165], v[84:87]\n\tv_mfma_f32_4x4x4_16b_bf16 v[88:91], v[100:101], v[166:167], v[88:91]\n\t"
                 "s_nop 4\n\tv_permlane32_swap_b32 v84, v88\n\tv_permlane32_swap_b32 v85, v89\n\tv_permlane32_swap_b32 v86, v90\n\tv_permlane32_swap_b32 v87, v91\n\t"
                 "v_add_f32 v84, v84, v88\n\tv_add_f32 v85, v85, v89\n\tv_add_f32 v86, v86, v90\n\tv_add_f32 v87, v87, v91\n\ts_nop 0\n\t"
                 "v_permlane16_swap_b32 v84, v85\n\tv_permlane16_swap_b32 v86, v87\n\ts_nop 0\n\tv_add_f32 v84, v84, v85\n\tv_add_f32 v86, v86, v87\n\ts_nop 1\n\t"
                 "v_add_f32_dpp v84, v84, v84 row_ror:8 row_mask:0xf bank_mask:0xf\n\tv_add_f32_dpp v86, v86, v86 row_ror:8 row_mask:0xf bank_mask:0xf\n\ts_nop 0\n\t"
                 "v_add_f32_dpp v84, v84, v84 row_ror:4 row_mask:0xf bank_mask:0xf\n\tv_add_f32_dpp v86, v86, v86 row_ror:4 row_mask:0xf bank_mask:0xf\n\t"
                 "ds_write2_b32 v99, v84, v86 offset1:8\n\ts_setpc_b64 s[92:93]\n\t"
                   :: "{s[0:1]}"(ap + 1), "{s2}"(bid), "{v0}"(wid * 64), KEPT() : "s90", "s91", "scc", "memory");
      __builtin_unreachable();
    }
    {
    constexpr int dk = 8;
    constexpr int BEST = FLG + 40, MEET = FLG + 48;
    __builtin_amdgcn_s_setprio(HPRIO);
    if (hw == 0) STAMP(dk, 0);
    const unsigned tb1 = (unsigned)__builtin_amdgcn_s_memrealtime();
    unsigned* hand2 = hand + HAND_N;
    const __amdgpu_buffer_rsrc_t rsh = __builtin_amdgcn_make_buffer_rsrc(hand2, 0, (HAND_N + PS_N) * 4, 0x00020000);
    const __amdgpu_buffer_rsrc_t rsx = __builtin_amdgcn_make_buffer_rsrc(xout, 0, S_ * D_ * 2, 0x00020000);
    constexpr int OOR = (HAND_N + PS_N) * 4;
    if (!p1blk && hw < 2) {
      f32x2 a[2], c[2];
#pragma unroll
      for (int e = 0; e < 4; ++e) {
        a[e >> 1][e & 1] = (__uint_as_float(pv[0][e] << 16) + __uint_as_float(pv[1][e] << 16)) + (__uint_as_float(pv[2][e] << 16) + __uint_as_float(pv[3][e] << 16));
        c[e >> 1][e & 1] = (__uint_as_float(pv[0][e] & 0xffff0000u) + __uint_as_float(pv[1][e] & 0xffff0000u)) + (__uint_as_float(pv[2][e] & 0xffff0000u) + __uint_as_float(pv[3][e] & 0xffff0000u));
      }
      const float r = (fnum + fold8(a[0], a[1])) * __builtin_amdgcn_rsqf(fvar + fold8(c[0], c[1]));
      unsigned key = (__float_as_uint(r) ^ ((unsigned)(__float_as_int(r) >> 31) | 0x80000000u)) & 0xfffff800u;
      if (r != r) key = 0u;
      key = wave_umax(key | (unsigned)(2047 - (rrow + (lane & 7) * 4 + (lane >> 4))));
      __builtin_amdgcn_raw_buffer_store_b64(u32x2{key, tb1 & 0x7fffffffu}, rsh, lane < NREP ? (HAND_N + PK_O + lane * 128 + (rrow >> 5) * 2) * 4 : OOR, 0, 16);
      if (hw == 1) STAMP(dk, 2);
    }
    gu_setup();
    bool got = true;
    auto get_fc = [&]() __attribute__((always_inline)) {
      const u32x2 fw = __builtin_amdgcn_raw_buffer_load_b64(rsh, (HAND_N + PK_O + (bid % NREP) * 128 + lane * 2) * 4, 0, 16);
      got = none(umax(fw[0], fw[1]) == SENT);
      if (got) {
        const unsigned c1 = wave_umax(fw[0]), c2 = wave_umax(fw[0] == c1 ? 0u : fw[0]);
#define BADFC 0
        const unsigned r1 = (2047u - (c1 & 0x7ffu)) ^ BADFC, r2 = 2047u - (c2 & 0x7ffu);
        unsigned dt = (unsigned)((int)((fw[1] - tb1) << 1) >> 1) + 512u;
        dt = dt < 1024u ? dt : 512u;
        *LDS_AT(volatile u32x2, lbase + SPM + 144) = u32x2{r1 | (r2 << 16), (unsigned)__builtin_amdgcn_readlane(dt, r1 >> 5) | (wave_umax(dt) << 16)};
      }
    };
    if (hw == 0) {
      __builtin_amdgcn_s_sleep(FCPRE);
#pragma unroll 1
      for (int i = 0; i < SPIN_MAX; ++i) {
        if (__builtin_amdgcn_readfirstlane(*LDS_AT(const volatile unsigned, lbase + FLG + 28)) - best >= 4u) break;
        __builtin_amdgcn_s_sleep(FCSLP);
      }
      asm volatile("" ::: "memory");
      get_fc();
      asm volatile("s_waitcnt lgkmcnt(0)" ::: "memory");
    }
    PIN();
    __builtin_amdgcn_s_barrier();
    asm volatile("" ::: "memory");
    PIN();
    const unsigned tb2 = (unsigned)__builtin_amdgcn_s_memrealtime() - 512u;
    LOGDEF;
    LOG();
#pragma unroll 1
    for (int spin = 0; spin < SPIN_MAX && !got; ++spin) { get_fc(); asm volatile("" ::: "memory"); }
    u32x2 fm = {SENT, 0u};
#pragma unroll 1
    for (int i = 0; i < 4 * SPIN_MAX && fm[0] == SENT; ++i) {
      const u32x2 q = *LDS_AT(const volatile u32x2, lbase + SPM + 144);
      fm = u32x2{(unsigned)__builtin_amdgcn_readfirstlane(q[0]), (unsigned)__builtin_amdgcn_readfirstlane(q[1])};
    }
    const int fr1 = fm[0] & 0x7ffu, fr2 = (fm[0] >> 16) & 0x7ffu;
    const unsigned t1 = tb2 + (fm[1] & 1023u), tmx = tb2 + ((fm[1] >> 16) & 1023u);
    auto until = [&](unsigned te) { while ((int)((unsigned)__builtin_amdgcn_s_memrealtime() - te) < 0) __builtin_amdgcn_s_sleep(1); asm volatile("" ::: "memory"); };
    lds_wr(lbase + SPM + 136, tmx);
    PIN();
    if (hw == 0) STAMP(dk, 3);
    const int xoff = (hw * 256 + (lane & 3) * 64 + (lane >> 2) * 4) * 2;
    const int oc = (omi * 2 + (oni >> 3)) * 32, col = oc + (lane & 7) * 4 + (lane >> 4);
    const int xoo = !p1blk && hw == 3 ? (col & ~1) * 2 : S_ * D_ * 2;
    auto get_row = [&](int r, u32x2& xr, unsigned& xo) __attribute__((always_inline)) {
      if ((r >> 7) == omi) {
#pragma unroll 1
        for (int i = 0; i < SPIN_MAX; ++i) {
          if (__builtin_amdgcn_readfirstlane(*LDS_AT(const volatile unsigned, lbase + FLG + 24)) >= 16u) break;
          __builtin_amdgcn_s_sleep(1);
        }
        asm volatile("" ::: "memory");
      }
#pragma unroll 1
      for (int spin = 0; spin < SPIN_MAX; ++spin) {
        LOG();
        xr = __builtin_amdgcn_raw_buffer_load_b64(rsx, xoff, r * (D_ * 2), 16);
        xo = __builtin_amdgcn_raw_buffer_load_b32(rsx, xoo, r * (D_ * 2), 16);
        LOG();
        if (none(umax(umax(xr[0], xr[1]), xo) == SENT)) break;
        __builtin_amdgcn_s_sleep(XRSLP);
        asm volatile("" ::: "memory");
      }
      PIN();
    };
    unsigned rk1;
    auto row_md = [&](const u32x2& xr, int k) __attribute__((always_inline)) {
      auto dot = [&](unsigned x, unsigned y, float z) { return __builtin_amdgcn_fdot2_f32_bf16(__builtin_bit_cast(bf16x2, x), __builtin_bit_cast(bf16x2, y), z, false); };
      const float p1 = dot(xr[1], 0x3f803f80u, dot(xr[0], 0x3f803f80u, 0.f)), p2 = dot(xr[1], xr[1], dot(xr[0], xr[0], 0.f)), p3 = dot(xr[1], wq[1], dot(xr[0], wq[0], 0.f));
      const auto q = __builtin_amdgcn_permlane32_swap(__float_as_uint(p1), __float_as_uint(p2), false, false);
      const auto q3 = __builtin_amdgcn_permlane32_swap(__float_as_uint(p3), __float_as_uint(p3), false, false);
      const auto q2 = __builtin_amdgcn_permlane16_swap(__float_as_uint(__uint_as_float(q[0]) + __uint_as_float(q[1])), __float_as_uint(__uint_as_float(q3[0]) + __uint_as_float(q3[1])), false, false);
      float sv = __uint_as_float(q2[0]) + __uint_as_float(q2[1]);
      sv = dpp_add<0x121, 0xf>(dpp_add<0x122, 0xf>(dpp_add<0x124, 0xf>(dpp_add<0x128, 0xf>(sv))));
      lds_wr(lbase + SPM + k * 64 + (lane >> 4) * 16 + hw * 4, f2word(sv));
      u32x4 a, c, e;
#pragma unroll 1
      for (int i = 0; i < SPIN_MAX; ++i) {
        a = lds_rd4(lbase + SPM + k * 64); c = lds_rd4(lbase + SPM + k * 64 + 32); e = lds_rd4(lbase + SPM + k * 64 + 16);
        if (__builtin_amdgcn_readfirstlane(umax(umax(a[0], a[1]), umax(a[2], a[3]))) != SENT) break;
      }
      auto sum4 = [&](const u32x4& v) { return (__uint_as_float(v[0]) + __uint_as_float(v[1])) + (__uint_as_float(v[2]) + __uint_as_float(v[3])); };
      const float mean = sum4(a) * (1.0f / 1024.0f), msq = sum4(c) * (1.0f / 1024.0f);
      const float den = rbf(__builtin_amdgcn_sqrtf(rbf(rbf(fmaxf(fmaf(-mean, mean, msq), 0.f)) + 1e-5f)));
      if (k == 0) { const float r = fmaf(-rbf(mean), wsum, sum4(e)) * frcp(den); rk1 = __builtin_amdgcn_readfirstlane(pack2(r, r)) >> 16; }
      return (unsigned)__builtin_amdgcn_readfirstlane((pack2(mean, mean) & 0xffffu) | (__float_as_uint(den) & 0xffff0000u));
    };
    auto ffn = [&](const u32x2& xr, unsigned md, unsigned n, const __amdgpu_buffer_rsrc_t rs, int yo, auto&& off) __attribute__((always_inline)) {
      gate_up(xr, md);
      if (hw == 0 && n == 2u) STAMP(dk, 8);
      asm volatile("" ::: "memory");
      PIN();
      lds_wr(lbase + MEET + hw * 4, n + off());
      u32x4 mt;
#pragma unroll 1
      for (int i = 0; i < SPIN_MAX; ++i) {
        mt = lds_rd4(lbase + MEET);
        if (__builtin_amdgcn_readfirstlane(min4(mt)) >= n) break;
      }
      asm volatile("" ::: "memory");
      PIN();
      if (hw == 0 && n == 2u) STAMP(dk, 9);
      if (__builtin_amdgcn_readfirstlane(mt[0]) != n + 1u)
        __builtin_amdgcn_raw_buffer_store_b128(down(act_diag(n == 4u ? 512 : 0), w), rs, (yo + fg * D_) * 4 + t * 16, 0, 16);
      dad += n == 2u ? 512 : -512;
      if (hw == 0 && n == 2u) STAMP(dk, 4);
    };
    auto y_sum4 = [&](const __amdgpu_buffer_rsrc_t rs, int yo, int k) __attribute__((always_inline)) {
      const int src = (yo + (hw * 56 + (lane >> 3)) * D_ + oc + (lane & 7) * 4) * 4;
      u32x4 v[7];
#pragma unroll
      for (int i = 0; i < 7; ++i) asm volatile("" : "=v"(v[i]));
      unsigned todo = 0x7fu;
#pragma unroll 1
      for (int spin = 0; spin < SPIN_MAX; ++spin) {
        LOG();
#pragma unroll
        for (int i = 0; i < 7; ++i) if ((todo >> i) & 1u) v[i] = __builtin_amdgcn_raw_buffer_load_b128(rs, src, i * (8 * D_ * 4), 16);
#pragma unroll
        for (int i = 0; i < 7; ++i) if (none(umax(umax(v[i][0], v[i][1]), umax(v[i][2], v[i][3])) == SENT)) todo &= ~(1u << i);
        if (!todo) break;
        __builtin_amdgcn_s_sleep(YSLP);
        asm volatile("" ::: "memory");
      }
      f32x2 z0 = {0.f, 0.f}, z1 = {0.f, 0.f};
#pragma unroll
      for (int i = 0; i < 7; ++i) {
        z0 += f32x2{__uint_as_float(v[i][0]), __uint_as_float(v[i][1])};
        z1 += f32x2{__uint_as_float(v[i][2]), __uint_as_float(v[i][3])};
      }
      const unsigned ya = lbase + SPM + 256 + k * 512 + ((lane & 7) * 4 + (lane >> 4)) * 16;
      lds_wr((lane & 8) ? lbase + DUM + lane * 4 : ya + hw * 4, f2word(fold8(z0, z1)));
      u32x4 q = {0u, 0u, 0u, 0u};
      if (hw == 3) {
#pragma unroll 1
        for (int i = 0; i < 4 * SPIN_MAX; ++i) {
          q = lds_rd4(ya);
          if (none(umax(umax(q[0], q[1]), umax(q[2], q[3])) == SENT)) break;
        }
      }
      return (__uint_as_float(q[0]) + __uint_as_float(q[1])) + (__uint_as_float(q[2]) + __uint_as_float(q[3]));
    };
    const __amdgpu_buffer_rsrc_t rs1 = __builtin_amdgcn_make_buffer_rsrc(hand, 0, HAND_N * 4, 0x00020000);
    u32x2 xr;
    unsigned xo, xo2;
    until(t1 + XDT);
    get_row(fr1, xr, xo);
    if (hw == 0) STAMP(dk, 7);
#define FAILFC 0
    const unsigned md1 = row_md(xr, 0) ^ (FAILFC & 1);
    if (hw == 0) STAMP(dk, 10);
    LOGAT(20);
    u32x2 xr2;
    unsigned md2 = 0u;
    if (p1blk) {
      xr2 = __builtin_amdgcn_raw_buffer_load_b64(rsx, (fr2 >> 7) == omi ? S_ * D_ * 2 : xoff, fr2 * (D_ * 2), 16);
      PIN();
      ffn(xr, md1, 2u, rsh, GRAN_N, [&]() { return 0u; });
      __builtin_amdgcn_s_setprio(R2PRIO);
      LOG();
      if ((fr2 >> 7) == omi || !none(umax(xr2[0], xr2[1]) == SENT)) get_row(fr2, xr2, xo2);
      md2 = row_md(xr2, 1) ^ (FAILFC & 2);
      ffn(xr2, md2, 4u, rs1, GRAN_N, [&]() {
        const u32x2 v = *LDS_AT(const volatile u32x2, lbase + BEST);
        return v[0] != 0u && (2047 - (int)(v[0] & 0x7ffu) != fr2 || v[1] != md2) ? 1u : 0u; });
    }
    if (hw == 0) STAMP(dk, 5);
    LOG();
    float y = 0.f;
    auto put_row = [&](int r, unsigned md, unsigned rb) __attribute__((always_inline)) {
      const float rden = __uint_as_float(md & 0xffff0000u), rs = rbf(frcp(1.0f + __expf(-__uint_as_float(rb << 16))));
      const float xa = __uint_as_float((col & 1) ? xo & 0xffff0000u : xo << 16);
      float ha = xa, hb = xa;
      ln_apply2(ha, hb, __uint_as_float(md << 16), rden, frcp(rden));
      const float o = xa + rbf(ha + rbf(rbf(y) * rs));
      __builtin_amdgcn_raw_buffer_store_b16((unsigned short)(pack2(o, o) & 0xffffu), __builtin_amdgcn_make_buffer_rsrc(out, 0, hw == 3 ? S_ * D_ * 2 : 0, 0x00020000),
                                            (lane & 8) ? S_ * D_ * 2 : (r * D_ + col) * 2, 0, OAUX);
    };
    if (!p1blk) {
      until(t1 + XDT + YD1); until(tmx + YD2);
      if (hw == 3) STAMP(dk, 13);
      LOGAT(8);
      y = y_sum4(rsh, GRAN_N, 0);
      put_row(fr1, md1, rk1);
      if (hw == 3) STAMP(dk, 14);
      LOGAT(24);
    }
    unsigned bmd = 0u;
    best = 0u;
#pragma unroll 1
    for (int spin = 0; spin < 4 * SPIN_MAX; ++spin) {
      const u32x2 v = *LDS_AT(const volatile u32x2, lbase + BEST);
      best = __builtin_amdgcn_readfirstlane(v[0]); bmd = __builtin_amdgcn_readfirstlane(v[1]);
      if (best) break;
      __builtin_amdgcn_s_sleep(1);
    }
    asm volatile("" ::: "memory");
    const int idx = 2047 - (int)(best & 0x7ffu);
    const unsigned rk = best >> 16, rb = (rk & 0x8000u) ? (rk & 0x7fffu) : (~rk & 0xffffu);
    if (hw == 0) STAMP(dk, 1);
    LOGAT(25);
    LOGOUT(hw);
    if (__builtin_expect(idx != fr1 || bmd != md1 || rb != rk1, 0)) {
      const __amdgpu_buffer_rsrc_t rsf = __builtin_amdgcn_make_buffer_rsrc(gran + GR_F, 0, !p1blk && hw == 3 && idx != fr1 ? 48 * 128 : 0, 0x00020000);
      asm volatile("s_waitcnt vmcnt(0)" ::: "memory");
      __builtin_amdgcn_raw_buffer_store_b32(tag, rsf, lane == 0 ? (16 + (oc >> 5)) * 128 : 48 * 128, 0, 16);
      if (idx != fr1 || bmd != md1) {
        if (!p1blk || idx != fr2 || bmd != md2) get_row(idx, xr, xo);
        if (!p1blk && idx == fr2) md2 = row_md(xr, 1) ^ (FAILFC & 2);
        if (idx == fr2 && bmd == md2) {
          if (!p1blk) y = y_sum4(rs1, GRAN_N, 1);
        } else {
          if (p1blk) ffn(xr, bmd, 6u, rsh, HAND_N + Y3_O, [&]() { return 0u; });
          else y = y_sum4(rsh, HAND_N + Y3_O, 2);
        }
      }
      if (p1blk || hw != 3) return;
#pragma unroll 1
      for (int spin = 0; spin < SPIN_MAX && idx != fr1; ++spin) {
        if (__builtin_amdgcn_readfirstlane(__builtin_amdgcn_raw_buffer_load_b32(rsf, (oc >> 6) * 128, 0, 16)) == tag) break;
        asm volatile("" ::: "memory");
      }
      put_row(idx, bmd, rb);
    }
    if (p1blk || hw != 3) return;
    }
    return;
  }
  const int lane = fresh_lane();
  if (wid >= 4) {
    const int lw = wid - 4;
    const long long lay_ = ap->lay;
    const u16* A = ap->a; const u16* W = ap->W; const u16* wc = ap->wc;
#define LPRIO 3
    __builtin_amdgcn_s_setprio(LPRIO);
    unsigned va[4], vw[6];
#pragma unroll
    for (int i = 0; i < 4; ++i) {
      const int L = (lw * 4 + i) * 64 + lane, row = L >> 3, cp = L & 7, c = cp ^ (((row >> 1) & 3) | (((row >> 4) & 1) << 2));
      va[i] = (unsigned)((m0 + row) * K + c * 8) * 2u;
    }
#pragma unroll
    for (int i = 0; i < 6; ++i) {
      const int L2 = (lw * 6 + i) * 64 + lane, k = L2 / 24, cp = L2 % 24, c = cp ^ (4 * ((k >> 1) & 1)) ^ ((k >> 3) & 1);
      vw[i] = (unsigned)(k * ldw + (c >> 3) * 1024 + n0 + (c & 7) * 8) * 2u;
    }
    PIN();
    asm volatile("" : "+v"(va[0]), "+v"(va[1]), "+v"(va[2]), "+v"(va[3]), "+v"(vw[0]), "+v"(vw[1]), "+v"(vw[2]), "+v"(vw[3]), "+v"(vw[4]), "+v"(vw[5]));
    const bool LAY2 = lay_ != 0;
    const int krot = tmi & 8;
    if (LAY2 && slow_cols(W, tni)) W = wc;
    auto stage_a = [&](int kt, int slot) {
      const char* pa = reinterpret_cast<const char*>(A) + (size_t)(kt ^ krot) * 128;
#pragma unroll
      for (int i = 0; i < 4; ++i) glds16s<true>(pa, va[i], lbase + slot * STB + (lw * 4 + i) * 1024);
    };
    auto stage_w = [&](int kt, int slot) {
      const char* pw = reinterpret_cast<const char*>(W) + (size_t)(kt ^ krot) * 128 * ldw;
#pragma unroll
      for (int i = 0; i < 6; ++i) glds16s<true>(pw, vw[i], lbase + slot * STB + ABYTES + (lw * 6 + i) * 1024);
    };
    if (LAY2) {
#pragma unroll
      for (int st = 0; st < NST; ++st) stage_w(st, st);
      PIN();
      __builtin_amdgcn_s_barrier();
      PIN();
#pragma unroll
      for (int st = 0; st < NST; ++st) stage_a(st, st);
      asm volatile("s_waitcnt vmcnt(8)" ::: "memory");
    } else {
      stage_a(0, 0); stage_w(0, 0);
      asm volatile("s_waitcnt vmcnt(0)" ::: "memory");
    }
    int slot = 0;
    __builtin_amdgcn_s_barrier();
    if (!LAY2) { stage_a(1, 1); stage_w(1, 1); stage_a(NST - 1, NST - 1); stage_w(NST - 1, NST - 1); }
    for (int kt = 0; kt + 1 < NKT; ++kt) {
      if (LAY2 && kt == 0) asm volatile("s_waitcnt vmcnt(4)" ::: "memory");
      else if (kt + 2 >= NKT) asm volatile("s_waitcnt vmcnt(0)" ::: "memory");
      else asm volatile("s_waitcnt vmcnt(10)" ::: "memory");
      __builtin_amdgcn_s_barrier();
      if (kt + NST < NKT) { stage_a(kt + NST, slot); stage_w(kt + NST, slot); }
      slot = slot == NST - 1 ? 0 : slot + 1;
    }
    __builtin_amdgcn_s_setprio(0);
    __builtin_amdgcn_s_barrier();
  } else {
  LAYER_HERE;
  const int dk = LAY2 ? 5 : 1;
  const int wm = wid & 1, wn = wid >> 1;
  if (wid == 0) STAMP(dk, 0);
  f32x4 acc[4][6];
#pragma unroll
  for (int i = 0; i < 4; ++i)
#pragma unroll
    for (int j = 0; j < 6; ++j) acc[i][j] = f32x4{0.f, 0.f, 0.f, 0.f};
  const int q16 = lane & 15, g4 = lane >> 4;
  const int e0 = 16 * (q16 >> 3) + (q16 & 7);
  s16x8 af[2][4], wf[6];
  unsigned aad[2], wad[2];
  {
    const int row = wm * 64 + e0, swz = ((row >> 1) & 3) | (((row >> 4) & 1) << 2);
#pragma unroll
    for (int h = 0; h < 2; ++h) aad[h] = lbase + row * 128 + (((4 * h + g4) ^ swz) << 4);
    const int tq = (lane & 15) >> 2, tp = lane & 3, k = 8 * g4 + tq, ch = wn * 4 + 2 * (tp >> 1);
    wad[0] = lbase + ABYTES + k * 384 + ((ch ^ (4 * ((k >> 1) & 1)) ^ (g4 & 1)) << 4) + 8 * (tp & 1);
    wad[1] = wad[0] ^ 16;
  }
  auto lda = [&](unsigned a, int i) { return *LDS_AT(const s16x8, a + (i >> 1) * 4096 + (i & 1) * 1024); };
  auto ldw_ = [&](int h, int j) {
    const unsigned w = wad[j & 1] + h * 12288 + (j >> 1) * 128;
    s16x4 lo = __builtin_amdgcn_ds_read_tr16_b64_v4i16(LDS_AT(s16x4, w));
    s16x4 h4 = __builtin_amdgcn_ds_read_tr16_b64_v4i16(LDS_AT(s16x4, w + 1536));
    return (s16x8)__builtin_shufflevector(lo, h4, 0, 1, 2, 3, 4, 5, 6, 7);
  };
  unsigned tba, tbw, tal;
  auto taba = [&](int i) { return *LDS_AT(const s16x8, tba + (i >> 1) * 512 + (i & 1) * 128); };
  auto tabw = [&](int j) { return *LDS_AT(const s16x8, tbw + ((j >> 1) * 64 + (j & 1) * 8) * 16); };
  ARGS_HERE;
  const __amdgpu_buffer_rsrc_t rso = __builtin_amdgcn_make_buffer_rsrc(KV, 0, 2 * S_ * D_ * 2, 0x00020000);
  int gto = ((tni * 2 + wn) * 64 + tmi * 4 + wm * 2) * 2048;
  unsigned lto = lbase + (wn * 2 + wm) * 4096, ato = lbase + ALPHA + wm * 256;
  asm volatile("" : "+s"(gto), "+s"(lto), "+s"(ato));
#define QMM(c, i, j, w)                                                                                                \
  do {                                                                                                                 \
    if ((j) >= 4) asm volatile("v_mfma_f32_16x16x32_bf16 %0, %1, %2, %0" : "+v"(acc[i][j]) : "v"(af[c][i]), "v"(wf[w])); \
    else asm volatile("v_mfma_f32_16x16x32_bf16 %0, %1, %2, %0" : "+v"(acc[i][j]) : "v"(wf[w]), "v"(af[c][i]));         \
    PIN();                                                                                                             \
  } while (0)
#pragma unroll
  for (int i = 0; i < 4; ++i)
#pragma unroll
    for (int j = 0; j < 6; ++j) asm volatile("" : "+v"(acc[i][j]));
  asm volatile("" : "+v"(aad[0]), "+v"(aad[1]), "+v"(wad[0]), "+v"(wad[1]));
  if (LAY2) __builtin_amdgcn_s_barrier();
  __builtin_amdgcn_s_barrier();
  PIN();
  if (wid == 0) STAMP(dk, 1);
  af[0][0] = lda(aad[0], 0); wf[0] = ldw_(0, 0); af[0][1] = lda(aad[0], 1); af[0][2] = lda(aad[0], 2); af[0][3] = lda(aad[0], 3);
  wf[1] = ldw_(0, 1); wf[2] = ldw_(0, 2); wf[3] = ldw_(0, 3);
  PIN();
  s16x4 wlo;
  auto col = [&](int c, int J0, int delta) __attribute__((always_inline)) {
    const int h = c / 6, j = c % 6;
    auto rw = [&](int n, int hi) __attribute__((always_inline)) {
      const int nh = n < 12 ? n / 6 : 0, nj = n < 12 ? n % 6 : J0 + n - 12;
      const s16x4 v = __builtin_amdgcn_ds_read_tr16_b64_v4i16(LDS_AT(s16x4, wad[nj & 1] + nh * 12288 + (nj >> 1) * 128 + hi * 1536));
      if (hi) wf[n % 6] = (s16x8)__builtin_shufflevector(wlo, v, 0, 1, 2, 3, 4, 5, 6, 7);
      else wlo = v;
    };
    auto mv = [&](unsigned& x) __attribute__((always_inline)) { x += delta; asm volatile("" : "+v"(x)); };
#pragma unroll
    for (int g = 0; g < 4; ++g) {
      QMM(h, g, j, j);
      if (c < 6 && g < 2) rw(c + 4, g);
      if (c < 4 && g == 2) af[1][c] = lda(aad[1], c);
      if (c == 4 && g >= 2) mv(aad[g - 2]);
      if (c == 5 && g >= 2) rw(10, g - 2);
      if (c == 6 && g < 2) rw(11, g);
      if (c == 6 && g == 2) mv(wad[0]);
      if (c == 7 && g == 0) mv(wad[1]);
      if (c == 8 && g == BARG) {
        asm volatile("" ::: "memory");
        __builtin_amdgcn_s_waitcnt(0xc07f);
        PIN();
        __builtin_amdgcn_s_barrier();
        asm volatile("" ::: "memory");
      }
      if (c == 8 && g >= 2) rw(12, g - 2);
      if (c == 9) af[0][g] = lda(aad[0], g);
      if (c == 10) rw(13 + (g >> 1), g & 1);
      if (c == 11 && g < 2) rw(15, g);
      PIN();
    }
  };
  auto move = [&](int delta) {
    wad[0] += delta; wad[1] += delta; aad[0] += delta; aad[1] += delta;
    asm volatile("" : "+v"(aad[0]), "+v"(aad[1]), "+v"(wad[0]), "+v"(wad[1]));
    PIN();
  };
#pragma unroll
  for (int c = 0; c < 8; ++c) col(c, 0, STB);
  {
    int slot = 1;
#pragma unroll 1
    for (int kt = 0; kt < NKT - NST - 1; ++kt) {
#pragma unroll
      for (int c = 8; c < 12; ++c) col(c, 0, 0);
      const int delta = slot == NST - 1 ? -(NST - 1) * STB : STB;
      slot = slot == NST - 1 ? 0 : slot + 1;
#pragma unroll
      for (int c = 0; c < 8; ++c) col(c, 0, delta);
    }
  }
  static_assert(NKT % NST == 1, "slots of the last stages");
#define TT(i)
  auto tabs = [&]() {
    const int ln = fresh_lane(), e1 = 16 * ((ln >> 3) & 1) + (ln & 7);
    tbw = lbase + TABW + (wn * 32 + e1) * 16;
    tba = ln >= 16 ? lbase + CRED : lbase + TABA + (wm * 64 + e1) * 16;
    asm volatile("" : "+v"(tba), "+v"(tbw));
    PIN();
  };
  auto bar = [&]() { PIN(); __builtin_amdgcn_s_barrier(); asm volatile("" ::: "memory"); PIN(); };
  TT(0);
  TT(1);
#pragma unroll
  for (int c = 8; c < 12; ++c) col(c, 2, 0);
#pragma unroll
  for (int ts = 0; ts < 2; ++ts) {
#pragma unroll
    for (int n = 0; n < 8; ++n) {
      const int m = 8 * ts + n, h = n >> 2, j = 2 + (n & 3);
      auto nxt = [&](int c) { wf[(8 + c) % 6] = ldw_(0, 2 + c); PIN(); };
      auto nxa = [&](int i) { af[0][i] = lda(aad[0], i); PIN(); };
      if (m == 6) {
        TT(2);
        bar();
        TT(3);
        move(STB);
        nxt(0); nxa(0);
      }
      if (m == 7) nxt(1);
      QMM(h, 0, j, m % 6);
      if (n < 4) { wf[(m + 4) % 6] = ldw_(1, j); PIN(); }
      if (m >= 12) { wf[(m + 4) % 6] = ldw_((n >> 1) & 1, n & 1); PIN(); }
      if (m == 6) nxa(1);
      if (m == 7) nxt(2);
      QMM(h, 1, j, m % 6);
      if (n < 2) { af[1][2 * n] = lda(aad[1], 2 * n); PIN(); }
      if (m == 6) nxa(2);
      QMM(h, 2, j, m % 6);
      if (n < 2) { af[1][2 * n + 1] = lda(aad[1], 2 * n + 1); PIN(); }
      if (m == 6) nxa(3);
      if (m == 7) nxt(3);
      QMM(h, 3, j, m % 6);
    }
  }
  int lo_, lok;
  unsigned sig, gv;
  float al[4];
  f32x4 av[4];
  u32x4 kvp[12];
  auto cacc = [&](int p, int x, int t) -> f32x4& { return p < 2 ? acc[x][2 * p + t] : acc[(x >> 1) * 2 + t][4 + (x & 1)]; };
  auto signal = [&](unsigned v) { __hip_atomic_fetch_add(LDS_AT(unsigned, sig), v, __ATOMIC_RELAXED, __HIP_MEMORY_SCOPE_WORKGROUP); PIN(); };
  auto outop = [&](int p, int k) {
    auto ord = [&](int hf) { return p == 1 && hf < 4 ? ((hf & 1) * 2 + (hf >> 1)) : hf; };
    if (k >= 0 && k < 16) {
      const int hk = ord(k >> 1), x = hk >> 1, t = hk & 1;
      f32x4& c = cacc(p, x, t);
#pragma unroll
      for (int o = 2 * (k & 1); o < 2 * (k & 1) + 2; ++o) {
        float r;
        asm volatile("v_mul_f32 %0, %1, %2" : "=v"(r) : "v"(c[o]), "v"(p < 2 ? al[x] : av[(x >> 1) * 2 + t][o]));
        c[o] = r;
      }
      PIN();
    }
    if (k >= 1 && k <= 16) {
      const int hk = ord((k - 1) >> 1), x = hk >> 1, t = hk & 1, o = (k - 1) & 1;
      kvp[p * 4 + x][2 * t + o] = pack2(cacc(p, x, t)[2 * o], cacc(p, x, t)[2 * o + 1]);
      PIN();
      if (t && o) {
        if (p) __builtin_amdgcn_raw_buffer_store_b128(kvp[p * 4 + x], rso, gv + (x >> 1) * 2048 + (x & 1) * (p == 1 ? 64 : 128), 0, 16);
        else *LDS_AT(u32x4, gv + (x >> 1) * 2048 + (x & 1) * 128) = kvp[x];
        PIN();
      }
    }
  };
  s16x8 aq[3][4];
#define QM2(a, i, j, w)                                                                                                \
  do {                                                                                                                 \
    if ((j) >= 4) asm volatile("v_mfma_f32_16x16x32_bf16 %0, %1, %2, %0" : "+v"(acc[i][j]) : "v"(a), "v"(wf[w]));       \
    else asm volatile("v_mfma_f32_16x16x32_bf16 %0, %1, %2, %0" : "+v"(acc[i][j]) : "v"(wf[w]), "v"(a));                \
    PIN();                                                                                                             \
  } while (0)
  static_for<16, 42>([&](auto mc) __attribute__((always_inline)) {
    constexpr int m = decltype(mc)::value;
    auto colj = [&](int c) { return c < 20 ? (c & 1) : (c < 24 ? c - 18 : (c < 32 ? 2 + ((c - 24) >> 2) * 2 + (c & 1) : (c & 1))); };
    auto rdw = [&](int c) { wf[c % 6] = c >= 40 ? tabw(c - 40) : (c < 24 ? tabw(c - 18) : ldw_((c >> 1) & 1, colj(c))); PIN(); };
    if (m == 18) {
      TT(6);
      bar();
      TT(7);
      rdw(20); af[0][0] = taba(0); PIN();
    }
    if (m == 28) TT(8);
    if (m == 32) {
      asm volatile("s_waitcnt lgkmcnt(0)" ::: "memory");
      TT(9);
      bar();
    }
    if (m == 36) TT(10);
    if (m == 38) TT(11);
#pragma unroll
    for (int i = 0; i < 4; ++i) {
      const int mq = m >= 38 ? 38 + i : m, iq = m >= 38 ? m - 38 : i, j = colj(mq);
      if (mq < 24) QM2(af[mq < 20 ? (mq >> 1) & 1 : 0][iq], iq, j, mq % 6);
      else if (mq < 36) QM2(af[1 - ((mq >> 1) & 1)][iq], iq, j, mq % 6);
      else QM2(aq[mq < 36 ? 0 : (mq - 36) >> 1][iq], iq, j, mq % 6);
      if (m == 16 && i == 0) move(-(NST - 1) * STB);
      if (m == 17 && i == 0) tabs();
      if (m == 18 && i < 3) { af[0][i + 1] = taba(i + 1); PIN(); }
      if (m == 18 && i == 3) rdw(21);
      if (m == 19 && (i == 0 || i == 2)) rdw(22 + (i >> 1));
      if (m == 20) { af[1][i] = lda(aad[0], i); PIN(); }
      if ((m == 20 || m == 24) && i == 3) rdw(m + 4);
      if (m >= 21 && m <= 29 && m != 24 && i == 0) rdw(m + 4);
      if (m == 24) { af[0][i] = lda(aad[1], i); PIN(); }
      if ((m == 29 || m == 30) && i == 3) rdw(m + 5);
      if (m == 31 && i == 3) { wad[0] += STB; wad[1] += STB; asm volatile("" : "+v"(wad[0]), "+v"(wad[1])); PIN(); }
      if (m == 25 && i == 3) { aad[0] += STB; aad[1] += STB; asm volatile("" : "+v"(aad[0]), "+v"(aad[1])); PIN(); }
      if (m >= 32 && m <= 36 && i == 0) rdw(m + 4);
      if (m == 36 && i == 2) rdw(41);
      if (m == 34 || m == 36) { aq[(m - 34) >> 1][i] = lda(aad[(m - 34) >> 1], i); PIN(); }
      if (m == 36 && i == 3) signal(0x10000u);
      if (m == 33 && i == 2) tabs();
      if (m == 35) { aq[2][i] = taba(i); PIN(); }
      if (m == 23 && i == 1) {
        const int ln = fresh_lane();
        tal = ato + (16 * ((ln >> 3) & 1) + (ln & 7)) * 4;
        asm volatile("" : "+v"(tal));
        PIN();
      }
      if (m == 26 && i == 0) {
#pragma unroll
        for (int x = 0; x < 4; ++x) al[x] = *LDS_AT(const float, tal + ((x >> 1) * 32 + (x & 1) * 8) * 4);
        PIN();
      }
      if (m == 27 && i == 0) {
        const int ln = fresh_lane();
        gv = gto + (ln >> 5) * 1024 + ((ln & 28) * 2 + (ln & 3)) * 16;
        tal = ato + (ln >> 4) * 16 + (ln >> 5) * 32;
        asm volatile("" : "+v"(tal), "+v"(gv));
        PIN();
      }
      if (m == 32 && i == 1) {
        const int ln = fresh_lane();
        lok = gv - gto + lto;
        lo_ = (ln >> 5) * 1024 + ((ln & 24) * 2 + (ln & 7)) * 16;
        gv = gto + VOFF + lo_;
        asm volatile("" : "+v"(gv), "+v"(lo_), "+v"(lok));
        PIN();
      }
      if (m == 36 && i == 1) {
        const int ln = fresh_lane();
        lo_ += lto; gv = lo_ + QOFF;
        sig = ln == 0 ? lbase + FLG + 32 : lbase + DUM + ln * 4;
        asm volatile("" : "+v"(lo_), "+v"(sig), "+v"(gv));
        PIN();
      }
      if ((m == 30 && i == 2) || (m == 33 && i == 1)) {
#pragma unroll
        for (int x = 2 * (m & 1); x < 2 * (m & 1) + 2; ++x) av[x] = *LDS_AT(const f32x4, tal + ((x >> 1) * 32 + (x & 1) * 8) * 4);
        PIN();
      }
      outop(1, (m - 28) * 4 + i);
      outop(2, (m - 32) * 4 + i);
      {
        constexpr int wg[8] = WGAPS;
#pragma unroll
        for (int e = 0; e < 8; ++e)
          if ((m - 32) * 4 + i == wg[e]) {
            *LDS_AT(u32x4, (e < 4 ? lok + (e & 1) * 64 : lo_ + 2048 + (e & 1) * 128) + ((e >> 1) & 1) * ASB) = kvp[4 + e];
            PIN();
          }
      }
      if (m >= 40) outop(0, (m - 40) * 4 + i - QOV);
      if (m == 37 && i == 0) {
#pragma unroll
        for (int x = 0; x < 4; ++x) al[x] *= QSCALE;
        asm volatile("" : "+v"(al[0]), "+v"(al[1]), "+v"(al[2]), "+v"(al[3]));
        PIN();
      }
    }
  });
#undef QM2
#undef QMM
  TT(13);
  if (wid == 0) STAMP(dk, 18);
#pragma unroll
  for (int k = 8 - QOV; k <= 16; ++k) {
    if (k == 4 * FLAT + 1) { asm volatile("s_waitcnt vmcnt(0)" ::: "memory"); signal(1u); }
    outop(0, k);
  }
  __hip_atomic_fetch_add(LDS_AT(unsigned, sig - (fresh_lane() == 0 ? 16 : 0)), 1u, __ATOMIC_RELAXED, __HIP_MEMORY_SCOPE_WORKGROUP);
  TT(14);
  if (FLAT >= 4) { asm volatile("s_waitcnt vmcnt(0)" ::: "memory"); signal(1u); }
  TT(15);
  if (wid == 0) STAMP(dk, 19);
  }
  PIN();
  {
  const int lane = fresh_lane(), tid = wid * 64 + lane;
  LAYER_HERE;
  const int dk = LAY2 ? 6 : 2;
  auto wait_arrivals = [&](unsigned n) {
#pragma unroll 1
    for (int i = 0; i < SPIN_MAX; ++i) {
      if (__builtin_amdgcn_readfirstlane(*LDS_AT(const volatile unsigned, lbase + FLG + 16)) >= n) break;
      __builtin_amdgcn_s_sleep(0);
    }
    asm volatile("" ::: "memory");
    PIN();
  };
  wait_arrivals(4u);
  const int g = wid >> 2, hh = (wid >> 1) & 1, qg = wid & 1;
  const int head = tni * 2 + hh, qt = tmi * 4 + qg * 2;
  const unsigned qad = lbase + QOFF + (hh * 2 + qg) * 4096 + lane * 16;
  const s16x8 qa0 = *LDS_AT(const s16x8, qad + (g ? 0 : 2048)), qa1 = *LDS_AT(const s16x8, qad + (g ? 1024 : 3072));
  const s16x8 qb0 = *LDS_AT(const s16x8, qad + (g ? 2048 : 0)), qb1 = *LDS_AT(const s16x8, qad + (g ? 3072 : 1024));
  const unsigned wbase = lbase + (hh * 2 + g) * 4096 + lane * 16;
  auto sptr = [&](int sl) { unsigned a = wbase + sl * ASB; asm volatile("" : "+v"(a)); return LDS_AT(const s16x8, a); };
  auto rd = [&](__attribute__((address_space(3))) const s16x8* sp, int off) { return sp[off >> 4]; };
  auto sl0 = sptr(0);
  s16x8 k00 = rd(sl0, 0), k01 = rd(sl0, 1024);
  const unsigned tag = __builtin_amdgcn_readfirstlane(*LDS_AT(const volatile unsigned, lbase + FLG + 20));
  const int idx = LAY2 ? (2047 - (int)(__builtin_amdgcn_readfirstlane(*LDS_AT(const volatile unsigned, lbase + FLG + 28)) & 0xffffu)) & 2047 : -64;
  const unsigned arr_a = lane == 0 ? lbase + FLG + 16 : lbase + DUM + lane * 4;
  auto arrive = [&]() { __hip_atomic_fetch_add(LDS_AT(unsigned, arr_a), 1u, __ATOMIC_RELAXED, __HIP_MEMORY_SCOPE_WORKGROUP); };
  const unsigned arr_w = lane == 0 ? lbase + FLG + 8 + g * 4 : lbase + DUM + lane * 4;
  const unsigned arr_i = 1u << (8 * (wid & 3));
  auto arrive_w = [&]() { __hip_atomic_fetch_add(LDS_AT(unsigned, arr_w), arr_i, __ATOMIC_RELAXED, __HIP_MEMORY_SCOPE_WORKGROUP); };
  s16x8 sel;
#pragma unroll
  for (int e = 0; e < 8; ++e) sel[e] = ((lane & 7) == (e & 4)) ? (short)0x3F80 : (short)0;
  int sidx = 0xDDDDDDDD;
  asm volatile("" : "+v"(sidx));
  f32x16 oa, ob, nn, sa, sb;
  f32x4 la = {0.f, 0.f, 0.f, 0.f}, lb = {0.f, 0.f, 0.f, 0.f};
#pragma unroll
  for (int e = 0; e < 16; ++e) { oa[e] = 0.f; ob[e] = 0.f; sa[e] = 0.f; sb[e] = 0.f; }
  float ma;
  const float zoff = __uint_as_float(__builtin_amdgcn_readfirstlane(LAY2 && g == ((idx >> 6) & 1) ? 0u : 0xff800000u));
  {
    sa = MFMA32(k00, qa0, sa); sa = MFMA32(k01, qa1, sa);
    sb = MFMA32(k00, qb0, sb); sb = MFMA32(k01, qb1, sb);
    float mb = sb[0];
    ma = sa[0];
#pragma unroll
    for (int e = 1; e < 16; ++e) { ma = fmaxf(ma, sa[e]); mb = fmaxf(mb, sb[e]); }
    ma = fmaxf(ma, mb);
    ma = fmaxf(ma, __shfl_xor(ma, 32, 64));
#pragma unroll
    for (int e = 0; e < 16; ++e) { nn[e] = -ma; sa[e] -= ma; sb[e] = 0.f; }
    la[0] = lb[0] = -__uint_as_float(__float_as_uint(__builtin_amdgcn_exp2f(zoff - ma)) & 0xffff0000u);
  }
#define SB() __builtin_amdgcn_sched_barrier(0)
#define EX(i) sc[i] = __builtin_amdgcn_exp2f(sc[i])
  auto unit = [&](f32x16& sc, f32x16& sn, const s16x8& kn0, const s16x8& kn1, const s16x8& qn0, const s16x8& qn1,
                  f32x16& op, f32x4& lp, const s16x8& vp,
                  f32x16& oc, f32x4& lc, const s16x8& vc, auto&& mid) {
    op = SMF32(vp, PHI(sn), op); SB();
    EX(0); EX(1); EX(2); SB();
    lp = SMF16(sel, PHI(sn), lp); SB();
    EX(3); EX(4); SB();
    sn = MFMA32(kn0, qn0, nn); SB();
    EX(5); EX(6); EX(7); SB();
    sn = MFMA32(kn1, qn1, sn); SB();
    mid();
    EX(8); EX(9); EX(10); SB();
    oc = SMF32(vc, PLO(sc), oc); SB();
    EX(11); EX(12); EX(13); SB();
    lc = SMF16(sel, PLO(sc), lc); SB();
    EX(14); EX(15); SB();
    asm volatile("" : "+v"(sc));
  };
  s16x8 v00, v01, v10, v11, k10, k11;
#pragma unroll
  for (int e = 0; e < 8; ++e) v11[e] = 0;
  v00 = rd(sl0, 2048); v01 = rd(sl0, 3072);
  sl0 = sptr(1);
  unsigned f0 = 0u;
  auto check = [&](unsigned need) {
    if (__builtin_expect(__builtin_amdgcn_readfirstlane(f0) < need, 0)) {
#pragma unroll 1
      for (int i = 0; i < SPIN_MAX; ++i) {
        if (__builtin_amdgcn_readfirstlane(*LDS_AT(const volatile unsigned, lbase + FLG)) >= need) break;
        __builtin_amdgcn_s_sleep(1);
      }
    }
    asm volatile("" ::: "memory");
  };
#define PAT 0x22221110
#define SAW8(i) do { if (((PAT >> (28 - 4 * (i))) & 3) != ((PAT >> ((32 - 4 * (i)) & 31)) & 3)) { __builtin_amdgcn_s_setprio((PAT >> (28 - 4 * (i))) & 3); SB(); } } while (0)
#define SAW(i) do { if (odd) SAW8(4 + (i)); else SAW8(i); } while (0)
  auto three = [&](int st, bool odd) {
    SAW(0);
    unit(sa, sb, k00, k01, qb0, qb1, ob, lb, v11, oa, la, v00, [&]() { k10 = rd(sl0, 0); k11 = rd(sl0, 1024); SB(); });
    SAW(1);
    unit(sb, sa, k10, k11, qa0, qa1, oa, la, v01, ob, lb, v00, [&]() { v10 = rd(sl0, 2048); v11 = rd(sl0, 3072); SB(); });
    sl0 = sptr((2 * st + 2) & 7);
    SB();
    SAW(2);
    unit(sa, sb, k10, k11, qb0, qb1, ob, lb, v01, oa, la, v10, [&]() { k00 = rd(sl0, 0); k01 = rd(sl0, 1024); SB(); });
  };
  auto fourth = [&](int st, bool odd) {
    SAW(3);
    unit(sb, sa, k00, k01, qa0, qa1, oa, la, v11, ob, lb, v10, [&]() {
      v00 = rd(sl0, 2048); v01 = rd(sl0, 3072);
      f0 = *LDS_AT(const volatile unsigned, lbase + FLG);
      SB();
      arrive_w();
      SB(); });
    check(st >= 14 ? 32u : 2u * st + 5u);
    sl0 = sptr((2 * st + 3) & 7);
    SB();
  };
  SB();
  SAW8(0);
  unit(sa, sb, k00, k01, qb0, qb1, ob, lb, v11, oa, la, v00, [&]() { k10 = rd(sl0, 0); k11 = rd(sl0, 1024); SB(); });
  SAW8(1);
  unit(sb, sa, k10, k11, qa0, qa1, oa, la, v01, ob, lb, v00, [&]() { v10 = rd(sl0, 2048); v11 = rd(sl0, 3072); SB(); });
  sl0 = sptr(2);
  SB();
  SAW8(2);
  unit(sa, sb, k10, k11, qb0, qb1, ob, lb, v01, oa, la, v10, [&]() {});
  check(3u); k00 = rd(sl0, 0); k01 = rd(sl0, 1024); SB();
  SAW8(3);
  unit(sb, sa, k00, k01, qa0, qa1, oa, la, v11, ob, lb, v10, [&]() { v00 = rd(sl0, 2048); v01 = rd(sl0, 3072); arrive_w(); SB(); });
  check(5u);
  sl0 = sptr(3);
  SB();
  three(1, true);
#pragma unroll 1
  for (int st = 2; st < 16; st += 2) { fourth(st - 1, true); three(st, false); fourth(st, false); three(st + 1, true); }
  SAW8(7);
  unsigned vr, tk, tv;
  u32x4 f4;
  auto table = [&]() {
    f4 = lds_rd4(lbase + FLG);
    k00 = *LDS_AT(const volatile s16x8, tk); k01 = *LDS_AT(const volatile s16x8, tk + 32);
    vr = *LDS_AT(const volatile u16, tv);
  };
  unsigned xw, xm;
  u32x2 ml;
  u32x4 o2[4];
  auto take = [&]() {
    ml = *LDS_AT(const volatile u32x2, xm);
#pragma unroll
    for (int e = 0; e < 4; ++e) o2[e] = lds_rd4(xw + e * 1024);
  };
  float pzt;
  int dso;
  f32x16& sc = sb;
#define EXP() do { asm volatile("" : "+v"(sc)); SB(); } while (0)
  oa = SMF32(v11, PHI(sa), oa); SB();
  tk = lbase + TAB + 128 + hh * 64 + (lane >> 5) * 16; tv = lbase + TAB + 256 + hh * 64 + (lane & 31) * 2;
  table();
  SB();
  EX(0); EX(1); EXP();
  la = SMF16(sel, PHI(sa), la); SB();
  EX(2); EX(3); EX(4); EXP();
  xw = wbase + (qg ? QOFF : 6 * ASB); xm = lbase + XML + wid * 512 + lane * 8;
  pzt = __builtin_amdgcn_exp2f(nn[0] + (zoff - 8.f));
  asm volatile("" : "+v"(xw), "+v"(xm), "+v"(pzt), "+v"(f4));
  SB();
  {
    const unsigned ph = LAY2 ? 0x79797979u : 0x80808080u, pg = qg ? 0u : 0x80u << (16 * hh + 8);
    auto ready = [&]() {
      return (((__builtin_amdgcn_readfirstlane(f4[1]) + ph) & 0x80808080u) == 0x80808080u) & (((__builtin_amdgcn_readfirstlane(g ? f4[3] : f4[2]) + 0x71717171u) & pg) == pg);
    };
    if (__builtin_expect(!ready(), 0)) {
#pragma unroll 1
      for (int i = 0; i < SPIN_MAX; ++i) {
        __builtin_amdgcn_s_sleep(1);
        table();
        if (ready()) break;
      }
    }
    asm volatile("" ::: "memory");
  }
  SB();
  sa = MFMA32(k00, qb0, nn); asm volatile("" : "+v"(sa)); SB();
#pragma unroll
  for (int e = 0; e < 4; ++e) *LDS_AT(volatile f32x4, xw + e * 1024) = f32x4{oa[4 * e], oa[4 * e + 1], oa[4 * e + 2], oa[4 * e + 3]};
  *LDS_AT(volatile u32x2, xm) = u32x2{__float_as_uint(ma), f2word(pzt <= la[0] ? la[0] : 0.f)};
  SB();
  EX(5); EX(6); EX(7); EXP();
  sa = MFMA32(k01, qb1, sa); asm volatile("" : "+v"(sa), "+v"(vr)); SB();
  ARGS_HERE;
  EX(8); EX(9); EX(10); EXP();
  ob = SMF32(v10, PLO(sb), ob); SB();
  EX(11); EX(12); EX(13); EXP();
  lb = SMF16(sel, PLO(sb), lb); SB();
  const bool used = !LAY2 || (qt + g) * 32 + (lane & 31) != idx;
  dso = used ? (((qt + g) * 32 + head) * 1024 + lane * 8) * 2 : S_ * D_ * 2;
  xw += g ? -4096 : 4096; xm += g ? -2048 : 2048;
  asm volatile("" : "+v"(dso), "+v"(xw), "+v"(xm));
  SB();
  EX(14); EX(15); EXP();
  take();
  SB();
  lb = SMF16(sel, PHI(sb), lb); SB();
  ob = SMF32(v11, PHI(sb), ob); SB();
  __builtin_amdgcn_s_setprio(EPRIO);
  TE(0, 0); TE(1, 0); TE(2, 0); TE(3, 0); TE(7, 0);
  float lsb = lb[0];
  if (LAY2) {
    sa[0] = __builtin_amdgcn_exp2f(sa[0]);
    ob = SMF32((u32x4{lane < 32 ? vr : 0u, 0u, 0u, 0u}), PLO(sa), ob);
    lsb += __uint_as_float(__float_as_uint(sa[0]) & 0xffff0000u);
    lsb = pzt <= lsb ? lsb : 0.f;
  }
  const __amdgpu_buffer_rsrc_t rso = __builtin_amdgcn_make_buffer_rsrc(Of, 0, S_ * D_ * 2, 0x00020000);
  TE(0, 1);
  if (__builtin_amdgcn_ballot_w64(ml[1] == SENT) != 0ull) {
#pragma unroll 1
    for (int i = 0; i < SPIN_MAX; ++i) {
      __builtin_amdgcn_s_sleep(XSLP);
      if (!__builtin_amdgcn_ballot_w64(*LDS_AT(const volatile unsigned, xm + 4) == SENT)) break;
    }
    asm volatile("s_waitcnt lgkmcnt(0)" ::: "memory");
    take();
  }
  arrive();
  TE(0, 2);
  TE(0, 3); TE(1, 1);
  {
    const float m2 = __uint_as_float(ml[0]), l2 = __uint_as_float(ml[1]);
    const float mx = fmaxf(ma, m2);
    const float f1 = __builtin_amdgcn_exp2f(ma - mx), f2 = __builtin_amdgcn_exp2f(m2 - mx);
    const float inv = frcp(lsb * f1 + l2 * f2);
    const float c1 = f1 * inv, c2 = f2 * inv;
    f32x2 r[8];
#pragma unroll
    for (int e = 0; e < 8; ++e) {
      const unsigned x0 = o2[e >> 1][(e & 1) * 2], x1 = o2[e >> 1][(e & 1) * 2 + 1];
      r[e] = f32x2{__uint_as_float(x0) * c2, __uint_as_float(x1) * c2};
    }
#pragma unroll
    for (int e = 0; e < 8; ++e) r[e] = f32x2{ob[2 * e], ob[2 * e + 1]} * f32x2{c1, c1} + r[e];
    auto put = [&]() {
#pragma unroll
      for (int sx = 0; sx < 2; ++sx)
        __builtin_amdgcn_raw_buffer_store_b128(u32x4{pack2(r[4 * sx][0], r[4 * sx][1]), pack2(r[4 * sx + 1][0], r[4 * sx + 1][1]),
            pack2(r[4 * sx + 2][0], r[4 * sx + 2][1]), pack2(r[4 * sx + 3][0], r[4 * sx + 3][1])}, rso, dso + sx * 1024, 0, 16);
    };
    put();
    PIN();
    __builtin_amdgcn_s_setprio(0);
    PIN();
    f32x2 chk = {lsb - __builtin_amdgcn_fmed3f(lsb, 8.6736174e-19f, 1.2676506e30f), l2 - __builtin_amdgcn_fmed3f(l2, 8.6736174e-19f, 1.2676506e30f)};
#pragma unroll
    for (int e = 0; e < 8; ++e) chk = r[e] * f32x2{0.f, 0.f} + chk;
    if (__builtin_expect(__builtin_amdgcn_ballot_w64(used && !(chk[0] + chk[1] == 0.f)) != 0ull, 0)) {
      float l;
      attn_safe(KV, head * 64, qb0, qb1, ob, l, LAY2 ? head * 64 + (idx >> 5) : -1, idx & 31, lbase + TAB + 128 + hh * 64);
      const float inv2 = 1.0f / l;
#pragma unroll
      for (int e = 0; e < 8; ++e) r[e] = f32x2{ob[2 * e] * inv2, ob[2 * e + 1] * inv2};
      asm volatile("s_waitcnt vmcnt(0)" ::: "memory");
      put();
    }
    unsigned pp = 0u;
    if (LAY2) {
      const unsigned ua = lbase + UGL + hh * 128 + (lane >> 5) * 16;
      f32x2 du = {0.f, 0.f}, dn = {0.f, 0.f};
#pragma unroll
      for (int q = 0; q < 4; ++q) {
        const f32x4 uu = *LDS_AT(const f32x4, ua + q * 32), gg = *LDS_AT(const f32x4, ua + 256 + q * 32);
        du = r[2 * q] * f32x2{uu[0], uu[1]} + du; du = r[2 * q + 1] * f32x2{uu[2], uu[3]} + du;
        const f32x2 t0 = r[2 * q] * f32x2{gg[0], gg[1]}, t1 = r[2 * q + 1] * f32x2{gg[2], gg[3]};
        dn = t0 * t0 + dn; dn = t1 * t1 + dn;
      }
      const auto a = __builtin_amdgcn_permlane32_swap(__float_as_uint(du[0] + du[1]), __float_as_uint(du[0] + du[1]), false, false);
      const auto c = __builtin_amdgcn_permlane32_swap(__float_as_uint(dn[0] + dn[1]), __float_as_uint(dn[0] + dn[1]), false, false);
      pp = f2word(__uint_as_float(pack2(__uint_as_float(a[0]) + __uint_as_float(a[1]), __uint_as_float(c[0]) + __uint_as_float(c[1]))));
    }
    __builtin_amdgcn_raw_buffer_store_b32(pp, __builtin_amdgcn_make_buffer_rsrc(hand + 2 * HAND_N + PP_O, 0, LAY2 ? H_ * S_ * 4 : 0, 0x00020000),
                                          lane < 32 ? (head * S_ + (qt + g) * 32 + lane) * 4 : H_ * S_ * 4, 0, 16);
    PIN();
  }
  TE(1, 2);
  u32x4 xres[2];
  oproj_tail(LAY2, tid, lbase, lbase + FLG, lbase + RED, lbase + SPM + 144, omi, oni, idx, gran + GR_O + ((LAY2 ? 16 : 0) + tmi) * (64 * RO) + tni,
             gran + GR_X + ((LAY2 ? 16 : 0) + omi) * (32 * RX) + 16 + oni, tag, Of, xout, A, w_r, stats_out, stats, hand, xres TEPASS);
  if (!LAY2) {
    asm volatile("s_waitcnt vmcnt(0) lgkmcnt(0)" ::: "memory");
    PIN();
    __builtin_amdgcn_s_barrier();
    PIN();
  } else {
    const int row = omi * 128 + (wid & 3) * 32 + (wid >> 2) * 16 + (lane >> 3), col = oni * 64 + (lane & 7) * 8;
    unsigned md[2], best = 0u;
    const __amdgpu_buffer_rsrc_t rsk = __builtin_amdgcn_make_buffer_rsrc(hand + HAND_N, 0, (HAND_N + PS_N) * 4, 0x00020000);
    f32x2 xf[2][4];
#pragma unroll
    for (int p = 0; p < 2; ++p)
#pragma unroll
      for (int e = 0; e < 4; ++e) {
        xf[p][e] = f32x2{__uint_as_float(xres[p][e] << 16), __uint_as_float(xres[p][e] & 0xffff0000u)};
        asm volatile("" : "+v"(xf[p][e]));
      }
    LOGDEF;
    LOG();
    const bool rtr = (oni & 7) == 7;
    {
      const int rl = (wid & 3) * 32 + (wid >> 2) * 16 + (lane & 15), rr = omi * 128 + rl;
      int src = (HAND_N + ((omi * 16 + (lane >> 4) * 4) * 128 + rl) * 4) * 4;
      int ko = rtr && (wid >> 2) == (oni >> 3) && lane < NREP ? lane * 1024 + (rr >> 4) * 8 : (HAND_N + PS_N) * 4;
      int bp = (lane >> 3) * 4;
      const float wsum = *LDS_AT(const volatile float, lbase + SPM + 132);
      asm volatile("" : "+v"(src), "+v"(ko), "+v"(bp));
      u32x4 v[4];
      __builtin_amdgcn_s_sleep(MPRE);
#pragma unroll 1
      for (int spin = 0; spin < SPIN_MAX; ++spin) {
        LOG();
#pragma unroll
        for (int i = 0; i < 4; ++i) v[i] = __builtin_amdgcn_raw_buffer_load_b128(rsk, src + (i & 1) * 2048, (i >> 1) * 4096, 16);
        unsigned mx = 0u;
#pragma unroll
        for (int i = 0; i < 4; ++i) mx = umax(umax(umax(mx, v[i][0]), v[i][1]), v[i][2]);
        if (none(mx == SENT)) break;
        __builtin_amdgcn_s_sleep(MSLP);
        asm volatile("" ::: "memory");
      }
      LOGAT(12);
      if (rtr) __builtin_amdgcn_s_setprio(3); else __builtin_amdgcn_s_setprio(LNPRIO);
      double sm[3];
#pragma unroll
      for (int c = 0; c < 3; ++c) {
        const double a = ((double)__uint_as_float(v[0][c]) + (double)__uint_as_float(v[1][c])) + ((double)__uint_as_float(v[2][c]) + (double)__uint_as_float(v[3][c]));
        u32x2 q = __builtin_bit_cast(u32x2, a);
        const auto lo = __builtin_amdgcn_permlane32_swap(q[0], q[0], false, false), hi = __builtin_amdgcn_permlane32_swap(q[1], q[1], false, false);
        q = __builtin_bit_cast(u32x2, __builtin_bit_cast(double, u32x2{lo[0], hi[0]}) + __builtin_bit_cast(double, u32x2{lo[1], hi[1]}));
        const auto l2 = __builtin_amdgcn_permlane16_swap(q[0], q[0], false, false), h2 = __builtin_amdgcn_permlane16_swap(q[1], q[1], false, false);
        sm[c] = __builtin_bit_cast(double, u32x2{l2[0], h2[0]}) + __builtin_bit_cast(double, u32x2{l2[1], h2[1]});
      }
      float mean, var, mb, den;
      ln_from_sums(sm[0], sm[1], mean, var, mb, den);
      const unsigned mdr = f2word(__uint_as_float((__float_as_uint(mb) >> 16) | (__float_as_uint(den) & 0xffff0000u)));
      md[0] = __builtin_amdgcn_ds_bpermute(bp, mdr); md[1] = __builtin_amdgcn_ds_bpermute(bp + 32, mdr);
      if (rtr) {
        const float r = (float)fma(-(double)mb, (double)wsum, sm[2]) * frcp(den);
        const unsigned rb = pack2(r, r);
        unsigned rkey = (rb ^ ((unsigned)((int)rb >> 31) | 0x80000000u)) & 0xffff0000u;
        if (r != r) rkey = 0u;
        const unsigned top = wave_umax(rkey | (unsigned)(2047 - rr));
        const unsigned mdt = __builtin_amdgcn_readlane(mdr, (2047 - (int)(top & 0xffffu)) & 15);
        __builtin_amdgcn_raw_buffer_store_b64(u32x2{top, mdt}, rsk, ko, 0, 16);
        PIN();
        __builtin_amdgcn_s_setprio(LNPRIO);
        __hip_atomic_fetch_add(LDS_AT(unsigned, lane == 0 ? lbase + SPM + 128 : lbase + DUM + lane * 4), 1u, __ATOMIC_RELAXED, __HIP_MEMORY_SCOPE_WORKGROUP);
        if (wid == 2 || wid == 5) STAMP(8, 6);
      }
      LOGAT(13);
    }
    auto keys = [&](const u32x4& v) __attribute__((always_inline)) {
      if (none(umax(umax(v[0], v[1]), umax(v[2], v[3])) == SENT)) {
        best = wave_umax(umax(v[0], v[2])) | 0x8000u;
        *LDS_AT(volatile u32x2, lbase + FLG + 40) = u32x2{best, (unsigned)__builtin_amdgcn_readlane(v[0] > v[2] ? v[1] : v[3], ((2047 - (int)(best & 0x7ffu)) >> 5) & 63)};
        STAMP(8, 11);
      }
    };
    auto ln_rows = [&]() __attribute__((always_inline)) {
#pragma unroll
      for (int p = 0; p < 2; ++p) {
        const float mb_ = __uint_as_float(md[p] << 16), den = __uint_as_float(md[p] & 0xffff0000u), rin = frcp(den);
#pragma unroll
        for (int e = 0; e < 4; ++e) {
          f32x2 h = xf[p][e] - f32x2{mb_, mb_};
          unsigned u = pack2(h[0], h[1]);
          h = f32x2{__uint_as_float(u << 16), __uint_as_float(u & 0xffff0000u)};
          f32x2 q = h * f32x2{rin, rin};
          if (REFINE) q = (h - q * f32x2{den, den}) * f32x2{rin, rin} + q;
          u = pack2(q[0], q[1]);
          h = xf[p][e] + f32x2{__uint_as_float(u << 16), __uint_as_float(u & 0xffff0000u)};
          xres[p][e] = pack2(h[0], h[1]);
        }
      }
      asm volatile("" : "+v"(xres[0]), "+v"(xres[1]));
    };
    if (wid == 0) STAMP(8, 12);
    ln_rows();
    LOGAT(14);
    const __amdgpu_buffer_rsrc_t rsu = __builtin_amdgcn_make_buffer_rsrc(out, 0, S_ * D_ * 2, 0x00020000);
    const unsigned fr1 = __builtin_amdgcn_readfirstlane(*LDS_AT(const volatile unsigned, lbase + SPM + 144)) & 0x7ffu;
    unsigned tmx = __builtin_amdgcn_readfirstlane(*LDS_AT(const volatile unsigned, lbase + SPM + 136)) + KDT;
    if (tmx - (unsigned)__builtin_amdgcn_s_memrealtime() > 1024u) tmx = 0u;
    auto due = [&]() { return tmx == 0u || (int)((unsigned)__builtin_amdgcn_s_memrealtime() - tmx) >= 0; };
    lk_set(15);
    if (wid == 6) {
      if (rtr) {
#pragma unroll 1
        for (int i = 0; i < SPIN_MAX; ++i) {
          if (__builtin_amdgcn_readfirstlane(*LDS_AT(const volatile unsigned, lbase + SPM + 128)) == 7u) break;
          __builtin_amdgcn_s_sleep(1);
        }
        asm volatile("" ::: "memory");
      }
    }
#pragma unroll 1
    for (int i = 0; i < OSLP && !best; ++i) {
      if (wid == 6 && due()) {
        LOG();
        keys(__builtin_amdgcn_raw_buffer_load_b128(rsk, (bid % NREP) * 1024 + lane * 16, 0, 16));
        i += 2;
      } else {
        if (__builtin_amdgcn_readfirstlane(*LDS_AT(const volatile unsigned, lbase + FLG + 40))) break;
        __builtin_amdgcn_s_sleep(2);
      }
      asm volatile("" ::: "memory");
    }
    u32x4 kv = {SENT, SENT, SENT, SENT};
    if (wid == 6 && !best && due()) kv = __builtin_amdgcn_raw_buffer_load_b128(rsk, (bid % NREP) * 1024 + lane * 16, 0, 16);
    PIN();
#pragma unroll
    for (int p = 0; p < 2; ++p) __builtin_amdgcn_raw_buffer_store_b128(xres[p], rsu, row + 8 * p != (int)fr1 ? ((row + 8 * p) * D_ + col) * 2 : S_ * D_ * 2, 0, OAUX);
    PIN();
    if (wid == 6 && !best) { keys(kv); while (!due()) __builtin_amdgcn_s_sleep(1); }
#pragma unroll 1
    for (int i = 0; i < 4 * SPIN_MAX && !best; ++i) {
      if (wid == 6) {
        LOG();
        keys(__builtin_amdgcn_raw_buffer_load_b128(rsk, (bid % NREP) * 1024 + lane * 16, 0, 16));
        if (!best) __builtin_amdgcn_s_sleep(KSLP);
      } else {
        best = __builtin_amdgcn_readfirstlane(*LDS_AT(const volatile unsigned, lbase + FLG + 40));
        if (!best) __builtin_amdgcn_s_sleep(1);
      }
      asm volatile("" ::: "memory");
    }
    const int idx2 = 2047 - (int)(best & 0x7ffu);
    if (__builtin_expect(idx2 != (int)fr1, 0)) {
      const __amdgpu_buffer_rsrc_t rsf = __builtin_amdgcn_make_buffer_rsrc(gran + GR_F, 0, 48 * 128, 0x00020000);
      if (!none((row | 8) == (idx2 | 8))) {
#pragma unroll
        for (int p = 0; p < 2; ++p)
          __builtin_amdgcn_raw_buffer_store_b128(u32x4{pack2(xf[p][0][0], xf[p][0][1]), pack2(xf[p][1][0], xf[p][1][1]), pack2(xf[p][2][0], xf[p][2][1]), pack2(xf[p][3][0], xf[p][3][1])},
              __builtin_amdgcn_make_buffer_rsrc(xout, 0, S_ * D_ * 2, 0x00020000), row + 8 * p == idx2 ? (idx2 * D_ + col) * 2 : S_ * D_ * 2, 0, 16);
        asm volatile("s_waitcnt vmcnt(0)" ::: "memory");
        __builtin_amdgcn_raw_buffer_store_b32(tag, rsf, lane == 0 ? oni * 128 : 48 * 128, 0, 16);
      }
      if (!none((row | 8) == ((int)fr1 | 8))) {
#pragma unroll 1
        for (int spin = 0; spin < SPIN_MAX; ++spin) {
          if (none(__builtin_amdgcn_raw_buffer_load_b32(rsf, (16 + oni * 2 + (lane & 1)) * 128, 0, 16) != tag)) break;
          asm volatile("" ::: "memory");
        }
#pragma unroll
        for (int p = 0; p < 2; ++p) __builtin_amdgcn_raw_buffer_store_b128(xres[p], rsu, row + 8 * p == (int)fr1 ? ((row + 8 * p) * D_ + col) * 2 : S_ * D_ * 2, 0, OAUX);
      }
    }
  }
  }
}
struct Args2 { Args l[2]; };
extern "C" __global__ __launch_bounds__(768, 3) void layers_kernel(const Args2 args) {
  __shared__ __attribute__((aligned(128))) char smem[163840];
  const unsigned long long t_entry = 0;
  const int tid = threadIdx.x, bid = blockIdx.x;
  int wid = __builtin_amdgcn_readfirstlane(tid >> 6);
  asm volatile("" : "+s"(wid));
  const ArgP ap = (ArgP)__builtin_amdgcn_kernarg_segment_ptr();
  layer_body(wid, bid, (unsigned)(uintptr_t)smem, ap, t_entry);
  LAYER_HERE;
  if (!LAY2) {
    asm volatile(AGAIN :: "{s[0:1]}"(ap + 1), "{s2}"(bid), "{v0}"(wid * 64) : "s90", "s91", "scc", "memory");
    __builtin_unreachable();
  }
}

extern "C" __global__ void end_marker_kernel() {}

extern "C" void kernel_launch(void* const* d_in, const int* in_sizes, int n_in, void* d_out, int out_size,
                              void* d_ws, size_t ws_size, hipStream_t stream) {
  const u16* x0 = (const u16*)d_in[0];
  const u16* wqkv = (const u16*)d_in[1];
  const u16* wo = (const u16*)d_in[2];
  const u16* w_r = (const u16*)d_in[4];
  const u16* wg = (const u16*)d_in[5];
  const u16* wu = (const u16*)d_in[6];
  const u16* wd = (const u16*)d_in[7];
  u16* out = (u16*)d_out;

  char* ws = (char*)d_ws;
  const size_t MB4 = (size_t)S_ * D_ * 2;
  u16* kv1 = (u16*)(ws);
  u16* kv2 = (u16*)(ws + 2 * MB4);
  u16* o1 = (u16*)(ws + 4 * MB4);
  u16* x1 = (u16*)(ws + 5 * MB4);
  u16* x3 = (u16*)(ws + 6 * MB4);
  unsigned* hand = (unsigned*)(ws + 7 * MB4);
  double* stats = (double*)(ws + 8 * MB4);
  unsigned* gran = (unsigned*)(ws + 9 * MB4);
  float* csf = (float*)(ws + 9 * MB4 + (1 << 20));
  u16* o2 = (u16*)(ws + 10 * MB4);
  u16* wc = (u16*)((((uintptr_t)ws + 11 * MB4 + 2047) & ~(uintptr_t)2047) + (((uintptr_t)wqkv + 128) & 2047));
  Args2 a;
  for (int l = 0; l < 2; ++l) {
    Args& r = a.l[l];
    r.lay = l; r.a = l ? x1 : x0; r.kv = l ? kv2 : kv1; r.o = l ? o2 : o1; r.xo = l ? x3 : x1; r.so = l ? stats + STAT_N : stats;
    r.W = wqkv; r.wc = wc; r.wg = wg; r.wu = wu; r.wd = wd; r.wo = wo; r.w_r = w_r; r.gran = gran; r.csf = csf; r.stats = stats; r.hand = hand; r.out = out;
  }
  layers_kernel<<<256, 768, 0, stream>>>(a);
}
```

```cpp
#include <hip/hip_runtime.h>
#include <hip/hip_bf16.h>
#include <stdint.h>
#include <type_traits>
#include <utility>

typedef unsigned short u16;
typedef short s16x4 __attribute__((ext_vector_type(4)));
typedef short s16x8 __attribute__((ext_vector_type(8)));
typedef float f32x16 __attribute__((ext_vector_type(16)));
typedef float f32x4 __attribute__((ext_vector_type(4)));
typedef unsigned int u32x4 __attribute__((ext_vector_type(4)));
typedef unsigned int u32x2 __attribute__((ext_vector_type(2)));
typedef __bf16 bf16x8 __attribute__((ext_vector_type(8)));
typedef __bf16 bf16x16 __attribute__((ext_vector_type(16)));

__device__ __forceinline__ int fresh_lane() {
  int l;
  asm volatile("v_mbcnt_lo_u32_b32 %0, -1, 0\n\tv_mbcnt_hi_u32_b32 %0, -1, %0" : "=v"(l));
  return l;
}
#define STAMP(k, i)
#define STAMPV(k, i, v)
#define CT0()
#define CT1(x)
#define LOGDEF
#define LOG()
#define LOGAT(k)
#define lk_set(k)
#define LOGOUT(slot)
#define TE(grp, k)
#define TEARG
#define TEPASS
constexpr int S_ = 2048, D_ = 1024, H_ = 32, DH_ = 32, F_ = 3584;

__device__ __forceinline__ int kpos(int r) { return (r & ~12) | ((r & 4) << 1) | ((r & 8) >> 1); }
__device__ __forceinline__ float bf2f(u16 v) { return __uint_as_float(((unsigned)v) << 16); }
__device__ __forceinline__ u16 f2bf(float f) {
  __hip_bfloat16 b = __float2bfloat16(f);
  return *reinterpret_cast<u16*>(&b);
}
typedef float f32x2 __attribute__((ext_vector_type(2)));
typedef __bf16 bf16x2 __attribute__((ext_vector_type(2)));
__device__ __forceinline__ unsigned pack2(float lo, float hi) {
  f32x2 v = {lo, hi};
  return __builtin_bit_cast(unsigned, __builtin_convertvector(v, bf16x2));
}
__device__ __forceinline__ float rbf(float f) { return __uint_as_float(pack2(f, f) << 16); }
__device__ __forceinline__ void rbf2(float& a, float& b) {
  const unsigned p = pack2(a, b);
  a = __uint_as_float(p << 16); b = __uint_as_float(p & 0xffff0000u);
}

template <int CTRL, int RMASK>
__device__ __forceinline__ float dpp_add(float v) {
  const int t = __builtin_amdgcn_update_dpp(0, __float_as_int(v), CTRL, RMASK, 0xf, false);
  return v + __int_as_float(t);
}
__device__ __forceinline__ float sum_hi3(float v) {
  v += __int_as_float(__builtin_amdgcn_update_dpp(0, __float_as_int(v), 0x128, 0xf, 0xf, false));
  const auto a = __builtin_amdgcn_permlane16_swap(__float_as_uint(v), __float_as_uint(v), false, false);
  v = __uint_as_float(a[0]) + __uint_as_float(a[1]);
  const auto b = __builtin_amdgcn_permlane32_swap(__float_as_uint(v), __float_as_uint(v), false, false);
  return __uint_as_float(b[0]) + __uint_as_float(b[1]);
}
__device__ __forceinline__ float wave_sum(float v) {
  v = dpp_add<0x111, 0xf>(v);
  v = dpp_add<0x112, 0xf>(v);
  v = dpp_add<0x114, 0xf>(v);
  v = dpp_add<0x118, 0xf>(v);
  v = dpp_add<0x142, 0xa>(v);
  v = dpp_add<0x143, 0xc>(v);
  return __int_as_float(__builtin_amdgcn_readlane(__float_as_int(v), 63));
}

template <int CTRL, int RMASK>
__device__ __forceinline__ unsigned dpp_umax(unsigned v) {
  const unsigned t = (unsigned)__builtin_amdgcn_update_dpp(0, (int)v, CTRL, RMASK, 0xf, false);
  return v > t ? v : t;
}
__device__ __forceinline__ unsigned wave_umax(unsigned v) {
  v = dpp_umax<0x111, 0xf>(v); v = dpp_umax<0x112, 0xf>(v); v = dpp_umax<0x114, 0xf>(v); v = dpp_umax<0x118, 0xf>(v);
  v = dpp_umax<0x142, 0xa>(v); v = dpp_umax<0x143, 0xc>(v);
  return (unsigned)__builtin_amdgcn_readlane((int)v, 63);
}

#define NREP 32
constexpr unsigned SENT = 0xFFFFFFFFu;
constexpr int GRAN_N = 256 * NREP;
constexpr int Y_N = F_ / 16 * D_;
constexpr int X2_N = D_ / 2;
constexpr int XS_N = 128;
constexpr int P4_N = 3 * D_ * 16;
constexpr int PA_N = H_ * 8 * 64;
constexpr int HAND_N = GRAN_N + Y_N + X2_N + XS_N + P4_N + PA_N;
constexpr int MD_O = 16 * 16 * 128 * 4;
constexpr int PK_O = MD_O;
constexpr int PP_O = PK_O + 128 * NREP;
constexpr int Y3_O = PP_O + H_ * S_;
constexpr int PS_N = Y3_O + Y_N;
constexpr int GR_ROW = 64, GR_COL = GR_ROW + 4 * S_, GR_KV = GR_COL + 2 * 16 * 16 * 192;
#define RO 16
#define RK 16
#define RX 1
constexpr int GR_O = GR_KV + 2 * 16 * 32 * RK, GR_X = GR_O + 2 * 16 * 64 * RO, GR_N = GR_X + 16 * 128 * RX;
constexpr int GR_F = GR_N + 256 * 64;
constexpr int STAT_N = 3 * S_;

__device__ __forceinline__ void st_ag(unsigned* p, unsigned v) { __hip_atomic_store(p, v, __ATOMIC_RELAXED, __HIP_MEMORY_SCOPE_AGENT); }
__device__ __forceinline__ unsigned ld_ag(const unsigned* p) { return __hip_atomic_load(p, __ATOMIC_RELAXED, __HIP_MEMORY_SCOPE_AGENT); }
__device__ __forceinline__ double ld_ag(const double* p) { return __hip_atomic_load(p, __ATOMIC_RELAXED, __HIP_MEMORY_SCOPE_AGENT); }
__device__ __forceinline__ unsigned f2word(float f) { const unsigned u = __float_as_uint(f); return u < 0xFFFFFFFEu ? u : 0xFFFFFFFEu; }
constexpr int SPIN_MAX = 1 << 18;
__device__ __forceinline__ unsigned umax(unsigned a, unsigned b) { return a > b ? a : b; }
__device__ __forceinline__ bool none(bool p) { return __builtin_amdgcn_ballot_w64(p) == 0ull; }

__device__ __forceinline__ void ln_from_sums(double s1, double s2, float& mean, float& var, float& mb, float& den) {
  const double md = s1 * (1.0 / 1024.0);
  const double vd = fma(-md, md, s2 * (1.0 / 1024.0));
  mean = (float)md; var = fmaxf((float)vd, 0.f);
  mb = rbf(mean);
  den = rbf(__builtin_amdgcn_sqrtf(rbf(rbf(var) + 1e-5f)));
}
__device__ __forceinline__ float frcp(float x) { return __builtin_amdgcn_rcpf(x); }
__device__ __forceinline__ void ln_apply2(float& a, float& b, float mb, float den, float rin) {
  a -= mb; b -= mb;
  rbf2(a, b);
  float qa = a * rin, qb = b * rin;
  qa = fmaf(fmaf(-qa, den, a), rin, qa);
  qb = fmaf(fmaf(-qb, den, b), rin, qb);
  rbf2(qa, qb);
  a = qa; b = qb;
}

#define CODE_ADDR(SYM, out64)                                                                                          \
  do {                                                                                                                 \
    unsigned lo_, hi_;                                                                                                 \
    asm volatile("s_getpc_b64 s[90:91]\n\ts_add_u32 s90, s90, " SYM "@rel32@lo+4\n\ts_addc_u32 s91, s91, " SYM          \
                 "@rel32@hi+12\n\ts_mov_b32 %0, s90\n\ts_mov_b32 %1, s91"                                             \
                 : "=s"(lo_), "=s"(hi_) :: "s90", "s91", "scc");                                                        \
    out64 = ((unsigned long long)hi_ << 32) | lo_;                                                                     \
  } while (0)

#define QNX 4
constexpr int QMX = 32 / QNX, QNG = 16 / QNX, QMG = 8 / QNG;

__device__ __forceinline__ unsigned touch(const void* p) { return *reinterpret_cast<const unsigned*>(p); }

__device__ __forceinline__ unsigned touch_code(unsigned long long lo, unsigned long long hi, int pc, int lane) {
  const unsigned long long p = lo + (unsigned)(pc * 8192 + lane * 128);
  return p + 4 <= hi ? *reinterpret_cast<__attribute__((address_space(1))) const unsigned*>(p) : 0u;
}

template <bool SC = false>
__device__ __forceinline__ void glds16s(const void* sbase, unsigned voff, unsigned lds_dst) {
  unsigned keep;
  if (SC)
    asm volatile("s_mov_b32 %0, m0\n\ts_mov_b32 m0, %3\n\ts_nop 2\n\tglobal_load_lds_dwordx4 %1, %2 sc1\n\ts_mov_b32 m0, %0"
                 : "=&s"(keep) : "v"(voff), "s"(sbase), "s"(lds_dst) : "memory");
  else
  asm volatile("s_mov_b32 %0, m0\n\ts_mov_b32 m0, %3\n\ts_nop 2\n\tglobal_load_lds_dwordx4 %1, %2\n\ts_mov_b32 m0, %0"
               : "=&s"(keep) : "v"(voff), "s"(sbase), "s"(lds_dst) : "memory");
}
template <bool SC>
__device__ __forceinline__ void glds16x4(const void* sbase, unsigned v0, unsigned v1, unsigned v2, unsigned v3, unsigned lds_dst) {
  unsigned keep;
  if (SC)
    asm volatile("s_mov_b32 %0, m0\n\ts_mov_b32 m0, %6\n\ts_nop 2\n\tglobal_load_lds_dwordx4 %1, %5 sc1\n\tglobal_load_lds_dwordx4 %2, %5 offset:1024 sc1\n\t"
                 "global_load_lds_dwordx4 %3, %5 offset:2048 sc1\n\tglobal_load_lds_dwordx4 %4, %5 offset:3072 sc1\n\ts_mov_b32 m0, %0"
                 : "=&s"(keep) : "v"(v0), "v"(v1), "v"(v2), "v"(v3), "s"(sbase), "s"(__builtin_amdgcn_readfirstlane(lds_dst)) : "memory");
  else
    asm volatile("s_mov_b32 %0, m0\n\ts_mov_b32 m0, %6\n\ts_nop 2\n\tglobal_load_lds_dwordx4 %1, %5\n\tglobal_load_lds_dwordx4 %2, %5 offset:1024\n\t"
                 "global_load_lds_dwordx4 %3, %5 offset:2048\n\tglobal_load_lds_dwordx4 %4, %5 offset:3072\n\ts_mov_b32 m0, %0"
                 : "=&s"(keep) : "v"(v0), "v"(v1), "v"(v2), "v"(v3), "s"(sbase), "s"(__builtin_amdgcn_readfirstlane(lds_dst)) : "memory");
}
constexpr float QSCALE = 0.17677669529663687f * 1.4426950408889634f;

__device__ __forceinline__ unsigned min4(u32x4 v) { const unsigned a = v[0] < v[1] ? v[0] : v[1], b = v[2] < v[3] ? v[2] : v[3]; return a < b ? a : b; }
#define LDS_AT(T, a) ((__attribute__((address_space(3))) T*)(uintptr_t)(a))
__device__ __forceinline__ u32x4 lds_rd4(unsigned a) { return *LDS_AT(const volatile u32x4, a); }
__device__ __forceinline__ void lds_wr(unsigned a, unsigned v) { *LDS_AT(volatile unsigned, a) = v; }
__device__ __forceinline__ void split3(float v, unsigned& p0, unsigned& p1, unsigned& p2) {
  p0 = pack2(v, v) & 0xffffu; v -= __uint_as_float(p0 << 16);
  p1 = pack2(v, v) & 0xffffu; v -= __uint_as_float(p1 << 16);
  p2 = pack2(v, v) & 0xffffu;
}

#define PIN() __builtin_amdgcn_sched_barrier(0)
template <int B, class F, int... I>
__device__ __forceinline__ void static_for_(F&& f, std::integer_sequence<int, I...>) { (f(std::integral_constant<int, B + I>{}), ...); }
template <int B, int E, class F>
__device__ __forceinline__ void static_for(F&& f) { static_for_<B>(f, std::make_integer_sequence<int, E - B>{}); }

#define RESAT 20
#define LNPRE 1
#define TPRE 60
#define TPSL 0
#define OSCH 0
#define FCW 0
#define FCPRE 44
#define FCSLP 2
#define SIGAT 24
#define XPRIO 1
#define OPD 6
#define LAUX 16
#define CSLP 2
#define EPRIO 3
#define OPRIO 1
#define XSLP 1
#define ROT 0
#define CSL1 8
#define XAUX 16
__device__ __forceinline__ void oproj_tail(const bool RES2, const int tid, const unsigned lbase, const unsigned flg, const int tmi, const int tni, const int idx,
                                           unsigned* osig, unsigned* xsig, const unsigned tag,
                                           const u16* A, u16* out,
                                           const u16* res, const u16* __restrict__ w_r, double* stats,
                                           const double* stats_in, unsigned* hand, u32x4 (&xres)[2] TEARG) {
  constexpr int ldw = D_, PD = OPD;
  const int lane = tid & 63, wid = __builtin_amdgcn_readfirstlane(tid >> 6);
  const int m0 = tmi * 128, n0 = tni * 64;
  const int dk = RES2 ? 7 : 3;
  if (wid == 0) STAMP(dk, 0);
  TE(1, 3);
  asm volatile("" : "+s"(A) :: "memory");
  PIN();
  const int row = m0 + (wid & 3) * 32 + (wid >> 2) * 16 + (lane >> 3), col = n0 + (lane & 7) * 8;
  const unsigned tb = lbase + 147456;
  typedef __attribute__((address_space(1))) double* gdp;
  typedef __attribute__((address_space(1))) unsigned* gup;
  unsigned tr, oad;
  gdp pst;
  gup pps;
  int xo;
  auto setup = [&]() __attribute__((always_inline)) {
    tr = tb + (row - m0) * 128 + (((lane ^ (lane >> 3)) & 7) << 4);
    pst = (gdp)(stats + ((lane & 3) == 3 ? 0 : (lane & 3)) * S_ + row + 8 * ((lane >> 2) & 1));
    pps = (gup)(hand + 2 * HAND_N + ((tmi * 16 + tni) * 128 + row - m0 + 8 * ((lane >> 2) & 1)) * 4 + (lane & 3));
    xo = (row * ldw + col) * 2;
    asm volatile("" : "+v"(tr), "+v"(pst), "+v"(pps), "+v"(xo));
    PIN();
  };
  {
    oad = lane == 0 ? flg + 24 : flg + 64 + lane * 4;
    gup ps = (gup)(osig + lane * 64);
    asm volatile("" : "+v"(oad), "+v"(ps));
    asm volatile("s_waitcnt vmcnt(1)" ::: "memory");
    TE(2, 1);
    const unsigned old = __hip_atomic_fetch_add(LDS_AT(unsigned, oad), 1u, __ATOMIC_RELAXED, __HIP_MEMORY_SCOPE_WORKGROUP);
    if (__builtin_amdgcn_readfirstlane(old) == 7u && lane < RO) __hip_atomic_store(ps, tag, __ATOMIC_RELAXED, __HIP_MEMORY_SCOPE_AGENT);
    PIN();
    TE(2, 2);
    if (wid == 0) STAMP(dk, 12);
  }
  auto announce = [&]() __attribute__((always_inline)) {
    asm volatile("" ::: "memory");
    PIN();
    __builtin_amdgcn_s_barrier();
    PIN();
    if (wid == 0) STAMP(dk, 1);
    TE(2, 3); TE(4, 0); TE(7, 1);
  };
  u32x4 rwr;
  auto sums = [&](const u32x4& x0, const u32x4& x1) __attribute__((always_inline)) {
    float f[2][3];
#pragma unroll
    for (int p = 0; p < 2; ++p) {
      f[p][0] = f[p][1] = f[p][2] = 0.f;
#pragma unroll
      for (int e = 0; e < 4; ++e) {
        const unsigned xu = p ? x1[e] : x0[e], wu = rwr[e];
        const bf16x2 x = __builtin_bit_cast(bf16x2, xu);
        f[p][0] = __builtin_amdgcn_fdot2_f32_bf16(x, __builtin_bit_cast(bf16x2, 0x3f803f80u), f[p][0], false);
        f[p][1] = __builtin_amdgcn_fdot2_f32_bf16(x, x, f[p][1], false);
        f[p][2] = __builtin_amdgcn_fdot2_f32_bf16(x, __builtin_bit_cast(bf16x2, wu), f[p][2], false);
      }
    }
    auto addx = [&](float keep, float give, auto ctl) {
      return keep + __int_as_float(__builtin_amdgcn_update_dpp(0, __float_as_int(give), decltype(ctl)::value, 0xf, 0xf, false));
    };
    const bool b2 = lane & 4, b1 = lane & 2, z0 = (lane & 3) == 0, z1 = (lane & 3) == 1;
    float g[3];
#pragma unroll
    for (int c = 0; c < 3; ++c) g[c] = addx(b2 ? f[1][c] : f[0][c], b2 ? f[0][c] : f[1][c], std::integral_constant<int, 0x141>{});
    const float u1 = addx(b1 ? g[2] : g[0], b1 ? g[0] : g[2], std::integral_constant<int, 0x4e>{});
    const float u2 = addx(g[1], g[1], std::integral_constant<int, 0x4e>{});
    return addx(z1 ? u2 : u1, z0 ? u2 : u1, std::integral_constant<int, 0xb1>{});
  };
  if (wid < 4) {
    const __amdgpu_buffer_rsrc_t rsa = __builtin_amdgcn_make_buffer_rsrc(const_cast<u16*>(A), 0, S_ * D_ * 2, 0x00020000);
    int sa0 = (tmi * 4 + wid) * 65536, va0 = (lane >> 4) * 512 + (lane & 15) * 16;
    asm volatile("" : "+v"(va0));
    asm volatile("" : "+s"(sa0));
    const int rot = ROT ? tni & 7 : 0;
    auto lda = [&](int i, int h) { return __builtin_amdgcn_raw_buffer_load_b128(rsa, va0 + (i & 1) * 2048 + h * 256, sa0 + (((i >> 2) + rot) & 7) * 8192 + (i & 2) * 2048, LAUX); };
    unsigned wad[4], tw[4];
    f32x4 acc[2][4];
    const int k = (lane >> 5) * 16 + ((lane >> 2) & 7);
#pragma unroll
    for (int j = 0; j < 4; ++j) {
      wad[j] = lbase + k * 128 + (((2 * j) ^ (k & 6)) << 4) + 8 * (lane & 3) + rot * 16384;
      tw[j] = tb + wid * 4096 + (lane & 15) * 128 + (((2 * j + (lane >> 5)) ^ (lane & 7)) << 4) + ((lane >> 4) & 1) * 8;
      asm volatile("" : "+v"(wad[j]), "+v"(tw[j]));
    }
    auto ldw_ = [&](int i, int j) {
      const unsigned a = wad[j] + (i & 3) * 4096;
      s16x4 lo = __builtin_amdgcn_ds_read_tr16_b64_v4i16(LDS_AT(s16x4, a));
      s16x4 h4 = __builtin_amdgcn_ds_read_tr16_b64_v4i16(LDS_AT(s16x4, a + 1024));
      return (s16x8)__builtin_shufflevector(lo, h4, 0, 1, 2, 3, 4, 5, 6, 7);
    };
    u32x4 id[2];
#pragma unroll
    for (int j = 0; j < 2; ++j) {
#pragma unroll
      for (int e = 0; e < 4; ++e) id[j][e] = (lane >> 4) == 2 * j + ((lane >> 3) & 1) && ((lane & 7) >> 1) == e ? ((lane & 1) ? 0x3f800000u : 0x3f80u) : 0u;
      asm volatile("" : "+v"(id[j]));
    }
    unsigned rad = tb + wid * 4096 + (lane & 15) * 128 + (((lane >> 4) ^ (lane & 7)) << 4), rfa = flg + 36 + wid;
    asm volatile("" : "+v"(rad), "+v"(rfa));
    u32x4 rb[2][2];
    rwr = *reinterpret_cast<const u32x4*>(w_r + col);
    setup();
    asm volatile("" : "+v"(rwr));
    announce();
    u32x4 af[PD][2];
    s16x8 wf[2][4];
#pragma unroll
    for (int i = 0; i < PD; ++i) { af[i][0] = lda(i, 0); af[i][1] = lda(i, 1); }
    PIN();
    __builtin_amdgcn_s_setprio(OPRIO);
#pragma unroll
    for (int q = 0; q < 4; ++q) rb[q >> 1][q & 1] = lds_rd4((rad ^ ((q & 1) * 64)) + (q >> 1) * 2048);
    PIN();
#pragma unroll
    for (int j = 0; j < 4; ++j)
#pragma unroll
      for (int h = 0; h < 2; ++h) { asm volatile("v_mfma_f32_16x16x32_bf16 %0, %1, %2, 0" : "=&v"(acc[h][j]) : "v"(id[j & 1]), "v"(rb[h][j >> 1])); PIN(); }
#pragma unroll 1
    for (int spin = 0; spin < SPIN_MAX; ++spin)
      if (__builtin_amdgcn_readfirstlane(*LDS_AT(const volatile unsigned, flg)) >= 40u) break;
    asm volatile("" : "+v"(va0) :: "memory");
    PIN();
#pragma unroll
    for (int j = 0; j < 4; ++j) wf[0][j] = ldw_(0, j);
    s16x4 wlo;
    static_for<0, 32>([&](auto ic) __attribute__((always_inline)) {
      constexpr int i = decltype(ic)::value;
      if (i == SIGAT) { __hip_atomic_fetch_add(LDS_AT(unsigned, oad + 4), 1u, __ATOMIC_RELAXED, __HIP_MEMORY_SCOPE_WORKGROUP); PIN(); }
#pragma unroll
      for (int g = 0; g < 8; ++g) {
        const int j = OSCH ? g & 3 : g >> 1, h = OSCH ? g >> 2 : g & 1;
        asm volatile("v_mfma_f32_16x16x32_bf16 %0, %1, %2, %0" : "+v"(acc[h][j]) : "v"(wf[i & 1][j]), "v"(af[i % PD][h]));
        PIN();
        if (i == 15 && !(g & 1)) { wad[g >> 1] += 65536; asm volatile("" : "+v"(wad[g >> 1])); }
        if (i + 1 < 32) {
          const s16x4 v = __builtin_amdgcn_ds_read_tr16_b64_v4i16(LDS_AT(s16x4, wad[g >> 1] + ((i + 1) & 15) * 4096 + (g & 1) * 1024));
          if (g & 1) wf[(i + 1) & 1][g >> 1] = (s16x8)__builtin_shufflevector(wlo, v, 0, 1, 2, 3, 4, 5, 6, 7);
          else wlo = v;
          PIN();
        }
        if (OSCH ? (g & 3) == 3 : g >= 6) { if (i + PD < 32) { af[i % PD][h] = lda(i + PD, h); PIN(); } }
        if (!OSCH && i + 1 < 32 && i != SIGAT && i + 1 != SIGAT) {
          if (g == 3) { __builtin_amdgcn_s_waitcnt(0xc47f); PIN(); }
          if (g == 7 && i + PD < 32) { __builtin_amdgcn_s_waitcnt(0x0470 | (2 * PD - 2)); PIN(); }
          if (g == 7 && i + PD >= 32) { __builtin_amdgcn_s_waitcnt(0xc47f); PIN(); }
        }
      }
    });
    asm volatile("s_nop 3" : "+v"(acc[0][0]), "+v"(acc[1][0]));
    TE(4, 1); TE(7, 2);
    if (wid == 0) STAMP(dk, 17);
    if (wid == 1 && !RES2) STAMP(4, 0);
#pragma unroll
    for (int j = 0; j < 4; ++j)
#pragma unroll
      for (int h = 0; h < 2; ++h) {
        *LDS_AT(u32x2, tw[j] + h * 2048) = u32x2{pack2(acc[h][j][0], acc[h][j][1]), pack2(acc[h][j][2], acc[h][j][3])};
        PIN();
      }
    asm volatile("" ::: "memory");
    *LDS_AT(volatile unsigned char, rfa) = (unsigned char)2;
#pragma unroll
    for (int p = 0; p < 2; ++p) xres[p] = lds_rd4(tr + p * 1024);
    PIN();
    if (!RES2) {
      const __amdgpu_buffer_rsrc_t rso = __builtin_amdgcn_make_buffer_rsrc(out, 0, S_ * D_ * 2, 0x00020000);
      const __amdgpu_buffer_rsrc_t rsf = __builtin_amdgcn_make_buffer_rsrc(xsig + wid, 0, RX * 512, 0x00020000);
      const u32x4 x2 = lds_rd4(tr + 2048), x3 = lds_rd4(tr + 3072);
      PIN();
#pragma unroll
      for (int p = 0; p < 4; ++p) { __builtin_amdgcn_raw_buffer_store_b128(p < 2 ? xres[p] : (p < 3 ? x2 : x3), rso, xo + p * (8 * ldw * 2), 0, XAUX); PIN(); }
      TE(4, 2); TE(5, 0);
      const float s0 = sums(xres[0], xres[1]), s1 = sums(x2, x3);
      if ((lane & 3) != 3) { __builtin_amdgcn_global_atomic_fadd_f64(pst, (double)s0); __builtin_amdgcn_global_atomic_fadd_f64(pst + 16, (double)s1); }
      PIN();
      TE(4, 3); TE(5, 1);
      asm volatile("s_waitcnt vmcnt(2)" ::: "memory");
      __builtin_amdgcn_raw_buffer_store_b32(tag, rsf, lane * 512 + 256, 0, 16);
      *LDS_AT(volatile unsigned char, rfa) = (unsigned char)3;
      PIN();
      TE(5, 2);
      asm volatile("s_waitcnt vmcnt(1)" ::: "memory");
      __builtin_amdgcn_raw_buffer_store_b32(tag, rsf, lane * 512, 0, 16);
      PIN();
      TE(5, 3);
      __builtin_amdgcn_s_setprio(0);
      return;
    }
    TE(4, 2); TE(5, 0);
  } else {
    u32x4 rres[4];
    double is1[4], is2[4];
#pragma unroll
    for (int p = 0; p < 4; ++p)
      rres[p] = __builtin_amdgcn_raw_buffer_load_b128(__builtin_amdgcn_make_buffer_rsrc(const_cast<u16*>(res), 0, S_ * D_ * 2, 0x00020000), ((row - 16 + 8 * p) * ldw + col) * 2, 0, 16);
    rwr = *reinterpret_cast<const u32x4*>(w_r + col);
#pragma unroll
    for (int p = 0; p < 4; ++p) { is1[p] = ld_ag(stats_in + row - 16 + 8 * p); is2[p] = ld_ag(stats_in + S_ + row - 16 + 8 * p); }
    PIN();
    setup();
    if (RES2) {
#pragma unroll
      for (int p = 0; p < 4; ++p) {
        float mean, var, mb, den;
        ln_from_sums(is1[p], is2[p], mean, var, mb, den);
        const float rin = frcp(den);
#pragma unroll
        for (int e = 0; e < 4; ++e) {
          const f32x2 x = {__uint_as_float(rres[p][e] << 16), __uint_as_float(rres[p][e] & 0xffff0000u)};
          const f32x2 h = (x - f32x2{mb, mb}) * f32x2{rin, rin} + x;
          rres[p][e] = pack2(h[0], h[1]);
        }
        asm volatile("" : "+v"(rres[p]));
      }
      PIN();
    }
    if ((idx >> 5) == tmi * 4 + wid - 4) {
      const int dr = idx - (row - 16);
      u32x4 qq;
#pragma unroll 1
      for (int i = 0; i < SPIN_MAX; ++i) {
        qq = __builtin_amdgcn_raw_buffer_load_b128(__builtin_amdgcn_make_buffer_rsrc(hand, 0, HAND_N * 4, 0x00020000), (GRAN_N + Y_N) * 4 + col * 2, 0, 16);
        if (none(umax(umax(qq[0], qq[1]), umax(qq[2], qq[3])) == SENT)) break;
        asm volatile("" ::: "memory");
      }
#pragma unroll
      for (int p = 0; p < 4; ++p)
#pragma unroll
        for (int e = 0; e < 4; ++e) rres[p][e] = dr == 8 * p ? qq[e] : rres[p][e];
    }
#pragma unroll 1
    for (int i = 0; i < 4 * SPIN_MAX; ++i)
      if (__builtin_amdgcn_readfirstlane(*LDS_AT(const volatile unsigned, flg + 16)) >= 12u) break;
#pragma unroll
    for (int p = 0; p < 4; ++p) *LDS_AT(volatile u32x4, tr - 2048 + p * 1024) = rres[p];
    asm volatile("s_waitcnt lgkmcnt(0)" ::: "memory");
    announce();
    TE(4, 1); TE(7, 2);
    if (!RES2) return;
    asm volatile("" : "+v"(rwr) :: "memory");
    __builtin_amdgcn_s_sleep(TPRE);
#pragma unroll 1
    for (int i = 0; i < 4 * SPIN_MAX; ++i) {
      if (__builtin_amdgcn_readfirstlane(*LDS_AT(const volatile unsigned char, flg + 32 + wid)) == 2u) break;
      __builtin_amdgcn_s_sleep(TPSL);
    }
    asm volatile("" ::: "memory");
    __builtin_amdgcn_s_setprio(XPRIO);
    PIN();
    TE(4, 2); TE(5, 0);
#pragma unroll
    for (int p = 0; p < 2; ++p) xres[p] = lds_rd4(tr + p * 1024);
  }
  TE(4, 3); TE(7, 3);
  const float sv = sums(xres[0], xres[1]);
  if (wid == 0) STAMP(dk, 14);
  if ((lane & 3) != 3) __hip_atomic_store(pps, f2word(sv), __ATOMIC_RELAXED, __HIP_MEMORY_SCOPE_AGENT);
  PIN();
  __builtin_amdgcn_s_setprio(0);
  __hip_atomic_fetch_add(LDS_AT(unsigned, oad), 1u, __ATOMIC_RELAXED, __HIP_MEMORY_SCOPE_WORKGROUP);
  TE(5, 1); TE(5, 2);
  PIN();
}

#define MFMA32(a, b, c) __builtin_amdgcn_mfma_f32_32x32x16_bf16(a, b, c, 0, 0, 0)
#define MFMA16(a, b, c) __builtin_amdgcn_mfma_f32_16x16x32_bf16(a, b, c, 0, 0, 0)

#define PLO(x) __builtin_bit_cast(bf16x16, __builtin_shufflevector(x, x, 0, 1, 2, 3, 4, 5, 6, 7))
#define PHI(x) __builtin_bit_cast(bf16x16, __builtin_shufflevector(x, x, 8, 9, 10, 11, 12, 13, 14, 15))
#define SMF32(a, b, c) __builtin_amdgcn_smfmac_f32_32x32x32_bf16(__builtin_bit_cast(bf16x8, a), b, c, sidx, 0, 0)
#define SMF16(a, b, c) __builtin_amdgcn_smfmac_f32_16x16x64_bf16(__builtin_bit_cast(bf16x8, a), b, c, sidx, 0, 0)

__device__ __forceinline__ void patch_kv(unsigned tab, int r, s16x8& k0, s16x8& k1, s16x8& v0, s16x8& v1, bool on = true) {
  const int lane = fresh_lane(), hi = lane >> 5;
  if (on && (lane & 31) == kpos(r)) {
    k0 = *LDS_AT(const s16x8, tab + hi * 16); k1 = *LDS_AT(const s16x8, tab + 32 + hi * 16);
  }
  const short vv = *LDS_AT(const short, tab + 128 + (lane & 31) * 2);
  const int js = (on && hi == ((r >> 2) & 1)) ? (r >> 4) * 8 + ((r >> 3) & 1) * 4 + (r & 3) : -1;
#pragma unroll
  for (int e = 0; e < 8; ++e) { v0[e] = js == e ? vv : v0[e]; v1[e] = js == 8 + e ? vv : v1[e]; }
}

__device__ __forceinline__ void attn_safe(const u16* KV, int tile0, s16x8 q0, s16x8 q1,
                                       f32x16& o, float& l_out, int ptile, int prow, unsigned ptab) {
  const int lane = fresh_lane();
  float m = -INFINITY, l = 0.f;
  const int sidx = 0xDDDDDDDD;
  const __amdgpu_buffer_rsrc_t rs = __builtin_amdgcn_make_buffer_rsrc(const_cast<u16*>(KV), 0, 2 * S_ * D_ * 2, 0x00020000);
#pragma unroll
  for (int e = 0; e < 16; ++e) o[e] = 0.f;
  for (int tt = 0; tt < 64; ++tt) {
    const int t = tile0 + tt, off = t * 2048 + lane * 16;
    s16x8 k0 = __builtin_bit_cast(s16x8, __builtin_amdgcn_raw_buffer_load_b128(rs, off, 0, 16));
    s16x8 k1 = __builtin_bit_cast(s16x8, __builtin_amdgcn_raw_buffer_load_b128(rs, off + 1024, 0, 16));
    s16x8 v0 = __builtin_bit_cast(s16x8, __builtin_amdgcn_raw_buffer_load_b128(rs, off + S_ * D_ * 2, 0, 16));
    s16x8 v1 = __builtin_bit_cast(s16x8, __builtin_amdgcn_raw_buffer_load_b128(rs, off + S_ * D_ * 2 + 1024, 0, 16));
    if (t == ptile) patch_kv(ptab, prow, k0, k1, v0, v1);
    f32x16 sc;
#pragma unroll
    for (int e = 0; e < 16; ++e) sc[e] = 0.f;
    sc = MFMA32(k0, q0, sc); sc = MFMA32(k1, q1, sc);
    float mt = sc[0];
#pragma unroll
    for (int e = 1; e < 16; ++e) mt = fmaxf(mt, sc[e]);
    mt = fmaxf(mt, __shfl_xor(mt, 32, 64));
    const float mn = fmaxf(m, mt);
    const float alpha = __builtin_amdgcn_exp2f(m - mn);
    m = mn;
#pragma unroll
    for (int e = 0; e < 16; ++e) sc[e] = __builtin_amdgcn_exp2f(sc[e] - mn);
    float ls = 0.f;
#pragma unroll
    for (int e = 0; e < 16; ++e) ls += __uint_as_float(__float_as_uint(sc[e]) & 0xffff0000u);
    l = l * alpha + ls;
#pragma unroll
    for (int e = 0; e < 16; ++e) o[e] *= alpha;
    o = SMF32(v0, PLO(sc), o); o = SMF32(v1, PHI(sc), o);
  }
  l_out = l + __shfl_xor(l, 32, 64);
}

template <int CTRL, int RMASK>
__device__ __forceinline__ float dpp_fmax(float v) {
  return fmaxf(v, __int_as_float(__builtin_amdgcn_update_dpp(__float_as_int(v), __float_as_int(v), CTRL, RMASK, 0xf, false)));
}
__device__ __forceinline__ float wave_fmax(float v) {
  v = dpp_fmax<0x111, 0xf>(v); v = dpp_fmax<0x112, 0xf>(v); v = dpp_fmax<0x114, 0xf>(v); v = dpp_fmax<0x118, 0xf>(v);
  v = dpp_fmax<0x142, 0xa>(v); v = dpp_fmax<0x143, 0xc>(v);
  return __int_as_float(__builtin_amdgcn_readlane(__float_as_int(v), 63));
}
__device__ __forceinline__ float sum_hi5(float v) {
  v = sum_hi3(v);
  v += __int_as_float(__builtin_amdgcn_update_dpp(0, __float_as_int(v), 0x124, 0xf, 0xf, false));
  v += __int_as_float(__builtin_amdgcn_update_dpp(0, __float_as_int(v), 0x122, 0xf, 0xf, false));
  return v;
}

#define PSLP 8
#define WSLP 3
#define PRE 28
#define PRE1 8
#define PRE5 8
#define W3SLP 4
#define FSTART 10
#define BAL1 false
#define FPRE0 20
#define FPRE 16
#define FSLP 4
#define OAUX 16
#define BALO 1
#define OMX 4
#define T0 2u
#define TS 2u
#define TW 10
#define HW 2
#define HP1 1
#define POLLAT 11
#define X2AT 2
#define HPRIO 3
#define SSLP 1
#define SPRE 12
#define QOV 2
#define FLAT 4
#define WGAPS {2, 6, 10, 14, 18, 21, 25, 29}
#define KVS 4
#define CKAT 3
#define VEARLY 2
#define GNS 12
#define GNS2 20
#define PNS 12
#define GEV 4
#define G0 5u
#define GS 2u
#define IAT 1u
#define WDAT 10u
#define W3AT 11u
#define WOAT 12u
#define YSLP 2
#define ISLP 8
#define X3SLP 1
#define RPRE 8
#define RSLP 2
#define KSLP 2
#define UAT 13u
#define UHW 0
#define JDL 4000u
#define PPSLP 2
#define PKPRE 24
#define PKSLP 4
#define XPRE 8
#define RPRE2 8
#define XRSLP 2
#define MPRE 20
#define KEARLY 0
#define REFINE 1
#define FPRE2 24
#define OSLP 2
#define YD1 150
#define YD2 180
#define KDT 200
#define XDT 65
#define R2PRIO 0
#define LNPRIO 1
#define K6 2
#define MPR2 8
#define MSLP 4
#define BARG 1
struct Args {
  long long lay;
  const u16* a; u16* kv; u16* o; u16* xo; double* so;
  const u16* W; u16* wc;
  const u16 *wg, *wu, *wd, *wo, *w_r;
  unsigned* gran; float* csf; double* stats; unsigned* hand; u16* out;
};
typedef __attribute__((address_space(4))) const Args* ArgP;
#define WCAT 3
#define CAP0 1
#define PRE4 50
#define PRE7 20
#define UARR 12u
#define GF0 10
#define XGATE 8
#define WMAP 0x32100333
#define SMAP 0
#define STCH 6
#define KPOLL 10
#define TPRIO 2
#define PG1 6
#define PG2 12
#define TSLP 9
__device__ __forceinline__ bool slow_cols(const u16* W, int tni) { return ((((unsigned)(uintptr_t)W >> 7) + tni) & 7) == 3; }
#define ARGS_HERE                                                                                                      \
  ArgP ap_ = ap; asm volatile("" : "+s"(ap_));                                                                          \
  const u16* A = ap_->a; const u16* __restrict__ W = ap_->W; u16* KV = ap_->kv; u16* Of = ap_->o;        \
  const u16* __restrict__ wg = ap_->wg; const u16* __restrict__ wu = ap_->wu; const u16* __restrict__ wd = ap_->wd;       \
  const u16* __restrict__ wo = ap_->wo; const u16* __restrict__ w_r = ap_->w_r; unsigned* gran = ap_->gran;              \
  float* csf = ap_->csf; const double* stats = ap_->stats; double* stats_out = ap_->so; unsigned* hand = ap_->hand; \
  u16* xout = ap_->xo; u16* __restrict__ out = ap_->out
#define AGAIN "s_getpc_b64 s[90:91]\n\ts_add_u32 s90, s90, layers_kernel@rel32@lo+4\n\ts_addc_u32 s91, s91, layers_kernel@rel32@hi+12\n\ts_setpc_b64 s[90:91]"
#define KEPT(c)                                                                                                        \
  c "{v[104:107]}"(gu[0]), c "{v[108:111]}"(gu[1]), c "{v[112:115]}"(gu[2]), c "{v[116:119]}"(gu[3]), c "{v[120:123]}"(gu[4]),      \
  c "{v[124:127]}"(gu[5]), c "{v[128:131]}"(gu[6]), c "{v[132:135]}"(gu[7]), c "{v[136:139]}"(gu[8]), c "{v[140:143]}"(gu[9]),      \
  c "{v[144:147]}"(gu[10]), c "{v[148:151]}"(gu[11]), c "{v[152:155]}"(gu[12]), c "{v[156:159]}"(gu[13]), c "{v[160:163]}"(gu[14]), \
  c "{v[164:167]}"(gu[15])
#define LAYER_HERE ArgP apl_ = ap; asm volatile("" : "+s"(apl_)); const bool LAY2 = apl_->lay != 0
__device__ __forceinline__ void layer_body(const int wid, const int bid, const unsigned lbase, const ArgP ap, const unsigned long long t_entry) {
  constexpr int K = 1024, NKT = 16, NST = 3, ABYTES = 16384, WBYTES = 24576, STB = ABYTES + WBYTES, ldw = 3 * D_;
  constexpr int TABA = NST * STB, TABW = TABA + 2048, ALPHA = TABW + 3072, CRED = ALPHA + 512;
  constexpr int ASB = 16384, FLG = 8 * ASB + 256;
  constexpr int DUM = FLG + 64, TAB = DUM + 256, RED = TAB + 512, SACT = RED + 4096, RED2 = SACT + 1024, PL = RED2 + 2048, XML = PL + 512, UGL = XML + 4096, SPM = UGL + 1024, QOFF = 9 * ASB;
  static_assert(SPM + 2048 <= QOFF && CRED <= 8 * ASB && RED + 8192 <= QOFF && RED % 16 == 0, "lds");
#define XPERM 1
  const int xcd = XPERM == 0 ? bid & 7 : (bid & 1) * 4 + (((bid >> 1) + (bid & 1) * (XPERM - 1)) & 3), loc = bid >> 3;
  const int tmi = (xcd / QNG) * QMX + loc / QNX, tni = (xcd % QNG) * QNX + loc % QNX;
  const int m0 = tmi * 128, n0 = tni * 64;
  const int omi = (xcd / (OMX / 2)) * OMX + loc / (32 / OMX), oni = (xcd % (OMX / 2)) * (32 / OMX) + loc % (32 / OMX);
  constexpr unsigned VOFF = (unsigned)S_ * D_ * 2;
  auto tile_at = [&](unsigned n) __attribute__((always_inline)) {
#pragma unroll 1
    for (int i = 0; i < 4 * SPIN_MAX; ++i) {
      if (__builtin_amdgcn_readfirstlane(*LDS_AT(const volatile unsigned, lbase + FLG + 36)) == n * 0x01010101u) break;
      __builtin_amdgcn_s_sleep(1);
    }
    asm volatile("" ::: "memory");
    PIN();
  };
  auto all_ge = [&](unsigned w, unsigned n) { return ((w + (128u - n) * 0x01010101u) & 0x80808080u) == 0x80808080u; };
  if (wid >= 8) {
    u32x4 gu[16];
    LAYER_HERE;
    constexpr int dk = 6;
    const int hw = wid - 8, b = bid;
    int nb = 0;
    constexpr int lim = NKT - 1;
    auto tick_nw = [&]() {
      PIN();
      if (nb < lim) { __builtin_amdgcn_s_barrier(); ++nb; } else __builtin_amdgcn_s_sleep(2);
      asm volatile("" ::: "memory");
      PIN();
    };
    auto tick = [&]() {
      asm volatile("s_waitcnt lgkmcnt(0)" ::: "memory");
      tick_nw();
    };
    unsigned best, tag, rmd;
    float cs, alpha, m, wsum;
    u32x2 xq;
    s16x8 sl[8];
    const int kcap = CAP0 + (tmi & 7);
    if (!LAY2) {
      ARGS_HERE;
      const int lane = fresh_lane(), t = hw * 64 + lane;
      const unsigned ep = ld_ag(gran);
      const int srow = m0 + tni * 8 + hw * 2;
      u32x4 xa[2][2], wv[8];
      const int wr_ = (3 - ((unsigned)(uintptr_t)W >> 7) - (tni & ~3)) & 7;
      const bool cpy = hw == 3 && wr_ < 4 && wr_ != (tni & 3), wld = hw < 3 ? wr_ != (tni & 3) : cpy;
      const int wcn = cpy ? (tni & ~3) + wr_ : tni, wcp = cpy ? (tni - wr_ - 1) & 3 : hw;
      const int wof = ((tmi * 64 + (lane >> 3)) * ldw + wcp * D_ + wcn * 64 + (lane & 7) * 8) * 2;
      auto ld_w = [&]() {
        const __amdgpu_buffer_rsrc_t rw = __builtin_amdgcn_make_buffer_rsrc(const_cast<u16*>(W), 0, wld ? D_ * ldw * 2 : 0, 0x00020000);
#pragma unroll
        for (int r = 0; r < 8; ++r) wv[r] = __builtin_amdgcn_raw_buffer_load_b128(rw, wof, r * 8 * ldw * 2, 0);
        PIN();
      };
      auto wcopy = [&]() __attribute__((always_inline)) {
        if (cpy) {
          const __amdgpu_buffer_rsrc_t rw = __builtin_amdgcn_make_buffer_rsrc(ap_->wc, 0, D_ * ldw * 2, 0x00020000);
#pragma unroll
          for (int r = 0; r < 8; ++r) __builtin_amdgcn_raw_buffer_store_b128(wv[r], rw, wof + r * 8 * ldw * 2, 0, 16);
        }
        PIN();
      };
      const int wat = ((hw == 3 ? SMAP : WMAP) >> (4 * (tmi & 7))) & 3;
      unsigned ps = 0u;
      auto stouch = [&](int at) __attribute__((always_inline)) {
        if (at == STCH && cpy) ps = __builtin_amdgcn_raw_buffer_load_b32(__builtin_amdgcn_make_buffer_rsrc(const_cast<u16*>(W), 0, D_ * ldw * 2, 0x00020000),
                                                                        (((tmi ^ 8) * 64 + lane) * ldw + wcp * D_ + wcn * 64) * 2, 0, 0);
        PIN();
      };
      auto ld_x = [&](int h) {
#pragma unroll
        for (int r = 0; r < 2; ++r) xa[r][h] = *reinterpret_cast<const u32x4*>(A + (size_t)(srow + r) * D_ + (h ^ (tmi >> 3)) * 512 + lane * 8);
        PIN();
      };
      ld_x(0);
      if (X2AT == 0) ld_x(1);
      if (wat == 0) ld_w();
      unsigned pc = 0u;
      if (hw == 3) {
        unsigned long long a0, a1;
        CODE_ADDR("layers_kernel", a0);
        CODE_ADDR("end_marker_kernel", a1);
        pc = touch_code(a0, a1, loc, lane);
      }
      if (hw == 2) st_ag(gran + GR_N + bid * 64 + lane, lane + 1u);
      {
        const int gi = bid * 256 + t;
        if (gi < GRAN_N / 4) __builtin_amdgcn_raw_buffer_store_b128(u32x4{SENT, SENT, SENT, SENT}, __builtin_amdgcn_make_buffer_rsrc(hand, 0, GRAN_N * 4, 0x00020000), gi * 16, 0, 16);
        if (t < 24) __hip_atomic_store(stats_out + (t >> 3) * S_ + m0 + tni * 8 + (t & 7), 0.0, __ATOMIC_RELAXED, __HIP_MEMORY_SCOPE_AGENT);
      }
      tick();
      if (wat == 1) ld_w();
      if (X2AT == 1) ld_x(1);
      tick();
      if (wat == 2) ld_w();
      if (WCAT == 2) wcopy();
      if (X2AT == 2) ld_x(1);
      asm volatile("" :: "v"(ep));
      tick();
      if (wat == 3) ld_w();
      if (WCAT == 3) wcopy();
      if (X2AT == 3) ld_x(1);
      stouch(3);
#pragma unroll
      for (int i = 0; i < HW; ++i) { tick(); stouch(4 + i); }
      __builtin_amdgcn_s_setprio(HP1);
      PIN();
      tag = ep + 1u;
      const __amdgpu_buffer_rsrc_t rsc = __builtin_amdgcn_make_buffer_rsrc(gran, 0, GR_N * 4, 0x00020000);
      {
        u32x4 od;
#pragma unroll
        for (int q = 0; q < 4; ++q) od[q] = ((lane & 7) >> 1) == q ? ((lane & 1) ? 0x3f800000u : 0x00003f80u) : 0u;
        f32x4 d[2] = {{0.f, 0.f, 0.f, 0.f}, {0.f, 0.f, 0.f, 0.f}};
#pragma unroll
        for (int r = 0; r < 8; ++r) d[r & 1] = __builtin_amdgcn_mfma_f32_16x16x32_bf16(__builtin_bit_cast(s16x8, od), __builtin_bit_cast(s16x8, wv[r]), d[r & 1], 0, 0, 0);
        const float c0 = dpp_add<0x128, 0xf>(lane >= 32 ? d[0][2] + d[1][2] : d[0][0] + d[1][0]), c1 = dpp_add<0x128, 0xf>(lane >= 32 ? d[0][3] + d[1][3] : d[0][1] + d[1][1]);
        const int col = wcp * 64 + (lane & 7) * 8 + ((lane >> 4) & 1) * 4 + (lane >> 5) * 2;
        asm volatile("s_waitcnt vmcnt(0)" ::: "memory");
        __builtin_amdgcn_raw_buffer_store_b128(u32x4{__float_as_uint(c0), tag, __float_as_uint(c1), tag}, rsc,
                                               (lane & 8) || !wld ? GR_N * 4 : (GR_COL + ((wcn * 16 + tmi) * 192 + col) * 2) * 4, 0, 16);
      }
      asm volatile("" :: "v"(pc));
      tick_nw();
      stouch(6);
      const int tc = t < 192 ? t : 0;
      {
        float xv[2][16], mean[2];
#pragma unroll
        for (int r = 0; r < 2; ++r) {
          float sm = 0.f;
#pragma unroll
          for (int i = 0; i < 4; ++i) {
            xv[r][2 * i] = __uint_as_float(xa[r][0][i] << 16); xv[r][2 * i + 1] = __uint_as_float(xa[r][0][i] & 0xffff0000u);
            xv[r][8 + 2 * i] = __uint_as_float(xa[r][1][i] << 16); xv[r][8 + 2 * i + 1] = __uint_as_float(xa[r][1][i] & 0xffff0000u);
          }
#pragma unroll
          for (int i = 0; i < 16; ++i) sm += xv[r][i];
          mean[r] = wave_sum(sm) * (1.0f / 1024.0f);
          tick();
          stouch(7 + r);
        }
        float vr[2];
#pragma unroll
        for (int r = 0; r < 2; ++r) {
          float v = 0.f;
#pragma unroll
          for (int i = 0; i < 16; ++i) { const float d = xv[r][i] - mean[r]; v += d * d; }
          vr[r] = wave_sum(v) * (1.0f / 1024.0f);
          if (r == 0) tick();
        }
        const float vv = lane & 2 ? vr[1] : vr[0], mm = lane & 2 ? mean[1] : mean[0];
        const float rin = 1.0f / rbf(sqrtf(rbf(rbf(vv) + 1e-5f)));
        if (lane < 4) __builtin_amdgcn_raw_buffer_store_b64(u32x2{__float_as_uint((lane & 1) ? rbf(mm) : rin), tag}, rsc, (GR_ROW + srow * 4 + lane * 2) * 4, 0, 16);
      }
      tick();
      while (nb < POLLAT) tick();
      asm volatile("" :: "v"(ps));
#pragma unroll 1
      for (int spin = 0;; ++spin) {
        u32x2 csv[16];
        unsigned lo = tag, hi = tag;
        auto use = [&](int i0, int i1) __attribute__((always_inline)) {
#pragma unroll
          for (int i = i0; i < i1; i += 2) {
            lo = min(min(lo, csv[i][1]), csv[i + 1][1]); hi = max(max(hi, csv[i][1]), csv[i + 1][1]);
            cs += __uint_as_float(csv[i][0]); cs += __uint_as_float(csv[i + 1][0]);
          }
          asm volatile("" : "+v"(lo), "+v"(hi), "+v"(cs));
          PIN();
        };
        cs = 0.f;
#pragma unroll
        for (int i = 0; i < 16; ++i) {
          csv[i] = __builtin_amdgcn_raw_buffer_load_b64(rsc, (GR_COL + (tni * 3072 + tc) * 2) * 4 + i * 1536, 0, 16);
          if (i == PG1 - 1 || i == PG2 - 1) tick();
        }
        const u32x4 rp = __builtin_amdgcn_raw_buffer_load_b128(rsc, (GR_ROW + (m0 + (t & 127)) * 4) * 4, 0, 16);
        PIN();
        use(0, PG1);
        tick();
        use(PG1, PG2);
        tick();
        use(PG2, 16);
        const unsigned bad = (min(min(lo, rp[1]), rp[3]) ^ tag) | (max(max(hi, rp[1]), rp[3]) ^ tag);
        alpha = __uint_as_float(rp[0]); m = __uint_as_float(rp[2]);
        if (none(bad != 0u) || spin >= SPIN_MAX) break;
      }
      if (tmi == 0 && t < 192) st_ag(reinterpret_cast<unsigned*>(csf) + (t >> 6) * 1024 + n0 + (t & 63), __float_as_uint(cs));
      best = rmd = 0u;
      wsum = 0.f;
      asm volatile("" : "=v"(xq), KEPT("="));
#pragma unroll
      for (int i = 0; i < 8; ++i) asm volatile("" : "=v"(sl[i]));
    } else {
      asm volatile("; kept registers" : KEPT("="));
      PIN();
      ARGS_HERE;
      const int lane = fresh_lane(), t = hw * 64 + lane;
      best = rmd = 0u;
      tag = __builtin_amdgcn_readfirstlane(*LDS_AT(const volatile unsigned, lbase + FLG + 20));
#pragma unroll
      for (int i = 0; i < 8; ++i) asm volatile("" : "=v"(sl[i]));
      unsigned sad;
      auto tickc = [&]() __attribute__((always_inline)) {
        tick();
        if (nb == kcap + 1) {
#pragma unroll
          for (int i = 0; i < 8; ++i) {
            const unsigned a = (sad ^ ((i >> 2) * 64) ^ (((i >> 1) & 1) * 16)) + (i & 1) * 12288;
            sl[i] = __builtin_shufflevector(__builtin_amdgcn_ds_read_tr16_b64_v4i16(LDS_AT(s16x4, a)), __builtin_amdgcn_ds_read_tr16_b64_v4i16(LDS_AT(s16x4, a + 1536)), 0, 1, 2, 3, 4, 5, 6, 7);
          }
        }
        PIN();
      };
      static_assert(CAP0 + 8 <= KPOLL, "the stage is taken before that");
      if (hw == 1) STAMP(4, 4);
      if (hw == 0) {
        __builtin_amdgcn_s_sleep(TPRE);
        tile_at(3u);
        __builtin_amdgcn_s_sleep(XGATE);
        STAMP(4, 3);
#pragma unroll 1
        for (int spin = 0; spin < SPIN_MAX; ++spin) {
          if (none(ld_ag(gran + GR_X + (tmi * RX + tni % RX) * 128 + 64 + lane) != tag)) break;
          __builtin_amdgcn_s_sleep(1);
        }
      }
      if (hw == 0) STAMP(4, 5);
      asm volatile("" ::: "memory");
      PIN();
      __builtin_amdgcn_s_barrier();
      PIN();
      if (hw == 1) STAMP(4, 6);
      {
        const int ln = fresh_lane(), g4 = ln >> 4, tp = ln & 3, k = 8 * g4 + ((ln & 15) >> 2);
        sad = lbase + (kcap % NST) * STB + ABYTES + k * 384 + (((2 * (tp >> 1)) ^ (4 * ((k >> 1) & 1)) ^ (g4 & 1)) << 4) + 8 * (tp & 1) + (hw % 3) * 128;
      }
      wsum = __uint_as_float(__builtin_amdgcn_readfirstlane(*LDS_AT(const volatile unsigned, lbase + FLG + 60)));
      asm volatile("" : "+v"(sad));
      PIN();
#pragma unroll 1
      for (int spin = 0; spin < SPIN_MAX; ++spin) {
        const unsigned v = ld_ag(gran + GR_X + (tmi * RX + tni % RX) * 128 + lane);
        tickc();
        if (none(v != tag)) break;
      }
      asm volatile("" ::: "memory");
      PIN();
      const int row = m0 + (t & 127);
      const double s1 = ld_ag(stats + row), s2 = ld_ag(stats + S_ + row), sw = ld_ag(stats + 2 * S_ + row);
      cs = __uint_as_float(ld_ag(reinterpret_cast<const unsigned*>(csf) + (t < 192 ? (t >> 6) : 0) * 1024 + n0 + (t & 63)));
      tickc();
      float mean, var, mb, den;
      ln_from_sums(s1, s2, mean, var, mb, den);
      const float rin = frcp(den), g1 = 1.0f + rin;
      const float mb2 = rbf(mean + (mean - mb) * rin);
      const float den2 = rbf(__builtin_amdgcn_sqrtf(rbf(rbf(var * g1 * g1) + 1e-5f)));
      alpha = g1 * frcp(den2); m = (mb * rin + mb2) * frcp(g1);
      const __amdgpu_buffer_rsrc_t rsg = __builtin_amdgcn_make_buffer_rsrc(hand, 0, GRAN_N * 4, 0x00020000);
      if (tmi == tni && hw >= 2) {
        const float r = (float)fma(-(double)mb, (double)wsum, sw) * rin;
        const unsigned rb = pack2(r, r);
        unsigned rkey = (rb ^ ((unsigned)((int)rb >> 31) | 0x80000000u)) & 0xffff0000u;
        if (r != r) rkey = 0u;
        rkey = wave_umax(rkey | (unsigned)(2047 - row));
        const unsigned md = __builtin_amdgcn_readlane(f2word(__uint_as_float((__float_as_uint(mb) >> 16) | (__float_as_uint(den) & 0xffff0000u))), (2047 - (int)(rkey & 0xffffu)) & 63);
        __builtin_amdgcn_raw_buffer_store_b64(u32x2{rkey, md}, rsg, lane < NREP ? lane * 256 + (tmi * 2 + hw - 2) * 8 : GRAN_N * 4, 0, 16);
      }
      while (nb < KPOLL) tickc();
#pragma unroll 1
      for (int spin = 0; spin < SPIN_MAX; ++spin) {
        const u32x2 v = __builtin_amdgcn_raw_buffer_load_b64(rsg, (bid % NREP) * 256 + (lane & 31) * 8, 0, 16);
        tick(); tick();
        if (none(umax(v[0], v[1]) == SENT)) {
          best = wave_umax(v[0]);
          rmd = __builtin_amdgcn_readlane(v[1], ((2047 - (int)(best & 0xffffu)) >> 6) & 31);
          break;
        }
      }
      const int ix = (2047 - (int)(best & 0xffffu)) & 2047;
      if (row == ix) alpha = 0.f;
      xq = __builtin_amdgcn_raw_buffer_load_b64(__builtin_amdgcn_make_buffer_rsrc(const_cast<u16*>(A), 0, S_ * D_ * 2, 0x00020000), (ix * D_ + hw * 256 + (lane & 3) * 64 + (lane >> 2) * 4) * 2, 0, 16);
      PIN();
    }
    tag = __builtin_amdgcn_readfirstlane(tag);
    wsum = __uint_as_float(__builtin_amdgcn_readfirstlane(__float_as_uint(wsum)));
    const int lane = fresh_lane(), t = hw * 64 + lane;
    {
      unsigned c0, c1, c2, a0, a1, a2;
      split3(cs, c0, c1, c2);
      split3(-m, a0, a1, a2);
      u32x4 tw = {c0 | (c1 << 16), c0 | (c2 << 16), c1 | (c0 << 16), 0u}, ta = {a0 | (a0 << 16), a1 | (a0 << 16), a1 | (a2 << 16), 0u};
      asm volatile("" : "+v"(tw), "+v"(ta), "+v"(alpha));
      __builtin_amdgcn_s_setprio(TPRIO);
      if (nb < lim) {
        while (nb < lim) tick();
        __builtin_amdgcn_s_sleep(TSLP);
      }
      if (t < 192) *LDS_AT(u32x4, lbase + TABW + t * 16) = tw;
      if (t < 128) {
        *LDS_AT(u32x4, lbase + TABA + t * 16) = ta;
        *LDS_AT(float, lbase + ALPHA + t * 4) = alpha;
      }
    }
    if (t >= 192 && t < 196) *LDS_AT(u32x4, lbase + CRED + ((t >> 1) & 1) * 512 + (t & 1) * 128) = u32x4{0u, 0u, 0u, 0u};
    asm volatile("s_waitcnt lgkmcnt(0)" ::: "memory");
    PIN();
    __builtin_amdgcn_s_barrier();
    PIN();
    *LDS_AT(u32x4, lbase + XML + t * 16) = u32x4{SENT, SENT, SENT, SENT};
    *LDS_AT(u32x4, lbase + SPM + (t & 127) * 16) = u32x4{SENT, SENT, SENT, SENT};
    if (hw == 0) {
      unsigned v;
      asm volatile("v_mov_b32 %0, 0\n\tv_writelane_b32 %0, 2, 0\n\tv_writelane_b32 %0, %1, 5\n\tv_writelane_b32 %0, %2, 7"
                   : "=&v"(v) : "s"(tag), "s"(best));
      lds_wr(lbase + FLG + lane * 4, v);
    }
    const int idx = LAY2 ? (2047 - (int)(best & 0xffffu)) & 2047 : -64;
    const bool p1blk = (oni & 7) != 7, fast = tmi < 8, own = fast && (loc & 1);
    const int fg = xcd * 28 + (p1blk ? loc - (loc >> 3) : 0), ow = (xcd & 3) * 16 + (loc >> 1);
    static_assert(OMX == 4, "oni % 8 = loc % 8");
#define GLD(a, b) "global_load_dwordx4 v[" #a ":" #b "], %0, off\n\tv_lshl_add_u64 %0, %0, 0, %1\n\t"
#define GLD4(...) asm volatile(__VA_ARGS__ : "+v"(p) : "s"((unsigned long long)(F_ * 2)), KEPT() : "memory")
    auto gptr = [&](const u16* wg, const u16* wu) {
      const int ln = fresh_lane();
      return ((ln & 2) ? wu : wg) + (size_t)(hw * 256 + (ln >> 2) * 4) * F_ + fg * 16 + (ln & 1) * 8;
    };
    auto gather = [&](const u16*& p, int e) __attribute__((always_inline)) {
      if (e == 0) GLD4(GLD(104, 107) GLD(108, 111) GLD(112, 115) GLD(116, 119));
      else if (e == 1) GLD4(GLD(120, 123) GLD(124, 127) GLD(128, 131) GLD(132, 135));
      else if (e == 2) GLD4(GLD(136, 139) GLD(140, 143) GLD(144, 147) GLD(148, 151));
      else GLD4(GLD(152, 155) GLD(156, 159) GLD(160, 163) GLD(164, 167));
      p += 60 * F_;
    };
    auto dmask = [&](int q) { const int ln = fresh_lane(); return ((ln >> 1) & 1) == q ? ((ln & 1) ? 0xffff0000u : 0x0000ffffu) : 0u; };
    unsigned dm0, dm1, dad;
    auto gu_setup = [&]() __attribute__((always_inline)) {
      const int ln = fresh_lane();
      dm0 = dmask(0); dm1 = dmask(1);
      dad = lbase + SACT + ((ln & 3) * 8 + (ln >> 5) * 4 + ((ln >> 4) & 1)) * 16 + hw * 4;
      asm volatile("" : "+v"(dm0), "+v"(dm1), "+v"(dad));
    };
    auto gate_up = [&](u32x2 x, unsigned md) __attribute__((always_inline)) {
      const unsigned rin = __builtin_amdgcn_readfirstlane(__float_as_uint(frcp(__uint_as_float(md & 0xffff0000u))));
      asm volatile("s_getpc_b64 s[90:91]\n\ts_add_u32 s90, s90, gate_up_code@rel32@lo+4\n\ts_addc_u32 s91, s91, gate_up_code@rel32@hi+12\n\ts_swappc_b64 s[92:93], s[90:91]"
                   : "+{v[100:101]}"(x) : "{s84}"(md << 16), "{s85}"(md & 0xffff0000u), "{s86}"(rin), "{v99}"(dad), "{v102}"(dm0), "{v103}"(dm1), KEPT()
                   : "v84", "v85", "v86", "v87", "v88", "v89", "v90", "v91", "v92", "v93", "v94", "v95", "v96", "v97", "s88", "s89", "s90", "s91", "s92", "s93", "scc", "memory");
    };
    auto act_diag = [&](int so = 0) __attribute__((always_inline)) {
      const int ln = fresh_lane();
      const f32x4 g4 = *LDS_AT(const volatile f32x4, lbase + SACT + so + (ln >> 2) * 16), u4 = *LDS_AT(const volatile f32x4, lbase + SACT + so + 256 + (ln >> 2) * 16);
      float gv = (g4[0] + g4[1]) + (g4[2] + g4[3]), uv = (u4[0] + u4[1]) + (u4[2] + u4[3]);
      rbf2(gv, uv);
      const float av = rbf(gv * frcp(1.0f + __expf(-gv))) * uv;
      const unsigned ap2 = pack2(av, av);
      return __builtin_bit_cast(s16x4, u32x2{ap2 & dm0, ap2 & dm1});
    };
    auto down = [&](const s16x4& a, const u32x2 (&w)[16]) __attribute__((always_inline)) {
      f32x4 d[2] = {{0.f, 0.f, 0.f, 0.f}, {0.f, 0.f, 0.f, 0.f}};
      static_for<0, 16>([&](auto fc) __attribute__((always_inline)) {
        constexpr int f = decltype(fc)::value;
        d[f & 1] = __builtin_amdgcn_mfma_f32_4x4x4bf16_1k(a, __builtin_bit_cast(s16x4, w[f]), d[f & 1], 4, f, 0);
      });
      return u32x4{f2word(d[0][0] + d[1][0]), f2word(d[0][1] + d[1][1]), f2word(d[0][2] + d[1][2]), f2word(d[0][3] + d[1][3])};
    };
    auto fold16 = [&](f32x2 y0, f32x2 y1) __attribute__((always_inline)) {
      const auto q0 = __builtin_amdgcn_permlane32_swap(__float_as_uint(y0[0]), __float_as_uint(y1[0]), false, false);
      const auto q1 = __builtin_amdgcn_permlane32_swap(__float_as_uint(y0[1]), __float_as_uint(y1[1]), false, false);
      const auto q = __builtin_amdgcn_permlane16_swap(__float_as_uint(__uint_as_float(q0[0]) + __uint_as_float(q0[1])),
                                                      __float_as_uint(__uint_as_float(q1[0]) + __uint_as_float(q1[1])), false, false);
      return __uint_as_float(q[0]) + __uint_as_float(q[1]);
    };
    auto fold8 = [&](f32x2 y0, f32x2 y1) __attribute__((always_inline)) { return dpp_add<0x128, 0xf>(fold16(y0, y1)); };
    auto fold4 = [&](f32x2 y0, f32x2 y1) __attribute__((always_inline)) { return dpp_add<0x124, 0xf>(fold8(y0, y1)); };
    auto y_sum = [&](const __amdgpu_buffer_rsrc_t rs, auto nbc) __attribute__((always_inline)) {
      constexpr int NB = decltype(nbc)::value, N = 14 / NB;
      const int ln = fresh_lane(), src = (GRAN_N + (ln >> 2) * D_ + ow * 16 + (ln & 3) * 4) * 4;
      f32x2 y0 = {0.f, 0.f}, y1 = {0.f, 0.f};
#pragma unroll 1
      for (int bt = 0; bt < NB; ++bt) {
        f32x2 z0, z1;
#pragma unroll 1
        for (int spin = 0; spin < SPIN_MAX; ++spin) {
          u32x4 v[N];
#pragma unroll
          for (int i = 0; i < N; ++i) v[i] = __builtin_amdgcn_raw_buffer_load_b128(rs, src, (bt * N + i) * (16 * D_ * 4), 16);
          z0 = y0; z1 = y1;
#pragma unroll
          for (int i = 0; i < N; ++i) {
            z0 += f32x2{__uint_as_float(v[i][0]), __uint_as_float(v[i][1])};
            z1 += f32x2{__uint_as_float(v[i][2]), __uint_as_float(v[i][3])};
          }
          const float chk = (z0[0] + z0[1]) + (z1[0] + z1[1]);
          if (none(chk != chk)) break;
          unsigned mx = 0u;
#pragma unroll
          for (int i = 0; i < N; ++i) mx = umax(umax(umax(mx, v[i][0]), v[i][1]), umax(v[i][2], v[i][3]));
          if (none(mx == SENT)) break;
          __builtin_amdgcn_s_sleep(YSLP);
          asm volatile("" ::: "memory");
        }
        y0 = z0; y1 = z1;
      }
      return fold4(y0, y1);
    };
    [&]() __attribute__((always_inline)) {
    ARGS_HERE;
    const unsigned l16 = lane * 16;
    unsigned* kvflag = gran + GR_KV + ((LAY2 ? 16 : 0) + tni) * (32 * RK);
    auto wait_sig = [&](int sh) {
#pragma unroll 1
      for (int i = 0; i < 4 * SPIN_MAX; ++i) {
        if (((__builtin_amdgcn_readfirstlane(*LDS_AT(const volatile unsigned, lbase + FLG + 32)) >> sh) & 7u) >= 4u) break;
        __builtin_amdgcn_s_sleep(SSLP);
      }
      asm volatile("" ::: "memory");
      PIN();
    };
    PIN();
    asm volatile("s_waitcnt lgkmcnt(0)" ::: "memory");
    if (!LAY2) asm volatile("s_waitcnt vmcnt(0)" ::: "memory");
    __builtin_amdgcn_s_barrier();
    PIN();
    __builtin_amdgcn_s_setprio(HPRIO);
    if (hw < 3) {
      __builtin_amdgcn_s_sleep(SPRE);
      wait_sig(0);
      __builtin_amdgcn_raw_buffer_store_b32(tag, __builtin_amdgcn_make_buffer_rsrc(kvflag, 0, hw == 0 ? RK * 128 : 0, 0x00020000), lane * 128 + tmi * 4, 0, 16);
      if (hw == 0) STAMP(LAY2 ? 5 : 1, 16);
    }
    if (hw == 3) {
      __builtin_amdgcn_s_sleep(KVS);
      const unsigned kv0 = ld_ag(kvflag + (tmi % RK) * 32 + (lane & 15));
      PIN();
      const char* cnt = reinterpret_cast<const char*>(gran + GR_N + bid * 64 + 2);
      unsigned vk[8], vp = lbase + FLG + 8 + (lane & 7), pw = 0u;
#pragma unroll
      for (int j = 0; j < 8; ++j) { vk[j] = l16 + (j >> 2) * 131072u + ((j >> 1) & 1) * 4096u + (j & 1) * (VOFF - 2048u); asm volatile("" : "+v"(vk[j])); }
#define G4(a, b, c, d, sc) "s_nop 0\n\tglobal_load_lds_dwordx4 %" #a ", %8" sc "\n\tglobal_load_lds_dwordx4 %" #b ", %8 offset:1024" sc "\n\t"             \
                       "global_load_lds_dwordx4 %" #c ", %8 offset:2048" sc "\n\tglobal_load_lds_dwordx4 %" #d ", %8 offset:3072" sc "\n\t"
#define M4 "s_addk_i32 m0, 0x1000\n\t"
      auto stage = [&](const char* sp, unsigned dst) __attribute__((always_inline)) {
        asm volatile("s_mov_b32 m0, %9\n\t" G4(0, 0, 1, 1, " sc1") M4 G4(2, 2, 3, 3, " sc1")
                     M4 G4(4, 4, 5, 5, " sc1") M4 G4(6, 6, 7, 7, " sc1")
                     :: "v"(vk[0]), "v"(vk[1]), "v"(vk[2]), "v"(vk[3]), "v"(vk[4]), "v"(vk[5]), "v"(vk[6]), "v"(vk[7]), "s"(sp), "s"(dst) : "m0", "scc", "memory");
      };
      auto count = [&]() __attribute__((always_inline)) {
        asm volatile("s_mov_b32 m0, %2\n\ts_nop 0\n\tglobal_load_lds_dword %0, %1 sc1" :: "v"(0u), "s"(cnt), "s"(lbase + FLG - 252) : "m0", "memory");
        cnt += 8;
      };
      auto freed = [&](unsigned n) __attribute__((always_inline)) {
        if (!none(pw < n)) {
#pragma unroll 1
          for (int i = 0; i < 4 * SPIN_MAX; ++i) {
            pw = *LDS_AT(const volatile unsigned char, vp);
            if (none(pw < n)) break;
            __builtin_amdgcn_s_sleep(W3SLP);
          }
        }
        asm volatile("" ::: "memory");
      };
      unsigned long long ord = (0xfedcba9876543210ull >> 4 & ~((1ull << 4 * (tmi & 7)) - 1ull)) | (0xfedcba9876543210ull & ((1ull << 4 * (tmi & 7)) - 1ull));
      ord ^= (tmi & 8) * 0x1111111111111111ull;
      unsigned seen;
      const char* kvb = reinterpret_cast<const char*>(KV) + (size_t)tni * (128 * 2048);
      auto panel = [&]() __attribute__((always_inline)) {
        const unsigned pn = (unsigned)ord & 15u;
        ord >>= 4;
        if (__builtin_expect(!((seen >> pn) & 1u), 0)) {
#pragma unroll 1
          for (int i = 0; i < SPIN_MAX && !((seen >> pn) & 1u); ++i) seen = (unsigned)__ballot(ld_ag(kvflag + (tmi % RK) * 32 + (lane & 15)) == tag);
        }
        asm volatile("" ::: "memory");
        return kvb + pn * 8192u;
      };
      asm volatile("" : "+s"(ord), "+s"(kvb), "+s"(cnt));
      asm volatile("" : "+v"(vp), "+v"(pw));
      PIN();
      wait_sig(16);
      seen = (unsigned)__ballot(kv0 == tag);
      const char* sp = panel();
      stage(sp, lbase + 2 * ASB);
      count();
      const int ksp = LAY2 ? PNS / 2 : -1, ksg = LAY2 ? 64 : GF0;
      const u16* gp = gptr(wg, wu);
#pragma unroll 1
      for (int k = 1; k < 15; ++k) {
        if (__builtin_expect(k == ksp, 0)) {
          gu_setup();
          gate_up(xq, rmd);
          PIN();
          *LDS_AT(volatile unsigned char, lbase + FLG + 7) = (unsigned char)0x7f;
        }
        freed((unsigned)(0xba9876543211000ull >> 4 * k) & 15u);
        stage(sp + 2048, lbase + ((2 * k + 1) & 7) * ASB);
        sp = panel();
        stage(sp, lbase + ((2 * k + 2) & 7) * ASB);
        count();
        if (__builtin_expect((unsigned)(k - ksg) < 4u, 0)) gather(gp, k - ksg);
      }
      freed(12u);
      glds16s<true>(csf + 4096 + tni * 128, l16, lbase + UGL);
      stage(sp + 2048, lbase + 7 * ASB);
      count();
      const unsigned vwo = (unsigned)((lane >> 3) * D_ + oni * 64 + ((lane & 7) ^ ((lane >> 3) & 6)) * 8) * 2u;
      constexpr unsigned vs = 8u * D_ * 2u - 1024u;
#pragma unroll 1
      for (int ns = 0; ns < 6; ++ns) {
        freed((unsigned)(ns + 25) >> 1);
        const char* wp = reinterpret_cast<const char*>(wo) + (size_t)ns * (128 * D_ * 2);
#pragma unroll
        for (int j = 0; j < 4; ++j)
          glds16x4<false>(wp + j * (32u * D_ * 2u), vwo, vwo + vs, vwo + 2 * vs, vwo + 3 * vs, lbase + ns * ASB + j * 4096);
      }
      asm volatile("s_waitcnt vmcnt(0)" ::: "memory");
      return;
    }
    if (!LAY2) {
      auto until = [&](unsigned n) {
        while ((__builtin_amdgcn_readfirstlane(*LDS_AT(const volatile unsigned, lbase + FLG + 8)) & 255u) < n) __builtin_amdgcn_s_sleep(16);
        asm volatile("" ::: "memory");
        PIN();
      };
      if (hw == 0) {
        const __amdgpu_buffer_rsrc_t rsx = __builtin_amdgcn_make_buffer_rsrc((ap_ + 1)->xo, 0, S_ * D_ * 2, 0x00020000);
        until(IAT);
#pragma unroll 1
        for (int i = 0; i < 16; ++i) {
          __builtin_amdgcn_raw_buffer_store_b128(u32x4{SENT, SENT, SENT, SENT}, rsx, ((omi * 128 + i * 8 + (lane >> 3)) * D_ + oni * 64 + (lane & 7) * 8) * 2, 0, 16);
          __builtin_amdgcn_s_sleep(ISLP);
        }
      } else if (hw == 2) {
        const __amdgpu_buffer_rsrc_t rsi = __builtin_amdgcn_make_buffer_rsrc(hand + GRAN_N, 0, (2 * HAND_N + PS_N - GRAN_N) * 4, 0x00020000);
        until(IAT);
#pragma unroll 1
        for (int i = 0; i < ((2 * HAND_N + PS_N - GRAN_N) / 4 + 16383) / 16384; ++i) {
          __builtin_amdgcn_raw_buffer_store_b128(u32x4{SENT, SENT, SENT, SENT}, rsi, ((i * 256 + bid) * 64 + lane) * 16, 0, 16);
          __builtin_amdgcn_s_sleep(ISLP);
        }
      }
      const int ng = loc & 15, fc = xcd * 2 + (loc >> 4);
      const size_t og = (size_t)((fg - xcd * 28) / 7 * 256 + lane) * F_ + (xcd * 7 + fg % 7) * 64;
      const u16* pd = wd + (size_t)(fc * 224 + (lane < 56 ? lane : 0)) * D_ + ng * 64;
      const u16* p = gptr(wg, wu);
#pragma unroll 1
      for (unsigned n = T0; n < T0 + 13u; ++n) {
        until(n);
        if (hw == 1) {
          const unsigned i = n - T0;
          const u16* q = i < 8u ? ((i & 1u) ? wu : wg) + og + (size_t)(i >> 1) * (64 * F_) : pd + (size_t)((i - 8u - (i > TW)) & 3u) * (56 * D_);
          if (i == TW) {
            q = wo + (size_t)(768 + (loc / (32 / OMX)) * (256 / OMX) + (lane & (256 / OMX - 1) & 63)) * D_ + oni * 64;
            unsigned long long a0, a1;
            CODE_ADDR("layers_kernel", a0); CODE_ADDR("end_marker_kernel", a1);
            if (loc < 8) { const unsigned p1 = touch_code(a0, a1, 3 + loc, lane); asm volatile("" :: "v"(p1)); }
          }
          const unsigned p0 = touch(q);
          asm volatile("" :: "v"(p0));
        }
        if (n >= G0 && (n - G0) % GS == 0u && (n - G0) / GS < 4u) gather(p, (n - G0) / GS);
        if (n == UAT && hw == UHW) {
          u32x4 a[4][2], c[2];
#pragma unroll
          for (int i = 0; i < 4; ++i)
#pragma unroll
            for (int h = 0; h < 2; ++h) a[i][h] = *reinterpret_cast<const u32x4*>(wo + (size_t)(tni * 64 + tmi * 4 + i) * D_ + h * 512 + lane * 8);
#pragma unroll
          for (int h = 0; h < 2; ++h) c[h] = *reinterpret_cast<const u32x4*>(w_r + h * 512 + lane * 8);
          auto dot = [&](unsigned x, unsigned y, float z) { return __builtin_amdgcn_fdot2_f32_bf16(__builtin_bit_cast(bf16x2, x), __builtin_bit_cast(bf16x2, y), z, false); };
          PIN();
          __builtin_amdgcn_s_setprio(0);
#pragma unroll 1
          for (int i = 0; i < SPIN_MAX; ++i) {
            if (__builtin_amdgcn_readfirstlane(*LDS_AT(const volatile unsigned, lbase + FLG + 16)) >= UARR) break;
            __builtin_amdgcn_s_sleep(4);
          }
          PIN();
          float wm = 0.f;
#pragma unroll
          for (int e = 0; e < 8; ++e) wm = dot(c[e >> 2][e & 3], 0x3f803f80u, wm);
          wm = wave_sum(wm);
          lds_wr(lbase + FLG + 60, __float_as_uint(wm));
          wm *= -1.0f / 1024.0f;
          f32x2 u[2], g[2];
#pragma unroll
          for (int i = 0; i < 4; ++i) {
            float p1 = 0.f, pw = 0.f, p2 = 0.f;
#pragma unroll
            for (int e = 0; e < 8; ++e) {
              const unsigned x = a[i][e >> 2][e & 3];
              p1 = dot(x, 0x3f803f80u, p1); pw = dot(x, c[e >> 2][e & 3], pw); p2 = dot(x, x, p2);
            }
            u[i >> 1][i & 1] = fmaf(p1, wm, pw); g[i >> 1][i & 1] = p2;
          }
          float us = fold4(u[0], u[1]), gs = fold4(g[0], g[1]);
          us = dpp_add<0x121, 0xf>(dpp_add<0x122, 0xf>(us)); gs = dpp_add<0x121, 0xf>(dpp_add<0x122, 0xf>(gs));
          __builtin_amdgcn_raw_buffer_store_b32(__float_as_uint((lane & 1) ? __builtin_amdgcn_sqrtf(gs) * (1.0f / 32.0f) : us),
              __builtin_amdgcn_make_buffer_rsrc(csf + 4096 + tni * 128, 0, 512, 0x00020000), (lane & 14) ? 512 : ((lane & 1) * 64 + tmi * 4 + (lane >> 4)) * 4, 0, 16);
        }
      }
      if (hw == 2 && bid == 0) {
        const __amdgpu_buffer_rsrc_t rsc = __builtin_amdgcn_make_buffer_rsrc(gran, 0, GR_N * 4, 0x00020000);
#pragma unroll 1
        for (int spin = 0; spin < SPIN_MAX; ++spin) {
          bool ok = true;
#pragma unroll
          for (int i = 0; i < 4; ++i) ok &= __builtin_amdgcn_raw_buffer_load_b32(rsc, (GR_ROW + (i * 64 + lane) * 32 + 1) * 4, 0, 16) == tag;
          if (__all(ok)) break;
          __builtin_amdgcn_s_sleep(8);
          asm volatile("" ::: "memory");
        }
        if (lane == 0) st_ag(gran, tag);
      }
      return;
    }
    unsigned* X2 = hand + GRAN_N + Y_N;
    unsigned* XS = X2 + X2_N;
    unsigned* P4 = XS + XS_N;
    unsigned* PA = P4 + P4_N;
    const __amdgpu_buffer_rsrc_t rsh = __builtin_amdgcn_make_buffer_rsrc(hand, 0, HAND_N * 4, 0x00020000);
    auto signal = [&](unsigned ph) {
      asm volatile("" ::: "memory");
      PIN();
      *LDS_AT(volatile unsigned char, lbase + FLG + 4 + hw) = (unsigned char)ph;
    };
    auto wait_all = [&](unsigned ph) {
#pragma unroll 1
      for (int i = 0; i < SPIN_MAX; ++i) {
        if (all_ge(__builtin_amdgcn_readfirstlane(*LDS_AT(const volatile unsigned, lbase + FLG + 4)), ph)) break;
        __builtin_amdgcn_s_sleep(WSLP);
      }
      asm volatile("" ::: "memory");
      PIN();
    };
    gu_setup();
    gate_up(xq, rmd);
    signal(hw == 2 ? 2u : 0x7fu);
    if (wid == 8) STAMP(dk, 2);
    if (hw < 2) {
#pragma unroll 1
      for (int i = 0; i < SPIN_MAX; ++i) {
        if (__builtin_amdgcn_readfirstlane(*LDS_AT(const volatile unsigned, lbase + FLG)) >= FSTART) break;
        __builtin_amdgcn_s_sleep(WSLP);
      }
      asm volatile("" ::: "memory");
      PIN();
      if (wid == 8) STAMP(dk, 10);
      u32x2 w[16];
      const __amdgpu_buffer_rsrc_t rd_ = __builtin_amdgcn_make_buffer_rsrc(const_cast<u16*>(wd), 0, D_ * F_ * 2, 0x00020000);
      auto ldw = [&](int c) __attribute__((always_inline)) {
#pragma unroll
        for (int f = 0; f < 16; ++f) w[f] = __builtin_amdgcn_raw_buffer_load_b64(rd_, lane * 8 + c * 512 + (f & 1) * 2048, (fg * 16 + (f & ~1)) * (D_ * 2) + hw * 1024, 0);
        PIN();
      };
      ldw(0);
      const unsigned pt = __builtin_amdgcn_raw_buffer_load_b32(rd_, (lane >> 2) * (D_ * 2) + (lane & 3) * 128 + 512, fg * 16 * (D_ * 2) + hw * 1024, 0);
      int yo = p1blk ? (GRAN_N + fg * D_ + hw * 512) * 4 + lane * 16 : HAND_N * 4;
      asm volatile("" : "+v"(yo));
      wait_all(2u);
      if (wid == 8) STAMP(dk, 3);
      const s16x4 a = act_diag();
      const u32x4 y0 = down(a, w);
      PIN();
      ldw(1);
      __builtin_amdgcn_raw_buffer_store_b128(y0, rsh, yo, 0, 16);
      PIN();
      __builtin_amdgcn_raw_buffer_store_b128(down(a, w), rsh, yo + 1024, 0, 16);
      asm volatile("" :: "v"(pt));
      if (wid == 8) STAMP(dk, 4);
    }
    PIN();
    [&]() __attribute__((always_inline)) {
    const int lane = fresh_lane();
    const float rmb = __uint_as_float(rmd << 16);
    if (hw == 2) {
      wait_all(2u);
      if (own) {
        const float rden = __uint_as_float(rmd & 0xffff0000u), rrin = frcp(rden);
        const int col = ow * 16 + (lane & 3) * 4 + (lane >> 4);
        const unsigned xo2 = ld_ag(reinterpret_cast<const unsigned*>(A + (size_t)idx * D_ + (col & ~1)));
        __builtin_amdgcn_s_sleep(PRE1);
        const float y = y_sum(rsh, std::integral_constant<int, 2>{});
        const unsigned rk = best >> 16;
        const float rv = __uint_as_float(((rk & 0x8000u) ? (rk & 0x7fffu) : (~rk & 0xffffu)) << 16);
        const float rs = rbf(frcp(1.0f + __expf(-rv)));
        const float x1v = __uint_as_float((col & 1) ? xo2 & 0xffff0000u : xo2 << 16);
        float h0 = x1v, h1 = x1v;
        ln_apply2(h0, h1, rmb, rden, rrin);
        const float x2v = rbf(x1v + rbf(h0 + rbf(rbf(y) * rs)));
        const auto pr = __builtin_amdgcn_permlane16_swap(__float_as_uint(x2v), __float_as_uint(x2v), false, false);
        __builtin_amdgcn_raw_buffer_store_b32(f2word(__uint_as_float((pr[0] >> 16) | pr[1])), rsh, (lane & 28) ? HAND_N * 4 : (int)(X2 - hand) * 4 + col * 2, 0, 16);
        const float d = x2v - rmb;
        const float sd = wave_sum(d), sq = wave_sum(d * d);
        if (lane < 2) st_ag(XS + ow * 2 + lane, f2word(lane ? sq : sd));
        STAMP(dk, 5);
      }
    }
    {
      const int ks = kcap ^ (tmi & 8), g4 = lane >> 4;
      const int src = (int)(X2 - hand) * 4 + ks * 128 + g4 * 16;
      int dst = (lane & 12) ? HAND_N * 4 : (int)((P4 - hand) + (tni * 16 + ks) * 192 + hw * 64 + (lane & 2) * 16 + (lane & 1) * 8 + (g4 >> 1) * 16 + (g4 & 1) * 4) * 4;
      asm volatile("" : "+v"(dst));
      u32x4 x0, x1;
      if (!(hw == 2 && own)) __builtin_amdgcn_s_sleep(PRE4);
#pragma unroll 1
      for (int spin = 0; spin < SPIN_MAX; ++spin) {
        x0 = __builtin_amdgcn_raw_buffer_load_b128(rsh, src, 0, 16); x1 = __builtin_amdgcn_raw_buffer_load_b128(rsh, src + 64, 0, 16);
        if (none(umax(umax(umax(x0[0], x0[1]), umax(x0[2], x0[3])), umax(umax(x1[0], x1[1]), umax(x1[2], x1[3]))) == SENT)) break;
        __builtin_amdgcn_s_sleep(PSLP);
        asm volatile("" ::: "memory");
      }
      f32x4 d[4];
#pragma unroll
      for (int i = 0; i < 4; ++i) d[i] = MFMA16(sl[2 * i], __builtin_bit_cast(s16x8, x0), (f32x4{0.f, 0.f, 0.f, 0.f}));
#pragma unroll
      for (int i = 0; i < 4; ++i) d[i] = MFMA16(sl[2 * i + 1], __builtin_bit_cast(s16x8, x1), d[i]);
      u32x4 o;
#pragma unroll
      for (int e = 0; e < 4; ++e) o[e] = f2word((lane & 2) ? ((lane & 1) ? d[3][e] : d[2][e]) : ((lane & 1) ? d[1][e] : d[0][e]));
      __builtin_amdgcn_raw_buffer_store_b128(o, rsh, dst, 0, 16);
      PIN();
      if (wid == 8) STAMP(dk, 6);
    }
    if (hw == 2) {
      const int col = (lane & 15) * 4 + (lane >> 4), src = (int)((P4 - hand) + (tni * 16 + (lane >> 4)) * 192 + (lane & 15) * 4) * 4;
      const __amdgpu_buffer_rsrc_t rsq = __builtin_amdgcn_make_buffer_rsrc(hand, 0, fast ? HAND_N * 4 : 0, 0x00020000);
      float cs[3], val[3];
#pragma unroll
      for (int p = 0; p < 3; ++p) cs[p] = __uint_as_float(ld_ag(reinterpret_cast<const unsigned*>(csf) + p * 1024 + n0 + col));
      u32x2 xs;
      __builtin_amdgcn_s_sleep(PRE7);
#pragma unroll 1
      for (int spin = 0; spin < SPIN_MAX; ++spin) {
        u32x4 v[3][4];
        xs = __builtin_amdgcn_raw_buffer_load_b64(rsh, (int)((XS - hand) + lane * 2) * 4, 0, 16);
#pragma unroll
        for (int p = 2; p >= 0; --p)
#pragma unroll
          for (int i = 0; i < 4; ++i) v[p][i] = __builtin_amdgcn_raw_buffer_load_b128(p ? rsh : rsq, src + p * 256, i * (4 * 192 * 4), 16);
        auto part = [&](int p) __attribute__((always_inline)) {
          float z[4];
#pragma unroll
          for (int e = 0; e < 4; ++e) {
            z[e] = (__uint_as_float(v[p][0][e]) + __uint_as_float(v[p][1][e])) + (__uint_as_float(v[p][2][e]) + __uint_as_float(v[p][3][e]));
            asm volatile("" : "+v"(z[e]));
          }
          val[p] = fold16(f32x2{z[0], z[1]}, f32x2{z[2], z[3]});
        };
        part(2); part(1);
        val[0] = 0.f;
        if (fast) part(0);
        const float chk = (val[0] + val[1]) + (val[2] + __uint_as_float(xs[0])) + __uint_as_float(xs[1]);
        if (none(chk != chk)) break;
        unsigned mx = umax(xs[0], xs[1]);
#pragma unroll
        for (int i = 0; i < 12; ++i) mx = umax(umax(umax(mx, v[i >> 2][i & 3][0]), v[i >> 2][i & 3][1]), umax(v[i >> 2][i & 3][2], v[i >> 2][i & 3][3]));
        if (none(mx == SENT)) break;
        __builtin_amdgcn_s_sleep(PSLP);
        asm volatile("" ::: "memory");
      }
      const auto sw = __builtin_amdgcn_permlane32_swap(xs[0], xs[1], false, false);
      float sm = __uint_as_float(sw[0]) + __uint_as_float(sw[1]);
      sm = dpp_add<0x142, 0xa>(dpp_add<0x118, 0xf>(dpp_add<0x114, 0xf>(dpp_add<0x112, 0xf>(dpp_add<0x111, 0xf>(sm)))));
      const float dm = __int_as_float(__builtin_amdgcn_readlane(__float_as_int(sm), 31)) * (0.25f / 1024.0f);
      const float var = fmaxf(__int_as_float(__builtin_amdgcn_readlane(__float_as_int(sm), 63)) * (0.25f / 1024.0f) - dm * dm, 0.f);
      const float mb = rbf(rmb + dm);
      const float rin = frcp(rbf(__builtin_amdgcn_sqrtf(rbf(rbf(var) + 1e-5f))));
      const unsigned kv = pack2((val[1] - mb * cs[1]) * rin, (val[2] - mb * cs[2]) * rin);
      *LDS_AT(u16, lbase + TAB + 128 + kpos(col) * 2) = (u16)kv;
      *LDS_AT(u16, lbase + TAB + 256 + col * 2) = (u16)(kv >> 16);
      if (fast) *LDS_AT(u16, lbase + TAB + kpos(col) * 2) = f2bf((val[0] - mb * cs[0]) * rin * QSCALE);
      signal(7u);
      STAMP(dk, 7);
      if (own) {
#pragma unroll
        for (int i = 0; i < 14; ++i)
          __builtin_amdgcn_raw_buffer_store_b128(u32x4{SENT, SENT, SENT, SENT}, rsh, (GRAN_N + (i * 16 + (lane >> 2)) * D_ + ow * 16 + (lane & 3) * 4) * 4, 0, 16);
      }
      return;
    }
    if (!fast) return;
    const int head = tni * 2 + hw;
    const unsigned tb = lbase + TAB + hw * 64;
    {
      const __amdgpu_buffer_rsrc_t rs = __builtin_amdgcn_make_buffer_rsrc(KV, 0, 2 * S_ * D_ * 2, 0x00020000);
      const int src = (head * 64 + tmi * 4) * 2048 + lane * 16;
      auto ld = [&](int i, unsigned off) { return __builtin_bit_cast(s16x8, __builtin_amdgcn_raw_buffer_load_b128(rs, src + (i & 3) * 1024, (i >> 3) * 65536 + ((i >> 2) & 1) * 4096 + off, 16)); };
#pragma unroll 1
      for (int i = 0; i < SPIN_MAX; ++i)
        if (__builtin_amdgcn_readfirstlane(ld_ag(kvflag + (tmi % RK) * 32 + tmi + 8)) == tag) break;
      asm volatile("" ::: "memory");
      s16x8 f[16];
#pragma unroll
      for (int i = 0; i < 16; ++i) f[i] = ld(i, 0u);
      PIN();
      const bool on = ((lane ^ (lane >> 4)) & 1) == 0;
      const unsigned pl = lbase + RED + hw * 1024;
      unsigned pw = pl + (lane >> 4) * 64 + (lane & 15) * 2, pr = pl + (on ? (lane >> 5) * 16 : 512), qr = on ? tb + (lane >> 5) * 16 : pl + 512;
      const int gone = (((idx >> 7) & 7) == tmi) ? ((idx >> 10) * 2 + (kpos(idx & 31) >> 4)) * 64 + ((idx >> 5) & 3) * 16 + (kpos(idx & 31) & 15) : -1;
      asm volatile("" : "+v"(pw), "+v"(pr), "+v"(qr));
      *LDS_AT(u32x2, pl + 512 + lane * 8) = u32x2{0u, 0u};
      wait_all(7u);
      u32x4 q[2];
#pragma unroll
      for (int sx = 0; sx < 2; ++sx) {
        q[sx] = *LDS_AT(const u32x4, qr + sx * 32);
      }
      float sc[4];
#pragma unroll
      for (int i = 0; i < 8; ++i) {
        f32x4 d = MFMA16(__builtin_bit_cast(s16x8, q[0]), f[2 * i], (f32x4{0.f, 0.f, 0.f, 0.f}));
        d = MFMA16(__builtin_bit_cast(s16x8, q[1]), f[2 * i + 1], d);
        PIN();
        f[2 * i] = ld(2 * i, VOFF); f[2 * i + 1] = ld(2 * i + 1, VOFF);
        PIN();
#pragma unroll
        for (int r = 0; r < 2; ++r) sc[(i >> 2) * 2 + r] = (i & 3) == 0 || (lane >> 4) == (i & 3) ? d[r] : sc[(i >> 2) * 2 + r];
      }
      if (gone >= 0) {
#pragma unroll
        for (int r = 0; r < 4; ++r) sc[r] = r * 64 + lane == gone ? -INFINITY : sc[r];
      }
      const float mx = wave_fmax(fmaxf(fmaxf(sc[0], sc[1]), fmaxf(sc[2], sc[3])));
      const unsigned p0 = pack2(__builtin_amdgcn_exp2f(sc[0] - mx), __builtin_amdgcn_exp2f(sc[1] - mx)), p1 = pack2(__builtin_amdgcn_exp2f(sc[2] - mx), __builtin_amdgcn_exp2f(sc[3] - mx));
      *LDS_AT(u16, pw) = (u16)p0; *LDS_AT(u16, pw + 32) = (u16)(p0 >> 16);
      *LDS_AT(u16, pw + 256) = (u16)p1; *LDS_AT(u16, pw + 288) = (u16)(p1 >> 16);
      asm volatile("" ::: "memory");
      const float ls = wave_sum((__uint_as_float(p0 << 16) + __uint_as_float(p0 & 0xffff0000u)) + (__uint_as_float(p1 << 16) + __uint_as_float(p1 & 0xffff0000u)));
      f32x4 o = {0.f, 0.f, 0.f, 0.f};
#pragma unroll
      for (int i = 0; i < 16; ++i) o = MFMA16(*LDS_AT(const s16x8, pr + i * 32), f[i], o);
      int pv = __float_as_int((lane & 16) ? o[1] : o[0]);
      asm volatile("s_nop 0\n\tv_writelane_b32 %0, %1, 32\n\tv_writelane_b32 %0, %2, 33" : "+v"(pv) : "s"(ls), "s"(mx));
      __builtin_amdgcn_raw_buffer_store_b32(f2word(__int_as_float(pv)), rsh, lane < 34 ? (int)((PA - hand) + (head * 8 + tmi) * 64 + lane) * 4 : HAND_N * 4, 0, 16);
    }
    if (wid == 8) STAMP(dk, 8);
    if (tmi != ((idx >> 7) & 7)) return;
    {
      const int src = (int)((PA - hand) + head * 512 + (lane < 34 ? lane : 0)) * 4;
      const unsigned ta = tb + (lane & 31) * 2;
      const float so = wave_sum(lane < 32 ? bf2f(*LDS_AT(const u16, ta)) * bf2f(*LDS_AT(const u16, ta + 128)) : 0.f);
      const float vo = lane < 32 ? bf2f(*LDS_AT(const u16, ta + 256)) : 1.0f;
      unsigned v[8];
      __builtin_amdgcn_s_sleep(PRE5);
#pragma unroll 1
      for (int spin = 0; spin < SPIN_MAX; ++spin) {
        unsigned mx = 0u;
#pragma unroll
        for (int i = 0; i < 8; ++i) v[i] = __builtin_amdgcn_raw_buffer_load_b32(rsh, src + i * 256, 0, 16);
#pragma unroll
        for (int i = 0; i < 8; ++i) mx = umax(mx, v[i]);
        if (none(mx == SENT)) break;
        __builtin_amdgcn_s_sleep(PSLP);
        asm volatile("" ::: "memory");
      }
      float m8[8], mx = so;
#pragma unroll
      for (int i = 0; i < 8; ++i) { m8[i] = __int_as_float(__builtin_amdgcn_readlane((int)v[i], 33)); mx = fmaxf(mx, m8[i]); }
      float acc = __builtin_amdgcn_exp2f(so - mx) * vo;
#pragma unroll
      for (int i = 0; i < 8; ++i) acc += __builtin_amdgcn_exp2f(m8[i] - mx) * __uint_as_float(v[i]);
      const float l = __int_as_float(__builtin_amdgcn_readlane(__float_as_int(acc), 32));
      const float ov = acc / l;
      const float o1 = __int_as_float(__builtin_amdgcn_update_dpp(0, __float_as_int(ov), 0x55, 0xf, 0xf, false));
      const float o2 = __int_as_float(__builtin_amdgcn_update_dpp(0, __float_as_int(ov), 0xaa, 0xf, 0xf, false));
      const float o3 = __int_as_float(__builtin_amdgcn_update_dpp(0, __float_as_int(ov), 0xff, 0xf, 0xf, false));
      const __amdgpu_buffer_rsrc_t rso = __builtin_amdgcn_make_buffer_rsrc(Of, 0, S_ * D_ * 2, 0x00020000);
      if (lane < 32 && (lane & 3) == 0)
        __builtin_amdgcn_raw_buffer_store_b64(u32x2{pack2(ov, o1), pack2(o2, o3)}, rso,
            (((idx >> 5) * 32 + head) * 1024 + (lane >> 4) * 512 + (((lane >> 2) & 1) * 32 + (idx & 31)) * 8 + ((lane >> 3) & 1) * 4) * 2, 0, 16);
      asm volatile("s_waitcnt vmcnt(0)" ::: "memory");
      if (lane < RO) st_ag(gran + GR_O + ((16 + (idx >> 7)) * RO + lane) * 64 + 16 + head, tag);
      STAMP(dk, 9);
    }
    }();
    }();
    __builtin_amdgcn_s_setprio(0);
    asm volatile("s_waitcnt vmcnt(0) lgkmcnt(0)" ::: "memory");
    PIN();
    ARGS_HERE;
    u32x2 w[16];
    auto ld_wd = [&]() __attribute__((always_inline)) {
      const __amdgpu_buffer_rsrc_t rd_ = __builtin_amdgcn_make_buffer_rsrc(const_cast<u16*>(wd), 0, D_ * F_ * 2, 0x00020000);
#pragma unroll
      for (int f = 0; f < 16; ++f) w[f] = __builtin_amdgcn_raw_buffer_load_b64(rd_, t * 8 + (f & 1) * 2048, (fg * 16 + (f & ~1)) * (D_ * 2), 0);
      PIN();
    };
    float fnum = 0.f, fvar = 0.f;
    u32x2 wq = {0u, 0u};
    const int rrow = omi * 128 + (oni >> 3) * 64 + (hw & 1) * 32;
    if (LAY2) {
      ld_wd();
      wq = *reinterpret_cast<const u32x2*>(w_r + hw * 256 + (lane & 3) * 64 + (lane >> 2) * 4);
      if (!p1blk) {
        const int fr = rrow + (lane & 7) * 4 + (lane >> 4);
        const double s1 = ld_ag(stats + fr), s2 = ld_ag(stats + S_ + fr), sw = ld_ag(stats + 2 * S_ + fr);
        lds_wr(lbase + SPM + 132, __float_as_uint(wsum));
        float mean, var, mb, den;
        ln_from_sums(s1, s2, mean, var, mb, den);
        const float g1 = 1.0f + frcp(den);
        fnum = fr == idx ? __uint_as_float(0x7fc00000u) : fmaf(-mean, wsum, (float)sw) * g1;
        fvar = var * g1 * g1;
      }
      asm volatile("" : "+v"(fnum), "+v"(fvar));
      PIN();
    }
    if (hw == 3) {

      const int nfl = (LAY2 && (idx >> 7) == omi) ? 48 : 16;
      auto pfl = (__attribute__((address_space(1))) const unsigned*)(gran + GR_O + (((LAY2 ? 16 : 0) + omi) * RO + oni % RO) * 64 + (lane < nfl ? lane : 0));
      constexpr unsigned vs = 8u * D_ * 2u - 1024u;
      unsigned vw0 = (unsigned)((lane >> 3) * D_ + oni * 64 + ((lane & 7) ^ ((lane >> 3) & 6)) * 8) * 2u, vw1 = vw0 + vs, vw2 = vw0 + 2 * vs, vw3 = vw0 + 3 * vs;
      asm volatile("" : "+v"(pfl), "+v"(vw0), "+v"(vw1), "+v"(vw2), "+v"(vw3));
      auto wait_arr = [&](unsigned n) {
#pragma unroll 1
        for (int i = 0; i < 4 * SPIN_MAX; ++i) {
          if (__builtin_amdgcn_readfirstlane(*LDS_AT(const volatile unsigned, lbase + FLG + 16)) >= n) break;
          __builtin_amdgcn_s_sleep(1);
        }
        asm volatile("" ::: "memory");
        PIN();
      };
      wait_arr(12u);
      unsigned fl;
      asm volatile("global_load_dword %0, %1, off sc1" : "=v"(fl) : "v"(pfl) : "memory");
#pragma unroll 1
      for (int i = 0; i < 4 * SPIN_MAX; ++i) {
        if (__builtin_amdgcn_readfirstlane(*LDS_AT(const volatile unsigned, lbase + FLG + 24)) >= 8u) break;
        __builtin_amdgcn_s_sleep(1);
      }
      asm volatile("" ::: "memory");
      PIN();
#pragma unroll
      for (int j = 24; j < 32; ++j)
        glds16x4<false>(reinterpret_cast<const char*>(wo) + j * (32u * D_ * 2u), vw0, vw1, vw2, vw3, lbase + j * 4096);
      unsigned keep;
      asm volatile("s_mov_b32 %0, m0\n\ts_mov_b32 m0, %3\n\ts_nop 2\n\tglobal_load_lds_dword %1, %2 sc1\n\ts_mov_b32 m0, %0"
                   : "=&s"(keep) : "v"(0u), "s"(reinterpret_cast<const char*>(gran + GR_N + bid * 64 + 39)), "s"(lbase + FLG - 252) : "memory");
      asm volatile("s_waitcnt vmcnt(33)" : "+v"(fl) :: "memory");
      if (__builtin_amdgcn_ballot_w64(fl != tag)) {
        __builtin_amdgcn_s_sleep(CSL1);
#pragma unroll 1
        for (int spin = 0; spin < SPIN_MAX; ++spin) {
          if (!__builtin_amdgcn_ballot_w64(__hip_atomic_load(pfl, __ATOMIC_RELAXED, __HIP_MEMORY_SCOPE_AGENT) != tag)) break;
          __builtin_amdgcn_s_sleep(CSLP);
        }
      }
      asm volatile("" ::: "memory");
      PIN();
    }
    u32x4 pv[4];
    asm volatile("" : "=v"(pv[0]), "=v"(pv[1]), "=v"(pv[2]), "=v"(pv[3]));
    if (LAY2 && !p1blk && hw < 2) {
      const __amdgpu_buffer_rsrc_t rsp = __builtin_amdgcn_make_buffer_rsrc(hand + 2 * HAND_N + PP_O, 0, H_ * S_ * 4, 0x00020000);
      const int src = ((lane >> 3) * S_ + rrow + (lane & 7) * 4) * 4;
#pragma unroll 1
      for (int i = 0; i < 4 * SPIN_MAX; ++i) {
        if (__builtin_amdgcn_readfirstlane(*LDS_AT(const volatile unsigned, lbase + FLG + 24)) >= 8u) break;
        __builtin_amdgcn_s_sleep(2);
      }
#pragma unroll 1
      for (int spin = 0; spin < SPIN_MAX; ++spin) {
        asm volatile("" ::: "memory");
        unsigned mx = 0u;
#pragma unroll
        for (int i = 0; i < 4; ++i) pv[i] = __builtin_amdgcn_raw_buffer_load_b128(rsp, src, i * (8 * S_ * 4), 16);
#pragma unroll
        for (int i = 0; i < 4; ++i) mx = umax(umax(umax(mx, pv[i][0]), pv[i][1]), umax(pv[i][2], pv[i][3]));
        if (none(mx == SENT)) break;
        __builtin_amdgcn_s_sleep(PPSLP);
      }
    }
    asm volatile("s_waitcnt lgkmcnt(0)" ::: "memory");
    __builtin_amdgcn_s_barrier();
    if (!LAY2) {
      PIN();
      asm volatile(AGAIN "\ngate_up_code:\n\t"
                 "v_lshlrev_b32 v84, 16, v100\n\tv_and_b32 v85, 0xffff0000, v100\n\tv_lshlrev_b32 v86, 16, v101\n\tv_and_b32 v87, 0xffff0000, v101\n\t"
                 "v_subrev_f32 v84, s84, v84\n\tv_subrev_f32 v85, s84, v85\n\tv_subrev_f32 v86, s84, v86\n\tv_subrev_f32 v87, s84, v87\n\t"
                 "v_cvt_pk_bf16_f32 v85, v84, v85\n\tv_cvt_pk_bf16_f32 v87, v86, v87\n\ts_mov_b32 s88, 0x01000100\n\ts_mov_b32 s89, 0x03020302\n\t"
                 "v_lshlrev_b32 v84, 16, v85\n\tv_and_b32 v85, 0xffff0000, v85\n\tv_lshlrev_b32 v86, 16, v87\n\tv_and_b32 v87, 0xffff0000, v87\n\t"
                 "v_mul_f32 v88, s86, v84\n\tv_mul_f32 v89, s86, v85\n\tv_mul_f32 v90, s86, v86\n\tv_mul_f32 v91, s86, v87\n\tv_fma_f32 v84, -v88, s85, v84\n\t"
                 "v_fma_f32 v85, -v89, s85, v85\n\tv_fma_f32 v86, -v90, s85, v86\n\tv_fma_f32 v87, -v91, s85, v87\n\tv_fmac_f32 v88, s86, v84\n\t"
                 "v_fmac_f32 v89, s86, v85\n\tv_fmac_f32 v90, s86, v86\n\tv_fmac_f32 v91, s86, v87\n\tv_cvt_pk_bf16_f32 v88, v88, v89\n\t"
                 "v_cvt_pk_bf16_f32 v90, v90, v91\n\tv_perm_b32 v92, v88, v88, s88\n\tv_perm_b32 v93, v88, v88, s89\n\tv_perm_b32 v94, v90, v90, s88\n\t"
                 "v_perm_b32 v95, v90, v90, s89\n\ts_nop 0\n\tv_and_b32_dpp v96, v92, v102 quad_perm:[0,0,0,0] row_mask:0xf bank_mask:0xf\n\t"
                 "v_and_b32_dpp v97, v92, v103 quad_perm:[0,0,0,0] row_mask:0xf bank_mask:0xf\n\t"
                 "v_and_b32_dpp v100, v93, v102 quad_perm:[0,0,0,0] row_mask:0xf bank_mask:0xf\n\t"
                 "v_and_b32_dpp v101, v93, v103 quad_perm:[0,0,0,0] row_mask:0xf bank_mask:0xf\n\tv_mfma_f32_4x4x4_16b_bf16 v[84:87], v[96:97], v[104:105], 0\n\t"
                 "v_mfma_f32_4x4x4_16b_bf16 v[88:91], v[96:97], v[106:107], 0\n\tv_and_b32_dpp v96, v94, v102 quad_perm:[0,0,0,0] row_mask:0xf bank_mask:0xf\n\t"
                 "v_and_b32_dpp v97, v94, v103 quad_perm:[0,0,0,0] row_mask:0xf bank_mask:0xf\n\t"
                 "v_mfma_f32_4x4x4_16b_bf16 v[84:87], v[100:101], v[108:109], v[84:87]\n\tv_mfma_f32_4x4x4_16b_bf16 v[88:91], v[100:101], v[110:111], v[88:91]\n\t"
                 "v_and_b32_dpp v100, v95, v102 quad_perm:[0,0,0,0] row_mask:0xf bank_mask:0xf\n\t"
                 "v_and_b32_dpp v101, v95, v103 quad_perm:[0,0,0,0] row_mask:0xf bank_mask:0xf\n\tv_mfma_f32_4x4x4_16b_bf16 v[84:87], v[96:97], v[112:113], v[84:87]\n\t"
                 "v_mfma_f32_4x4x4_16b_bf16 v[88:91], v[96:97], v[114:115], v[88:91]\n\tv_and_b32_dpp v96, v92, v102 quad_perm:[1,1,1,1] row_mask:0xf bank_mask:0xf\n\t"
                 "v_and_b32_dpp v97, v92, v103 quad_perm:[1,1,1,1] row_mask:0xf bank_mask:0xf\n\t"
                 "v_mfma_f32_4x4x4_16b_bf16 v[84:87], v[100:101], v[116:117], v[84:87]\n\tv_mfma_f32_4x4x4_16b_bf16 v[88:91], v[100:101], v[118:119], v[88:91]\n\t"
                 "v_and_b32_dpp v100, v93, v102 quad_perm:[1,1,1,1] row_mask:0xf bank_mask:0xf\n\t"
                 "v_and_b32_dpp v101, v93, v103 quad_perm:[1,1,1,1] row_mask:0xf bank_mask:0xf\n\tv_mfma_f32_4x4x4_16b_bf16 v[84:87], v[96:97], v[120:121], v[84:87]\n\t"
                 "v_mfma_f32_4x4x4_16b_bf16 v[88:91], v[96:97], v[122:123], v[88:91]\n\tv_and_b32_dpp v96, v94, v102 quad_perm:[1,1,1,1] row_mask:0xf bank_mask:0xf\n\t"
                 "v_and_b32_dpp v97, v94, v103 quad_perm:[1,1,1,1] row_mask:0xf bank_mask:0xf\n\t"
                 "v_mfma_f32_4x4x4_16b_bf16 v[84:87], v[100:101], v[124:125], v[84:87]\n\tv_mfma_f32_4x4x4_16b_bf16 v[88:91], v[100:101], v[126:127], v[88:91]\n\t"
                 "v_and_b32_dpp v100, v95, v102 quad_perm:[1,1,1,1] row_mask:0xf bank_mask:0xf\n\t"
                 "v_and_b32_dpp v101, v95, v103 quad_perm:[1,1,1,1] row_mask:0xf bank_mask:0xf\n\tv_mfma_f32_4x4x4_16b_bf16 v[84:87], v[96:97], v[128:129], v[84:87]\n\t"
                 "v_mfma_f32_4x4x4_16b_bf16 v[88:91], v[96:97], v[130:131], v[88:91]\n\tv_and_b32_dpp v96, v92, v102 quad_perm:[2,2,2,2] row_mask:0xf bank_mask:0xf\n\t"
                 "v_and_b32_dpp v97, v92, v103 quad_perm:[2,2,2,2] row_mask:0xf bank_mask:0xf\n\t"
                 "v_mfma_f32_4x4x4_16b_bf16 v[84:87], v[100:101], v[132:133], v[84:87]\n\tv_mfma_f32_4x4x4_16b_bf16 v[88:91], v[100:101], v[134:135], v[88:91]\n\t"
                 "v_and_b32_dpp v100, v93, v102 quad_perm:[2,2,2,2] row_mask:0xf bank_mask:0xf\n\t"
                 "v_and_b32_dpp v101, v93, v103 quad_perm:[2,2,2,2] row_mask:0xf bank_mask:0xf\n\tv_mfma_f32_4x4x4_16b_bf16 v[84:87], v[96:97], v[136:137], v[84:87]\n\t"
                 "v_mfma_f32_4x4x4_16b_bf16 v[88:91], v[96:97], v[138:139], v[88:91]\n\tv_and_b32_dpp v96, v94, v102 quad_perm:[2,2,2,2] row_mask:0xf bank_mask:0xf\n\t"
                 "v_and_b32_dpp v97, v94, v103 quad_perm:[2,2,2,2] row_mask:0xf bank_mask:0xf\n\t"
                 "v_mfma_f32_4x4x4_16b_bf16 v[84:87], v[100:101], v[140:141], v[84:87]\n\tv_mfma_f32_4x4x4_16b_bf16 v[88:91], v[100:101], v[142:143], v[88:91]\n\t"
                 "v_and_b32_dpp v100, v95, v102 quad_perm:[2,2,2,2] row_mask:0xf bank_mask:0xf\n\t"
                 "v_and_b32_dpp v101, v95, v103 quad_perm:[2,2,2,2] row_mask:0xf bank_mask:0xf\n\tv_mfma_f32_4x4x4_16b_bf16 v[84:87], v[96:97], v[144:145], v[84:87]\n\t"
                 "v_mfma_f32_4x4x4_16b_bf16 v[88:91], v[96:97], v[146:147], v[88:91]\n\tv_and_b32_dpp v96, v92, v102 quad_perm:[3,3,3,3] row_mask:0xf bank_mask:0xf\n\t"
                 "v_and_b32_dpp v97, v92, v103 quad_perm:[3,3,3,3] row_mask:0xf bank_mask:0xf\n\t"
                 "v_mfma_f32_4x4x4_16b_bf16 v[84:87], v[100:101], v[148:149], v[84:87]\n\tv_mfma_f32_4x4x4_16b_bf16 v[88:91], v[100:101], v[150:151], v[88:91]\n\t"
                 "v_and_b32_dpp v100, v93, v102 quad_perm:[3,3,3,3] row_mask:0xf bank_mask:0xf\n\t"
                 "v_and_b32_dpp v101, v93, v103 quad_perm:[3,3,3,3] row_mask:0xf bank_mask:0xf\n\tv_mfma_f32_4x4x4_16b_bf16 v[84:87], v[96:97], v[152:153], v[84:87]\n\t"
                 "v_mfma_f32_4x4x4_16b_bf16 v[88:91], v[96:97], v[154:155], v[88:91]\n\tv_and_b32_dpp v96, v94, v102 quad_perm:[3,3,3,3] row_mask:0xf bank_mask:0xf\n\t"
                 "v_and_b32_dpp v97, v94, v103 quad_perm:[3,3,3,3] row_mask:0xf bank_mask:0xf\n\t"
                 "v_mfma_f32_4x4x4_16b_bf16 v[84:87], v[100:101], v[156:157], v[84:87]\n\tv_mfma_f32_4x4x4_16b_bf16 v[88:91], v[100:101], v[158:159], v[88:91]\n\t"
                 "v_and_b32_dpp v100, v95, v102 quad_perm:[3,3,3,3] row_mask:0xf bank_mask:0xf\n\t"
                 "v_and_b32_dpp v101, v95, v103 quad_perm:[3,3,3,3] row_mask:0xf bank_mask:0xf\n\tv_mfma_f32_4x4x4_16b_bf16 v[84:87], v[96:97], v[160:161], v[84:87]\n\t"
                 "v_mfma_f32_4x4x4_16b_bf16 v[88:91], v[96:97], v[162:163], v[88:91]\n\ts_nop 1\n\t"
                 "v_mfma_f32_4x4x4_16b_bf16 v[84:87], v[100:101], v[164:165], v[84:87]\n\tv_mfma_f32_4x4x4_16b_bf16 v[88:91], v[100:101], v[166:167], v[88:91]\n\t"
                 "s_nop 4\n\tv_permlane32_swap_b32 v84, v88\n\tv_permlane32_swap_b32 v85, v89\n\tv_permlane32_swap_b32 v86, v90\n\tv_permlane32_swap_b32 v87, v91\n\t"
                 "v_add_f32 v84, v84, v88\n\tv_add_f32 v85, v85, v89\n\tv_add_f32 v86, v86, v90\n\tv_add_f32 v87, v87, v91\n\ts_nop 0\n\t"
                 "v_permlane16_swap_b32 v84, v85\n\tv_permlane16_swap_b32 v86, v87\n\ts_nop 0\n\tv_add_f32 v84, v84, v85\n\tv_add_f32 v86, v86, v87\n\ts_nop 1\n\t"
                 "v_add_f32_dpp v84, v84, v84 row_ror:8 row_mask:0xf bank_mask:0xf\n\tv_add_f32_dpp v86, v86, v86 row_ror:8 row_mask:0xf bank_mask:0xf\n\ts_nop 0\n\t"
                 "v_add_f32_dpp v84, v84, v84 row_ror:4 row_mask:0xf bank_mask:0xf\n\tv_add_f32_dpp v86, v86, v86 row_ror:4 row_mask:0xf bank_mask:0xf\n\t"
                 "ds_write2_b32 v99, v84, v86 offset1:8\n\ts_setpc_b64 s[92:93]\n\t"
                   :: "{s[0:1]}"(ap + 1), "{s2}"(bid), "{v0}"(wid * 64), KEPT() : "s90", "s91", "scc", "memory");
      __builtin_unreachable();
    }
    {
    constexpr int dk = 8;
    constexpr int BEST = FLG + 40, MEET = FLG + 48;
    __builtin_amdgcn_s_setprio(HPRIO);
    if (hw == 0) STAMP(dk, 0);
    const unsigned tb1 = (unsigned)__builtin_amdgcn_s_memrealtime();
    unsigned* hand2 = hand + HAND_N;
    const __amdgpu_buffer_rsrc_t rsh = __builtin_amdgcn_make_buffer_rsrc(hand2, 0, (HAND_N + PS_N) * 4, 0x00020000);
    const __amdgpu_buffer_rsrc_t rsx = __builtin_amdgcn_make_buffer_rsrc(xout, 0, S_ * D_ * 2, 0x00020000);
    constexpr int OOR = (HAND_N + PS_N) * 4;
    if (!p1blk && hw < 2) {
      f32x2 a[2], c[2];
#pragma unroll
      for (int e = 0; e < 4; ++e) {
        a[e >> 1][e & 1] = (__uint_as_float(pv[0][e] << 16) + __uint_as_float(pv[1][e] << 16)) + (__uint_as_float(pv[2][e] << 16) + __uint_as_float(pv[3][e] << 16));
        c[e >> 1][e & 1] = (__uint_as_float(pv[0][e] & 0xffff0000u) + __uint_as_float(pv[1][e] & 0xffff0000u)) + (__uint_as_float(pv[2][e] & 0xffff0000u) + __uint_as_float(pv[3][e] & 0xffff0000u));
      }
      const float r = (fnum + fold8(a[0], a[1])) * __builtin_amdgcn_rsqf(fvar + fold8(c[0], c[1]));
      unsigned key = (__float_as_uint(r) ^ ((unsigned)(__float_as_int(r) >> 31) | 0x80000000u)) & 0xfffff800u;
      if (r != r) key = 0u;
      key = wave_umax(key | (unsigned)(2047 - (rrow + (lane & 7) * 4 + (lane >> 4))));
      __builtin_amdgcn_raw_buffer_store_b64(u32x2{key, tb1 & 0x7fffffffu}, rsh, lane < NREP ? (HAND_N + PK_O + lane * 128 + (rrow >> 5) * 2) * 4 : OOR, 0, 16);
      if (hw == 1) STAMP(dk, 2);
    }
    gu_setup();
    bool got = true;
    unsigned fra = lbase;
    int fro = S_ * D_ * 2;
    auto get_fc = [&]() __attribute__((always_inline)) {
      const u32x2 fw = __builtin_amdgcn_raw_buffer_load_b64(rsh, (HAND_N + PK_O + (bid % NREP) * 128 + lane * 2) * 4, 0, 16);
      got = none(umax(fw[0], fw[1]) == SENT);
      if (got) {
        const unsigned c1 = wave_umax(fw[0]), c2 = wave_umax(fw[0] == c1 ? 0u : fw[0]);
#define BADFC 0
        const unsigned r1 = (2047u - (c1 & 0x7ffu)) ^ BADFC, r2 = 2047u - (c2 & 0x7ffu);
        const unsigned r = (lane & 8) ? r2 : r1;
        fra = lbase + QOFF + (r & 127u) * 128 + (((r ^ lane) & 7u) << 4);
        fro = (r >> 7) == (unsigned)omi && lane < 16 ? (int)(r * D_ + oni * 64 + (lane & 7) * 8) * 2 : S_ * D_ * 2;
        unsigned dt = (unsigned)((int)((fw[1] - tb1) << 1) >> 1) + 512u;
        dt = dt < 1024u ? dt : 512u;
        *LDS_AT(volatile u32x2, lbase + SPM + 144) = u32x2{r1 | (r2 << 16), (unsigned)__builtin_amdgcn_readlane(dt, r1 >> 5) | (wave_umax(dt) << 16)};
      }
    };
    if (hw == 0) {
      __builtin_amdgcn_s_sleep(FCPRE);
#pragma unroll 1
      for (int i = 0; i < SPIN_MAX; ++i) {
        if (__builtin_amdgcn_readfirstlane(*LDS_AT(const volatile unsigned, lbase + FLG + 28)) - best >= 4u) break;
        __builtin_amdgcn_s_sleep(FCSLP);
      }
      asm volatile("" ::: "memory");
      get_fc();
    } else __builtin_amdgcn_s_sleep(TPRE - 10);
    PIN();
    tile_at(2u);
    const unsigned tb2 = (unsigned)__builtin_amdgcn_s_memrealtime() - 512u;
    LOGDEF;
    LOG();
#pragma unroll 1
    for (int spin = 0; spin < SPIN_MAX && !got; ++spin) { get_fc(); asm volatile("" ::: "memory"); }
    if (hw == 0) __builtin_amdgcn_raw_buffer_store_b128(lds_rd4(fra), rsx, fro, 0, XAUX);
    PIN();
    u32x2 fm = {SENT, 0u};
#pragma unroll 1
    for (int i = 0; i < 4 * SPIN_MAX && fm[0] == SENT; ++i) {
      const u32x2 q = *LDS_AT(const volatile u32x2, lbase + SPM + 144);
      fm = u32x2{(unsigned)__builtin_amdgcn_readfirstlane(q[0]), (unsigned)__builtin_amdgcn_readfirstlane(q[1])};
    }
    const int fr1 = fm[0] & 0x7ffu, fr2 = (fm[0] >> 16) & 0x7ffu;
    const unsigned t1 = tb2 + (fm[1] & 1023u), tmx = tb2 + ((fm[1] >> 16) & 1023u);
    auto until = [&](unsigned te) { while ((int)((unsigned)__builtin_amdgcn_s_memrealtime() - te) < 0) __builtin_amdgcn_s_sleep(1); asm volatile("" ::: "memory"); };
    lds_wr(lbase + SPM + 136, tmx);
    PIN();
    if (hw == 0) STAMP(dk, 3);
    const int xoff = (hw * 256 + (lane & 3) * 64 + (lane >> 2) * 4) * 2;
    const int oc = (omi * 2 + (oni >> 3)) * 32, col = oc + (lane & 7) * 4 + (lane >> 4);
    const int xoo = !p1blk && hw == 3 ? (col & ~1) * 2 : S_ * D_ * 2;
    auto get_row = [&](int r, u32x2& xr, unsigned& xo) __attribute__((always_inline)) {
      if ((r >> 7) == omi) {
#pragma unroll 1
        for (int i = 0; i < SPIN_MAX; ++i) {
          if (__builtin_amdgcn_readfirstlane(*LDS_AT(const volatile unsigned, lbase + FLG + 24)) >= 16u) break;
          __builtin_amdgcn_s_sleep(1);
        }
        asm volatile("" ::: "memory");
      }
#pragma unroll 1
      for (int spin = 0; spin < SPIN_MAX; ++spin) {
        LOG();
        xr = __builtin_amdgcn_raw_buffer_load_b64(rsx, xoff, r * (D_ * 2), 16);
        xo = __builtin_amdgcn_raw_buffer_load_b32(rsx, xoo, r * (D_ * 2), 16);
        LOG();
        if (none(umax(umax(xr[0], xr[1]), xo) == SENT)) break;
        __builtin_amdgcn_s_sleep(XRSLP);
        asm volatile("" ::: "memory");
      }
      PIN();
    };
    unsigned rk1;
    auto row_md = [&](const u32x2& xr, int k) __attribute__((always_inline)) {
      auto dot = [&](unsigned x, unsigned y, float z) { return __builtin_amdgcn_fdot2_f32_bf16(__builtin_bit_cast(bf16x2, x), __builtin_bit_cast(bf16x2, y), z, false); };
      const float p1 = dot(xr[1], 0x3f803f80u, dot(xr[0], 0x3f803f80u, 0.f)), p2 = dot(xr[1], xr[1], dot(xr[0], xr[0], 0.f)), p3 = dot(xr[1], wq[1], dot(xr[0], wq[0], 0.f));
      const auto q = __builtin_amdgcn_permlane32_swap(__float_as_uint(p1), __float_as_uint(p2), false, false);
      const auto q3 = __builtin_amdgcn_permlane32_swap(__float_as_uint(p3), __float_as_uint(p3), false, false);
      const auto q2 = __builtin_amdgcn_permlane16_swap(__float_as_uint(__uint_as_float(q[0]) + __uint_as_float(q[1])), __float_as_uint(__uint_as_float(q3[0]) + __uint_as_float(q3[1])), false, false);
      float sv = __uint_as_float(q2[0]) + __uint_as_float(q2[1]);
      sv = dpp_add<0x121, 0xf>(dpp_add<0x122, 0xf>(dpp_add<0x124, 0xf>(dpp_add<0x128, 0xf>(sv))));
      lds_wr(lbase + SPM + k * 64 + (lane >> 4) * 16 + hw * 4, f2word(sv));
      u32x4 a, c, e;
#pragma unroll 1
      for (int i = 0; i < SPIN_MAX; ++i) {
        a = lds_rd4(lbase + SPM + k * 64); c = lds_rd4(lbase + SPM + k * 64 + 32); e = lds_rd4(lbase + SPM + k * 64 + 16);
        if (__builtin_amdgcn_readfirstlane(umax(umax(a[0], a[1]), umax(a[2], a[3]))) != SENT) break;
      }
      auto sum4 = [&](const u32x4& v) { return (__uint_as_float(v[0]) + __uint_as_float(v[1])) + (__uint_as_float(v[2]) + __uint_as_float(v[3])); };
      const float mean = sum4(a) * (1.0f / 1024.0f), msq = sum4(c) * (1.0f / 1024.0f);
      const float den = rbf(__builtin_amdgcn_sqrtf(rbf(rbf(fmaxf(fmaf(-mean, mean, msq), 0.f)) + 1e-5f)));
      if (k == 0) { const float r = fmaf(-rbf(mean), wsum, sum4(e)) * frcp(den); rk1 = __builtin_amdgcn_readfirstlane(pack2(r, r)) >> 16; }
      return (unsigned)__builtin_amdgcn_readfirstlane((pack2(mean, mean) & 0xffffu) | (__float_as_uint(den) & 0xffff0000u));
    };
    auto ffn = [&](const u32x2& xr, unsigned md, unsigned n, const __amdgpu_buffer_rsrc_t rs, int yo, auto&& off) __attribute__((always_inline)) {
      gate_up(xr, md);
      if (hw == 0 && n == 2u) STAMP(dk, 8);
      asm volatile("" ::: "memory");
      PIN();
      lds_wr(lbase + MEET + hw * 4, n + off());
      u32x4 mt;
#pragma unroll 1
      for (int i = 0; i < SPIN_MAX; ++i) {
        mt = lds_rd4(lbase + MEET);
        if (__builtin_amdgcn_readfirstlane(min4(mt)) >= n) break;
      }
      asm volatile("" ::: "memory");
      PIN();
      if (hw == 0 && n == 2u) STAMP(dk, 9);
      if (__builtin_amdgcn_readfirstlane(mt[0]) != n + 1u)
        __builtin_amdgcn_raw_buffer_store_b128(down(act_diag(n == 4u ? 512 : 0), w), rs, (yo + fg * D_) * 4 + t * 16, 0, 16);
      dad += n == 2u ? 512 : -512;
      if (hw == 0 && n == 2u) STAMP(dk, 4);
    };
    auto y_sum4 = [&](const __amdgpu_buffer_rsrc_t rs, int yo, int k) __attribute__((always_inline)) {
      const int src = (yo + (hw * 56 + (lane >> 3)) * D_ + oc + (lane & 7) * 4) * 4;
      u32x4 v[7];
#pragma unroll
      for (int i = 0; i < 7; ++i) asm volatile("" : "=v"(v[i]));
      unsigned todo = 0x7fu;
#pragma unroll 1
      for (int spin = 0; spin < SPIN_MAX; ++spin) {
        LOG();
#pragma unroll
        for (int i = 0; i < 7; ++i) if ((todo >> i) & 1u) v[i] = __builtin_amdgcn_raw_buffer_load_b128(rs, src, i * (8 * D_ * 4), 16);
#pragma unroll
        for (int i = 0; i < 7; ++i) if (none(umax(umax(v[i][0], v[i][1]), umax(v[i][2], v[i][3])) == SENT)) todo &= ~(1u << i);
        if (!todo) break;
        __builtin_amdgcn_s_sleep(YSLP);
        asm volatile("" ::: "memory");
      }
      f32x2 z0 = {0.f, 0.f}, z1 = {0.f, 0.f};
#pragma unroll
      for (int i = 0; i < 7; ++i) {
        z0 += f32x2{__uint_as_float(v[i][0]), __uint_as_float(v[i][1])};
        z1 += f32x2{__uint_as_float(v[i][2]), __uint_as_float(v[i][3])};
      }
      const unsigned ya = lbase + SPM + 256 + k * 512 + ((lane & 7) * 4 + (lane >> 4)) * 16;
      lds_wr((lane & 8) ? lbase + DUM + lane * 4 : ya + hw * 4, f2word(fold8(z0, z1)));
      u32x4 q = {0u, 0u, 0u, 0u};
      if (hw == 3) {
#pragma unroll 1
        for (int i = 0; i < 4 * SPIN_MAX; ++i) {
          q = lds_rd4(ya);
          if (none(umax(umax(q[0], q[1]), umax(q[2], q[3])) == SENT)) break;
        }
      }
      return (__uint_as_float(q[0]) + __uint_as_float(q[1])) + (__uint_as_float(q[2]) + __uint_as_float(q[3]));
    };
    const __amdgpu_buffer_rsrc_t rs1 = __builtin_amdgcn_make_buffer_rsrc(hand, 0, HAND_N * 4, 0x00020000);
    u32x2 xr;
    unsigned xo, xo2;
    until(t1 + XDT);
    get_row(fr1, xr, xo);
    if (hw == 0) STAMP(dk, 7);
#define FAILFC 0
    const unsigned md1 = row_md(xr, 0) ^ (FAILFC & 1);
    if (hw == 0) STAMP(dk, 10);
    LOGAT(20);
    u32x2 xr2;
    unsigned md2 = 0u;
    if (p1blk) {
      xr2 = __builtin_amdgcn_raw_buffer_load_b64(rsx, (fr2 >> 7) == omi ? S_ * D_ * 2 : xoff, fr2 * (D_ * 2), 16);
      PIN();
      ffn(xr, md1, 2u, rsh, GRAN_N, [&]() { return 0u; });
      __builtin_amdgcn_s_setprio(R2PRIO);
      LOG();
      if ((fr2 >> 7) == omi || !none(umax(xr2[0], xr2[1]) == SENT)) get_row(fr2, xr2, xo2);
      md2 = row_md(xr2, 1) ^ (FAILFC & 2);
      ffn(xr2, md2, 4u, rs1, GRAN_N, [&]() {
        const u32x2 v = *LDS_AT(const volatile u32x2, lbase + BEST);
        return v[0] != 0u && (2047 - (int)(v[0] & 0x7ffu) != fr2 || v[1] != md2) ? 1u : 0u; });
    }
    if (hw == 0) STAMP(dk, 5);
    LOG();
    float y = 0.f;
    auto put_row = [&](int r, unsigned md, unsigned rb) __attribute__((always_inline)) {
      const float rden = __uint_as_float(md & 0xffff0000u), rs = rbf(frcp(1.0f + __expf(-__uint_as_float(rb << 16))));
      const float xa = __uint_as_float((col & 1) ? xo & 0xffff0000u : xo << 16);
      float ha = xa, hb = xa;
      ln_apply2(ha, hb, __uint_as_float(md << 16), rden, frcp(rden));
      const float o = xa + rbf(ha + rbf(rbf(y) * rs));
      __builtin_amdgcn_raw_buffer_store_b16((unsigned short)(pack2(o, o) & 0xffffu), __builtin_amdgcn_make_buffer_rsrc(out, 0, hw == 3 ? S_ * D_ * 2 : 0, 0x00020000),
                                            (lane & 8) ? S_ * D_ * 2 : (r * D_ + col) * 2, 0, OAUX);
    };
    if (!p1blk) {
      until(t1 + XDT + YD1); until(tmx + YD2);
      if (hw == 3) STAMP(dk, 13);
      LOGAT(8);
      y = y_sum4(rsh, GRAN_N, 0);
      put_row(fr1, md1, rk1);
      if (hw == 3) STAMP(dk, 14);
      LOGAT(24);
    }
    unsigned bmd = 0u;
    best = 0u;
#pragma unroll 1
    for (int spin = 0; spin < 4 * SPIN_MAX; ++spin) {
      const u32x2 v = *LDS_AT(const volatile u32x2, lbase + BEST);
      best = __builtin_amdgcn_readfirstlane(v[0]); bmd = __builtin_amdgcn_readfirstlane(v[1]);
      if (best) break;
      __builtin_amdgcn_s_sleep(1);
    }
    asm volatile("" ::: "memory");
    const int idx = 2047 - (int)(best & 0x7ffu);
    const unsigned rk = best >> 16, rb = (rk & 0x8000u) ? (rk & 0x7fffu) : (~rk & 0xffffu);
    if (hw == 0) STAMP(dk, 1);
    LOGAT(25);
    LOGOUT(hw);
    if (__builtin_expect(idx != fr1 || bmd != md1 || rb != rk1, 0)) {
      const __amdgpu_buffer_rsrc_t rsf = __builtin_amdgcn_make_buffer_rsrc(gran + GR_F, 0, !p1blk && hw == 3 && idx != fr1 ? 48 * 128 : 0, 0x00020000);
      asm volatile("s_waitcnt vmcnt(0)" ::: "memory");
      __builtin_amdgcn_raw_buffer_store_b32(tag, rsf, lane == 0 ? (16 + (oc >> 5)) * 128 : 48 * 128, 0, 16);
      if (idx != fr1 || bmd != md1) {
        if (!p1blk || idx != fr2 || bmd != md2) get_row(idx, xr, xo);
        if (!p1blk && idx == fr2) md2 = row_md(xr, 1) ^ (FAILFC & 2);
        if (idx == fr2 && bmd == md2) {
          if (!p1blk) y = y_sum4(rs1, GRAN_N, 1);
        } else {
          if (p1blk) ffn(xr, bmd, 6u, rsh, HAND_N + Y3_O, [&]() { return 0u; });
          else y = y_sum4(rsh, HAND_N + Y3_O, 2);
        }
      }
      if (p1blk || hw != 3) return;
#pragma unroll 1
      for (int spin = 0; spin < SPIN_MAX && idx != fr1; ++spin) {
        if (__builtin_amdgcn_readfirstlane(__builtin_amdgcn_raw_buffer_load_b32(rsf, (oc >> 6) * 128, 0, 16)) == tag) break;
        asm volatile("" ::: "memory");
      }
      put_row(idx, bmd, rb);
    }
    if (p1blk || hw != 3) return;
    }
    return;
  }
  const int lane = fresh_lane();
  if (wid >= 4) {
    const int lw = wid - 4;
    const long long lay_ = ap->lay;
    const u16* A = ap->a; const u16* W = ap->W; const u16* wc = ap->wc;
#define LPRIO 3
#define LP0 3
    __builtin_amdgcn_s_setprio(LP0);
    unsigned va[4], vw[6];
#pragma unroll
    for (int i = 0; i < 4; ++i) {
      const int L = (lw * 4 + i) * 64 + lane, row = L >> 3, cp = L & 7, c = cp ^ (((row >> 1) & 3) | (((row >> 4) & 1) << 2));
      va[i] = (unsigned)((m0 + row) * K + c * 8) * 2u;
    }
#pragma unroll
    for (int i = 0; i < 6; ++i) {
      const int L2 = (lw * 6 + i) * 64 + lane, k = L2 / 24, cp = L2 % 24, c = cp ^ (4 * ((k >> 1) & 1)) ^ ((k >> 3) & 1);
      vw[i] = (unsigned)(k * ldw + (c >> 3) * 1024 + n0 + (c & 7) * 8) * 2u;
    }
    PIN();
    asm volatile("" : "+v"(va[0]), "+v"(va[1]), "+v"(va[2]), "+v"(va[3]), "+v"(vw[0]), "+v"(vw[1]), "+v"(vw[2]), "+v"(vw[3]), "+v"(vw[4]), "+v"(vw[5]));
    const bool LAY2 = lay_ != 0;
    const int krot = tmi & 8;
    if (LAY2 && slow_cols(W, tni)) W = wc;
    auto stage_a = [&](int kt, int slot) {
      const char* pa = reinterpret_cast<const char*>(A) + (size_t)(kt ^ krot) * 128;
#pragma unroll
      for (int i = 0; i < 4; ++i) glds16s<true>(pa, va[i], lbase + slot * STB + (lw * 4 + i) * 1024);
    };
    auto stage_w = [&](int kt, int slot) {
      const char* pw = reinterpret_cast<const char*>(W) + (size_t)(kt ^ krot) * 128 * ldw;
#pragma unroll
      for (int i = 0; i < 6; ++i) glds16s<true>(pw, vw[i], lbase + slot * STB + ABYTES + (lw * 6 + i) * 1024);
    };
    if (LAY2) {
      __builtin_amdgcn_s_setprio(0);
      __builtin_amdgcn_s_sleep(TPRE - 10);
      tile_at(3u);
      __builtin_amdgcn_s_setprio(LPRIO);
#pragma unroll
      for (int st = 0; st < NST; ++st) stage_w(st, st);
      PIN();
      __builtin_amdgcn_s_barrier();
      PIN();
#pragma unroll
      for (int st = 0; st < NST; ++st) stage_a(st, st);
      asm volatile("s_waitcnt vmcnt(8)" ::: "memory");
    } else {
      __builtin_amdgcn_s_setprio(LPRIO);
      stage_a(0, 0); stage_w(0, 0);
      asm volatile("s_waitcnt vmcnt(0)" ::: "memory");
    }
    int slot = 0;
    __builtin_amdgcn_s_barrier();
    if (!LAY2) { stage_a(1, 1); stage_w(1, 1); stage_a(NST - 1, NST - 1); stage_w(NST - 1, NST - 1); }
    for (int kt = 0; kt + 1 < NKT; ++kt) {
      if (LAY2 && kt == 0) asm volatile("s_waitcnt vmcnt(4)" ::: "memory");
      else if (kt + 2 >= NKT) asm volatile("s_waitcnt vmcnt(0)" ::: "memory");
      else asm volatile("s_waitcnt vmcnt(10)" ::: "memory");
      __builtin_amdgcn_s_barrier();
      if (kt + NST < NKT) { stage_a(kt + NST, slot); stage_w(kt + NST, slot); }
      slot = slot == NST - 1 ? 0 : slot + 1;
    }
    __builtin_amdgcn_s_setprio(0);
    __builtin_amdgcn_s_barrier();
  } else {
  LAYER_HERE;
  const int dk = LAY2 ? 5 : 1;
  const int wm = wid & 1, wn = wid >> 1;
  if (wid == 0) STAMP(dk, 0);
  f32x4 acc[4][6];
#pragma unroll
  for (int i = 0; i < 4; ++i)
#pragma unroll
    for (int j = 0; j < 6; ++j) acc[i][j] = f32x4{0.f, 0.f, 0.f, 0.f};
  const int q16 = lane & 15, g4 = lane >> 4;
  const int e0 = 16 * (q16 >> 3) + (q16 & 7);
  s16x8 af[2][4], wf[6];
  unsigned aad[2], wad[2];
  {
    const int row = wm * 64 + e0, swz = ((row >> 1) & 3) | (((row >> 4) & 1) << 2);
#pragma unroll
    for (int h = 0; h < 2; ++h) aad[h] = lbase + row * 128 + (((4 * h + g4) ^ swz) << 4);
    const int tq = (lane & 15) >> 2, tp = lane & 3, k = 8 * g4 + tq, ch = wn * 4 + 2 * (tp >> 1);
    wad[0] = lbase + ABYTES + k * 384 + ((ch ^ (4 * ((k >> 1) & 1)) ^ (g4 & 1)) << 4) + 8 * (tp & 1);
    wad[1] = wad[0] ^ 16;
  }
  auto lda = [&](unsigned a, int i) { return *LDS_AT(const s16x8, a + (i >> 1) * 4096 + (i & 1) * 1024); };
  auto ldw_ = [&](int h, int j) {
    const unsigned w = wad[j & 1] + h * 12288 + (j >> 1) * 128;
    s16x4 lo = __builtin_amdgcn_ds_read_tr16_b64_v4i16(LDS_AT(s16x4, w));
    s16x4 h4 = __builtin_amdgcn_ds_read_tr16_b64_v4i16(LDS_AT(s16x4, w + 1536));
    return (s16x8)__builtin_shufflevector(lo, h4, 0, 1, 2, 3, 4, 5, 6, 7);
  };
  unsigned tba, tbw, tal;
  auto taba = [&](int i) { return *LDS_AT(const s16x8, tba + (i >> 1) * 512 + (i & 1) * 128); };
  auto tabw = [&](int j) { return *LDS_AT(const s16x8, tbw + ((j >> 1) * 64 + (j & 1) * 8) * 16); };
  ARGS_HERE;
  const __amdgpu_buffer_rsrc_t rso = __builtin_amdgcn_make_buffer_rsrc(KV, 0, 2 * S_ * D_ * 2, 0x00020000);
  int gto = ((tni * 2 + wn) * 64 + tmi * 4 + wm * 2) * 2048;
  unsigned lto = lbase + (wn * 2 + wm) * 4096, ato = lbase + ALPHA + wm * 256;
  asm volatile("" : "+s"(gto), "+s"(lto), "+s"(ato));
#define QMM(c, i, j, w)                                                                                                \
  do {                                                                                                                 \
    if ((j) >= 4) asm volatile("v_mfma_f32_16x16x32_bf16 %0, %1, %2, %0" : "+v"(acc[i][j]) : "v"(af[c][i]), "v"(wf[w])); \
    else asm volatile("v_mfma_f32_16x16x32_bf16 %0, %1, %2, %0" : "+v"(acc[i][j]) : "v"(wf[w]), "v"(af[c][i]));         \
    PIN();                                                                                                             \
  } while (0)
#pragma unroll
  for (int i = 0; i < 4; ++i)
#pragma unroll
    for (int j = 0; j < 6; ++j) asm volatile("" : "+v"(acc[i][j]));
  asm volatile("" : "+v"(aad[0]), "+v"(aad[1]), "+v"(wad[0]), "+v"(wad[1]));
  if (LAY2) __builtin_amdgcn_s_barrier();
  __builtin_amdgcn_s_barrier();
  PIN();
  if (wid == 0) STAMP(dk, 1);
  af[0][0] = lda(aad[0], 0); wf[0] = ldw_(0, 0); af[0][1] = lda(aad[0], 1); af[0][2] = lda(aad[0], 2); af[0][3] = lda(aad[0], 3);
  wf[1] = ldw_(0, 1); wf[2] = ldw_(0, 2); wf[3] = ldw_(0, 3);
  PIN();
  s16x4 wlo;
  auto col = [&](int c, int J0, int delta) __attribute__((always_inline)) {
    const int h = c / 6, j = c % 6;
    auto rw = [&](int n, int hi) __attribute__((always_inline)) {
      const int nh = n < 12 ? n / 6 : 0, nj = n < 12 ? n % 6 : J0 + n - 12;
      const s16x4 v = __builtin_amdgcn_ds_read_tr16_b64_v4i16(LDS_AT(s16x4, wad[nj & 1] + nh * 12288 + (nj >> 1) * 128 + hi * 1536));
      if (hi) wf[n % 6] = (s16x8)__builtin_shufflevector(wlo, v, 0, 1, 2, 3, 4, 5, 6, 7);
      else wlo = v;
    };
    auto mv = [&](unsigned& x) __attribute__((always_inline)) { x += delta; asm volatile("" : "+v"(x)); };
#pragma unroll
    for (int g = 0; g < 4; ++g) {
      QMM(h, g, j, j);
      if (c < 6 && g < 2) rw(c + 4, g);
      if (c < 4 && g == 2) af[1][c] = lda(aad[1], c);
      if (c == 4 && g >= 2) mv(aad[g - 2]);
      if (c == 5 && g >= 2) rw(10, g - 2);
      if (c == 6 && g < 2) rw(11, g);
      if (c == 6 && g == 2) mv(wad[0]);
      if (c == 7 && g == 0) mv(wad[1]);
      if (c == 8 && g == BARG) {
        asm volatile("" ::: "memory");
        __builtin_amdgcn_s_waitcnt(0xc07f);
        PIN();
        __builtin_amdgcn_s_barrier();
        asm volatile("" ::: "memory");
      }
      if (c == 8 && g >= 2) rw(12, g - 2);
      if (c == 9) af[0][g] = lda(aad[0], g);
      if (c == 10) rw(13 + (g >> 1), g & 1);
      if (c == 11 && g < 2) rw(15, g);
      PIN();
    }
  };
  auto move = [&](int delta) {
    wad[0] += delta; wad[1] += delta; aad[0] += delta; aad[1] += delta;
    asm volatile("" : "+v"(aad[0]), "+v"(aad[1]), "+v"(wad[0]), "+v"(wad[1]));
    PIN();
  };
#pragma unroll
  for (int c = 0; c < 8; ++c) col(c, 0, STB);
  {
    int slot = 1;
#pragma unroll 1
    for (int kt = 0; kt < NKT - NST - 1; ++kt) {
#pragma unroll
      for (int c = 8; c < 12; ++c) col(c, 0, 0);
      const int delta = slot == NST - 1 ? -(NST - 1) * STB : STB;
      slot = slot == NST - 1 ? 0 : slot + 1;
#pragma unroll
      for (int c = 0; c < 8; ++c) col(c, 0, delta);
    }
  }
  static_assert(NKT % NST == 1, "slots of the last stages");
#define TT(i)
  auto tabs = [&]() {
    const int ln = fresh_lane(), e1 = 16 * ((ln >> 3) & 1) + (ln & 7);
    tbw = lbase + TABW + (wn * 32 + e1) * 16;
    tba = ln >= 16 ? lbase + CRED : lbase + TABA + (wm * 64 + e1) * 16;
    asm volatile("" : "+v"(tba), "+v"(tbw));
    PIN();
  };
  auto bar = [&]() { PIN(); __builtin_amdgcn_s_barrier(); asm volatile("" ::: "memory"); PIN(); };
  TT(0);
  TT(1);
#pragma unroll
  for (int c = 8; c < 12; ++c) col(c, 2, 0);
#pragma unroll
  for (int ts = 0; ts < 2; ++ts) {
#pragma unroll
    for (int n = 0; n < 8; ++n) {
      const int m = 8 * ts + n, h = n >> 2, j = 2 + (n & 3);
      auto nxt = [&](int c) { wf[(8 + c) % 6] = ldw_(0, 2 + c); PIN(); };
      auto nxa = [&](int i) { af[0][i] = lda(aad[0], i); PIN(); };
      if (m == 6) {
        TT(2);
        bar();
        TT(3);
        move(STB);
        nxt(0); nxa(0);
      }
      if (m == 7) nxt(1);
      QMM(h, 0, j, m % 6);
      if (n < 4) { wf[(m + 4) % 6] = ldw_(1, j); PIN(); }
      if (m >= 12) { wf[(m + 4) % 6] = ldw_((n >> 1) & 1, n & 1); PIN(); }
      if (m == 6) nxa(1);
      if (m == 7) nxt(2);
      QMM(h, 1, j, m % 6);
      if (n < 2) { af[1][2 * n] = lda(aad[1], 2 * n); PIN(); }
      if (m == 6) nxa(2);
      QMM(h, 2, j, m % 6);
      if (n < 2) { af[1][2 * n + 1] = lda(aad[1], 2 * n + 1); PIN(); }
      if (m == 6) nxa(3);
      if (m == 7) nxt(3);
      QMM(h, 3, j, m % 6);
    }
  }
  int lo_, lok;
  unsigned sig, gv;
  float al[4];
  f32x4 av[4];
  u32x4 kvp[12];
  auto cacc = [&](int p, int x, int t) -> f32x4& { return p < 2 ? acc[x][2 * p + t] : acc[(x >> 1) * 2 + t][4 + (x & 1)]; };
  auto signal = [&](unsigned v) { __hip_atomic_fetch_add(LDS_AT(unsigned, sig), v, __ATOMIC_RELAXED, __HIP_MEMORY_SCOPE_WORKGROUP); PIN(); };
  auto outop = [&](int p, int k) {
    auto ord = [&](int hf) { return p == 1 && hf < 4 ? ((hf & 1) * 2 + (hf >> 1)) : hf; };
    if (k >= 0 && k < 16) {
      const int hk = ord(k >> 1), x = hk >> 1, t = hk & 1;
      f32x4& c = cacc(p, x, t);
#pragma unroll
      for (int o = 2 * (k & 1); o < 2 * (k & 1) + 2; ++o) {
        float r;
        asm volatile("v_mul_f32 %0, %1, %2" : "=v"(r) : "v"(c[o]), "v"(p < 2 ? al[x] : av[(x >> 1) * 2 + t][o]));
        c[o] = r;
      }
      PIN();
    }
    if (k >= 1 && k <= 16) {
      const int hk = ord((k - 1) >> 1), x = hk >> 1, t = hk & 1, o = (k - 1) & 1;
      kvp[p * 4 + x][2 * t + o] = pack2(cacc(p, x, t)[2 * o], cacc(p, x, t)[2 * o + 1]);
      PIN();
      if (t && o) {
        if (p) __builtin_amdgcn_raw_buffer_store_b128(kvp[p * 4 + x], rso, gv + (x >> 1) * 2048 + (x & 1) * (p == 1 ? 64 : 128), 0, 16);
        else *LDS_AT(u32x4, gv + (x >> 1) * 2048 + (x & 1) * 128) = kvp[x];
        PIN();
      }
    }
  };
  s16x8 aq[3][4];
#define QM2(a, i, j, w)                                                                                                \
  do {                                                                                                                 \
    if ((j) >= 4) asm volatile("v_mfma_f32_16x16x32_bf16 %0, %1, %2, %0" : "+v"(acc[i][j]) : "v"(a), "v"(wf[w]));       \
    else asm volatile("v_mfma_f32_16x16x32_bf16 %0, %1, %2, %0" : "+v"(acc[i][j]) : "v"(wf[w]), "v"(a));                \
    PIN();                                                                                                             \
  } while (0)
  static_for<16, 42>([&](auto mc) __attribute__((always_inline)) {
    constexpr int m = decltype(mc)::value;
    auto colj = [&](int c) { return c < 20 ? (c & 1) : (c < 24 ? c - 18 : (c < 32 ? 2 + ((c - 24) >> 2) * 2 + (c & 1) : (c & 1))); };
    auto rdw = [&](int c) { wf[c % 6] = c >= 40 ? tabw(c - 40) : (c < 24 ? tabw(c - 18) : ldw_((c >> 1) & 1, colj(c))); PIN(); };
    if (m == 18) {
      TT(6);
      bar();
      TT(7);
      rdw(20); af[0][0] = taba(0); PIN();
    }
    if (m == 28) TT(8);
    if (m == 32) {
      asm volatile("s_waitcnt lgkmcnt(0)" ::: "memory");
      TT(9);
      bar();
    }
    if (m == 36) TT(10);
    if (m == 38) TT(11);
#pragma unroll
    for (int i = 0; i < 4; ++i) {
      const int mq = m >= 38 ? 38 + i : m, iq = m >= 38 ? m - 38 : i, j = colj(mq);
      if (mq < 24) QM2(af[mq < 20 ? (mq >> 1) & 1 : 0][iq], iq, j, mq % 6);
      else if (mq < 36) QM2(af[1 - ((mq >> 1) & 1)][iq], iq, j, mq % 6);
      else QM2(aq[mq < 36 ? 0 : (mq - 36) >> 1][iq], iq, j, mq % 6);
      if (m == 16 && i == 0) move(-(NST - 1) * STB);
      if (m == 17 && i == 0) tabs();
      if (m == 18 && i < 3) { af[0][i + 1] = taba(i + 1); PIN(); }
      if (m == 18 && i == 3) rdw(21);
      if (m == 19 && (i == 0 || i == 2)) rdw(22 + (i >> 1));
      if (m == 20) { af[1][i] = lda(aad[0], i); PIN(); }
      if ((m == 20 || m == 24) && i == 3) rdw(m + 4);
      if (m >= 21 && m <= 29 && m != 24 && i == 0) rdw(m + 4);
      if (m == 24) { af[0][i] = lda(aad[1], i); PIN(); }
      if ((m == 29 || m == 30) && i == 3) rdw(m + 5);
      if (m == 31 && i == 3) { wad[0] += STB; wad[1] += STB; asm volatile("" : "+v"(wad[0]), "+v"(wad[1])); PIN(); }
      if (m == 25 && i == 3) { aad[0] += STB; aad[1] += STB; asm volatile("" : "+v"(aad[0]), "+v"(aad[1])); PIN(); }
      if (m >= 32 && m <= 36 && i == 0) rdw(m + 4);
      if (m == 36 && i == 2) rdw(41);
      if (m == 34 || m == 36) { aq[(m - 34) >> 1][i] = lda(aad[(m - 34) >> 1], i); PIN(); }
      if (m == 36 && i == 3) signal(0x10000u);
      if (m == 33 && i == 2) tabs();
      if (m == 35) { aq[2][i] = taba(i); PIN(); }
      if (m == 23 && i == 1) {
        const int ln = fresh_lane();
        tal = ato + (16 * ((ln >> 3) & 1) + (ln & 7)) * 4;
        asm volatile("" : "+v"(tal));
        PIN();
      }
      if (m == 26 && i == 0) {
#pragma unroll
        for (int x = 0; x < 4; ++x) al[x] = *LDS_AT(const float, tal + ((x >> 1) * 32 + (x & 1) * 8) * 4);
        PIN();
      }
      if (m == 27 && i == 0) {
        const int ln = fresh_lane();
        gv = gto + (ln >> 5) * 1024 + ((ln & 28) * 2 + (ln & 3)) * 16;
        tal = ato + (ln >> 4) * 16 + (ln >> 5) * 32;
        asm volatile("" : "+v"(tal), "+v"(gv));
        PIN();
      }
      if (m == 32 && i == 1) {
        const int ln = fresh_lane();
        lok = gv - gto + lto;
        lo_ = (ln >> 5) * 1024 + ((ln & 24) * 2 + (ln & 7)) * 16;
        gv = gto + VOFF + lo_;
        asm volatile("" : "+v"(gv), "+v"(lo_), "+v"(lok));
        PIN();
      }
      if (m == 36 && i == 1) {
        const int ln = fresh_lane();
        lo_ += lto; gv = lo_ + QOFF;
        sig = ln == 0 ? lbase + FLG + 32 : lbase + DUM + ln * 4;
        asm volatile("" : "+v"(lo_), "+v"(sig), "+v"(gv));
        PIN();
      }
      if ((m == 30 && i == 2) || (m == 33 && i == 1)) {
#pragma unroll
        for (int x = 2 * (m & 1); x < 2 * (m & 1) + 2; ++x) av[x] = *LDS_AT(const f32x4, tal + ((x >> 1) * 32 + (x & 1) * 8) * 4);
        PIN();
      }
      outop(1, (m - 28) * 4 + i);
      outop(2, (m - 32) * 4 + i);
      {
        constexpr int wg[8] = WGAPS;
#pragma unroll
        for (int e = 0; e < 8; ++e)
          if ((m - 32) * 4 + i == wg[e]) {
            *LDS_AT(u32x4, (e < 4 ? lok + (e & 1) * 64 : lo_ + 2048 + (e & 1) * 128) + ((e >> 1) & 1) * ASB) = kvp[4 + e];
            PIN();
          }
      }
      if (m >= 40) outop(0, (m - 40) * 4 + i - QOV);
      if (m == 37 && i == 0) {
#pragma unroll
        for (int x = 0; x < 4; ++x) al[x] *= QSCALE;
        asm volatile("" : "+v"(al[0]), "+v"(al[1]), "+v"(al[2]), "+v"(al[3]));
        PIN();
      }
    }
  });
#undef QM2
#undef QMM
  TT(13);
  if (wid == 0) STAMP(dk, 18);
#pragma unroll
  for (int k = 8 - QOV; k <= 16; ++k) {
    if (k == 4 * FLAT + 1) { asm volatile("s_waitcnt vmcnt(0)" ::: "memory"); signal(1u); }
    outop(0, k);
  }
  __hip_atomic_fetch_add(LDS_AT(unsigned, sig - (fresh_lane() == 0 ? 16 : 0)), 1u, __ATOMIC_RELAXED, __HIP_MEMORY_SCOPE_WORKGROUP);
  TT(14);
  if (FLAT >= 4) { asm volatile("s_waitcnt vmcnt(0)" ::: "memory"); signal(1u); }
  TT(15);
  if (wid == 0) STAMP(dk, 19);
  }
  PIN();
  {
  const int lane = fresh_lane(), tid = wid * 64 + lane;
  LAYER_HERE;
  const int dk = LAY2 ? 6 : 2;
  auto wait_arrivals = [&](unsigned n) {
#pragma unroll 1
    for (int i = 0; i < SPIN_MAX; ++i) {
      if (__builtin_amdgcn_readfirstlane(*LDS_AT(const volatile unsigned, lbase + FLG + 16)) >= n) break;
      __builtin_amdgcn_s_sleep(0);
    }
    asm volatile("" ::: "memory");
    PIN();
  };
  wait_arrivals(4u);
  const int g = wid >> 2, hh = (wid >> 1) & 1, qg = wid & 1;
  const int head = tni * 2 + hh, qt = tmi * 4 + qg * 2;
  const unsigned qad = lbase + QOFF + (hh * 2 + qg) * 4096 + lane * 16;
  const s16x8 qa0 = *LDS_AT(const s16x8, qad + (g ? 0 : 2048)), qa1 = *LDS_AT(const s16x8, qad + (g ? 1024 : 3072));
  const s16x8 qb0 = *LDS_AT(const s16x8, qad + (g ? 2048 : 0)), qb1 = *LDS_AT(const s16x8, qad + (g ? 3072 : 1024));
  const unsigned wbase = lbase + (hh * 2 + g) * 4096 + lane * 16;
  auto sptr = [&](int sl) { unsigned a = wbase + sl * ASB; asm volatile("" : "+v"(a)); return LDS_AT(const s16x8, a); };
  auto rd = [&](__attribute__((address_space(3))) const s16x8* sp, int off) { return sp[off >> 4]; };
  auto sl0 = sptr(0);
  s16x8 k00 = rd(sl0, 0), k01 = rd(sl0, 1024);
  const unsigned tag = __builtin_amdgcn_readfirstlane(*LDS_AT(const volatile unsigned, lbase + FLG + 20));
  const int idx = LAY2 ? (2047 - (int)(__builtin_amdgcn_readfirstlane(*LDS_AT(const volatile unsigned, lbase + FLG + 28)) & 0xffffu)) & 2047 : -64;
  const unsigned arr_a = lane == 0 ? lbase + FLG + 16 : lbase + DUM + lane * 4;
  auto arrive = [&]() { __hip_atomic_fetch_add(LDS_AT(unsigned, arr_a), 1u, __ATOMIC_RELAXED, __HIP_MEMORY_SCOPE_WORKGROUP); };
  const unsigned arr_w = lane == 0 ? lbase + FLG + 8 + g * 4 : lbase + DUM + lane * 4;
  const unsigned arr_i = 1u << (8 * (wid & 3));
  auto arrive_w = [&]() { __hip_atomic_fetch_add(LDS_AT(unsigned, arr_w), arr_i, __ATOMIC_RELAXED, __HIP_MEMORY_SCOPE_WORKGROUP); };
  s16x8 sel;
#pragma unroll
  for (int e = 0; e < 8; ++e) sel[e] = ((lane & 7) == (e & 4)) ? (short)0x3F80 : (short)0;
  int sidx = 0xDDDDDDDD;
  asm volatile("" : "+v"(sidx));
  f32x16 oa, ob, nn, sa, sb;
  f32x4 la = {0.f, 0.f, 0.f, 0.f}, lb = {0.f, 0.f, 0.f, 0.f};
#pragma unroll
  for (int e = 0; e < 16; ++e) { oa[e] = 0.f; ob[e] = 0.f; sa[e] = 0.f; sb[e] = 0.f; }
  float ma;
  const float zoff = __uint_as_float(__builtin_amdgcn_readfirstlane(LAY2 && g == ((idx >> 6) & 1) ? 0u : 0xff800000u));
  {
    sa = MFMA32(k00, qa0, sa); sa = MFMA32(k01, qa1, sa);
    sb = MFMA32(k00, qb0, sb); sb = MFMA32(k01, qb1, sb);
    float mb = sb[0];
    ma = sa[0];
#pragma unroll
    for (int e = 1; e < 16; ++e) { ma = fmaxf(ma, sa[e]); mb = fmaxf(mb, sb[e]); }
    ma = fmaxf(ma, mb);
    ma = fmaxf(ma, __shfl_xor(ma, 32, 64));
#pragma unroll
    for (int e = 0; e < 16; ++e) { nn[e] = -ma; sa[e] -= ma; sb[e] = 0.f; }
    la[0] = lb[0] = -__uint_as_float(__float_as_uint(__builtin_amdgcn_exp2f(zoff - ma)) & 0xffff0000u);
  }
#define SB() __builtin_amdgcn_sched_barrier(0)
#define EX(i) sc[i] = __builtin_amdgcn_exp2f(sc[i])
  auto unit = [&](f32x16& sc, f32x16& sn, const s16x8& kn0, const s16x8& kn1, const s16x8& qn0, const s16x8& qn1,
                  f32x16& op, f32x4& lp, const s16x8& vp,
                  f32x16& oc, f32x4& lc, const s16x8& vc, auto&& mid) {
    op = SMF32(vp, PHI(sn), op); SB();
    EX(0); EX(1); EX(2); SB();
    lp = SMF16(sel, PHI(sn), lp); SB();
    EX(3); EX(4); SB();
    sn = MFMA32(kn0, qn0, nn); SB();
    EX(5); EX(6); EX(7); SB();
    sn = MFMA32(kn1, qn1, sn); SB();
    mid();
    EX(8); EX(9); EX(10); SB();
    oc = SMF32(vc, PLO(sc), oc); SB();
    EX(11); EX(12); EX(13); SB();
    lc = SMF16(sel, PLO(sc), lc); SB();
    EX(14); EX(15); SB();
    asm volatile("" : "+v"(sc));
  };
  s16x8 v00, v01, v10, v11, k10, k11;
#pragma unroll
  for (int e = 0; e < 8; ++e) v11[e] = 0;
  v00 = rd(sl0, 2048); v01 = rd(sl0, 3072);
  sl0 = sptr(1);
  unsigned f0 = 0u;
  auto check = [&](unsigned need) {
    if (__builtin_expect(__builtin_amdgcn_readfirstlane(f0) < need, 0)) {
#pragma unroll 1
      for (int i = 0; i < SPIN_MAX; ++i) {
        if (__builtin_amdgcn_readfirstlane(*LDS_AT(const volatile unsigned, lbase + FLG)) >= need) break;
        __builtin_amdgcn_s_sleep(1);
      }
    }
    asm volatile("" ::: "memory");
  };
#define PAT 0x22221110
#define SAW8(i) do { if (((PAT >> (28 - 4 * (i))) & 3) != ((PAT >> ((32 - 4 * (i)) & 31)) & 3)) { __builtin_amdgcn_s_setprio((PAT >> (28 - 4 * (i))) & 3); SB(); } } while (0)
#define SAW(i) do { if (odd) SAW8(4 + (i)); else SAW8(i); } while (0)
  auto three = [&](int st, bool odd) {
    SAW(0);
    unit(sa, sb, k00, k01, qb0, qb1, ob, lb, v11, oa, la, v00, [&]() { k10 = rd(sl0, 0); k11 = rd(sl0, 1024); SB(); });
    SAW(1);
    unit(sb, sa, k10, k11, qa0, qa1, oa, la, v01, ob, lb, v00, [&]() { v10 = rd(sl0, 2048); v11 = rd(sl0, 3072); SB(); });
    sl0 = sptr((2 * st + 2) & 7);
    SB();
    SAW(2);
    unit(sa, sb, k10, k11, qb0, qb1, ob, lb, v01, oa, la, v10, [&]() { k00 = rd(sl0, 0); k01 = rd(sl0, 1024); SB(); });
  };
  auto fourth = [&](int st, bool odd) {
    SAW(3);
    unit(sb, sa, k00, k01, qa0, qa1, oa, la, v11, ob, lb, v10, [&]() {
      v00 = rd(sl0, 2048); v01 = rd(sl0, 3072);
      f0 = *LDS_AT(const volatile unsigned, lbase + FLG);
      SB();
      arrive_w();
      SB(); });
    check(st >= 14 ? 32u : 2u * st + 5u);
    sl0 = sptr((2 * st + 3) & 7);
    SB();
  };
  SB();
  SAW8(0);
  unit(sa, sb, k00, k01, qb0, qb1, ob, lb, v11, oa, la, v00, [&]() { k10 = rd(sl0, 0); k11 = rd(sl0, 1024); SB(); });
  SAW8(1);
  unit(sb, sa, k10, k11, qa0, qa1, oa, la, v01, ob, lb, v00, [&]() { v10 = rd(sl0, 2048); v11 = rd(sl0, 3072); SB(); });
  sl0 = sptr(2);
  SB();
  SAW8(2);
  unit(sa, sb, k10, k11, qb0, qb1, ob, lb, v01, oa, la, v10, [&]() {});
  check(3u); k00 = rd(sl0, 0); k01 = rd(sl0, 1024); SB();
  SAW8(3);
  unit(sb, sa, k00, k01, qa0, qa1, oa, la, v11, ob, lb, v10, [&]() { v00 = rd(sl0, 2048); v01 = rd(sl0, 3072); arrive_w(); SB(); });
  check(5u);
  sl0 = sptr(3);
  SB();
  three(1, true);
#pragma unroll 1
  for (int st = 2; st < 16; st += 2) { fourth(st - 1, true); three(st, false); fourth(st, false); three(st + 1, true); }
  SAW8(7);
  unsigned vr, tk, tv;
  u32x4 f4;
  auto table = [&]() {
    f4 = lds_rd4(lbase + FLG);
    k00 = *LDS_AT(const volatile s16x8, tk); k01 = *LDS_AT(const volatile s16x8, tk + 32);
    vr = *LDS_AT(const volatile u16, tv);
  };
  unsigned xw, xm;
  u32x2 ml;
  u32x4 o2[4];
  auto take = [&]() {
    ml = *LDS_AT(const volatile u32x2, xm);
#pragma unroll
    for (int e = 0; e < 4; ++e) o2[e] = lds_rd4(xw + e * 1024);
  };
  float pzt;
  int dso;
  f32x16& sc = sb;
#define EXP() do { asm volatile("" : "+v"(sc)); SB(); } while (0)
  oa = SMF32(v11, PHI(sa), oa); SB();
  tk = lbase + TAB + 128 + hh * 64 + (lane >> 5) * 16; tv = lbase + TAB + 256 + hh * 64 + (lane & 31) * 2;
  table();
  SB();
  EX(0); EX(1); EXP();
  la = SMF16(sel, PHI(sa), la); SB();
  EX(2); EX(3); EX(4); EXP();
  xw = wbase + (qg ? QOFF : 6 * ASB); xm = lbase + XML + wid * 512 + lane * 8;
  pzt = __builtin_amdgcn_exp2f(nn[0] + (zoff - 8.f));
  asm volatile("" : "+v"(xw), "+v"(xm), "+v"(pzt), "+v"(f4));
  SB();
  {
    const unsigned ph = LAY2 ? 0x79797979u : 0x80808080u, pg = qg ? 0u : 0x80u << (16 * hh + 8);
    auto ready = [&]() {
      return (((__builtin_amdgcn_readfirstlane(f4[1]) + ph) & 0x80808080u) == 0x80808080u) & (((__builtin_amdgcn_readfirstlane(g ? f4[3] : f4[2]) + 0x71717171u) & pg) == pg);
    };
    if (__builtin_expect(!ready(), 0)) {
#pragma unroll 1
      for (int i = 0; i < SPIN_MAX; ++i) {
        __builtin_amdgcn_s_sleep(1);
        table();
        if (ready()) break;
      }
    }
    asm volatile("" ::: "memory");
  }
  SB();
  sa = MFMA32(k00, qb0, nn); asm volatile("" : "+v"(sa)); SB();
#pragma unroll
  for (int e = 0; e < 4; ++e) *LDS_AT(volatile f32x4, xw + e * 1024) = f32x4{oa[4 * e], oa[4 * e + 1], oa[4 * e + 2], oa[4 * e + 3]};
  *LDS_AT(volatile u32x2, xm) = u32x2{__float_as_uint(ma), f2word(pzt <= la[0] ? la[0] : 0.f)};
  SB();
  EX(5); EX(6); EX(7); EXP();
  sa = MFMA32(k01, qb1, sa); asm volatile("" : "+v"(sa), "+v"(vr)); SB();
  ARGS_HERE;
  EX(8); EX(9); EX(10); EXP();
  ob = SMF32(v10, PLO(sb), ob); SB();
  EX(11); EX(12); EX(13); EXP();
  lb = SMF16(sel, PLO(sb), lb); SB();
  const bool used = !LAY2 || (qt + g) * 32 + (lane & 31) != idx;
  dso = used ? (((qt + g) * 32 + head) * 1024 + lane * 8) * 2 : S_ * D_ * 2;
  xw += g ? -4096 : 4096; xm += g ? -2048 : 2048;
  asm volatile("" : "+v"(dso), "+v"(xw), "+v"(xm));
  SB();
  EX(14); EX(15); EXP();
  take();
  SB();
  lb = SMF16(sel, PHI(sb), lb); SB();
  ob = SMF32(v11, PHI(sb), ob); SB();
  __builtin_amdgcn_s_setprio(EPRIO);
  TE(0, 0); TE(1, 0); TE(2, 0); TE(3, 0); TE(7, 0);
  float lsb = lb[0];
  if (LAY2) {
    sa[0] = __builtin_amdgcn_exp2f(sa[0]);
    ob = SMF32((u32x4{lane < 32 ? vr : 0u, 0u, 0u, 0u}), PLO(sa), ob);
    lsb += __uint_as_float(__float_as_uint(sa[0]) & 0xffff0000u);
    lsb = pzt <= lsb ? lsb : 0.f;
  }
  const __amdgpu_buffer_rsrc_t rso = __builtin_amdgcn_make_buffer_rsrc(Of, 0, S_ * D_ * 2, 0x00020000);
  TE(0, 1);
  if (__builtin_amdgcn_ballot_w64(ml[1] == SENT) != 0ull) {
#pragma unroll 1
    for (int i = 0; i < SPIN_MAX; ++i) {
      __builtin_amdgcn_s_sleep(XSLP);
      if (!__builtin_amdgcn_ballot_w64(*LDS_AT(const volatile unsigned, xm + 4) == SENT)) break;
    }
    asm volatile("s_waitcnt lgkmcnt(0)" ::: "memory");
    take();
  }
  arrive();
  TE(0, 2);
  TE(0, 3); TE(1, 1);
  {
    const float m2 = __uint_as_float(ml[0]), l2 = __uint_as_float(ml[1]);
    const float mx = fmaxf(ma, m2);
    const float f1 = __builtin_amdgcn_exp2f(ma - mx), f2 = __builtin_amdgcn_exp2f(m2 - mx);
    const float inv = frcp(lsb * f1 + l2 * f2);
    const float c1 = f1 * inv, c2 = f2 * inv;
    f32x2 r[8];
#pragma unroll
    for (int e = 0; e < 8; ++e) {
      const unsigned x0 = o2[e >> 1][(e & 1) * 2], x1 = o2[e >> 1][(e & 1) * 2 + 1];
      r[e] = f32x2{__uint_as_float(x0) * c2, __uint_as_float(x1) * c2};
    }
#pragma unroll
    for (int e = 0; e < 8; ++e) r[e] = f32x2{ob[2 * e], ob[2 * e + 1]} * f32x2{c1, c1} + r[e];
    auto put = [&]() {
#pragma unroll
      for (int sx = 0; sx < 2; ++sx)
        __builtin_amdgcn_raw_buffer_store_b128(u32x4{pack2(r[4 * sx][0], r[4 * sx][1]), pack2(r[4 * sx + 1][0], r[4 * sx + 1][1]),
            pack2(r[4 * sx + 2][0], r[4 * sx + 2][1]), pack2(r[4 * sx + 3][0], r[4 * sx + 3][1])}, rso, dso + sx * 1024, 0, 16);
    };
    put();
    PIN();
    __builtin_amdgcn_s_setprio(0);
    PIN();
    f32x2 chk = {lsb - __builtin_amdgcn_fmed3f(lsb, 8.6736174e-19f, 1.2676506e30f), l2 - __builtin_amdgcn_fmed3f(l2, 8.6736174e-19f, 1.2676506e30f)};
#pragma unroll
    for (int e = 0; e < 8; ++e) chk = r[e] * f32x2{0.f, 0.f} + chk;
    if (__builtin_expect(__builtin_amdgcn_ballot_w64(used && !(chk[0] + chk[1] == 0.f)) != 0ull, 0)) {
      float l;
      attn_safe(KV, head * 64, qb0, qb1, ob, l, LAY2 ? head * 64 + (idx >> 5) : -1, idx & 31, lbase + TAB + 128 + hh * 64);
      const float inv2 = 1.0f / l;
#pragma unroll
      for (int e = 0; e < 8; ++e) r[e] = f32x2{ob[2 * e] * inv2, ob[2 * e + 1] * inv2};
      asm volatile("s_waitcnt vmcnt(0)" ::: "memory");
      put();
    }
    unsigned pp = 0u;
    if (LAY2) {
      const unsigned ua = lbase + UGL + hh * 128 + (lane >> 5) * 16;
      f32x2 du = {0.f, 0.f}, dn = {0.f, 0.f};
#pragma unroll
      for (int q = 0; q < 4; ++q) {
        const f32x4 uu = *LDS_AT(const f32x4, ua + q * 32), gg = *LDS_AT(const f32x4, ua + 256 + q * 32);
        du = r[2 * q] * f32x2{uu[0], uu[1]} + du; du = r[2 * q + 1] * f32x2{uu[2], uu[3]} + du;
        const f32x2 t0 = r[2 * q] * f32x2{gg[0], gg[1]}, t1 = r[2 * q + 1] * f32x2{gg[2], gg[3]};
        dn = t0 * t0 + dn; dn = t1 * t1 + dn;
      }
      const auto a = __builtin_amdgcn_permlane32_swap(__float_as_uint(du[0] + du[1]), __float_as_uint(du[0] + du[1]), false, false);
      const auto c = __builtin_amdgcn_permlane32_swap(__float_as_uint(dn[0] + dn[1]), __float_as_uint(dn[0] + dn[1]), false, false);
      pp = f2word(__uint_as_float(pack2(__uint_as_float(a[0]) + __uint_as_float(a[1]), __uint_as_float(c[0]) + __uint_as_float(c[1]))));
    }
    __builtin_amdgcn_raw_buffer_store_b32(pp, __builtin_amdgcn_make_buffer_rsrc(hand + 2 * HAND_N + PP_O, 0, LAY2 ? H_ * S_ * 4 : 0, 0x00020000),
                                          lane < 32 ? (head * S_ + (qt + g) * 32 + lane) * 4 : H_ * S_ * 4, 0, 16);
    PIN();
  }
  TE(1, 2);
  u32x4 xres[2];
  oproj_tail(LAY2, tid, lbase, lbase + FLG, omi, oni, idx, gran + GR_O + ((LAY2 ? 16 : 0) + tmi) * (64 * RO) + tni,
             gran + GR_X + omi * (128 * RX) + oni * 4, tag, Of, xout, A, w_r, stats_out, stats, hand, xres TEPASS);
  if (LAY2) {
    const int row = omi * 128 + (wid & 3) * 32 + (wid >> 2) * 16 + (lane >> 3), col = oni * 64 + (lane & 7) * 8;
    unsigned md[2], best = 0u;
    const __amdgpu_buffer_rsrc_t rsk = __builtin_amdgcn_make_buffer_rsrc(hand + HAND_N, 0, (HAND_N + PS_N) * 4, 0x00020000);
    f32x2 xf[2][4];
#pragma unroll
    for (int p = 0; p < 2; ++p)
#pragma unroll
      for (int e = 0; e < 4; ++e) {
        xf[p][e] = f32x2{__uint_as_float(xres[p][e] << 16), __uint_as_float(xres[p][e] & 0xffff0000u)};
        asm volatile("" : "+v"(xf[p][e]));
      }
    LOGDEF;
    LOG();
    const bool rtr = (oni & 7) == 7;
    {
      const int rl = (wid & 3) * 32 + (wid >> 2) * 16 + (lane & 15), rr = omi * 128 + rl;
      int src = (HAND_N + ((omi * 16 + (lane >> 4) * 4) * 128 + rl) * 4) * 4;
      int ko = rtr && (wid >> 2) == (oni >> 3) && lane < NREP ? lane * 1024 + (rr >> 4) * 8 : (HAND_N + PS_N) * 4;
      int bp = (lane >> 3) * 4;
      const float wsum = *LDS_AT(const volatile float, lbase + SPM + 132);
      asm volatile("" : "+v"(src), "+v"(ko), "+v"(bp));
      u32x4 v[4];
      __builtin_amdgcn_s_sleep(MPRE);
#pragma unroll 1
      for (int spin = 0; spin < SPIN_MAX; ++spin) {
        LOG();
#pragma unroll
        for (int i = 0; i < 4; ++i) v[i] = __builtin_amdgcn_raw_buffer_load_b128(rsk, src + (i & 1) * 2048, (i >> 1) * 4096, 16);
        unsigned mx = 0u;
#pragma unroll
        for (int i = 0; i < 4; ++i) mx = umax(umax(umax(mx, v[i][0]), v[i][1]), v[i][2]);
        if (none(mx == SENT)) break;
        __builtin_amdgcn_s_sleep(MSLP);
        asm volatile("" ::: "memory");
      }
      LOGAT(12);
      if (rtr) __builtin_amdgcn_s_setprio(3); else __builtin_amdgcn_s_setprio(LNPRIO);
      double sm[3];
#pragma unroll
      for (int c = 0; c < 3; ++c) {
        const double a = ((double)__uint_as_float(v[0][c]) + (double)__uint_as_float(v[1][c])) + ((double)__uint_as_float(v[2][c]) + (double)__uint_as_float(v[3][c]));
        u32x2 q = __builtin_bit_cast(u32x2, a);
        const auto lo = __builtin_amdgcn_permlane32_swap(q[0], q[0], false, false), hi = __builtin_amdgcn_permlane32_swap(q[1], q[1], false, false);
        q = __builtin_bit_cast(u32x2, __builtin_bit_cast(double, u32x2{lo[0], hi[0]}) + __builtin_bit_cast(double, u32x2{lo[1], hi[1]}));
        const auto l2 = __builtin_amdgcn_permlane16_swap(q[0], q[0], false, false), h2 = __builtin_amdgcn_permlane16_swap(q[1], q[1], false, false);
        sm[c] = __builtin_bit_cast(double, u32x2{l2[0], h2[0]}) + __builtin_bit_cast(double, u32x2{l2[1], h2[1]});
      }
      float mean, var, mb, den;
      ln_from_sums(sm[0], sm[1], mean, var, mb, den);
      const unsigned mdr = f2word(__uint_as_float((__float_as_uint(mb) >> 16) | (__float_as_uint(den) & 0xffff0000u)));
      md[0] = __builtin_amdgcn_ds_bpermute(bp, mdr); md[1] = __builtin_amdgcn_ds_bpermute(bp + 32, mdr);
      if (rtr) {
        const float r = (float)fma(-(double)mb, (double)wsum, sm[2]) * frcp(den);
        const unsigned rb = pack2(r, r);
        unsigned rkey = (rb ^ ((unsigned)((int)rb >> 31) | 0x80000000u)) & 0xffff0000u;
        if (r != r) rkey = 0u;
        const unsigned top = wave_umax(rkey | (unsigned)(2047 - rr));
        const unsigned mdt = __builtin_amdgcn_readlane(mdr, (2047 - (int)(top & 0xffffu)) & 15);
        __builtin_amdgcn_raw_buffer_store_b64(u32x2{top, mdt}, rsk, ko, 0, 16);
        PIN();
        __builtin_amdgcn_s_setprio(LNPRIO);
        __hip_atomic_fetch_add(LDS_AT(unsigned, lane == 0 ? lbase + SPM + 128 : lbase + DUM + lane * 4), 1u, __ATOMIC_RELAXED, __HIP_MEMORY_SCOPE_WORKGROUP);
        if (wid == 2 || wid == 5) STAMP(8, 6);
      }
      LOGAT(13);
    }
    auto keys = [&](const u32x4& v) __attribute__((always_inline)) {
      if (none(umax(umax(v[0], v[1]), umax(v[2], v[3])) == SENT)) {
        best = wave_umax(umax(v[0], v[2])) | 0x8000u;
        *LDS_AT(volatile u32x2, lbase + FLG + 40) = u32x2{best, (unsigned)__builtin_amdgcn_readlane(v[0] > v[2] ? v[1] : v[3], ((2047 - (int)(best & 0x7ffu)) >> 5) & 63)};
        STAMP(8, 11);
      }
    };
    auto ln_rows = [&]() __attribute__((always_inline)) {
#pragma unroll
      for (int p = 0; p < 2; ++p) {
        const float mb_ = __uint_as_float(md[p] << 16), rin = frcp(__uint_as_float(md[p] & 0xffff0000u));
#pragma unroll
        for (int e = 0; e < 4; ++e) {
          const f32x2 h = (xf[p][e] - f32x2{mb_, mb_}) * f32x2{rin, rin} + xf[p][e];
          xres[p][e] = pack2(h[0], h[1]);
        }
      }
      asm volatile("" : "+v"(xres[0]), "+v"(xres[1]));
    };
    if (wid == 0) STAMP(8, 12);
    ln_rows();
    LOGAT(14);
    const __amdgpu_buffer_rsrc_t rsu = __builtin_amdgcn_make_buffer_rsrc(out, 0, S_ * D_ * 2, 0x00020000);
    unsigned fr1 = SENT;
#pragma unroll 1
    for (int i = 0; i < 4 * SPIN_MAX && fr1 == SENT; ++i) fr1 = __builtin_amdgcn_readfirstlane(*LDS_AT(const volatile unsigned, lbase + SPM + 144));
    fr1 &= 0x7ffu;
    unsigned tmx = __builtin_amdgcn_readfirstlane(*LDS_AT(const volatile unsigned, lbase + SPM + 136)) + KDT;
    if (tmx - (unsigned)__builtin_amdgcn_s_memrealtime() > 1024u) tmx = 0u;
    auto due = [&]() { return tmx == 0u || (int)((unsigned)__builtin_amdgcn_s_memrealtime() - tmx) >= 0; };
    lk_set(15);
    if (wid == 6) {
      if (rtr) {
#pragma unroll 1
        for (int i = 0; i < SPIN_MAX; ++i) {
          if (__builtin_amdgcn_readfirstlane(*LDS_AT(const volatile unsigned, lbase + SPM + 128)) == 7u) break;
          __builtin_amdgcn_s_sleep(1);
        }
        asm volatile("" ::: "memory");
      }
    }
#pragma unroll 1
    for (int i = 0; i < OSLP && !best; ++i) {
      if (wid == 6 && due()) {
        LOG();
        keys(__builtin_amdgcn_raw_buffer_load_b128(rsk, (bid % NREP) * 1024 + lane * 16, 0, 16));
        i += 2;
      } else {
        if (__builtin_amdgcn_readfirstlane(*LDS_AT(const volatile unsigned, lbase + FLG + 40))) break;
        __builtin_amdgcn_s_sleep(2);
      }
      asm volatile("" ::: "memory");
    }
    u32x4 kv = {SENT, SENT, SENT, SENT};
    if (wid == 6 && !best && due()) kv = __builtin_amdgcn_raw_buffer_load_b128(rsk, (bid % NREP) * 1024 + lane * 16, 0, 16);
    PIN();
#pragma unroll
    for (int p = 0; p < 2; ++p) __builtin_amdgcn_raw_buffer_store_b128(xres[p], rsu, row + 8 * p != (int)fr1 ? ((row + 8 * p) * D_ + col) * 2 : S_ * D_ * 2, 0, OAUX);
    PIN();
    if (wid == 6 && !best) { keys(kv); while (!due()) __builtin_amdgcn_s_sleep(1); }
#pragma unroll 1
    for (int i = 0; i < 4 * SPIN_MAX && !best; ++i) {
      if (wid == 6) {
        LOG();
        keys(__builtin_amdgcn_raw_buffer_load_b128(rsk, (bid % NREP) * 1024 + lane * 16, 0, 16));
        if (!best) __builtin_amdgcn_s_sleep(KSLP);
      } else {
        best = __builtin_amdgcn_readfirstlane(*LDS_AT(const volatile unsigned, lbase + FLG + 40));
        if (!best) __builtin_amdgcn_s_sleep(1);
      }
      asm volatile("" ::: "memory");
    }
    const int idx2 = 2047 - (int)(best & 0x7ffu);
    if (__builtin_expect(idx2 != (int)fr1, 0)) {
      const __amdgpu_buffer_rsrc_t rsf = __builtin_amdgcn_make_buffer_rsrc(gran + GR_F, 0, 48 * 128, 0x00020000);
      if (!none((row | 8) == (idx2 | 8))) {
#pragma unroll
        for (int p = 0; p < 2; ++p)
          __builtin_amdgcn_raw_buffer_store_b128(u32x4{pack2(xf[p][0][0], xf[p][0][1]), pack2(xf[p][1][0], xf[p][1][1]), pack2(xf[p][2][0], xf[p][2][1]), pack2(xf[p][3][0], xf[p][3][1])},
              __builtin_amdgcn_make_buffer_rsrc(xout, 0, S_ * D_ * 2, 0x00020000), row + 8 * p == idx2 ? (idx2 * D_ + col) * 2 : S_ * D_ * 2, 0, 16);
        asm volatile("s_waitcnt vmcnt(0)" ::: "memory");
        __builtin_amdgcn_raw_buffer_store_b32(tag, rsf, lane == 0 ? oni * 128 : 48 * 128, 0, 16);
      }
      if (!none((row | 8) == ((int)fr1 | 8))) {
#pragma unroll 1
        for (int spin = 0; spin < SPIN_MAX; ++spin) {
          if (none(__builtin_amdgcn_raw_buffer_load_b32(rsf, (16 + oni * 2 + (lane & 1)) * 128, 0, 16) != tag)) break;
          asm volatile("" ::: "memory");
        }
#pragma unroll
        for (int p = 0; p < 2; ++p) __builtin_amdgcn_raw_buffer_store_b128(xres[p], rsu, row + 8 * p == (int)fr1 ? ((row + 8 * p) * D_ + col) * 2 : S_ * D_ * 2, 0, OAUX);
      }
    }
  }
  }
}
struct Args2 { Args l[2]; };
extern "C" __global__ __launch_bounds__(768, 3) void layers_kernel(const Args2 args) {
  __shared__ __attribute__((aligned(128))) char smem[163840];
  const unsigned long long t_entry = 0;
  const int tid = threadIdx.x, bid = blockIdx.x;
  int wid = __builtin_amdgcn_readfirstlane(tid >> 6);
  asm volatile("" : "+s"(wid));
  const ArgP ap = (ArgP)__builtin_amdgcn_kernarg_segment_ptr();
  layer_body(wid, bid, (unsigned)(uintptr_t)smem, ap, t_entry);
  LAYER_HERE;
  if (!LAY2) {
    asm volatile(AGAIN :: "{s[0:1]}"(ap + 1), "{s2}"(bid), "{v0}"(wid * 64) : "s90", "s91", "scc", "memory");
    __builtin_unreachable();
  }
}

extern "C" __global__ void end_marker_kernel() {}

extern "C" void kernel_launch(void* const* d_in, const int* in_sizes, int n_in, void* d_out, int out_size,
                              void* d_ws, size_t ws_size, hipStream_t stream) {
  const u16* x0 = (const u16*)d_in[0];
  const u16* wqkv = (const u16*)d_in[1];
  const u16* wo = (const u16*)d_in[2];
  const u16* w_r = (const u16*)d_in[4];
  const u16* wg = (const u16*)d_in[5];
  const u16* wu = (const u16*)d_in[6];
  const u16* wd = (const u16*)d_in[7];
  u16* out = (u16*)d_out;

  char* ws = (char*)d_ws;
  const size_t MB4 = (size_t)S_ * D_ * 2;
  u16* kv1 = (u16*)(ws);
  u16* kv2 = (u16*)(ws + 2 * MB4);
  u16* o1 = (u16*)(ws + 4 * MB4);
  u16* x1 = (u16*)(ws + 5 * MB4);
  u16* x3 = (u16*)(ws + 6 * MB4);
  unsigned* hand = (unsigned*)(ws + 7 * MB4);
  double* stats = (double*)(ws + 8 * MB4);
  unsigned* gran = (unsigned*)(ws + 9 * MB4);
  float* csf = (float*)(ws + 9 * MB4 + (1 << 20));
  u16* o2 = (u16*)(ws + 10 * MB4);
  u16* wc = (u16*)((((uintptr_t)ws + 11 * MB4 + 2047) & ~(uintptr_t)2047) + (((uintptr_t)wqkv + 128) & 2047));
  Args2 a;
  for (int l = 0; l < 2; ++l) {
    Args& r = a.l[l];
    r.lay = l; r.a = l ? x1 : x0; r.kv = l ? kv2 : kv1; r.o = l ? o2 : o1; r.xo = l ? x3 : x1; r.so = l ? stats + STAT_N : stats;
    r.W = wqkv; r.wc = wc; r.wg = wg; r.wu = wu; r.wd = wd; r.wo = wo; r.w_r = w_r; r.gran = gran; r.csf = csf; r.stats = stats; r.hand = hand; r.out = out;
  }
  layers_kernel<<<256, 768, 0, stream>>>(a);
}
```

```cpp
#include <hip/hip_runtime.h>
#include <hip/hip_bf16.h>
#include <stdint.h>
#include <type_traits>
#include <utility>

typedef unsigned short u16;
typedef short s16x4 __attribute__((ext_vector_type(4)));
typedef short s16x8 __attribute__((ext_vector_type(8)));
typedef float f32x16 __attribute__((ext_vector_type(16)));
typedef float f32x4 __attribute__((ext_vector_type(4)));
typedef unsigned int u32x4 __attribute__((ext_vector_type(4)));
typedef unsigned int u32x2 __attribute__((ext_vector_type(2)));
typedef __bf16 bf16x8 __attribute__((ext_vector_type(8)));
typedef __bf16 bf16x16 __attribute__((ext_vector_type(16)));

__device__ __forceinline__ int fresh_lane() {
  int l;
  asm volatile("v_mbcnt_lo_u32_b32 %0, -1, 0\n\tv_mbcnt_hi_u32_b32 %0, -1, %0" : "=v"(l));
  return l;
}
#define STAMP(k, i)
#define STAMPV(k, i, v)
#define CT0()
#define CT1(x)
#define LOGDEF
#define LOG()
#define LOGAT(k)
#define lk_set(k)
#define LOGOUT(slot)
#define TE(grp, k)
#define TEARG
#define TEPASS
constexpr int S_ = 2048, D_ = 1024, H_ = 32, DH_ = 32, F_ = 3584;

__device__ __forceinline__ int kpos(int r) { return (r & ~12) | ((r & 4) << 1) | ((r & 8) >> 1); }
__device__ __forceinline__ float bf2f(u16 v) { return __uint_as_float(((unsigned)v) << 16); }
__device__ __forceinline__ u16 f2bf(float f) {
  __hip_bfloat16 b = __float2bfloat16(f);
  return *reinterpret_cast<u16*>(&b);
}
typedef float f32x2 __attribute__((ext_vector_type(2)));
typedef __bf16 bf16x2 __attribute__((ext_vector_type(2)));
__device__ __forceinline__ unsigned pack2(float lo, float hi) {
  f32x2 v = {lo, hi};
  return __builtin_bit_cast(unsigned, __builtin_convertvector(v, bf16x2));
}
__device__ __forceinline__ float rbf(float f) { return __uint_as_float(pack2(f, f) << 16); }
__device__ __forceinline__ void rbf2(float& a, float& b) {
  const unsigned p = pack2(a, b);
  a = __uint_as_float(p << 16); b = __uint_as_float(p & 0xffff0000u);
}

__device__ __forceinline__ float fadd(float a, float b) { float r = a + b; asm("" : "+v"(r)); return r; }
template <int CTRL, int RMASK>
__device__ __forceinline__ float dpp_add(float v) {
  const int t = __builtin_amdgcn_update_dpp(0, __float_as_int(v), CTRL, RMASK, 0xf, false);
  return v + __int_as_float(t);
}
__device__ __forceinline__ float sum_hi3(float v) {
  v += __int_as_float(__builtin_amdgcn_update_dpp(0, __float_as_int(v), 0x128, 0xf, 0xf, false));
  const auto a = __builtin_amdgcn_permlane16_swap(__float_as_uint(v), __float_as_uint(v), false, false);
  v = __uint_as_float(a[0]) + __uint_as_float(a[1]);
  const auto b = __builtin_amdgcn_permlane32_swap(__float_as_uint(v), __float_as_uint(v), false, false);
  return __uint_as_float(b[0]) + __uint_as_float(b[1]);
}
__device__ __forceinline__ float wave_sum(float v) {
  v = dpp_add<0x111, 0xf>(v);
  v = dpp_add<0x112, 0xf>(v);
  v = dpp_add<0x114, 0xf>(v);
  v = dpp_add<0x118, 0xf>(v);
  v = dpp_add<0x142, 0xa>(v);
  v = dpp_add<0x143, 0xc>(v);
  return __int_as_float(__builtin_amdgcn_readlane(__float_as_int(v), 63));
}

template <int CTRL, int RMASK>
__device__ __forceinline__ unsigned dpp_umax(unsigned v) {
  const unsigned t = (unsigned)__builtin_amdgcn_update_dpp(0, (int)v, CTRL, RMASK, 0xf, false);
  return v > t ? v : t;
}
__device__ __forceinline__ unsigned wave_umax(unsigned v) {
  v = dpp_umax<0x111, 0xf>(v); v = dpp_umax<0x112, 0xf>(v); v = dpp_umax<0x114, 0xf>(v); v = dpp_umax<0x118, 0xf>(v);
  v = dpp_umax<0x142, 0xa>(v); v = dpp_umax<0x143, 0xc>(v);
  return (unsigned)__builtin_amdgcn_readlane((int)v, 63);
}

#define NREP 32
constexpr unsigned SENT = 0xFFFFFFFFu;
constexpr int GRAN_N = 256 * NREP;
constexpr int Y_N = F_ / 16 * D_;
constexpr int X2_N = D_ / 2;
constexpr int XS_N = 128;
constexpr int P4_N = 3 * D_ * 16;
constexpr int PA_N = H_ * 8 * 64;
constexpr int HAND_N = GRAN_N + Y_N + X2_N + XS_N + P4_N + PA_N;
constexpr int MD_O = 16 * 16 * 128 * 4;
constexpr int PK_O = MD_O;
constexpr int PP_O = PK_O + 128 * NREP;
constexpr int Y3_O = PP_O + H_ * S_;
constexpr int PS_N = Y3_O + Y_N;
constexpr int GR_ROW = 64, GR_COL = GR_ROW + 4 * S_, GR_KV = GR_COL + 2 * 16 * 16 * 192;
#define RO 16
#define RK 16
#define RX 1
constexpr int GR_O = GR_KV + 2 * 16 * 32 * RK, GR_X = GR_O + 2 * 16 * 64 * RO, GR_N = GR_X + 16 * 128 * RX;
constexpr int GR_F = GR_N + 256 * 64;
constexpr int STAT_N = 3 * S_;

__device__ __forceinline__ void st_ag(unsigned* p, unsigned v) { __hip_atomic_store(p, v, __ATOMIC_RELAXED, __HIP_MEMORY_SCOPE_AGENT); }
__device__ __forceinline__ unsigned ld_ag(const unsigned* p) { return __hip_atomic_load(p, __ATOMIC_RELAXED, __HIP_MEMORY_SCOPE_AGENT); }
__device__ __forceinline__ double ld_ag(const double* p) { return __hip_atomic_load(p, __ATOMIC_RELAXED, __HIP_MEMORY_SCOPE_AGENT); }
__device__ __forceinline__ unsigned f2word(float f) { const unsigned u = __float_as_uint(f); return u < 0xFFFFFFFEu ? u : 0xFFFFFFFEu; }
constexpr int SPIN_MAX = 1 << 18;
__device__ __forceinline__ unsigned umax(unsigned a, unsigned b) { return a > b ? a : b; }
__device__ __forceinline__ bool none(bool p) { return __builtin_amdgcn_ballot_w64(p) == 0ull; }

__device__ __forceinline__ void ln_from_sums(double s1, double s2, float& mean, float& var, float& mb, float& den) {
  const double md = s1 * (1.0 / 1024.0);
  const double vd = fma(-md, md, s2 * (1.0 / 1024.0));
  mean = (float)md; var = fmaxf((float)vd, 0.f);
  mb = rbf(mean);
  den = rbf(__builtin_amdgcn_sqrtf(rbf(rbf(var) + 1e-5f)));
}
__device__ __forceinline__ float frcp(float x) { return __builtin_amdgcn_rcpf(x); }
__device__ __forceinline__ void ln_apply2(float& a, float& b, float mb, float den, float rin) {
  a -= mb; b -= mb;
  rbf2(a, b);
  float qa = a * rin, qb = b * rin;
  qa = fmaf(fmaf(-qa, den, a), rin, qa);
  qb = fmaf(fmaf(-qb, den, b), rin, qb);
  rbf2(qa, qb);
  a = qa; b = qb;
}

#define CODE_ADDR(SYM, out64)                                                                                          \
  do {                                                                                                                 \
    unsigned lo_, hi_;                                                                                                 \
    asm volatile("s_getpc_b64 s[90:91]\n\ts_add_u32 s90, s90, " SYM "@rel32@lo+4\n\ts_addc_u32 s91, s91, " SYM          \
                 "@rel32@hi+12\n\ts_mov_b32 %0, s90\n\ts_mov_b32 %1, s91"                                             \
                 : "=s"(lo_), "=s"(hi_) :: "s90", "s91", "scc");                                                        \
    out64 = ((unsigned long long)hi_ << 32) | lo_;                                                                     \
  } while (0)

#define QNX 4
constexpr int QMX = 32 / QNX, QNG = 16 / QNX, QMG = 8 / QNG;

__device__ __forceinline__ unsigned touch(const void* p) { return *reinterpret_cast<const unsigned*>(p); }

__device__ __forceinline__ unsigned touch_code(unsigned long long lo, unsigned long long hi, int pc, int lane) {
  const unsigned long long p = lo + (unsigned)(pc * 8192 + lane * 128);
  return p + 4 <= hi ? *reinterpret_cast<__attribute__((address_space(1))) const unsigned*>(p) : 0u;
}

template <bool SC = false>
__device__ __forceinline__ void glds16s(const void* sbase, unsigned voff, unsigned lds_dst) {
  unsigned keep;
  if (SC)
    asm volatile("s_mov_b32 %0, m0\n\ts_mov_b32 m0, %3\n\ts_nop 2\n\tglobal_load_lds_dwordx4 %1, %2 sc1\n\ts_mov_b32 m0, %0"
                 : "=&s"(keep) : "v"(voff), "s"(sbase), "s"(lds_dst) : "memory");
  else
  asm volatile("s_mov_b32 %0, m0\n\ts_mov_b32 m0, %3\n\ts_nop 2\n\tglobal_load_lds_dwordx4 %1, %2\n\ts_mov_b32 m0, %0"
               : "=&s"(keep) : "v"(voff), "s"(sbase), "s"(lds_dst) : "memory");
}
template <bool SC>
__device__ __forceinline__ void glds16x4(const void* sbase, unsigned v0, unsigned v1, unsigned v2, unsigned v3, unsigned lds_dst) {
  unsigned keep;
  if (SC)
    asm volatile("s_mov_b32 %0, m0\n\ts_mov_b32 m0, %6\n\ts_nop 2\n\tglobal_load_lds_dwordx4 %1, %5 sc1\n\tglobal_load_lds_dwordx4 %2, %5 offset:1024 sc1\n\t"
                 "global_load_lds_dwordx4 %3, %5 offset:2048 sc1\n\tglobal_load_lds_dwordx4 %4, %5 offset:3072 sc1\n\ts_mov_b32 m0, %0"
                 : "=&s"(keep) : "v"(v0), "v"(v1), "v"(v2), "v"(v3), "s"(sbase), "s"(__builtin_amdgcn_readfirstlane(lds_dst)) : "memory");
  else
    asm volatile("s_mov_b32 %0, m0\n\ts_mov_b32 m0, %6\n\ts_nop 2\n\tglobal_load_lds_dwordx4 %1, %5\n\tglobal_load_lds_dwordx4 %2, %5 offset:1024\n\t"
                 "global_load_lds_dwordx4 %3, %5 offset:2048\n\tglobal_load_lds_dwordx4 %4, %5 offset:3072\n\ts_mov_b32 m0, %0"
                 : "=&s"(keep) : "v"(v0), "v"(v1), "v"(v2), "v"(v3), "s"(sbase), "s"(__builtin_amdgcn_readfirstlane(lds_dst)) : "memory");
}
constexpr float QSCALE = 0.17677669529663687f * 1.4426950408889634f;

__device__ __forceinline__ unsigned min4(u32x4 v) { const unsigned a = v[0] < v[1] ? v[0] : v[1], b = v[2] < v[3] ? v[2] : v[3]; return a < b ? a : b; }
#define LDS_AT(T, a) ((__attribute__((address_space(3))) T*)(uintptr_t)(a))
__device__ __forceinline__ u32x4 lds_rd4(unsigned a) { return *LDS_AT(const volatile u32x4, a); }
__device__ __forceinline__ void lds_wr(unsigned a, unsigned v) { *LDS_AT(volatile unsigned, a) = v; }
__device__ __forceinline__ void split3(float v, unsigned& p0, unsigned& p1, unsigned& p2) {
  p0 = pack2(v, v) & 0xffffu; v -= __uint_as_float(p0 << 16);
  p1 = pack2(v, v) & 0xffffu; v -= __uint_as_float(p1 << 16);
  p2 = pack2(v, v) & 0xffffu;
}

#define PIN() __builtin_amdgcn_sched_barrier(0)
template <int B, class F, int... I>
__device__ __forceinline__ void static_for_(F&& f, std::integer_sequence<int, I...>) { (f(std::integral_constant<int, B + I>{}), ...); }
template <int B, int E, class F>
__device__ __forceinline__ void static_for(F&& f) { static_for_<B>(f, std::make_integer_sequence<int, E - B>{}); }

#define RESAT 20
#define LNPRE 1
#define TPRE 60
#define TPSL 0
#define OSCH 0
#define FCW 0
#define FCPRE 44
#define FCSLP 2
#define SIGAT 24
#define XPRIO 1
#define OPD 6
#define LAUX 16
#define CSLP 2
#define EPRIO 3
#define OPRIO 1
#define XSLP 1
#define ROT 0
#define CSL1 8
#define XAUX 16
__device__ __forceinline__ void oproj_tail(const bool RES2, const int tid, const unsigned lbase, const unsigned flg, const int tmi, const int tni, const int idx,
                                           unsigned* osig, unsigned* xsig, const unsigned tag,
                                           const u16* A, u16* out,
                                           const u16* res, const u16* __restrict__ w_r, double* stats,
                                           const double* stats_in, unsigned* hand, u32x4 (&xres)[2] TEARG) {
  constexpr int ldw = D_, PD = OPD;
  const int lane = tid & 63, wid = __builtin_amdgcn_readfirstlane(tid >> 6);
  const int m0 = tmi * 128, n0 = tni * 64;
  const int dk = RES2 ? 7 : 3;
  if (wid == 0) STAMP(dk, 0);
  TE(1, 3);
  asm volatile("" : "+s"(A) :: "memory");
  PIN();
  const int row = m0 + (wid & 3) * 32 + (wid >> 2) * 16 + (lane >> 3), col = n0 + (lane & 7) * 8;
  const unsigned tb = lbase + 147456;
  typedef __attribute__((address_space(1))) double* gdp;
  typedef __attribute__((address_space(1))) unsigned* gup;
  unsigned tr, oad;
  gdp pst;
  gup pps;
  int xo;
  auto setup = [&]() __attribute__((always_inline)) {
    tr = tb + (row - m0) * 128 + (((lane ^ (lane >> 3)) & 7) << 4);
    pst = (gdp)(stats + ((lane & 3) == 3 ? 0 : (lane & 3)) * S_ + row + 8 * ((lane >> 2) & 1));
    pps = (gup)(hand + 2 * HAND_N + ((tmi * 16 + tni) * 128 + row - m0 + 8 * ((lane >> 2) & 1)) * 4 + (lane & 3));
    xo = (row * ldw + col) * 2;
    asm volatile("" : "+v"(tr), "+v"(pst), "+v"(pps), "+v"(xo));
    PIN();
  };
  {
    oad = lane == 0 ? flg + 24 : flg + 64 + lane * 4;
    gup ps = (gup)(osig + lane * 64);
    asm volatile("" : "+v"(oad), "+v"(ps));
    asm volatile("s_waitcnt vmcnt(1)" ::: "memory");
    TE(2, 1);
    const unsigned old = __hip_atomic_fetch_add(LDS_AT(unsigned, oad), 1u, __ATOMIC_RELAXED, __HIP_MEMORY_SCOPE_WORKGROUP);
    if (__builtin_amdgcn_readfirstlane(old) == 7u && lane < RO) __hip_atomic_store(ps, tag, __ATOMIC_RELAXED, __HIP_MEMORY_SCOPE_AGENT);
    PIN();
    TE(2, 2);
    if (wid == 0) STAMP(dk, 12);
  }
  auto announce = [&]() __attribute__((always_inline)) {
    asm volatile("" ::: "memory");
    PIN();
    __builtin_amdgcn_s_barrier();
    PIN();
    if (wid == 0) STAMP(dk, 1);
    TE(2, 3); TE(4, 0); TE(7, 1);
  };
  u32x4 rwr;
  auto sums = [&](const u32x4& x0, const u32x4& x1) __attribute__((always_inline)) {
    float f[2][3];
#pragma unroll
    for (int p = 0; p < 2; ++p) {
      f[p][0] = f[p][1] = f[p][2] = 0.f;
#pragma unroll
      for (int e = 0; e < 4; ++e) {
        const unsigned xu = p ? x1[e] : x0[e], wu = rwr[e];
        const bf16x2 x = __builtin_bit_cast(bf16x2, xu);
        f[p][0] = __builtin_amdgcn_fdot2_f32_bf16(x, __builtin_bit_cast(bf16x2, 0x3f803f80u), f[p][0], false);
        f[p][1] = __builtin_amdgcn_fdot2_f32_bf16(x, x, f[p][1], false);
        f[p][2] = __builtin_amdgcn_fdot2_f32_bf16(x, __builtin_bit_cast(bf16x2, wu), f[p][2], false);
      }
    }
    auto addx = [&](float keep, float give, auto ctl) {
      return keep + __int_as_float(__builtin_amdgcn_update_dpp(0, __float_as_int(give), decltype(ctl)::value, 0xf, 0xf, false));
    };
    const bool b2 = lane & 4, b1 = lane & 2, z0 = (lane & 3) == 0, z1 = (lane & 3) == 1;
    float g[3];
#pragma unroll
    for (int c = 0; c < 3; ++c) g[c] = addx(b2 ? f[1][c] : f[0][c], b2 ? f[0][c] : f[1][c], std::integral_constant<int, 0x141>{});
    const float u1 = addx(b1 ? g[2] : g[0], b1 ? g[0] : g[2], std::integral_constant<int, 0x4e>{});
    const float u2 = addx(g[1], g[1], std::integral_constant<int, 0x4e>{});
    return addx(z1 ? u2 : u1, z0 ? u2 : u1, std::integral_constant<int, 0xb1>{});
  };
  if (wid < 4) {
    const __amdgpu_buffer_rsrc_t rsa = __builtin_amdgcn_make_buffer_rsrc(const_cast<u16*>(A), 0, S_ * D_ * 2, 0x00020000);
    int sa0 = (tmi * 4 + wid) * 65536, va0 = (lane >> 4) * 512 + (lane & 15) * 16;
    asm volatile("" : "+v"(va0));
    asm volatile("" : "+s"(sa0));
    const int rot = ROT ? tni & 7 : 0;
    auto lda = [&](int i, int h) { return __builtin_amdgcn_raw_buffer_load_b128(rsa, va0 + (i & 1) * 2048 + h * 256, sa0 + (((i >> 2) + rot) & 7) * 8192 + (i & 2) * 2048, LAUX); };
    unsigned wad[4], tw[4];
    f32x4 acc[2][4];
    const int k = (lane >> 5) * 16 + ((lane >> 2) & 7);
#pragma unroll
    for (int j = 0; j < 4; ++j) {
      wad[j] = lbase + k * 128 + (((2 * j) ^ (k & 6)) << 4) + 8 * (lane & 3) + rot * 16384;
      tw[j] = tb + wid * 4096 + (lane & 15) * 128 + (((2 * j + (lane >> 5)) ^ (lane & 7)) << 4) + ((lane >> 4) & 1) * 8;
      asm volatile("" : "+v"(wad[j]), "+v"(tw[j]));
    }
    auto ldw_ = [&](int i, int j) {
      const unsigned a = wad[j] + (i & 3) * 4096;
      s16x4 lo = __builtin_amdgcn_ds_read_tr16_b64_v4i16(LDS_AT(s16x4, a));
      s16x4 h4 = __builtin_amdgcn_ds_read_tr16_b64_v4i16(LDS_AT(s16x4, a + 1024));
      return (s16x8)__builtin_shufflevector(lo, h4, 0, 1, 2, 3, 4, 5, 6, 7);
    };
    u32x4 id[2];
#pragma unroll
    for (int j = 0; j < 2; ++j) {
#pragma unroll
      for (int e = 0; e < 4; ++e) id[j][e] = (lane >> 4) == 2 * j + ((lane >> 3) & 1) && ((lane & 7) >> 1) == e ? ((lane & 1) ? 0x3f800000u : 0x3f80u) : 0u;
      asm volatile("" : "+v"(id[j]));
    }
    unsigned rad = tb + wid * 4096 + (lane & 15) * 128 + (((lane >> 4) ^ (lane & 7)) << 4), rfa = flg + 36 + wid;
    asm volatile("" : "+v"(rad), "+v"(rfa));
    u32x4 rb[2][2];
    rwr = *reinterpret_cast<const u32x4*>(w_r + col);
    setup();
    asm volatile("" : "+v"(rwr));
    announce();
    u32x4 af[PD][2];
    s16x8 wf[2][4];
#pragma unroll
    for (int i = 0; i < PD; ++i) { af[i][0] = lda(i, 0); af[i][1] = lda(i, 1); }
    PIN();
    __builtin_amdgcn_s_setprio(OPRIO);
#pragma unroll
    for (int q = 0; q < 4; ++q) rb[q >> 1][q & 1] = lds_rd4((rad ^ ((q & 1) * 64)) + (q >> 1) * 2048);
    PIN();
#pragma unroll
    for (int j = 0; j < 4; ++j)
#pragma unroll
      for (int h = 0; h < 2; ++h) { asm volatile("v_mfma_f32_16x16x32_bf16 %0, %1, %2, 0" : "=&v"(acc[h][j]) : "v"(id[j & 1]), "v"(rb[h][j >> 1])); PIN(); }
#pragma unroll 1
    for (int spin = 0; spin < SPIN_MAX; ++spin)
      if (__builtin_amdgcn_readfirstlane(*LDS_AT(const volatile unsigned, flg)) >= 40u) break;
    asm volatile("" : "+v"(va0) :: "memory");
    PIN();
#pragma unroll
    for (int j = 0; j < 4; ++j) wf[0][j] = ldw_(0, j);
    s16x4 wlo;
    static_for<0, 32>([&](auto ic) __attribute__((always_inline)) {
      constexpr int i = decltype(ic)::value;
      if (i == SIGAT) { __hip_atomic_fetch_add(LDS_AT(unsigned, oad + 4), 1u, __ATOMIC_RELAXED, __HIP_MEMORY_SCOPE_WORKGROUP); PIN(); }
#pragma unroll
      for (int g = 0; g < 8; ++g) {
        const int j = OSCH ? g & 3 : g >> 1, h = OSCH ? g >> 2 : g & 1;
        asm volatile("v_mfma_f32_16x16x32_bf16 %0, %1, %2, %0" : "+v"(acc[h][j]) : "v"(wf[i & 1][j]), "v"(af[i % PD][h]));
        PIN();
        if (i == 15 && !(g & 1)) { wad[g >> 1] += 65536; asm volatile("" : "+v"(wad[g >> 1])); }
        if (i + 1 < 32) {
          const s16x4 v = __builtin_amdgcn_ds_read_tr16_b64_v4i16(LDS_AT(s16x4, wad[g >> 1] + ((i + 1) & 15) * 4096 + (g & 1) * 1024));
          if (g & 1) wf[(i + 1) & 1][g >> 1] = (s16x8)__builtin_shufflevector(wlo, v, 0, 1, 2, 3, 4, 5, 6, 7);
          else wlo = v;
          PIN();
        }
        if (OSCH ? (g & 3) == 3 : g >= 6) { if (i + PD < 32) { af[i % PD][h] = lda(i + PD, h); PIN(); } }
        if (!OSCH && i + 1 < 32 && i != SIGAT && i + 1 != SIGAT) {
          if (g == 3) { __builtin_amdgcn_s_waitcnt(0xc47f); PIN(); }
          if (g == 7 && i + PD < 32) { __builtin_amdgcn_s_waitcnt(0x0470 | (2 * PD - 2)); PIN(); }
          if (g == 7 && i + PD >= 32) { __builtin_amdgcn_s_waitcnt(0xc47f); PIN(); }
        }
      }
    });
    asm volatile("s_nop 3" : "+v"(acc[0][0]), "+v"(acc[1][0]));
    TE(4, 1); TE(7, 2);
    if (wid == 0) STAMP(dk, 17);
    if (wid == 1 && !RES2) STAMP(4, 0);
#pragma unroll
    for (int j = 0; j < 4; ++j)
#pragma unroll
      for (int h = 0; h < 2; ++h) {
        *LDS_AT(u32x2, tw[j] + h * 2048) = u32x2{pack2(acc[h][j][0], acc[h][j][1]), pack2(acc[h][j][2], acc[h][j][3])};
        PIN();
      }
    asm volatile("" ::: "memory");
    *LDS_AT(volatile unsigned char, rfa) = (unsigned char)2;
#pragma unroll
    for (int p = 0; p < 2; ++p) xres[p] = lds_rd4(tr + p * 1024);
    PIN();
    if (!RES2) {
      const __amdgpu_buffer_rsrc_t rso = __builtin_amdgcn_make_buffer_rsrc(out, 0, S_ * D_ * 2, 0x00020000);
      const __amdgpu_buffer_rsrc_t rsf = __builtin_amdgcn_make_buffer_rsrc(xsig + wid, 0, RX * 512, 0x00020000);
      const u32x4 x2 = lds_rd4(tr + 2048), x3 = lds_rd4(tr + 3072);
      PIN();
#pragma unroll
      for (int p = 0; p < 4; ++p) { __builtin_amdgcn_raw_buffer_store_b128(p < 2 ? xres[p] : (p < 3 ? x2 : x3), rso, xo + p * (8 * ldw * 2), 0, XAUX); PIN(); }
      TE(4, 2); TE(5, 0);
      const float s0 = sums(xres[0], xres[1]), s1 = sums(x2, x3);
      if ((lane & 3) != 3) { __builtin_amdgcn_global_atomic_fadd_f64(pst, (double)s0); __builtin_amdgcn_global_atomic_fadd_f64(pst + 16, (double)s1); }
      PIN();
      TE(4, 3); TE(5, 1);
      asm volatile("s_waitcnt vmcnt(2)" ::: "memory");
      __builtin_amdgcn_raw_buffer_store_b32(tag, rsf, lane * 512 + 256, 0, 16);
      *LDS_AT(volatile unsigned char, rfa) = (unsigned char)3;
      PIN();
      TE(5, 2);
      asm volatile("s_waitcnt vmcnt(1)" ::: "memory");
      __builtin_amdgcn_raw_buffer_store_b32(tag, rsf, lane * 512, 0, 16);
      PIN();
      TE(5, 3);
      __builtin_amdgcn_s_setprio(0);
      return;
    }
    TE(4, 2); TE(5, 0);
  } else {
    u32x4 rres[4];
    double is1[4], is2[4];
#pragma unroll
    for (int p = 0; p < 4; ++p)
      rres[p] = __builtin_amdgcn_raw_buffer_load_b128(__builtin_amdgcn_make_buffer_rsrc(const_cast<u16*>(res), 0, S_ * D_ * 2, 0x00020000), ((row - 16 + 8 * p) * ldw + col) * 2, 0, 16);
    rwr = *reinterpret_cast<const u32x4*>(w_r + col);
#pragma unroll
    for (int p = 0; p < 4; ++p) { is1[p] = ld_ag(stats_in + row - 16 + 8 * p); is2[p] = ld_ag(stats_in + S_ + row - 16 + 8 * p); }
    PIN();
    setup();
    if (RES2) {
#pragma unroll
      for (int p = 0; p < 4; ++p) {
        float mean, var, mb, den;
        ln_from_sums(is1[p], is2[p], mean, var, mb, den);
        const float rin = frcp(den);
#pragma unroll
        for (int e = 0; e < 4; ++e) {
          const f32x2 x = {__uint_as_float(rres[p][e] << 16), __uint_as_float(rres[p][e] & 0xffff0000u)};
          const f32x2 h = (x - f32x2{mb, mb}) * f32x2{rin, rin} + x;
          rres[p][e] = pack2(h[0], h[1]);
        }
        asm volatile("" : "+v"(rres[p]));
      }
      PIN();
    }
    if ((idx >> 5) == tmi * 4 + wid - 4) {
      const int dr = idx - (row - 16);
      u32x4 qq;
#pragma unroll 1
      for (int i = 0; i < SPIN_MAX; ++i) {
        qq = __builtin_amdgcn_raw_buffer_load_b128(__builtin_amdgcn_make_buffer_rsrc(hand, 0, HAND_N * 4, 0x00020000), (GRAN_N + Y_N) * 4 + col * 2, 0, 16);
        if (none(umax(umax(qq[0], qq[1]), umax(qq[2], qq[3])) == SENT)) break;
        asm volatile("" ::: "memory");
      }
#pragma unroll
      for (int p = 0; p < 4; ++p)
#pragma unroll
        for (int e = 0; e < 4; ++e) rres[p][e] = dr == 8 * p ? qq[e] : rres[p][e];
    }
#pragma unroll 1
    for (int i = 0; i < 4 * SPIN_MAX; ++i)
      if (__builtin_amdgcn_readfirstlane(*LDS_AT(const volatile unsigned, flg + 16)) >= 12u) break;
#pragma unroll
    for (int p = 0; p < 4; ++p) *LDS_AT(volatile u32x4, tr - 2048 + p * 1024) = rres[p];
    asm volatile("s_waitcnt lgkmcnt(0)" ::: "memory");
    announce();
    TE(4, 1); TE(7, 2);
    if (!RES2) return;
    asm volatile("" : "+v"(rwr) :: "memory");
    __builtin_amdgcn_s_sleep(TPRE);
#pragma unroll 1
    for (int i = 0; i < 4 * SPIN_MAX; ++i) {
      if (__builtin_amdgcn_readfirstlane(*LDS_AT(const volatile unsigned char, flg + 32 + wid)) == 2u) break;
      __builtin_amdgcn_s_sleep(TPSL);
    }
    asm volatile("" ::: "memory");
    __builtin_amdgcn_s_setprio(XPRIO);
    PIN();
    TE(4, 2); TE(5, 0);
#pragma unroll
    for (int p = 0; p < 2; ++p) xres[p] = lds_rd4(tr + p * 1024);
  }
  TE(4, 3); TE(7, 3);
  const float sv = sums(xres[0], xres[1]);
  if (wid == 0) STAMP(dk, 14);
  if ((lane & 3) != 3) __hip_atomic_store(pps, f2word(sv), __ATOMIC_RELAXED, __HIP_MEMORY_SCOPE_AGENT);
  PIN();
  __builtin_amdgcn_s_setprio(0);
  __hip_atomic_fetch_add(LDS_AT(unsigned, oad), 1u, __ATOMIC_RELAXED, __HIP_MEMORY_SCOPE_WORKGROUP);
  TE(5, 1); TE(5, 2);
  PIN();
}

#define MFMA32(a, b, c) __builtin_amdgcn_mfma_f32_32x32x16_bf16(a, b, c, 0, 0, 0)
#define MFMA16(a, b, c) __builtin_amdgcn_mfma_f32_16x16x32_bf16(a, b, c, 0, 0, 0)

#define PLO(x) __builtin_bit_cast(bf16x16, __builtin_shufflevector(x, x, 0, 1, 2, 3, 4, 5, 6, 7))
#define PHI(x) __builtin_bit_cast(bf16x16, __builtin_shufflevector(x, x, 8, 9, 10, 11, 12, 13, 14, 15))
#define SMF32(a, b, c) __builtin_amdgcn_smfmac_f32_32x32x32_bf16(__builtin_bit_cast(bf16x8, a), b, c, sidx, 0, 0)
#define SMF16(a, b, c) __builtin_amdgcn_smfmac_f32_16x16x64_bf16(__builtin_bit_cast(bf16x8, a), b, c, sidx, 0, 0)

__device__ __forceinline__ void patch_kv(unsigned tab, int r, s16x8& k0, s16x8& k1, s16x8& v0, s16x8& v1, bool on = true) {
  const int lane = fresh_lane(), hi = lane >> 5;
  if (on && (lane & 31) == kpos(r)) {
    k0 = *LDS_AT(const s16x8, tab + hi * 16); k1 = *LDS_AT(const s16x8, tab + 32 + hi * 16);
  }
  const short vv = *LDS_AT(const short, tab + 128 + (lane & 31) * 2);
  const int js = (on && hi == ((r >> 2) & 1)) ? (r >> 4) * 8 + ((r >> 3) & 1) * 4 + (r & 3) : -1;
#pragma unroll
  for (int e = 0; e < 8; ++e) { v0[e] = js == e ? vv : v0[e]; v1[e] = js == 8 + e ? vv : v1[e]; }
}

__device__ __forceinline__ void attn_safe(const u16* KV, int tile0, s16x8 q0, s16x8 q1,
                                       f32x16& o, float& l_out, int ptile, int prow, unsigned ptab) {
  const int lane = fresh_lane();
  float m = -INFINITY, l = 0.f;
  const int sidx = 0xDDDDDDDD;
  const __amdgpu_buffer_rsrc_t rs = __builtin_amdgcn_make_buffer_rsrc(const_cast<u16*>(KV), 0, 2 * S_ * D_ * 2, 0x00020000);
#pragma unroll
  for (int e = 0; e < 16; ++e) o[e] = 0.f;
  for (int tt = 0; tt < 64; ++tt) {
    const int t = tile0 + tt, off = t * 2048 + lane * 16;
    s16x8 k0 = __builtin_bit_cast(s16x8, __builtin_amdgcn_raw_buffer_load_b128(rs, off, 0, 16));
    s16x8 k1 = __builtin_bit_cast(s16x8, __builtin_amdgcn_raw_buffer_load_b128(rs, off + 1024, 0, 16));
    s16x8 v0 = __builtin_bit_cast(s16x8, __builtin_amdgcn_raw_buffer_load_b128(rs, off + S_ * D_ * 2, 0, 16));
    s16x8 v1 = __builtin_bit_cast(s16x8, __builtin_amdgcn_raw_buffer_load_b128(rs, off + S_ * D_ * 2 + 1024, 0, 16));
    if (t == ptile) patch_kv(ptab, prow, k0, k1, v0, v1);
    f32x16 sc;
#pragma unroll
    for (int e = 0; e < 16; ++e) sc[e] = 0.f;
    sc = MFMA32(k0, q0, sc); sc = MFMA32(k1, q1, sc);
    float mt = sc[0];
#pragma unroll
    for (int e = 1; e < 16; ++e) mt = fmaxf(mt, sc[e]);
    mt = fmaxf(mt, __shfl_xor(mt, 32, 64));
    const float mn = fmaxf(m, mt);
    const float alpha = __builtin_amdgcn_exp2f(m - mn);
    m = mn;
#pragma unroll
    for (int e = 0; e < 16; ++e) sc[e] = __builtin_amdgcn_exp2f(sc[e] - mn);
    float ls = 0.f;
#pragma unroll
    for (int e = 0; e < 16; ++e) ls += __uint_as_float(__float_as_uint(sc[e]) & 0xffff0000u);
    l = l * alpha + ls;
#pragma unroll
    for (int e = 0; e < 16; ++e) o[e] *= alpha;
    o = SMF32(v0, PLO(sc), o); o = SMF32(v1, PHI(sc), o);
  }
  l_out = l + __shfl_xor(l, 32, 64);
}

template <int CTRL, int RMASK>
__device__ __forceinline__ float dpp_fmax(float v) {
  return fmaxf(v, __int_as_float(__builtin_amdgcn_update_dpp(__float_as_int(v), __float_as_int(v), CTRL, RMASK, 0xf, false)));
}
__device__ __forceinline__ float wave_fmax(float v) {
  asm volatile("s_nop 1\n\tv_max_f32_dpp %0, %0, %0 row_shr:1 row_mask:0xf bank_mask:0xf\n\ts_nop 1\n\tv_max_f32_dpp %0, %0, %0 row_shr:2 row_mask:0xf bank_mask:0xf\n\t"
               "s_nop 1\n\tv_max_f32_dpp %0, %0, %0 row_shr:4 row_mask:0xf bank_mask:0xf\n\ts_nop 1\n\tv_max_f32_dpp %0, %0, %0 row_shr:8 row_mask:0xf bank_mask:0xf\n\t"
               "s_nop 1\n\tv_max_f32_dpp %0, %0, %0 row_bcast:15 row_mask:0xa bank_mask:0xf\n\ts_nop 1\n\tv_max_f32_dpp %0, %0, %0 row_bcast:31 row_mask:0xc bank_mask:0xf\n\ts_nop 0"
               : "+v"(v));
  return __int_as_float(__builtin_amdgcn_readlane(__float_as_int(v), 63));
}
__device__ __forceinline__ float sum_hi5(float v) {
  v = sum_hi3(v);
  v += __int_as_float(__builtin_amdgcn_update_dpp(0, __float_as_int(v), 0x124, 0xf, 0xf, false));
  v += __int_as_float(__builtin_amdgcn_update_dpp(0, __float_as_int(v), 0x122, 0xf, 0xf, false));
  return v;
}

#define PSLP 8
#define WSLP 3
#define PRE 28
#define PRE1 8
#define PRE5 8
#define W3SLP 4
#define FSTART 10
#define BAL1 false
#define FPRE0 20
#define FPRE 16
#define FSLP 4
#define OAUX 16
#define BALO 1
#define OMX 4
#define T0 2u
#define TS 2u
#define TW 10
#define HW 2
#define HP1 1
#define POLLAT 11
#define X2AT 2
#define HPRIO 3
#define SSLP 1
#define SPRE 6
#define QOV 2
#define FLAT 4
#define WGAPS {0, 1, 7, 8, 9, 10, 11, 12}
#define KVS 4
#define CKAT 3
#define VEARLY 2
#define GNS 12
#define GNS2 20
#define PNS 12
#define GEV 4
#define G0 5u
#define GS 2u
#define IAT 1u
#define WDAT 10u
#define W3AT 11u
#define WOAT 12u
#define YSLP 2
#define ISLP 8
#define X3SLP 1
#define RPRE 8
#define RSLP 2
#define KSLP 2
#define UAT 13u
#define UHW 0
#define JDL 4000u
#define PPSLP 2
#define PKPRE 24
#define PKSLP 4
#define XPRE 8
#define RPRE2 8
#define XRSLP 2
#define MPRE 20
#define KEARLY 0
#define REFINE 1
#define FPRE2 24
#define OSLP 2
#define YD1 150
#define YD2 180
#define KDT 200
#define XDT 65
#define R2PRIO 0
#define LNPRIO 1
#define K6 2
#define MPR2 8
#define MSLP 4
#define BARG 0
struct Args {
  long long lay;
  const u16* a; u16* kv; u16* o; u16* xo; double* so;
  const u16* W; u16* wc;
  const u16 *wg, *wu, *wd, *wo, *w_r;
  unsigned* gran; float* csf; double* stats; unsigned* hand; u16* out;
};
typedef __attribute__((address_space(4))) const Args* ArgP;
#define WCAT 3
#define CAP0 1
#define PRE4 50
#define PRE7 20
#define UARR 12u
#define GF0 10
#define XGATE 8
#define KVW 2
#define ASLP 1
#define B15 3
#define W5AT 10
#define KO 5
#define PKM 0
#define PFIRST 1
#define LPOLL 3
#define QW 1
#define QU 1
#define PRO 2
#define WMAP 0x32100333
#define SMAP 0
#define STCH 6
#define KPOLL 10
#define TPRIO 2
#define PG1 6
#define PG2 12
#define TSLP 9
__device__ __forceinline__ bool slow_cols(const u16* W, int tni) { return ((((unsigned)(uintptr_t)W >> 7) + tni) & 7) == 3; }
#define ARGS_HERE                                                                                                      \
  ArgP ap_ = ap; asm volatile("" : "+s"(ap_));                                                                          \
  const u16* A = ap_->a; const u16* __restrict__ W = ap_->W; u16* KV = ap_->kv; u16* Of = ap_->o;        \
  const u16* __restrict__ wg = ap_->wg; const u16* __restrict__ wu = ap_->wu; const u16* __restrict__ wd = ap_->wd;       \
  const u16* __restrict__ wo = ap_->wo; const u16* __restrict__ w_r = ap_->w_r; unsigned* gran = ap_->gran;              \
  float* csf = ap_->csf; const double* stats = ap_->stats; double* stats_out = ap_->so; unsigned* hand = ap_->hand; \
  u16* xout = ap_->xo; u16* __restrict__ out = ap_->out
#define AGAIN "s_getpc_b64 s[90:91]\n\ts_add_u32 s90, s90, layers_kernel@rel32@lo+4\n\ts_addc_u32 s91, s91, layers_kernel@rel32@hi+12\n\ts_setpc_b64 s[90:91]"
#define KEPT(c)                                                                                                        \
  c "{v[104:107]}"(gu[0]), c "{v[108:111]}"(gu[1]), c "{v[112:115]}"(gu[2]), c "{v[116:119]}"(gu[3]), c "{v[120:123]}"(gu[4]),      \
  c "{v[124:127]}"(gu[5]), c "{v[128:131]}"(gu[6]), c "{v[132:135]}"(gu[7]), c "{v[136:139]}"(gu[8]), c "{v[140:143]}"(gu[9]),      \
  c "{v[144:147]}"(gu[10]), c "{v[148:151]}"(gu[11]), c "{v[152:155]}"(gu[12]), c "{v[156:159]}"(gu[13]), c "{v[160:163]}"(gu[14]), \
  c "{v[164:167]}"(gu[15])
#define LAYER_HERE ArgP apl_ = ap; asm volatile("" : "+s"(apl_)); const bool LAY2 = apl_->lay != 0
__device__ __forceinline__ void layer_body(const int wid, const int bid, const unsigned lbase, const ArgP ap, const unsigned long long t_entry) {
  constexpr int K = 1024, NKT = 16, NST = 3, ABYTES = 16384, WBYTES = 24576, STB = ABYTES + WBYTES, ldw = 3 * D_;
  constexpr int TABA = NST * STB, TABW = TABA + 2048, ALPHA = TABW + 3072, CRED = ALPHA + 512;
  constexpr int ASB = 16384, FLG = 8 * ASB + 256;
  constexpr int DUM = FLG + 64, TAB = DUM + 256, RED = TAB + 512, SACT = RED + 4096, RED2 = SACT + 1024, PL = RED2 + 2048, XML = PL + 512, UGL = XML + 4096, SPM = UGL + 1024, QOFF = 9 * ASB;
  static_assert(SPM + 2048 <= QOFF && CRED <= 8 * ASB && RED + 8192 <= QOFF && RED % 16 == 0, "lds");
#define XPERM 1
  const int xcd = XPERM == 0 ? bid & 7 : (bid & 1) * 4 + (((bid >> 1) + (bid & 1) * (XPERM - 1)) & 3), loc = bid >> 3;
  const int tmi = (xcd / QNG) * QMX + loc / QNX, tni = (xcd % QNG) * QNX + loc % QNX;
  const int m0 = tmi * 128, n0 = tni * 64;
  const int omi = (xcd / (OMX / 2)) * OMX + loc / (32 / OMX), oni = (xcd % (OMX / 2)) * (32 / OMX) + loc % (32 / OMX);
  constexpr unsigned VOFF = (unsigned)S_ * D_ * 2;
  auto tile_at = [&](unsigned n) __attribute__((always_inline)) {
#pragma unroll 1
    for (int i = 0; i < 4 * SPIN_MAX; ++i) {
      if (__builtin_amdgcn_readfirstlane(*LDS_AT(const volatile unsigned, lbase + FLG + 36)) == n * 0x01010101u) break;
      __builtin_amdgcn_s_sleep(1);
    }
    asm volatile("" ::: "memory");
    PIN();
  };
  auto all_ge = [&](unsigned w, unsigned n) { return ((w + (128u - n) * 0x01010101u) & 0x80808080u) == 0x80808080u; };
  if (wid >= 8) {
    u32x4 gu[16];
    LAYER_HERE;
    constexpr int dk = 6;
    const int hw = wid - 8, b = bid;
    int nb = 0;
    constexpr int lim = NKT - 1;
    auto tick_nw = [&]() {
      PIN();
      if (nb < lim) { __builtin_amdgcn_s_barrier(); ++nb; } else __builtin_amdgcn_s_sleep(2);
      asm volatile("" ::: "memory");
      PIN();
    };
    auto tick = [&]() {
      asm volatile("s_waitcnt lgkmcnt(0)" ::: "memory");
      tick_nw();
    };
    unsigned best, tag, rmd;
    float cs, alpha, m, wsum;
    u32x2 xq;
    s16x8 sl[8];
    const int kcap = CAP0 + (tmi & 7);
    if (!LAY2) {
      ARGS_HERE;
      const int lane = fresh_lane(), t = hw * 64 + lane;
      const unsigned ep = ld_ag(gran);
      const int srow = m0 + tni * 8 + hw * 2;
      u32x4 xa[2][2], wv[8];
      const int wr_ = (3 - ((unsigned)(uintptr_t)W >> 7) - (tni & ~3)) & 7;
      const bool cpy = hw == 3 && wr_ < 4 && wr_ != (tni & 3), wld = hw < 3 ? wr_ != (tni & 3) : cpy;
      const int wcn = cpy ? (tni & ~3) + wr_ : tni, wcp = cpy ? (tni - wr_ - 1) & 3 : hw;
      const int wof = ((tmi * 64 + (lane >> 3)) * ldw + wcp * D_ + wcn * 64 + (lane & 7) * 8) * 2;
      auto ld_w = [&]() {
        const __amdgpu_buffer_rsrc_t rw = __builtin_amdgcn_make_buffer_rsrc(const_cast<u16*>(W), 0, wld ? D_ * ldw * 2 : 0, 0x00020000);
#pragma unroll
        for (int r = 0; r < 8; ++r) wv[r] = __builtin_amdgcn_raw_buffer_load_b128(rw, wof, r * 8 * ldw * 2, 0);
        PIN();
      };
      auto wcopy = [&]() __attribute__((always_inline)) {
        if (cpy) {
          const __amdgpu_buffer_rsrc_t rw = __builtin_amdgcn_make_buffer_rsrc(ap_->wc, 0, D_ * ldw * 2, 0x00020000);
#pragma unroll
          for (int r = 0; r < 8; ++r) __builtin_amdgcn_raw_buffer_store_b128(wv[r], rw, wof + r * 8 * ldw * 2, 0, 16);
        }
        PIN();
      };
      const int wat = ((hw == 3 ? SMAP : WMAP) >> (4 * (tmi & 7))) & 3;
      unsigned ps = 0u;
      auto stouch = [&](int at) __attribute__((always_inline)) {
        if (at == STCH && cpy) ps = __builtin_amdgcn_raw_buffer_load_b32(__builtin_amdgcn_make_buffer_rsrc(const_cast<u16*>(W), 0, D_ * ldw * 2, 0x00020000),
                                                                        (((tmi ^ 8) * 64 + lane) * ldw + wcp * D_ + wcn * 64) * 2, 0, 0);
        PIN();
      };
      auto ld_x = [&](int h) {
#pragma unroll
        for (int r = 0; r < 2; ++r) xa[r][h] = *reinterpret_cast<const u32x4*>(A + (size_t)(srow + r) * D_ + (h ^ (tmi >> 3)) * 512 + lane * 8);
        PIN();
      };
      ld_x(0);
      if (X2AT == 0) ld_x(1);
      if (wat == 0) ld_w();
      unsigned pc = 0u;
      if (hw == 3) {
        unsigned long long a0, a1;
        CODE_ADDR("layers_kernel", a0);
        CODE_ADDR("end_marker_kernel", a1);
        pc = touch_code(a0, a1, loc, lane);
      }
      if (hw == 2) st_ag(gran + GR_N + bid * 64 + lane, lane + 1u);
      {
        const int gi = bid * 256 + t;
        if (gi < GRAN_N / 4) __builtin_amdgcn_raw_buffer_store_b128(u32x4{SENT, SENT, SENT, SENT}, __builtin_amdgcn_make_buffer_rsrc(hand, 0, GRAN_N * 4, 0x00020000), gi * 16, 0, 16);
        if (t < 24) __hip_atomic_store(stats_out + (t >> 3) * S_ + m0 + tni * 8 + (t & 7), 0.0, __ATOMIC_RELAXED, __HIP_MEMORY_SCOPE_AGENT);
      }
      tick();
      if (wat == 1) ld_w();
      if (X2AT == 1) ld_x(1);
      tick();
      if (wat == 2) ld_w();
      if (WCAT == 2) wcopy();
      if (X2AT == 2) ld_x(1);
      asm volatile("" :: "v"(ep));
      tick();
      if (wat == 3) ld_w();
      if (WCAT == 3) wcopy();
      if (X2AT == 3) ld_x(1);
      stouch(3);
#pragma unroll
      for (int i = 0; i < HW; ++i) { tick(); stouch(4 + i); }
      __builtin_amdgcn_s_setprio(HP1);
      PIN();
      tag = ep + 1u;
      const __amdgpu_buffer_rsrc_t rsc = __builtin_amdgcn_make_buffer_rsrc(gran, 0, GR_N * 4, 0x00020000);
      {
        u32x4 od;
#pragma unroll
        for (int q = 0; q < 4; ++q) od[q] = ((lane & 7) >> 1) == q ? ((lane & 1) ? 0x3f800000u : 0x00003f80u) : 0u;
        f32x4 d[2] = {{0.f, 0.f, 0.f, 0.f}, {0.f, 0.f, 0.f, 0.f}};
#pragma unroll
        for (int r = 0; r < 8; ++r) d[r & 1] = __builtin_amdgcn_mfma_f32_16x16x32_bf16(__builtin_bit_cast(s16x8, od), __builtin_bit_cast(s16x8, wv[r]), d[r & 1], 0, 0, 0);
        const float c0 = dpp_add<0x128, 0xf>(lane >= 32 ? d[0][2] + d[1][2] : d[0][0] + d[1][0]), c1 = dpp_add<0x128, 0xf>(lane >= 32 ? d[0][3] + d[1][3] : d[0][1] + d[1][1]);
        const int col = wcp * 64 + (lane & 7) * 8 + ((lane >> 4) & 1) * 4 + (lane >> 5) * 2;
        asm volatile("s_waitcnt vmcnt(0)" ::: "memory");
        __builtin_amdgcn_raw_buffer_store_b128(u32x4{__float_as_uint(c0), tag, __float_as_uint(c1), tag}, rsc,
                                               (lane & 8) || !wld ? GR_N * 4 : (GR_COL + ((wcn * 16 + tmi) * 192 + col) * 2) * 4, 0, 16);
      }
      asm volatile("" :: "v"(pc));
      tick_nw();
      stouch(6);
      const int tc = t < 192 ? t : 0;
      {
        float xv[2][16], mean[2];
#pragma unroll
        for (int r = 0; r < 2; ++r) {
          float sm = 0.f;
#pragma unroll
          for (int i = 0; i < 4; ++i) {
            xv[r][2 * i] = __uint_as_float(xa[r][0][i] << 16); xv[r][2 * i + 1] = __uint_as_float(xa[r][0][i] & 0xffff0000u);
            xv[r][8 + 2 * i] = __uint_as_float(xa[r][1][i] << 16); xv[r][8 + 2 * i + 1] = __uint_as_float(xa[r][1][i] & 0xffff0000u);
          }
#pragma unroll
          for (int i = 0; i < 16; ++i) sm += xv[r][i];
          mean[r] = wave_sum(sm) * (1.0f / 1024.0f);
          tick();
          stouch(7 + r);
        }
        float vr[2];
#pragma unroll
        for (int r = 0; r < 2; ++r) {
          float v = 0.f;
#pragma unroll
          for (int i = 0; i < 16; ++i) { const float d = xv[r][i] - mean[r]; v += d * d; }
          vr[r] = wave_sum(v) * (1.0f / 1024.0f);
          if (r == 0) tick();
        }
        const float vv = lane & 2 ? vr[1] : vr[0], mm = lane & 2 ? mean[1] : mean[0];
        const float rin = 1.0f / rbf(sqrtf(rbf(rbf(vv) + 1e-5f)));
        if (lane < 4) __builtin_amdgcn_raw_buffer_store_b64(u32x2{__float_as_uint((lane & 1) ? rbf(mm) : rin), tag}, rsc, (GR_ROW + srow * 4 + lane * 2) * 4, 0, 16);
      }
      tick();
      while (nb < POLLAT) tick();
      asm volatile("" :: "v"(ps));
#pragma unroll 1
      for (int spin = 0;; ++spin) {
        u32x2 csv[16];
        unsigned lo = tag, hi = tag;
        auto use = [&](int i0, int i1) __attribute__((always_inline)) {
#pragma unroll
          for (int i = i0; i < i1; i += 2) {
            lo = min(min(lo, csv[i][1]), csv[i + 1][1]); hi = max(max(hi, csv[i][1]), csv[i + 1][1]);
            cs += __uint_as_float(csv[i][0]); cs += __uint_as_float(csv[i + 1][0]);
          }
          asm volatile("" : "+v"(lo), "+v"(hi), "+v"(cs));
          PIN();
        };
        cs = 0.f;
#pragma unroll
        for (int i = 0; i < 16; ++i) {
          csv[i] = __builtin_amdgcn_raw_buffer_load_b64(rsc, (GR_COL + (tni * 3072 + tc) * 2) * 4 + i * 1536, 0, 16);
          if (i == PG1 - 1 || i == PG2 - 1) tick();
        }
        const u32x4 rp = __builtin_amdgcn_raw_buffer_load_b128(rsc, (GR_ROW + (m0 + (t & 127)) * 4) * 4, 0, 16);
        PIN();
        use(0, PG1);
        tick();
        use(PG1, PG2);
        tick();
        use(PG2, 16);
        const unsigned bad = (min(min(lo, rp[1]), rp[3]) ^ tag) | (max(max(hi, rp[1]), rp[3]) ^ tag);
        alpha = __uint_as_float(rp[0]); m = __uint_as_float(rp[2]);
        if (none(bad != 0u) || spin >= SPIN_MAX) break;
      }
      if (tmi == 0 && t < 192) st_ag(reinterpret_cast<unsigned*>(csf) + (t >> 6) * 1024 + n0 + (t & 63), __float_as_uint(cs));
      best = rmd = 0u;
      wsum = 0.f;
      asm volatile("" : "=v"(xq), KEPT("="));
#pragma unroll
      for (int i = 0; i < 8; ++i) asm volatile("" : "=v"(sl[i]));
    } else {
      asm volatile("; kept registers" : KEPT("="));
      PIN();
      ARGS_HERE;
      const int lane = fresh_lane(), t = hw * 64 + lane;
      best = rmd = 0u;
      tag = __builtin_amdgcn_readfirstlane(*LDS_AT(const volatile unsigned, lbase + FLG + 20));
#pragma unroll
      for (int i = 0; i < 8; ++i) asm volatile("" : "=v"(sl[i]));
      unsigned sad;
      auto tickc = [&]() __attribute__((always_inline)) {
        tick();
        if (nb == kcap + 1) {
#pragma unroll
          for (int i = 0; i < 8; ++i) {
            const unsigned a = (sad ^ ((i >> 2) * 64) ^ (((i >> 1) & 1) * 16)) + (i & 1) * 12288;
            sl[i] = __builtin_shufflevector(__builtin_amdgcn_ds_read_tr16_b64_v4i16(LDS_AT(s16x4, a)), __builtin_amdgcn_ds_read_tr16_b64_v4i16(LDS_AT(s16x4, a + 1536)), 0, 1, 2, 3, 4, 5, 6, 7);
          }
        }
        PIN();
      };
      static_assert(CAP0 + 8 <= KPOLL, "the stage is taken before that");
      if (hw == 1) STAMP(4, 4);
      if (hw == 0) {
        __builtin_amdgcn_s_sleep(TPRE);
        tile_at(3u);
        __builtin_amdgcn_s_sleep(XGATE);
        STAMP(4, 3);
#pragma unroll 1
        for (int spin = 0; spin < SPIN_MAX; ++spin) {
          if (none(ld_ag(gran + GR_X + (tmi * RX + tni % RX) * 128 + 64 + lane) != tag)) break;
          __builtin_amdgcn_s_sleep(1);
        }
      }
      if (hw == 0) STAMP(4, 5);
      asm volatile("" ::: "memory");
      PIN();
      __builtin_amdgcn_s_barrier();
      PIN();
      if (hw == 1) STAMP(4, 6);
      {
        const int ln = fresh_lane(), g4 = ln >> 4, tp = ln & 3, k = 8 * g4 + ((ln & 15) >> 2);
        sad = lbase + (kcap % NST) * STB + ABYTES + k * 384 + (((2 * (tp >> 1)) ^ (4 * ((k >> 1) & 1)) ^ (g4 & 1)) << 4) + 8 * (tp & 1) + (hw % 3) * 128;
      }
      wsum = __uint_as_float(__builtin_amdgcn_readfirstlane(*LDS_AT(const volatile unsigned, lbase + FLG + 60)));
      asm volatile("" : "+v"(sad));
      PIN();
#pragma unroll 1
      for (int spin = 0; spin < SPIN_MAX; ++spin) {
        const unsigned v = ld_ag(gran + GR_X + (tmi * RX + tni % RX) * 128 + lane);
        tickc();
        if (none(v != tag)) break;
      }
      asm volatile("" ::: "memory");
      PIN();
      const int row = m0 + (t & 127);
      const double s1 = ld_ag(stats + row), s2 = ld_ag(stats + S_ + row), sw = ld_ag(stats + 2 * S_ + row);
      cs = __uint_as_float(ld_ag(reinterpret_cast<const unsigned*>(csf) + (t < 192 ? (t >> 6) : 0) * 1024 + n0 + (t & 63)));
      tickc();
      float mean, var, mb, den;
      ln_from_sums(s1, s2, mean, var, mb, den);
      const float rin = frcp(den), g1 = 1.0f + rin;
      const float mb2 = rbf(mean + (mean - mb) * rin);
      const float den2 = rbf(__builtin_amdgcn_sqrtf(rbf(rbf(var * g1 * g1) + 1e-5f)));
      alpha = g1 * frcp(den2); m = (mb * rin + mb2) * frcp(g1);
      const __amdgpu_buffer_rsrc_t rsg = __builtin_amdgcn_make_buffer_rsrc(hand, 0, GRAN_N * 4, 0x00020000);
      if (tmi == tni && hw >= 2) {
        const float r = (float)fma(-(double)mb, (double)wsum, sw) * rin;
        const unsigned rb = pack2(r, r);
        unsigned rkey = (rb ^ ((unsigned)((int)rb >> 31) | 0x80000000u)) & 0xffff0000u;
        if (r != r) rkey = 0u;
        rkey = wave_umax(rkey | (unsigned)(2047 - row));
        const unsigned md = __builtin_amdgcn_readlane(f2word(__uint_as_float((__float_as_uint(mb) >> 16) | (__float_as_uint(den) & 0xffff0000u))), (2047 - (int)(rkey & 0xffffu)) & 63);
        __builtin_amdgcn_raw_buffer_store_b64(u32x2{rkey, md}, rsg, lane < NREP ? lane * 256 + (tmi * 2 + hw - 2) * 8 : GRAN_N * 4, 0, 16);
      }
      while (nb < KPOLL) tickc();
#pragma unroll 1
      for (int spin = 0; spin < SPIN_MAX; ++spin) {
        const u32x2 v = __builtin_amdgcn_raw_buffer_load_b64(rsg, (bid % NREP) * 256 + (lane & 31) * 8, 0, 16);
        tick(); tick();
        if (none(umax(v[0], v[1]) == SENT)) {
          best = wave_umax(v[0]);
          rmd = __builtin_amdgcn_readlane(v[1], ((2047 - (int)(best & 0xffffu)) >> 6) & 31);
          break;
        }
      }
      const int ix = (2047 - (int)(best & 0xffffu)) & 2047;
      if (row == ix) alpha = 0.f;
      xq = __builtin_amdgcn_raw_buffer_load_b64(__builtin_amdgcn_make_buffer_rsrc(const_cast<u16*>(A), 0, S_ * D_ * 2, 0x00020000), (ix * D_ + hw * 256 + (lane & 3) * 64 + (lane >> 2) * 4) * 2, 0, 16);
      PIN();
    }
    tag = __builtin_amdgcn_readfirstlane(tag);
    wsum = __uint_as_float(__builtin_amdgcn_readfirstlane(__float_as_uint(wsum)));
    const int lane = fresh_lane(), t = hw * 64 + lane;
    {
      unsigned c0, c1, c2, a0, a1, a2;
      split3(cs, c0, c1, c2);
      split3(-m, a0, a1, a2);
      u32x4 tw = {c0 | (c1 << 16), c0 | (c2 << 16), c1 | (c0 << 16), 0u}, ta = {a0 | (a0 << 16), a1 | (a0 << 16), a1 | (a2 << 16), 0u};
      asm volatile("" : "+v"(tw), "+v"(ta), "+v"(alpha));
      __builtin_amdgcn_s_setprio(TPRIO);
      if (nb < lim) {
        while (nb < lim) tick();
        __builtin_amdgcn_s_sleep(TSLP);
      }
      if (t < 192) *LDS_AT(u32x4, lbase + TABW + t * 16) = tw;
      if (t < 128) {
        *LDS_AT(u32x4, lbase + TABA + t * 16) = ta;
        *LDS_AT(float, lbase + ALPHA + t * 4) = alpha;
      }
    }
    if (t >= 192 && t < 196) *LDS_AT(u32x4, lbase + CRED + ((t >> 1) & 1) * 512 + (t & 1) * 128) = u32x4{0u, 0u, 0u, 0u};
    asm volatile("s_waitcnt lgkmcnt(0)" ::: "memory");
    PIN();
    __builtin_amdgcn_s_barrier();
    PIN();
    *LDS_AT(u32x4, lbase + XML + t * 16) = u32x4{SENT, SENT, SENT, SENT};
    *LDS_AT(u32x4, lbase + SPM + (t & 127) * 16) = u32x4{SENT, SENT, SENT, SENT};
    if (hw == 0) {
      unsigned v;
      asm volatile("v_mov_b32 %0, 0\n\tv_writelane_b32 %0, 2, 0\n\tv_writelane_b32 %0, %1, 5\n\tv_writelane_b32 %0, %2, 7"
                   : "=&v"(v) : "s"(tag), "s"(best));
      lds_wr(lbase + FLG + lane * 4, v);
    }
    const int idx = LAY2 ? (2047 - (int)(best & 0xffffu)) & 2047 : -64;
    const bool p1blk = (oni & 7) != 7, fast = tmi < 8, own = fast && (loc & 1);
    const int fg = xcd * 28 + (p1blk ? loc - (loc >> 3) : 0), ow = (xcd & 3) * 16 + (loc >> 1);
    static_assert(OMX == 4, "oni % 8 = loc % 8");
#define GLD(a, b) "global_load_dwordx4 v[" #a ":" #b "], %0, off\n\tv_lshl_add_u64 %0, %0, 0, %1\n\t"
#define GLD4(...) asm volatile(__VA_ARGS__ : "+v"(p) : "s"((unsigned long long)(F_ * 2)), KEPT() : "memory")
    auto gptr = [&](const u16* wg, const u16* wu) {
      const int ln = fresh_lane();
      return ((ln & 2) ? wu : wg) + (size_t)(hw * 256 + (ln >> 2) * 4) * F_ + fg * 16 + (ln & 1) * 8;
    };
    auto gather = [&](const u16*& p, int e) __attribute__((always_inline)) {
      if (e == 0) GLD4(GLD(104, 107) GLD(108, 111) GLD(112, 115) GLD(116, 119));
      else if (e == 1) GLD4(GLD(120, 123) GLD(124, 127) GLD(128, 131) GLD(132, 135));
      else if (e == 2) GLD4(GLD(136, 139) GLD(140, 143) GLD(144, 147) GLD(148, 151));
      else GLD4(GLD(152, 155) GLD(156, 159) GLD(160, 163) GLD(164, 167));
      p += 60 * F_;
    };
    auto dmask = [&](int q) { const int ln = fresh_lane(); return ((ln >> 1) & 1) == q ? ((ln & 1) ? 0xffff0000u : 0x0000ffffu) : 0u; };
    unsigned dm0, dm1, dad;
    auto gu_setup = [&]() __attribute__((always_inline)) {
      const int ln = fresh_lane();
      dm0 = dmask(0); dm1 = dmask(1);
      dad = lbase + SACT + ((ln & 3) * 8 + (ln >> 5) * 4 + ((ln >> 4) & 1)) * 16 + hw * 4;
      asm volatile("" : "+v"(dm0), "+v"(dm1), "+v"(dad));
    };
    auto gate_up = [&](u32x2 x, unsigned md) __attribute__((always_inline)) {
      const unsigned rin = __builtin_amdgcn_readfirstlane(__float_as_uint(frcp(__uint_as_float(md & 0xffff0000u))));
      asm volatile("s_getpc_b64 s[90:91]\n\ts_add_u32 s90, s90, gate_up_code@rel32@lo+4\n\ts_addc_u32 s91, s91, gate_up_code@rel32@hi+12\n\ts_swappc_b64 s[92:93], s[90:91]"
                   : "+{v[100:101]}"(x) : "{s84}"(md << 16), "{s85}"(md & 0xffff0000u), "{s86}"(rin), "{v99}"(dad), "{v102}"(dm0), "{v103}"(dm1), KEPT()
                   : "v84", "v85", "v86", "v87", "v88", "v89", "v90", "v91", "v92", "v93", "v94", "v95", "v96", "v97", "s88", "s89", "s90", "s91", "s92", "s93", "scc", "memory");
    };
    auto act_diag = [&](int so = 0) __attribute__((always_inline)) {
      const int ln = fresh_lane();
      const f32x4 g4 = *LDS_AT(const volatile f32x4, lbase + SACT + so + (ln >> 2) * 16), u4 = *LDS_AT(const volatile f32x4, lbase + SACT + so + 256 + (ln >> 2) * 16);
      float gv = fadd(fadd(g4[0], g4[1]), fadd(g4[2], g4[3])), uv = fadd(fadd(u4[0], u4[1]), fadd(u4[2], u4[3]));
      rbf2(gv, uv);
      const float av = rbf(gv * frcp(1.0f + __expf(-gv))) * uv;
      const unsigned ap2 = pack2(av, av);
      return __builtin_bit_cast(s16x4, u32x2{ap2 & dm0, ap2 & dm1});
    };
    auto down = [&](const s16x4& a, const u32x2 (&w)[16]) __attribute__((always_inline)) {
      f32x4 d[2] = {{0.f, 0.f, 0.f, 0.f}, {0.f, 0.f, 0.f, 0.f}};
      static_for<0, 16>([&](auto fc) __attribute__((always_inline)) {
        constexpr int f = decltype(fc)::value;
        d[f & 1] = __builtin_amdgcn_mfma_f32_4x4x4bf16_1k(a, __builtin_bit_cast(s16x4, w[f]), d[f & 1], 4, f, 0);
      });
      f32x4 z;
#pragma unroll
      for (int e = 0; e < 4; ++e) { z[e] = d[0][e] + d[1][e]; asm volatile("" : "+v"(z[e])); }
      return u32x4{f2word(z[0]), f2word(z[1]), f2word(z[2]), f2word(z[3])};
    };
    auto fold16 = [&](f32x2 y0, f32x2 y1) __attribute__((always_inline)) {
      const auto q0 = __builtin_amdgcn_permlane32_swap(__float_as_uint(y0[0]), __float_as_uint(y1[0]), false, false);
      const auto q1 = __builtin_amdgcn_permlane32_swap(__float_as_uint(y0[1]), __float_as_uint(y1[1]), false, false);
      const auto q = __builtin_amdgcn_permlane16_swap(__float_as_uint(__uint_as_float(q0[0]) + __uint_as_float(q0[1])),
                                                      __float_as_uint(__uint_as_float(q1[0]) + __uint_as_float(q1[1])), false, false);
      return __uint_as_float(q[0]) + __uint_as_float(q[1]);
    };
    auto fold8 = [&](f32x2 y0, f32x2 y1) __attribute__((always_inline)) { return dpp_add<0x128, 0xf>(fold16(y0, y1)); };
    auto fold4 = [&](f32x2 y0, f32x2 y1) __attribute__((always_inline)) { return dpp_add<0x124, 0xf>(fold8(y0, y1)); };
    auto y_sum = [&](const __amdgpu_buffer_rsrc_t rs, auto nbc) __attribute__((always_inline)) {
      constexpr int NB = decltype(nbc)::value, N = 14 / NB;
      const int ln = fresh_lane(), src = (GRAN_N + (ln >> 2) * D_ + ow * 16 + (ln & 3) * 4) * 4;
      f32x2 y0 = {0.f, 0.f}, y1 = {0.f, 0.f};
#pragma unroll 1
      for (int bt = 0; bt < NB; ++bt) {
        f32x2 z0, z1;
#pragma unroll 1
        for (int spin = 0; spin < SPIN_MAX; ++spin) {
          u32x4 v[N];
#pragma unroll
          for (int i = 0; i < N; ++i) v[i] = __builtin_amdgcn_raw_buffer_load_b128(rs, src, (bt * N + i) * (16 * D_ * 4), 16);
          z0 = y0; z1 = y1;
#pragma unroll
          for (int i = 0; i < N; ++i) {
            z0 = f32x2{fadd(z0[0], __uint_as_float(v[i][0])), fadd(z0[1], __uint_as_float(v[i][1]))};
            z1 = f32x2{fadd(z1[0], __uint_as_float(v[i][2])), fadd(z1[1], __uint_as_float(v[i][3]))};
          }
          const float chk = fadd(fadd(z0[0], z0[1]), fadd(z1[0], z1[1]));
          if (none(chk != chk)) break;
          unsigned mx = 0u;
#pragma unroll
          for (int i = 0; i < N; ++i) mx = umax(umax(umax(mx, v[i][0]), v[i][1]), umax(v[i][2], v[i][3]));
          if (none(mx == SENT)) break;
          __builtin_amdgcn_s_sleep(YSLP);
          asm volatile("" ::: "memory");
        }
        y0 = z0; y1 = z1;
      }
      return fold4(y0, y1);
    };
    [&]() __attribute__((always_inline)) {
    ARGS_HERE;
    const unsigned l16 = lane * 16;
    unsigned* kvflag = gran + GR_KV + ((LAY2 ? 16 : 0) + tni) * (32 * RK);
    auto wait_sig = [&](int sh) {
#pragma unroll 1
      for (int i = 0; i < 4 * SPIN_MAX; ++i) {
        if (((__builtin_amdgcn_readfirstlane(*LDS_AT(const volatile unsigned, lbase + FLG + 32)) >> sh) & 7u) >= 4u) break;
        __builtin_amdgcn_s_sleep(SSLP);
      }
      asm volatile("" ::: "memory");
      PIN();
    };
    PIN();
    asm volatile("s_waitcnt lgkmcnt(0)" ::: "memory");
    if (!LAY2) asm volatile("s_waitcnt vmcnt(0)" ::: "memory");
    __builtin_amdgcn_s_barrier();
    PIN();
    __builtin_amdgcn_s_setprio(HPRIO);
    if (hw < 3) {
      __builtin_amdgcn_s_sleep(SPRE);
      wait_sig(0);
      __builtin_amdgcn_raw_buffer_store_b32(tag, __builtin_amdgcn_make_buffer_rsrc(kvflag, 0, hw == 0 ? RK * 128 : 0, 0x00020000), lane * 128 + tmi * 4, 0, 16);
      if (hw == 0) STAMP(LAY2 ? 5 : 1, 16);
    }
    if (hw == 3) {
      __builtin_amdgcn_s_sleep(KVS);
      const unsigned kv0 = ld_ag(kvflag + (tmi % RK) * 32 + (lane & 15));
      PIN();
      const char* cnt = reinterpret_cast<const char*>(gran + GR_N + bid * 64 + 2);
      unsigned vk[8], vp = lbase + FLG + 8 + (lane & 7), pw = 0u;
#pragma unroll
      for (int j = 0; j < 8; ++j) { vk[j] = l16 + (j >> 2) * 131072u + ((j >> 1) & 1) * 4096u + (j & 1) * (VOFF - 2048u); asm volatile("" : "+v"(vk[j])); }
#define G4(a, b, c, d, sc) "s_nop 0\n\tglobal_load_lds_dwordx4 %" #a ", %8" sc "\n\tglobal_load_lds_dwordx4 %" #b ", %8 offset:1024" sc "\n\t"             \
                       "global_load_lds_dwordx4 %" #c ", %8 offset:2048" sc "\n\tglobal_load_lds_dwordx4 %" #d ", %8 offset:3072" sc "\n\t"
#define M4 "s_addk_i32 m0, 0x1000\n\t"
      auto stage = [&](const char* sp, unsigned dst) __attribute__((always_inline)) {
        asm volatile("s_mov_b32 m0, %9\n\t" G4(0, 0, 1, 1, " sc1") M4 G4(2, 2, 3, 3, " sc1")
                     M4 G4(4, 4, 5, 5, " sc1") M4 G4(6, 6, 7, 7, " sc1")
                     :: "v"(vk[0]), "v"(vk[1]), "v"(vk[2]), "v"(vk[3]), "v"(vk[4]), "v"(vk[5]), "v"(vk[6]), "v"(vk[7]), "s"(sp), "s"(dst) : "m0", "scc", "memory");
      };
      auto count = [&]() __attribute__((always_inline)) {
        asm volatile("s_mov_b32 m0, %2\n\ts_nop 0\n\tglobal_load_lds_dword %0, %1 sc1" :: "v"(0u), "s"(cnt), "s"(lbase + FLG - 252) : "m0", "memory");
        cnt += 8;
      };
      auto freed = [&](unsigned n) __attribute__((always_inline)) {
        if (!none(pw < n)) {
#pragma unroll 1
          for (int i = 0; i < 4 * SPIN_MAX; ++i) {
            pw = *LDS_AT(const volatile unsigned char, vp);
            if (none(pw < n)) break;
            __builtin_amdgcn_s_sleep(W3SLP);
          }
        }
        asm volatile("" ::: "memory");
      };
      unsigned long long ord = (0xfedcba9876543210ull >> 4 & ~((1ull << 4 * (tmi & 7)) - 1ull)) | (0xfedcba9876543210ull & ((1ull << 4 * (tmi & 7)) - 1ull));
      ord ^= (tmi & 8) * 0x1111111111111111ull;
      unsigned seen;
      const char* kvb = reinterpret_cast<const char*>(KV) + (size_t)tni * (128 * 2048);
      auto panel = [&]() __attribute__((always_inline)) {
        const unsigned pn = (unsigned)ord & 15u;
        ord >>= 4;
        if (__builtin_expect(!((seen >> pn) & 1u), 0)) {
#pragma unroll 1
          for (int i = 0; i < SPIN_MAX && !((seen >> pn) & 1u); ++i) seen = (unsigned)__ballot(ld_ag(kvflag + (tmi % RK) * 32 + (lane & 15)) == tag);
        }
        asm volatile("" ::: "memory");
        return kvb + pn * 8192u;
      };
      asm volatile("" : "+s"(ord), "+s"(kvb), "+s"(cnt));
      asm volatile("" : "+v"(vp), "+v"(pw));
      PIN();
      if (!PFIRST) wait_sig(16);
      seen = (unsigned)__ballot(kv0 == tag);
      const char* sp = panel();
      if (PFIRST) wait_sig(16);
      stage(sp, lbase + 2 * ASB);
      count();
      const int ksp = LAY2 ? PNS / 2 : -1, ksg = LAY2 ? 64 : GF0;
      const u16* gp = gptr(wg, wu);
#pragma unroll 1
      for (int k = 1; k < 15; ++k) {
        if (__builtin_expect(k == ksp, 0)) {
          gu_setup();
          gate_up(xq, rmd);
          PIN();
          *LDS_AT(volatile unsigned char, lbase + FLG + 7) = (unsigned char)0x7f;
        }
        freed((unsigned)(0xba9876543211000ull >> 4 * k) & 15u);
        stage(sp + 2048, lbase + ((2 * k + 1) & 7) * ASB);
        sp = panel();
        stage(sp, lbase + ((2 * k + 2) & 7) * ASB);
        count();
        if (__builtin_expect((unsigned)(k - ksg) < 4u, 0)) gather(gp, k - ksg);
      }
      freed(12u);
      glds16s<true>(csf + 4096 + tni * 128, l16, lbase + UGL);
      stage(sp + 2048, lbase + 7 * ASB);
      count();
      const unsigned vwo = (unsigned)((lane >> 3) * D_ + oni * 64 + ((lane & 7) ^ ((lane >> 3) & 6)) * 8) * 2u;
      constexpr unsigned vs = 8u * D_ * 2u - 1024u;
#pragma unroll 1
      for (int ns = 0; ns < 6; ++ns) {
        freed((unsigned)(ns + 25) >> 1);
        const char* wp = reinterpret_cast<const char*>(wo) + (size_t)ns * (128 * D_ * 2);
#pragma unroll
        for (int j = 0; j < 4; ++j)
          glds16x4<false>(wp + j * (32u * D_ * 2u), vwo, vwo + vs, vwo + 2 * vs, vwo + 3 * vs, lbase + ns * ASB + j * 4096);
      }
      asm volatile("s_waitcnt vmcnt(0)" ::: "memory");
      return;
    }
    if (!LAY2) {
      auto until = [&](unsigned n) {
        while ((__builtin_amdgcn_readfirstlane(*LDS_AT(const volatile unsigned, lbase + FLG + 8)) & 255u) < n) __builtin_amdgcn_s_sleep(16);
        asm volatile("" ::: "memory");
        PIN();
      };
      if (hw == 0) {
        const __amdgpu_buffer_rsrc_t rsx = __builtin_amdgcn_make_buffer_rsrc((ap_ + 1)->xo, 0, S_ * D_ * 2, 0x00020000);
        until(IAT);
#pragma unroll 1
        for (int i = 0; i < 16; ++i) {
          __builtin_amdgcn_raw_buffer_store_b128(u32x4{SENT, SENT, SENT, SENT}, rsx, ((omi * 128 + i * 8 + (lane >> 3)) * D_ + oni * 64 + (lane & 7) * 8) * 2, 0, 16);
          __builtin_amdgcn_s_sleep(ISLP);
        }
      } else if (hw == 2) {
        const __amdgpu_buffer_rsrc_t rsi = __builtin_amdgcn_make_buffer_rsrc(hand + GRAN_N, 0, (2 * HAND_N + PS_N - GRAN_N) * 4, 0x00020000);
        until(IAT);
#pragma unroll 1
        for (int i = 0; i < ((2 * HAND_N + PS_N - GRAN_N) / 4 + 16383) / 16384; ++i) {
          __builtin_amdgcn_raw_buffer_store_b128(u32x4{SENT, SENT, SENT, SENT}, rsi, ((i * 256 + bid) * 64 + lane) * 16, 0, 16);
          __builtin_amdgcn_s_sleep(ISLP);
        }
      }
      const int ng = loc & 15, fc = xcd * 2 + (loc >> 4);
      const size_t og = (size_t)((fg - xcd * 28) / 7 * 256 + lane) * F_ + (xcd * 7 + fg % 7) * 64;
      const u16* pd = wd + (size_t)(fc * 224 + (lane < 56 ? lane : 0)) * D_ + ng * 64;
      const u16* p = gptr(wg, wu);
#pragma unroll 1
      for (unsigned n = T0; n < T0 + 13u; ++n) {
        until(n);
        if (hw == 1) {
          const unsigned i = n - T0;
          const u16* q = i < 8u ? ((i & 1u) ? wu : wg) + og + (size_t)(i >> 1) * (64 * F_) : pd + (size_t)((i - 8u - (i > TW)) & 3u) * (56 * D_);
          if (i == TW) {
            q = wo + (size_t)(768 + (loc / (32 / OMX)) * (256 / OMX) + (lane & (256 / OMX - 1) & 63)) * D_ + oni * 64;
            unsigned long long a0, a1;
            CODE_ADDR("layers_kernel", a0); CODE_ADDR("end_marker_kernel", a1);
            if (loc < 8) { const unsigned p1 = touch_code(a0, a1, 3 + loc, lane); asm volatile("" :: "v"(p1)); }
          }
          const unsigned p0 = touch(q);
          asm volatile("" :: "v"(p0));
        }
        if (n >= G0 && (n - G0) % GS == 0u && (n - G0) / GS < 4u) gather(p, (n - G0) / GS);
        if (n == UAT && hw == UHW) {
          u32x4 a[4][2], c[2];
#pragma unroll
          for (int i = 0; i < 4; ++i)
#pragma unroll
            for (int h = 0; h < 2; ++h) a[i][h] = *reinterpret_cast<const u32x4*>(wo + (size_t)(tni * 64 + tmi * 4 + i) * D_ + h * 512 + lane * 8);
#pragma unroll
          for (int h = 0; h < 2; ++h) c[h] = *reinterpret_cast<const u32x4*>(w_r + h * 512 + lane * 8);
          auto dot = [&](unsigned x, unsigned y, float z) { return __builtin_amdgcn_fdot2_f32_bf16(__builtin_bit_cast(bf16x2, x), __builtin_bit_cast(bf16x2, y), z, false); };
          PIN();
          __builtin_amdgcn_s_setprio(0);
#pragma unroll 1
          for (int i = 0; i < SPIN_MAX; ++i) {
            if (__builtin_amdgcn_readfirstlane(*LDS_AT(const volatile unsigned, lbase + FLG + 16)) >= UARR) break;
            __builtin_amdgcn_s_sleep(4);
          }
          PIN();
          float wm = 0.f;
#pragma unroll
          for (int e = 0; e < 8; ++e) wm = dot(c[e >> 2][e & 3], 0x3f803f80u, wm);
          wm = wave_sum(wm);
          lds_wr(lbase + FLG + 60, __float_as_uint(wm));
          wm *= -1.0f / 1024.0f;
          f32x2 u[2], g[2];
#pragma unroll
          for (int i = 0; i < 4; ++i) {
            float p1 = 0.f, pw = 0.f, p2 = 0.f;
#pragma unroll
            for (int e = 0; e < 8; ++e) {
              const unsigned x = a[i][e >> 2][e & 3];
              p1 = dot(x, 0x3f803f80u, p1); pw = dot(x, c[e >> 2][e & 3], pw); p2 = dot(x, x, p2);
            }
            u[i >> 1][i & 1] = fmaf(p1, wm, pw); g[i >> 1][i & 1] = p2;
          }
          float us = fold4(u[0], u[1]), gs = fold4(g[0], g[1]);
          us = dpp_add<0x121, 0xf>(dpp_add<0x122, 0xf>(us)); gs = dpp_add<0x121, 0xf>(dpp_add<0x122, 0xf>(gs));
          __builtin_amdgcn_raw_buffer_store_b32(__float_as_uint((lane & 1) ? __builtin_amdgcn_sqrtf(gs) * (1.0f / 32.0f) : us),
              __builtin_amdgcn_make_buffer_rsrc(csf + 4096 + tni * 128, 0, 512, 0x00020000), (lane & 14) ? 512 : ((lane & 1) * 64 + tmi * 4 + (lane >> 4)) * 4, 0, 16);
        }
      }
      if (hw == 2 && bid == 0) {
        const __amdgpu_buffer_rsrc_t rsc = __builtin_amdgcn_make_buffer_rsrc(gran, 0, GR_N * 4, 0x00020000);
#pragma unroll 1
        for (int spin = 0; spin < SPIN_MAX; ++spin) {
          bool ok = true;
#pragma unroll
          for (int i = 0; i < 4; ++i) ok &= __builtin_amdgcn_raw_buffer_load_b32(rsc, (GR_ROW + (i * 64 + lane) * 32 + 1) * 4, 0, 16) == tag;
          if (__all(ok)) break;
          __builtin_amdgcn_s_sleep(8);
          asm volatile("" ::: "memory");
        }
        if (lane == 0) st_ag(gran, tag);
      }
      return;
    }
    unsigned* X2 = hand + GRAN_N + Y_N;
    unsigned* XS = X2 + X2_N;
    unsigned* P4 = XS + XS_N;
    unsigned* PA = P4 + P4_N;
    const __amdgpu_buffer_rsrc_t rsh = __builtin_amdgcn_make_buffer_rsrc(hand, 0, HAND_N * 4, 0x00020000);
    auto signal = [&](unsigned ph) {
      asm volatile("" ::: "memory");
      PIN();
      *LDS_AT(volatile unsigned char, lbase + FLG + 4 + hw) = (unsigned char)ph;
    };
    auto wait_all = [&](unsigned ph) {
#pragma unroll 1
      for (int i = 0; i < SPIN_MAX; ++i) {
        if (all_ge(__builtin_amdgcn_readfirstlane(*LDS_AT(const volatile unsigned, lbase + FLG + 4)), ph)) break;
        __builtin_amdgcn_s_sleep(WSLP);
      }
      asm volatile("" ::: "memory");
      PIN();
    };
    gu_setup();
    gate_up(xq, rmd);
    signal(hw == 2 ? 2u : 0x7fu);
    if (wid == 8) STAMP(dk, 2);
    if (hw < 2) {
#pragma unroll 1
      for (int i = 0; i < SPIN_MAX; ++i) {
        if (__builtin_amdgcn_readfirstlane(*LDS_AT(const volatile unsigned, lbase + FLG)) >= FSTART) break;
        __builtin_amdgcn_s_sleep(WSLP);
      }
      asm volatile("" ::: "memory");
      PIN();
      if (wid == 8) STAMP(dk, 10);
      u32x2 w[16];
      const __amdgpu_buffer_rsrc_t rd_ = __builtin_amdgcn_make_buffer_rsrc(const_cast<u16*>(wd), 0, D_ * F_ * 2, 0x00020000);
      auto ldw = [&](int c) __attribute__((always_inline)) {
#pragma unroll
        for (int f = 0; f < 16; ++f) w[f] = __builtin_amdgcn_raw_buffer_load_b64(rd_, lane * 8 + c * 512 + (f & 1) * 2048, (fg * 16 + (f & ~1)) * (D_ * 2) + hw * 1024, 0);
        PIN();
      };
      ldw(0);
      const unsigned pt = __builtin_amdgcn_raw_buffer_load_b32(rd_, (lane >> 2) * (D_ * 2) + (lane & 3) * 128 + 512, fg * 16 * (D_ * 2) + hw * 1024, 0);
      int yo = p1blk ? (GRAN_N + fg * D_ + hw * 512) * 4 + lane * 16 : HAND_N * 4;
      asm volatile("" : "+v"(yo));
      wait_all(2u);
      if (wid == 8) STAMP(dk, 3);
      const s16x4 a = act_diag();
      const u32x4 y0 = down(a, w);
      PIN();
      ldw(1);
      __builtin_amdgcn_raw_buffer_store_b128(y0, rsh, yo, 0, 16);
      PIN();
      __builtin_amdgcn_raw_buffer_store_b128(down(a, w), rsh, yo + 1024, 0, 16);
      asm volatile("" :: "v"(pt));
      if (wid == 8) STAMP(dk, 4);
    }
    PIN();
    [&]() __attribute__((always_inline)) {
    const int lane = fresh_lane();
    const float rmb = __uint_as_float(rmd << 16);
    auto first_words = [&](int src, auto nc) __attribute__((always_inline)) {
#pragma unroll 1
      for (int spin = 0; spin < SPIN_MAX; ++spin) {
        unsigned mx = 0u;
#pragma unroll
        for (int i = 0; i < decltype(nc)::value; ++i) mx = umax(mx, __builtin_amdgcn_raw_buffer_load_b32(rsh, src, i * (56 * D_ * 4), 16));
        if (none(mx == SENT)) break;
        __builtin_amdgcn_s_sleep(PSLP);
        asm volatile("" ::: "memory");
      }
    };
    if (hw == 2) {
      wait_all(2u);
      if (own) {
        const float rden = __uint_as_float(rmd & 0xffff0000u), rrin = frcp(rden);
        const int col = ow * 16 + (lane & 3) * 4 + (lane >> 4);
        const unsigned xo2 = ld_ag(reinterpret_cast<const unsigned*>(A + (size_t)idx * D_ + (col & ~1)));
        __builtin_amdgcn_s_sleep(PRE1);
        if (LPOLL & 2) first_words((GRAN_N + (lane < 56 ? lane : 0) * D_ + ow * 16) * 4, std::integral_constant<int, 4>{});
        const float y = y_sum(rsh, std::integral_constant<int, 2>{});
        const unsigned rk = best >> 16;
        const float rv = __uint_as_float(((rk & 0x8000u) ? (rk & 0x7fffu) : (~rk & 0xffffu)) << 16);
        const float rs = rbf(frcp(1.0f + __expf(-rv)));
        const float x1v = __uint_as_float((col & 1) ? xo2 & 0xffff0000u : xo2 << 16);
        float h0 = x1v, h1 = x1v;
        ln_apply2(h0, h1, rmb, rden, rrin);
        const float x2v = rbf(x1v + rbf(h0 + rbf(rbf(y) * rs)));
        const auto pr = __builtin_amdgcn_permlane16_swap(__float_as_uint(x2v), __float_as_uint(x2v), false, false);
        __builtin_amdgcn_raw_buffer_store_b32(f2word(__uint_as_float((pr[0] >> 16) | pr[1])), rsh, (lane & 28) ? HAND_N * 4 : (int)(X2 - hand) * 4 + col * 2, 0, 16);
        const float d = x2v - rmb;
        const float sd = wave_sum(d), sq = wave_sum(d * d);
        if (lane < 2) st_ag(XS + ow * 2 + lane, f2word(lane ? sq : sd));
        STAMP(dk, 5);
      }
    }
    {
      const int ks = kcap ^ (tmi & 8), g4 = lane >> 4;
      const int src = (int)(X2 - hand) * 4 + ks * 128 + g4 * 16;
      int dst = (lane & 12) ? HAND_N * 4 : (int)((P4 - hand) + (tni * 16 + ks) * 192 + hw * 64 + (lane & 2) * 16 + (lane & 1) * 8 + (g4 >> 1) * 16 + (g4 & 1) * 4) * 4;
      asm volatile("" : "+v"(dst));
      u32x4 x0, x1;
      if (!(hw == 2 && own)) __builtin_amdgcn_s_sleep(PRE4);
#pragma unroll 1
      for (int spin = 0; spin < SPIN_MAX; ++spin) {
        x0 = __builtin_amdgcn_raw_buffer_load_b128(rsh, src, 0, 16); x1 = __builtin_amdgcn_raw_buffer_load_b128(rsh, src + 64, 0, 16);
        if (none(umax(umax(umax(x0[0], x0[1]), umax(x0[2], x0[3])), umax(umax(x1[0], x1[1]), umax(x1[2], x1[3]))) == SENT)) break;
        __builtin_amdgcn_s_sleep(PSLP);
        asm volatile("" ::: "memory");
      }
      f32x4 d[4];
#pragma unroll
      for (int i = 0; i < 4; ++i) d[i] = MFMA16(sl[2 * i], __builtin_bit_cast(s16x8, x0), (f32x4{0.f, 0.f, 0.f, 0.f}));
#pragma unroll
      for (int i = 0; i < 4; ++i) d[i] = MFMA16(sl[2 * i + 1], __builtin_bit_cast(s16x8, x1), d[i]);
      u32x4 o;
#pragma unroll
      for (int e = 0; e < 4; ++e) o[e] = f2word((lane & 2) ? ((lane & 1) ? d[3][e] : d[2][e]) : ((lane & 1) ? d[1][e] : d[0][e]));
      __builtin_amdgcn_raw_buffer_store_b128(o, rsh, dst, 0, 16);
      PIN();
      if (wid == 8) STAMP(dk, 6);
    }
    if (hw == 2) {
      const int col = (lane & 15) * 4 + (lane >> 4), src = (int)((P4 - hand) + (tni * 16 + (lane >> 4)) * 192 + (lane & 15) * 4) * 4;
      const __amdgpu_buffer_rsrc_t rsq = __builtin_amdgcn_make_buffer_rsrc(hand, 0, fast ? HAND_N * 4 : 0, 0x00020000);
      float cs[3], val[3];
#pragma unroll
      for (int p = 0; p < 3; ++p) cs[p] = __uint_as_float(ld_ag(reinterpret_cast<const unsigned*>(csf) + p * 1024 + n0 + col));
      u32x2 xs;
      __builtin_amdgcn_s_sleep(PRE7);
      if (LPOLL & 1) first_words((int)((P4 - hand) + (tni * 16 + (lane & 15)) * 192 + ((lane >> 4) % 3) * 64) * 4, std::integral_constant<int, 1>{});
#pragma unroll 1
      for (int spin = 0; spin < SPIN_MAX; ++spin) {
        u32x4 v[3][4];
        xs = __builtin_amdgcn_raw_buffer_load_b64(rsh, (int)((XS - hand) + lane * 2) * 4, 0, 16);
#pragma unroll
        for (int p = 2; p >= 0; --p)
#pragma unroll
          for (int i = 0; i < 4; ++i) v[p][i] = __builtin_amdgcn_raw_buffer_load_b128(p ? rsh : rsq, src + p * 256, i * (4 * 192 * 4), 16);
        auto part = [&](int p) __attribute__((always_inline)) {
          float z[4];
#pragma unroll
          for (int e = 0; e < 4; ++e) {
            z[e] = fadd(fadd(__uint_as_float(v[p][0][e]), __uint_as_float(v[p][1][e])), fadd(__uint_as_float(v[p][2][e]), __uint_as_float(v[p][3][e])));
          }
          val[p] = fold16(f32x2{z[0], z[1]}, f32x2{z[2], z[3]});
        };
        part(2); part(1);
        val[0] = 0.f;
        if (fast) part(0);
        const float chk = fadd(fadd(fadd(val[0], val[1]), fadd(val[2], __uint_as_float(xs[0]))), __uint_as_float(xs[1]));
        if (none(chk != chk)) break;
        unsigned mx = umax(xs[0], xs[1]);
#pragma unroll
        for (int i = 0; i < 12; ++i) mx = umax(umax(umax(mx, v[i >> 2][i & 3][0]), v[i >> 2][i & 3][1]), umax(v[i >> 2][i & 3][2], v[i >> 2][i & 3][3]));
        if (none(mx == SENT)) break;
        __builtin_amdgcn_s_sleep(PSLP);
        asm volatile("" ::: "memory");
      }
      const auto sw = __builtin_amdgcn_permlane32_swap(xs[0], xs[1], false, false);
      float sm = __uint_as_float(sw[0]) + __uint_as_float(sw[1]);
      sm = dpp_add<0x142, 0xa>(dpp_add<0x118, 0xf>(dpp_add<0x114, 0xf>(dpp_add<0x112, 0xf>(dpp_add<0x111, 0xf>(sm)))));
      const float dm = __int_as_float(__builtin_amdgcn_readlane(__float_as_int(sm), 31)) * (0.25f / 1024.0f);
      const float var = fmaxf(__int_as_float(__builtin_amdgcn_readlane(__float_as_int(sm), 63)) * (0.25f / 1024.0f) - dm * dm, 0.f);
      const float mb = rbf(rmb + dm);
      const float rin = frcp(rbf(__builtin_amdgcn_sqrtf(rbf(rbf(var) + 1e-5f))));
      const unsigned kv = pack2((val[1] - mb * cs[1]) * rin, (val[2] - mb * cs[2]) * rin);
      *LDS_AT(u16, lbase + TAB + 128 + kpos(col) * 2) = (u16)kv;
      *LDS_AT(u16, lbase + TAB + 256 + col * 2) = (u16)(kv >> 16);
      if (fast) *LDS_AT(u16, lbase + TAB + kpos(col) * 2) = f2bf((val[0] - mb * cs[0]) * rin * QSCALE);
      signal(7u);
      STAMP(dk, 7);
      if (own) {
#pragma unroll
        for (int i = 0; i < 14; ++i)
          __builtin_amdgcn_raw_buffer_store_b128(u32x4{SENT, SENT, SENT, SENT}, rsh, (GRAN_N + (i * 16 + (lane >> 2)) * D_ + ow * 16 + (lane & 3) * 4) * 4, 0, 16);
      }
      return;
    }
    if (!fast) return;
    const int head = tni * 2 + hw;
    const unsigned tb = lbase + TAB + hw * 64;
    {
      const __amdgpu_buffer_rsrc_t rs = __builtin_amdgcn_make_buffer_rsrc(KV, 0, 2 * S_ * D_ * 2, 0x00020000);
      const int src = (head * 64 + tmi * 4) * 2048 + lane * 16;
      auto ld = [&](int i, unsigned off) { return __builtin_bit_cast(s16x8, __builtin_amdgcn_raw_buffer_load_b128(rs, src + (i & 3) * 1024, (i >> 3) * 65536 + ((i >> 2) & 1) * 4096 + off, 16)); };
#pragma unroll 1
      for (int i = 0; i < SPIN_MAX; ++i)
        if (__builtin_amdgcn_readfirstlane(ld_ag(kvflag + (tmi % RK) * 32 + tmi + 8)) == tag) break;
      asm volatile("" ::: "memory");
      s16x8 f[16];
#pragma unroll
      for (int i = 0; i < 16; ++i) f[i] = ld(i, 0u);
      PIN();
      const bool on = ((lane ^ (lane >> 4)) & 1) == 0;
      const unsigned pl = lbase + RED + hw * 1024;
      unsigned pw = pl + (lane >> 4) * 64 + (lane & 15) * 2, pr = pl + (on ? (lane >> 5) * 16 : 512), qr = on ? tb + (lane >> 5) * 16 : pl + 512;
      const int gone = (((idx >> 7) & 7) == tmi) ? ((idx >> 10) * 2 + (kpos(idx & 31) >> 4)) * 64 + ((idx >> 5) & 3) * 16 + (kpos(idx & 31) & 15) : -1;
      asm volatile("" : "+v"(pw), "+v"(pr), "+v"(qr));
      *LDS_AT(u32x2, pl + 512 + lane * 8) = u32x2{0u, 0u};
      wait_all(7u);
      u32x4 q[2];
#pragma unroll
      for (int sx = 0; sx < 2; ++sx) {
        q[sx] = *LDS_AT(const u32x4, qr + sx * 32);
      }
      float sc[4];
#pragma unroll
      for (int i = 0; i < 8; ++i) {
        f32x4 d = MFMA16(__builtin_bit_cast(s16x8, q[0]), f[2 * i], (f32x4{0.f, 0.f, 0.f, 0.f}));
        d = MFMA16(__builtin_bit_cast(s16x8, q[1]), f[2 * i + 1], d);
        PIN();
        f[2 * i] = ld(2 * i, VOFF); f[2 * i + 1] = ld(2 * i + 1, VOFF);
        PIN();
#pragma unroll
        for (int r = 0; r < 2; ++r) sc[(i >> 2) * 2 + r] = (i & 3) == 0 || (lane >> 4) == (i & 3) ? d[r] : sc[(i >> 2) * 2 + r];
      }
      if (gone >= 0) {
#pragma unroll
        for (int r = 0; r < 4; ++r) sc[r] = r * 64 + lane == gone ? -INFINITY : sc[r];
      }
      const float mx = wave_fmax(fmaxf(fmaxf(sc[0], sc[1]), fmaxf(sc[2], sc[3])));
      const unsigned p0 = pack2(__builtin_amdgcn_exp2f(sc[0] - mx), __builtin_amdgcn_exp2f(sc[1] - mx)), p1 = pack2(__builtin_amdgcn_exp2f(sc[2] - mx), __builtin_amdgcn_exp2f(sc[3] - mx));
      *LDS_AT(u16, pw) = (u16)p0; *LDS_AT(u16, pw + 32) = (u16)(p0 >> 16);
      *LDS_AT(u16, pw + 256) = (u16)p1; *LDS_AT(u16, pw + 288) = (u16)(p1 >> 16);
      asm volatile("" ::: "memory");
      const float ls = wave_sum((__uint_as_float(p0 << 16) + __uint_as_float(p0 & 0xffff0000u)) + (__uint_as_float(p1 << 16) + __uint_as_float(p1 & 0xffff0000u)));
      f32x4 o = {0.f, 0.f, 0.f, 0.f};
#pragma unroll
      for (int i = 0; i < 16; ++i) o = MFMA16(*LDS_AT(const s16x8, pr + i * 32), f[i], o);
      int pv = __float_as_int((lane & 16) ? o[1] : o[0]);
      asm volatile("s_nop 0\n\tv_writelane_b32 %0, %1, 32\n\tv_writelane_b32 %0, %2, 33" : "+v"(pv) : "s"(ls), "s"(mx));
      __builtin_amdgcn_raw_buffer_store_b32(f2word(__int_as_float(pv)), rsh, lane < 34 ? (int)((PA - hand) + (head * 8 + tmi) * 64 + lane) * 4 : HAND_N * 4, 0, 16);
    }
    if (wid == 8) STAMP(dk, 8);
    if (tmi != ((idx >> 7) & 7)) return;
    {
      const int src = (int)((PA - hand) + head * 512 + (lane < 34 ? lane : 0)) * 4;
      const unsigned ta = tb + (lane & 31) * 2;
      const float so = wave_sum(lane < 32 ? bf2f(*LDS_AT(const u16, ta)) * bf2f(*LDS_AT(const u16, ta + 128)) : 0.f);
      const float vo = lane < 32 ? bf2f(*LDS_AT(const u16, ta + 256)) : 1.0f;
      unsigned v[8];
      __builtin_amdgcn_s_sleep(PRE5);
#pragma unroll 1
      for (int spin = 0; spin < SPIN_MAX; ++spin) {
        unsigned mx = 0u;
#pragma unroll
        for (int i = 0; i < 8; ++i) v[i] = __builtin_amdgcn_raw_buffer_load_b32(rsh, src + i * 256, 0, 16);
#pragma unroll
        for (int i = 0; i < 8; ++i) mx = umax(mx, v[i]);
        if (none(mx == SENT)) break;
        __builtin_amdgcn_s_sleep(PSLP);
        asm volatile("" ::: "memory");
      }
      float m8[8], mx = so;
#pragma unroll
      for (int i = 0; i < 8; ++i) { m8[i] = __int_as_float(__builtin_amdgcn_readlane((int)v[i], 33)); mx = fmaxf(mx, m8[i]); }
      float acc = __builtin_amdgcn_exp2f(so - mx) * vo;
#pragma unroll
      for (int i = 0; i < 8; ++i) acc += __builtin_amdgcn_exp2f(m8[i] - mx) * __uint_as_float(v[i]);
      const float l = __int_as_float(__builtin_amdgcn_readlane(__float_as_int(acc), 32));
      const float ov = acc / l;
      const float o1 = __int_as_float(__builtin_amdgcn_update_dpp(0, __float_as_int(ov), 0x55, 0xf, 0xf, false));
      const float o2 = __int_as_float(__builtin_amdgcn_update_dpp(0, __float_as_int(ov), 0xaa, 0xf, 0xf, false));
      const float o3 = __int_as_float(__builtin_amdgcn_update_dpp(0, __float_as_int(ov), 0xff, 0xf, 0xf, false));
      const __amdgpu_buffer_rsrc_t rso = __builtin_amdgcn_make_buffer_rsrc(Of, 0, S_ * D_ * 2, 0x00020000);
      if (lane < 32 && (lane & 3) == 0)
        __builtin_amdgcn_raw_buffer_store_b64(u32x2{pack2(ov, o1), pack2(o2, o3)}, rso,
            (((idx >> 5) * 32 + head) * 1024 + (lane >> 4) * 512 + (((lane >> 2) & 1) * 32 + (idx & 31)) * 8 + ((lane >> 3) & 1) * 4) * 2, 0, 16);
      asm volatile("s_waitcnt vmcnt(0)" ::: "memory");
      if (lane < RO) st_ag(gran + GR_O + ((16 + (idx >> 7)) * RO + lane) * 64 + 16 + head, tag);
      STAMP(dk, 9);
    }
    }();
    }();
    __builtin_amdgcn_s_setprio(0);
    asm volatile("s_waitcnt vmcnt(0) lgkmcnt(0)" ::: "memory");
    PIN();
    ARGS_HERE;
    u32x2 w[16];
    auto ld_wd = [&]() __attribute__((always_inline)) {
      const __amdgpu_buffer_rsrc_t rd_ = __builtin_amdgcn_make_buffer_rsrc(const_cast<u16*>(wd), 0, D_ * F_ * 2, 0x00020000);
#pragma unroll
      for (int f = 0; f < 16; ++f) w[f] = __builtin_amdgcn_raw_buffer_load_b64(rd_, t * 8 + (f & 1) * 2048, (fg * 16 + (f & ~1)) * (D_ * 2), 0);
      PIN();
    };
    float fnum = 0.f, fvar = 0.f;
    u32x2 wq = {0u, 0u};
    const int rrow = omi * 128 + (oni >> 3) * 64 + (hw & 1) * 32;
    if (LAY2) {
      ld_wd();
      wq = *reinterpret_cast<const u32x2*>(w_r + hw * 256 + (lane & 3) * 64 + (lane >> 2) * 4);
      if (!p1blk) {
        const int fr = rrow + (lane & 7) * 4 + (lane >> 4);
        const double s1 = ld_ag(stats + fr), s2 = ld_ag(stats + S_ + fr), sw = ld_ag(stats + 2 * S_ + fr);
        lds_wr(lbase + SPM + 132, __float_as_uint(wsum));
        float mean, var, mb, den;
        ln_from_sums(s1, s2, mean, var, mb, den);
        const float g1 = 1.0f + frcp(den);
        fnum = fr == idx ? __uint_as_float(0x7fc00000u) : fmaf(-mean, wsum, (float)sw) * g1;
        fvar = var * g1 * g1;
      }
      asm volatile("" : "+v"(fnum), "+v"(fvar));
      PIN();
    }
    if (hw == 3) {

      const int nfl = (LAY2 && (idx >> 7) == omi) ? 48 : 16;
      auto pfl = (__attribute__((address_space(1))) const unsigned*)(gran + GR_O + (((LAY2 ? 16 : 0) + omi) * RO + oni % RO) * 64 + (lane < nfl ? lane : 0));
      constexpr unsigned vs = 8u * D_ * 2u - 1024u;
      unsigned vw0 = (unsigned)((lane >> 3) * D_ + oni * 64 + ((lane & 7) ^ ((lane >> 3) & 6)) * 8) * 2u, vw1 = vw0 + vs, vw2 = vw0 + 2 * vs, vw3 = vw0 + 3 * vs;
      asm volatile("" : "+v"(pfl), "+v"(vw0), "+v"(vw1), "+v"(vw2), "+v"(vw3));
      auto wait_arr = [&](unsigned n) {
#pragma unroll 1
        for (int i = 0; i < 4 * SPIN_MAX; ++i) {
          if (__builtin_amdgcn_readfirstlane(*LDS_AT(const volatile unsigned, lbase + FLG + 16)) >= n) break;
          __builtin_amdgcn_s_sleep(1);
        }
        asm volatile("" ::: "memory");
        PIN();
      };
      wait_arr(12u);
      unsigned fl;
      asm volatile("global_load_dword %0, %1, off sc1" : "=v"(fl) : "v"(pfl) : "memory");
#pragma unroll 1
      for (int i = 0; i < 4 * SPIN_MAX; ++i) {
        if (__builtin_amdgcn_readfirstlane(*LDS_AT(const volatile unsigned, lbase + FLG + 24)) >= 8u) break;
        __builtin_amdgcn_s_sleep(1);
      }
      asm volatile("" ::: "memory");
      PIN();
#pragma unroll
      for (int j = 24; j < 32; ++j)
        glds16x4<false>(reinterpret_cast<const char*>(wo) + j * (32u * D_ * 2u), vw0, vw1, vw2, vw3, lbase + j * 4096);
      unsigned keep;
      asm volatile("s_mov_b32 %0, m0\n\ts_mov_b32 m0, %3\n\ts_nop 2\n\tglobal_load_lds_dword %1, %2 sc1\n\ts_mov_b32 m0, %0"
                   : "=&s"(keep) : "v"(0u), "s"(reinterpret_cast<const char*>(gran + GR_N + bid * 64 + 39)), "s"(lbase + FLG - 252) : "memory");
      asm volatile("s_waitcnt vmcnt(33)" : "+v"(fl) :: "memory");
      if (__builtin_amdgcn_ballot_w64(fl != tag)) {
        __builtin_amdgcn_s_sleep(CSL1);
#pragma unroll 1
        for (int spin = 0; spin < SPIN_MAX; ++spin) {
          if (!__builtin_amdgcn_ballot_w64(__hip_atomic_load(pfl, __ATOMIC_RELAXED, __HIP_MEMORY_SCOPE_AGENT) != tag)) break;
          __builtin_amdgcn_s_sleep(CSLP);
        }
      }
      asm volatile("" ::: "memory");
      PIN();
    }
    u32x4 pv[4];
    asm volatile("" : "=v"(pv[0]), "=v"(pv[1]), "=v"(pv[2]), "=v"(pv[3]));
    if (LAY2 && !p1blk && hw < 2) {
      const __amdgpu_buffer_rsrc_t rsp = __builtin_amdgcn_make_buffer_rsrc(hand + 2 * HAND_N + PP_O, 0, H_ * S_ * 4, 0x00020000);
      const int src = ((lane >> 3) * S_ + rrow + (lane & 7) * 4) * 4;
#pragma unroll 1
      for (int i = 0; i < 4 * SPIN_MAX; ++i) {
        if (__builtin_amdgcn_readfirstlane(*LDS_AT(const volatile unsigned, lbase + FLG + 24)) >= 8u) break;
        __builtin_amdgcn_s_sleep(2);
      }
#pragma unroll 1
      for (int spin = 0; spin < SPIN_MAX; ++spin) {
        asm volatile("" ::: "memory");
        unsigned mx = 0u;
#pragma unroll
        for (int i = 0; i < 4; ++i) pv[i] = __builtin_amdgcn_raw_buffer_load_b128(rsp, src, i * (8 * S_ * 4), 16);
#pragma unroll
        for (int i = 0; i < 4; ++i) mx = umax(umax(umax(mx, pv[i][0]), pv[i][1]), umax(pv[i][2], pv[i][3]));
        if (none(mx == SENT)) break;
        __builtin_amdgcn_s_sleep(PPSLP);
      }
    }
    asm volatile("s_waitcnt lgkmcnt(0)" ::: "memory");
    __builtin_amdgcn_s_barrier();
    if (!LAY2) {
      PIN();
      asm volatile(AGAIN "\ngate_up_code:\n\t"
                 "v_lshlrev_b32 v84, 16, v100\n\tv_and_b32 v85, 0xffff0000, v100\n\tv_lshlrev_b32 v86, 16, v101\n\tv_and_b32 v87, 0xffff0000, v101\n\t"
                 "v_subrev_f32 v84, s84, v84\n\tv_subrev_f32 v85, s84, v85\n\tv_subrev_f32 v86, s84, v86\n\tv_subrev_f32 v87, s84, v87\n\t"
                 "v_cvt_pk_bf16_f32 v85, v84, v85\n\tv_cvt_pk_bf16_f32 v87, v86, v87\n\ts_mov_b32 s88, 0x01000100\n\ts_mov_b32 s89, 0x03020302\n\t"
                 "v_lshlrev_b32 v84, 16, v85\n\tv_and_b32 v85, 0xffff0000, v85\n\tv_lshlrev_b32 v86, 16, v87\n\tv_and_b32 v87, 0xffff0000, v87\n\t"
                 "v_mul_f32 v88, s86, v84\n\tv_mul_f32 v89, s86, v85\n\tv_mul_f32 v90, s86, v86\n\tv_mul_f32 v91, s86, v87\n\tv_fma_f32 v84, -v88, s85, v84\n\t"
                 "v_fma_f32 v85, -v89, s85, v85\n\tv_fma_f32 v86, -v90, s85, v86\n\tv_fma_f32 v87, -v91, s85, v87\n\tv_fmac_f32 v88, s86, v84\n\t"
                 "v_fmac_f32 v89, s86, v85\n\tv_fmac_f32 v90, s86, v86\n\tv_fmac_f32 v91, s86, v87\n\tv_cvt_pk_bf16_f32 v88, v88, v89\n\t"
                 "v_cvt_pk_bf16_f32 v90, v90, v91\n\tv_perm_b32 v92, v88, v88, s88\n\tv_perm_b32 v93, v88, v88, s89\n\tv_perm_b32 v94, v90, v90, s88\n\t"
                 "v_perm_b32 v95, v90, v90, s89\n\ts_nop 0\n\tv_and_b32_dpp v96, v92, v102 quad_perm:[0,0,0,0] row_mask:0xf bank_mask:0xf\n\t"
                 "v_and_b32_dpp v97, v92, v103 quad_perm:[0,0,0,0] row_mask:0xf bank_mask:0xf\n\t"
                 "v_and_b32_dpp v100, v93, v102 quad_perm:[0,0,0,0] row_mask:0xf bank_mask:0xf\n\t"
                 "v_and_b32_dpp v101, v93, v103 quad_perm:[0,0,0,0] row_mask:0xf bank_mask:0xf\n\tv_mfma_f32_4x4x4_16b_bf16 v[84:87], v[96:97], v[104:105], 0\n\t"
                 "v_mfma_f32_4x4x4_16b_bf16 v[88:91], v[96:97], v[106:107], 0\n\tv_and_b32_dpp v96, v94, v102 quad_perm:[0,0,0,0] row_mask:0xf bank_mask:0xf\n\t"
                 "v_and_b32_dpp v97, v94, v103 quad_perm:[0,0,0,0] row_mask:0xf bank_mask:0xf\n\t"
                 "v_mfma_f32_4x4x4_16b_bf16 v[84:87], v[100:101], v[108:109], v[84:87]\n\tv_mfma_f32_4x4x4_16b_bf16 v[88:91], v[100:101], v[110:111], v[88:91]\n\t"
                 "v_and_b32_dpp v100, v95, v102 quad_perm:[0,0,0,0] row_mask:0xf bank_mask:0xf\n\t"
                 "v_and_b32_dpp v101, v95, v103 quad_perm:[0,0,0,0] row_mask:0xf bank_mask:0xf\n\tv_mfma_f32_4x4x4_16b_bf16 v[84:87], v[96:97], v[112:113], v[84:87]\n\t"
                 "v_mfma_f32_4x4x4_16b_bf16 v[88:91], v[96:97], v[114:115], v[88:91]\n\tv_and_b32_dpp v96, v92, v102 quad_perm:[1,1,1,1] row_mask:0xf bank_mask:0xf\n\t"
                 "v_and_b32_dpp v97, v92, v103 quad_perm:[1,1,1,1] row_mask:0xf bank_mask:0xf\n\t"
                 "v_mfma_f32_4x4x4_16b_bf16 v[84:87], v[100:101], v[116:117], v[84:87]\n\tv_mfma_f32_4x4x4_16b_bf16 v[88:91], v[100:101], v[118:119], v[88:91]\n\t"
                 "v_and_b32_dpp v100, v93, v102 quad_perm:[1,1,1,1] row_mask:0xf bank_mask:0xf\n\t"
                 "v_and_b32_dpp v101, v93, v103 quad_perm:[1,1,1,1] row_mask:0xf bank_mask:0xf\n\tv_mfma_f32_4x4x4_16b_bf16 v[84:87], v[96:97], v[120:121], v[84:87]\n\t"
                 "v_mfma_f32_4x4x4_16b_bf16 v[88:91], v[96:97], v[122:123], v[88:91]\n\tv_and_b32_dpp v96, v94, v102 quad_perm:[1,1,1,1] row_mask:0xf bank_mask:0xf\n\t"
                 "v_and_b32_dpp v97, v94, v103 quad_perm:[1,1,1,1] row_mask:0xf bank_mask:0xf\n\t"
                 "v_mfma_f32_4x4x4_16b_bf16 v[84:87], v[100:101], v[124:125], v[84:87]\n\tv_mfma_f32_4x4x4_16b_bf16 v[88:91], v[100:101], v[126:127], v[88:91]\n\t"
                 "v_and_b32_dpp v100, v95, v102 quad_perm:[1,1,1,1] row_mask:0xf bank_mask:0xf\n\t"
                 "v_and_b32_dpp v101, v95, v103 quad_perm:[1,1,1,1] row_mask:0xf bank_mask:0xf\n\tv_mfma_f32_4x4x4_16b_bf16 v[84:87], v[96:97], v[128:129], v[84:87]\n\t"
                 "v_mfma_f32_4x4x4_16b_bf16 v[88:91], v[96:97], v[130:131], v[88:91]\n\tv_and_b32_dpp v96, v92, v102 quad_perm:[2,2,2,2] row_mask:0xf bank_mask:0xf\n\t"
                 "v_and_b32_dpp v97, v92, v103 quad_perm:[2,2,2,2] row_mask:0xf bank_mask:0xf\n\t"
                 "v_mfma_f32_4x4x4_16b_bf16 v[84:87], v[100:101], v[132:133], v[84:87]\n\tv_mfma_f32_4x4x4_16b_bf16 v[88:91], v[100:101], v[134:135], v[88:91]\n\t"
                 "v_and_b32_dpp v100, v93, v102 quad_perm:[2,2,2,2] row_mask:0xf bank_mask:0xf\n\t"
                 "v_and_b32_dpp v101, v93, v103 quad_perm:[2,2,2,2] row_mask:0xf bank_mask:0xf\n\tv_mfma_f32_4x4x4_16b_bf16 v[84:87], v[96:97], v[136:137], v[84:87]\n\t"
                 "v_mfma_f32_4x4x4_16b_bf16 v[88:91], v[96:97], v[138:139], v[88:91]\n\tv_and_b32_dpp v96, v94, v102 quad_perm:[2,2,2,2] row_mask:0xf bank_mask:0xf\n\t"
                 "v_and_b32_dpp v97, v94, v103 quad_perm:[2,2,2,2] row_mask:0xf bank_mask:0xf\n\t"
                 "v_mfma_f32_4x4x4_16b_bf16 v[84:87], v[100:101], v[140:141], v[84:87]\n\tv_mfma_f32_4x4x4_16b_bf16 v[88:91], v[100:101], v[142:143], v[88:91]\n\t"
                 "v_and_b32_dpp v100, v95, v102 quad_perm:[2,2,2,2] row_mask:0xf bank_mask:0xf\n\t"
                 "v_and_b32_dpp v101, v95, v103 quad_perm:[2,2,2,2] row_mask:0xf bank_mask:0xf\n\tv_mfma_f32_4x4x4_16b_bf16 v[84:87], v[96:97], v[144:145], v[84:87]\n\t"
                 "v_mfma_f32_4x4x4_16b_bf16 v[88:91], v[96:97], v[146:147], v[88:91]\n\tv_and_b32_dpp v96, v92, v102 quad_perm:[3,3,3,3] row_mask:0xf bank_mask:0xf\n\t"
                 "v_and_b32_dpp v97, v92, v103 quad_perm:[3,3,3,3] row_mask:0xf bank_mask:0xf\n\t"
                 "v_mfma_f32_4x4x4_16b_bf16 v[84:87], v[100:101], v[148:149], v[84:87]\n\tv_mfma_f32_4x4x4_16b_bf16 v[88:91], v[100:101], v[150:151], v[88:91]\n\t"
                 "v_and_b32_dpp v100, v93, v102 quad_perm:[3,3,3,3] row_mask:0xf bank_mask:0xf\n\t"
                 "v_and_b32_dpp v101, v93, v103 quad_perm:[3,3,3,3] row_mask:0xf bank_mask:0xf\n\tv_mfma_f32_4x4x4_16b_bf16 v[84:87], v[96:97], v[152:153], v[84:87]\n\t"
                 "v_mfma_f32_4x4x4_16b_bf16 v[88:91], v[96:97], v[154:155], v[88:91]\n\tv_and_b32_dpp v96, v94, v102 quad_perm:[3,3,3,3] row_mask:0xf bank_mask:0xf\n\t"
                 "v_and_b32_dpp v97, v94, v103 quad_perm:[3,3,3,3] row_mask:0xf bank_mask:0xf\n\t"
                 "v_mfma_f32_4x4x4_16b_bf16 v[84:87], v[100:101], v[156:157], v[84:87]\n\tv_mfma_f32_4x4x4_16b_bf16 v[88:91], v[100:101], v[158:159], v[88:91]\n\t"
                 "v_and_b32_dpp v100, v95, v102 quad_perm:[3,3,3,3] row_mask:0xf bank_mask:0xf\n\t"
                 "v_and_b32_dpp v101, v95, v103 quad_perm:[3,3,3,3] row_mask:0xf bank_mask:0xf\n\tv_mfma_f32_4x4x4_16b_bf16 v[84:87], v[96:97], v[160:161], v[84:87]\n\t"
                 "v_mfma_f32_4x4x4_16b_bf16 v[88:91], v[96:97], v[162:163], v[88:91]\n\ts_nop 1\n\t"
                 "v_mfma_f32_4x4x4_16b_bf16 v[84:87], v[100:101], v[164:165], v[84:87]\n\tv_mfma_f32_4x4x4_16b_bf16 v[88:91], v[100:101], v[166:167], v[88:91]\n\t"
                 "s_nop 4\n\tv_permlane32_swap_b32 v84, v88\n\tv_permlane32_swap_b32 v85, v89\n\tv_permlane32_swap_b32 v86, v90\n\tv_permlane32_swap_b32 v87, v91\n\t"
                 "v_add_f32 v84, v84, v88\n\tv_add_f32 v85, v85, v89\n\tv_add_f32 v86, v86, v90\n\tv_add_f32 v87, v87, v91\n\ts_nop 0\n\t"
                 "v_permlane16_swap_b32 v84, v85\n\tv_permlane16_swap_b32 v86, v87\n\ts_nop 0\n\tv_add_f32 v84, v84, v85\n\tv_add_f32 v86, v86, v87\n\ts_nop 1\n\t"
                 "v_add_f32_dpp v84, v84, v84 row_ror:8 row_mask:0xf bank_mask:0xf\n\tv_add_f32_dpp v86, v86, v86 row_ror:8 row_mask:0xf bank_mask:0xf\n\ts_nop 0\n\t"
                 "v_add_f32_dpp v84, v84, v84 row_ror:4 row_mask:0xf bank_mask:0xf\n\tv_add_f32_dpp v86, v86, v86 row_ror:4 row_mask:0xf bank_mask:0xf\n\t"
                 "ds_write2_b32 v99, v84, v86 offset1:8\n\ts_setpc_b64 s[92:93]\n\t"
                   :: "{s[0:1]}"(ap + 1), "{s2}"(bid), "{v0}"(wid * 64), KEPT() : "s90", "s91", "scc", "memory");
      __builtin_unreachable();
    }
    {
    constexpr int dk = 8;
    constexpr int BEST = FLG + 40, MEET = FLG + 48;
    __builtin_amdgcn_s_setprio(HPRIO);
    if (hw == 0) STAMP(dk, 0);
    const unsigned tb1 = (unsigned)__builtin_amdgcn_s_memrealtime();
    unsigned* hand2 = hand + HAND_N;
    const __amdgpu_buffer_rsrc_t rsh = __builtin_amdgcn_make_buffer_rsrc(hand2, 0, (HAND_N + PS_N) * 4, 0x00020000);
    const __amdgpu_buffer_rsrc_t rsx = __builtin_amdgcn_make_buffer_rsrc(xout, 0, S_ * D_ * 2, 0x00020000);
    constexpr int OOR = (HAND_N + PS_N) * 4;
    if (!p1blk && hw < 2) {
      f32x2 a[2], c[2];
#pragma unroll
      for (int e = 0; e < 4; ++e) {
        a[e >> 1][e & 1] = (__uint_as_float(pv[0][e] << 16) + __uint_as_float(pv[1][e] << 16)) + (__uint_as_float(pv[2][e] << 16) + __uint_as_float(pv[3][e] << 16));
        c[e >> 1][e & 1] = (__uint_as_float(pv[0][e] & 0xffff0000u) + __uint_as_float(pv[1][e] & 0xffff0000u)) + (__uint_as_float(pv[2][e] & 0xffff0000u) + __uint_as_float(pv[3][e] & 0xffff0000u));
      }
      const float r = (fnum + fold8(a[0], a[1])) * __builtin_amdgcn_rsqf(fvar + fold8(c[0], c[1]));
      unsigned key = (__float_as_uint(r) ^ ((unsigned)(__float_as_int(r) >> 31) | 0x80000000u)) & 0xfffff800u;
      if (r != r) key = 0u;
      key = wave_umax(key | (unsigned)(2047 - (rrow + (lane & 7) * 4 + (lane >> 4))));
      __builtin_amdgcn_raw_buffer_store_b64(u32x2{key, tb1 & 0x7fffffffu}, rsh, lane < NREP ? (HAND_N + PK_O + lane * 128 + (rrow >> 5) * 2) * 4 : OOR, 0, 16);
      if (hw == 1) STAMP(dk, 2);
    }
    gu_setup();
    bool got = true;
    unsigned fra = lbase;
    int fro = S_ * D_ * 2;
    auto get_fc = [&]() __attribute__((always_inline)) {
      const u32x2 fw = __builtin_amdgcn_raw_buffer_load_b64(rsh, (HAND_N + PK_O + (bid % NREP) * 128 + lane * 2) * 4, 0, 16);
      got = none(umax(fw[0], fw[1]) == SENT);
      if (got) {
        const unsigned c1 = wave_umax(fw[0]), c2 = wave_umax(fw[0] == c1 ? 0u : fw[0]);
#define BADFC 0
        const unsigned r1 = (2047u - (c1 & 0x7ffu)) ^ BADFC, r2 = 2047u - (c2 & 0x7ffu);
        const unsigned r = (lane & 8) ? r2 : r1;
        fra = lbase + QOFF + (r & 127u) * 128 + (((r ^ lane) & 7u) << 4);
        fro = (r >> 7) == (unsigned)omi && lane < 16 ? (int)(r * D_ + oni * 64 + (lane & 7) * 8) * 2 : S_ * D_ * 2;
        unsigned dt = (unsigned)((int)((fw[1] - tb1) << 1) >> 1) + 512u;
        dt = dt < 1024u ? dt : 512u;
        *LDS_AT(volatile u32x2, lbase + SPM + 144) = u32x2{r1 | (r2 << 16), (unsigned)__builtin_amdgcn_readlane(dt, r1 >> 5) | (wave_umax(dt) << 16)};
      }
    };
    if (hw == 0) {
      __builtin_amdgcn_s_sleep(FCPRE);
#pragma unroll 1
      for (int i = 0; i < SPIN_MAX; ++i) {
        if (__builtin_amdgcn_readfirstlane(*LDS_AT(const volatile unsigned, lbase + FLG + 28)) - best >= 4u) break;
        __builtin_amdgcn_s_sleep(FCSLP);
      }
      asm volatile("" ::: "memory");
      get_fc();
    } else __builtin_amdgcn_s_sleep(TPRE - 10);
    PIN();
    tile_at(2u);
    const unsigned tb2 = (unsigned)__builtin_amdgcn_s_memrealtime() - 512u;
    LOGDEF;
    LOG();
#pragma unroll 1
    for (int spin = 0; spin < SPIN_MAX && !got; ++spin) { get_fc(); asm volatile("" ::: "memory"); }
    if (hw == 0) __builtin_amdgcn_raw_buffer_store_b128(lds_rd4(fra), rsx, fro, 0, XAUX);
    PIN();
    u32x2 fm = {SENT, 0u};
#pragma unroll 1
    for (int i = 0; i < 4 * SPIN_MAX && fm[0] == SENT; ++i) {
      const u32x2 q = *LDS_AT(const volatile u32x2, lbase + SPM + 144);
      fm = u32x2{(unsigned)__builtin_amdgcn_readfirstlane(q[0]), (unsigned)__builtin_amdgcn_readfirstlane(q[1])};
    }
    const int fr1 = fm[0] & 0x7ffu, fr2 = (fm[0] >> 16) & 0x7ffu;
    const unsigned t1 = tb2 + (fm[1] & 1023u), tmx = tb2 + ((fm[1] >> 16) & 1023u);
    auto until = [&](unsigned te) { while ((int)((unsigned)__builtin_amdgcn_s_memrealtime() - te) < 0) __builtin_amdgcn_s_sleep(1); asm volatile("" ::: "memory"); };
    lds_wr(lbase + SPM + 136, tmx);
    PIN();
    if (hw == 0) STAMP(dk, 3);
    const int xoff = (hw * 256 + (lane & 3) * 64 + (lane >> 2) * 4) * 2;
    const int oc = (omi * 2 + (oni >> 3)) * 32, col = oc + (lane & 7) * 4 + (lane >> 4);
    const int xoo = !p1blk && hw == 3 ? (col & ~1) * 2 : S_ * D_ * 2;
    auto get_row = [&](int r, u32x2& xr, unsigned& xo) __attribute__((always_inline)) {
      if ((r >> 7) == omi) {
#pragma unroll 1
        for (int i = 0; i < SPIN_MAX; ++i) {
          if (__builtin_amdgcn_readfirstlane(*LDS_AT(const volatile unsigned, lbase + FLG + 24)) >= 16u) break;
          __builtin_amdgcn_s_sleep(1);
        }
        asm volatile("" ::: "memory");
      }
#pragma unroll 1
      for (int spin = 0; spin < SPIN_MAX; ++spin) {
        LOG();
        xr = __builtin_amdgcn_raw_buffer_load_b64(rsx, xoff, r * (D_ * 2), 16);
        xo = __builtin_amdgcn_raw_buffer_load_b32(rsx, xoo, r * (D_ * 2), 16);
        LOG();
        if (none(umax(umax(xr[0], xr[1]), xo) == SENT)) break;
        __builtin_amdgcn_s_sleep(XRSLP);
        asm volatile("" ::: "memory");
      }
      PIN();
    };
    unsigned rk1;
    auto row_md = [&](const u32x2& xr, int k) __attribute__((always_inline)) {
      auto dot = [&](unsigned x, unsigned y, float z) { return __builtin_amdgcn_fdot2_f32_bf16(__builtin_bit_cast(bf16x2, x), __builtin_bit_cast(bf16x2, y), z, false); };
      const float p1 = dot(xr[1], 0x3f803f80u, dot(xr[0], 0x3f803f80u, 0.f)), p2 = dot(xr[1], xr[1], dot(xr[0], xr[0], 0.f)), p3 = dot(xr[1], wq[1], dot(xr[0], wq[0], 0.f));
      const auto q = __builtin_amdgcn_permlane32_swap(__float_as_uint(p1), __float_as_uint(p2), false, false);
      const auto q3 = __builtin_amdgcn_permlane32_swap(__float_as_uint(p3), __float_as_uint(p3), false, false);
      const auto q2 = __builtin_amdgcn_permlane16_swap(__float_as_uint(__uint_as_float(q[0]) + __uint_as_float(q[1])), __float_as_uint(__uint_as_float(q3[0]) + __uint_as_float(q3[1])), false, false);
      float sv = __uint_as_float(q2[0]) + __uint_as_float(q2[1]);
      sv = dpp_add<0x121, 0xf>(dpp_add<0x122, 0xf>(dpp_add<0x124, 0xf>(dpp_add<0x128, 0xf>(sv))));
      lds_wr(lbase + SPM + k * 64 + (lane >> 4) * 16 + hw * 4, f2word(sv));
      u32x4 a, c, e;
#pragma unroll 1
      for (int i = 0; i < SPIN_MAX; ++i) {
        a = lds_rd4(lbase + SPM + k * 64); c = lds_rd4(lbase + SPM + k * 64 + 32); e = lds_rd4(lbase + SPM + k * 64 + 16);
        if (__builtin_amdgcn_readfirstlane(umax(umax(a[0], a[1]), umax(a[2], a[3]))) != SENT) break;
      }
      auto sum4 = [&](const u32x4& v) { return (__uint_as_float(v[0]) + __uint_as_float(v[1])) + (__uint_as_float(v[2]) + __uint_as_float(v[3])); };
      const float mean = sum4(a) * (1.0f / 1024.0f), msq = sum4(c) * (1.0f / 1024.0f);
      const float den = rbf(__builtin_amdgcn_sqrtf(rbf(rbf(fmaxf(fmaf(-mean, mean, msq), 0.f)) + 1e-5f)));
      if (k == 0) { const float r = fmaf(-rbf(mean), wsum, sum4(e)) * frcp(den); rk1 = __builtin_amdgcn_readfirstlane(pack2(r, r)) >> 16; }
      return (unsigned)__builtin_amdgcn_readfirstlane((pack2(mean, mean) & 0xffffu) | (__float_as_uint(den) & 0xffff0000u));
    };
    auto ffn = [&](const u32x2& xr, unsigned md, unsigned n, const __amdgpu_buffer_rsrc_t rs, int yo, auto&& off) __attribute__((always_inline)) {
      gate_up(xr, md);
      if (hw == 0 && n == 2u) STAMP(dk, 8);
      asm volatile("" ::: "memory");
      PIN();
      lds_wr(lbase + MEET + hw * 4, n + off());
      u32x4 mt;
#pragma unroll 1
      for (int i = 0; i < SPIN_MAX; ++i) {
        mt = lds_rd4(lbase + MEET);
        if (__builtin_amdgcn_readfirstlane(min4(mt)) >= n) break;
      }
      asm volatile("" ::: "memory");
      PIN();
      if (hw == 0 && n == 2u) STAMP(dk, 9);
      if (__builtin_amdgcn_readfirstlane(mt[0]) != n + 1u)
        __builtin_amdgcn_raw_buffer_store_b128(down(act_diag(n == 4u ? 512 : 0), w), rs, (yo + fg * D_) * 4 + t * 16, 0, 16);
      dad += n == 2u ? 512 : -512;
      if (hw == 0 && n == 2u) STAMP(dk, 4);
    };
    auto y_sum4 = [&](const __amdgpu_buffer_rsrc_t rs, int yo, int k) __attribute__((always_inline)) {
      const int src = (yo + (hw * 56 + (lane >> 3)) * D_ + oc + (lane & 7) * 4) * 4;
      u32x4 v[7];
#pragma unroll
      for (int i = 0; i < 7; ++i) asm volatile("" : "=v"(v[i]));
      unsigned todo = 0x7fu;
#pragma unroll 1
      for (int spin = 0; spin < SPIN_MAX; ++spin) {
        LOG();
#pragma unroll
        for (int i = 0; i < 7; ++i) if ((todo >> i) & 1u) v[i] = __builtin_amdgcn_raw_buffer_load_b128(rs, src, i * (8 * D_ * 4), 16);
#pragma unroll
        for (int i = 0; i < 7; ++i) if (none(umax(umax(v[i][0], v[i][1]), umax(v[i][2], v[i][3])) == SENT)) todo &= ~(1u << i);
        if (!todo) break;
        __builtin_amdgcn_s_sleep(YSLP);
        asm volatile("" ::: "memory");
      }
      f32x2 z0 = {0.f, 0.f}, z1 = {0.f, 0.f};
#pragma unroll
      for (int i = 0; i < 7; ++i) {
        z0 += f32x2{__uint_as_float(v[i][0]), __uint_as_float(v[i][1])};
        z1 += f32x2{__uint_as_float(v[i][2]), __uint_as_float(v[i][3])};
      }
      const unsigned ya = lbase + SPM + 256 + k * 512 + ((lane & 7) * 4 + (lane >> 4)) * 16;
      lds_wr((lane & 8) ? lbase + DUM + lane * 4 : ya + hw * 4, f2word(fold8(z0, z1)));
      u32x4 q = {0u, 0u, 0u, 0u};
      if (hw == 3) {
#pragma unroll 1
        for (int i = 0; i < 4 * SPIN_MAX; ++i) {
          q = lds_rd4(ya);
          if (none(umax(umax(q[0], q[1]), umax(q[2], q[3])) == SENT)) break;
        }
      }
      return (__uint_as_float(q[0]) + __uint_as_float(q[1])) + (__uint_as_float(q[2]) + __uint_as_float(q[3]));
    };
    const __amdgpu_buffer_rsrc_t rs1 = __builtin_amdgcn_make_buffer_rsrc(hand, 0, HAND_N * 4, 0x00020000);
    u32x2 xr;
    unsigned xo, xo2;
    until(t1 + XDT);
    get_row(fr1, xr, xo);
    if (hw == 0) STAMP(dk, 7);
#define FAILFC 0
    const unsigned md1 = row_md(xr, 0) ^ (FAILFC & 1);
    if (hw == 0) STAMP(dk, 10);
    LOGAT(20);
    u32x2 xr2;
    unsigned md2 = 0u;
    if (p1blk) {
      xr2 = __builtin_amdgcn_raw_buffer_load_b64(rsx, (fr2 >> 7) == omi ? S_ * D_ * 2 : xoff, fr2 * (D_ * 2), 16);
      PIN();
      ffn(xr, md1, 2u, rsh, GRAN_N, [&]() { return 0u; });
      __builtin_amdgcn_s_setprio(R2PRIO);
      LOG();
      if ((fr2 >> 7) == omi || !none(umax(xr2[0], xr2[1]) == SENT)) get_row(fr2, xr2, xo2);
      md2 = row_md(xr2, 1) ^ (FAILFC & 2);
      ffn(xr2, md2, 4u, rs1, GRAN_N, [&]() {
        const u32x2 v = *LDS_AT(const volatile u32x2, lbase + BEST);
        return v[0] != 0u && (2047 - (int)(v[0] & 0x7ffu) != fr2 || v[1] != md2) ? 1u : 0u; });
    }
    if (hw == 0) STAMP(dk, 5);
    LOG();
    float y = 0.f;
    auto put_row = [&](int r, unsigned md, unsigned rb) __attribute__((always_inline)) {
      const float rden = __uint_as_float(md & 0xffff0000u), rs = rbf(frcp(1.0f + __expf(-__uint_as_float(rb << 16))));
      const float xa = __uint_as_float((col & 1) ? xo & 0xffff0000u : xo << 16);
      float ha = xa, hb = xa;
      ln_apply2(ha, hb, __uint_as_float(md << 16), rden, frcp(rden));
      const float o = xa + rbf(ha + rbf(rbf(y) * rs));
      __builtin_amdgcn_raw_buffer_store_b16((unsigned short)(pack2(o, o) & 0xffffu), __builtin_amdgcn_make_buffer_rsrc(out, 0, hw == 3 ? S_ * D_ * 2 : 0, 0x00020000),
                                            (lane & 8) ? S_ * D_ * 2 : (r * D_ + col) * 2, 0, OAUX);
    };
    if (!p1blk) {
      until(t1 + XDT + YD1); until(tmx + YD2);
      if (hw == 3) STAMP(dk, 13);
      LOGAT(8);
      y = y_sum4(rsh, GRAN_N, 0);
      put_row(fr1, md1, rk1);
      if (hw == 3) STAMP(dk, 14);
      LOGAT(24);
    }
    unsigned bmd = 0u;
    best = 0u;
#pragma unroll 1
    for (int spin = 0; spin < 4 * SPIN_MAX; ++spin) {
      const u32x2 v = *LDS_AT(const volatile u32x2, lbase + BEST);
      best = __builtin_amdgcn_readfirstlane(v[0]); bmd = __builtin_amdgcn_readfirstlane(v[1]);
      if (best) break;
      __builtin_amdgcn_s_sleep(1);
    }
    asm volatile("" ::: "memory");
    const int idx = 2047 - (int)(best & 0x7ffu);
    const unsigned rk = best >> 16, rb = (rk & 0x8000u) ? (rk & 0x7fffu) : (~rk & 0xffffu);
    if (hw == 0) STAMP(dk, 1);
    LOGAT(25);
    LOGOUT(hw);
    if (__builtin_expect(idx != fr1 || bmd != md1 || rb != rk1, 0)) {
      const __amdgpu_buffer_rsrc_t rsf = __builtin_amdgcn_make_buffer_rsrc(gran + GR_F, 0, !p1blk && hw == 3 && idx != fr1 ? 48 * 128 : 0, 0x00020000);
      asm volatile("s_waitcnt vmcnt(0)" ::: "memory");
      __builtin_amdgcn_raw_buffer_store_b32(tag, rsf, lane == 0 ? (16 + (oc >> 5)) * 128 : 48 * 128, 0, 16);
      if (idx != fr1 || bmd != md1) {
        if (!p1blk || idx != fr2 || bmd != md2) get_row(idx, xr, xo);
        if (!p1blk && idx == fr2) md2 = row_md(xr, 1) ^ (FAILFC & 2);
        if (idx == fr2 && bmd == md2) {
          if (!p1blk) y = y_sum4(rs1, GRAN_N, 1);
        } else {
          if (p1blk) ffn(xr, bmd, 6u, rsh, HAND_N + Y3_O, [&]() { return 0u; });
          else y = y_sum4(rsh, HAND_N + Y3_O, 2);
        }
      }
      if (p1blk || hw != 3) return;
#pragma unroll 1
      for (int spin = 0; spin < SPIN_MAX && idx != fr1; ++spin) {
        if (__builtin_amdgcn_readfirstlane(__builtin_amdgcn_raw_buffer_load_b32(rsf, (oc >> 6) * 128, 0, 16)) == tag) break;
        asm volatile("" ::: "memory");
      }
      put_row(idx, bmd, rb);
    }
    if (p1blk || hw != 3) return;
    }
    return;
  }
  if (wid >= 4) {
    const int lane = fresh_lane();
    const int lw = wid - 4;
    const long long lay_ = ap->lay;
    const u16* A = ap->a; const u16* W = ap->W; const u16* wc = ap->wc;
#define LPRIO 3
#define LP0 3
    __builtin_amdgcn_s_setprio(LP0);
    unsigned va[4], vw[6];
#pragma unroll
    for (int i = 0; i < 4; ++i) {
      const int L = (lw * 4 + i) * 64 + lane, row = L >> 3, cp = L & 7, c = cp ^ (((row >> 1) & 3) | (((row >> 4) & 1) << 2));
      va[i] = (unsigned)((m0 + row) * K + c * 8) * 2u - i * 1024u;
    }
#pragma unroll
    for (int i = 0; i < 6; ++i) {
      const int L2 = (lw * 6 + i) * 64 + lane, k = L2 / 24, cp = L2 % 24, c = cp ^ (4 * ((k >> 1) & 1)) ^ ((k >> 3) & 1);
      vw[i] = (unsigned)(k * ldw + (c >> 3) * 1024 + n0 + (c & 7) * 8) * 2u - i % 3 * 1024u;
    }
    PIN();
    asm volatile("" : "+v"(va[0]), "+v"(va[1]), "+v"(va[2]), "+v"(va[3]), "+v"(vw[0]), "+v"(vw[1]), "+v"(vw[2]), "+v"(vw[3]), "+v"(vw[4]), "+v"(vw[5]));
    const bool LAY2 = lay_ != 0;
    const int krot = tmi & 8;
    if (LAY2 && slow_cols(W, tni)) W = wc;
    auto stage_a = [&](int kt, int slot) {
      const char* pa = reinterpret_cast<const char*>(A) + (size_t)(kt ^ krot) * 128;
      asm volatile("s_mov_b32 m0, %5\n\ts_nop 0\n\tglobal_load_lds_dwordx4 %0, %4 sc1\n\tglobal_load_lds_dwordx4 %1, %4 offset:1024 sc1\n\t"
                   "global_load_lds_dwordx4 %2, %4 offset:2048 sc1\n\tglobal_load_lds_dwordx4 %3, %4 offset:3072 sc1"
                   :: "v"(va[0]), "v"(va[1]), "v"(va[2]), "v"(va[3]), "s"(pa), "s"(lbase + slot * STB + lw * 4096) : "m0", "memory");
    };
    auto stage_w = [&](int kt, int slot) {
      const char* pw = reinterpret_cast<const char*>(W) + (size_t)(kt ^ krot) * 128 * ldw;
      asm volatile("s_mov_b32 m0, %7\n\ts_nop 0\n\tglobal_load_lds_dwordx4 %0, %6 sc1\n\tglobal_load_lds_dwordx4 %1, %6 offset:1024 sc1\n\t"
                   "global_load_lds_dwordx4 %2, %6 offset:2048 sc1\n\ts_addk_i32 m0, 0xc00\n\ts_nop 0\n\tglobal_load_lds_dwordx4 %3, %6 sc1\n\t"
                   "global_load_lds_dwordx4 %4, %6 offset:1024 sc1\n\tglobal_load_lds_dwordx4 %5, %6 offset:2048 sc1"
                   :: "v"(vw[0]), "v"(vw[1]), "v"(vw[2]), "v"(vw[3]), "v"(vw[4]), "v"(vw[5]), "s"(pw), "s"(lbase + slot * STB + ABYTES + lw * 6144) : "m0", "scc", "memory");
    };
    if (LAY2) {
      __builtin_amdgcn_s_setprio(0);
      __builtin_amdgcn_s_sleep(TPRE - 10);
      tile_at(3u);
      __builtin_amdgcn_s_setprio(LPRIO);
#pragma unroll
      for (int st = 0; st < NST; ++st) stage_w(st, st);
      PIN();
      __builtin_amdgcn_s_barrier();
      PIN();
#pragma unroll
      for (int st = 0; st < NST; ++st) stage_a(st, st);
      asm volatile("s_waitcnt vmcnt(8)" ::: "memory");
    } else {
      __builtin_amdgcn_s_setprio(LPRIO);
      stage_a(0, 0); stage_w(0, 0);
      if (PRO > 1) { stage_a(1, 1); stage_w(1, 1); }
      if (PRO > 2) { stage_a(2, 2); stage_w(2, 2); }
      asm volatile("s_waitcnt vmcnt(%0)" :: "n"(10 * PRO - 10) : "memory");
    }
    int slot = 0;
    __builtin_amdgcn_s_barrier();
    if (!LAY2) {
      if (PRO < 2) { stage_a(1, 1); stage_w(1, 1); }
      if (PRO < 3) { stage_a(2, 2); stage_w(2, 2); }
    }
    for (int kt = 0; kt + 1 < NKT; ++kt) {
      if (LAY2 && kt == 0) asm volatile("s_waitcnt vmcnt(4)" ::: "memory");
      else if (kt + 2 >= NKT) asm volatile("s_waitcnt vmcnt(0)" ::: "memory");
      else asm volatile("s_waitcnt vmcnt(10)" ::: "memory");
      __builtin_amdgcn_s_barrier();
      if (kt + NST < NKT) { stage_a(kt + NST, slot); stage_w(kt + NST, slot); }
      slot = slot == NST - 1 ? 0 : slot + 1;
    }
    __builtin_amdgcn_s_setprio(0);
    __builtin_amdgcn_s_barrier();
  } else {
  LAYER_HERE;
  const int dk = LAY2 ? 5 : 1;
  const int lane = fresh_lane(), wm = wid & 1, wn = wid >> 1;
  if (wid == 0) STAMP(dk, 0);
  f32x4 acc[4][6];
#pragma unroll
  for (int i = 0; i < 4; ++i)
#pragma unroll
    for (int j = 0; j < 6; ++j) acc[i][j] = f32x4{0.f, 0.f, 0.f, 0.f};
  const int q16 = lane & 15, g4 = lane >> 4;
  const int e0 = 16 * (q16 >> 3) + (q16 & 7);
  s16x8 af[2][4];
  unsigned aad[2], wad[2];
  {
    const int row = wm * 64 + e0, swz = ((row >> 1) & 3) | (((row >> 4) & 1) << 2);
#pragma unroll
    for (int h = 0; h < 2; ++h) aad[h] = lbase + row * 128 + (((4 * h + g4) ^ swz) << 4);
    const int tq = (lane & 15) >> 2, tp = lane & 3, k = 8 * g4 + tq, ch = wn * 4 + 2 * (tp >> 1);
    wad[0] = lbase + ABYTES + k * 384 + ((ch ^ (4 * ((k >> 1) & 1)) ^ (g4 & 1)) << 4) + 8 * (tp & 1);
    wad[1] = wad[0] ^ 16;
  }
  auto lda = [&](unsigned a, int i) { return *LDS_AT(const s16x8, a + (i >> 1) * 4096 + (i & 1) * 1024); };
  auto ldw_ = [&](int h, int j) {
    const unsigned w = wad[j & 1] + h * 12288 + (j >> 1) * 128;
    s16x4 lo = __builtin_amdgcn_ds_read_tr16_b64_v4i16(LDS_AT(s16x4, w));
    s16x4 h4 = __builtin_amdgcn_ds_read_tr16_b64_v4i16(LDS_AT(s16x4, w + 1536));
    return (s16x8)__builtin_shufflevector(lo, h4, 0, 1, 2, 3, 4, 5, 6, 7);
  };
  ARGS_HERE;
  unsigned lto = lbase + wid * 4096;
  char* kvb = reinterpret_cast<char*>(KV) + ((tni * 2 + wn) * 64 + tmi * 4 + wm * 2) * 2048 - lto;
  {
    const unsigned ato = lbase + ALPHA + wm * 256, dum = lbase + DUM + lane * 4;
    *LDS_AT(u32x4, lto + QOFF + lane * 16) = u32x4{lane >= 16 ? lbase + CRED : lbase + TABA + (wm * 64 + e0) * 16, lbase + TABW + (wn * 32 + e0) * 16,
                                                   ato + e0 * 4, lto + (lane >> 5) * 1024 + ((lane & 28) * 2 + (lane & 3)) * 16};
    *LDS_AT(u32x2, lto + QOFF + 2048 + lane * 16) = u32x2{aad[0], aad[1]};
    *LDS_AT(u32x4, lto + QOFF + 1024 + lane * 16) = u32x4{ato + g4 * 16 + (lane >> 5) * 32, lto + (lane >> 5) * 1024 + ((lane & 24) * 2 + (lane & 7)) * 16,
                                                          lane == 0 ? lbase + FLG + 32 : dum, lane == 0 ? lbase + FLG + 16 : dum};
  }
  asm volatile("" : "+s"(lto), "+s"(kvb) :: "memory");
#pragma unroll
  for (int i = 0; i < 4; ++i)
#pragma unroll
    for (int j = 0; j < 6; ++j) asm volatile("" : "+v"(acc[i][j]));
  asm volatile("" : "+v"(aad[0]), "+v"(aad[1]), "+v"(wad[0]), "+v"(wad[1]));
  if (LAY2) __builtin_amdgcn_s_barrier();
  __builtin_amdgcn_s_barrier();
  PIN();
  if (wid == 0) STAMP(dk, 1);
  s16x8 wq[4][2];
  wq[0][0] = ldw_(0, 0); af[0][0] = lda(aad[0], 0); wq[0][1] = ldw_(1, 0); af[1][0] = lda(aad[1], 0); wq[1][0] = ldw_(0, 1); wq[1][1] = ldw_(1, 1);
  af[0][1] = lda(aad[0], 1); af[1][1] = lda(aad[1], 1); af[0][2] = lda(aad[0], 2); af[1][2] = lda(aad[1], 2);
  PIN();
  s16x4 wlo;
  auto rw = [&](s16x8& d, int h, int j, int hi, int off = 0) __attribute__((always_inline)) {
    const s16x4 v = __builtin_amdgcn_ds_read_tr16_b64_v4i16(LDS_AT(s16x4, wad[j & 1] + h * 12288 + (j >> 1) * 128 + hi * 1536 + off));
    if (hi) d = (s16x8)__builtin_shufflevector(wlo, v, 0, 1, 2, 3, 4, 5, 6, 7);
    else wlo = v;
  };
  auto at = [&](s16x8& x, int r) __attribute__((always_inline)) {
    if (r == 0) asm volatile("" :: "{v[104:107]}"(x));
    if (r == 1) asm volatile("" :: "{v[108:111]}"(x));
    if (r == 2) asm volatile("" :: "{v[112:115]}"(x));
    if (r == 3) asm volatile("" :: "{v[116:119]}"(x));
    if (r == 4) asm volatile("" :: "{v[120:123]}"(x));
    if (r == 5) asm volatile("" :: "{v[124:127]}"(x));
    if (r == 6) asm volatile("" :: "{v[128:131]}"(x));
    if (r == 7) asm volatile("" :: "{v[132:135]}"(x));
    if (r == 8) asm volatile("" :: "{v[136:139]}"(x));
    if (r == 9) asm volatile("" :: "{v[140:143]}"(x));
    if (r == 10) asm volatile("" :: "{v[144:147]}"(x));
    if (r == 11) asm volatile("" :: "{v[148:151]}"(x));
    if (r == 12) asm volatile("" :: "{v[152:155]}"(x));
    if (r == 13) asm volatile("" :: "{v[156:159]}"(x));
    if (r == 14) asm volatile("" :: "{v[160:163]}"(x));
    if (r == 15) asm volatile("" :: "{v[164:167]}"(x));
  };
  auto mm = [&](int i, int j, int h, int sl, int hw_) __attribute__((always_inline)) {
    s16x8 &a = af[h][i], &w = wq[sl][hw_];
    at(a, h * 4 + i); at(w, 8 + sl * 2 + hw_);
    if (j >= 4) asm volatile("v_mfma_f32_16x16x32_bf16 %0, %1, %2, %0" : "+v"(acc[i][j]) : "v"(a), "v"(w));
    else asm volatile("v_mfma_f32_16x16x32_bf16 %0, %1, %2, %0" : "+v"(acc[i][j]) : "v"(w), "v"(a));
    PIN();
  };
  auto next = [&](int g, int J0, int s0, int s1, int off) __attribute__((always_inline)) {
    if (g == 2 || g == 3) rw(wq[s0][0], 0, J0, g & 1, off);
    if (g == 4 || g == 5) af[g & 1][0] = lda(aad[g & 1] + off, 0);
    if (g == 6 || g == 7) rw(wq[s0][1], 1, J0, g & 1, off);
    if (g >= 8 && g < 12) rw(wq[s1][(g - 8) >> 1], (g - 8) >> 1, J0 + 1, g & 1, off);
    if (g >= 12) af[g & 1][(g - 10) >> 1] = lda(aad[g & 1] + off, (g - 10) >> 1);
  };
  auto third = [&](int p, int J0, int delta) __attribute__((always_inline)) {
    auto mv = [&](unsigned& x) __attribute__((always_inline)) { asm volatile("v_add_u32 %0, %1, %0" : "+v"(x) : "s"(delta)); };
#pragma unroll
    for (int g = 0; g < 16; ++g) {
      const int h = g & 1, i = p == 1 ? (g >> 1) & 3 : g >> 2, t = p == 1 ? g >> 3 : (g >> 1) & 1, j = 2 * p + t, sl = p == 1 ? 2 - 2 * t : p + t;
      mm(i, j, h, sl, h);
      if (p == 0) {
        if (g < 2) af[g][3] = lda(aad[g], 3);
        else if (g < 6) rw(wq[2][(g - 2) >> 1], (g - 2) >> 1, 2, g & 1);
        else if (g < 10) rw(wq[3][(g - 6) >> 1], (g - 6) >> 1, 5, g & 1);
        else if (g < 12) mv(aad[g - 10]);
      }
      if (p == 1) {
        if (g < 4) rw(wq[0][g >> 1], g >> 1, 3, g & 1);
        if (g == 4) mv(wad[1]);
        if (g >= 8 && g < 12) rw(wq[2][(g - 8) >> 1], (g - 8) >> 1, 4, g & 1);
        if (g == 12) mv(wad[0]);
      }
      if (p == 2) {
        if (g == BARG) {
          asm volatile("" ::: "memory");
          if (!QW) __builtin_amdgcn_s_waitcnt(0xc07f);
          PIN();
          __builtin_amdgcn_s_barrier();
          asm volatile("" ::: "memory");
        }
        next(g, J0, 0, 1, 0);
      }
      PIN();
      if (QW) {
        if ((p == 2 && g == 15) || (p == 0 && g == 7)) __builtin_amdgcn_s_waitcnt(0xc67f);
        if (p == 0 && g == 2) __builtin_amdgcn_s_waitcnt(0xc57f);
        if ((p == 0 && g == 14) || (p == 1 && (g & 7) == 7)) __builtin_amdgcn_s_waitcnt(0xc07f);
        PIN();
      }
    }
  };
  third(0, 0, STB); third(1, 0, STB);
  {
    static_assert((NKT - NST - 1) % NST == 0, "");
    if (QU) {
#pragma unroll 1
      for (int kt = 0; kt < NKT - NST - 1; kt += NST) {
#pragma unroll
        for (int u = 0; u < NST; ++u) {
          const int delta = u == 1 ? -(NST - 1) * STB : STB;
          third(2, 0, 0); third(0, 0, delta); third(1, 0, delta);
        }
      }
    } else {
      unsigned long long seq = 0x492492ull;
#pragma unroll 1
      for (int kt = NKT - NST - 1; kt > 0; --kt) {
        int delta = (seq & 1ull) ? -(NST - 1) * STB : STB;
        seq >>= 1;
        third(2, 0, 0); third(0, 0, delta); third(1, 0, delta);
      }
    }
  }
  static_assert(NKT % NST == 1, "slots of the last stages");
#define TT(i)
  auto bar = [&]() { PIN(); __builtin_amdgcn_s_barrier(); asm volatile("" ::: "memory"); PIN(); };
  TT(0);
  third(2, 2, 0);
  const __amdgpu_buffer_rsrc_t rsk = __builtin_amdgcn_make_buffer_rsrc(kvb, 0, 0x7fffffff, 0x00020000), rsv = __builtin_amdgcn_make_buffer_rsrc(kvb + VOFF, 0, 0x7fffffff, 0x00020000);
  unsigned pk;
  u32x2 t0, t1, t2, t3;
  auto park = [&](int i) { return *LDS_AT(const volatile u32x2, pk + (i >> 1) * 1024 + (i & 1) * 8); };
  float al[4];
  f32x4 av[4];
  u32x4 kvp[12];
  auto cacc = [&](int p, int x, int t) -> f32x4& { return p < 2 ? acc[x][2 * p + t] : acc[(x >> 1) * 2 + t][4 + (x & 1)]; };
  auto signal = [&](unsigned v) { __hip_atomic_fetch_add(LDS_AT(unsigned, t3[0]), v, __ATOMIC_RELAXED, __HIP_MEMORY_SCOPE_WORKGROUP); PIN(); };
  auto outop = [&](int p, int k) {
    if (k >= 0 && k < 16) {
      const int x = k >> 2, t = (k >> 1) & 1;
      f32x4& c = cacc(p, x, t);
      if (PKM) {
        const int o = 2 * (k & 1);
        f32x2 r = {c[o], c[o + 1]};
        if (p == 2) asm volatile("v_pk_mul_f32 %0, %0, %1" : "+v"(r) : "v"(f32x2{av[(x >> 1) * 2 + t][o], av[(x >> 1) * 2 + t][o + 1]}));
        else if (x & 1) asm volatile("v_pk_mul_f32 %0, %0, %1 op_sel:[0,1] op_sel_hi:[1,1]" : "+v"(r) : "v"(f32x2{al[x & 2], al[x | 1]}));
        else asm volatile("v_pk_mul_f32 %0, %0, %1 op_sel_hi:[1,0]" : "+v"(r) : "v"(f32x2{al[x & 2], al[x | 1]}));
        c[o] = r[0]; c[o + 1] = r[1];
      } else
#pragma unroll
      for (int o = 2 * (k & 1); o < 2 * (k & 1) + 2; ++o) {
        float r;
        asm volatile("v_mul_f32 %0, %1, %2" : "=v"(r) : "v"(c[o]), "v"(p < 2 ? al[x] : av[(x >> 1) * 2 + t][o]));
        c[o] = r;
      }
      PIN();
    }
    if (k >= 1 && k <= 16) {
      const int x = (k - 1) >> 2, t = ((k - 1) >> 1) & 1, o = (k - 1) & 1;
      kvp[p * 4 + x][2 * t + o] = pack2(cacc(p, x, t)[2 * o], cacc(p, x, t)[2 * o + 1]);
      PIN();
      if (t && o) {
        if (p) __builtin_amdgcn_raw_buffer_store_b128(kvp[p * 4 + x], p == 1 ? rsk : rsv, (p == 1 ? t1[1] : t2[1]) + (x >> 1) * 2048 + (x & 1) * (p == 1 ? 64 : 128), 0, 16);
        else *LDS_AT(u32x4, t2[1] + (x >> 1) * 2048 + (x & 1) * 128) = kvp[x];
        PIN();
      }
    }
  };
  static_for<0, 168>([&](auto nc) __attribute__((always_inline)) {
    constexpr int n = decltype(nc)::value, sec = n < 80 ? n >> 4 : (n < 104 ? 5 : 6 + ((n - 104) >> 4)), g = n < 104 ? n - 16 * sec : (n - 104) & 15;
    auto taba = [&](int i) { af[0][i] = *LDS_AT(const s16x8, t0[0] + (i >> 1) * 512 + (i & 1) * 128); };
    auto tabw = [&](int j) { wq[(2 + (j >> 1)) & 3][j & 1] = *LDS_AT(const s16x8, t0[1] + ((j >> 1) * 64 + (j & 1) * 8) * 16); };
    auto rwn = [&](int e, int sl, int j, int off) { if (e >= 0 && e < 4) rw(wq[sl][e >> 1], e >> 1, j, e & 1, off); };
    if (g == 0) TT(1 + sec);
    if (sec == 5) {
      const int i = (g / 12) * 2 + (g & 1), j = (g % 12) >> 1;
      mm(i, j, 0, (2 + (j >> 1)) & 3, j & 1);
    } else {
      constexpr int part[10] = {1, 2, 1, 2, 0, 0, 1, 2, 0, 0}, s0[10] = {0, 2, 0, 2, 0, 0, 1, 3, 1, 3};
      const int h = g & 1, i = sec == 7 ? (g >> 3) * 2 + ((g >> 1) & 1) : g >> 2, t = sec == 7 ? (g >> 2) & 1 : (g >> 1) & 1;
      mm(i, 2 * part[sec] + t, h, (s0[sec] + t) & 3, h);
    }
    if (sec == 0 || sec == 2) {
      if (g < 2) af[g][3] = lda(aad[g] + sec / 2 * STB, 3);
      rwn(g - 2, 2, 4, sec / 2 * STB); rwn(g - 6, 3, 5, sec / 2 * STB);
    }
    if (sec == 1) {
      if (g == 1) bar();
      next(g, 2, 0, 1, STB);
    }
    if ((sec == 3 && g == 12) || (sec == 6 && g == 13)) { pk = lto + QOFF + fresh_lane() * 16; asm volatile("" : "+v"(pk)); }
    if (sec == 3) {
      rwn(g, 0, 0, STB); rwn(g - 4, 1, 1, STB);
      if (g >= 8 && g < 12) asm volatile("v_subrev_u32 %0, %1, %0" : "+v"(g < 10 ? aad[g & 1] : wad[g & 1]) : "s"(STB));
      if (g == 13) t1 = park(1);
      if (g == 14) t0 = park(0);
    }
    if (sec == 4) {
      if (g == B15) bar();
      if (g == 4) taba(0);
      if (g == 5 || g == 6) tabw(g - 5);
      if (g == 7) taba(1);
      if (g == 8 || g == 9) tabw(g - 6);
      if (g == 10 || g == 11) af[1][g - 10] = lda(aad[1], g - 10);
      if (g == 12) taba(2);
      if (g == 15) taba(3);
    }
    if (sec == 5) {
      if (g < 2) tabw(4 + g);
      rwn(g - 2, 1, 2, 0);
      if (g == 12 || g == 13) af[0][g - 12] = lda(aad[0], g - 12);
      if (g == 22) { al[0] = *LDS_AT(const float, t1[0]); al[1] = *LDS_AT(const float, t1[0] + 32); }
      rwn(g - 16, 2, 3, 0);
      if (g == 20 || g == 21) af[1][g - 18] = lda(aad[1], g - 18);
    }
    if (sec == 6) {
      if (g < 2) af[0][2 + g] = lda(aad[0], 2 + g);
      rwn(g - 2, 3, 4, 0);
      if (g == 9) { al[2] = *LDS_AT(const float, t1[0] + 128); al[3] = *LDS_AT(const float, t1[0] + 160); }
      if (g == 14) t2 = park(2);
      if ((g & 3) >= 2) rwn((g - 10) / 4 * 2 + (g & 1) - (g < 10) * 8, 0, 5, 0);
    }
    if (sec == 7) {
      if (g == 2 || g == 3 || g == 10 || g == 11) av[(g >> 3) * 2 + (g & 1)] = *LDS_AT(const f32x4, t2[0] + ((g >> 3) * 32 + (g & 1) * 8) * 4);
      rwn(g < 2 ? g : (g < 4 ? -1 : g - 2), 1, 0, 0); rwn(g - 6, 2, 1, 0);
      if (g == 12) t3 = park(3);
      if (g == 13) { const u32x2 a = park(4); aad[0] = a[0]; aad[1] = a[1]; }
      if (g == 15) {
        asm volatile("" ::: "memory");
        __builtin_amdgcn_s_waitcnt(0xc07f);
        bar();
      }
    }
    if (sec == 8) next(g, 0, 3, 0, STB);
    if (sec == 9) {
      if (g < 2) af[g][3] = lda(aad[g] + STB, 3);
      if (g == 2) signal(0x10000u);
    }
    PIN();
    outop(1, n - 104 - KO);
    outop(2, n - 120 - KO);
    {
      constexpr int wg[8] = WGAPS;
#pragma unroll
      for (int e = 0; e < 8; ++e)
        if (n - 136 == wg[e]) {
          *LDS_AT(u32x4, (e < 4 ? t1[1] + (e & 1) * 64 : t2[1] + 2048 + (e & 1) * 128) + ((e >> 1) & 1) * ASB) = kvp[4 + e];
          PIN();
        }
    }
    if (n == 152 + 3 || n == 152 + 4) {
      al[2 * (n & 1)] *= QSCALE; al[2 * (n & 1) + 1] *= QSCALE;
      asm volatile("" : "+v"(al[2 * (n & 1)]), "+v"(al[2 * (n & 1) + 1]));
      if (n & 1) asm volatile("v_add_u32 %0, %1, %0" : "+v"(t2[1]) : "s"(QOFF));
      PIN();
    }
    outop(0, n - 152 - KO);
  });
  TT(13);
  if (wid == 0) STAMP(dk, 18);
#pragma unroll
  for (int k = 16 - KO; k <= 16; ++k) outop(0, k);
  __hip_atomic_fetch_add(LDS_AT(unsigned, t3[1]), 1u, __ATOMIC_RELAXED, __HIP_MEMORY_SCOPE_WORKGROUP);
  TT(14);
  if (KVW == 2) { asm volatile("s_waitcnt vmcnt(0)" ::: "memory"); signal(1u); }
  TT(15);
  if (wid == 0) STAMP(dk, 19);
  }
  PIN();
  {
  const int lane = fresh_lane(), tid = wid * 64 + lane;
  LAYER_HERE;
  const int dk = LAY2 ? 6 : 2;
  const int g = wid >> 2, hh = (wid >> 1) & 1, qg = wid & 1;
  const int head = tni * 2 + hh, qt = tmi * 4 + qg * 2;
  unsigned qaa = lbase + QOFF + (hh * 2 + qg) * 4096 + lane * 16 + (g ? 0 : 2048), qab = qaa ^ 2048;
  const unsigned wbase = lbase + (hh * 2 + g) * 4096 + lane * 16;
  auto sptr = [&](int sl) { unsigned a = wbase + sl * ASB; asm volatile("" : "+v"(a)); return LDS_AT(const s16x8, a); };
  auto rd = [&](__attribute__((address_space(3))) const s16x8* sp, int off) { return sp[off >> 4]; };
  auto sl0 = sptr(0);
  const unsigned arr_a = lane == 0 ? lbase + FLG + 16 : lbase + DUM + lane * 4;
  auto arrive = [&]() { __hip_atomic_fetch_add(LDS_AT(unsigned, arr_a), 1u, __ATOMIC_RELAXED, __HIP_MEMORY_SCOPE_WORKGROUP); };
  unsigned arr_w = lane == 0 ? lbase + FLG + 8 + g * 4 : lbase + DUM + lane * 4;
  unsigned arr_i = 1u << (8 * (wid & 3));
  auto arrive_w = [&]() { __hip_atomic_fetch_add(LDS_AT(unsigned, arr_w), arr_i, __ATOMIC_RELAXED, __HIP_MEMORY_SCOPE_WORKGROUP); };
  s16x8 sel;
#pragma unroll
  for (int e = 0; e < 8; ++e) sel[e] = ((lane & 7) == (e & 4)) ? (short)0x3F80 : (short)0;
  int sidx = 0xDDDDDDDD;
  f32x16 oa, ob, nn, sa, sb;
  f32x4 la = {0.f, 0.f, 0.f, 0.f}, lb = {0.f, 0.f, 0.f, 0.f};
#pragma unroll
  for (int e = 0; e < 16; ++e) { oa[e] = 0.f; ob[e] = 0.f; sa[e] = 0.f; sb[e] = 0.f; }
  asm volatile("" : "+v"(sidx), "+v"(sel), "+v"(oa), "+v"(ob), "+v"(la), "+v"(lb), "+v"(qaa), "+v"(qab), "+v"(arr_w), "+v"(arr_i));
  PIN();
  s16x8 qa0, qa1, qb0, qb1, k00, k01, v00, v01, v10, v11, k10, k11;
  u32x4 fw;
  auto first = [&]() {
    fw = lds_rd4(lbase + FLG + 16);
    asm volatile("" ::: "memory");
    k00 = rd(sl0, 0); qa0 = *LDS_AT(const s16x8, qaa); qb0 = *LDS_AT(const s16x8, qab);
    k01 = rd(sl0, 1024); qa1 = *LDS_AT(const s16x8, qaa + 1024); qb1 = *LDS_AT(const s16x8, qab + 1024);
    v00 = rd(sl0, 2048); v01 = rd(sl0, 3072);
    PIN();
  };
  if (KVW == 0 && g == 0) {
    asm volatile("s_waitcnt vmcnt(0)" ::: "memory");
    __hip_atomic_fetch_add(LDS_AT(unsigned, arr_w + 24), 1u, __ATOMIC_RELAXED, __HIP_MEMORY_SCOPE_WORKGROUP);
  }
  PIN();
  first();
  if (__builtin_amdgcn_readfirstlane(fw[0]) < 4u) {
#pragma unroll 1
    for (int i = 0; i < SPIN_MAX; ++i) {
      if (__builtin_amdgcn_readfirstlane(*LDS_AT(const volatile unsigned, lbase + FLG + 16)) >= 4u) break;
      __builtin_amdgcn_s_sleep(ASLP);
    }
    asm volatile("" ::: "memory");
    first();
  }
  const unsigned tag = __builtin_amdgcn_readfirstlane(fw[1]);
  const int idx = LAY2 ? (2047 - (int)(__builtin_amdgcn_readfirstlane(fw[3]) & 0xffffu)) & 2047 : -64;
  float ma;
  const float zoff = __uint_as_float(__builtin_amdgcn_readfirstlane(LAY2 && g == ((idx >> 6) & 1) ? 0u : 0xff800000u));
  {
    sa = MFMA32(k00, qa0, sa); sb = MFMA32(k00, qb0, sb);
    sa = MFMA32(k01, qa1, sa); sb = MFMA32(k01, qb1, sb);
    if (KVW == 1 && g == 0) {
      asm volatile("s_waitcnt vmcnt(0)" : "+v"(sa), "+v"(sb) :: "memory");
      __hip_atomic_fetch_add(LDS_AT(unsigned, arr_w + 24), 1u, __ATOMIC_RELAXED, __HIP_MEMORY_SCOPE_WORKGROUP);
    }
    float mb = sb[0];
    ma = sa[0];
#pragma unroll
    for (int e = 1; e < 16; ++e) { ma = fmaxf(ma, sa[e]); mb = fmaxf(mb, sb[e]); }
    ma = fmaxf(ma, mb);
    const auto sw = __builtin_amdgcn_permlane32_swap(__float_as_uint(ma), __float_as_uint(ma), false, false);
    ma = fmaxf(__uint_as_float(sw[0]), __uint_as_float(sw[1]));
    f32x2 n2 = {-ma, -ma};
    asm volatile("" : "+v"(n2));
#pragma unroll
    for (int e = 0; e < 16; ++e) nn[e] = n2[e & 1];
    sa = MFMA32(k00, qa0, nn); sa = MFMA32(k01, qa1, sa);
    la[0] = lb[0] = -__uint_as_float(__float_as_uint(__builtin_amdgcn_exp2f(zoff - ma)) & 0xffff0000u);
  }
#define SB() __builtin_amdgcn_sched_barrier(0)
#define EX(i) sc[i] = __builtin_amdgcn_exp2f(sc[i])
  auto unit = [&](f32x16& sc, f32x16& sn, const s16x8& kn0, const s16x8& kn1, const s16x8& qn0, const s16x8& qn1,
                  f32x16& op, f32x4& lp, const s16x8& vp,
                  f32x16& oc, f32x4& lc, const s16x8& vc, auto&& mid) {
#define UORD 1
    if (UORD == 0) {
    op = SMF32(vp, PHI(sn), op); SB();
    EX(0); EX(1); EX(2); SB();
    lp = SMF16(sel, PHI(sn), lp); SB();
    EX(3); EX(4); SB();
    sn = MFMA32(kn0, qn0, nn); SB();
    EX(5); EX(6); EX(7); SB();
    sn = MFMA32(kn1, qn1, sn); SB();
    mid();
    EX(8); EX(9); EX(10); SB();
    oc = SMF32(vc, PLO(sc), oc); SB();
    EX(11); EX(12); EX(13); SB();
    lc = SMF16(sel, PLO(sc), lc); SB();
    EX(14); EX(15); SB();
    } else {
    lp = SMF16(sel, PHI(sn), lp); SB();
    op = SMF32(vp, PHI(sn), op); SB();
    EX(0); EX(1); EX(2); EX(3); SB();
    sn = MFMA32(kn0, qn0, nn); SB();
    EX(4); EX(5); EX(6); EX(7); SB();
    sn = MFMA32(kn1, qn1, sn); SB();
    mid();
    EX(8); EX(9); EX(10); EX(11); SB();
    lc = SMF16(sel, PLO(sc), lc); SB();
    oc = SMF32(vc, PLO(sc), oc); SB();
    EX(12); EX(13); EX(14); EX(15); SB();
    }
    asm volatile("" : "+v"(sc));
  };
  sl0 = sptr(1);
  unsigned f0 = 0u;
  auto check = [&](unsigned need) {
    if (__builtin_expect(__builtin_amdgcn_readfirstlane(f0) < need, 0)) {
#pragma unroll 1
      for (int i = 0; i < SPIN_MAX; ++i) {
        if (__builtin_amdgcn_readfirstlane(*LDS_AT(const volatile unsigned, lbase + FLG)) >= need) break;
        __builtin_amdgcn_s_sleep(1);
      }
    }
    asm volatile("" ::: "memory");
  };
#define PAT 0x22221110
#define SAW8(i) do { if (((PAT >> (28 - 4 * (i))) & 3) != ((PAT >> ((32 - 4 * (i)) & 31)) & 3)) { __builtin_amdgcn_s_setprio((PAT >> (28 - 4 * (i))) & 3); SB(); } } while (0)
#define SAW(i) do { if (odd) SAW8(4 + (i)); else SAW8(i); } while (0)
  auto three = [&](int st, bool odd) {
    SAW(0);
    unit(sa, sb, k00, k01, qb0, qb1, ob, lb, v11, oa, la, v00, [&]() { k10 = rd(sl0, 0); k11 = rd(sl0, 1024); SB(); });
    SAW(1);
    unit(sb, sa, k10, k11, qa0, qa1, oa, la, v01, ob, lb, v00, [&]() { v10 = rd(sl0, 2048); v11 = rd(sl0, 3072); SB(); });
    sl0 = sptr((2 * st + 2) & 7);
    SB();
    SAW(2);
    unit(sa, sb, k10, k11, qb0, qb1, ob, lb, v01, oa, la, v10, [&]() { k00 = rd(sl0, 0); k01 = rd(sl0, 1024); SB(); });
  };
  auto fourth = [&](int st, bool odd) {
    SAW(3);
    unit(sb, sa, k00, k01, qa0, qa1, oa, la, v11, ob, lb, v10, [&]() {
      v00 = rd(sl0, 2048); v01 = rd(sl0, 3072);
      f0 = *LDS_AT(const volatile unsigned, lbase + FLG);
      SB();
      arrive_w();
      SB(); });
    check(st >= 14 ? 32u : 2u * st + 5u);
    sl0 = sptr((2 * st + 3) & 7);
    SB();
  };
  SB();
  SAW8(0);
  {
    f32x16& sc = sa;
    sb = MFMA32(k00, qb0, nn); SB();
    k10 = rd(sl0, 0); k11 = rd(sl0, 1024); SB();
    sb = MFMA32(k01, qb1, sb); SB();
    EX(0); EX(1); EX(2); EX(3); EX(4); EX(5); EX(6); EX(7); EX(8); SB();
    oa = SMF32(v00, PLO(sa), oa); SB();
    EX(9); EX(10); EX(11); SB();
    la = SMF16(sel, PLO(sa), la); SB();
    EX(12); EX(13); EX(14); EX(15); SB();
    asm volatile("" : "+v"(sa));
  }
  SAW8(1);
  unit(sb, sa, k10, k11, qa0, qa1, oa, la, v01, ob, lb, v00, [&]() { v10 = rd(sl0, 2048); v11 = rd(sl0, 3072); SB(); });
  sl0 = sptr(2);
  SB();
  SAW8(2);
  unit(sa, sb, k10, k11, qb0, qb1, ob, lb, v01, oa, la, v10, [&]() {
    f0 = *LDS_AT(const volatile unsigned, lbase + FLG);
    asm volatile("" ::: "memory");
    k00 = rd(sl0, 0); k01 = rd(sl0, 1024); SB(); });
  if (__builtin_amdgcn_readfirstlane(f0) < 3u) { check(3u); k00 = rd(sl0, 0); k01 = rd(sl0, 1024); }
  SB();
  SAW8(3);
  unit(sb, sa, k00, k01, qa0, qa1, oa, la, v11, ob, lb, v10, [&]() {
    v00 = rd(sl0, 2048); v01 = rd(sl0, 3072); f0 = *LDS_AT(const volatile unsigned, lbase + FLG); SB(); arrive_w(); SB(); });
  check(5u);
  sl0 = sptr(3);
  SB();
  three(1, true);
#pragma unroll 1
  for (int st = 2; st < 16; st += 2) { fourth(st - 1, true); three(st, false); fourth(st, false); three(st + 1, true); }
  SAW8(7);
  unsigned vr, tk, tv;
  u32x4 f4;
  auto table = [&]() {
    f4 = lds_rd4(lbase + FLG);
    k00 = *LDS_AT(const volatile s16x8, tk); k01 = *LDS_AT(const volatile s16x8, tk + 32);
    vr = *LDS_AT(const volatile u16, tv);
  };
  unsigned xw, xm;
  u32x2 ml;
  u32x4 o2[4];
  auto take = [&]() {
    ml = *LDS_AT(const volatile u32x2, xm);
#pragma unroll
    for (int e = 0; e < 4; ++e) o2[e] = lds_rd4(xw + e * 1024);
  };
  float pzt;
  int dso;
  f32x16& sc = sb;
#define EXP() do { asm volatile("" : "+v"(sc)); SB(); } while (0)
  oa = SMF32(v11, PHI(sa), oa); SB();
  tk = lbase + TAB + 128 + hh * 64 + (lane >> 5) * 16; tv = lbase + TAB + 256 + hh * 64 + (lane & 31) * 2;
  table();
  SB();
  EX(0); EX(1); EXP();
  la = SMF16(sel, PHI(sa), la); SB();
  EX(2); EX(3); EX(4); EXP();
  xw = wbase + (qg ? QOFF : 6 * ASB); xm = lbase + XML + wid * 512 + lane * 8;
  pzt = __builtin_amdgcn_exp2f(nn[0] + (zoff - 8.f));
  asm volatile("" : "+v"(xw), "+v"(xm), "+v"(pzt), "+v"(f4));
  SB();
  {
    const unsigned ph = LAY2 ? 0x79797979u : 0x80808080u, pg = qg ? 0u : 0x80u << (16 * hh + 8);
    auto ready = [&]() {
      return (((__builtin_amdgcn_readfirstlane(f4[1]) + ph) & 0x80808080u) == 0x80808080u) & (((__builtin_amdgcn_readfirstlane(g ? f4[3] : f4[2]) + 0x71717171u) & pg) == pg);
    };
    if (__builtin_expect(!ready(), 0)) {
#pragma unroll 1
      for (int i = 0; i < SPIN_MAX; ++i) {
        __builtin_amdgcn_s_sleep(1);
        table();
        if (ready()) break;
      }
    }
    asm volatile("" ::: "memory");
  }
  SB();
  sa = MFMA32(k00, qb0, nn); asm volatile("" : "+v"(sa)); SB();
#pragma unroll
  for (int e = 0; e < 4; ++e) *LDS_AT(volatile f32x4, xw + e * 1024) = f32x4{oa[4 * e], oa[4 * e + 1], oa[4 * e + 2], oa[4 * e + 3]};
  *LDS_AT(volatile u32x2, xm) = u32x2{__float_as_uint(ma), f2word(pzt <= la[0] ? la[0] : 0.f)};
  SB();
  EX(5); EX(6); EX(7); EXP();
  sa = MFMA32(k01, qb1, sa); asm volatile("" : "+v"(sa), "+v"(vr)); SB();
  ARGS_HERE;
  EX(8); EX(9); EX(10); EXP();
  ob = SMF32(v10, PLO(sb), ob); SB();
  EX(11); EX(12); EX(13); EXP();
  lb = SMF16(sel, PLO(sb), lb); SB();
  const bool used = !LAY2 || (qt + g) * 32 + (lane & 31) != idx;
  dso = used ? (((qt + g) * 32 + head) * 1024 + lane * 8) * 2 : S_ * D_ * 2;
  xw += g ? -4096 : 4096; xm += g ? -2048 : 2048;
  asm volatile("" : "+v"(dso), "+v"(xw), "+v"(xm));
  SB();
  EX(14); EX(15); EXP();
  take();
  SB();
  lb = SMF16(sel, PHI(sb), lb); SB();
  ob = SMF32(v11, PHI(sb), ob); SB();
  __builtin_amdgcn_s_setprio(EPRIO);
  TE(0, 0); TE(1, 0); TE(2, 0); TE(3, 0); TE(7, 0);
  float lsb = lb[0];
  if (LAY2) {
    sa[0] = __builtin_amdgcn_exp2f(sa[0]);
    ob = SMF32((u32x4{lane < 32 ? vr : 0u, 0u, 0u, 0u}), PLO(sa), ob);
    lsb += __uint_as_float(__float_as_uint(sa[0]) & 0xffff0000u);
    lsb = pzt <= lsb ? lsb : 0.f;
  }
  const __amdgpu_buffer_rsrc_t rso = __builtin_amdgcn_make_buffer_rsrc(Of, 0, S_ * D_ * 2, 0x00020000);
  TE(0, 1);
  if (__builtin_amdgcn_ballot_w64(ml[1] == SENT) != 0ull) {
#pragma unroll 1
    for (int i = 0; i < SPIN_MAX; ++i) {
      __builtin_amdgcn_s_sleep(XSLP);
      if (!__builtin_amdgcn_ballot_w64(*LDS_AT(const volatile unsigned, xm + 4) == SENT)) break;
    }
    asm volatile("s_waitcnt lgkmcnt(0)" ::: "memory");
    take();
  }
  arrive();
  TE(0, 2);
  TE(0, 3); TE(1, 1);
  {
    const float m2 = __uint_as_float(ml[0]), l2 = __uint_as_float(ml[1]);
    const float mx = fmaxf(ma, m2);
    const float f1 = __builtin_amdgcn_exp2f(ma - mx), f2 = __builtin_amdgcn_exp2f(m2 - mx);
    const float inv = frcp(lsb * f1 + l2 * f2);
    const float c1 = f1 * inv, c2 = f2 * inv;
    f32x2 r[8];
#pragma unroll
    for (int e = 0; e < 8; ++e) {
      const unsigned x0 = o2[e >> 1][(e & 1) * 2], x1 = o2[e >> 1][(e & 1) * 2 + 1];
      r[e] = f32x2{__uint_as_float(x0) * c2, __uint_as_float(x1) * c2};
    }
#pragma unroll
    for (int e = 0; e < 8; ++e) r[e] = f32x2{ob[2 * e], ob[2 * e + 1]} * f32x2{c1, c1} + r[e];
    auto put = [&]() {
#pragma unroll
      for (int sx = 0; sx < 2; ++sx)
        __builtin_amdgcn_raw_buffer_store_b128(u32x4{pack2(r[4 * sx][0], r[4 * sx][1]), pack2(r[4 * sx + 1][0], r[4 * sx + 1][1]),
            pack2(r[4 * sx + 2][0], r[4 * sx + 2][1]), pack2(r[4 * sx + 3][0], r[4 * sx + 3][1])}, rso, dso + sx * 1024, 0, 16);
    };
    put();
    PIN();
    __builtin_amdgcn_s_setprio(0);
    PIN();
    f32x2 chk = {lsb - __builtin_amdgcn_fmed3f(lsb, 8.6736174e-19f, 1.2676506e30f), l2 - __builtin_amdgcn_fmed3f(l2, 8.6736174e-19f, 1.2676506e30f)};
#pragma unroll
    for (int e = 0; e < 8; ++e) chk = r[e] * f32x2{0.f, 0.f} + chk;
    if (__builtin_expect(__builtin_amdgcn_ballot_w64(used && !(chk[0] + chk[1] == 0.f)) != 0ull, 0)) {
      float l;
      attn_safe(KV, head * 64, qb0, qb1, ob, l, LAY2 ? head * 64 + (idx >> 5) : -1, idx & 31, lbase + TAB + 128 + hh * 64);
      const float inv2 = 1.0f / l;
#pragma unroll
      for (int e = 0; e < 8; ++e) r[e] = f32x2{ob[2 * e] * inv2, ob[2 * e + 1] * inv2};
      asm volatile("s_waitcnt vmcnt(0)" ::: "memory");
      put();
    }
    unsigned pp = 0u;
    if (LAY2) {
      const unsigned ua = lbase + UGL + hh * 128 + (lane >> 5) * 16;
      f32x2 du = {0.f, 0.f}, dn = {0.f, 0.f};
#pragma unroll
      for (int q = 0; q < 4; ++q) {
        const f32x4 uu = *LDS_AT(const f32x4, ua + q * 32), gg = *LDS_AT(const f32x4, ua + 256 + q * 32);
        du = r[2 * q] * f32x2{uu[0], uu[1]} + du; du = r[2 * q + 1] * f32x2{uu[2], uu[3]} + du;
        const f32x2 t0 = r[2 * q] * f32x2{gg[0], gg[1]}, t1 = r[2 * q + 1] * f32x2{gg[2], gg[3]};
        dn = t0 * t0 + dn; dn = t1 * t1 + dn;
      }
      const auto a = __builtin_amdgcn_permlane32_swap(__float_as_uint(du[0] + du[1]), __float_as_uint(du[0] + du[1]), false, false);
      const auto c = __builtin_amdgcn_permlane32_swap(__float_as_uint(dn[0] + dn[1]), __float_as_uint(dn[0] + dn[1]), false, false);
      pp = f2word(__uint_as_float(pack2(__uint_as_float(a[0]) + __uint_as_float(a[1]), __uint_as_float(c[0]) + __uint_as_float(c[1]))));
    }
    __builtin_amdgcn_raw_buffer_store_b32(pp, __builtin_amdgcn_make_buffer_rsrc(hand + 2 * HAND_N + PP_O, 0, LAY2 ? H_ * S_ * 4 : 0, 0x00020000),
                                          lane < 32 ? (head * S_ + (qt + g) * 32 + lane) * 4 : H_ * S_ * 4, 0, 16);
    PIN();
  }
  TE(1, 2);
  u32x4 xres[2];
  oproj_tail(LAY2, tid, lbase, lbase + FLG, omi, oni, idx, gran + GR_O + ((LAY2 ? 16 : 0) + tmi) * (64 * RO) + tni,
             gran + GR_X + omi * (128 * RX) + oni * 4, tag, Of, xout, A, w_r, stats_out, stats, hand, xres TEPASS);
  if (LAY2) {
    const int row = omi * 128 + (wid & 3) * 32 + (wid >> 2) * 16 + (lane >> 3), col = oni * 64 + (lane & 7) * 8;
    unsigned md[2], best = 0u;
    const __amdgpu_buffer_rsrc_t rsk = __builtin_amdgcn_make_buffer_rsrc(hand + HAND_N, 0, (HAND_N + PS_N) * 4, 0x00020000);
    f32x2 xf[2][4];
#pragma unroll
    for (int p = 0; p < 2; ++p)
#pragma unroll
      for (int e = 0; e < 4; ++e) {
        xf[p][e] = f32x2{__uint_as_float(xres[p][e] << 16), __uint_as_float(xres[p][e] & 0xffff0000u)};
        asm volatile("" : "+v"(xf[p][e]));
      }
    LOGDEF;
    LOG();
    const bool rtr = (oni & 7) == 7;
    {
      const int rl = (wid & 3) * 32 + (wid >> 2) * 16 + (lane & 15), rr = omi * 128 + rl;
      int src = (HAND_N + ((omi * 16 + (lane >> 4) * 4) * 128 + rl) * 4) * 4;
      int ko = rtr && (wid >> 2) == (oni >> 3) && lane < NREP ? lane * 1024 + (rr >> 4) * 8 : (HAND_N + PS_N) * 4;
      int bp = (lane >> 3) * 4;
      const float wsum = *LDS_AT(const volatile float, lbase + SPM + 132);
      asm volatile("" : "+v"(src), "+v"(ko), "+v"(bp));
      u32x4 v[4];
      __builtin_amdgcn_s_sleep(MPRE);
#pragma unroll 1
      for (int spin = 0; spin < SPIN_MAX; ++spin) {
        LOG();
#pragma unroll
        for (int i = 0; i < 4; ++i) v[i] = __builtin_amdgcn_raw_buffer_load_b128(rsk, src + (i & 1) * 2048, (i >> 1) * 4096, 16);
        unsigned mx = 0u;
#pragma unroll
        for (int i = 0; i < 4; ++i) mx = umax(umax(umax(mx, v[i][0]), v[i][1]), v[i][2]);
        if (none(mx == SENT)) break;
        __builtin_amdgcn_s_sleep(MSLP);
        asm volatile("" ::: "memory");
      }
      LOGAT(12);
      if (rtr) __builtin_amdgcn_s_setprio(3); else __builtin_amdgcn_s_setprio(LNPRIO);
      double sm[3];
#pragma unroll
      for (int c = 0; c < 3; ++c) {
        const double a = ((double)__uint_as_float(v[0][c]) + (double)__uint_as_float(v[1][c])) + ((double)__uint_as_float(v[2][c]) + (double)__uint_as_float(v[3][c]));
        u32x2 q = __builtin_bit_cast(u32x2, a);
        const auto lo = __builtin_amdgcn_permlane32_swap(q[0], q[0], false, false), hi = __builtin_amdgcn_permlane32_swap(q[1], q[1], false, false);
        q = __builtin_bit_cast(u32x2, __builtin_bit_cast(double, u32x2{lo[0], hi[0]}) + __builtin_bit_cast(double, u32x2{lo[1], hi[1]}));
        const auto l2 = __builtin_amdgcn_permlane16_swap(q[0], q[0], false, false), h2 = __builtin_amdgcn_permlane16_swap(q[1], q[1], false, false);
        sm[c] = __builtin_bit_cast(double, u32x2{l2[0], h2[0]}) + __builtin_bit_cast(double, u32x2{l2[1], h2[1]});
      }
      float mean, var, mb, den;
      ln_from_sums(sm[0], sm[1], mean, var, mb, den);
      const unsigned mdr = f2word(__uint_as_float((__float_as_uint(mb) >> 16) | (__float_as_uint(den) & 0xffff0000u)));
      md[0] = __builtin_amdgcn_ds_bpermute(bp, mdr); md[1] = __builtin_amdgcn_ds_bpermute(bp + 32, mdr);
      if (rtr) {
        const float r = (float)fma(-(double)mb, (double)wsum, sm[2]) * frcp(den);
        const unsigned rb = pack2(r, r);
        unsigned rkey = (rb ^ ((unsigned)((int)rb >> 31) | 0x80000000u)) & 0xffff0000u;
        if (r != r) rkey = 0u;
        const unsigned top = wave_umax(rkey | (unsigned)(2047 - rr));
        const unsigned mdt = __builtin_amdgcn_readlane(mdr, (2047 - (int)(top & 0xffffu)) & 15);
        __builtin_amdgcn_raw_buffer_store_b64(u32x2{top, mdt}, rsk, ko, 0, 16);
        PIN();
        __builtin_amdgcn_s_setprio(LNPRIO);
        __hip_atomic_fetch_add(LDS_AT(unsigned, lane == 0 ? lbase + SPM + 128 : lbase + DUM + lane * 4), 1u, __ATOMIC_RELAXED, __HIP_MEMORY_SCOPE_WORKGROUP);
        if (wid == 2 || wid == 5) STAMP(8, 6);
      }
      LOGAT(13);
    }
    auto keys = [&](const u32x4& v) __attribute__((always_inline)) {
      if (none(umax(umax(v[0], v[1]), umax(v[2], v[3])) == SENT)) {
        best = wave_umax(umax(v[0], v[2])) | 0x8000u;
        *LDS_AT(volatile u32x2, lbase + FLG + 40) = u32x2{best, (unsigned)__builtin_amdgcn_readlane(v[0] > v[2] ? v[1] : v[3], ((2047 - (int)(best & 0x7ffu)) >> 5) & 63)};
        STAMP(8, 11);
      }
    };
    auto ln_rows = [&]() __attribute__((always_inline)) {
#pragma unroll
      for (int p = 0; p < 2; ++p) {
        const float mb_ = __uint_as_float(md[p] << 16), rin = frcp(__uint_as_float(md[p] & 0xffff0000u));
#pragma unroll
        for (int e = 0; e < 4; ++e) {
          const f32x2 h = (xf[p][e] - f32x2{mb_, mb_}) * f32x2{rin, rin} + xf[p][e];
          xres[p][e] = pack2(h[0], h[1]);
        }
      }
      asm volatile("" : "+v"(xres[0]), "+v"(xres[1]));
    };
    if (wid == 0) STAMP(8, 12);
    ln_rows();
    LOGAT(14);
    const __amdgpu_buffer_rsrc_t rsu = __builtin_amdgcn_make_buffer_rsrc(out, 0, S_ * D_ * 2, 0x00020000);
    unsigned fr1 = SENT;
#pragma unroll 1
    for (int i = 0; i < 4 * SPIN_MAX && fr1 == SENT; ++i) fr1 = __builtin_amdgcn_readfirstlane(*LDS_AT(const volatile unsigned, lbase + SPM + 144));
    fr1 &= 0x7ffu;
    unsigned tmx = __builtin_amdgcn_readfirstlane(*LDS_AT(const volatile unsigned, lbase + SPM + 136)) + KDT;
    if (tmx - (unsigned)__builtin_amdgcn_s_memrealtime() > 1024u) tmx = 0u;
    auto due = [&]() { return tmx == 0u || (int)((unsigned)__builtin_amdgcn_s_memrealtime() - tmx) >= 0; };
    lk_set(15);
    if (wid == 6) {
      if (rtr) {
#pragma unroll 1
        for (int i = 0; i < SPIN_MAX; ++i) {
          if (__builtin_amdgcn_readfirstlane(*LDS_AT(const volatile unsigned, lbase + SPM + 128)) == 7u) break;
          __builtin_amdgcn_s_sleep(1);
        }
        asm volatile("" ::: "memory");
      }
    }
#pragma unroll 1
    for (int i = 0; i < OSLP && !best; ++i) {
      if (wid == 6 && due()) {
        LOG();
        keys(__builtin_amdgcn_raw_buffer_load_b128(rsk, (bid % NREP) * 1024 + lane * 16, 0, 16));
        i += 2;
      } else {
        if (__builtin_amdgcn_readfirstlane(*LDS_AT(const volatile unsigned, lbase + FLG + 40))) break;
        __builtin_amdgcn_s_sleep(2);
      }
      asm volatile("" ::: "memory");
    }
    u32x4 kv = {SENT, SENT, SENT, SENT};
    if (wid == 6 && !best && due()) kv = __builtin_amdgcn_raw_buffer_load_b128(rsk, (bid % NREP) * 1024 + lane * 16, 0, 16);
    PIN();
#pragma unroll
    for (int p = 0; p < 2; ++p) __builtin_amdgcn_raw_buffer_store_b128(xres[p], rsu, row + 8 * p != (int)fr1 ? ((row + 8 * p) * D_ + col) * 2 : S_ * D_ * 2, 0, OAUX);
    PIN();
    if (wid == 6 && !best) { keys(kv); while (!due()) __builtin_amdgcn_s_sleep(1); }
#pragma unroll 1
    for (int i = 0; i < 4 * SPIN_MAX && !best; ++i) {
      if (wid == 6) {
        LOG();
        keys(__builtin_amdgcn_raw_buffer_load_b128(rsk, (bid % NREP) * 1024 + lane * 16, 0, 16));
        if (!best) __builtin_amdgcn_s_sleep(KSLP);
      } else {
        best = __builtin_amdgcn_readfirstlane(*LDS_AT(const volatile unsigned, lbase + FLG + 40));
        if (!best) __builtin_amdgcn_s_sleep(1);
      }
      asm volatile("" ::: "memory");
    }
    const int idx2 = 2047 - (int)(best & 0x7ffu);
    if (__builtin_expect(idx2 != (int)fr1, 0)) {
      const __amdgpu_buffer_rsrc_t rsf = __builtin_amdgcn_make_buffer_rsrc(gran + GR_F, 0, 48 * 128, 0x00020000);
      if (!none((row | 8) == (idx2 | 8))) {
#pragma unroll
        for (int p = 0; p < 2; ++p)
          __builtin_amdgcn_raw_buffer_store_b128(u32x4{pack2(xf[p][0][0], xf[p][0][1]), pack2(xf[p][1][0], xf[p][1][1]), pack2(xf[p][2][0], xf[p][2][1]), pack2(xf[p][3][0], xf[p][3][1])},
              __builtin_amdgcn_make_buffer_rsrc(xout, 0, S_ * D_ * 2, 0x00020000), row + 8 * p == idx2 ? (idx2 * D_ + col) * 2 : S_ * D_ * 2, 0, 16);
        asm volatile("s_waitcnt vmcnt(0)" ::: "memory");
        __builtin_amdgcn_raw_buffer_store_b32(tag, rsf, lane == 0 ? oni * 128 : 48 * 128, 0, 16);
      }
      if (!none((row | 8) == ((int)fr1 | 8))) {
#pragma unroll 1
        for (int spin = 0; spin < SPIN_MAX; ++spin) {
          if (none(__builtin_amdgcn_raw_buffer_load_b32(rsf, (16 + oni * 2 + (lane & 1)) * 128, 0, 16) != tag)) break;
          asm volatile("" ::: "memory");
        }
#pragma unroll
        for (int p = 0; p < 2; ++p) __builtin_amdgcn_raw_buffer_store_b128(xres[p], rsu, row + 8 * p == (int)fr1 ? ((row + 8 * p) * D_ + col) * 2 : S_ * D_ * 2, 0, OAUX);
      }
    }
  }
  }
}
struct Args2 { Args l[2]; };
extern "C" __global__ __launch_bounds__(768, 3) void layers_kernel(const Args2 args) {
  __shared__ __attribute__((aligned(128))) char smem[163840];
  const unsigned long long t_entry = 0;
  const int tid = threadIdx.x, bid = blockIdx.x;
  int wid = __builtin_amdgcn_readfirstlane(tid >> 6);
  asm volatile("" : "+s"(wid));
  const ArgP ap = (ArgP)__builtin_amdgcn_kernarg_segment_ptr();
  layer_body(wid, bid, (unsigned)(uintptr_t)smem, ap, t_entry);
  LAYER_HERE;
  if (!LAY2) {
    asm volatile(AGAIN :: "{s[0:1]}"(ap + 1), "{s2}"(bid), "{v0}"(wid * 64) : "s90", "s91", "scc", "memory");
    __builtin_unreachable();
  }
}

extern "C" __global__ void end_marker_kernel() {}

extern "C" void kernel_launch(void* const* d_in, const int* in_sizes, int n_in, void* d_out, int out_size,
                              void* d_ws, size_t ws_size, hipStream_t stream) {
  const u16* x0 = (const u16*)d_in[0];
  const u16* wqkv = (const u16*)d_in[1];
  const u16* wo = (const u16*)d_in[2];
  const u16* w_r = (const u16*)d_in[4];
  const u16* wg = (const u16*)d_in[5];
  const u16* wu = (const u16*)d_in[6];
  const u16* wd = (const u16*)d_in[7];
  u16* out = (u16*)d_out;

  char* ws = (char*)d_ws;
  const size_t MB4 = (size_t)S_ * D_ * 2;
  u16* kv1 = (u16*)(ws);
  u16* kv2 = (u16*)(ws + 2 * MB4);
  u16* o1 = (u16*)(ws + 4 * MB4);
  u16* x1 = (u16*)(ws + 5 * MB4);
  u16* x3 = (u16*)(ws + 6 * MB4);
  unsigned* hand = (unsigned*)(ws + 7 * MB4);
  double* stats = (double*)(ws + 8 * MB4);
  unsigned* gran = (unsigned*)(ws + 9 * MB4);
  float* csf = (float*)(ws + 9 * MB4 + (1 << 20));
  u16* o2 = (u16*)(ws + 10 * MB4);
  u16* wc = (u16*)((((uintptr_t)ws + 11 * MB4 + 2047) & ~(uintptr_t)2047) + (((uintptr_t)wqkv + 128) & 2047));
  Args2 a;
  for (int l = 0; l < 2; ++l) {
    Args& r = a.l[l];
    r.lay = l; r.a = l ? x1 : x0; r.kv = l ? kv2 : kv1; r.o = l ? o2 : o1; r.xo = l ? x3 : x1; r.so = l ? stats + STAT_N : stats;
    r.W = wqkv; r.wc = wc; r.wg = wg; r.wu = wu; r.wd = wd; r.wo = wo; r.w_r = w_r; r.gran = gran; r.csf = csf; r.stats = stats; r.hand = hand; r.out = out;
  }
  layers_kernel<<<256, 768, 0, stream>>>(a);
}
```

```cpp
#include <hip/hip_runtime.h>
#include <hip/hip_bf16.h>
#include <stdint.h>
#include <type_traits>
#include <utility>

typedef unsigned short u16;
typedef short s16x4 __attribute__((ext_vector_type(4)));
typedef short s16x8 __attribute__((ext_vector_type(8)));
typedef float f32x16 __attribute__((ext_vector_type(16)));
typedef float f32x4 __attribute__((ext_vector_type(4)));
typedef unsigned int u32x4 __attribute__((ext_vector_type(4)));
typedef unsigned int u32x2 __attribute__((ext_vector_type(2)));
typedef __bf16 bf16x8 __attribute__((ext_vector_type(8)));
typedef __bf16 bf16x16 __attribute__((ext_vector_type(16)));

__device__ __forceinline__ int fresh_lane() {
  int l;
  asm volatile("v_mbcnt_lo_u32_b32 %0, -1, 0\n\tv_mbcnt_hi_u32_b32 %0, -1, %0" : "=v"(l));
  return l;
}
#define STAMP(k, i)
#define STAMPV(k, i, v)
#define CT0()
#define CT1(x)
#define LOGDEF
#define LOG()
#define LOGAT(k)
#define lk_set(k)
#define LOGOUT(slot)
#define TE(grp, k)
#define TEARG
#define TEPASS
constexpr int S_ = 2048, D_ = 1024, H_ = 32, DH_ = 32, F_ = 3584;

__device__ __forceinline__ int kpos(int r) { return (r & ~12) | ((r & 4) << 1) | ((r & 8) >> 1); }
__device__ __forceinline__ float bf2f(u16 v) { return __uint_as_float(((unsigned)v) << 16); }
__device__ __forceinline__ u16 f2bf(float f) {
  __hip_bfloat16 b = __float2bfloat16(f);
  return *reinterpret_cast<u16*>(&b);
}
typedef float f32x2 __attribute__((ext_vector_type(2)));
typedef __bf16 bf16x2 __attribute__((ext_vector_type(2)));
__device__ __forceinline__ unsigned pack2(float lo, float hi) {
  f32x2 v = {lo, hi};
  return __builtin_bit_cast(unsigned, __builtin_convertvector(v, bf16x2));
}
__device__ __forceinline__ float rbf(float f) { return __uint_as_float(pack2(f, f) << 16); }
__device__ __forceinline__ void rbf2(float& a, float& b) {
  const unsigned p = pack2(a, b);
  a = __uint_as_float(p << 16); b = __uint_as_float(p & 0xffff0000u);
}

__device__ __forceinline__ float fadd(float a, float b) { float r = a + b; asm("" : "+v"(r)); return r; }
template <int CTRL, int RMASK>
__device__ __forceinline__ float dpp_add(float v) {
  const int t = __builtin_amdgcn_update_dpp(0, __float_as_int(v), CTRL, RMASK, 0xf, false);
  return v + __int_as_float(t);
}
__device__ __forceinline__ float sum_hi3(float v) {
  v += __int_as_float(__builtin_amdgcn_update_dpp(0, __float_as_int(v), 0x128, 0xf, 0xf, false));
  const auto a = __builtin_amdgcn_permlane16_swap(__float_as_uint(v), __float_as_uint(v), false, false);
  v = __uint_as_float(a[0]) + __uint_as_float(a[1]);
  const auto b = __builtin_amdgcn_permlane32_swap(__float_as_uint(v), __float_as_uint(v), false, false);
  return __uint_as_float(b[0]) + __uint_as_float(b[1]);
}
__device__ __forceinline__ float wave_sum(float v) {
  v = dpp_add<0x111, 0xf>(v);
  v = dpp_add<0x112, 0xf>(v);
  v = dpp_add<0x114, 0xf>(v);
  v = dpp_add<0x118, 0xf>(v);
  v = dpp_add<0x142, 0xa>(v);
  v = dpp_add<0x143, 0xc>(v);
  return __int_as_float(__builtin_amdgcn_readlane(__float_as_int(v), 63));
}

template <int CTRL, int RMASK>
__device__ __forceinline__ unsigned dpp_umax(unsigned v) {
  const unsigned t = (unsigned)__builtin_amdgcn_update_dpp(0, (int)v, CTRL, RMASK, 0xf, false);
  return v > t ? v : t;
}
__device__ __forceinline__ unsigned wave_umax(unsigned v) {
  v = dpp_umax<0x111, 0xf>(v); v = dpp_umax<0x112, 0xf>(v); v = dpp_umax<0x114, 0xf>(v); v = dpp_umax<0x118, 0xf>(v);
  v = dpp_umax<0x142, 0xa>(v); v = dpp_umax<0x143, 0xc>(v);
  return (unsigned)__builtin_amdgcn_readlane((int)v, 63);
}

#define NREP 32
constexpr unsigned SENT = 0xFFFFFFFFu;
constexpr int GRAN_N = 256 * NREP;
constexpr int Y_N = F_ / 16 * D_;
constexpr int X2_N = D_ / 2;
constexpr int XS_N = 128;
constexpr int P4_N = 3 * D_ * 16;
constexpr int PA_N = H_ * 8 * 64;
constexpr int HAND_N = GRAN_N + Y_N + X2_N + XS_N + P4_N + PA_N;
constexpr int MD_O = 16 * 16 * 128 * 4;
constexpr int PK_O = MD_O;
constexpr int PP_O = PK_O + 128 * NREP;
constexpr int Y3_O = PP_O + H_ * S_;
constexpr int PS_N = Y3_O + Y_N;
constexpr int GR_ROW = 64, GR_COL = GR_ROW + 4 * S_, GR_KV = GR_COL + 16 * 16 * 192;
#define RO 16
#define RK 16
#define RX 1
constexpr int GR_O = GR_KV + 2 * 16 * 32 * RK, GR_X = GR_O + 2 * 16 * 64 * RO, GR_N = GR_X + 16 * 128 * RX;
constexpr int GR_F = GR_N + 256 * 64;
constexpr int STAT_N = 3 * S_;

__device__ __forceinline__ void st_ag(unsigned* p, unsigned v) { __hip_atomic_store(p, v, __ATOMIC_RELAXED, __HIP_MEMORY_SCOPE_AGENT); }
__device__ __forceinline__ unsigned ld_ag(const unsigned* p) { return __hip_atomic_load(p, __ATOMIC_RELAXED, __HIP_MEMORY_SCOPE_AGENT); }
__device__ __forceinline__ double ld_ag(const double* p) { return __hip_atomic_load(p, __ATOMIC_RELAXED, __HIP_MEMORY_SCOPE_AGENT); }
__device__ __forceinline__ unsigned f2word(float f) { const unsigned u = __float_as_uint(f); return u < 0xFFFFFFFEu ? u : 0xFFFFFFFEu; }
constexpr int SPIN_MAX = 1 << 18;
__device__ __forceinline__ unsigned umax(unsigned a, unsigned b) { return a > b ? a : b; }
__device__ __forceinline__ bool none(bool p) { return __builtin_amdgcn_ballot_w64(p) == 0ull; }

__device__ __forceinline__ void ln_from_sums(double s1, double s2, float& mean, float& var, float& mb, float& den) {
  const double md = s1 * (1.0 / 1024.0);
  const double vd = fma(-md, md, s2 * (1.0 / 1024.0));
  mean = (float)md; var = fmaxf((float)vd, 0.f);
  mb = rbf(mean);
  den = rbf(__builtin_amdgcn_sqrtf(rbf(rbf(var) + 1e-5f)));
}
__device__ __forceinline__ float frcp(float x) { return __builtin_amdgcn_rcpf(x); }
__device__ __forceinline__ void ln_apply2(float& a, float& b, float mb, float den, float rin) {
  a -= mb; b -= mb;
  rbf2(a, b);
  float qa = a * rin, qb = b * rin;
  qa = fmaf(fmaf(-qa, den, a), rin, qa);
  qb = fmaf(fmaf(-qb, den, b), rin, qb);
  rbf2(qa, qb);
  a = qa; b = qb;
}

#define CODE_ADDR(SYM, out64)                                                                                          \
  do {                                                                                                                 \
    unsigned lo_, hi_;                                                                                                 \
    asm volatile("s_getpc_b64 s[90:91]\n\ts_add_u32 s90, s90, " SYM "@rel32@lo+4\n\ts_addc_u32 s91, s91, " SYM          \
                 "@rel32@hi+12\n\ts_mov_b32 %0, s90\n\ts_mov_b32 %1, s91"                                             \
                 : "=s"(lo_), "=s"(hi_) :: "s90", "s91", "scc");                                                        \
    out64 = ((unsigned long long)hi_ << 32) | lo_;                                                                     \
  } while (0)

#define QNX 4
constexpr int QMX = 32 / QNX, QNG = 16 / QNX, QMG = 8 / QNG;

__device__ __forceinline__ unsigned touch(const void* p) { return *reinterpret_cast<const unsigned*>(p); }

__device__ __forceinline__ unsigned touch_code(unsigned long long lo, unsigned long long hi, int pc, int lane) {
  const unsigned long long p = lo + (unsigned)(pc * 8192 + lane * 128);
  return p + 4 <= hi ? *reinterpret_cast<__attribute__((address_space(1))) const unsigned*>(p) : 0u;
}

template <bool SC = false>
__device__ __forceinline__ void glds16s(const void* sbase, unsigned voff, unsigned lds_dst) {
  unsigned keep;
  if (SC)
    asm volatile("s_mov_b32 %0, m0\n\ts_mov_b32 m0, %3\n\ts_nop 2\n\tglobal_load_lds_dwordx4 %1, %2 sc1\n\ts_mov_b32 m0, %0"
                 : "=&s"(keep) : "v"(voff), "s"(sbase), "s"(lds_dst) : "memory");
  else
  asm volatile("s_mov_b32 %0, m0\n\ts_mov_b32 m0, %3\n\ts_nop 2\n\tglobal_load_lds_dwordx4 %1, %2\n\ts_mov_b32 m0, %0"
               : "=&s"(keep) : "v"(voff), "s"(sbase), "s"(lds_dst) : "memory");
}
template <bool SC>
__device__ __forceinline__ void glds16x4(const void* sbase, unsigned v0, unsigned v1, unsigned v2, unsigned v3, unsigned lds_dst) {
  unsigned keep;
  if (SC)
    asm volatile("s_mov_b32 %0, m0\n\ts_mov_b32 m0, %6\n\ts_nop 2\n\tglobal_load_lds_dwordx4 %1, %5 sc1\n\tglobal_load_lds_dwordx4 %2, %5 offset:1024 sc1\n\t"
                 "global_load_lds_dwordx4 %3, %5 offset:2048 sc1\n\tglobal_load_lds_dwordx4 %4, %5 offset:3072 sc1\n\ts_mov_b32 m0, %0"
                 : "=&s"(keep) : "v"(v0), "v"(v1), "v"(v2), "v"(v3), "s"(sbase), "s"(__builtin_amdgcn_readfirstlane(lds_dst)) : "memory");
  else
    asm volatile("s_mov_b32 %0, m0\n\ts_mov_b32 m0, %6\n\ts_nop 2\n\tglobal_load_lds_dwordx4 %1, %5\n\tglobal_load_lds_dwordx4 %2, %5 offset:1024\n\t"
                 "global_load_lds_dwordx4 %3, %5 offset:2048\n\tglobal_load_lds_dwordx4 %4, %5 offset:3072\n\ts_mov_b32 m0, %0"
                 : "=&s"(keep) : "v"(v0), "v"(v1), "v"(v2), "v"(v3), "s"(sbase), "s"(__builtin_amdgcn_readfirstlane(lds_dst)) : "memory");
}
constexpr float QSCALE = 0.17677669529663687f * 1.4426950408889634f;

__device__ __forceinline__ unsigned min4(u32x4 v) { const unsigned a = v[0] < v[1] ? v[0] : v[1], b = v[2] < v[3] ? v[2] : v[3]; return a < b ? a : b; }
#define LDS_AT(T, a) ((__attribute__((address_space(3))) T*)(uintptr_t)(a))
__device__ __forceinline__ u32x4 lds_rd4(unsigned a) { return *LDS_AT(const volatile u32x4, a); }
__device__ __forceinline__ void lds_wr(unsigned a, unsigned v) { *LDS_AT(volatile unsigned, a) = v; }
__device__ __forceinline__ void split3(float v, unsigned& p0, unsigned& p1, unsigned& p2) {
  p0 = pack2(v, v) & 0xffffu; v -= __uint_as_float(p0 << 16);
  p1 = pack2(v, v) & 0xffffu; v -= __uint_as_float(p1 << 16);
  p2 = pack2(v, v) & 0xffffu;
}

#define PIN() __builtin_amdgcn_sched_barrier(0)
template <int B, class F, int... I>
__device__ __forceinline__ void static_for_(F&& f, std::integer_sequence<int, I...>) { (f(std::integral_constant<int, B + I>{}), ...); }
template <int B, int E, class F>
__device__ __forceinline__ void static_for(F&& f) { static_for_<B>(f, std::make_integer_sequence<int, E - B>{}); }

#define RESAT 20
#define LNPRE 1
#define TPRE 60
#define TPSL 0
#define OSCH 0
#define FCW 0
#define FCPRE 44
#define FCSLP 2
#define SIGAT 24
#define XPRIO 1
#define OPD 6
#define LAUX 16
#define CSLP 2
#define EPRIO 3
#define OPRIO 1
#define XSLP 1
#define ROT 0
#define CSL1 8
#define XAUX 16
__device__ __forceinline__ void oproj_tail(const bool RES2, const int tid, const unsigned lbase, const unsigned flg, const int tmi, const int tni, const int idx,
                                           unsigned* osig, unsigned* xsig, const unsigned tag,
                                           const u16* A, u16* out,
                                           const u16* res, const u16* __restrict__ w_r, double* stats,
                                           const double* stats_in, unsigned* hand, u32x4 (&xres)[2] TEARG) {
  constexpr int ldw = D_, PD = OPD;
  const int lane = tid & 63, wid = __builtin_amdgcn_readfirstlane(tid >> 6);
  const int m0 = tmi * 128, n0 = tni * 64;
  const int dk = RES2 ? 7 : 3;
  if (wid == 0) STAMP(dk, 0);
  TE(1, 3);
  asm volatile("" : "+s"(A) :: "memory");
  PIN();
  const int row = m0 + (wid & 3) * 32 + (wid >> 2) * 16 + (lane >> 3), col = n0 + (lane & 7) * 8;
  const unsigned tb = lbase + 147456;
  typedef __attribute__((address_space(1))) double* gdp;
  typedef __attribute__((address_space(1))) unsigned* gup;
  unsigned tr, oad;
  gdp pst;
  gup pps;
  int xo;
  auto setup = [&]() __attribute__((always_inline)) {
    tr = tb + (row - m0) * 128 + (((lane ^ (lane >> 3)) & 7) << 4);
    pst = (gdp)(stats + ((lane & 3) == 3 ? 0 : (lane & 3)) * S_ + row + 8 * ((lane >> 2) & 1));
    pps = (gup)(hand + 2 * HAND_N + ((tmi * 16 + tni) * 128 + row - m0 + 8 * ((lane >> 2) & 1)) * 4 + (lane & 3));
    xo = (row * ldw + col) * 2;
    asm volatile("" : "+v"(tr), "+v"(pst), "+v"(pps), "+v"(xo));
    PIN();
  };
  {
    oad = lane == 0 ? flg + 24 : flg + 64 + lane * 4;
    gup ps = (gup)(osig + lane * 64);
    asm volatile("" : "+v"(oad), "+v"(ps));
    asm volatile("s_waitcnt vmcnt(1)" ::: "memory");
    TE(2, 1);
    const unsigned old = __hip_atomic_fetch_add(LDS_AT(unsigned, oad), 1u, __ATOMIC_RELAXED, __HIP_MEMORY_SCOPE_WORKGROUP);
    if (__builtin_amdgcn_readfirstlane(old) == 7u && lane < RO) __hip_atomic_store(ps, tag, __ATOMIC_RELAXED, __HIP_MEMORY_SCOPE_AGENT);
    PIN();
    TE(2, 2);
    if (wid == 0) STAMP(dk, 12);
  }
  auto announce = [&]() __attribute__((always_inline)) {
    asm volatile("" ::: "memory");
    PIN();
    __builtin_amdgcn_s_barrier();
    PIN();
    if (wid == 0) STAMP(dk, 1);
    TE(2, 3); TE(4, 0); TE(7, 1);
  };
  u32x4 rwr;
  auto sums = [&](const u32x4& x0, const u32x4& x1) __attribute__((always_inline)) {
    float f[2][3];
#pragma unroll
    for (int p = 0; p < 2; ++p) {
      f[p][0] = f[p][1] = f[p][2] = 0.f;
#pragma unroll
      for (int e = 0; e < 4; ++e) {
        const unsigned xu = p ? x1[e] : x0[e], wu = rwr[e];
        const bf16x2 x = __builtin_bit_cast(bf16x2, xu);
        f[p][0] = __builtin_amdgcn_fdot2_f32_bf16(x, __builtin_bit_cast(bf16x2, 0x3f803f80u), f[p][0], false);
        f[p][1] = __builtin_amdgcn_fdot2_f32_bf16(x, x, f[p][1], false);
        f[p][2] = __builtin_amdgcn_fdot2_f32_bf16(x, __builtin_bit_cast(bf16x2, wu), f[p][2], false);
      }
    }
    auto addx = [&](float keep, float give, auto ctl) {
      return keep + __int_as_float(__builtin_amdgcn_update_dpp(0, __float_as_int(give), decltype(ctl)::value, 0xf, 0xf, false));
    };
    const bool b2 = lane & 4, b1 = lane & 2, z0 = (lane & 3) == 0, z1 = (lane & 3) == 1;
    float g[3];
#pragma unroll
    for (int c = 0; c < 3; ++c) g[c] = addx(b2 ? f[1][c] : f[0][c], b2 ? f[0][c] : f[1][c], std::integral_constant<int, 0x141>{});
    const float u1 = addx(b1 ? g[2] : g[0], b1 ? g[0] : g[2], std::integral_constant<int, 0x4e>{});
    const float u2 = addx(g[1], g[1], std::integral_constant<int, 0x4e>{});
    return addx(z1 ? u2 : u1, z0 ? u2 : u1, std::integral_constant<int, 0xb1>{});
  };
  if (wid < 4) {
    const __amdgpu_buffer_rsrc_t rsa = __builtin_amdgcn_make_buffer_rsrc(const_cast<u16*>(A), 0, S_ * D_ * 2, 0x00020000);
    int sa0 = (tmi * 4 + wid) * 65536, va0 = (lane >> 4) * 512 + (lane & 15) * 16;
    asm volatile("" : "+v"(va0));
    asm volatile("" : "+s"(sa0));
    const int rot = ROT ? tni & 7 : 0;
    auto lda = [&](int i, int h) { return __builtin_amdgcn_raw_buffer_load_b128(rsa, va0 + (i & 1) * 2048 + h * 256, sa0 + (((i >> 2) + rot) & 7) * 8192 + (i & 2) * 2048, LAUX); };
    unsigned wad[4], tw[4];
    f32x4 acc[2][4];
    const int k = (lane >> 5) * 16 + ((lane >> 2) & 7);
#pragma unroll
    for (int j = 0; j < 4; ++j) {
      wad[j] = lbase + k * 128 + (((2 * j) ^ (k & 6)) << 4) + 8 * (lane & 3) + rot * 16384;
      tw[j] = tb + wid * 4096 + (lane & 15) * 128 + (((2 * j + (lane >> 5)) ^ (lane & 7)) << 4) + ((lane >> 4) & 1) * 8;
      asm volatile("" : "+v"(wad[j]), "+v"(tw[j]));
    }
    auto ldw_ = [&](int i, int j) {
      const unsigned a = wad[j] + (i & 3) * 4096;
      s16x4 lo = __builtin_amdgcn_ds_read_tr16_b64_v4i16(LDS_AT(s16x4, a));
      s16x4 h4 = __builtin_amdgcn_ds_read_tr16_b64_v4i16(LDS_AT(s16x4, a + 1024));
      return (s16x8)__builtin_shufflevector(lo, h4, 0, 1, 2, 3, 4, 5, 6, 7);
    };
    u32x4 id[2];
#pragma unroll
    for (int j = 0; j < 2; ++j) {
#pragma unroll
      for (int e = 0; e < 4; ++e) id[j][e] = (lane >> 4) == 2 * j + ((lane >> 3) & 1) && ((lane & 7) >> 1) == e ? ((lane & 1) ? 0x3f800000u : 0x3f80u) : 0u;
      asm volatile("" : "+v"(id[j]));
    }
    unsigned rad = tb + wid * 4096 + (lane & 15) * 128 + (((lane >> 4) ^ (lane & 7)) << 4), rfa = flg + 36 + wid;
    asm volatile("" : "+v"(rad), "+v"(rfa));
    u32x4 rb[2][2];
    rwr = *reinterpret_cast<const u32x4*>(w_r + col);
    setup();
    asm volatile("" : "+v"(rwr));
    announce();
    u32x4 af[PD][2];
    s16x8 wf[2][4];
#pragma unroll
    for (int i = 0; i < PD; ++i) { af[i][0] = lda(i, 0); af[i][1] = lda(i, 1); }
    PIN();
    __builtin_amdgcn_s_setprio(OPRIO);
#pragma unroll
    for (int q = 0; q < 4; ++q) rb[q >> 1][q & 1] = lds_rd4((rad ^ ((q & 1) * 64)) + (q >> 1) * 2048);
    PIN();
#pragma unroll
    for (int j = 0; j < 4; ++j)
#pragma unroll
      for (int h = 0; h < 2; ++h) { asm volatile("v_mfma_f32_16x16x32_bf16 %0, %1, %2, 0" : "=&v"(acc[h][j]) : "v"(id[j & 1]), "v"(rb[h][j >> 1])); PIN(); }
#pragma unroll 1
    for (int spin = 0; spin < SPIN_MAX; ++spin)
      if (__builtin_amdgcn_readfirstlane(*LDS_AT(const volatile unsigned, flg)) >= 40u) break;
    asm volatile("" : "+v"(va0) :: "memory");
    PIN();
#pragma unroll
    for (int j = 0; j < 4; ++j) wf[0][j] = ldw_(0, j);
    s16x4 wlo;
    static_for<0, 32>([&](auto ic) __attribute__((always_inline)) {
      constexpr int i = decltype(ic)::value;
      if (i == SIGAT) { __hip_atomic_fetch_add(LDS_AT(unsigned, oad + 4), 1u, __ATOMIC_RELAXED, __HIP_MEMORY_SCOPE_WORKGROUP); PIN(); }
#pragma unroll
      for (int g = 0; g < 8; ++g) {
        const int j = OSCH ? g & 3 : g >> 1, h = OSCH ? g >> 2 : g & 1;
        asm volatile("v_mfma_f32_16x16x32_bf16 %0, %1, %2, %0" : "+v"(acc[h][j]) : "v"(wf[i & 1][j]), "v"(af[i % PD][h]));
        PIN();
        if (i == 15 && !(g & 1)) { wad[g >> 1] += 65536; asm volatile("" : "+v"(wad[g >> 1])); }
        if (i + 1 < 32) {
          const s16x4 v = __builtin_amdgcn_ds_read_tr16_b64_v4i16(LDS_AT(s16x4, wad[g >> 1] + ((i + 1) & 15) * 4096 + (g & 1) * 1024));
          if (g & 1) wf[(i + 1) & 1][g >> 1] = (s16x8)__builtin_shufflevector(wlo, v, 0, 1, 2, 3, 4, 5, 6, 7);
          else wlo = v;
          PIN();
        }
        if (OSCH ? (g & 3) == 3 : g >= 6) { if (i + PD < 32) { af[i % PD][h] = lda(i + PD, h); PIN(); } }
        if (!OSCH && i + 1 < 32 && i != SIGAT && i + 1 != SIGAT) {
          if (g == 3) { __builtin_amdgcn_s_waitcnt(0xc47f); PIN(); }
          if (g == 7 && i + PD < 32) { __builtin_amdgcn_s_waitcnt(0x0470 | (2 * PD - 2)); PIN(); }
          if (g == 7 && i + PD >= 32) { __builtin_amdgcn_s_waitcnt(0xc47f); PIN(); }
        }
      }
    });
    asm volatile("s_nop 3" : "+v"(acc[0][0]), "+v"(acc[1][0]));
    TE(4, 1); TE(7, 2);
    if (wid == 0) STAMP(dk, 17);
    if (wid == 1 && !RES2) STAMP(4, 0);
#pragma unroll
    for (int j = 0; j < 4; ++j)
#pragma unroll
      for (int h = 0; h < 2; ++h) {
        *LDS_AT(u32x2, tw[j] + h * 2048) = u32x2{pack2(acc[h][j][0], acc[h][j][1]), pack2(acc[h][j][2], acc[h][j][3])};
        PIN();
      }
    asm volatile("" ::: "memory");
    *LDS_AT(volatile unsigned char, rfa) = (unsigned char)2;
#pragma unroll
    for (int p = 0; p < 2; ++p) xres[p] = lds_rd4(tr + p * 1024);
    PIN();
    if (!RES2) {
      const __amdgpu_buffer_rsrc_t rso = __builtin_amdgcn_make_buffer_rsrc(out, 0, S_ * D_ * 2, 0x00020000);
      const __amdgpu_buffer_rsrc_t rsf = __builtin_amdgcn_make_buffer_rsrc(xsig + wid, 0, RX * 512, 0x00020000);
      const u32x4 x2 = lds_rd4(tr + 2048), x3 = lds_rd4(tr + 3072);
      PIN();
#pragma unroll
      for (int p = 0; p < 4; ++p) { __builtin_amdgcn_raw_buffer_store_b128(p < 2 ? xres[p] : (p < 3 ? x2 : x3), rso, xo + p * (8 * ldw * 2), 0, XAUX); PIN(); }
      TE(4, 2); TE(5, 0);
      const float s0 = sums(xres[0], xres[1]), s1 = sums(x2, x3);
      if ((lane & 3) != 3) { __builtin_amdgcn_global_atomic_fadd_f64(pst, (double)s0); __builtin_amdgcn_global_atomic_fadd_f64(pst + 16, (double)s1); }
      PIN();
      TE(4, 3); TE(5, 1);
      asm volatile("s_waitcnt vmcnt(2)" ::: "memory");
      __builtin_amdgcn_raw_buffer_store_b32(tag, rsf, lane * 512 + 256, 0, 16);
      *LDS_AT(volatile unsigned char, rfa) = (unsigned char)3;
      PIN();
      TE(5, 2);
      asm volatile("s_waitcnt vmcnt(1)" ::: "memory");
      __builtin_amdgcn_raw_buffer_store_b32(tag, rsf, lane * 512, 0, 16);
      PIN();
      TE(5, 3);
      __builtin_amdgcn_s_setprio(0);
      return;
    }
    TE(4, 2); TE(5, 0);
  } else {
    u32x4 rres[4];
    double is1[4], is2[4];
#pragma unroll
    for (int p = 0; p < 4; ++p)
      rres[p] = __builtin_amdgcn_raw_buffer_load_b128(__builtin_amdgcn_make_buffer_rsrc(const_cast<u16*>(res), 0, S_ * D_ * 2, 0x00020000), ((row - 16 + 8 * p) * ldw + col) * 2, 0, 16);
    rwr = *reinterpret_cast<const u32x4*>(w_r + col);
#pragma unroll
    for (int p = 0; p < 4; ++p) { is1[p] = ld_ag(stats_in + row - 16 + 8 * p); is2[p] = ld_ag(stats_in + S_ + row - 16 + 8 * p); }
    PIN();
    setup();
    if (RES2) {
#pragma unroll
      for (int p = 0; p < 4; ++p) {
        float mean, var, mb, den;
        ln_from_sums(is1[p], is2[p], mean, var, mb, den);
        const float rin = frcp(den);
#pragma unroll
        for (int e = 0; e < 4; ++e) {
          const f32x2 x = {__uint_as_float(rres[p][e] << 16), __uint_as_float(rres[p][e] & 0xffff0000u)};
          const f32x2 h = (x - f32x2{mb, mb}) * f32x2{rin, rin} + x;
          rres[p][e] = pack2(h[0], h[1]);
        }
        asm volatile("" : "+v"(rres[p]));
      }
      PIN();
    }
    if ((idx >> 5) == tmi * 4 + wid - 4) {
      const int dr = idx - (row - 16);
      u32x4 qq;
#pragma unroll 1
      for (int i = 0; i < SPIN_MAX; ++i) {
        qq = __builtin_amdgcn_raw_buffer_load_b128(__builtin_amdgcn_make_buffer_rsrc(hand, 0, HAND_N * 4, 0x00020000), (GRAN_N + Y_N) * 4 + col * 2, 0, 16);
        if (none(umax(umax(qq[0], qq[1]), umax(qq[2], qq[3])) == SENT)) break;
        asm volatile("" ::: "memory");
      }
#pragma unroll
      for (int p = 0; p < 4; ++p)
#pragma unroll
        for (int e = 0; e < 4; ++e) rres[p][e] = dr == 8 * p ? qq[e] : rres[p][e];
    }
#pragma unroll 1
    for (int i = 0; i < 4 * SPIN_MAX; ++i)
      if (__builtin_amdgcn_readfirstlane(*LDS_AT(const volatile unsigned, flg + 16)) >= 12u) break;
#pragma unroll
    for (int p = 0; p < 4; ++p) *LDS_AT(volatile u32x4, tr - 2048 + p * 1024) = rres[p];
    asm volatile("s_waitcnt lgkmcnt(0)" ::: "memory");
    announce();
    TE(4, 1); TE(7, 2);
    asm volatile("s_waitcnt vmcnt(0)" : "+v"(rwr) :: "memory");
    if (!RES2) return;
    __builtin_amdgcn_s_sleep(TPRE);
#pragma unroll 1
    for (int i = 0; i < 4 * SPIN_MAX; ++i) {
      if (__builtin_amdgcn_readfirstlane(*LDS_AT(const volatile unsigned char, flg + 32 + wid)) == 2u) break;
      __builtin_amdgcn_s_sleep(TPSL);
    }
    asm volatile("" ::: "memory");
    __builtin_amdgcn_s_setprio(XPRIO);
    PIN();
    TE(4, 2); TE(5, 0);
#pragma unroll
    for (int p = 0; p < 2; ++p) xres[p] = lds_rd4(tr + p * 1024);
  }
  TE(4, 3); TE(7, 3);
  const float sv = sums(xres[0], xres[1]);
  if (wid == 0) STAMP(dk, 14);
  if ((lane & 3) != 3) __hip_atomic_store(pps, f2word(sv), __ATOMIC_RELAXED, __HIP_MEMORY_SCOPE_AGENT);
  PIN();
  __builtin_amdgcn_s_setprio(0);
  __hip_atomic_fetch_add(LDS_AT(unsigned, oad), 1u, __ATOMIC_RELAXED, __HIP_MEMORY_SCOPE_WORKGROUP);
  TE(5, 1); TE(5, 2);
  PIN();
}

#define MFMA32(a, b, c) __builtin_amdgcn_mfma_f32_32x32x16_bf16(a, b, c, 0, 0, 0)
#define MFMA16(a, b, c) __builtin_amdgcn_mfma_f32_16x16x32_bf16(a, b, c, 0, 0, 0)

#define PLO(x) __builtin_bit_cast(bf16x16, __builtin_shufflevector(x, x, 0, 1, 2, 3, 4, 5, 6, 7))
#define PHI(x) __builtin_bit_cast(bf16x16, __builtin_shufflevector(x, x, 8, 9, 10, 11, 12, 13, 14, 15))
#define SMF32(a, b, c) __builtin_amdgcn_smfmac_f32_32x32x32_bf16(__builtin_bit_cast(bf16x8, a), b, c, sidx, 0, 0)
#define SMF16(a, b, c) __builtin_amdgcn_smfmac_f32_16x16x64_bf16(__builtin_bit_cast(bf16x8, a), b, c, sidx, 0, 0)

__device__ __forceinline__ void patch_kv(unsigned tab, int r, s16x8& k0, s16x8& k1, s16x8& v0, s16x8& v1, bool on = true) {
  const int lane = fresh_lane(), hi = lane >> 5;
  if (on && (lane & 31) == kpos(r)) {
    k0 = *LDS_AT(const s16x8, tab + hi * 16); k1 = *LDS_AT(const s16x8, tab + 32 + hi * 16);
  }
  const short vv = *LDS_AT(const short, tab + 128 + (lane & 31) * 2);
  const int js = (on && hi == ((r >> 2) & 1)) ? (r >> 4) * 8 + ((r >> 3) & 1) * 4 + (r & 3) : -1;
#pragma unroll
  for (int e = 0; e < 8; ++e) { v0[e] = js == e ? vv : v0[e]; v1[e] = js == 8 + e ? vv : v1[e]; }
}

__device__ __forceinline__ void attn_safe(const u16* KV, int tile0, s16x8 q0, s16x8 q1,
                                       f32x16& o, float& l_out, int ptile, int prow, unsigned ptab) {
  const int lane = fresh_lane();
  float m = -INFINITY, l = 0.f;
  const int sidx = 0xDDDDDDDD;
  const __amdgpu_buffer_rsrc_t rs = __builtin_amdgcn_make_buffer_rsrc(const_cast<u16*>(KV), 0, 2 * S_ * D_ * 2, 0x00020000);
#pragma unroll
  for (int e = 0; e < 16; ++e) o[e] = 0.f;
  for (int tt = 0; tt < 64; ++tt) {
    const int t = tile0 + tt, off = t * 2048 + lane * 16;
    s16x8 k0 = __builtin_bit_cast(s16x8, __builtin_amdgcn_raw_buffer_load_b128(rs, off, 0, 16));
    s16x8 k1 = __builtin_bit_cast(s16x8, __builtin_amdgcn_raw_buffer_load_b128(rs, off + 1024, 0, 16));
    s16x8 v0 = __builtin_bit_cast(s16x8, __builtin_amdgcn_raw_buffer_load_b128(rs, off + S_ * D_ * 2, 0, 16));
    s16x8 v1 = __builtin_bit_cast(s16x8, __builtin_amdgcn_raw_buffer_load_b128(rs, off + S_ * D_ * 2 + 1024, 0, 16));
    if (t == ptile) patch_kv(ptab, prow, k0, k1, v0, v1);
    f32x16 sc;
#pragma unroll
    for (int e = 0; e < 16; ++e) sc[e] = 0.f;
    sc = MFMA32(k0, q0, sc); sc = MFMA32(k1, q1, sc);
    float mt = sc[0];
#pragma unroll
    for (int e = 1; e < 16; ++e) mt = fmaxf(mt, sc[e]);
    mt = fmaxf(mt, __shfl_xor(mt, 32, 64));
    const float mn = fmaxf(m, mt);
    const float alpha = __builtin_amdgcn_exp2f(m - mn);
    m = mn;
#pragma unroll
    for (int e = 0; e < 16; ++e) sc[e] = __builtin_amdgcn_exp2f(sc[e] - mn);
    float ls = 0.f;
#pragma unroll
    for (int e = 0; e < 16; ++e) ls += __uint_as_float(__float_as_uint(sc[e]) & 0xffff0000u);
    l = l * alpha + ls;
#pragma unroll
    for (int e = 0; e < 16; ++e) o[e] *= alpha;
    o = SMF32(v0, PLO(sc), o); o = SMF32(v1, PHI(sc), o);
  }
  l_out = l + __shfl_xor(l, 32, 64);
}

template <int CTRL, int RMASK>
__device__ __forceinline__ float dpp_fmax(float v) {
  return fmaxf(v, __int_as_float(__builtin_amdgcn_update_dpp(__float_as_int(v), __float_as_int(v), CTRL, RMASK, 0xf, false)));
}
__device__ __forceinline__ float wave_fmax(float v) {
  asm volatile("s_nop 1\n\tv_max_f32_dpp %0, %0, %0 row_shr:1 row_mask:0xf bank_mask:0xf\n\ts_nop 1\n\tv_max_f32_dpp %0, %0, %0 row_shr:2 row_mask:0xf bank_mask:0xf\n\t"
               "s_nop 1\n\tv_max_f32_dpp %0, %0, %0 row_shr:4 row_mask:0xf bank_mask:0xf\n\ts_nop 1\n\tv_max_f32_dpp %0, %0, %0 row_shr:8 row_mask:0xf bank_mask:0xf\n\t"
               "s_nop 1\n\tv_max_f32_dpp %0, %0, %0 row_bcast:15 row_mask:0xa bank_mask:0xf\n\ts_nop 1\n\tv_max_f32_dpp %0, %0, %0 row_bcast:31 row_mask:0xc bank_mask:0xf\n\ts_nop 0"
               : "+v"(v));
  return __int_as_float(__builtin_amdgcn_readlane(__float_as_int(v), 63));
}
__device__ __forceinline__ float sum_hi5(float v) {
  v = sum_hi3(v);
  v += __int_as_float(__builtin_amdgcn_update_dpp(0, __float_as_int(v), 0x124, 0xf, 0xf, false));
  v += __int_as_float(__builtin_amdgcn_update_dpp(0, __float_as_int(v), 0x122, 0xf, 0xf, false));
  return v;
}

#define PSLP 8
#define WSLP 3
#define PRE 28
#define PRE1 8
#define PRE5 8
#define W3SLP 4
#define FSTART 10
#define BAL1 false
#define FPRE0 20
#define FPRE 16
#define FSLP 4
#define OAUX 16
#define BALO 1
#define OMX 4
#define T0 2u
#define TS 2u
#define TW 10
#define HW 2
#define HP1 1
#define POLLAT 11
#define X2AT 2
#define HPRIO 3
#define SSLP 1
#define SPRE 6
#define QOV 2
#define FLAT 4
#define WGAPS {0, 1, 7, 8, 9, 10, 11, 12}
#define KVS 4
#define CKAT 3
#define VEARLY 2
#define GNS 12
#define GNS2 20
#define PNS 12
#define GEV 4
#define G0 5u
#define GS 2u
#define IAT 1u
#define WDAT 10u
#define W3AT 11u
#define WOAT 12u
#define YSLP 2
#define ISLP 8
#define X3SLP 1
#define RPRE 8
#define RSLP 2
#define KSLP 2
#define UAT 13u
#define UHW 0
#define JDL 4000u
#define PPSLP 2
#define PKPRE 24
#define PKSLP 4
#define XPRE 8
#define RPRE2 8
#define XRSLP 2
#define MPRE 20
#define KEARLY 0
#define REFINE 1
#define FPRE2 24
#define OSLP 2
#define YD1 150
#define YD2 180
#define KDT 200
#define XDT 65
#define R2PRIO 0
#define LNPRIO 1
#define K6 2
#define MPR2 8
#define MSLP 4
#define BARG 0
struct Args {
  long long lay;
  const u16* a; u16* kv; u16* o; u16* xo; double* so;
  const u16* W; u16* wc;
  const u16 *wg, *wu, *wd, *wo, *w_r;
  unsigned* gran; float* csf; double* stats; unsigned* hand; u16* out;
};
typedef __attribute__((address_space(4))) const Args* ArgP;
#define WCAT 3
#define CAP0 1
#define PRE4 50
#define PRE7 20
#define UARR 12u
#define GF0 10
#define XGATE 8
#define KVW 2
#define ASLP 1
#define B15 3
#define W5AT 10
#define KO 5
#define PKM 0
#define PFIRST 1
#define LPOLL 3
#define QW 1
#define QU 1
#define PRO 2
#define H2SK 1
#define HS 2
#define HT 0
#define WMAP 0x32100333
#define SMAP 0
#define STCH 6
#define KPOLL 10
#define TPRIO 2
#define PG1 6
#define PG2 12
#define TSLP 9
__device__ __forceinline__ bool slow_cols(const u16* W, int tni) { return ((((unsigned)(uintptr_t)W >> 7) + tni) & 7) == 3; }
#define ARGS_HERE                                                                                                      \
  ArgP ap_ = ap; asm volatile("" : "+s"(ap_));                                                                          \
  const u16* A = ap_->a; const u16* __restrict__ W = ap_->W; u16* KV = ap_->kv; u16* Of = ap_->o;        \
  const u16* __restrict__ wg = ap_->wg; const u16* __restrict__ wu = ap_->wu; const u16* __restrict__ wd = ap_->wd;       \
  const u16* __restrict__ wo = ap_->wo; const u16* __restrict__ w_r = ap_->w_r; unsigned* gran = ap_->gran;              \
  float* csf = ap_->csf; const double* stats = ap_->stats; double* stats_out = ap_->so; unsigned* hand = ap_->hand; \
  u16* xout = ap_->xo; u16* __restrict__ out = ap_->out
#define AGAIN "s_getpc_b64 s[90:91]\n\ts_add_u32 s90, s90, layers_kernel@rel32@lo+4\n\ts_addc_u32 s91, s91, layers_kernel@rel32@hi+12\n\ts_setpc_b64 s[90:91]"
#define KEPT(c)                                                                                                        \
  c "{v[104:107]}"(gu[0]), c "{v[108:111]}"(gu[1]), c "{v[112:115]}"(gu[2]), c "{v[116:119]}"(gu[3]), c "{v[120:123]}"(gu[4]),      \
  c "{v[124:127]}"(gu[5]), c "{v[128:131]}"(gu[6]), c "{v[132:135]}"(gu[7]), c "{v[136:139]}"(gu[8]), c "{v[140:143]}"(gu[9]),      \
  c "{v[144:147]}"(gu[10]), c "{v[148:151]}"(gu[11]), c "{v[152:155]}"(gu[12]), c "{v[156:159]}"(gu[13]), c "{v[160:163]}"(gu[14]), \
  c "{v[164:167]}"(gu[15])
#define LAYER_HERE ArgP apl_ = ap; asm volatile("" : "+s"(apl_)); const bool LAY2 = apl_->lay != 0
__device__ __forceinline__ void layer_body(const int wid, const int bid, const unsigned lbase, const ArgP ap, const unsigned long long t_entry) {
  constexpr int K = 1024, NKT = 16, NST = 3, ABYTES = 16384, WBYTES = 24576, STB = ABYTES + WBYTES, ldw = 3 * D_;
  constexpr int TABA = NST * STB, ALPHA = TABA + 2048, CRED = ALPHA + 512;
  constexpr int ASB = 16384, FLG = 8 * ASB + 256;
  constexpr int DUM = FLG + 64, TAB = DUM + 256, RED = TAB + 512, SACT = RED + 4096, RED2 = SACT + 1024, PL = RED2 + 2048, XML = PL + 512, UGL = XML + 4096, SPM = UGL + 1024, QOFF = 9 * ASB;
  constexpr int TABW = RED;
  static_assert(SPM + 2048 <= QOFF && CRED <= 8 * ASB && RED + 8192 <= QOFF && RED % 16 == 0, "lds");
#define XPERM 1
  const int xcd = XPERM == 0 ? bid & 7 : (bid & 1) * 4 + (((bid >> 1) + (bid & 1) * (XPERM - 1)) & 3), loc = bid >> 3;
  const int tmi = (xcd / QNG) * QMX + loc / QNX, tni = (xcd % QNG) * QNX + loc % QNX;
  const int m0 = tmi * 128, n0 = tni * 64;
  const int omi = (xcd / (OMX / 2)) * OMX + loc / (32 / OMX), oni = (xcd % (OMX / 2)) * (32 / OMX) + loc % (32 / OMX);
  constexpr unsigned VOFF = (unsigned)S_ * D_ * 2;
  auto tile_at = [&](unsigned n) __attribute__((always_inline)) {
#pragma unroll 1
    for (int i = 0; i < 4 * SPIN_MAX; ++i) {
      if (__builtin_amdgcn_readfirstlane(*LDS_AT(const volatile unsigned, lbase + FLG + 36)) == n * 0x01010101u) break;
      __builtin_amdgcn_s_sleep(1);
    }
    asm volatile("" ::: "memory");
    PIN();
  };
  auto all_ge = [&](unsigned w, unsigned n) { return ((w + (128u - n) * 0x01010101u) & 0x80808080u) == 0x80808080u; };
  if (wid >= 8) {
    u32x4 gu[16];
    LAYER_HERE;
    constexpr int dk = 6;
    const int hw = wid - 8, b = bid;
    int nb = 0;
    constexpr int lim = NKT - 1;
    auto tick_nw = [&]() {
      PIN();
      if (nb < lim) { __builtin_amdgcn_s_barrier(); ++nb; } else __builtin_amdgcn_s_sleep(2);
      asm volatile("" ::: "memory");
      PIN();
    };
    auto tick = [&]() {
      asm volatile("s_waitcnt lgkmcnt(0)" ::: "memory");
      tick_nw();
    };
    auto fold16 = [&](f32x2 y0, f32x2 y1) __attribute__((always_inline)) {
      const auto q0 = __builtin_amdgcn_permlane32_swap(__float_as_uint(y0[0]), __float_as_uint(y1[0]), false, false);
      const auto q1 = __builtin_amdgcn_permlane32_swap(__float_as_uint(y0[1]), __float_as_uint(y1[1]), false, false);
      const auto q = __builtin_amdgcn_permlane16_swap(__float_as_uint(__uint_as_float(q0[0]) + __uint_as_float(q0[1])),
                                                      __float_as_uint(__uint_as_float(q1[0]) + __uint_as_float(q1[1])), false, false);
      return __uint_as_float(q[0]) + __uint_as_float(q[1]);
    };
    unsigned best, tag, rmd;
    float alpha, m, wsum;
    u32x4 tw;
    u32x2 xq;
    s16x8 sl[8];
    const int kcap = CAP0 + (tmi & 7);
    if (!LAY2) {
      ARGS_HERE;
      const int lane = fresh_lane(), t = hw * 64 + lane;
      const unsigned ep = ld_ag(gran);
      const int srow = m0 + tni * 8 + hw * 2;
      u32x4 xa[2][2], wv[8];
      const int wr_ = (3 - ((unsigned)(uintptr_t)W >> 7) - (tni & ~3)) & 7;
      const bool cpy = hw == 3 && wr_ < 4 && wr_ != (tni & 3), wld = hw < 3 ? wr_ != (tni & 3) : cpy;
      const int wcn = cpy ? (tni & ~3) + wr_ : tni, wcp = cpy ? (tni - wr_ - 1) & 3 : hw;
      const int wof = ((tmi * 64 + (lane >> 3)) * ldw + wcp * D_ + wcn * 64 + (lane & 7) * 8) * 2;
      auto ld_w = [&]() {
        const __amdgpu_buffer_rsrc_t rw = __builtin_amdgcn_make_buffer_rsrc(const_cast<u16*>(W), 0, wld ? D_ * ldw * 2 : 0, 0x00020000);
#pragma unroll
        for (int r = 0; r < 8; ++r) wv[r] = __builtin_amdgcn_raw_buffer_load_b128(rw, wof, r * 8 * ldw * 2, 0);
        PIN();
      };
      auto wcopy = [&]() __attribute__((always_inline)) {
        if (cpy) {
          const __amdgpu_buffer_rsrc_t rw = __builtin_amdgcn_make_buffer_rsrc(ap_->wc, 0, D_ * ldw * 2, 0x00020000);
#pragma unroll
          for (int r = 0; r < 8; ++r) __builtin_amdgcn_raw_buffer_store_b128(wv[r], rw, wof + r * 8 * ldw * 2, 0, 16);
        }
        PIN();
      };
      const int wat = ((hw == 3 ? SMAP : WMAP) >> (4 * (tmi & 7))) & 3;
      unsigned ps = 0u;
      auto stouch = [&](int at) __attribute__((always_inline)) {
        if (at == STCH && cpy) ps = __builtin_amdgcn_raw_buffer_load_b32(__builtin_amdgcn_make_buffer_rsrc(const_cast<u16*>(W), 0, D_ * ldw * 2, 0x00020000),
                                                                        (((tmi ^ 8) * 64 + lane) * ldw + wcp * D_ + wcn * 64) * 2, 0, 0);
        PIN();
      };
      auto ld_x = [&](int h) {
#pragma unroll
        for (int r = 0; r < 2; ++r) xa[r][h] = *reinterpret_cast<const u32x4*>(A + (size_t)(srow + r) * D_ + (h ^ (tmi >> 3)) * 512 + lane * 8);
        PIN();
      };
      ld_x(0);
      if (X2AT == 0) ld_x(1);
      if (wat == 0) ld_w();
      unsigned pc = 0u;
      if (hw == 3) {
        unsigned long long a0, a1;
        CODE_ADDR("layers_kernel", a0);
        CODE_ADDR("end_marker_kernel", a1);
        pc = touch_code(a0, a1, loc, lane);
      }
      if (hw == 2) st_ag(gran + GR_N + bid * 64 + lane, lane + 1u);
      {
        const int gi = bid * 256 + t;
        if (gi < GRAN_N / 4) __builtin_amdgcn_raw_buffer_store_b128(u32x4{SENT, SENT, SENT, SENT}, __builtin_amdgcn_make_buffer_rsrc(hand, 0, GRAN_N * 4, 0x00020000), gi * 16, 0, 16);
        if (t < 24) __hip_atomic_store(stats_out + (t >> 3) * S_ + m0 + tni * 8 + (t & 7), 0.0, __ATOMIC_RELAXED, __HIP_MEMORY_SCOPE_AGENT);
      }
      tick();
      if (wat == 1) ld_w();
      if (X2AT == 1) ld_x(1);
      tick();
      if (wat == 2) ld_w();
      if (WCAT == 2) wcopy();
      if (X2AT == 2) ld_x(1);
      asm volatile("" :: "v"(ep));
      tick();
      if (wat == 3) ld_w();
      if (WCAT == 3) wcopy();
      if (X2AT == 3) ld_x(1);
      stouch(3);
#pragma unroll
      for (int i = 0; i < HW; ++i) { tick(); stouch(4 + i); }
      __builtin_amdgcn_s_setprio(HP1);
      PIN();
      tag = ep + 1u;
      const __amdgpu_buffer_rsrc_t rsc = __builtin_amdgcn_make_buffer_rsrc(gran, 0, GR_N * 4, 0x00020000);
      {
        u32x4 od;
#pragma unroll
        for (int q = 0; q < 4; ++q) od[q] = ((lane & 7) >> 1) == q ? ((lane & 1) ? 0x3f800000u : 0x00003f80u) : 0u;
        f32x4 d[2] = {{0.f, 0.f, 0.f, 0.f}, {0.f, 0.f, 0.f, 0.f}};
#pragma unroll
        for (int r = 0; r < 8; ++r) d[r & 1] = __builtin_amdgcn_mfma_f32_16x16x32_bf16(__builtin_bit_cast(s16x8, od), __builtin_bit_cast(s16x8, wv[r]), d[r & 1], 0, 0, 0);
        u32x4 gr;
#pragma unroll
        for (int q = 0; q < 4; ++q) gr[q] = ((__float_as_uint(dpp_add<0x128, 0xf>(fadd(d[0][q], d[1][q]))) + 0x80u) & ~0xffu) | ((tag >> (8 * q)) & 0xffu);
        asm volatile("s_waitcnt vmcnt(0)" ::: "memory");
        __builtin_amdgcn_raw_buffer_store_b128(gr, rsc, (lane & 40) || !wld ? GR_N * 4 : (GR_COL + (wcn * 16 + tmi) * 192 + wcp * 64 + (lane & 7) * 8 + (lane >> 4) * 4) * 4, 0, 16);
      }
      asm volatile("" :: "v"(pc));
      tick_nw();
      stouch(6);
      {
        float xv[2][16], mean[2];
#pragma unroll
        for (int r = 0; r < 2; ++r) {
          float sm = 0.f;
#pragma unroll
          for (int i = 0; i < 4; ++i) {
            xv[r][2 * i] = __uint_as_float(xa[r][0][i] << 16); xv[r][2 * i + 1] = __uint_as_float(xa[r][0][i] & 0xffff0000u);
            xv[r][8 + 2 * i] = __uint_as_float(xa[r][1][i] << 16); xv[r][8 + 2 * i + 1] = __uint_as_float(xa[r][1][i] & 0xffff0000u);
          }
#pragma unroll
          for (int i = 0; i < 16; ++i) sm += xv[r][i];
          mean[r] = wave_sum(sm) * (1.0f / 1024.0f);
          tick();
          stouch(7 + r);
        }
        float vr[2];
#pragma unroll
        for (int r = 0; r < 2; ++r) {
          float v = 0.f;
#pragma unroll
          for (int i = 0; i < 16; ++i) { const float d = xv[r][i] - mean[r]; v += d * d; }
          vr[r] = wave_sum(v) * (1.0f / 1024.0f);
          if (r == 0) tick();
        }
        const float vv = lane & 2 ? vr[1] : vr[0], mm = lane & 2 ? mean[1] : mean[0];
        const float rin = 1.0f / rbf(sqrtf(rbf(rbf(vv) + 1e-5f)));
        if (lane < 4) __builtin_amdgcn_raw_buffer_store_b64(u32x2{__float_as_uint((lane & 1) ? rbf(mm) : rin), tag}, rsc, (GR_ROW + srow * 4 + lane * 2) * 4, 0, 16);
      }
      tick();
      float cs;
      while (nb < POLLAT) tick();
      asm volatile("" :: "v"(ps));
#pragma unroll 1
      for (int spin = 0;; ++spin) {
        u32x4 csv[4];
        const __amdgpu_buffer_rsrc_t rsw = __builtin_amdgcn_make_buffer_rsrc(gran, 0, hw < 3 ? GR_N * 4 : 0, 0x00020000);
#pragma unroll
        for (int i = 0; i < 4; ++i) csv[i] = __builtin_amdgcn_raw_buffer_load_b128(rsw, (GR_COL + (tni * 16 + (lane >> 4)) * 192 + hw * 64 + (lane & 15) * 4) * 4, i * (4 * 192 * 4), 16);
        const u32x4 rp = __builtin_amdgcn_raw_buffer_load_b128(rsc, (GR_ROW + (m0 + (t & 127)) * 4) * 4, 0, 16);
        PIN();
        tick(); tick();
        unsigned bad = 0u;
        float z[4];
#pragma unroll
        for (int q = 0; q < 4; ++q) {
          const unsigned tq = tag >> (8 * q);
          bad |= ((csv[0][q] ^ tq) | (csv[1][q] ^ tq)) | ((csv[2][q] ^ tq) | (csv[3][q] ^ tq));
          z[q] = fadd(fadd(__uint_as_float(csv[0][q] & ~0xffu), __uint_as_float(csv[1][q] & ~0xffu)), fadd(__uint_as_float(csv[2][q] & ~0xffu), __uint_as_float(csv[3][q] & ~0xffu)));
        }
        cs = fold16(f32x2{z[0], z[1]}, f32x2{z[2], z[3]});
        bad = (hw < 3 ? bad & 0xffu : 0u) | (rp[1] ^ tag) | (rp[3] ^ tag);
        alpha = __uint_as_float(rp[0]); m = __uint_as_float(rp[2]);
        if (none(bad != 0u) || spin >= SPIN_MAX) break;
      }
      if (tmi == 0 && hw < 3) st_ag(reinterpret_cast<unsigned*>(csf) + hw * 1024 + n0 + (lane & 15) * 4 + (lane >> 4), __float_as_uint(cs));
      unsigned c0, c1, c2;
      split3(cs, c0, c1, c2);
      tw = u32x4{c0 | (c1 << 16), c0 | (c2 << 16), c1 | (c0 << 16), 0u};
      best = rmd = 0u;
      wsum = 0.f;
      asm volatile("" : "=v"(xq), KEPT("="));
#pragma unroll
      for (int i = 0; i < 8; ++i) asm volatile("" : "=v"(sl[i]));
    } else {
      asm volatile("; kept registers" : KEPT("="));
      PIN();
      ARGS_HERE;
      const int lane = fresh_lane(), t = hw * 64 + lane;
      best = rmd = 0u;
      tag = __builtin_amdgcn_readfirstlane(*LDS_AT(const volatile unsigned, lbase + FLG + 20));
#pragma unroll
      for (int i = 0; i < 8; ++i) asm volatile("" : "=v"(sl[i]));
      unsigned sad;
      auto tickc = [&]() __attribute__((always_inline)) {
        tick();
        if (nb == kcap + 1) {
#pragma unroll
          for (int i = 0; i < 8; ++i) {
            const unsigned a = (sad ^ ((i >> 2) * 64) ^ (((i >> 1) & 1) * 16)) + (i & 1) * 12288;
            sl[i] = __builtin_shufflevector(__builtin_amdgcn_ds_read_tr16_b64_v4i16(LDS_AT(s16x4, a)), __builtin_amdgcn_ds_read_tr16_b64_v4i16(LDS_AT(s16x4, a + 1536)), 0, 1, 2, 3, 4, 5, 6, 7);
          }
        }
        PIN();
      };
      static_assert(CAP0 + 8 <= KPOLL, "the stage is taken before that");
      if (hw == 1) STAMP(4, 4);
      if (hw == 0) {
        __builtin_amdgcn_s_sleep(TPRE);
        tile_at(3u);
        __builtin_amdgcn_s_sleep(XGATE);
        STAMP(4, 3);
#pragma unroll 1
        for (int spin = 0; spin < SPIN_MAX; ++spin) {
          if (none(ld_ag(gran + GR_X + (tmi * RX + tni % RX) * 128 + 64 + lane) != tag)) break;
          __builtin_amdgcn_s_sleep(1);
        }
      }
      if (hw == 0) STAMP(4, 5);
      asm volatile("" ::: "memory");
      PIN();
      __builtin_amdgcn_s_barrier();
      PIN();
      if (hw == 1) STAMP(4, 6);
      {
        const int ln = fresh_lane(), g4 = ln >> 4, tp = ln & 3, k = 8 * g4 + ((ln & 15) >> 2);
        sad = lbase + (kcap % NST) * STB + ABYTES + k * 384 + (((2 * (tp >> 1)) ^ (4 * ((k >> 1) & 1)) ^ (g4 & 1)) << 4) + 8 * (tp & 1) + (hw % 3) * 128;
      }
      wsum = __uint_as_float(__builtin_amdgcn_readfirstlane(*LDS_AT(const volatile unsigned, lbase + FLG + 60)));
      asm volatile("" : "+v"(sad), "=v"(tw), "=v"(alpha), "=v"(m));
      PIN();
      const int row = m0 + (t & 127);
      const __amdgpu_buffer_rsrc_t rsg = __builtin_amdgcn_make_buffer_rsrc(hand, 0, GRAN_N * 4, 0x00020000);
      if (!H2SK || hw < 2 || tmi == tni) {
      while (nb < HS) tickc();
#pragma unroll 1
      for (int spin = 0; spin < SPIN_MAX; ++spin) {
        const unsigned v = ld_ag(gran + GR_X + (tmi * RX + tni % RX) * 128 + lane);
        tickc();
        if (HT & 1) tickc();
        if (none(v != tag)) break;
      }
      asm volatile("" ::: "memory");
      PIN();
      const double s1 = ld_ag(stats + row), s2 = ld_ag(stats + S_ + row), sw = ld_ag(stats + 2 * S_ + row);
      tickc();
      if (HT & 2) tickc();
      float mean, var, mb, den;
      ln_from_sums(s1, s2, mean, var, mb, den);
      const float rin = frcp(den), g1 = 1.0f + rin;
      const float mb2 = rbf(mean + (mean - mb) * rin);
      const float den2 = rbf(__builtin_amdgcn_sqrtf(rbf(rbf(var * g1 * g1) + 1e-5f)));
      alpha = g1 * frcp(den2); m = (mb * rin + mb2) * frcp(g1);
      if (tmi == tni && hw >= 2) {
        const float r = (float)fma(-(double)mb, (double)wsum, sw) * rin;
        const unsigned rb = pack2(r, r);
        unsigned rkey = (rb ^ ((unsigned)((int)rb >> 31) | 0x80000000u)) & 0xffff0000u;
        if (r != r) rkey = 0u;
        rkey = wave_umax(rkey | (unsigned)(2047 - row));
        const unsigned md = __builtin_amdgcn_readlane(f2word(__uint_as_float((__float_as_uint(mb) >> 16) | (__float_as_uint(den) & 0xffff0000u))), (2047 - (int)(rkey & 0xffffu)) & 63);
        __builtin_amdgcn_raw_buffer_store_b64(u32x2{rkey, md}, rsg, lane < NREP ? lane * 256 + (tmi * 2 + hw - 2) * 8 : GRAN_N * 4, 0, 16);
      }
      }
      while (nb < KPOLL) tickc();
#pragma unroll 1
      for (int spin = 0; spin < SPIN_MAX; ++spin) {
        const u32x2 v = __builtin_amdgcn_raw_buffer_load_b64(rsg, (bid % NREP) * 256 + (lane & 31) * 8, 0, 16);
        tick(); tick();
        if (none(umax(v[0], v[1]) == SENT)) {
          best = wave_umax(v[0]);
          rmd = __builtin_amdgcn_readlane(v[1], ((2047 - (int)(best & 0xffffu)) >> 6) & 31);
          break;
        }
      }
      const int ix = (2047 - (int)(best & 0xffffu)) & 2047;
      if (row == ix) alpha = 0.f;
      xq = __builtin_amdgcn_raw_buffer_load_b64(__builtin_amdgcn_make_buffer_rsrc(const_cast<u16*>(A), 0, S_ * D_ * 2, 0x00020000), (ix * D_ + hw * 256 + (lane & 3) * 64 + (lane >> 2) * 4) * 2, 0, 16);
      PIN();
    }
    tag = __builtin_amdgcn_readfirstlane(tag);
    wsum = __uint_as_float(__builtin_amdgcn_readfirstlane(__float_as_uint(wsum)));
    const int lane = fresh_lane(), t = hw * 64 + lane;
    {
      unsigned a0, a1, a2;
      split3(-m, a0, a1, a2);
      u32x4 ta = {a0 | (a0 << 16), a1 | (a0 << 16), a1 | (a2 << 16), 0u};
      asm volatile("" : "+v"(tw), "+v"(ta), "+v"(alpha));
      __builtin_amdgcn_s_setprio(TPRIO);
      if (nb < lim) {
        while (nb < lim) tick();
        __builtin_amdgcn_s_sleep(TSLP);
      }
      if (!LAY2 && hw < 3) *LDS_AT(u32x4, lbase + TABW + (hw * 64 + (lane & 15) * 4 + (lane >> 4)) * 16) = tw;
      if (t < 128) {
        *LDS_AT(u32x4, lbase + TABA + t * 16) = ta;
        *LDS_AT(float, lbase + ALPHA + t * 4) = alpha;
      }
    }
    if (t >= 192 && t < 196) *LDS_AT(u32x4, lbase + CRED + ((t >> 1) & 1) * 512 + (t & 1) * 128) = u32x4{0u, 0u, 0u, 0u};
    asm volatile("s_waitcnt lgkmcnt(0)" ::: "memory");
    PIN();
    __builtin_amdgcn_s_barrier();
    PIN();
    *LDS_AT(u32x4, lbase + XML + t * 16) = u32x4{SENT, SENT, SENT, SENT};
    *LDS_AT(u32x4, lbase + SPM + (t & 127) * 16) = u32x4{SENT, SENT, SENT, SENT};
    if (hw == 0) {
      unsigned v;
      asm volatile("v_mov_b32 %0, 0\n\tv_writelane_b32 %0, 2, 0\n\tv_writelane_b32 %0, %1, 5\n\tv_writelane_b32 %0, %2, 7"
                   : "=&v"(v) : "s"(tag), "s"(best));
      lds_wr(lbase + FLG + lane * 4, v);
    }
    const int idx = LAY2 ? (2047 - (int)(best & 0xffffu)) & 2047 : -64;
    const bool p1blk = (oni & 7) != 7, fast = tmi < 8, own = fast && (loc & 1);
    const int fg = xcd * 28 + (p1blk ? loc - (loc >> 3) : 0), ow = (xcd & 3) * 16 + (loc >> 1);
    static_assert(OMX == 4, "oni % 8 = loc % 8");
#define GLD(a, b) "global_load_dwordx4 v[" #a ":" #b "], %0, off\n\tv_lshl_add_u64 %0, %0, 0, %1\n\t"
#define GLD4(...) asm volatile(__VA_ARGS__ : "+v"(p) : "s"((unsigned long long)(F_ * 2)), KEPT() : "memory")
    auto gptr = [&](const u16* wg, const u16* wu) {
      const int ln = fresh_lane();
      return ((ln & 2) ? wu : wg) + (size_t)(hw * 256 + (ln >> 2) * 4) * F_ + fg * 16 + (ln & 1) * 8;
    };
    auto gather = [&](const u16*& p, int e) __attribute__((always_inline)) {
      if (e == 0) GLD4(GLD(104, 107) GLD(108, 111) GLD(112, 115) GLD(116, 119));
      else if (e == 1) GLD4(GLD(120, 123) GLD(124, 127) GLD(128, 131) GLD(132, 135));
      else if (e == 2) GLD4(GLD(136, 139) GLD(140, 143) GLD(144, 147) GLD(148, 151));
      else GLD4(GLD(152, 155) GLD(156, 159) GLD(160, 163) GLD(164, 167));
      p += 60 * F_;
    };
    auto dmask = [&](int q) { const int ln = fresh_lane(); return ((ln >> 1) & 1) == q ? ((ln & 1) ? 0xffff0000u : 0x0000ffffu) : 0u; };
    unsigned dm0, dm1, dad;
    auto gu_setup = [&]() __attribute__((always_inline)) {
      const int ln = fresh_lane();
      dm0 = dmask(0); dm1 = dmask(1);
      dad = lbase + SACT + ((ln & 3) * 8 + (ln >> 5) * 4 + ((ln >> 4) & 1)) * 16 + hw * 4;
      asm volatile("" : "+v"(dm0), "+v"(dm1), "+v"(dad));
    };
    auto gate_up = [&](u32x2 x, unsigned md) __attribute__((always_inline)) {
      const unsigned rin = __builtin_amdgcn_readfirstlane(__float_as_uint(frcp(__uint_as_float(md & 0xffff0000u))));
      asm volatile("s_getpc_b64 s[90:91]\n\ts_add_u32 s90, s90, gate_up_code@rel32@lo+4\n\ts_addc_u32 s91, s91, gate_up_code@rel32@hi+12\n\ts_swappc_b64 s[92:93], s[90:91]"
                   : "+{v[100:101]}"(x) : "{s84}"(md << 16), "{s85}"(md & 0xffff0000u), "{s86}"(rin), "{v99}"(dad), "{v102}"(dm0), "{v103}"(dm1), KEPT()
                   : "v84", "v85", "v86", "v87", "v88", "v89", "v90", "v91", "v92", "v93", "v94", "v95", "v96", "v97", "s88", "s89", "s90", "s91", "s92", "s93", "scc", "memory");
    };
    auto act_diag = [&](int so = 0) __attribute__((always_inline)) {
      const int ln = fresh_lane();
      const f32x4 g4 = *LDS_AT(const volatile f32x4, lbase + SACT + so + (ln >> 2) * 16), u4 = *LDS_AT(const volatile f32x4, lbase + SACT + so + 256 + (ln >> 2) * 16);
      float gv = fadd(fadd(g4[0], g4[1]), fadd(g4[2], g4[3])), uv = fadd(fadd(u4[0], u4[1]), fadd(u4[2], u4[3]));
      rbf2(gv, uv);
      const float av = rbf(gv * frcp(1.0f + __expf(-gv))) * uv;
      const unsigned ap2 = pack2(av, av);
      return __builtin_bit_cast(s16x4, u32x2{ap2 & dm0, ap2 & dm1});
    };
    auto down = [&](const s16x4& a, const u32x2 (&w)[16]) __attribute__((always_inline)) {
      f32x4 d[2] = {{0.f, 0.f, 0.f, 0.f}, {0.f, 0.f, 0.f, 0.f}};
      static_for<0, 16>([&](auto fc) __attribute__((always_inline)) {
        constexpr int f = decltype(fc)::value;
        d[f & 1] = __builtin_amdgcn_mfma_f32_4x4x4bf16_1k(a, __builtin_bit_cast(s16x4, w[f]), d[f & 1], 4, f, 0);
      });
      f32x4 z;
#pragma unroll
      for (int e = 0; e < 4; ++e) { z[e] = d[0][e] + d[1][e]; asm volatile("" : "+v"(z[e])); }
      return u32x4{f2word(z[0]), f2word(z[1]), f2word(z[2]), f2word(z[3])};
    };
    auto fold8 = [&](f32x2 y0, f32x2 y1) __attribute__((always_inline)) { return dpp_add<0x128, 0xf>(fold16(y0, y1)); };
    auto fold4 = [&](f32x2 y0, f32x2 y1) __attribute__((always_inline)) { return dpp_add<0x124, 0xf>(fold8(y0, y1)); };
    auto y_sum = [&](const __amdgpu_buffer_rsrc_t rs, auto nbc) __attribute__((always_inline)) {
      constexpr int NB = decltype(nbc)::value, N = 14 / NB;
      const int ln = fresh_lane(), src = (GRAN_N + (ln >> 2) * D_ + ow * 16 + (ln & 3) * 4) * 4;
      f32x2 y0 = {0.f, 0.f}, y1 = {0.f, 0.f};
#pragma unroll 1
      for (int bt = 0; bt < NB; ++bt) {
        f32x2 z0, z1;
#pragma unroll 1
        for (int spin = 0; spin < SPIN_MAX; ++spin) {
          u32x4 v[N];
#pragma unroll
          for (int i = 0; i < N; ++i) v[i] = __builtin_amdgcn_raw_buffer_load_b128(rs, src, (bt * N + i) * (16 * D_ * 4), 16);
          z0 = y0; z1 = y1;
#pragma unroll
          for (int i = 0; i < N; ++i) {
            z0 = f32x2{fadd(z0[0], __uint_as_float(v[i][0])), fadd(z0[1], __uint_as_float(v[i][1]))};
            z1 = f32x2{fadd(z1[0], __uint_as_float(v[i][2])), fadd(z1[1], __uint_as_float(v[i][3]))};
          }
          const float chk = fadd(fadd(z0[0], z0[1]), fadd(z1[0], z1[1]));
          if (none(chk != chk)) break;
          unsigned mx = 0u;
#pragma unroll
          for (int i = 0; i < N; ++i) mx = umax(umax(umax(mx, v[i][0]), v[i][1]), umax(v[i][2], v[i][3]));
          if (none(mx == SENT)) break;
          __builtin_amdgcn_s_sleep(YSLP);
          asm volatile("" ::: "memory");
        }
        y0 = z0; y1 = z1;
      }
      return fold4(y0, y1);
    };
    [&]() __attribute__((always_inline)) {
    ARGS_HERE;
    const unsigned l16 = lane * 16;
    unsigned* kvflag = gran + GR_KV + ((LAY2 ? 16 : 0) + tni) * (32 * RK);
    auto wait_sig = [&](int sh) {
#pragma unroll 1
      for (int i = 0; i < 4 * SPIN_MAX; ++i) {
        if (((__builtin_amdgcn_readfirstlane(*LDS_AT(const volatile unsigned, lbase + FLG + 32)) >> sh) & 7u) >= 4u) break;
        __builtin_amdgcn_s_sleep(SSLP);
      }
      asm volatile("" ::: "memory");
      PIN();
    };
    PIN();
    asm volatile("s_waitcnt lgkmcnt(0)" ::: "memory");
    if (!LAY2) asm volatile("s_waitcnt vmcnt(0)" ::: "memory");
    __builtin_amdgcn_s_barrier();
    PIN();
    __builtin_amdgcn_s_setprio(HPRIO);
    if (hw < 3) {
      __builtin_amdgcn_s_sleep(SPRE);
      wait_sig(0);
      __builtin_amdgcn_raw_buffer_store_b32(tag, __builtin_amdgcn_make_buffer_rsrc(kvflag, 0, hw == 0 ? RK * 128 : 0, 0x00020000), lane * 128 + tmi * 4, 0, 16);
      if (hw == 0) STAMP(LAY2 ? 5 : 1, 16);
    }
    if (hw == 3) {
      __builtin_amdgcn_s_sleep(KVS);
      const unsigned kv0 = ld_ag(kvflag + (tmi % RK) * 32 + (lane & 15));
      PIN();
      const char* cnt = reinterpret_cast<const char*>(gran + GR_N + bid * 64 + 2);
      unsigned vk[8], vp = lbase + FLG + 8 + (lane & 7), pw = 0u;
#pragma unroll
      for (int j = 0; j < 8; ++j) { vk[j] = l16 + (j >> 2) * 131072u + ((j >> 1) & 1) * 4096u + (j & 1) * (VOFF - 2048u); asm volatile("" : "+v"(vk[j])); }
#define G4(a, b, c, d, sc) "s_nop 0\n\tglobal_load_lds_dwordx4 %" #a ", %8" sc "\n\tglobal_load_lds_dwordx4 %" #b ", %8 offset:1024" sc "\n\t"             \
                       "global_load_lds_dwordx4 %" #c ", %8 offset:2048" sc "\n\tglobal_load_lds_dwordx4 %" #d ", %8 offset:3072" sc "\n\t"
#define M4 "s_addk_i32 m0, 0x1000\n\t"
      auto stage = [&](const char* sp, unsigned dst) __attribute__((always_inline)) {
        asm volatile("s_mov_b32 m0, %9\n\t" G4(0, 0, 1, 1, " sc1") M4 G4(2, 2, 3, 3, " sc1")
                     M4 G4(4, 4, 5, 5, " sc1") M4 G4(6, 6, 7, 7, " sc1")
                     :: "v"(vk[0]), "v"(vk[1]), "v"(vk[2]), "v"(vk[3]), "v"(vk[4]), "v"(vk[5]), "v"(vk[6]), "v"(vk[7]), "s"(sp), "s"(dst) : "m0", "scc", "memory");
      };
      auto count = [&]() __attribute__((always_inline)) {
        asm volatile("s_mov_b32 m0, %2\n\ts_nop 0\n\tglobal_load_lds_dword %0, %1 sc1" :: "v"(0u), "s"(cnt), "s"(lbase + FLG - 252) : "m0", "memory");
        cnt += 8;
      };
      auto freed = [&](unsigned n) __attribute__((always_inline)) {
        if (!none(pw < n)) {
#pragma unroll 1
          for (int i = 0; i < 4 * SPIN_MAX; ++i) {
            pw = *LDS_AT(const volatile unsigned char, vp);
            if (none(pw < n)) break;
            __builtin_amdgcn_s_sleep(W3SLP);
          }
        }
        asm volatile("" ::: "memory");
      };
      unsigned long long ord = (0xfedcba9876543210ull >> 4 & ~((1ull << 4 * (tmi & 7)) - 1ull)) | (0xfedcba9876543210ull & ((1ull << 4 * (tmi & 7)) - 1ull));
      ord ^= (tmi & 8) * 0x1111111111111111ull;
      unsigned seen;
      const char* kvb = reinterpret_cast<const char*>(KV) + (size_t)tni * (128 * 2048);
      auto panel = [&]() __attribute__((always_inline)) {
        const unsigned pn = (unsigned)ord & 15u;
        ord >>= 4;
        if (__builtin_expect(!((seen >> pn) & 1u), 0)) {
#pragma unroll 1
          for (int i = 0; i < SPIN_MAX && !((seen >> pn) & 1u); ++i) seen = (unsigned)__ballot(ld_ag(kvflag + (tmi % RK) * 32 + (lane & 15)) == tag);
        }
        asm volatile("" ::: "memory");
        return kvb + pn * 8192u;
      };
      asm volatile("" : "+s"(ord), "+s"(kvb), "+s"(cnt));
      asm volatile("" : "+v"(vp), "+v"(pw));
      PIN();
      if (!PFIRST) wait_sig(16);
      seen = (unsigned)__ballot(kv0 == tag);
      const char* sp = panel();
      if (PFIRST) wait_sig(16);
      stage(sp, lbase + 2 * ASB);
      count();
      const int ksp = LAY2 ? PNS / 2 : -1, ksg = LAY2 ? 64 : GF0;
      const u16* gp = gptr(wg, wu);
#pragma unroll 1
      for (int k = 1; k < 15; ++k) {
        if (__builtin_expect(k == ksp, 0)) {
          gu_setup();
          gate_up(xq, rmd);
          PIN();
          *LDS_AT(volatile unsigned char, lbase + FLG + 7) = (unsigned char)0x7f;
        }
        freed((unsigned)(0xba9876543211000ull >> 4 * k) & 15u);
        stage(sp + 2048, lbase + ((2 * k + 1) & 7) * ASB);
        sp = panel();
        stage(sp, lbase + ((2 * k + 2) & 7) * ASB);
        count();
        if (__builtin_expect((unsigned)(k - ksg) < 4u, 0)) gather(gp, k - ksg);
      }
      freed(12u);
      glds16s<true>(csf + 4096 + tni * 128, l16, lbase + UGL);
      stage(sp + 2048, lbase + 7 * ASB);
      count();
      const unsigned vwo = (unsigned)((lane >> 3) * D_ + oni * 64 + ((lane & 7) ^ ((lane >> 3) & 6)) * 8) * 2u;
      constexpr unsigned vs = 8u * D_ * 2u - 1024u;
#pragma unroll 1
      for (int ns = 0; ns < 6; ++ns) {
        freed((unsigned)(ns + 25) >> 1);
        const char* wp = reinterpret_cast<const char*>(wo) + (size_t)ns * (128 * D_ * 2);
#pragma unroll
        for (int j = 0; j < 4; ++j)
          glds16x4<false>(wp + j * (32u * D_ * 2u), vwo, vwo + vs, vwo + 2 * vs, vwo + 3 * vs, lbase + ns * ASB + j * 4096);
      }
      asm volatile("s_waitcnt vmcnt(0)" ::: "memory");
      return;
    }
    if (!LAY2) {
      auto until = [&](unsigned n) {
        while ((__builtin_amdgcn_readfirstlane(*LDS_AT(const volatile unsigned, lbase + FLG + 8)) & 255u) < n) __builtin_amdgcn_s_sleep(16);
        asm volatile("" ::: "memory");
        PIN();
      };
      if (hw != 1) {
        constexpr int N2 = HAND_N + Y_N + 3 * D_ / 2;
        const __amdgpu_buffer_rsrc_t rsi = __builtin_amdgcn_make_buffer_rsrc(hand + (hw ? GRAN_N : 2 * HAND_N), 0, (hw ? N2 : PS_N) * 4, 0x00020000);
        until(IAT);
#pragma unroll 1
        for (int i = 0; i < ((hw ? N2 : PS_N) / 4 + 16383) / 16384; ++i) {
          __builtin_amdgcn_raw_buffer_store_b128(u32x4{SENT, SENT, SENT, SENT}, rsi, ((i * 256 + bid) * 64 + lane) * 16, 0, 16);
          __builtin_amdgcn_s_sleep(ISLP);
        }
      }
      const int ng = loc & 15, fc = xcd * 2 + (loc >> 4);
      const size_t og = (size_t)((fg - xcd * 28) / 7 * 256 + lane) * F_ + (xcd * 7 + fg % 7) * 64;
      const u16* pd = wd + (size_t)(fc * 224 + (lane < 56 ? lane : 0)) * D_ + ng * 64;
      const u16* p = gptr(wg, wu);
#pragma unroll 1
      for (unsigned n = T0; n < T0 + 13u; ++n) {
        until(n);
        if (hw == 1) {
          const unsigned i = n - T0;
          const u16* q = i < 8u ? ((i & 1u) ? wu : wg) + og + (size_t)(i >> 1) * (64 * F_) : pd + (size_t)((i - 8u - (i > TW)) & 3u) * (56 * D_);
          if (i == TW) {
            q = wo + (size_t)(768 + (loc / (32 / OMX)) * (256 / OMX) + (lane & (256 / OMX - 1) & 63)) * D_ + oni * 64;
            unsigned long long a0, a1;
            CODE_ADDR("layers_kernel", a0); CODE_ADDR("end_marker_kernel", a1);
            if (loc < 8) { const unsigned p1 = touch_code(a0, a1, 3 + loc, lane); asm volatile("" :: "v"(p1)); }
          }
          const unsigned p0 = touch(q);
          asm volatile("" :: "v"(p0));
        }
        if (n >= G0 && (n - G0) % GS == 0u && (n - G0) / GS < 4u) gather(p, (n - G0) / GS);
        if (n == UAT && hw == UHW) {
          u32x4 a[4][2], c[2];
#pragma unroll
          for (int i = 0; i < 4; ++i)
#pragma unroll
            for (int h = 0; h < 2; ++h) a[i][h] = *reinterpret_cast<const u32x4*>(wo + (size_t)(tni * 64 + tmi * 4 + i) * D_ + h * 512 + lane * 8);
#pragma unroll
          for (int h = 0; h < 2; ++h) c[h] = *reinterpret_cast<const u32x4*>(w_r + h * 512 + lane * 8);
          auto dot = [&](unsigned x, unsigned y, float z) { return __builtin_amdgcn_fdot2_f32_bf16(__builtin_bit_cast(bf16x2, x), __builtin_bit_cast(bf16x2, y), z, false); };
          PIN();
          __builtin_amdgcn_s_setprio(0);
#pragma unroll 1
          for (int i = 0; i < SPIN_MAX; ++i) {
            if (__builtin_amdgcn_readfirstlane(*LDS_AT(const volatile unsigned, lbase + FLG + 16)) >= UARR) break;
            __builtin_amdgcn_s_sleep(4);
          }
          PIN();
          float wm = 0.f;
#pragma unroll
          for (int e = 0; e < 8; ++e) wm = dot(c[e >> 2][e & 3], 0x3f803f80u, wm);
          wm = wave_sum(wm);
          lds_wr(lbase + FLG + 60, __float_as_uint(wm));
          wm *= -1.0f / 1024.0f;
          f32x2 u[2], g[2];
#pragma unroll
          for (int i = 0; i < 4; ++i) {
            float p1 = 0.f, pw = 0.f, p2 = 0.f;
#pragma unroll
            for (int e = 0; e < 8; ++e) {
              const unsigned x = a[i][e >> 2][e & 3];
              p1 = dot(x, 0x3f803f80u, p1); pw = dot(x, c[e >> 2][e & 3], pw); p2 = dot(x, x, p2);
            }
            u[i >> 1][i & 1] = fmaf(p1, wm, pw); g[i >> 1][i & 1] = p2;
          }
          float us = fold4(u[0], u[1]), gs = fold4(g[0], g[1]);
          us = dpp_add<0x121, 0xf>(dpp_add<0x122, 0xf>(us)); gs = dpp_add<0x121, 0xf>(dpp_add<0x122, 0xf>(gs));
          __builtin_amdgcn_raw_buffer_store_b32(__float_as_uint((lane & 1) ? __builtin_amdgcn_sqrtf(gs) * (1.0f / 32.0f) : us),
              __builtin_amdgcn_make_buffer_rsrc(csf + 4096 + tni * 128, 0, 512, 0x00020000), (lane & 14) ? 512 : ((lane & 1) * 64 + tmi * 4 + (lane >> 4)) * 4, 0, 16);
        }
      }
      if (hw == 2 && bid == 0) {
        const __amdgpu_buffer_rsrc_t rsc = __builtin_amdgcn_make_buffer_rsrc(gran, 0, GR_N * 4, 0x00020000);
#pragma unroll 1
        for (int spin = 0; spin < SPIN_MAX; ++spin) {
          bool ok = true;
#pragma unroll
          for (int i = 0; i < 4; ++i) ok &= __builtin_amdgcn_raw_buffer_load_b32(rsc, (GR_ROW + (i * 64 + lane) * 32 + 1) * 4, 0, 16) == tag;
          if (__all(ok)) break;
          __builtin_amdgcn_s_sleep(8);
          asm volatile("" ::: "memory");
        }
        if (lane == 0) st_ag(gran, tag);
      }
      return;
    }
    unsigned* X2 = hand + GRAN_N + Y_N;
    unsigned* XS = X2 + X2_N;
    unsigned* P4 = XS + XS_N;
    unsigned* PA = P4 + P4_N;
    const __amdgpu_buffer_rsrc_t rsh = __builtin_amdgcn_make_buffer_rsrc(hand, 0, HAND_N * 4, 0x00020000);
    auto signal = [&](unsigned ph) {
      asm volatile("" ::: "memory");
      PIN();
      *LDS_AT(volatile unsigned char, lbase + FLG + 4 + hw) = (unsigned char)ph;
    };
    auto wait_all = [&](unsigned ph) {
#pragma unroll 1
      for (int i = 0; i < SPIN_MAX; ++i) {
        if (all_ge(__builtin_amdgcn_readfirstlane(*LDS_AT(const volatile unsigned, lbase + FLG + 4)), ph)) break;
        __builtin_amdgcn_s_sleep(WSLP);
      }
      asm volatile("" ::: "memory");
      PIN();
    };
    gu_setup();
    gate_up(xq, rmd);
    signal(hw == 2 ? 2u : 0x7fu);
    if (wid == 8) STAMP(dk, 2);
    if (hw < 2) {
#pragma unroll 1
      for (int i = 0; i < SPIN_MAX; ++i) {
        if (__builtin_amdgcn_readfirstlane(*LDS_AT(const volatile unsigned, lbase + FLG)) >= FSTART) break;
        __builtin_amdgcn_s_sleep(WSLP);
      }
      asm volatile("" ::: "memory");
      PIN();
      if (wid == 8) STAMP(dk, 10);
      u32x2 w[16];
      const __amdgpu_buffer_rsrc_t rd_ = __builtin_amdgcn_make_buffer_rsrc(const_cast<u16*>(wd), 0, D_ * F_ * 2, 0x00020000);
      auto ldw = [&](int c) __attribute__((always_inline)) {
#pragma unroll
        for (int f = 0; f < 16; ++f) w[f] = __builtin_amdgcn_raw_buffer_load_b64(rd_, lane * 8 + c * 512 + (f & 1) * 2048, (fg * 16 + (f & ~1)) * (D_ * 2) + hw * 1024, 0);
        PIN();
      };
      ldw(0);
      const unsigned pt = __builtin_amdgcn_raw_buffer_load_b32(rd_, (lane >> 2) * (D_ * 2) + (lane & 3) * 128 + 512, fg * 16 * (D_ * 2) + hw * 1024, 0);
      int yo = p1blk ? (GRAN_N + fg * D_ + hw * 512) * 4 + lane * 16 : HAND_N * 4;
      asm volatile("" : "+v"(yo));
      wait_all(2u);
      if (wid == 8) STAMP(dk, 3);
      const s16x4 a = act_diag();
      const u32x4 y0 = down(a, w);
      PIN();
      ldw(1);
      __builtin_amdgcn_raw_buffer_store_b128(y0, rsh, yo, 0, 16);
      PIN();
      __builtin_amdgcn_raw_buffer_store_b128(down(a, w), rsh, yo + 1024, 0, 16);
      asm volatile("" :: "v"(pt));
      if (wid == 8) STAMP(dk, 4);
    }
    PIN();
    [&]() __attribute__((always_inline)) {
    const int lane = fresh_lane();
    const float rmb = __uint_as_float(rmd << 16);
    auto first_words = [&](int src, auto nc) __attribute__((always_inline)) {
#pragma unroll 1
      for (int spin = 0; spin < SPIN_MAX; ++spin) {
        unsigned mx = 0u;
#pragma unroll
        for (int i = 0; i < decltype(nc)::value; ++i) mx = umax(mx, __builtin_amdgcn_raw_buffer_load_b32(rsh, src, i * (56 * D_ * 4), 16));
        if (none(mx == SENT)) break;
        __builtin_amdgcn_s_sleep(PSLP);
        asm volatile("" ::: "memory");
      }
    };
    if (hw == 2) {
      wait_all(2u);
      if (own) {
        const float rden = __uint_as_float(rmd & 0xffff0000u), rrin = frcp(rden);
        const int col = ow * 16 + (lane & 3) * 4 + (lane >> 4);
        const unsigned xo2 = ld_ag(reinterpret_cast<const unsigned*>(A + (size_t)idx * D_ + (col & ~1)));
        __builtin_amdgcn_s_sleep(PRE1);
        if (LPOLL & 2) first_words((GRAN_N + (lane < 56 ? lane : 0) * D_ + ow * 16) * 4, std::integral_constant<int, 4>{});
        const float y = y_sum(rsh, std::integral_constant<int, 2>{});
        const unsigned rk = best >> 16;
        const float rv = __uint_as_float(((rk & 0x8000u) ? (rk & 0x7fffu) : (~rk & 0xffffu)) << 16);
        const float rs = rbf(frcp(1.0f + __expf(-rv)));
        const float x1v = __uint_as_float((col & 1) ? xo2 & 0xffff0000u : xo2 << 16);
        float h0 = x1v, h1 = x1v;
        ln_apply2(h0, h1, rmb, rden, rrin);
        const float x2v = rbf(x1v + rbf(h0 + rbf(rbf(y) * rs)));
        const auto pr = __builtin_amdgcn_permlane16_swap(__float_as_uint(x2v), __float_as_uint(x2v), false, false);
        __builtin_amdgcn_raw_buffer_store_b32(f2word(__uint_as_float((pr[0] >> 16) | pr[1])), rsh, (lane & 28) ? HAND_N * 4 : (int)(X2 - hand) * 4 + col * 2, 0, 16);
        const float d = x2v - rmb;
        const float sd = wave_sum(d), sq = wave_sum(d * d);
        if (lane < 2) st_ag(XS + ow * 2 + lane, f2word(lane ? sq : sd));
        STAMP(dk, 5);
      }
    }
    {
      const int ks = kcap ^ (tmi & 8), g4 = lane >> 4;
      const int src = (int)(X2 - hand) * 4 + ks * 128 + g4 * 16;
      int dst = (lane & 12) ? HAND_N * 4 : (int)((P4 - hand) + (tni * 16 + ks) * 192 + hw * 64 + (lane & 2) * 16 + (lane & 1) * 8 + (g4 >> 1) * 16 + (g4 & 1) * 4) * 4;
      asm volatile("" : "+v"(dst));
      u32x4 x0, x1;
      if (!(hw == 2 && own)) __builtin_amdgcn_s_sleep(PRE4);
#pragma unroll 1
      for (int spin = 0; spin < SPIN_MAX; ++spin) {
        x0 = __builtin_amdgcn_raw_buffer_load_b128(rsh, src, 0, 16); x1 = __builtin_amdgcn_raw_buffer_load_b128(rsh, src + 64, 0, 16);
        if (none(umax(umax(umax(x0[0], x0[1]), umax(x0[2], x0[3])), umax(umax(x1[0], x1[1]), umax(x1[2], x1[3]))) == SENT)) break;
        __builtin_amdgcn_s_sleep(PSLP);
        asm volatile("" ::: "memory");
      }
      f32x4 d[4];
#pragma unroll
      for (int i = 0; i < 4; ++i) d[i] = MFMA16(sl[2 * i], __builtin_bit_cast(s16x8, x0), (f32x4{0.f, 0.f, 0.f, 0.f}));
#pragma unroll
      for (int i = 0; i < 4; ++i) d[i] = MFMA16(sl[2 * i + 1], __builtin_bit_cast(s16x8, x1), d[i]);
      u32x4 o;
#pragma unroll
      for (int e = 0; e < 4; ++e) o[e] = f2word((lane & 2) ? ((lane & 1) ? d[3][e] : d[2][e]) : ((lane & 1) ? d[1][e] : d[0][e]));
      __builtin_amdgcn_raw_buffer_store_b128(o, rsh, dst, 0, 16);
      PIN();
      if (wid == 8) STAMP(dk, 6);
    }
    if (hw == 2) {
      const int col = (lane & 15) * 4 + (lane >> 4), src = (int)((P4 - hand) + (tni * 16 + (lane >> 4)) * 192 + (lane & 15) * 4) * 4;
      const __amdgpu_buffer_rsrc_t rsq = __builtin_amdgcn_make_buffer_rsrc(hand, 0, fast ? HAND_N * 4 : 0, 0x00020000);
      float cs[3], val[3];
#pragma unroll
      for (int p = 0; p < 3; ++p) cs[p] = __uint_as_float(ld_ag(reinterpret_cast<const unsigned*>(csf) + p * 1024 + n0 + col));
      u32x2 xs;
      __builtin_amdgcn_s_sleep(PRE7);
      if (LPOLL & 1) first_words((int)((P4 - hand) + (tni * 16 + (lane & 15)) * 192 + ((lane >> 4) % 3) * 64) * 4, std::integral_constant<int, 1>{});
#pragma unroll 1
      for (int spin = 0; spin < SPIN_MAX; ++spin) {
        u32x4 v[3][4];
        xs = __builtin_amdgcn_raw_buffer_load_b64(rsh, (int)((XS - hand) + lane * 2) * 4, 0, 16);
#pragma unroll
        for (int p = 2; p >= 0; --p)
#pragma unroll
          for (int i = 0; i < 4; ++i) v[p][i] = __builtin_amdgcn_raw_buffer_load_b128(p ? rsh : rsq, src + p * 256, i * (4 * 192 * 4), 16);
        auto part = [&](int p) __attribute__((always_inline)) {
          float z[4];
#pragma unroll
          for (int e = 0; e < 4; ++e) {
            z[e] = fadd(fadd(__uint_as_float(v[p][0][e]), __uint_as_float(v[p][1][e])), fadd(__uint_as_float(v[p][2][e]), __uint_as_float(v[p][3][e])));
          }
          val[p] = fold16(f32x2{z[0], z[1]}, f32x2{z[2], z[3]});
        };
        part(2); part(1);
        val[0] = 0.f;
        if (fast) part(0);
        const float chk = fadd(fadd(fadd(val[0], val[1]), fadd(val[2], __uint_as_float(xs[0]))), __uint_as_float(xs[1]));
        if (none(chk != chk)) break;
        unsigned mx = umax(xs[0], xs[1]);
#pragma unroll
        for (int i = 0; i < 12; ++i) mx = umax(umax(umax(mx, v[i >> 2][i & 3][0]), v[i >> 2][i & 3][1]), umax(v[i >> 2][i & 3][2], v[i >> 2][i & 3][3]));
        if (none(mx == SENT)) break;
        __builtin_amdgcn_s_sleep(PSLP);
        asm volatile("" ::: "memory");
      }
      const auto sw = __builtin_amdgcn_permlane32_swap(xs[0], xs[1], false, false);
      float sm = __uint_as_float(sw[0]) + __uint_as_float(sw[1]);
      sm = dpp_add<0x142, 0xa>(dpp_add<0x118, 0xf>(dpp_add<0x114, 0xf>(dpp_add<0x112, 0xf>(dpp_add<0x111, 0xf>(sm)))));
      const float dm = __int_as_float(__builtin_amdgcn_readlane(__float_as_int(sm), 31)) * (0.25f / 1024.0f);
      const float var = fmaxf(__int_as_float(__builtin_amdgcn_readlane(__float_as_int(sm), 63)) * (0.25f / 1024.0f) - dm * dm, 0.f);
      const float mb = rbf(rmb + dm);
      const float rin = frcp(rbf(__builtin_amdgcn_sqrtf(rbf(rbf(var) + 1e-5f))));
      const unsigned kv = pack2((val[1] - mb * cs[1]) * rin, (val[2] - mb * cs[2]) * rin);
      *LDS_AT(u16, lbase + TAB + 128 + kpos(col) * 2) = (u16)kv;
      *LDS_AT(u16, lbase + TAB + 256 + col * 2) = (u16)(kv >> 16);
      if (fast) *LDS_AT(u16, lbase + TAB + kpos(col) * 2) = f2bf((val[0] - mb * cs[0]) * rin * QSCALE);
      signal(7u);
      STAMP(dk, 7);
      if (own) {
#pragma unroll
        for (int i = 0; i < 14; ++i)
          __builtin_amdgcn_raw_buffer_store_b128(u32x4{SENT, SENT, SENT, SENT}, rsh, (GRAN_N + (i * 16 + (lane >> 2)) * D_ + ow * 16 + (lane & 3) * 4) * 4, 0, 16);
      }
      return;
    }
    if (!fast) return;
    const int head = tni * 2 + hw;
    const unsigned tb = lbase + TAB + hw * 64;
    {
      const __amdgpu_buffer_rsrc_t rs = __builtin_amdgcn_make_buffer_rsrc(KV, 0, 2 * S_ * D_ * 2, 0x00020000);
      const int src = (head * 64 + tmi * 4) * 2048 + lane * 16;
      auto ld = [&](int i, unsigned off) { return __builtin_bit_cast(s16x8, __builtin_amdgcn_raw_buffer_load_b128(rs, src + (i & 3) * 1024, (i >> 3) * 65536 + ((i >> 2) & 1) * 4096 + off, 16)); };
#pragma unroll 1
      for (int i = 0; i < SPIN_MAX; ++i)
        if (__builtin_amdgcn_readfirstlane(ld_ag(kvflag + (tmi % RK) * 32 + tmi + 8)) == tag) break;
      asm volatile("" ::: "memory");
      s16x8 f[16];
#pragma unroll
      for (int i = 0; i < 16; ++i) f[i] = ld(i, 0u);
      PIN();
      const bool on = ((lane ^ (lane >> 4)) & 1) == 0;
      const unsigned pl = lbase + RED + hw * 1024;
      unsigned pw = pl + (lane >> 4) * 64 + (lane & 15) * 2, pr = pl + (on ? (lane >> 5) * 16 : 512), qr = on ? tb + (lane >> 5) * 16 : pl + 512;
      const int gone = (((idx >> 7) & 7) == tmi) ? ((idx >> 10) * 2 + (kpos(idx & 31) >> 4)) * 64 + ((idx >> 5) & 3) * 16 + (kpos(idx & 31) & 15) : -1;
      asm volatile("" : "+v"(pw), "+v"(pr), "+v"(qr));
      *LDS_AT(u32x2, pl + 512 + lane * 8) = u32x2{0u, 0u};
      wait_all(7u);
      u32x4 q[2];
#pragma unroll
      for (int sx = 0; sx < 2; ++sx) {
        q[sx] = *LDS_AT(const u32x4, qr + sx * 32);
      }
      float sc[4];
#pragma unroll
      for (int i = 0; i < 8; ++i) {
        f32x4 d = MFMA16(__builtin_bit_cast(s16x8, q[0]), f[2 * i], (f32x4{0.f, 0.f, 0.f, 0.f}));
        d = MFMA16(__builtin_bit_cast(s16x8, q[1]), f[2 * i + 1], d);
        PIN();
        f[2 * i] = ld(2 * i, VOFF); f[2 * i + 1] = ld(2 * i + 1, VOFF);
        PIN();
#pragma unroll
        for (int r = 0; r < 2; ++r) sc[(i >> 2) * 2 + r] = (i & 3) == 0 || (lane >> 4) == (i & 3) ? d[r] : sc[(i >> 2) * 2 + r];
      }
      if (gone >= 0) {
#pragma unroll
        for (int r = 0; r < 4; ++r) sc[r] = r * 64 + lane == gone ? -INFINITY : sc[r];
      }
      const float mx = wave_fmax(fmaxf(fmaxf(sc[0], sc[1]), fmaxf(sc[2], sc[3])));
      const unsigned p0 = pack2(__builtin_amdgcn_exp2f(sc[0] - mx), __builtin_amdgcn_exp2f(sc[1] - mx)), p1 = pack2(__builtin_amdgcn_exp2f(sc[2] - mx), __builtin_amdgcn_exp2f(sc[3] - mx));
      *LDS_AT(u16, pw) = (u16)p0; *LDS_AT(u16, pw + 32) = (u16)(p0 >> 16);
      *LDS_AT(u16, pw + 256) = (u16)p1; *LDS_AT(u16, pw + 288) = (u16)(p1 >> 16);
      asm volatile("" ::: "memory");
      const float ls = wave_sum((__uint_as_float(p0 << 16) + __uint_as_float(p0 & 0xffff0000u)) + (__uint_as_float(p1 << 16) + __uint_as_float(p1 & 0xffff0000u)));
      f32x4 o = {0.f, 0.f, 0.f, 0.f};
#pragma unroll
      for (int i = 0; i < 16; ++i) o = MFMA16(*LDS_AT(const s16x8, pr + i * 32), f[i], o);
      int pv = __float_as_int((lane & 16) ? o[1] : o[0]);
      asm volatile("s_nop 0\n\tv_writelane_b32 %0, %1, 32\n\tv_writelane_b32 %0, %2, 33" : "+v"(pv) : "s"(ls), "s"(mx));
      __builtin_amdgcn_raw_buffer_store_b32(f2word(__int_as_float(pv)), rsh, lane < 34 ? (int)((PA - hand) + (head * 8 + tmi) * 64 + lane) * 4 : HAND_N * 4, 0, 16);
    }
    if (wid == 8) STAMP(dk, 8);
    if (tmi != ((idx >> 7) & 7)) return;
    {
      const int src = (int)((PA - hand) + head * 512 + (lane < 34 ? lane : 0)) * 4;
      const unsigned ta = tb + (lane & 31) * 2;
      const float so = wave_sum(lane < 32 ? bf2f(*LDS_AT(const u16, ta)) * bf2f(*LDS_AT(const u16, ta + 128)) : 0.f);
      const float vo = lane < 32 ? bf2f(*LDS_AT(const u16, ta + 256)) : 1.0f;
      unsigned v[8];
      __builtin_amdgcn_s_sleep(PRE5);
#pragma unroll 1
      for (int spin = 0; spin < SPIN_MAX; ++spin) {
        unsigned mx = 0u;
#pragma unroll
        for (int i = 0; i < 8; ++i) v[i] = __builtin_amdgcn_raw_buffer_load_b32(rsh, src + i * 256, 0, 16);
#pragma unroll
        for (int i = 0; i < 8; ++i) mx = umax(mx, v[i]);
        if (none(mx == SENT)) break;
        __builtin_amdgcn_s_sleep(PSLP);
        asm volatile("" ::: "memory");
      }
      float m8[8], mx = so;
#pragma unroll
      for (int i = 0; i < 8; ++i) { m8[i] = __int_as_float(__builtin_amdgcn_readlane((int)v[i], 33)); mx = fmaxf(mx, m8[i]); }
      float acc = __builtin_amdgcn_exp2f(so - mx) * vo;
#pragma unroll
      for (int i = 0; i < 8; ++i) acc += __builtin_amdgcn_exp2f(m8[i] - mx) * __uint_as_float(v[i]);
      const float l = __int_as_float(__builtin_amdgcn_readlane(__float_as_int(acc), 32));
      const float ov = acc / l;
      const float o1 = __int_as_float(__builtin_amdgcn_update_dpp(0, __float_as_int(ov), 0x55, 0xf, 0xf, false));
      const float o2 = __int_as_float(__builtin_amdgcn_update_dpp(0, __float_as_int(ov), 0xaa, 0xf, 0xf, false));
      const float o3 = __int_as_float(__builtin_amdgcn_update_dpp(0, __float_as_int(ov), 0xff, 0xf, 0xf, false));
      const __amdgpu_buffer_rsrc_t rso = __builtin_amdgcn_make_buffer_rsrc(Of, 0, S_ * D_ * 2, 0x00020000);
      if (lane < 32 && (lane & 3) == 0)
        __builtin_amdgcn_raw_buffer_store_b64(u32x2{pack2(ov, o1), pack2(o2, o3)}, rso,
            (((idx >> 5) * 32 + head) * 1024 + (lane >> 4) * 512 + (((lane >> 2) & 1) * 32 + (idx & 31)) * 8 + ((lane >> 3) & 1) * 4) * 2, 0, 16);
      asm volatile("s_waitcnt vmcnt(0)" ::: "memory");
      if (lane < RO) st_ag(gran + GR_O + ((16 + (idx >> 7)) * RO + lane) * 64 + 16 + head, tag);
      STAMP(dk, 9);
    }
    }();
    }();
    __builtin_amdgcn_s_setprio(0);
    asm volatile("s_waitcnt vmcnt(0) lgkmcnt(0)" ::: "memory");
    PIN();
    ARGS_HERE;
    u32x2 w[16];
    auto ld_wd = [&]() __attribute__((always_inline)) {
      const __amdgpu_buffer_rsrc_t rd_ = __builtin_amdgcn_make_buffer_rsrc(const_cast<u16*>(wd), 0, D_ * F_ * 2, 0x00020000);
#pragma unroll
      for (int f = 0; f < 16; ++f) w[f] = __builtin_amdgcn_raw_buffer_load_b64(rd_, t * 8 + (f & 1) * 2048, (fg * 16 + (f & ~1)) * (D_ * 2), 0);
      PIN();
    };
    float fnum = 0.f, fvar = 0.f;
    u32x2 wq = {0u, 0u};
    const int rrow = omi * 128 + (oni >> 3) * 64 + (hw & 1) * 32;
    if (LAY2) {
      ld_wd();
      wq = *reinterpret_cast<const u32x2*>(w_r + hw * 256 + (lane & 3) * 64 + (lane >> 2) * 4);
      if (!p1blk) {
        const int fr = rrow + (lane & 7) * 4 + (lane >> 4);
        const double s1 = ld_ag(stats + fr), s2 = ld_ag(stats + S_ + fr), sw = ld_ag(stats + 2 * S_ + fr);
        lds_wr(lbase + SPM + 132, __float_as_uint(wsum));
        float mean, var, mb, den;
        ln_from_sums(s1, s2, mean, var, mb, den);
        const float g1 = 1.0f + frcp(den);
        fnum = fr == idx ? __uint_as_float(0x7fc00000u) : fmaf(-mean, wsum, (float)sw) * g1;
        fvar = var * g1 * g1;
      }
      asm volatile("" : "+v"(fnum), "+v"(fvar));
      PIN();
    }
    if (hw == 3) {

      const int nfl = (LAY2 && (idx >> 7) == omi) ? 48 : 16;
      auto pfl = (__attribute__((address_space(1))) const unsigned*)(gran + GR_O + (((LAY2 ? 16 : 0) + omi) * RO + oni % RO) * 64 + (lane < nfl ? lane : 0));
      constexpr unsigned vs = 8u * D_ * 2u - 1024u;
      unsigned vw0 = (unsigned)((lane >> 3) * D_ + oni * 64 + ((lane & 7) ^ ((lane >> 3) & 6)) * 8) * 2u, vw1 = vw0 + vs, vw2 = vw0 + 2 * vs, vw3 = vw0 + 3 * vs;
      asm volatile("" : "+v"(pfl), "+v"(vw0), "+v"(vw1), "+v"(vw2), "+v"(vw3));
      auto wait_arr = [&](unsigned n) {
#pragma unroll 1
        for (int i = 0; i < 4 * SPIN_MAX; ++i) {
          if (__builtin_amdgcn_readfirstlane(*LDS_AT(const volatile unsigned, lbase + FLG + 16)) >= n) break;
          __builtin_amdgcn_s_sleep(1);
        }
        asm volatile("" ::: "memory");
        PIN();
      };
      wait_arr(12u);
      unsigned fl;
      asm volatile("global_load_dword %0, %1, off sc1" : "=v"(fl) : "v"(pfl) : "memory");
#pragma unroll 1
      for (int i = 0; i < 4 * SPIN_MAX; ++i) {
        if (__builtin_amdgcn_readfirstlane(*LDS_AT(const volatile unsigned, lbase + FLG + 24)) >= 8u) break;
        __builtin_amdgcn_s_sleep(1);
      }
      asm volatile("" ::: "memory");
      PIN();
#pragma unroll
      for (int j = 24; j < 32; ++j)
        glds16x4<false>(reinterpret_cast<const char*>(wo) + j * (32u * D_ * 2u), vw0, vw1, vw2, vw3, lbase + j * 4096);
      unsigned keep;
      asm volatile("s_mov_b32 %0, m0\n\ts_mov_b32 m0, %3\n\ts_nop 2\n\tglobal_load_lds_dword %1, %2 sc1\n\ts_mov_b32 m0, %0"
                   : "=&s"(keep) : "v"(0u), "s"(reinterpret_cast<const char*>(gran + GR_N + bid * 64 + 39)), "s"(lbase + FLG - 252) : "memory");
      asm volatile("s_waitcnt vmcnt(33)" : "+v"(fl) :: "memory");
      if (__builtin_amdgcn_ballot_w64(fl != tag)) {
        __builtin_amdgcn_s_sleep(CSL1);
#pragma unroll 1
        for (int spin = 0; spin < SPIN_MAX; ++spin) {
          if (!__builtin_amdgcn_ballot_w64(__hip_atomic_load(pfl, __ATOMIC_RELAXED, __HIP_MEMORY_SCOPE_AGENT) != tag)) break;
          __builtin_amdgcn_s_sleep(CSLP);
        }
      }
      asm volatile("" ::: "memory");
      PIN();
    }
    u32x4 pv[4];
    asm volatile("" : "=v"(pv[0]), "=v"(pv[1]), "=v"(pv[2]), "=v"(pv[3]));
    if (LAY2 && !p1blk && hw < 2) {
      const __amdgpu_buffer_rsrc_t rsp = __builtin_amdgcn_make_buffer_rsrc(hand + 2 * HAND_N + PP_O, 0, H_ * S_ * 4, 0x00020000);
      const int src = ((lane >> 3) * S_ + rrow + (lane & 7) * 4) * 4;
#pragma unroll 1
      for (int i = 0; i < 4 * SPIN_MAX; ++i) {
        if (__builtin_amdgcn_readfirstlane(*LDS_AT(const volatile unsigned, lbase + FLG + 24)) >= 8u) break;
        __builtin_amdgcn_s_sleep(2);
      }
#pragma unroll 1
      for (int spin = 0; spin < SPIN_MAX; ++spin) {
        asm volatile("" ::: "memory");
        unsigned mx = 0u;
#pragma unroll
        for (int i = 0; i < 4; ++i) pv[i] = __builtin_amdgcn_raw_buffer_load_b128(rsp, src, i * (8 * S_ * 4), 16);
#pragma unroll
        for (int i = 0; i < 4; ++i) mx = umax(umax(umax(mx, pv[i][0]), pv[i][1]), umax(pv[i][2], pv[i][3]));
        if (none(mx == SENT)) break;
        __builtin_amdgcn_s_sleep(PPSLP);
      }
    }
    asm volatile("s_waitcnt lgkmcnt(0)" ::: "memory");
    __builtin_amdgcn_s_barrier();
    if (!LAY2) {
      PIN();
      asm volatile(AGAIN "\ngate_up_code:\n\t"
                 "v_lshlrev_b32 v84, 16, v100\n\tv_and_b32 v85, 0xffff0000, v100\n\tv_lshlrev_b32 v86, 16, v101\n\tv_and_b32 v87, 0xffff0000, v101\n\t"
                 "v_subrev_f32 v84, s84, v84\n\tv_subrev_f32 v85, s84, v85\n\tv_subrev_f32 v86, s84, v86\n\tv_subrev_f32 v87, s84, v87\n\t"
                 "v_cvt_pk_bf16_f32 v85, v84, v85\n\tv_cvt_pk_bf16_f32 v87, v86, v87\n\ts_mov_b32 s88, 0x01000100\n\ts_mov_b32 s89, 0x03020302\n\t"
                 "v_lshlrev_b32 v84, 16, v85\n\tv_and_b32 v85, 0xffff0000, v85\n\tv_lshlrev_b32 v86, 16, v87\n\tv_and_b32 v87, 0xffff0000, v87\n\t"
                 "v_mul_f32 v88, s86, v84\n\tv_mul_f32 v89, s86, v85\n\tv_mul_f32 v90, s86, v86\n\tv_mul_f32 v91, s86, v87\n\tv_fma_f32 v84, -v88, s85, v84\n\t"
                 "v_fma_f32 v85, -v89, s85, v85\n\tv_fma_f32 v86, -v90, s85, v86\n\tv_fma_f32 v87, -v91, s85, v87\n\tv_fmac_f32 v88, s86, v84\n\t"
                 "v_fmac_f32 v89, s86, v85\n\tv_fmac_f32 v90, s86, v86\n\tv_fmac_f32 v91, s86, v87\n\tv_cvt_pk_bf16_f32 v88, v88, v89\n\t"
                 "v_cvt_pk_bf16_f32 v90, v90, v91\n\tv_perm_b32 v92, v88, v88, s88\n\tv_perm_b32 v93, v88, v88, s89\n\tv_perm_b32 v94, v90, v90, s88\n\t"
                 "v_perm_b32 v95, v90, v90, s89\n\ts_nop 0\n\tv_and_b32_dpp v96, v92, v102 quad_perm:[0,0,0,0] row_mask:0xf bank_mask:0xf\n\t"
                 "v_and_b32_dpp v97, v92, v103 quad_perm:[0,0,0,0] row_mask:0xf bank_mask:0xf\n\t"
                 "v_and_b32_dpp v100, v93, v102 quad_perm:[0,0,0,0] row_mask:0xf bank_mask:0xf\n\t"
                 "v_and_b32_dpp v101, v93, v103 quad_perm:[0,0,0,0] row_mask:0xf bank_mask:0xf\n\tv_mfma_f32_4x4x4_16b_bf16 v[84:87], v[96:97], v[104:105], 0\n\t"
                 "v_mfma_f32_4x4x4_16b_bf16 v[88:91], v[96:97], v[106:107], 0\n\tv_and_b32_dpp v96, v94, v102 quad_perm:[0,0,0,0] row_mask:0xf bank_mask:0xf\n\t"
                 "v_and_b32_dpp v97, v94, v103 quad_perm:[0,0,0,0] row_mask:0xf bank_mask:0xf\n\t"
                 "v_mfma_f32_4x4x4_16b_bf16 v[84:87], v[100:101], v[108:109], v[84:87]\n\tv_mfma_f32_4x4x4_16b_bf16 v[88:91], v[100:101], v[110:111], v[88:91]\n\t"
                 "v_and_b32_dpp v100, v95, v102 quad_perm:[0,0,0,0] row_mask:0xf bank_mask:0xf\n\t"
                 "v_and_b32_dpp v101, v95, v103 quad_perm:[0,0,0,0] row_mask:0xf bank_mask:0xf\n\tv_mfma_f32_4x4x4_16b_bf16 v[84:87], v[96:97], v[112:113], v[84:87]\n\t"
                 "v_mfma_f32_4x4x4_16b_bf16 v[88:91], v[96:97], v[114:115], v[88:91]\n\tv_and_b32_dpp v96, v92, v102 quad_perm:[1,1,1,1] row_mask:0xf bank_mask:0xf\n\t"
                 "v_and_b32_dpp v97, v92, v103 quad_perm:[1,1,1,1] row_mask:0xf bank_mask:0xf\n\t"
                 "v_mfma_f32_4x4x4_16b_bf16 v[84:87], v[100:101], v[116:117], v[84:87]\n\tv_mfma_f32_4x4x4_16b_bf16 v[88:91], v[100:101], v[118:119], v[88:91]\n\t"
                 "v_and_b32_dpp v100, v93, v102 quad_perm:[1,1,1,1] row_mask:0xf bank_mask:0xf\n\t"
                 "v_and_b32_dpp v101, v93, v103 quad_perm:[1,1,1,1] row_mask:0xf bank_mask:0xf\n\tv_mfma_f32_4x4x4_16b_bf16 v[84:87], v[96:97], v[120:121], v[84:87]\n\t"
                 "v_mfma_f32_4x4x4_16b_bf16 v[88:91], v[96:97], v[122:123], v[88:91]\n\tv_and_b32_dpp v96, v94, v102 quad_perm:[1,1,1,1] row_mask:0xf bank_mask:0xf\n\t"
                 "v_and_b32_dpp v97, v94, v103 quad_perm:[1,1,1,1] row_mask:0xf bank_mask:0xf\n\t"
                 "v_mfma_f32_4x4x4_16b_bf16 v[84:87], v[100:101], v[124:125], v[84:87]\n\tv_mfma_f32_4x4x4_16b_bf16 v[88:91], v[100:101], v[126:127], v[88:91]\n\t"
                 "v_and_b32_dpp v100, v95, v102 quad_perm:[1,1,1,1] row_mask:0xf bank_mask:0xf\n\t"
                 "v_and_b32_dpp v101, v95, v103 quad_perm:[1,1,1,1] row_mask:0xf bank_mask:0xf\n\tv_mfma_f32_4x4x4_16b_bf16 v[84:87], v[96:97], v[128:129], v[84:87]\n\t"
                 "v_mfma_f32_4x4x4_16b_bf16 v[88:91], v[96:97], v[130:131], v[88:91]\n\tv_and_b32_dpp v96, v92, v102 quad_perm:[2,2,2,2] row_mask:0xf bank_mask:0xf\n\t"
                 "v_and_b32_dpp v97, v92, v103 quad_perm:[2,2,2,2] row_mask:0xf bank_mask:0xf\n\t"
                 "v_mfma_f32_4x4x4_16b_bf16 v[84:87], v[100:101], v[132:133], v[84:87]\n\tv_mfma_f32_4x4x4_16b_bf16 v[88:91], v[100:101], v[134:135], v[88:91]\n\t"
                 "v_and_b32_dpp v100, v93, v102 quad_perm:[2,2,2,2] row_mask:0xf bank_mask:0xf\n\t"
                 "v_and_b32_dpp v101, v93, v103 quad_perm:[2,2,2,2] row_mask:0xf bank_mask:0xf\n\tv_mfma_f32_4x4x4_16b_bf16 v[84:87], v[96:97], v[136:137], v[84:87]\n\t"
                 "v_mfma_f32_4x4x4_16b_bf16 v[88:91], v[96:97], v[138:139], v[88:91]\n\tv_and_b32_dpp v96, v94, v102 quad_perm:[2,2,2,2] row_mask:0xf bank_mask:0xf\n\t"
                 "v_and_b32_dpp v97, v94, v103 quad_perm:[2,2,2,2] row_mask:0xf bank_mask:0xf\n\t"
                 "v_mfma_f32_4x4x4_16b_bf16 v[84:87], v[100:101], v[140:141], v[84:87]\n\tv_mfma_f32_4x4x4_16b_bf16 v[88:91], v[100:101], v[142:143], v[88:91]\n\t"
                 "v_and_b32_dpp v100, v95, v102 quad_perm:[2,2,2,2] row_mask:0xf bank_mask:0xf\n\t"
                 "v_and_b32_dpp v101, v95, v103 quad_perm:[2,2,2,2] row_mask:0xf bank_mask:0xf\n\tv_mfma_f32_4x4x4_16b_bf16 v[84:87], v[96:97], v[144:145], v[84:87]\n\t"
                 "v_mfma_f32_4x4x4_16b_bf16 v[88:91], v[96:97], v[146:147], v[88:91]\n\tv_and_b32_dpp v96, v92, v102 quad_perm:[3,3,3,3] row_mask:0xf bank_mask:0xf\n\t"
                 "v_and_b32_dpp v97, v92, v103 quad_perm:[3,3,3,3] row_mask:0xf bank_mask:0xf\n\t"
                 "v_mfma_f32_4x4x4_16b_bf16 v[84:87], v[100:101], v[148:149], v[84:87]\n\tv_mfma_f32_4x4x4_16b_bf16 v[88:91], v[100:101], v[150:151], v[88:91]\n\t"
                 "v_and_b32_dpp v100, v93, v102 quad_perm:[3,3,3,3] row_mask:0xf bank_mask:0xf\n\t"
                 "v_and_b32_dpp v101, v93, v103 quad_perm:[3,3,3,3] row_mask:0xf bank_mask:0xf\n\tv_mfma_f32_4x4x4_16b_bf16 v[84:87], v[96:97], v[152:153], v[84:87]\n\t"
                 "v_mfma_f32_4x4x4_16b_bf16 v[88:91], v[96:97], v[154:155], v[88:91]\n\tv_and_b32_dpp v96, v94, v102 quad_perm:[3,3,3,3] row_mask:0xf bank_mask:0xf\n\t"
                 "v_and_b32_dpp v97, v94, v103 quad_perm:[3,3,3,3] row_mask:0xf bank_mask:0xf\n\t"
                 "v_mfma_f32_4x4x4_16b_bf16 v[84:87], v[100:101], v[156:157], v[84:87]\n\tv_mfma_f32_4x4x4_16b_bf16 v[88:91], v[100:101], v[158:159], v[88:91]\n\t"
                 "v_and_b32_dpp v100, v95, v102 quad_perm:[3,3,3,3] row_mask:0xf bank_mask:0xf\n\t"
                 "v_and_b32_dpp v101, v95, v103 quad_perm:[3,3,3,3] row_mask:0xf bank_mask:0xf\n\tv_mfma_f32_4x4x4_16b_bf16 v[84:87], v[96:97], v[160:161], v[84:87]\n\t"
                 "v_mfma_f32_4x4x4_16b_bf16 v[88:91], v[96:97], v[162:163], v[88:91]\n\ts_nop 1\n\t"
                 "v_mfma_f32_4x4x4_16b_bf16 v[84:87], v[100:101], v[164:165], v[84:87]\n\tv_mfma_f32_4x4x4_16b_bf16 v[88:91], v[100:101], v[166:167], v[88:91]\n\t"
                 "s_nop 4\n\tv_permlane32_swap_b32 v84, v88\n\tv_permlane32_swap_b32 v85, v89\n\tv_permlane32_swap_b32 v86, v90\n\tv_permlane32_swap_b32 v87, v91\n\t"
                 "v_add_f32 v84, v84, v88\n\tv_add_f32 v85, v85, v89\n\tv_add_f32 v86, v86, v90\n\tv_add_f32 v87, v87, v91\n\ts_nop 0\n\t"
                 "v_permlane16_swap_b32 v84, v85\n\tv_permlane16_swap_b32 v86, v87\n\ts_nop 0\n\tv_add_f32 v84, v84, v85\n\tv_add_f32 v86, v86, v87\n\ts_nop 1\n\t"
                 "v_add_f32_dpp v84, v84, v84 row_ror:8 row_mask:0xf bank_mask:0xf\n\tv_add_f32_dpp v86, v86, v86 row_ror:8 row_mask:0xf bank_mask:0xf\n\ts_nop 0\n\t"
                 "v_add_f32_dpp v84, v84, v84 row_ror:4 row_mask:0xf bank_mask:0xf\n\tv_add_f32_dpp v86, v86, v86 row_ror:4 row_mask:0xf bank_mask:0xf\n\t"
                 "ds_write2_b32 v99, v84, v86 offset1:8\n\ts_setpc_b64 s[92:93]\n\t"
                   :: "{s[0:1]}"(ap + 1), "{s2}"(bid), "{v0}"(wid * 64), KEPT() : "s90", "s91", "scc", "memory");
      __builtin_unreachable();
    }
    {
    constexpr int dk = 8;
    constexpr int BEST = FLG + 40, MEET = FLG + 48;
    __builtin_amdgcn_s_setprio(HPRIO);
    if (hw == 0) STAMP(dk, 0);
    const unsigned tb1 = (unsigned)__builtin_amdgcn_s_memrealtime();
    unsigned* hand2 = hand + HAND_N;
    const __amdgpu_buffer_rsrc_t rsh = __builtin_amdgcn_make_buffer_rsrc(hand2, 0, (HAND_N + PS_N) * 4, 0x00020000);
    const __amdgpu_buffer_rsrc_t rsx = __builtin_amdgcn_make_buffer_rsrc(xout, 0, 3 * D_ * 2, 0x00020000);
    constexpr int OOR = (HAND_N + PS_N) * 4;
    if (!p1blk && hw < 2) {
      f32x2 a[2], c[2];
#pragma unroll
      for (int e = 0; e < 4; ++e) {
        a[e >> 1][e & 1] = (__uint_as_float(pv[0][e] << 16) + __uint_as_float(pv[1][e] << 16)) + (__uint_as_float(pv[2][e] << 16) + __uint_as_float(pv[3][e] << 16));
        c[e >> 1][e & 1] = (__uint_as_float(pv[0][e] & 0xffff0000u) + __uint_as_float(pv[1][e] & 0xffff0000u)) + (__uint_as_float(pv[2][e] & 0xffff0000u) + __uint_as_float(pv[3][e] & 0xffff0000u));
      }
      const float r = (fnum + fold8(a[0], a[1])) * __builtin_amdgcn_rsqf(fvar + fold8(c[0], c[1]));
      unsigned key = (__float_as_uint(r) ^ ((unsigned)(__float_as_int(r) >> 31) | 0x80000000u)) & 0xfffff800u;
      if (r != r) key = 0u;
      key = wave_umax(key | (unsigned)(2047 - (rrow + (lane & 7) * 4 + (lane >> 4))));
      __builtin_amdgcn_raw_buffer_store_b64(u32x2{key, tb1 & 0x7fffffffu}, rsh, lane < NREP ? (HAND_N + PK_O + lane * 128 + (rrow >> 5) * 2) * 4 : OOR, 0, 16);
      if (hw == 1) STAMP(dk, 2);
    }
    gu_setup();
    bool got = true;
    unsigned fra = lbase;
    int fro = S_ * D_ * 2;
    auto get_fc = [&]() __attribute__((always_inline)) {
      const u32x2 fw = __builtin_amdgcn_raw_buffer_load_b64(rsh, (HAND_N + PK_O + (bid % NREP) * 128 + lane * 2) * 4, 0, 16);
      got = none(umax(fw[0], fw[1]) == SENT);
      if (got) {
        const unsigned c1 = wave_umax(fw[0]), c2 = wave_umax(fw[0] == c1 ? 0u : fw[0]);
#define BADFC 0
        const unsigned r1 = (2047u - (c1 & 0x7ffu)) ^ BADFC, r2 = 2047u - (c2 & 0x7ffu);
        const unsigned r = (lane & 8) ? r2 : r1;
        fra = lbase + QOFF + (r & 127u) * 128 + (((r ^ lane) & 7u) << 4);
        fro = (r >> 7) == (unsigned)omi && lane < 16 ? ((lane >> 3) * D_ + oni * 64 + (lane & 7) * 8) * 2 : S_ * D_ * 2;
        unsigned dt = (unsigned)((int)((fw[1] - tb1) << 1) >> 1) + 512u;
        dt = dt < 1024u ? dt : 512u;
        *LDS_AT(volatile u32x2, lbase + SPM + 144) = u32x2{r1 | (r2 << 16), (unsigned)__builtin_amdgcn_readlane(dt, r1 >> 5) | (wave_umax(dt) << 16)};
      }
    };
    if (hw == 0) {
      __builtin_amdgcn_s_sleep(FCPRE);
#pragma unroll 1
      for (int i = 0; i < SPIN_MAX; ++i) {
        if (__builtin_amdgcn_readfirstlane(*LDS_AT(const volatile unsigned, lbase + FLG + 28)) - best >= 4u) break;
        __builtin_amdgcn_s_sleep(FCSLP);
      }
      asm volatile("" ::: "memory");
      get_fc();
    } else __builtin_amdgcn_s_sleep(TPRE - 10);
    PIN();
    tile_at(2u);
    const unsigned tb2 = (unsigned)__builtin_amdgcn_s_memrealtime() - 512u;
    LOGDEF;
    LOG();
#pragma unroll 1
    for (int spin = 0; spin < SPIN_MAX && !got; ++spin) { get_fc(); asm volatile("" ::: "memory"); }
    if (hw == 0) __builtin_amdgcn_raw_buffer_store_b128(lds_rd4(fra), rsx, fro, 0, XAUX);
    PIN();
    u32x2 fm = {SENT, 0u};
#pragma unroll 1
    for (int i = 0; i < 4 * SPIN_MAX && fm[0] == SENT; ++i) {
      const u32x2 q = *LDS_AT(const volatile u32x2, lbase + SPM + 144);
      fm = u32x2{(unsigned)__builtin_amdgcn_readfirstlane(q[0]), (unsigned)__builtin_amdgcn_readfirstlane(q[1])};
    }
    const int fr1 = fm[0] & 0x7ffu, fr2 = (fm[0] >> 16) & 0x7ffu;
    const unsigned t1 = tb2 + (fm[1] & 1023u), tmx = tb2 + ((fm[1] >> 16) & 1023u);
    auto until = [&](unsigned te) { while ((int)((unsigned)__builtin_amdgcn_s_memrealtime() - te) < 0) __builtin_amdgcn_s_sleep(1); asm volatile("" ::: "memory"); };
    lds_wr(lbase + SPM + 136, tmx);
    PIN();
    if (hw == 0) STAMP(dk, 3);
    const int xoff = (hw * 256 + (lane & 3) * 64 + (lane >> 2) * 4) * 2;
    const int oc = (omi * 2 + (oni >> 3)) * 32, col = oc + (lane & 7) * 4 + (lane >> 4);
    const int xoo = !p1blk && hw == 3 ? (col & ~1) * 2 : S_ * D_ * 2;
    auto get_row = [&](int r, int sl, u32x2& xr, unsigned& xo) __attribute__((always_inline)) {
      if ((r >> 7) == omi) {
#pragma unroll 1
        for (int i = 0; i < SPIN_MAX; ++i) {
          if (__builtin_amdgcn_readfirstlane(*LDS_AT(const volatile unsigned, lbase + FLG + 24)) >= 16u) break;
          __builtin_amdgcn_s_sleep(1);
        }
        asm volatile("" ::: "memory");
      }
#pragma unroll 1
      for (int spin = 0; spin < SPIN_MAX; ++spin) {
        LOG();
        xr = __builtin_amdgcn_raw_buffer_load_b64(rsx, xoff, sl * (D_ * 2), 16);
        xo = __builtin_amdgcn_raw_buffer_load_b32(rsx, xoo, sl * (D_ * 2), 16);
        LOG();
        if (none(umax(umax(xr[0], xr[1]), xo) == SENT)) break;
        __builtin_amdgcn_s_sleep(XRSLP);
        asm volatile("" ::: "memory");
      }
      PIN();
    };
    unsigned rk1;
    auto row_md = [&](const u32x2& xr, int k) __attribute__((always_inline)) {
      auto dot = [&](unsigned x, unsigned y, float z) { return __builtin_amdgcn_fdot2_f32_bf16(__builtin_bit_cast(bf16x2, x), __builtin_bit_cast(bf16x2, y), z, false); };
      const float p1 = dot(xr[1], 0x3f803f80u, dot(xr[0], 0x3f803f80u, 0.f)), p2 = dot(xr[1], xr[1], dot(xr[0], xr[0], 0.f)), p3 = dot(xr[1], wq[1], dot(xr[0], wq[0], 0.f));
      const auto q = __builtin_amdgcn_permlane32_swap(__float_as_uint(p1), __float_as_uint(p2), false, false);
      const auto q3 = __builtin_amdgcn_permlane32_swap(__float_as_uint(p3), __float_as_uint(p3), false, false);
      const auto q2 = __builtin_amdgcn_permlane16_swap(__float_as_uint(__uint_as_float(q[0]) + __uint_as_float(q[1])), __float_as_uint(__uint_as_float(q3[0]) + __uint_as_float(q3[1])), false, false);
      float sv = __uint_as_float(q2[0]) + __uint_as_float(q2[1]);
      sv = dpp_add<0x121, 0xf>(dpp_add<0x122, 0xf>(dpp_add<0x124, 0xf>(dpp_add<0x128, 0xf>(sv))));
      lds_wr(lbase + SPM + k * 64 + (lane >> 4) * 16 + hw * 4, f2word(sv));
      u32x4 a, c, e;
#pragma unroll 1
      for (int i = 0; i < SPIN_MAX; ++i) {
        a = lds_rd4(lbase + SPM + k * 64); c = lds_rd4(lbase + SPM + k * 64 + 32); e = lds_rd4(lbase + SPM + k * 64 + 16);
        if (__builtin_amdgcn_readfirstlane(umax(umax(a[0], a[1]), umax(a[2], a[3]))) != SENT) break;
      }
      auto sum4 = [&](const u32x4& v) { return (__uint_as_float(v[0]) + __uint_as_float(v[1])) + (__uint_as_float(v[2]) + __uint_as_float(v[3])); };
      const float mean = sum4(a) * (1.0f / 1024.0f), msq = sum4(c) * (1.0f / 1024.0f);
      const float den = rbf(__builtin_amdgcn_sqrtf(rbf(rbf(fmaxf(fmaf(-mean, mean, msq), 0.f)) + 1e-5f)));
      if (k == 0) { const float r = fmaf(-rbf(mean), wsum, sum4(e)) * frcp(den); rk1 = __builtin_amdgcn_readfirstlane(pack2(r, r)) >> 16; }
      return (unsigned)__builtin_amdgcn_readfirstlane((pack2(mean, mean) & 0xffffu) | (__float_as_uint(den) & 0xffff0000u));
    };
    auto ffn = [&](const u32x2& xr, unsigned md, unsigned n, const __amdgpu_buffer_rsrc_t rs, int yo, auto&& off) __attribute__((always_inline)) {
      gate_up(xr, md);
      if (hw == 0 && n == 2u) STAMP(dk, 8);
      asm volatile("" ::: "memory");
      PIN();
      lds_wr(lbase + MEET + hw * 4, n + off());
      u32x4 mt;
#pragma unroll 1
      for (int i = 0; i < SPIN_MAX; ++i) {
        mt = lds_rd4(lbase + MEET);
        if (__builtin_amdgcn_readfirstlane(min4(mt)) >= n) break;
      }
      asm volatile("" ::: "memory");
      PIN();
      if (hw == 0 && n == 2u) STAMP(dk, 9);
      if (__builtin_amdgcn_readfirstlane(mt[0]) != n + 1u)
        __builtin_amdgcn_raw_buffer_store_b128(down(act_diag(n == 4u ? 512 : 0), w), rs, (yo + fg * D_) * 4 + t * 16, 0, 16);
      dad += n == 2u ? 512 : -512;
      if (hw == 0 && n == 2u) STAMP(dk, 4);
    };
    auto y_sum4 = [&](const __amdgpu_buffer_rsrc_t rs, int yo, int k) __attribute__((always_inline)) {
      const int src = (yo + (hw * 56 + (lane >> 3)) * D_ + oc + (lane & 7) * 4) * 4;
      u32x4 v[7];
#pragma unroll
      for (int i = 0; i < 7; ++i) asm volatile("" : "=v"(v[i]));
      unsigned todo = 0x7fu;
#pragma unroll 1
      for (int spin = 0; spin < SPIN_MAX; ++spin) {
        LOG();
#pragma unroll
        for (int i = 0; i < 7; ++i) if ((todo >> i) & 1u) v[i] = __builtin_amdgcn_raw_buffer_load_b128(rs, src, i * (8 * D_ * 4), 16);
#pragma unroll
        for (int i = 0; i < 7; ++i) if (none(umax(umax(v[i][0], v[i][1]), umax(v[i][2], v[i][3])) == SENT)) todo &= ~(1u << i);
        if (!todo) break;
        __builtin_amdgcn_s_sleep(YSLP);
        asm volatile("" ::: "memory");
      }
      f32x2 z0 = {0.f, 0.f}, z1 = {0.f, 0.f};
#pragma unroll
      for (int i = 0; i < 7; ++i) {
        z0 += f32x2{__uint_as_float(v[i][0]), __uint_as_float(v[i][1])};
        z1 += f32x2{__uint_as_float(v[i][2]), __uint_as_float(v[i][3])};
      }
      const unsigned ya = lbase + SPM + 256 + k * 512 + ((lane & 7) * 4 + (lane >> 4)) * 16;
      lds_wr((lane & 8) ? lbase + DUM + lane * 4 : ya + hw * 4, f2word(fold8(z0, z1)));
      u32x4 q = {0u, 0u, 0u, 0u};
      if (hw == 3) {
#pragma unroll 1
        for (int i = 0; i < 4 * SPIN_MAX; ++i) {
          q = lds_rd4(ya);
          if (none(umax(umax(q[0], q[1]), umax(q[2], q[3])) == SENT)) break;
        }
      }
      return (__uint_as_float(q[0]) + __uint_as_float(q[1])) + (__uint_as_float(q[2]) + __uint_as_float(q[3]));
    };
    const __amdgpu_buffer_rsrc_t rs1 = __builtin_amdgcn_make_buffer_rsrc(hand, 0, HAND_N * 4, 0x00020000);
    u32x2 xr;
    unsigned xo, xo2;
    until(t1 + XDT);
    get_row(fr1, 0, xr, xo);
    if (hw == 0) STAMP(dk, 7);
#define FAILFC 0
    const unsigned md1 = row_md(xr, 0) ^ (FAILFC & 1);
    if (hw == 0) STAMP(dk, 10);
    LOGAT(20);
    u32x2 xr2;
    unsigned md2 = 0u;
    if (p1blk) {
      xr2 = __builtin_amdgcn_raw_buffer_load_b64(rsx, (fr2 >> 7) == omi ? S_ * D_ * 2 : xoff, D_ * 2, 16);
      PIN();
      ffn(xr, md1, 2u, rsh, GRAN_N, [&]() { return 0u; });
      __builtin_amdgcn_s_setprio(R2PRIO);
      LOG();
      if ((fr2 >> 7) == omi || !none(umax(xr2[0], xr2[1]) == SENT)) get_row(fr2, 1, xr2, xo2);
      md2 = row_md(xr2, 1) ^ (FAILFC & 2);
      ffn(xr2, md2, 4u, rs1, GRAN_N, [&]() {
        const u32x2 v = *LDS_AT(const volatile u32x2, lbase + BEST);
        return v[0] != 0u && (2047 - (int)(v[0] & 0x7ffu) != fr2 || v[1] != md2) ? 1u : 0u; });
    }
    if (hw == 0) STAMP(dk, 5);
    LOG();
    float y = 0.f;
    auto put_row = [&](int r, unsigned md, unsigned rb) __attribute__((always_inline)) {
      const float rden = __uint_as_float(md & 0xffff0000u), rs = rbf(frcp(1.0f + __expf(-__uint_as_float(rb << 16))));
      const float xa = __uint_as_float((col & 1) ? xo & 0xffff0000u : xo << 16);
      float ha = xa, hb = xa;
      ln_apply2(ha, hb, __uint_as_float(md << 16), rden, frcp(rden));
      const float o = xa + rbf(ha + rbf(rbf(y) * rs));
      __builtin_amdgcn_raw_buffer_store_b16((unsigned short)(pack2(o, o) & 0xffffu), __builtin_amdgcn_make_buffer_rsrc(out, 0, hw == 3 ? S_ * D_ * 2 : 0, 0x00020000),
                                            (lane & 8) ? S_ * D_ * 2 : (r * D_ + col) * 2, 0, OAUX);
    };
    if (!p1blk) {
      until(t1 + XDT + YD1); until(tmx + YD2);
      if (hw == 3) STAMP(dk, 13);
      LOGAT(8);
      y = y_sum4(rsh, GRAN_N, 0);
      put_row(fr1, md1, rk1);
      if (hw == 3) STAMP(dk, 14);
      LOGAT(24);
    }
    unsigned bmd = 0u;
    best = 0u;
#pragma unroll 1
    for (int spin = 0; spin < 4 * SPIN_MAX; ++spin) {
      const u32x2 v = *LDS_AT(const volatile u32x2, lbase + BEST);
      best = __builtin_amdgcn_readfirstlane(v[0]); bmd = __builtin_amdgcn_readfirstlane(v[1]);
      if (best) break;
      __builtin_amdgcn_s_sleep(1);
    }
    asm volatile("" ::: "memory");
    const int idx = 2047 - (int)(best & 0x7ffu);
    const unsigned rk = best >> 16, rb = (rk & 0x8000u) ? (rk & 0x7fffu) : (~rk & 0xffffu);
    if (hw == 0) STAMP(dk, 1);
    LOGAT(25);
    LOGOUT(hw);
    if (__builtin_expect(idx != fr1 || bmd != md1 || rb != rk1, 0)) {
      const __amdgpu_buffer_rsrc_t rsf = __builtin_amdgcn_make_buffer_rsrc(gran + GR_F, 0, !p1blk && hw == 3 && idx != fr1 ? 48 * 128 : 0, 0x00020000);
      asm volatile("s_waitcnt vmcnt(0)" ::: "memory");
      __builtin_amdgcn_raw_buffer_store_b32(tag, rsf, lane == 0 ? (16 + (oc >> 5)) * 128 : 48 * 128, 0, 16);
      if (idx != fr1 || bmd != md1) {
        if (!p1blk || idx != fr2 || bmd != md2) get_row(idx, idx == fr1 ? 0 : (idx == fr2 ? 1 : 2), xr, xo);
        if (!p1blk && idx == fr2) md2 = row_md(xr, 1) ^ (FAILFC & 2);
        if (idx == fr2 && bmd == md2) {
          if (!p1blk) y = y_sum4(rs1, GRAN_N, 1);
        } else {
          if (p1blk) ffn(xr, bmd, 6u, rsh, HAND_N + Y3_O, [&]() { return 0u; });
          else y = y_sum4(rsh, HAND_N + Y3_O, 2);
        }
      }
      if (p1blk || hw != 3) return;
#pragma unroll 1
      for (int spin = 0; spin < SPIN_MAX && idx != fr1; ++spin) {
        if (__builtin_amdgcn_readfirstlane(__builtin_amdgcn_raw_buffer_load_b32(rsf, (oc >> 6) * 128, 0, 16)) == tag) break;
        asm volatile("" ::: "memory");
      }
      put_row(idx, bmd, rb);
    }
    if (p1blk || hw != 3) return;
    }
    return;
  }
  if (wid >= 4) {
    const int lane = fresh_lane();
    const int lw = wid - 4;
    const long long lay_ = ap->lay;
    const u16* A = ap->a; const u16* W = ap->W; const u16* wc = ap->wc;
#define LPRIO 3
#define LP0 3
    __builtin_amdgcn_s_setprio(LP0);
    unsigned va[4], vw[6];
#pragma unroll
    for (int i = 0; i < 4; ++i) {
      const int L = (lw * 4 + i) * 64 + lane, row = L >> 3, cp = L & 7, c = cp ^ (((row >> 1) & 3) | (((row >> 4) & 1) << 2));
      va[i] = (unsigned)((m0 + row) * K + c * 8) * 2u - i * 1024u;
    }
#pragma unroll
    for (int i = 0; i < 6; ++i) {
      const int L2 = (lw * 6 + i) * 64 + lane, k = L2 / 24, cp = L2 % 24, c = cp ^ (4 * ((k >> 1) & 1)) ^ ((k >> 3) & 1);
      vw[i] = (unsigned)(k * ldw + (c >> 3) * 1024 + n0 + (c & 7) * 8) * 2u - i % 3 * 1024u;
    }
    PIN();
    asm volatile("" : "+v"(va[0]), "+v"(va[1]), "+v"(va[2]), "+v"(va[3]), "+v"(vw[0]), "+v"(vw[1]), "+v"(vw[2]), "+v"(vw[3]), "+v"(vw[4]), "+v"(vw[5]));
    const bool LAY2 = lay_ != 0;
    const int krot = tmi & 8;
    if (LAY2 && slow_cols(W, tni)) W = wc;
    auto stage_a = [&](int kt, int slot) {
      const char* pa = reinterpret_cast<const char*>(A) + (size_t)(kt ^ krot) * 128;
      asm volatile("s_mov_b32 m0, %5\n\ts_nop 0\n\tglobal_load_lds_dwordx4 %0, %4 sc1\n\tglobal_load_lds_dwordx4 %1, %4 offset:1024 sc1\n\t"
                   "global_load_lds_dwordx4 %2, %4 offset:2048 sc1\n\tglobal_load_lds_dwordx4 %3, %4 offset:3072 sc1"
                   :: "v"(va[0]), "v"(va[1]), "v"(va[2]), "v"(va[3]), "s"(pa), "s"(lbase + slot * STB + lw * 4096) : "m0", "memory");
    };
    auto stage_w = [&](int kt, int slot) {
      const char* pw = reinterpret_cast<const char*>(W) + (size_t)(kt ^ krot) * 128 * ldw;
      asm volatile("s_mov_b32 m0, %7\n\ts_nop 0\n\tglobal_load_lds_dwordx4 %0, %6 sc1\n\tglobal_load_lds_dwordx4 %1, %6 offset:1024 sc1\n\t"
                   "global_load_lds_dwordx4 %2, %6 offset:2048 sc1\n\ts_addk_i32 m0, 0xc00\n\ts_nop 0\n\tglobal_load_lds_dwordx4 %3, %6 sc1\n\t"
                   "global_load_lds_dwordx4 %4, %6 offset:1024 sc1\n\tglobal_load_lds_dwordx4 %5, %6 offset:2048 sc1"
                   :: "v"(vw[0]), "v"(vw[1]), "v"(vw[2]), "v"(vw[3]), "v"(vw[4]), "v"(vw[5]), "s"(pw), "s"(lbase + slot * STB + ABYTES + lw * 6144) : "m0", "scc", "memory");
    };
    if (LAY2) {
      __builtin_amdgcn_s_setprio(0);
      __builtin_amdgcn_s_sleep(TPRE - 10);
      tile_at(3u);
      __builtin_amdgcn_s_setprio(LPRIO);
#pragma unroll
      for (int st = 0; st < NST; ++st) stage_w(st, st);
      PIN();
      __builtin_amdgcn_s_barrier();
      PIN();
#pragma unroll
      for (int st = 0; st < NST; ++st) stage_a(st, st);
      asm volatile("s_waitcnt vmcnt(8)" ::: "memory");
    } else {
      __builtin_amdgcn_s_setprio(LPRIO);
      stage_a(0, 0); stage_w(0, 0);
      if (PRO > 1) { stage_a(1, 1); stage_w(1, 1); }
      if (PRO > 2) { stage_a(2, 2); stage_w(2, 2); }
      asm volatile("s_waitcnt vmcnt(%0)" :: "n"(10 * PRO - 10) : "memory");
    }
    int slot = 0;
    __builtin_amdgcn_s_barrier();
    if (!LAY2) {
      if (PRO < 2) { stage_a(1, 1); stage_w(1, 1); }
      if (PRO < 3) { stage_a(2, 2); stage_w(2, 2); }
    }
    for (int kt = 0; kt + 1 < NKT; ++kt) {
      if (LAY2 && kt == 0) asm volatile("s_waitcnt vmcnt(4)" ::: "memory");
      else if (kt + 2 >= NKT) asm volatile("s_waitcnt vmcnt(0)" ::: "memory");
      else asm volatile("s_waitcnt vmcnt(10)" ::: "memory");
      __builtin_amdgcn_s_barrier();
      if (kt + NST < NKT) { stage_a(kt + NST, slot); stage_w(kt + NST, slot); }
      slot = slot == NST - 1 ? 0 : slot + 1;
    }
    __builtin_amdgcn_s_setprio(0);
    __builtin_amdgcn_s_barrier();
  } else {
  LAYER_HERE;
  const int dk = LAY2 ? 5 : 1;
  const int lane = fresh_lane(), wm = wid & 1, wn = wid >> 1;
  if (wid == 0) STAMP(dk, 0);
  f32x4 acc[4][6];
#pragma unroll
  for (int i = 0; i < 4; ++i)
#pragma unroll
    for (int j = 0; j < 6; ++j) acc[i][j] = f32x4{0.f, 0.f, 0.f, 0.f};
  const int q16 = lane & 15, g4 = lane >> 4;
  const int e0 = 16 * (q16 >> 3) + (q16 & 7);
  s16x8 af[2][4];
  unsigned aad[2], wad[2];
  {
    const int row = wm * 64 + e0, swz = ((row >> 1) & 3) | (((row >> 4) & 1) << 2);
#pragma unroll
    for (int h = 0; h < 2; ++h) aad[h] = lbase + row * 128 + (((4 * h + g4) ^ swz) << 4);
    const int tq = (lane & 15) >> 2, tp = lane & 3, k = 8 * g4 + tq, ch = wn * 4 + 2 * (tp >> 1);
    wad[0] = lbase + ABYTES + k * 384 + ((ch ^ (4 * ((k >> 1) & 1)) ^ (g4 & 1)) << 4) + 8 * (tp & 1);
    wad[1] = wad[0] ^ 16;
  }
  auto lda = [&](unsigned a, int i) { return *LDS_AT(const s16x8, a + (i >> 1) * 4096 + (i & 1) * 1024); };
  auto ldw_ = [&](int h, int j) {
    const unsigned w = wad[j & 1] + h * 12288 + (j >> 1) * 128;
    s16x4 lo = __builtin_amdgcn_ds_read_tr16_b64_v4i16(LDS_AT(s16x4, w));
    s16x4 h4 = __builtin_amdgcn_ds_read_tr16_b64_v4i16(LDS_AT(s16x4, w + 1536));
    return (s16x8)__builtin_shufflevector(lo, h4, 0, 1, 2, 3, 4, 5, 6, 7);
  };
  ARGS_HERE;
  unsigned lto = lbase + wid * 4096;
  char* kvb = reinterpret_cast<char*>(KV) + ((tni * 2 + wn) * 64 + tmi * 4 + wm * 2) * 2048 - lto;
  {
    const unsigned ato = lbase + ALPHA + wm * 256, dum = lbase + DUM + lane * 4;
    *LDS_AT(u32x4, lto + QOFF + lane * 16) = u32x4{lane >= 16 ? lbase + CRED : lbase + TABA + (wm * 64 + e0) * 16, lbase + TABW + (wn * 32 + e0) * 16,
                                                   ato + e0 * 4, lto + (lane >> 5) * 1024 + ((lane & 28) * 2 + (lane & 3)) * 16};
    *LDS_AT(u32x2, lto + QOFF + 2048 + lane * 16) = u32x2{aad[0], aad[1]};
    *LDS_AT(u32x4, lto + QOFF + 1024 + lane * 16) = u32x4{ato + g4 * 16 + (lane >> 5) * 32, lto + (lane >> 5) * 1024 + ((lane & 24) * 2 + (lane & 7)) * 16,
                                                          lane == 0 ? lbase + FLG + 32 : dum, lane == 0 ? lbase + FLG + 16 : dum};
  }
  asm volatile("" : "+s"(lto), "+s"(kvb) :: "memory");
#pragma unroll
  for (int i = 0; i < 4; ++i)
#pragma unroll
    for (int j = 0; j < 6; ++j) asm volatile("" : "+v"(acc[i][j]));
  asm volatile("" : "+v"(aad[0]), "+v"(aad[1]), "+v"(wad[0]), "+v"(wad[1]));
  if (LAY2) __builtin_amdgcn_s_barrier();
  __builtin_amdgcn_s_barrier();
  PIN();
  if (wid == 0) STAMP(dk, 1);
  s16x8 wq[4][2];
  wq[0][0] = ldw_(0, 0); af[0][0] = lda(aad[0], 0); wq[0][1] = ldw_(1, 0); af[1][0] = lda(aad[1], 0); wq[1][0] = ldw_(0, 1); wq[1][1] = ldw_(1, 1);
  af[0][1] = lda(aad[0], 1); af[1][1] = lda(aad[1], 1); af[0][2] = lda(aad[0], 2); af[1][2] = lda(aad[1], 2);
  PIN();
  s16x4 wlo;
  auto rw = [&](s16x8& d, int h, int j, int hi, int off = 0) __attribute__((always_inline)) {
    const s16x4 v = __builtin_amdgcn_ds_read_tr16_b64_v4i16(LDS_AT(s16x4, wad[j & 1] + h * 12288 + (j >> 1) * 128 + hi * 1536 + off));
    if (hi) d = (s16x8)__builtin_shufflevector(wlo, v, 0, 1, 2, 3, 4, 5, 6, 7);
    else wlo = v;
  };
  auto at = [&](s16x8& x, int r) __attribute__((always_inline)) {
    if (r == 0) asm volatile("" :: "{v[104:107]}"(x));
    if (r == 1) asm volatile("" :: "{v[108:111]}"(x));
    if (r == 2) asm volatile("" :: "{v[112:115]}"(x));
    if (r == 3) asm volatile("" :: "{v[116:119]}"(x));
    if (r == 4) asm volatile("" :: "{v[120:123]}"(x));
    if (r == 5) asm volatile("" :: "{v[124:127]}"(x));
    if (r == 6) asm volatile("" :: "{v[128:131]}"(x));
    if (r == 7) asm volatile("" :: "{v[132:135]}"(x));
    if (r == 8) asm volatile("" :: "{v[136:139]}"(x));
    if (r == 9) asm volatile("" :: "{v[140:143]}"(x));
    if (r == 10) asm volatile("" :: "{v[144:147]}"(x));
    if (r == 11) asm volatile("" :: "{v[148:151]}"(x));
    if (r == 12) asm volatile("" :: "{v[152:155]}"(x));
    if (r == 13) asm volatile("" :: "{v[156:159]}"(x));
    if (r == 14) asm volatile("" :: "{v[160:163]}"(x));
    if (r == 15) asm volatile("" :: "{v[164:167]}"(x));
  };
  auto mm = [&](int i, int j, int h, int sl, int hw_) __attribute__((always_inline)) {
    s16x8 &a = af[h][i], &w = wq[sl][hw_];
    at(a, h * 4 + i); at(w, 8 + sl * 2 + hw_);
    if (j >= 4) asm volatile("v_mfma_f32_16x16x32_bf16 %0, %1, %2, %0" : "+v"(acc[i][j]) : "v"(a), "v"(w));
    else asm volatile("v_mfma_f32_16x16x32_bf16 %0, %1, %2, %0" : "+v"(acc[i][j]) : "v"(w), "v"(a));
    PIN();
  };
  auto next = [&](int g, int J0, int s0, int s1, int off) __attribute__((always_inline)) {
    if (g == 2 || g == 3) rw(wq[s0][0], 0, J0, g & 1, off);
    if (g == 4 || g == 5) af[g & 1][0] = lda(aad[g & 1] + off, 0);
    if (g == 6 || g == 7) rw(wq[s0][1], 1, J0, g & 1, off);
    if (g >= 8 && g < 12) rw(wq[s1][(g - 8) >> 1], (g - 8) >> 1, J0 + 1, g & 1, off);
    if (g >= 12) af[g & 1][(g - 10) >> 1] = lda(aad[g & 1] + off, (g - 10) >> 1);
  };
  auto third = [&](int p, int J0, int delta) __attribute__((always_inline)) {
    auto mv = [&](unsigned& x) __attribute__((always_inline)) { asm volatile("v_add_u32 %0, %1, %0" : "+v"(x) : "s"(delta)); };
#pragma unroll
    for (int g = 0; g < 16; ++g) {
      const int h = g & 1, i = p == 1 ? (g >> 1) & 3 : g >> 2, t = p == 1 ? g >> 3 : (g >> 1) & 1, j = 2 * p + t, sl = p == 1 ? 2 - 2 * t : p + t;
      mm(i, j, h, sl, h);
      if (p == 0) {
        if (g < 2) af[g][3] = lda(aad[g], 3);
        else if (g < 6) rw(wq[2][(g - 2) >> 1], (g - 2) >> 1, 2, g & 1);
        else if (g < 10) rw(wq[3][(g - 6) >> 1], (g - 6) >> 1, 5, g & 1);
        else if (g < 12) mv(aad[g - 10]);
      }
      if (p == 1) {
        if (g < 4) rw(wq[0][g >> 1], g >> 1, 3, g & 1);
        if (g == 4) mv(wad[1]);
        if (g >= 8 && g < 12) rw(wq[2][(g - 8) >> 1], (g - 8) >> 1, 4, g & 1);
        if (g == 12) mv(wad[0]);
      }
      if (p == 2) {
        if (g == BARG) {
          asm volatile("" ::: "memory");
          if (!QW) __builtin_amdgcn_s_waitcnt(0xc07f);
          PIN();
          __builtin_amdgcn_s_barrier();
          asm volatile("" ::: "memory");
        }
        next(g, J0, 0, 1, 0);
      }
      PIN();
      if (QW) {
        if ((p == 2 && g == 15) || (p == 0 && g == 7)) __builtin_amdgcn_s_waitcnt(0xc67f);
        if (p == 0 && g == 2) __builtin_amdgcn_s_waitcnt(0xc57f);
        if ((p == 0 && g == 14) || (p == 1 && (g & 7) == 7)) __builtin_amdgcn_s_waitcnt(0xc07f);
        PIN();
      }
    }
  };
  third(0, 0, STB); third(1, 0, STB);
  {
    static_assert((NKT - NST - 1) % NST == 0, "");
    if (QU) {
#pragma unroll 1
      for (int kt = 0; kt < NKT - NST - 1; kt += NST) {
#pragma unroll
        for (int u = 0; u < NST; ++u) {
          const int delta = u == 1 ? -(NST - 1) * STB : STB;
          third(2, 0, 0); third(0, 0, delta); third(1, 0, delta);
        }
      }
    } else {
      unsigned long long seq = 0x492492ull;
#pragma unroll 1
      for (int kt = NKT - NST - 1; kt > 0; --kt) {
        int delta = (seq & 1ull) ? -(NST - 1) * STB : STB;
        seq >>= 1;
        third(2, 0, 0); third(0, 0, delta); third(1, 0, delta);
      }
    }
  }
  static_assert(NKT % NST == 1, "slots of the last stages");
#define TT(i)
  auto bar = [&]() { PIN(); __builtin_amdgcn_s_barrier(); asm volatile("" ::: "memory"); PIN(); };
  TT(0);
  third(2, 2, 0);
  const __amdgpu_buffer_rsrc_t rsk = __builtin_amdgcn_make_buffer_rsrc(kvb, 0, 0x7fffffff, 0x00020000), rsv = __builtin_amdgcn_make_buffer_rsrc(kvb + VOFF, 0, 0x7fffffff, 0x00020000);
  unsigned pk;
  u32x2 t0, t1, t2, t3;
  auto park = [&](int i) { return *LDS_AT(const volatile u32x2, pk + (i >> 1) * 1024 + (i & 1) * 8); };
  float al[4];
  f32x4 av[4];
  u32x4 kvp[12];
  auto cacc = [&](int p, int x, int t) -> f32x4& { return p < 2 ? acc[x][2 * p + t] : acc[(x >> 1) * 2 + t][4 + (x & 1)]; };
  auto signal = [&](unsigned v) { __hip_atomic_fetch_add(LDS_AT(unsigned, t3[0]), v, __ATOMIC_RELAXED, __HIP_MEMORY_SCOPE_WORKGROUP); PIN(); };
  auto outop = [&](int p, int k) {
    if (k >= 0 && k < 16) {
      const int x = k >> 2, t = (k >> 1) & 1;
      f32x4& c = cacc(p, x, t);
      if (PKM) {
        const int o = 2 * (k & 1);
        f32x2 r = {c[o], c[o + 1]};
        if (p == 2) asm volatile("v_pk_mul_f32 %0, %0, %1" : "+v"(r) : "v"(f32x2{av[(x >> 1) * 2 + t][o], av[(x >> 1) * 2 + t][o + 1]}));
        else if (x & 1) asm volatile("v_pk_mul_f32 %0, %0, %1 op_sel:[0,1] op_sel_hi:[1,1]" : "+v"(r) : "v"(f32x2{al[x & 2], al[x | 1]}));
        else asm volatile("v_pk_mul_f32 %0, %0, %1 op_sel_hi:[1,0]" : "+v"(r) : "v"(f32x2{al[x & 2], al[x | 1]}));
        c[o] = r[0]; c[o + 1] = r[1];
      } else
#pragma unroll
      for (int o = 2 * (k & 1); o < 2 * (k & 1) + 2; ++o) {
        float r;
        asm volatile("v_mul_f32 %0, %1, %2" : "=v"(r) : "v"(c[o]), "v"(p < 2 ? al[x] : av[(x >> 1) * 2 + t][o]));
        c[o] = r;
      }
      PIN();
    }
    if (k >= 1 && k <= 16) {
      const int x = (k - 1) >> 2, t = ((k - 1) >> 1) & 1, o = (k - 1) & 1;
      kvp[p * 4 + x][2 * t + o] = pack2(cacc(p, x, t)[2 * o], cacc(p, x, t)[2 * o + 1]);
      PIN();
      if (t && o) {
        if (p) __builtin_amdgcn_raw_buffer_store_b128(kvp[p * 4 + x], p == 1 ? rsk : rsv, (p == 1 ? t1[1] : t2[1]) + (x >> 1) * 2048 + (x & 1) * (p == 1 ? 64 : 128), 0, 16);
        else *LDS_AT(u32x4, t2[1] + (x >> 1) * 2048 + (x & 1) * 128) = kvp[x];
        PIN();
      }
    }
  };
  static_for<0, 168>([&](auto nc) __attribute__((always_inline)) {
    constexpr int n = decltype(nc)::value, sec = n < 80 ? n >> 4 : (n < 104 ? 5 : 6 + ((n - 104) >> 4)), g = n < 104 ? n - 16 * sec : (n - 104) & 15;
    auto taba = [&](int i) { af[0][i] = *LDS_AT(const s16x8, t0[0] + (i >> 1) * 512 + (i & 1) * 128); };
    auto tabw = [&](int j) { wq[(2 + (j >> 1)) & 3][j & 1] = *LDS_AT(const s16x8, t0[1] + ((j >> 1) * 64 + (j & 1) * 8) * 16); };
    auto rwn = [&](int e, int sl, int j, int off) { if (e >= 0 && e < 4) rw(wq[sl][e >> 1], e >> 1, j, e & 1, off); };
    if (g == 0) TT(1 + sec);
    if (sec == 5) {
      const int i = (g / 12) * 2 + (g & 1), j = (g % 12) >> 1;
      mm(i, j, 0, (2 + (j >> 1)) & 3, j & 1);
    } else {
      constexpr int part[10] = {1, 2, 1, 2, 0, 0, 1, 2, 0, 0}, s0[10] = {0, 2, 0, 2, 0, 0, 1, 3, 1, 3};
      const int h = g & 1, i = sec == 7 ? (g >> 3) * 2 + ((g >> 1) & 1) : g >> 2, t = sec == 7 ? (g >> 2) & 1 : (g >> 1) & 1;
      mm(i, 2 * part[sec] + t, h, (s0[sec] + t) & 3, h);
    }
    if (sec == 0 || sec == 2) {
      if (g < 2) af[g][3] = lda(aad[g] + sec / 2 * STB, 3);
      rwn(g - 2, 2, 4, sec / 2 * STB); rwn(g - 6, 3, 5, sec / 2 * STB);
    }
    if (sec == 1) {
      if (g == 1) bar();
      next(g, 2, 0, 1, STB);
    }
    if ((sec == 3 && g == 12) || (sec == 6 && g == 13)) { pk = lto + QOFF + fresh_lane() * 16; asm volatile("" : "+v"(pk)); }
    if (sec == 3) {
      rwn(g, 0, 0, STB); rwn(g - 4, 1, 1, STB);
      if (g >= 8 && g < 12) asm volatile("v_subrev_u32 %0, %1, %0" : "+v"(g < 10 ? aad[g & 1] : wad[g & 1]) : "s"(STB));
      if (g == 13) t1 = park(1);
      if (g == 14) t0 = park(0);
    }
    if (sec == 4) {
      if (g == B15) bar();
      if (g == 4) taba(0);
      if (g == 5 || g == 6) tabw(g - 5);
      if (g == 7) taba(1);
      if (g == 8 || g == 9) tabw(g - 6);
      if (g == 10 || g == 11) af[1][g - 10] = lda(aad[1], g - 10);
      if (g == 12) taba(2);
      if (g == 15) taba(3);
    }
    if (sec == 5) {
      if (g < 2) tabw(4 + g);
      rwn(g - 2, 1, 2, 0);
      if (g == 12 || g == 13) af[0][g - 12] = lda(aad[0], g - 12);
      if (g == 22) { al[0] = *LDS_AT(const float, t1[0]); al[1] = *LDS_AT(const float, t1[0] + 32); }
      rwn(g - 16, 2, 3, 0);
      if (g == 20 || g == 21) af[1][g - 18] = lda(aad[1], g - 18);
    }
    if (sec == 6) {
      if (g < 2) af[0][2 + g] = lda(aad[0], 2 + g);
      rwn(g - 2, 3, 4, 0);
      if (g == 9) { al[2] = *LDS_AT(const float, t1[0] + 128); al[3] = *LDS_AT(const float, t1[0] + 160); }
      if (g == 14) t2 = park(2);
      if ((g & 3) >= 2) rwn((g - 10) / 4 * 2 + (g & 1) - (g < 10) * 8, 0, 5, 0);
    }
    if (sec == 7) {
      if (g == 2 || g == 3 || g == 10 || g == 11) av[(g >> 3) * 2 + (g & 1)] = *LDS_AT(const f32x4, t2[0] + ((g >> 3) * 32 + (g & 1) * 8) * 4);
      rwn(g < 2 ? g : (g < 4 ? -1 : g - 2), 1, 0, 0); rwn(g - 6, 2, 1, 0);
      if (g == 12) t3 = park(3);
      if (g == 13) { const u32x2 a = park(4); aad[0] = a[0]; aad[1] = a[1]; }
      if (g == 15) {
        asm volatile("" ::: "memory");
        __builtin_amdgcn_s_waitcnt(0xc07f);
        bar();
      }
    }
    if (sec == 8) next(g, 0, 3, 0, STB);
    if (sec == 9) {
      if (g < 2) af[g][3] = lda(aad[g] + STB, 3);
      if (g == 2) signal(0x10000u);
    }
    PIN();
    outop(1, n - 104 - KO);
    outop(2, n - 120 - KO);
    {
      constexpr int wg[8] = WGAPS;
#pragma unroll
      for (int e = 0; e < 8; ++e)
        if (n - 136 == wg[e]) {
          *LDS_AT(u32x4, (e < 4 ? t1[1] + (e & 1) * 64 : t2[1] + 2048 + (e & 1) * 128) + ((e >> 1) & 1) * ASB) = kvp[4 + e];
          PIN();
        }
    }
    if (n == 152 + 3 || n == 152 + 4) {
      al[2 * (n & 1)] *= QSCALE; al[2 * (n & 1) + 1] *= QSCALE;
      asm volatile("" : "+v"(al[2 * (n & 1)]), "+v"(al[2 * (n & 1) + 1]));
      if (n & 1) asm volatile("v_add_u32 %0, %1, %0" : "+v"(t2[1]) : "s"(QOFF));
      PIN();
    }
    outop(0, n - 152 - KO);
  });
  TT(13);
  if (wid == 0) STAMP(dk, 18);
#pragma unroll
  for (int k = 16 - KO; k <= 16; ++k) outop(0, k);
  __hip_atomic_fetch_add(LDS_AT(unsigned, t3[1]), 1u, __ATOMIC_RELAXED, __HIP_MEMORY_SCOPE_WORKGROUP);
  TT(14);
  if (KVW == 2) { asm volatile("s_waitcnt vmcnt(0)" ::: "memory"); signal(1u); }
  TT(15);
  if (wid == 0) STAMP(dk, 19);
  }
  PIN();
  {
  const int lane = fresh_lane(), tid = wid * 64 + lane;
  LAYER_HERE;
  const int dk = LAY2 ? 6 : 2;
  const int g = wid >> 2, hh = (wid >> 1) & 1, qg = wid & 1;
  const int head = tni * 2 + hh, qt = tmi * 4 + qg * 2;
  unsigned qaa = lbase + QOFF + (hh * 2 + qg) * 4096 + lane * 16 + (g ? 0 : 2048), qab = qaa ^ 2048;
  const unsigned wbase = lbase + (hh * 2 + g) * 4096 + lane * 16;
  auto sptr = [&](int sl) { unsigned a = wbase + sl * ASB; asm volatile("" : "+v"(a)); return LDS_AT(const s16x8, a); };
  auto rd = [&](__attribute__((address_space(3))) const s16x8* sp, int off) { return sp[off >> 4]; };
  auto sl0 = sptr(0);
  const unsigned arr_a = lane == 0 ? lbase + FLG + 16 : lbase + DUM + lane * 4;
  auto arrive = [&]() { __hip_atomic_fetch_add(LDS_AT(unsigned, arr_a), 1u, __ATOMIC_RELAXED, __HIP_MEMORY_SCOPE_WORKGROUP); };
  unsigned arr_w = lane == 0 ? lbase + FLG + 8 + g * 4 : lbase + DUM + lane * 4;
  unsigned arr_i = 1u << (8 * (wid & 3));
  auto arrive_w = [&]() { __hip_atomic_fetch_add(LDS_AT(unsigned, arr_w), arr_i, __ATOMIC_RELAXED, __HIP_MEMORY_SCOPE_WORKGROUP); };
  s16x8 sel;
#pragma unroll
  for (int e = 0; e < 8; ++e) sel[e] = ((lane & 7) == (e & 4)) ? (short)0x3F80 : (short)0;
  int sidx = 0xDDDDDDDD;
  f32x16 oa, ob, nn, sa, sb;
  f32x4 la = {0.f, 0.f, 0.f, 0.f}, lb = {0.f, 0.f, 0.f, 0.f};
#pragma unroll
  for (int e = 0; e < 16; ++e) { oa[e] = 0.f; ob[e] = 0.f; sa[e] = 0.f; sb[e] = 0.f; }
  asm volatile("" : "+v"(sidx), "+v"(sel), "+v"(oa), "+v"(ob), "+v"(la), "+v"(lb), "+v"(qaa), "+v"(qab), "+v"(arr_w), "+v"(arr_i));
  PIN();
  s16x8 qa0, qa1, qb0, qb1, k00, k01, v00, v01, v10, v11, k10, k11;
  u32x4 fw;
  auto first = [&]() {
    fw = lds_rd4(lbase + FLG + 16);
    asm volatile("" ::: "memory");
    k00 = rd(sl0, 0); qa0 = *LDS_AT(const s16x8, qaa); qb0 = *LDS_AT(const s16x8, qab);
    k01 = rd(sl0, 1024); qa1 = *LDS_AT(const s16x8, qaa + 1024); qb1 = *LDS_AT(const s16x8, qab + 1024);
    v00 = rd(sl0, 2048); v01 = rd(sl0, 3072);
    PIN();
  };
  if (KVW == 0 && g == 0) {
    asm volatile("s_waitcnt vmcnt(0)" ::: "memory");
    __hip_atomic_fetch_add(LDS_AT(unsigned, arr_w + 24), 1u, __ATOMIC_RELAXED, __HIP_MEMORY_SCOPE_WORKGROUP);
  }
  PIN();
  first();
  if (__builtin_amdgcn_readfirstlane(fw[0]) < 4u) {
#pragma unroll 1
    for (int i = 0; i < SPIN_MAX; ++i) {
      if (__builtin_amdgcn_readfirstlane(*LDS_AT(const volatile unsigned, lbase + FLG + 16)) >= 4u) break;
      __builtin_amdgcn_s_sleep(ASLP);
    }
    asm volatile("" ::: "memory");
    first();
  }
  const unsigned tag = __builtin_amdgcn_readfirstlane(fw[1]);
  const int idx = LAY2 ? (2047 - (int)(__builtin_amdgcn_readfirstlane(fw[3]) & 0xffffu)) & 2047 : -64;
  float ma;
  const float zoff = __uint_as_float(__builtin_amdgcn_readfirstlane(LAY2 && g == ((idx >> 6) & 1) ? 0u : 0xff800000u));
  {
    sa = MFMA32(k00, qa0, sa); sb = MFMA32(k00, qb0, sb);
    sa = MFMA32(k01, qa1, sa); sb = MFMA32(k01, qb1, sb);
    if (KVW == 1 && g == 0) {
      asm volatile("s_waitcnt vmcnt(0)" : "+v"(sa), "+v"(sb) :: "memory");
      __hip_atomic_fetch_add(LDS_AT(unsigned, arr_w + 24), 1u, __ATOMIC_RELAXED, __HIP_MEMORY_SCOPE_WORKGROUP);
    }
    float mb = sb[0];
    ma = sa[0];
#pragma unroll
    for (int e = 1; e < 16; ++e) { ma = fmaxf(ma, sa[e]); mb = fmaxf(mb, sb[e]); }
    ma = fmaxf(ma, mb);
    const auto sw = __builtin_amdgcn_permlane32_swap(__float_as_uint(ma), __float_as_uint(ma), false, false);
    ma = fmaxf(__uint_as_float(sw[0]), __uint_as_float(sw[1]));
    f32x2 n2 = {-ma, -ma};
    asm volatile("" : "+v"(n2));
#pragma unroll
    for (int e = 0; e < 16; ++e) nn[e] = n2[e & 1];
    sa = MFMA32(k00, qa0, nn); sa = MFMA32(k01, qa1, sa);
    la[0] = lb[0] = -__uint_as_float(__float_as_uint(__builtin_amdgcn_exp2f(zoff - ma)) & 0xffff0000u);
  }
#define SB() __builtin_amdgcn_sched_barrier(0)
#define EX(i) sc[i] = __builtin_amdgcn_exp2f(sc[i])
  auto unit = [&](f32x16& sc, f32x16& sn, const s16x8& kn0, const s16x8& kn1, const s16x8& qn0, const s16x8& qn1,
                  f32x16& op, f32x4& lp, const s16x8& vp,
                  f32x16& oc, f32x4& lc, const s16x8& vc, auto&& mid) {
#define UORD 1
    if (UORD == 0) {
    op = SMF32(vp, PHI(sn), op); SB();
    EX(0); EX(1); EX(2); SB();
    lp = SMF16(sel, PHI(sn), lp); SB();
    EX(3); EX(4); SB();
    sn = MFMA32(kn0, qn0, nn); SB();
    EX(5); EX(6); EX(7); SB();
    sn = MFMA32(kn1, qn1, sn); SB();
    mid();
    EX(8); EX(9); EX(10); SB();
    oc = SMF32(vc, PLO(sc), oc); SB();
    EX(11); EX(12); EX(13); SB();
    lc = SMF16(sel, PLO(sc), lc); SB();
    EX(14); EX(15); SB();
    } else {
    lp = SMF16(sel, PHI(sn), lp); SB();
    op = SMF32(vp, PHI(sn), op); SB();
    EX(0); EX(1); EX(2); EX(3); SB();
    sn = MFMA32(kn0, qn0, nn); SB();
    EX(4); EX(5); EX(6); EX(7); SB();
    sn = MFMA32(kn1, qn1, sn); SB();
    mid();
    EX(8); EX(9); EX(10); EX(11); SB();
    lc = SMF16(sel, PLO(sc), lc); SB();
    oc = SMF32(vc, PLO(sc), oc); SB();
    EX(12); EX(13); EX(14); EX(15); SB();
    }
    asm volatile("" : "+v"(sc));
  };
  sl0 = sptr(1);
  unsigned f0 = 0u;
  auto check = [&](unsigned need) {
    if (__builtin_expect(__builtin_amdgcn_readfirstlane(f0) < need, 0)) {
#pragma unroll 1
      for (int i = 0; i < SPIN_MAX; ++i) {
        if (__builtin_amdgcn_readfirstlane(*LDS_AT(const volatile unsigned, lbase + FLG)) >= need) break;
        __builtin_amdgcn_s_sleep(1);
      }
    }
    asm volatile("" ::: "memory");
  };
#define PAT 0x22221110
#define SAW8(i) do { if (((PAT >> (28 - 4 * (i))) & 3) != ((PAT >> ((32 - 4 * (i)) & 31)) & 3)) { __builtin_amdgcn_s_setprio((PAT >> (28 - 4 * (i))) & 3); SB(); } } while (0)
#define SAW(i) do { if (odd) SAW8(4 + (i)); else SAW8(i); } while (0)
  auto three = [&](int st, bool odd) {
    SAW(0);
    unit(sa, sb, k00, k01, qb0, qb1, ob, lb, v11, oa, la, v00, [&]() { k10 = rd(sl0, 0); k11 = rd(sl0, 1024); SB(); });
    SAW(1);
    unit(sb, sa, k10, k11, qa0, qa1, oa, la, v01, ob, lb, v00, [&]() { v10 = rd(sl0, 2048); v11 = rd(sl0, 3072); SB(); });
    sl0 = sptr((2 * st + 2) & 7);
    SB();
    SAW(2);
    unit(sa, sb, k10, k11, qb0, qb1, ob, lb, v01, oa, la, v10, [&]() { k00 = rd(sl0, 0); k01 = rd(sl0, 1024); SB(); });
  };
  auto fourth = [&](int st, bool odd) {
    SAW(3);
    unit(sb, sa, k00, k01, qa0, qa1, oa, la, v11, ob, lb, v10, [&]() {
      v00 = rd(sl0, 2048); v01 = rd(sl0, 3072);
      f0 = *LDS_AT(const volatile unsigned, lbase + FLG);
      SB();
      arrive_w();
      SB(); });
    check(st >= 14 ? 32u : 2u * st + 5u);
    sl0 = sptr((2 * st + 3) & 7);
    SB();
  };
  SB();
  SAW8(0);
  {
    f32x16& sc = sa;
    sb = MFMA32(k00, qb0, nn); SB();
    k10 = rd(sl0, 0); k11 = rd(sl0, 1024); SB();
    sb = MFMA32(k01, qb1, sb); SB();
    EX(0); EX(1); EX(2); EX(3); EX(4); EX(5); EX(6); EX(7); EX(8); SB();
    oa = SMF32(v00, PLO(sa), oa); SB();
    EX(9); EX(10); EX(11); SB();
    la = SMF16(sel, PLO(sa), la); SB();
    EX(12); EX(13); EX(14); EX(15); SB();
    asm volatile("" : "+v"(sa));
  }
  SAW8(1);
  unit(sb, sa, k10, k11, qa0, qa1, oa, la, v01, ob, lb, v00, [&]() { v10 = rd(sl0, 2048); v11 = rd(sl0, 3072); SB(); });
  sl0 = sptr(2);
  SB();
  SAW8(2);
  unit(sa, sb, k10, k11, qb0, qb1, ob, lb, v01, oa, la, v10, [&]() {
    f0 = *LDS_AT(const volatile unsigned, lbase + FLG);
    asm volatile("" ::: "memory");
    k00 = rd(sl0, 0); k01 = rd(sl0, 1024); SB(); });
  if (__builtin_amdgcn_readfirstlane(f0) < 3u) { check(3u); k00 = rd(sl0, 0); k01 = rd(sl0, 1024); }
  SB();
  SAW8(3);
  unit(sb, sa, k00, k01, qa0, qa1, oa, la, v11, ob, lb, v10, [&]() {
    v00 = rd(sl0, 2048); v01 = rd(sl0, 3072); f0 = *LDS_AT(const volatile unsigned, lbase + FLG); SB(); arrive_w(); SB(); });
  check(5u);
  sl0 = sptr(3);
  SB();
  three(1, true);
#pragma unroll 1
  for (int st = 2; st < 16; st += 2) { fourth(st - 1, true); three(st, false); fourth(st, false); three(st + 1, true); }
  SAW8(7);
  unsigned vr, tk, tv;
  u32x4 f4;
  auto table = [&]() {
    f4 = lds_rd4(lbase + FLG);
    k00 = *LDS_AT(const volatile s16x8, tk); k01 = *LDS_AT(const volatile s16x8, tk + 32);
    vr = *LDS_AT(const volatile u16, tv);
  };
  unsigned xw, xm;
  u32x2 ml;
  u32x4 o2[4];
  auto take = [&]() {
    ml = *LDS_AT(const volatile u32x2, xm);
#pragma unroll
    for (int e = 0; e < 4; ++e) o2[e] = lds_rd4(xw + e * 1024);
  };
  float pzt;
  int dso;
  f32x16& sc = sb;
#define EXP() do { asm volatile("" : "+v"(sc)); SB(); } while (0)
  oa = SMF32(v11, PHI(sa), oa); SB();
  tk = lbase + TAB + 128 + hh * 64 + (lane >> 5) * 16; tv = lbase + TAB + 256 + hh * 64 + (lane & 31) * 2;
  table();
  SB();
  EX(0); EX(1); EXP();
  la = SMF16(sel, PHI(sa), la); SB();
  EX(2); EX(3); EX(4); EXP();
  xw = wbase + (qg ? QOFF : 6 * ASB); xm = lbase + XML + wid * 512 + lane * 8;
  pzt = __builtin_amdgcn_exp2f(nn[0] + (zoff - 8.f));
  asm volatile("" : "+v"(xw), "+v"(xm), "+v"(pzt), "+v"(f4));
  SB();
  {
    const unsigned ph = LAY2 ? 0x79797979u : 0x80808080u, pg = qg ? 0u : 0x80u << (16 * hh + 8);
    auto ready = [&]() {
      return (((__builtin_amdgcn_readfirstlane(f4[1]) + ph) & 0x80808080u) == 0x80808080u) & (((__builtin_amdgcn_readfirstlane(g ? f4[3] : f4[2]) + 0x71717171u) & pg) == pg);
    };
    if (__builtin_expect(!ready(), 0)) {
#pragma unroll 1
      for (int i = 0; i < SPIN_MAX; ++i) {
        __builtin_amdgcn_s_sleep(1);
        table();
        if (ready()) break;
      }
    }
    asm volatile("" ::: "memory");
  }
  SB();
  sa = MFMA32(k00, qb0, nn); asm volatile("" : "+v"(sa)); SB();
#pragma unroll
  for (int e = 0; e < 4; ++e) *LDS_AT(volatile f32x4, xw + e * 1024) = f32x4{oa[4 * e], oa[4 * e + 1], oa[4 * e + 2], oa[4 * e + 3]};
  *LDS_AT(volatile u32x2, xm) = u32x2{__float_as_uint(ma), f2word(pzt <= la[0] ? la[0] : 0.f)};
  SB();
  EX(5); EX(6); EX(7); EXP();
  sa = MFMA32(k01, qb1, sa); asm volatile("" : "+v"(sa), "+v"(vr)); SB();
  ARGS_HERE;
  EX(8); EX(9); EX(10); EXP();
  ob = SMF32(v10, PLO(sb), ob); SB();
  EX(11); EX(12); EX(13); EXP();
  lb = SMF16(sel, PLO(sb), lb); SB();
  const bool used = !LAY2 || (qt + g) * 32 + (lane & 31) != idx;
  dso = used ? (((qt + g) * 32 + head) * 1024 + lane * 8) * 2 : S_ * D_ * 2;
  xw += g ? -4096 : 4096; xm += g ? -2048 : 2048;
  asm volatile("" : "+v"(dso), "+v"(xw), "+v"(xm));
  SB();
  EX(14); EX(15); EXP();
  take();
  SB();
  lb = SMF16(sel, PHI(sb), lb); SB();
  ob = SMF32(v11, PHI(sb), ob); SB();
  __builtin_amdgcn_s_setprio(EPRIO);
  TE(0, 0); TE(1, 0); TE(2, 0); TE(3, 0); TE(7, 0);
  float lsb = lb[0];
  if (LAY2) {
    sa[0] = __builtin_amdgcn_exp2f(sa[0]);
    ob = SMF32((u32x4{lane < 32 ? vr : 0u, 0u, 0u, 0u}), PLO(sa), ob);
    lsb += __uint_as_float(__float_as_uint(sa[0]) & 0xffff0000u);
    lsb = pzt <= lsb ? lsb : 0.f;
  }
  const __amdgpu_buffer_rsrc_t rso = __builtin_amdgcn_make_buffer_rsrc(Of, 0, S_ * D_ * 2, 0x00020000);
  TE(0, 1);
  if (__builtin_amdgcn_ballot_w64(ml[1] == SENT) != 0ull) {
#pragma unroll 1
    for (int i = 0; i < SPIN_MAX; ++i) {
      __builtin_amdgcn_s_sleep(XSLP);
      if (!__builtin_amdgcn_ballot_w64(*LDS_AT(const volatile unsigned, xm + 4) == SENT)) break;
    }
    asm volatile("s_waitcnt lgkmcnt(0)" ::: "memory");
    take();
  }
  arrive();
  TE(0, 2);
  TE(0, 3); TE(1, 1);
  {
    const float m2 = __uint_as_float(ml[0]), l2 = __uint_as_float(ml[1]);
    const float mx = fmaxf(ma, m2);
    const float f1 = __builtin_amdgcn_exp2f(ma - mx), f2 = __builtin_amdgcn_exp2f(m2 - mx);
    const float inv = frcp(lsb * f1 + l2 * f2);
    const float c1 = f1 * inv, c2 = f2 * inv;
    f32x2 r[8];
#pragma unroll
    for (int e = 0; e < 8; ++e) {
      const unsigned x0 = o2[e >> 1][(e & 1) * 2], x1 = o2[e >> 1][(e & 1) * 2 + 1];
      r[e] = f32x2{__uint_as_float(x0) * c2, __uint_as_float(x1) * c2};
    }
#pragma unroll
    for (int e = 0; e < 8; ++e) r[e] = f32x2{ob[2 * e], ob[2 * e + 1]} * f32x2{c1, c1} + r[e];
    auto put = [&]() {
#pragma unroll
      for (int sx = 0; sx < 2; ++sx)
        __builtin_amdgcn_raw_buffer_store_b128(u32x4{pack2(r[4 * sx][0], r[4 * sx][1]), pack2(r[4 * sx + 1][0], r[4 * sx + 1][1]),
            pack2(r[4 * sx + 2][0], r[4 * sx + 2][1]), pack2(r[4 * sx + 3][0], r[4 * sx + 3][1])}, rso, dso + sx * 1024, 0, 16);
    };
    put();
    PIN();
    __builtin_amdgcn_s_setprio(0);
    PIN();
    f32x2 chk = {lsb - __builtin_amdgcn_fmed3f(lsb, 8.6736174e-19f, 1.2676506e30f), l2 - __builtin_amdgcn_fmed3f(l2, 8.6736174e-19f, 1.2676506e30f)};
#pragma unroll
    for (int e = 0; e < 8; ++e) chk = r[e] * f32x2{0.f, 0.f} + chk;
    if (__builtin_expect(__builtin_amdgcn_ballot_w64(used && !(chk[0] + chk[1] == 0.f)) != 0ull, 0)) {
      float l;
      attn_safe(KV, head * 64, qb0, qb1, ob, l, LAY2 ? head * 64 + (idx >> 5) : -1, idx & 31, lbase + TAB + 128 + hh * 64);
      const float inv2 = 1.0f / l;
#pragma unroll
      for (int e = 0; e < 8; ++e) r[e] = f32x2{ob[2 * e] * inv2, ob[2 * e + 1] * inv2};
      asm volatile("s_waitcnt vmcnt(0)" ::: "memory");
      put();
    }
    unsigned pp = 0u;
    if (LAY2) {
      const unsigned ua = lbase + UGL + hh * 128 + (lane >> 5) * 16;
      f32x2 du = {0.f, 0.f}, dn = {0.f, 0.f};
#pragma unroll
      for (int q = 0; q < 4; ++q) {
        const f32x4 uu = *LDS_AT(const f32x4, ua + q * 32), gg = *LDS_AT(const f32x4, ua + 256 + q * 32);
        du = r[2 * q] * f32x2{uu[0], uu[1]} + du; du = r[2 * q + 1] * f32x2{uu[2], uu[3]} + du;
        const f32x2 t0 = r[2 * q] * f32x2{gg[0], gg[1]}, t1 = r[2 * q + 1] * f32x2{gg[2], gg[3]};
        dn = t0 * t0 + dn; dn = t1 * t1 + dn;
      }
      const auto a = __builtin_amdgcn_permlane32_swap(__float_as_uint(du[0] + du[1]), __float_as_uint(du[0] + du[1]), false, false);
      const auto c = __builtin_amdgcn_permlane32_swap(__float_as_uint(dn[0] + dn[1]), __float_as_uint(dn[0] + dn[1]), false, false);
      pp = f2word(__uint_as_float(pack2(__uint_as_float(a[0]) + __uint_as_float(a[1]), __uint_as_float(c[0]) + __uint_as_float(c[1]))));
    }
    __builtin_amdgcn_raw_buffer_store_b32(pp, __builtin_amdgcn_make_buffer_rsrc(hand + 2 * HAND_N + PP_O, 0, LAY2 ? H_ * S_ * 4 : 0, 0x00020000),
                                          lane < 32 ? (head * S_ + (qt + g) * 32 + lane) * 4 : H_ * S_ * 4, 0, 16);
    PIN();
  }
  TE(1, 2);
  u32x4 xres[2];
  oproj_tail(LAY2, tid, lbase, lbase + FLG, omi, oni, idx, gran + GR_O + ((LAY2 ? 16 : 0) + tmi) * (64 * RO) + tni,
             gran + GR_X + omi * (128 * RX) + oni * 4, tag, Of, xout, A, w_r, stats_out, stats, hand, xres TEPASS);
  if (LAY2) {
    const int row = omi * 128 + (wid & 3) * 32 + (wid >> 2) * 16 + (lane >> 3), col = oni * 64 + (lane & 7) * 8;
    unsigned md[2], best = 0u;
    const __amdgpu_buffer_rsrc_t rsk = __builtin_amdgcn_make_buffer_rsrc(hand + HAND_N, 0, (HAND_N + PS_N) * 4, 0x00020000);
    f32x2 xf[2][4];
#pragma unroll
    for (int p = 0; p < 2; ++p)
#pragma unroll
      for (int e = 0; e < 4; ++e) {
        xf[p][e] = f32x2{__uint_as_float(xres[p][e] << 16), __uint_as_float(xres[p][e] & 0xffff0000u)};
        asm volatile("" : "+v"(xf[p][e]));
      }
    LOGDEF;
    LOG();
    const bool rtr = (oni & 7) == 7;
    {
      const int rl = (wid & 3) * 32 + (wid >> 2) * 16 + (lane & 15), rr = omi * 128 + rl;
      int src = (HAND_N + ((omi * 16 + (lane >> 4) * 4) * 128 + rl) * 4) * 4;
      int ko = rtr && (wid >> 2) == (oni >> 3) && lane < NREP ? lane * 1024 + (rr >> 4) * 8 : (HAND_N + PS_N) * 4;
      int bp = (lane >> 3) * 4;
      const float wsum = *LDS_AT(const volatile float, lbase + SPM + 132);
      asm volatile("" : "+v"(src), "+v"(ko), "+v"(bp));
      u32x4 v[4];
      __builtin_amdgcn_s_sleep(MPRE);
#pragma unroll 1
      for (int spin = 0; spin < SPIN_MAX; ++spin) {
        LOG();
#pragma unroll
        for (int i = 0; i < 4; ++i) v[i] = __builtin_amdgcn_raw_buffer_load_b128(rsk, src + (i & 1) * 2048, (i >> 1) * 4096, 16);
        unsigned mx = 0u;
#pragma unroll
        for (int i = 0; i < 4; ++i) mx = umax(umax(umax(mx, v[i][0]), v[i][1]), v[i][2]);
        if (none(mx == SENT)) break;
        __builtin_amdgcn_s_sleep(MSLP);
        asm volatile("" ::: "memory");
      }
      LOGAT(12);
      if (rtr) __builtin_amdgcn_s_setprio(3); else __builtin_amdgcn_s_setprio(LNPRIO);
      double sm[3];
#pragma unroll
      for (int c = 0; c < 3; ++c) {
        const double a = ((double)__uint_as_float(v[0][c]) + (double)__uint_as_float(v[1][c])) + ((double)__uint_as_float(v[2][c]) + (double)__uint_as_float(v[3][c]));
        u32x2 q = __builtin_bit_cast(u32x2, a);
        const auto lo = __builtin_amdgcn_permlane32_swap(q[0], q[0], false, false), hi = __builtin_amdgcn_permlane32_swap(q[1], q[1], false, false);
        q = __builtin_bit_cast(u32x2, __builtin_bit_cast(double, u32x2{lo[0], hi[0]}) + __builtin_bit_cast(double, u32x2{lo[1], hi[1]}));
        const auto l2 = __builtin_amdgcn_permlane16_swap(q[0], q[0], false, false), h2 = __builtin_amdgcn_permlane16_swap(q[1], q[1], false, false);
        sm[c] = __builtin_bit_cast(double, u32x2{l2[0], h2[0]}) + __builtin_bit_cast(double, u32x2{l2[1], h2[1]});
      }
      float mean, var, mb, den;
      ln_from_sums(sm[0], sm[1], mean, var, mb, den);
      const unsigned mdr = f2word(__uint_as_float((__float_as_uint(mb) >> 16) | (__float_as_uint(den) & 0xffff0000u)));
      md[0] = __builtin_amdgcn_ds_bpermute(bp, mdr); md[1] = __builtin_amdgcn_ds_bpermute(bp + 32, mdr);
      if (rtr) {
        const float r = (float)fma(-(double)mb, (double)wsum, sm[2]) * frcp(den);
        const unsigned rb = pack2(r, r);
        unsigned rkey = (rb ^ ((unsigned)((int)rb >> 31) | 0x80000000u)) & 0xffff0000u;
        if (r != r) rkey = 0u;
        const unsigned top = wave_umax(rkey | (unsigned)(2047 - rr));
        const unsigned mdt = __builtin_amdgcn_readlane(mdr, (2047 - (int)(top & 0xffffu)) & 15);
        __builtin_amdgcn_raw_buffer_store_b64(u32x2{top, mdt}, rsk, ko, 0, 16);
        PIN();
        __builtin_amdgcn_s_setprio(LNPRIO);
        __hip_atomic_fetch_add(LDS_AT(unsigned, lane == 0 ? lbase + SPM + 128 : lbase + DUM + lane * 4), 1u, __ATOMIC_RELAXED, __HIP_MEMORY_SCOPE_WORKGROUP);
        if (wid == 2 || wid == 5) STAMP(8, 6);
      }
      LOGAT(13);
    }
    auto keys = [&](const u32x4& v) __attribute__((always_inline)) {
      if (none(umax(umax(v[0], v[1]), umax(v[2], v[3])) == SENT)) {
        best = wave_umax(umax(v[0], v[2])) | 0x8000u;
        *LDS_AT(volatile u32x2, lbase + FLG + 40) = u32x2{best, (unsigned)__builtin_amdgcn_readlane(v[0] > v[2] ? v[1] : v[3], ((2047 - (int)(best & 0x7ffu)) >> 5) & 63)};
        STAMP(8, 11);
      }
    };
    auto ln_rows = [&]() __attribute__((always_inline)) {
#pragma unroll
      for (int p = 0; p < 2; ++p) {
        const float mb_ = __uint_as_float(md[p] << 16), rin = frcp(__uint_as_float(md[p] & 0xffff0000u));
#pragma unroll
        for (int e = 0; e < 4; ++e) {
          const f32x2 h = (xf[p][e] - f32x2{mb_, mb_}) * f32x2{rin, rin} + xf[p][e];
          xres[p][e] = pack2(h[0], h[1]);
        }
      }
      asm volatile("" : "+v"(xres[0]), "+v"(xres[1]));
    };
    if (wid == 0) STAMP(8, 12);
    ln_rows();
    LOGAT(14);
    const __amdgpu_buffer_rsrc_t rsu = __builtin_amdgcn_make_buffer_rsrc(out, 0, S_ * D_ * 2, 0x00020000);
    unsigned fr1 = SENT;
#pragma unroll 1
    for (int i = 0; i < 4 * SPIN_MAX && fr1 == SENT; ++i) fr1 = __builtin_amdgcn_readfirstlane(*LDS_AT(const volatile unsigned, lbase + SPM + 144));
    fr1 &= 0x7ffu;
    unsigned tmx = __builtin_amdgcn_readfirstlane(*LDS_AT(const volatile unsigned, lbase + SPM + 136)) + KDT;
    if (tmx - (unsigned)__builtin_amdgcn_s_memrealtime() > 1024u) tmx = 0u;
    auto due = [&]() { return tmx == 0u || (int)((unsigned)__builtin_amdgcn_s_memrealtime() - tmx) >= 0; };
    lk_set(15);
    if (wid == 6) {
      if (rtr) {
#pragma unroll 1
        for (int i = 0; i < SPIN_MAX; ++i) {
          if (__builtin_amdgcn_readfirstlane(*LDS_AT(const volatile unsigned, lbase + SPM + 128)) == 7u) break;
          __builtin_amdgcn_s_sleep(1);
        }
        asm volatile("" ::: "memory");
      }
    }
#pragma unroll 1
    for (int i = 0; i < OSLP && !best; ++i) {
      if (wid == 6 && due()) {
        LOG();
        keys(__builtin_amdgcn_raw_buffer_load_b128(rsk, (bid % NREP) * 1024 + lane * 16, 0, 16));
        i += 2;
      } else {
        if (__builtin_amdgcn_readfirstlane(*LDS_AT(const volatile unsigned, lbase + FLG + 40))) break;
        __builtin_amdgcn_s_sleep(2);
      }
      asm volatile("" ::: "memory");
    }
    u32x4 kv = {SENT, SENT, SENT, SENT};
    if (wid == 6 && !best && due()) kv = __builtin_amdgcn_raw_buffer_load_b128(rsk, (bid % NREP) * 1024 + lane * 16, 0, 16);
    PIN();
#pragma unroll
    for (int p = 0; p < 2; ++p) __builtin_amdgcn_raw_buffer_store_b128(xres[p], rsu, row + 8 * p != (int)fr1 ? ((row + 8 * p) * D_ + col) * 2 : S_ * D_ * 2, 0, OAUX);
    PIN();
    if (wid == 6 && !best) { keys(kv); while (!due()) __builtin_amdgcn_s_sleep(1); }
#pragma unroll 1
    for (int i = 0; i < 4 * SPIN_MAX && !best; ++i) {
      if (wid == 6) {
        LOG();
        keys(__builtin_amdgcn_raw_buffer_load_b128(rsk, (bid % NREP) * 1024 + lane * 16, 0, 16));
        if (!best) __builtin_amdgcn_s_sleep(KSLP);
      } else {
        best = __builtin_amdgcn_readfirstlane(*LDS_AT(const volatile unsigned, lbase + FLG + 40));
        if (!best) __builtin_amdgcn_s_sleep(1);
      }
      asm volatile("" ::: "memory");
    }
    const int idx2 = 2047 - (int)(best & 0x7ffu);
    if (__builtin_expect(idx2 != (int)fr1, 0)) {
      const __amdgpu_buffer_rsrc_t rsf = __builtin_amdgcn_make_buffer_rsrc(gran + GR_F, 0, 48 * 128, 0x00020000);
      if (!none((row | 8) == (idx2 | 8))) {
#pragma unroll
        for (int p = 0; p < 2; ++p)
          __builtin_amdgcn_raw_buffer_store_b128(u32x4{pack2(xf[p][0][0], xf[p][0][1]), pack2(xf[p][1][0], xf[p][1][1]), pack2(xf[p][2][0], xf[p][2][1]), pack2(xf[p][3][0], xf[p][3][1])},
              __builtin_amdgcn_make_buffer_rsrc(xout, 0, 3 * D_ * 2, 0x00020000), row + 8 * p == idx2 ? (2 * D_ + col) * 2 : S_ * D_ * 2, 0, 16);
        asm volatile("s_waitcnt vmcnt(0)" ::: "memory");
        __builtin_amdgcn_raw_buffer_store_b32(tag, rsf, lane == 0 ? oni * 128 : 48 * 128, 0, 16);
      }
      if (!none((row | 8) == ((int)fr1 | 8))) {
#pragma unroll 1
        for (int spin = 0; spin < SPIN_MAX; ++spin) {
          if (none(__builtin_amdgcn_raw_buffer_load_b32(rsf, (16 + oni * 2 + (lane & 1)) * 128, 0, 16) != tag)) break;
          asm volatile("" ::: "memory");
        }
#pragma unroll
        for (int p = 0; p < 2; ++p) __builtin_amdgcn_raw_buffer_store_b128(xres[p], rsu, row + 8 * p == (int)fr1 ? ((row + 8 * p) * D_ + col) * 2 : S_ * D_ * 2, 0, OAUX);
      }
    }
  }
  }
}
struct Args2 { Args l[2]; };
extern "C" __global__ __launch_bounds__(768, 3) void layers_kernel(const Args2 args) {
  __shared__ __attribute__((aligned(128))) char smem[163840];
  const unsigned long long t_entry = 0;
  const int tid = threadIdx.x, bid = blockIdx.x;
  int wid = __builtin_amdgcn_readfirstlane(tid >> 6);
  asm volatile("" : "+s"(wid));
  const ArgP ap = (ArgP)__builtin_amdgcn_kernarg_segment_ptr();
  layer_body(wid, bid, (unsigned)(uintptr_t)smem, ap, t_entry);
  LAYER_HERE;
  if (!LAY2) {
    asm volatile(AGAIN :: "{s[0:1]}"(ap + 1), "{s2}"(bid), "{v0}"(wid * 64) : "s90", "s91", "scc", "memory");
    __builtin_unreachable();
  }
}

extern "C" __global__ void end_marker_kernel() {}

extern "C" void kernel_launch(void* const* d_in, const int* in_sizes, int n_in, void* d_out, int out_size,
                              void* d_ws, size_t ws_size, hipStream_t stream) {
  const u16* x0 = (const u16*)d_in[0];
  const u16* wqkv = (const u16*)d_in[1];
  const u16* wo = (const u16*)d_in[2];
  const u16* w_r = (const u16*)d_in[4];
  const u16* wg = (const u16*)d_in[5];
  const u16* wu = (const u16*)d_in[6];
  const u16* wd = (const u16*)d_in[7];
  u16* out = (u16*)d_out;

  char* ws = (char*)d_ws;
  const size_t MB4 = (size_t)S_ * D_ * 2;
  u16* kv1 = (u16*)(ws);
  u16* kv2 = (u16*)(ws + 2 * MB4);
  u16* o1 = (u16*)(ws + 4 * MB4);
  u16* x1 = (u16*)(ws + 5 * MB4);
  unsigned* hand = (unsigned*)(ws + 7 * MB4);
  u16* x3 = (u16*)(hand + HAND_N + GRAN_N + Y_N);
  double* stats = (double*)(ws + 8 * MB4);
  unsigned* gran = (unsigned*)(ws + 9 * MB4);
  float* csf = (float*)(ws + 9 * MB4 + (1 << 20));
  u16* o2 = (u16*)(ws + 10 * MB4);
  u16* wc = (u16*)((((uintptr_t)ws + 11 * MB4 + 2047) & ~(uintptr_t)2047) + (((uintptr_t)wqkv + 128) & 2047));
  Args2 a;
  for (int l = 0; l < 2; ++l) {
    Args& r = a.l[l];
    r.lay = l; r.a = l ? x1 : x0; r.kv = l ? kv2 : kv1; r.o = l ? o2 : o1; r.xo = l ? x3 : x1; r.so = l ? stats + STAT_N : stats;
    r.W = wqkv; r.wc = wc; r.wg = wg; r.wu = wu; r.wd = wd; r.wo = wo; r.w_r = w_r; r.gran = gran; r.csf = csf; r.stats = stats; r.hand = hand; r.out = out;
  }
  layers_kernel<<<256, 768, 0, stream>>>(a);
}
```

```cpp
#include <hip/hip_runtime.h>
#include <hip/hip_bf16.h>
#include <stdint.h>
#include <type_traits>
#include <utility>

typedef unsigned short u16;
typedef short s16x4 __attribute__((ext_vector_type(4)));
typedef short s16x8 __attribute__((ext_vector_type(8)));
typedef float f32x16 __attribute__((ext_vector_type(16)));
typedef float f32x4 __attribute__((ext_vector_type(4)));
typedef unsigned int u32x4 __attribute__((ext_vector_type(4)));
typedef unsigned int u32x2 __attribute__((ext_vector_type(2)));
typedef __bf16 bf16x8 __attribute__((ext_vector_type(8)));
typedef __bf16 bf16x16 __attribute__((ext_vector_type(16)));

__device__ __forceinline__ int fresh_lane() {
  int l;
  asm volatile("v_mbcnt_lo_u32_b32 %0, -1, 0\n\tv_mbcnt_hi_u32_b32 %0, -1, %0" : "=v"(l));
  return l;
}
#define STAMP(k, i)
#define STAMPV(k, i, v)
#define LOGDEF
#define LOG()
#define LOGAT(k)
#define lk_set(k)
#define LOGOUT(slot)
#define TE(grp, k)
#define TEARG
#define TEPASS
constexpr int S_ = 2048, D_ = 1024, H_ = 32, F_ = 3584;

__device__ __forceinline__ int kpos(int r) { return (r & ~12) | ((r & 4) << 1) | ((r & 8) >> 1); }
__device__ __forceinline__ float bf2f(u16 v) { return __uint_as_float(((unsigned)v) << 16); }
__device__ __forceinline__ u16 f2bf(float f) {
  __hip_bfloat16 b = __float2bfloat16(f);
  return *reinterpret_cast<u16*>(&b);
}
typedef float f32x2 __attribute__((ext_vector_type(2)));
typedef __bf16 bf16x2 __attribute__((ext_vector_type(2)));
__device__ __forceinline__ unsigned pack2(float lo, float hi) {
  f32x2 v = {lo, hi};
  return __builtin_bit_cast(unsigned, __builtin_convertvector(v, bf16x2));
}
__device__ __forceinline__ float rbf(float f) { return __uint_as_float(pack2(f, f) << 16); }
__device__ __forceinline__ void rbf2(float& a, float& b) {
  const unsigned p = pack2(a, b);
  a = __uint_as_float(p << 16); b = __uint_as_float(p & 0xffff0000u);
}

__device__ __forceinline__ float fadd(float a, float b) { float r = a + b; asm("" : "+v"(r)); return r; }
template <int CTRL, int RMASK>
__device__ __forceinline__ float dpp_add(float v) {
  const int t = __builtin_amdgcn_update_dpp(0, __float_as_int(v), CTRL, RMASK, 0xf, false);
  return v + __int_as_float(t);
}
__device__ __forceinline__ float wave_sum(float v) {
  v = dpp_add<0x111, 0xf>(v);
  v = dpp_add<0x112, 0xf>(v);
  v = dpp_add<0x114, 0xf>(v);
  v = dpp_add<0x118, 0xf>(v);
  v = dpp_add<0x142, 0xa>(v);
  v = dpp_add<0x143, 0xc>(v);
  return __int_as_float(__builtin_amdgcn_readlane(__float_as_int(v), 63));
}

template <int CTRL, int RMASK>
__device__ __forceinline__ unsigned dpp_umax(unsigned v) {
  const unsigned t = (unsigned)__builtin_amdgcn_update_dpp(0, (int)v, CTRL, RMASK, 0xf, false);
  return v > t ? v : t;
}
__device__ __forceinline__ unsigned wave_umax(unsigned v) {
  v = dpp_umax<0x111, 0xf>(v); v = dpp_umax<0x112, 0xf>(v); v = dpp_umax<0x114, 0xf>(v); v = dpp_umax<0x118, 0xf>(v);
  v = dpp_umax<0x142, 0xa>(v); v = dpp_umax<0x143, 0xc>(v);
  return (unsigned)__builtin_amdgcn_readlane((int)v, 63);
}

#define NREP 32
constexpr unsigned SENT = 0xFFFFFFFFu;
constexpr int GRAN_N = 256 * NREP;
constexpr int Y_N = F_ / 16 * D_;
constexpr int X2_N = D_ / 2;
constexpr int XS_N = 128;
constexpr int P4_N = 3 * D_ * 16;
constexpr int PA_N = H_ * 8 * 64;
constexpr int HAND_N = GRAN_N + Y_N + X2_N + XS_N + P4_N + PA_N;
constexpr int MD_O = 16 * 16 * 128 * 4;
constexpr int PK_O = MD_O;
constexpr int PP_O = PK_O + 128 * NREP;
constexpr int Y3_O = PP_O + H_ * S_;
constexpr int PS_N = Y3_O + Y_N;
constexpr int GR_ROW = 64, GR_COL = GR_ROW + 4 * S_, GR_KV = GR_COL + 16 * 16 * 192;
#define RO 16
#define RK 16
#define RX 1
constexpr int GR_O = GR_KV + 2 * 16 * 32 * RK, GR_X = GR_O + 2 * 16 * 64 * RO, GR_N = GR_X + 16 * 128 * RX;
constexpr int GR_F = GR_N + 256 * 64;
constexpr int STAT_N = 3 * S_;

__device__ __forceinline__ void st_ag(unsigned* p, unsigned v) { __hip_atomic_store(p, v, __ATOMIC_RELAXED, __HIP_MEMORY_SCOPE_AGENT); }
__device__ __forceinline__ unsigned ld_ag(const unsigned* p) { return __hip_atomic_load(p, __ATOMIC_RELAXED, __HIP_MEMORY_SCOPE_AGENT); }
__device__ __forceinline__ double ld_ag(const double* p) { return __hip_atomic_load(p, __ATOMIC_RELAXED, __HIP_MEMORY_SCOPE_AGENT); }
__device__ __forceinline__ unsigned f2word(float f) { const unsigned u = __float_as_uint(f); return u < 0xFFFFFFFEu ? u : 0xFFFFFFFEu; }
constexpr int SPIN_MAX = 1 << 18;
__device__ __forceinline__ unsigned umax(unsigned a, unsigned b) { return a > b ? a : b; }
__device__ __forceinline__ bool none(bool p) { return __builtin_amdgcn_ballot_w64(p) == 0ull; }

__device__ __forceinline__ void ln_from_sums(double s1, double s2, float& mean, float& var, float& mb, float& den) {
  const double md = s1 * (1.0 / 1024.0);
  const double vd = fma(-md, md, s2 * (1.0 / 1024.0));
  mean = (float)md; var = fmaxf((float)vd, 0.f);
  mb = rbf(mean);
  den = rbf(__builtin_amdgcn_sqrtf(rbf(rbf(var) + 1e-5f)));
}
__device__ __forceinline__ float frcp(float x) { return __builtin_amdgcn_rcpf(x); }
__device__ __forceinline__ void ln_apply2(float& a, float& b, float mb, float den, float rin) {
  a -= mb; b -= mb;
  rbf2(a, b);
  float qa = a * rin, qb = b * rin;
  qa = fmaf(fmaf(-qa, den, a), rin, qa);
  qb = fmaf(fmaf(-qb, den, b), rin, qb);
  rbf2(qa, qb);
  a = qa; b = qb;
}

#define CODE_ADDR(SYM, out64)                                                                                          \
  do {                                                                                                                 \
    unsigned lo_, hi_;                                                                                                 \
    asm volatile("s_getpc_b64 s[90:91]\n\ts_add_u32 s90, s90, " SYM "@rel32@lo+4\n\ts_addc_u32 s91, s91, " SYM          \
                 "@rel32@hi+12\n\ts_mov_b32 %0, s90\n\ts_mov_b32 %1, s91"                                             \
                 : "=s"(lo_), "=s"(hi_) :: "s90", "s91", "scc");                                                        \
    out64 = ((unsigned long long)hi_ << 32) | lo_;                                                                     \
  } while (0)

#define QNX 4
constexpr int QMX = 32 / QNX, QNG = 16 / QNX, QMG = 8 / QNG;

__device__ __forceinline__ unsigned touch(const void* p) { return *reinterpret_cast<const unsigned*>(p); }

__device__ __forceinline__ unsigned touch_code(unsigned long long lo, unsigned long long hi, int pc, int lane) {
  const unsigned long long p = lo + (unsigned)(pc * 8192 + lane * 128);
  return p + 4 <= hi ? *reinterpret_cast<__attribute__((address_space(1))) const unsigned*>(p) : 0u;
}

template <bool SC = false>
__device__ __forceinline__ void glds16s(const void* sbase, unsigned voff, unsigned lds_dst) {
  unsigned keep;
  if (SC)
    asm volatile("s_mov_b32 %0, m0\n\ts_mov_b32 m0, %3\n\ts_nop 2\n\tglobal_load_lds_dwordx4 %1, %2 sc1\n\ts_mov_b32 m0, %0"
                 : "=&s"(keep) : "v"(voff), "s"(sbase), "s"(lds_dst) : "memory");
  else
  asm volatile("s_mov_b32 %0, m0\n\ts_mov_b32 m0, %3\n\ts_nop 2\n\tglobal_load_lds_dwordx4 %1, %2\n\ts_mov_b32 m0, %0"
               : "=&s"(keep) : "v"(voff), "s"(sbase), "s"(lds_dst) : "memory");
}
template <bool SC>
__device__ __forceinline__ void glds16x4(const void* sbase, unsigned v0, unsigned v1, unsigned v2, unsigned v3, unsigned lds_dst) {
  unsigned keep;
  if (SC)
    asm volatile("s_mov_b32 %0, m0\n\ts_mov_b32 m0, %6\n\ts_nop 2\n\tglobal_load_lds_dwordx4 %1, %5 sc1\n\tglobal_load_lds_dwordx4 %2, %5 offset:1024 sc1\n\t"
                 "global_load_lds_dwordx4 %3, %5 offset:2048 sc1\n\tglobal_load_lds_dwordx4 %4, %5 offset:3072 sc1\n\ts_mov_b32 m0, %0"
                 : "=&s"(keep) : "v"(v0), "v"(v1), "v"(v2), "v"(v3), "s"(sbase), "s"(__builtin_amdgcn_readfirstlane(lds_dst)) : "memory");
  else
    asm volatile("s_mov_b32 %0, m0\n\ts_mov_b32 m0, %6\n\ts_nop 2\n\tglobal_load_lds_dwordx4 %1, %5\n\tglobal_load_lds_dwordx4 %2, %5 offset:1024\n\t"
                 "global_load_lds_dwordx4 %3, %5 offset:2048\n\tglobal_load_lds_dwordx4 %4, %5 offset:3072\n\ts_mov_b32 m0, %0"
                 : "=&s"(keep) : "v"(v0), "v"(v1), "v"(v2), "v"(v3), "s"(sbase), "s"(__builtin_amdgcn_readfirstlane(lds_dst)) : "memory");
}
constexpr float QSCALE = 0.17677669529663687f * 1.4426950408889634f;

__device__ __forceinline__ unsigned min4(u32x4 v) { const unsigned a = v[0] < v[1] ? v[0] : v[1], b = v[2] < v[3] ? v[2] : v[3]; return a < b ? a : b; }
#define LDS_AT(T, a) ((__attribute__((address_space(3))) T*)(uintptr_t)(a))
__device__ __forceinline__ u32x4 lds_rd4(unsigned a) { return *LDS_AT(const volatile u32x4, a); }
__device__ __forceinline__ void lds_wr(unsigned a, unsigned v) { *LDS_AT(volatile unsigned, a) = v; }
__device__ __forceinline__ void split3(float v, unsigned& p0, unsigned& p1, unsigned& p2) {
  p0 = pack2(v, v) & 0xffffu; v -= __uint_as_float(p0 << 16);
  p1 = pack2(v, v) & 0xffffu; v -= __uint_as_float(p1 << 16);
  p2 = pack2(v, v) & 0xffffu;
}

#define PIN() __builtin_amdgcn_sched_barrier(0)
template <int B, class F, int... I>
__device__ __forceinline__ void static_for_(F&& f, std::integer_sequence<int, I...>) { (f(std::integral_constant<int, B + I>{}), ...); }
template <int B, int E, class F>
__device__ __forceinline__ void static_for(F&& f) { static_for_<B>(f, std::make_integer_sequence<int, E - B>{}); }

#define TPRE 60
#define TPSL 0
#define FCPRE 44
#define FCSLP 2
#define SIGAT 24
#define XPRIO 1
#define OPD 6
#define LAUX 16
#define CSLP 2
#define EPRIO 3
#define OPRIO 1
#define XSLP 1
#define CSL1 8
#define XAUX 16
__device__ __forceinline__ void oproj_tail(const bool RES2, const int tid, const unsigned lbase, const unsigned flg, const int tmi, const int tni, const int idx,
                                           unsigned* osig, unsigned* xsig, const unsigned tag,
                                           const u16* A, u16* out,
                                           const u16* res, const u16* __restrict__ w_r, double* stats,
                                           const double* stats_in, unsigned* hand, u32x4 (&xres)[2] TEARG) {
  constexpr int ldw = D_, PD = OPD;
  const int lane = tid & 63, wid = __builtin_amdgcn_readfirstlane(tid >> 6);
  const int m0 = tmi * 128, n0 = tni * 64;
  const int dk = RES2 ? 7 : 3;
  if (wid == 0) STAMP(dk, 0);
  TE(1, 3);
  asm volatile("" : "+s"(A) :: "memory");
  PIN();
  const int row = m0 + (wid & 3) * 32 + (wid >> 2) * 16 + (lane >> 3), col = n0 + (lane & 7) * 8;
  const unsigned tb = lbase + 147456;
  typedef __attribute__((address_space(1))) double* gdp;
  typedef __attribute__((address_space(1))) unsigned* gup;
  unsigned tr, oad;
  gdp pst;
  gup pps;
  int xo;
  auto setup = [&]() __attribute__((always_inline)) {
    tr = tb + (row - m0) * 128 + (((lane ^ (lane >> 3)) & 7) << 4);
    pst = (gdp)(stats + ((lane & 3) == 3 ? 0 : (lane & 3)) * S_ + row + 8 * ((lane >> 2) & 1));
    pps = (gup)(hand + 2 * HAND_N + ((tmi * 16 + tni) * 128 + row - m0 + 8 * ((lane >> 2) & 1)) * 4 + (lane & 3));
    xo = (row * ldw + col) * 2;
    asm volatile("" : "+v"(tr), "+v"(pst), "+v"(pps), "+v"(xo));
    PIN();
  };
  {
    oad = lane == 0 ? flg + 24 : flg + 64 + lane * 4;
    gup ps = (gup)(osig + lane * 64);
    asm volatile("" : "+v"(oad), "+v"(ps));
    asm volatile("s_waitcnt vmcnt(1)" ::: "memory");
    TE(2, 1);
    const unsigned old = __hip_atomic_fetch_add(LDS_AT(unsigned, oad), 1u, __ATOMIC_RELAXED, __HIP_MEMORY_SCOPE_WORKGROUP);
    if (__builtin_amdgcn_readfirstlane(old) == 7u && lane < RO) __hip_atomic_store(ps, tag, __ATOMIC_RELAXED, __HIP_MEMORY_SCOPE_AGENT);
    PIN();
    TE(2, 2);
    if (wid == 0) STAMP(dk, 12);
  }
  auto announce = [&]() __attribute__((always_inline)) {
    asm volatile("" ::: "memory");
    PIN();
    __builtin_amdgcn_s_barrier();
    PIN();
    if (wid == 0) STAMP(dk, 1);
    TE(2, 3); TE(4, 0); TE(7, 1);
  };
  u32x4 rwr;
  auto sums = [&](const u32x4& x0, const u32x4& x1) __attribute__((always_inline)) {
    float f[2][3];
#pragma unroll
    for (int p = 0; p < 2; ++p) {
      f[p][0] = f[p][1] = f[p][2] = 0.f;
#pragma unroll
      for (int e = 0; e < 4; ++e) {
        const unsigned xu = p ? x1[e] : x0[e], wu = rwr[e];
        const bf16x2 x = __builtin_bit_cast(bf16x2, xu);
        f[p][0] = __builtin_amdgcn_fdot2_f32_bf16(x, __builtin_bit_cast(bf16x2, 0x3f803f80u), f[p][0], false);
        f[p][1] = __builtin_amdgcn_fdot2_f32_bf16(x, x, f[p][1], false);
        f[p][2] = __builtin_amdgcn_fdot2_f32_bf16(x, __builtin_bit_cast(bf16x2, wu), f[p][2], false);
      }
    }
    auto addx = [&](float keep, float give, auto ctl) {
      return keep + __int_as_float(__builtin_amdgcn_update_dpp(0, __float_as_int(give), decltype(ctl)::value, 0xf, 0xf, false));
    };
    const bool b2 = lane & 4, b1 = lane & 2, z0 = (lane & 3) == 0, z1 = (lane & 3) == 1;
    float g[3];
#pragma unroll
    for (int c = 0; c < 3; ++c) g[c] = addx(b2 ? f[1][c] : f[0][c], b2 ? f[0][c] : f[1][c], std::integral_constant<int, 0x141>{});
    const float u1 = addx(b1 ? g[2] : g[0], b1 ? g[0] : g[2], std::integral_constant<int, 0x4e>{});
    const float u2 = addx(g[1], g[1], std::integral_constant<int, 0x4e>{});
    return addx(z1 ? u2 : u1, z0 ? u2 : u1, std::integral_constant<int, 0xb1>{});
  };
  if (wid < 4) {
    const __amdgpu_buffer_rsrc_t rsa = __builtin_amdgcn_make_buffer_rsrc(const_cast<u16*>(A), 0, S_ * D_ * 2, 0x00020000);
    int sa0 = (tmi * 4 + wid) * 65536, va0 = (lane >> 4) * 512 + (lane & 15) * 16;
    asm volatile("" : "+v"(va0));
    asm volatile("" : "+s"(sa0));
    auto lda = [&](int i, int h) { return __builtin_amdgcn_raw_buffer_load_b128(rsa, va0 + (i & 1) * 2048 + h * 256, sa0 + (i >> 1) * 4096, LAUX); };
    unsigned wad[4], tw[4];
    f32x4 acc[2][4];
    const int k = (lane >> 5) * 16 + ((lane >> 2) & 7);
#pragma unroll
    for (int j = 0; j < 4; ++j) {
      wad[j] = lbase + k * 128 + (((2 * j) ^ (k & 6)) << 4) + 8 * (lane & 3);
      tw[j] = tb + wid * 4096 + (lane & 15) * 128 + (((2 * j + (lane >> 5)) ^ (lane & 7)) << 4) + ((lane >> 4) & 1) * 8;
      asm volatile("" : "+v"(wad[j]), "+v"(tw[j]));
    }
    auto ldw_ = [&](int i, int j) {
      const unsigned a = wad[j] + (i & 3) * 4096;
      s16x4 lo = __builtin_amdgcn_ds_read_tr16_b64_v4i16(LDS_AT(s16x4, a));
      s16x4 h4 = __builtin_amdgcn_ds_read_tr16_b64_v4i16(LDS_AT(s16x4, a + 1024));
      return (s16x8)__builtin_shufflevector(lo, h4, 0, 1, 2, 3, 4, 5, 6, 7);
    };
    u32x4 id[2];
#pragma unroll
    for (int j = 0; j < 2; ++j) {
#pragma unroll
      for (int e = 0; e < 4; ++e) id[j][e] = (lane >> 4) == 2 * j + ((lane >> 3) & 1) && ((lane & 7) >> 1) == e ? ((lane & 1) ? 0x3f800000u : 0x3f80u) : 0u;
      asm volatile("" : "+v"(id[j]));
    }
    unsigned rad = tb + wid * 4096 + (lane & 15) * 128 + (((lane >> 4) ^ (lane & 7)) << 4), rfa = flg + 36 + wid;
    asm volatile("" : "+v"(rad), "+v"(rfa));
    u32x4 rb[2][2];
    rwr = *reinterpret_cast<const u32x4*>(w_r + col);
    setup();
    asm volatile("" : "+v"(rwr));
    announce();
    u32x4 af[PD][2];
    s16x8 wf[2][4];
#pragma unroll
    for (int i = 0; i < PD; ++i) { af[i][0] = lda(i, 0); af[i][1] = lda(i, 1); }
    PIN();
    __builtin_amdgcn_s_setprio(OPRIO);
#pragma unroll
    for (int q = 0; q < 4; ++q) rb[q >> 1][q & 1] = lds_rd4((rad ^ ((q & 1) * 64)) + (q >> 1) * 2048);
    PIN();
#pragma unroll
    for (int j = 0; j < 4; ++j)
#pragma unroll
      for (int h = 0; h < 2; ++h) { asm volatile("v_mfma_f32_16x16x32_bf16 %0, %1, %2, 0" : "=&v"(acc[h][j]) : "v"(id[j & 1]), "v"(rb[h][j >> 1])); PIN(); }
#pragma unroll 1
    for (int spin = 0; spin < SPIN_MAX; ++spin)
      if (__builtin_amdgcn_readfirstlane(*LDS_AT(const volatile unsigned, flg)) >= 40u) break;
    asm volatile("" : "+v"(va0) :: "memory");
    PIN();
#pragma unroll
    for (int j = 0; j < 4; ++j) wf[0][j] = ldw_(0, j);
    s16x4 wlo;
    static_for<0, 32>([&](auto ic) __attribute__((always_inline)) {
      constexpr int i = decltype(ic)::value;
      if (i == SIGAT) { __hip_atomic_fetch_add(LDS_AT(unsigned, oad + 4), 1u, __ATOMIC_RELAXED, __HIP_MEMORY_SCOPE_WORKGROUP); PIN(); }
#pragma unroll
      for (int g = 0; g < 8; ++g) {
        const int j = g >> 1, h = g & 1;
        asm volatile("v_mfma_f32_16x16x32_bf16 %0, %1, %2, %0" : "+v"(acc[h][j]) : "v"(wf[i & 1][j]), "v"(af[i % PD][h]));
        PIN();
        if (i == 15 && !(g & 1)) { wad[g >> 1] += 65536; asm volatile("" : "+v"(wad[g >> 1])); }
        if (i + 1 < 32) {
          const s16x4 v = __builtin_amdgcn_ds_read_tr16_b64_v4i16(LDS_AT(s16x4, wad[g >> 1] + ((i + 1) & 15) * 4096 + (g & 1) * 1024));
          if (g & 1) wf[(i + 1) & 1][g >> 1] = (s16x8)__builtin_shufflevector(wlo, v, 0, 1, 2, 3, 4, 5, 6, 7);
          else wlo = v;
          PIN();
        }
        if (g >= 6 && i + PD < 32) { af[i % PD][h] = lda(i + PD, h); PIN(); }
        if (i + 1 < 32 && i != SIGAT && i + 1 != SIGAT) {
          if (g == 3) { __builtin_amdgcn_s_waitcnt(0xc47f); PIN(); }
          if (g == 7 && i + PD < 32) { __builtin_amdgcn_s_waitcnt(0x0470 | (2 * PD - 2)); PIN(); }
          if (g == 7 && i + PD >= 32) { __builtin_amdgcn_s_waitcnt(0xc47f); PIN(); }
        }
      }
    });
    asm volatile("s_nop 3" : "+v"(acc[0][0]), "+v"(acc[1][0]));
    TE(4, 1); TE(7, 2);
    if (wid == 0) STAMP(dk, 17);
    if (wid == 1 && !RES2) STAMP(4, 0);
#pragma unroll
    for (int j = 0; j < 4; ++j)
#pragma unroll
      for (int h = 0; h < 2; ++h) {
        *LDS_AT(u32x2, tw[j] + h * 2048) = u32x2{pack2(acc[h][j][0], acc[h][j][1]), pack2(acc[h][j][2], acc[h][j][3])};
        PIN();
      }
    asm volatile("" ::: "memory");
    *LDS_AT(volatile unsigned char, rfa) = (unsigned char)2;
#pragma unroll
    for (int p = 0; p < 2; ++p) xres[p] = lds_rd4(tr + p * 1024);
    PIN();
    if (!RES2) {
      const __amdgpu_buffer_rsrc_t rso = __builtin_amdgcn_make_buffer_rsrc(out, 0, S_ * D_ * 2, 0x00020000);
      const __amdgpu_buffer_rsrc_t rsf = __builtin_amdgcn_make_buffer_rsrc(xsig + wid, 0, RX * 512, 0x00020000);
      const u32x4 x2 = lds_rd4(tr + 2048), x3 = lds_rd4(tr + 3072);
      PIN();
#pragma unroll
      for (int p = 0; p < 4; ++p) { __builtin_amdgcn_raw_buffer_store_b128(p < 2 ? xres[p] : (p < 3 ? x2 : x3), rso, xo + p * (8 * ldw * 2), 0, XAUX); PIN(); }
      TE(4, 2); TE(5, 0);
      const float s0 = sums(xres[0], xres[1]), s1 = sums(x2, x3);
      if ((lane & 3) != 3) { __builtin_amdgcn_global_atomic_fadd_f64(pst, (double)s0); __builtin_amdgcn_global_atomic_fadd_f64(pst + 16, (double)s1); }
      PIN();
      TE(4, 3); TE(5, 1);
      asm volatile("s_waitcnt vmcnt(2)" ::: "memory");
      __builtin_amdgcn_raw_buffer_store_b32(tag, rsf, lane * 512 + 256, 0, 16);
      *LDS_AT(volatile unsigned char, rfa) = (unsigned char)3;
      PIN();
      TE(5, 2);
      asm volatile("s_waitcnt vmcnt(1)" ::: "memory");
      __builtin_amdgcn_raw_buffer_store_b32(tag, rsf, lane * 512, 0, 16);
      PIN();
      TE(5, 3);
      __builtin_amdgcn_s_setprio(0);
      return;
    }
    TE(4, 2); TE(5, 0);
  } else {
    u32x4 rres[4];
    double is1[4], is2[4];
#pragma unroll
    for (int p = 0; p < 4; ++p)
      rres[p] = __builtin_amdgcn_raw_buffer_load_b128(__builtin_amdgcn_make_buffer_rsrc(const_cast<u16*>(res), 0, S_ * D_ * 2, 0x00020000), ((row - 16 + 8 * p) * ldw + col) * 2, 0, 16);
    rwr = *reinterpret_cast<const u32x4*>(w_r + col);
#pragma unroll
    for (int p = 0; p < 4; ++p) { is1[p] = ld_ag(stats_in + row - 16 + 8 * p); is2[p] = ld_ag(stats_in + S_ + row - 16 + 8 * p); }
    PIN();
    setup();
    if (RES2) {
#pragma unroll
      for (int p = 0; p < 4; ++p) {
        float mean, var, mb, den;
        ln_from_sums(is1[p], is2[p], mean, var, mb, den);
        const float rin = frcp(den);
#pragma unroll
        for (int e = 0; e < 4; ++e) {
          const f32x2 x = {__uint_as_float(rres[p][e] << 16), __uint_as_float(rres[p][e] & 0xffff0000u)};
          const f32x2 h = (x - f32x2{mb, mb}) * f32x2{rin, rin} + x;
          rres[p][e] = pack2(h[0], h[1]);
        }
        asm volatile("" : "+v"(rres[p]));
      }
      PIN();
    }
    if ((idx >> 5) == tmi * 4 + wid - 4) {
      const int dr = idx - (row - 16);
      u32x4 qq;
#pragma unroll 1
      for (int i = 0; i < SPIN_MAX; ++i) {
        qq = __builtin_amdgcn_raw_buffer_load_b128(__builtin_amdgcn_make_buffer_rsrc(hand, 0, HAND_N * 4, 0x00020000), (GRAN_N + Y_N) * 4 + col * 2, 0, 16);
        if (none(umax(umax(qq[0], qq[1]), umax(qq[2], qq[3])) == SENT)) break;
        asm volatile("" ::: "memory");
      }
#pragma unroll
      for (int p = 0; p < 4; ++p)
#pragma unroll
        for (int e = 0; e < 4; ++e) rres[p][e] = dr == 8 * p ? qq[e] : rres[p][e];
    }
#pragma unroll 1
    for (int i = 0; i < 4 * SPIN_MAX; ++i)
      if (__builtin_amdgcn_readfirstlane(*LDS_AT(const volatile unsigned, flg + 16)) >= 12u) break;
#pragma unroll
    for (int p = 0; p < 4; ++p) *LDS_AT(volatile u32x4, tr - 2048 + p * 1024) = rres[p];
    asm volatile("s_waitcnt lgkmcnt(0)" ::: "memory");
    announce();
    TE(4, 1); TE(7, 2);
    asm volatile("s_waitcnt vmcnt(0)" : "+v"(rwr) :: "memory");
    if (!RES2) return;
    __builtin_amdgcn_s_sleep(TPRE);
#pragma unroll 1
    for (int i = 0; i < 4 * SPIN_MAX; ++i) {
      if (__builtin_amdgcn_readfirstlane(*LDS_AT(const volatile unsigned char, flg + 32 + wid)) == 2u) break;
      __builtin_amdgcn_s_sleep(TPSL);
    }
    asm volatile("" ::: "memory");
    __builtin_amdgcn_s_setprio(XPRIO);
    PIN();
    TE(4, 2); TE(5, 0);
#pragma unroll
    for (int p = 0; p < 2; ++p) xres[p] = lds_rd4(tr + p * 1024);
  }
  TE(4, 3); TE(7, 3);
  const float sv = sums(xres[0], xres[1]);
  if (wid == 0) STAMP(dk, 14);
  if ((lane & 3) != 3) __hip_atomic_store(pps, f2word(sv), __ATOMIC_RELAXED, __HIP_MEMORY_SCOPE_AGENT);
  PIN();
  __builtin_amdgcn_s_setprio(0);
  __hip_atomic_fetch_add(LDS_AT(unsigned, oad), 1u, __ATOMIC_RELAXED, __HIP_MEMORY_SCOPE_WORKGROUP);
  TE(5, 1); TE(5, 2);
  PIN();
}

#define MFMA32(a, b, c) __builtin_amdgcn_mfma_f32_32x32x16_bf16(a, b, c, 0, 0, 0)
#define MFMA16(a, b, c) __builtin_amdgcn_mfma_f32_16x16x32_bf16(a, b, c, 0, 0, 0)

#define PLO(x) __builtin_bit_cast(bf16x16, __builtin_shufflevector(x, x, 0, 1, 2, 3, 4, 5, 6, 7))
#define PHI(x) __builtin_bit_cast(bf16x16, __builtin_shufflevector(x, x, 8, 9, 10, 11, 12, 13, 14, 15))
#define SMF32(a, b, c) __builtin_amdgcn_smfmac_f32_32x32x32_bf16(__builtin_bit_cast(bf16x8, a), b, c, sidx, 0, 0)
#define SMF16(a, b, c) __builtin_amdgcn_smfmac_f32_16x16x64_bf16(__builtin_bit_cast(bf16x8, a), b, c, sidx, 0, 0)

__device__ __forceinline__ void patch_kv(unsigned tab, int r, s16x8& k0, s16x8& k1, s16x8& v0, s16x8& v1, bool on = true) {
  const int lane = fresh_lane(), hi = lane >> 5;
  if (on && (lane & 31) == kpos(r)) {
    k0 = *LDS_AT(const s16x8, tab + hi * 16); k1 = *LDS_AT(const s16x8, tab + 32 + hi * 16);
  }
  const short vv = *LDS_AT(const short, tab + 128 + (lane & 31) * 2);
  const int js = (on && hi == ((r >> 2) & 1)) ? (r >> 4) * 8 + ((r >> 3) & 1) * 4 + (r & 3) : -1;
#pragma unroll
  for (int e = 0; e < 8; ++e) { v0[e] = js == e ? vv : v0[e]; v1[e] = js == 8 + e ? vv : v1[e]; }
}

__device__ __forceinline__ void attn_safe(const u16* KV, int tile0, s16x8 q0, s16x8 q1,
                                       f32x16& o, float& l_out, int ptile, int prow, unsigned ptab) {
  const int lane = fresh_lane();
  float m = -INFINITY, l = 0.f;
  const int sidx = 0xDDDDDDDD;
  const __amdgpu_buffer_rsrc_t rs = __builtin_amdgcn_make_buffer_rsrc(const_cast<u16*>(KV), 0, 2 * S_ * D_ * 2, 0x00020000);
#pragma unroll
  for (int e = 0; e < 16; ++e) o[e] = 0.f;
  for (int tt = 0; tt < 64; ++tt) {
    const int t = tile0 + tt, off = t * 2048 + lane * 16;
    s16x8 k0 = __builtin_bit_cast(s16x8, __builtin_amdgcn_raw_buffer_load_b128(rs, off, 0, 16));
    s16x8 k1 = __builtin_bit_cast(s16x8, __builtin_amdgcn_raw_buffer_load_b128(rs, off + 1024, 0, 16));
    s16x8 v0 = __builtin_bit_cast(s16x8, __builtin_amdgcn_raw_buffer_load_b128(rs, off + S_ * D_ * 2, 0, 16));
    s16x8 v1 = __builtin_bit_cast(s16x8, __builtin_amdgcn_raw_buffer_load_b128(rs, off + S_ * D_ * 2 + 1024, 0, 16));
    if (t == ptile) patch_kv(ptab, prow, k0, k1, v0, v1);
    f32x16 sc;
#pragma unroll
    for (int e = 0; e < 16; ++e) sc[e] = 0.f;
    sc = MFMA32(k0, q0, sc); sc = MFMA32(k1, q1, sc);
    float mt = sc[0];
#pragma unroll
    for (int e = 1; e < 16; ++e) mt = fmaxf(mt, sc[e]);
    mt = fmaxf(mt, __shfl_xor(mt, 32, 64));
    const float mn = fmaxf(m, mt);
    const float alpha = __builtin_amdgcn_exp2f(m - mn);
    m = mn;
#pragma unroll
    for (int e = 0; e < 16; ++e) sc[e] = __builtin_amdgcn_exp2f(sc[e] - mn);
    float ls = 0.f;
#pragma unroll
    for (int e = 0; e < 16; ++e) ls += __uint_as_float(__float_as_uint(sc[e]) & 0xffff0000u);
    l = l * alpha + ls;
#pragma unroll
    for (int e = 0; e < 16; ++e) o[e] *= alpha;
    o = SMF32(v0, PLO(sc), o); o = SMF32(v1, PHI(sc), o);
  }
  l_out = l + __shfl_xor(l, 32, 64);
}

__device__ __forceinline__ float wave_fmax(float v) {
  asm volatile("s_nop 1\n\tv_max_f32_dpp %0, %0, %0 row_shr:1 row_mask:0xf bank_mask:0xf\n\ts_nop 1\n\tv_max_f32_dpp %0, %0, %0 row_shr:2 row_mask:0xf bank_mask:0xf\n\t"
               "s_nop 1\n\tv_max_f32_dpp %0, %0, %0 row_shr:4 row_mask:0xf bank_mask:0xf\n\ts_nop 1\n\tv_max_f32_dpp %0, %0, %0 row_shr:8 row_mask:0xf bank_mask:0xf\n\t"
               "s_nop 1\n\tv_max_f32_dpp %0, %0, %0 row_bcast:15 row_mask:0xa bank_mask:0xf\n\ts_nop 1\n\tv_max_f32_dpp %0, %0, %0 row_bcast:31 row_mask:0xc bank_mask:0xf\n\ts_nop 0"
               : "+v"(v));
  return __int_as_float(__builtin_amdgcn_readlane(__float_as_int(v), 63));
}

#define PSLP 8
#define WSLP 3
#define PRE1 8
#define PRE5 8
#define W3SLP 4
#define FSTART 10
#define OAUX 16
#define OMX 4
#define T0 2u
#define TW 10
#define HW 2
#define HP1 1
#define POLLAT 11
#define X2AT 2
#define HPRIO 3
#define SSLP 1
#define SPRE 6
#define WGAPS {0, 1, 7, 8, 9, 10, 11, 12}
#define KVS 4
#define PNS 12
#define G0 5u
#define GS 2u
#define IAT 1u
#define YSLP 2
#define ISLP 8
#define KSLP 2
#define UAT 13u
#define UHW 0
#define PPSLP 2
#define XRSLP 2
#define MPRE 20
#define OSLP 2
#define YD1 150
#define YD2 180
#define KDT 200
#define XDT 65
#define R2PRIO 0
#define LNPRIO 1
#define MSLP 4
#define BARG 0
struct Args {
  long long lay;
  const u16* a; u16* kv; u16* o; u16* xo; double* so;
  const u16* W; u16* wc;
  const u16 *wg, *wu, *wd, *wo, *w_r;
  unsigned* gran; float* csf; double* stats; unsigned* hand; u16* out;
};
typedef __attribute__((address_space(4))) const Args* ArgP;
#define WCAT 3
#define CAP0 1
#define PRE4 50
#define PRE7 20
#define UARR 12u
#define GF0 10
#define XGATE 8
#define ASLP 1
#define B15 3
#define KO 5
#define PRO 2
#define HS 2
#define WMAP 0x32100333
#define SMAP 0
#define STCH 6
#define KPOLL 10
#define TPRIO 2
#define TSLP 9
__device__ __forceinline__ bool slow_cols(const u16* W, int tni) { return ((((unsigned)(uintptr_t)W >> 7) + tni) & 7) == 3; }
#define ARGS_HERE                                                                                                      \
  ArgP ap_ = ap; asm volatile("" : "+s"(ap_));                                                                          \
  const u16* A = ap_->a; const u16* __restrict__ W = ap_->W; u16* KV = ap_->kv; u16* Of = ap_->o;        \
  const u16* __restrict__ wg = ap_->wg; const u16* __restrict__ wu = ap_->wu; const u16* __restrict__ wd = ap_->wd;       \
  const u16* __restrict__ wo = ap_->wo; const u16* __restrict__ w_r = ap_->w_r; unsigned* gran = ap_->gran;              \
  float* csf = ap_->csf; const double* stats = ap_->stats; double* stats_out = ap_->so; unsigned* hand = ap_->hand; \
  u16* xout = ap_->xo; u16* __restrict__ out = ap_->out
#define AGAIN "s_getpc_b64 s[90:91]\n\ts_add_u32 s90, s90, layers_kernel@rel32@lo+4\n\ts_addc_u32 s91, s91, layers_kernel@rel32@hi+12\n\ts_setpc_b64 s[90:91]"
#define KEPT(c)                                                                                                        \
  c "{v[104:107]}"(gu[0]), c "{v[108:111]}"(gu[1]), c "{v[112:115]}"(gu[2]), c "{v[116:119]}"(gu[3]), c "{v[120:123]}"(gu[4]),      \
  c "{v[124:127]}"(gu[5]), c "{v[128:131]}"(gu[6]), c "{v[132:135]}"(gu[7]), c "{v[136:139]}"(gu[8]), c "{v[140:143]}"(gu[9]),      \
  c "{v[144:147]}"(gu[10]), c "{v[148:151]}"(gu[11]), c "{v[152:155]}"(gu[12]), c "{v[156:159]}"(gu[13]), c "{v[160:163]}"(gu[14]), \
  c "{v[164:167]}"(gu[15])
#define LAYER_HERE ArgP apl_ = ap; asm volatile("" : "+s"(apl_)); const bool LAY2 = apl_->lay != 0
__device__ __forceinline__ void layer_body(const int wid, const int bid, const unsigned lbase, const ArgP ap, const unsigned long long t_entry) {
  constexpr int K = 1024, NKT = 16, NST = 3, ABYTES = 16384, WBYTES = 24576, STB = ABYTES + WBYTES, ldw = 3 * D_;
  constexpr int TABA = NST * STB, ALPHA = TABA + 2048, CRED = ALPHA + 512;
  constexpr int ASB = 16384, FLG = 8 * ASB + 256;
  constexpr int DUM = FLG + 64, TAB = DUM + 256, RED = TAB + 512, SACT = RED + 4096, XML = SACT + 3584, UGL = XML + 4096, SPM = UGL + 1024, QOFF = 9 * ASB;
  constexpr int TABW = RED;
  static_assert(SPM + 2048 <= QOFF && CRED <= 8 * ASB && RED + 8192 <= QOFF && RED % 16 == 0, "lds");
  const int xcd = (bid & 1) * 4 + ((bid >> 1) & 3), loc = bid >> 3;
  const int tmi = (xcd / QNG) * QMX + loc / QNX, tni = (xcd % QNG) * QNX + loc % QNX;
  const int m0 = tmi * 128, n0 = tni * 64;
  const int omi = (xcd / (OMX / 2)) * OMX + loc / (32 / OMX), oni = (xcd % (OMX / 2)) * (32 / OMX) + loc % (32 / OMX);
  constexpr unsigned VOFF = (unsigned)S_ * D_ * 2;
  auto tile_at = [&](unsigned n) __attribute__((always_inline)) {
#pragma unroll 1
    for (int i = 0; i < 4 * SPIN_MAX; ++i) {
      if (__builtin_amdgcn_readfirstlane(*LDS_AT(const volatile unsigned, lbase + FLG + 36)) == n * 0x01010101u) break;
      __builtin_amdgcn_s_sleep(1);
    }
    asm volatile("" ::: "memory");
    PIN();
  };
  auto all_ge = [&](unsigned w, unsigned n) { return ((w + (128u - n) * 0x01010101u) & 0x80808080u) == 0x80808080u; };
  if (wid >= 8) {
    u32x4 gu[16];
    LAYER_HERE;
    constexpr int dk = 6;
    const int hw = wid - 8, b = bid;
    int nb = 0;
    constexpr int lim = NKT - 1;
    auto tick_nw = [&]() {
      PIN();
      if (nb < lim) { __builtin_amdgcn_s_barrier(); ++nb; } else __builtin_amdgcn_s_sleep(2);
      asm volatile("" ::: "memory");
      PIN();
    };
    auto tick = [&]() {
      asm volatile("s_waitcnt lgkmcnt(0)" ::: "memory");
      tick_nw();
    };
    auto fold16 = [&](f32x2 y0, f32x2 y1) __attribute__((always_inline)) {
      const auto q0 = __builtin_amdgcn_permlane32_swap(__float_as_uint(y0[0]), __float_as_uint(y1[0]), false, false);
      const auto q1 = __builtin_amdgcn_permlane32_swap(__float_as_uint(y0[1]), __float_as_uint(y1[1]), false, false);
      const auto q = __builtin_amdgcn_permlane16_swap(__float_as_uint(__uint_as_float(q0[0]) + __uint_as_float(q0[1])),
                                                      __float_as_uint(__uint_as_float(q1[0]) + __uint_as_float(q1[1])), false, false);
      return __uint_as_float(q[0]) + __uint_as_float(q[1]);
    };
    unsigned best, tag, rmd;
    float alpha, m, wsum;
    u32x4 tw;
    u32x2 xq;
    s16x8 sl[8];
    const int kcap = CAP0 + (tmi & 7);
    if (!LAY2) {
      ARGS_HERE;
      const int lane = fresh_lane(), t = hw * 64 + lane;
      const unsigned ep = ld_ag(gran);
      const int srow = m0 + tni * 8 + hw * 2;
      u32x4 xa[2][2], wv[8];
      const int wr_ = (3 - ((unsigned)(uintptr_t)W >> 7) - (tni & ~3)) & 7;
      const bool cpy = hw == 3 && wr_ < 4 && wr_ != (tni & 3), wld = hw < 3 ? wr_ != (tni & 3) : cpy;
      const int wcn = cpy ? (tni & ~3) + wr_ : tni, wcp = cpy ? (tni - wr_ - 1) & 3 : hw;
      const int wof = ((tmi * 64 + (lane >> 3)) * ldw + wcp * D_ + wcn * 64 + (lane & 7) * 8) * 2;
      auto ld_w = [&]() {
        const __amdgpu_buffer_rsrc_t rw = __builtin_amdgcn_make_buffer_rsrc(const_cast<u16*>(W), 0, wld ? D_ * ldw * 2 : 0, 0x00020000);
#pragma unroll
        for (int r = 0; r < 8; ++r) wv[r] = __builtin_amdgcn_raw_buffer_load_b128(rw, wof, r * 8 * ldw * 2, 0);
        PIN();
      };
      auto wcopy = [&]() __attribute__((always_inline)) {
        if (cpy) {
          const __amdgpu_buffer_rsrc_t rw = __builtin_amdgcn_make_buffer_rsrc(ap_->wc, 0, D_ * ldw * 2, 0x00020000);
#pragma unroll
          for (int r = 0; r < 8; ++r) __builtin_amdgcn_raw_buffer_store_b128(wv[r], rw, wof + r * 8 * ldw * 2, 0, 16);
        }
        PIN();
      };
      const int wat = ((hw == 3 ? SMAP : WMAP) >> (4 * (tmi & 7))) & 3;
      unsigned ps = 0u;
      auto stouch = [&](int at) __attribute__((always_inline)) {
        if (at == STCH && cpy) ps = __builtin_amdgcn_raw_buffer_load_b32(__builtin_amdgcn_make_buffer_rsrc(const_cast<u16*>(W), 0, D_ * ldw * 2, 0x00020000),
                                                                        (((tmi ^ 8) * 64 + lane) * ldw + wcp * D_ + wcn * 64) * 2, 0, 0);
        PIN();
      };
      auto ld_x = [&](int h) {
#pragma unroll
        for (int r = 0; r < 2; ++r) xa[r][h] = *reinterpret_cast<const u32x4*>(A + (size_t)(srow + r) * D_ + (h ^ (tmi >> 3)) * 512 + lane * 8);
        PIN();
      };
      ld_x(0);
      if (X2AT == 0) ld_x(1);
      if (wat == 0) ld_w();
      unsigned pc = 0u;
      if (hw == 3) {
        unsigned long long a0, a1;
        CODE_ADDR("layers_kernel", a0);
        CODE_ADDR("end_marker_kernel", a1);
        pc = touch_code(a0, a1, loc, lane);
      }
      if (hw == 2) st_ag(gran + GR_N + bid * 64 + lane, lane + 1u);
      {
        const int gi = bid * 256 + t;
        if (gi < GRAN_N / 4) __builtin_amdgcn_raw_buffer_store_b128(u32x4{SENT, SENT, SENT, SENT}, __builtin_amdgcn_make_buffer_rsrc(hand, 0, GRAN_N * 4, 0x00020000), gi * 16, 0, 16);
        if (t < 24) __hip_atomic_store(stats_out + (t >> 3) * S_ + m0 + tni * 8 + (t & 7), 0.0, __ATOMIC_RELAXED, __HIP_MEMORY_SCOPE_AGENT);
      }
      tick();
      if (wat == 1) ld_w();
      if (X2AT == 1) ld_x(1);
      tick();
      if (wat == 2) ld_w();
      if (WCAT == 2) wcopy();
      if (X2AT == 2) ld_x(1);
      asm volatile("" :: "v"(ep));
      tick();
      if (wat == 3) ld_w();
      if (WCAT == 3) wcopy();
      if (X2AT == 3) ld_x(1);
      stouch(3);
#pragma unroll
      for (int i = 0; i < HW; ++i) { tick(); stouch(4 + i); }
      __builtin_amdgcn_s_setprio(HP1);
      PIN();
      tag = ep + 1u;
      const __amdgpu_buffer_rsrc_t rsc = __builtin_amdgcn_make_buffer_rsrc(gran, 0, GR_N * 4, 0x00020000);
      {
        u32x4 od;
#pragma unroll
        for (int q = 0; q < 4; ++q) od[q] = ((lane & 7) >> 1) == q ? ((lane & 1) ? 0x3f800000u : 0x00003f80u) : 0u;
        f32x4 d[2] = {{0.f, 0.f, 0.f, 0.f}, {0.f, 0.f, 0.f, 0.f}};
#pragma unroll
        for (int r = 0; r < 8; ++r) d[r & 1] = __builtin_amdgcn_mfma_f32_16x16x32_bf16(__builtin_bit_cast(s16x8, od), __builtin_bit_cast(s16x8, wv[r]), d[r & 1], 0, 0, 0);
        u32x4 gr;
#pragma unroll
        for (int q = 0; q < 4; ++q) gr[q] = ((__float_as_uint(dpp_add<0x128, 0xf>(fadd(d[0][q], d[1][q]))) + 0x80u) & ~0xffu) | ((tag >> (8 * q)) & 0xffu);
        asm volatile("s_waitcnt vmcnt(0)" ::: "memory");
        __builtin_amdgcn_raw_buffer_store_b128(gr, rsc, (lane & 40) || !wld ? GR_N * 4 : (GR_COL + (wcn * 16 + tmi) * 192 + wcp * 64 + (lane & 7) * 8 + (lane >> 4) * 4) * 4, 0, 16);
      }
      asm volatile("" :: "v"(pc));
      tick_nw();
      stouch(6);
      {
        float xv[2][16], mean[2];
#pragma unroll
        for (int r = 0; r < 2; ++r) {
          float sm = 0.f;
#pragma unroll
          for (int i = 0; i < 4; ++i) {
            xv[r][2 * i] = __uint_as_float(xa[r][0][i] << 16); xv[r][2 * i + 1] = __uint_as_float(xa[r][0][i] & 0xffff0000u);
            xv[r][8 + 2 * i] = __uint_as_float(xa[r][1][i] << 16); xv[r][8 + 2 * i + 1] = __uint_as_float(xa[r][1][i] & 0xffff0000u);
          }
#pragma unroll
          for (int i = 0; i < 16; ++i) sm += xv[r][i];
          mean[r] = wave_sum(sm) * (1.0f / 1024.0f);
          tick();
          stouch(7 + r);
        }
        float vr[2];
#pragma unroll
        for (int r = 0; r < 2; ++r) {
          float v = 0.f;
#pragma unroll
          for (int i = 0; i < 16; ++i) { const float d = xv[r][i] - mean[r]; v += d * d; }
          vr[r] = wave_sum(v) * (1.0f / 1024.0f);
          if (r == 0) tick();
        }
        const float vv = lane & 2 ? vr[1] : vr[0], mm = lane & 2 ? mean[1] : mean[0];
        const float rin = 1.0f / rbf(sqrtf(rbf(rbf(vv) + 1e-5f)));
        if (lane < 4) __builtin_amdgcn_raw_buffer_store_b64(u32x2{__float_as_uint((lane & 1) ? rbf(mm) : rin), tag}, rsc, (GR_ROW + srow * 4 + lane * 2) * 4, 0, 16);
      }
      tick();
      float cs;
      while (nb < POLLAT) tick();
      asm volatile("" :: "v"(ps));
#pragma unroll 1
      for (int spin = 0;; ++spin) {
        u32x4 csv[4];
        const __amdgpu_buffer_rsrc_t rsw = __builtin_amdgcn_make_buffer_rsrc(gran, 0, hw < 3 ? GR_N * 4 : 0, 0x00020000);
#pragma unroll
        for (int i = 0; i < 4; ++i) csv[i] = __builtin_amdgcn_raw_buffer_load_b128(rsw, (GR_COL + (tni * 16 + (lane >> 4)) * 192 + hw * 64 + (lane & 15) * 4) * 4, i * (4 * 192 * 4), 16);
        const u32x4 rp = __builtin_amdgcn_raw_buffer_load_b128(rsc, (GR_ROW + (m0 + (t & 127)) * 4) * 4, 0, 16);
        PIN();
        tick(); tick();
        unsigned bad = 0u;
        float z[4];
#pragma unroll
        for (int q = 0; q < 4; ++q) {
          const unsigned tq = tag >> (8 * q);
          bad |= ((csv[0][q] ^ tq) | (csv[1][q] ^ tq)) | ((csv[2][q] ^ tq) | (csv[3][q] ^ tq));
          z[q] = fadd(fadd(__uint_as_float(csv[0][q] & ~0xffu), __uint_as_float(csv[1][q] & ~0xffu)), fadd(__uint_as_float(csv[2][q] & ~0xffu), __uint_as_float(csv[3][q] & ~0xffu)));
        }
        cs = fold16(f32x2{z[0], z[1]}, f32x2{z[2], z[3]});
        bad = (hw < 3 ? bad & 0xffu : 0u) | (rp[1] ^ tag) | (rp[3] ^ tag);
        alpha = __uint_as_float(rp[0]); m = __uint_as_float(rp[2]);
        if (none(bad != 0u) || spin >= SPIN_MAX) break;
      }
      if (tmi == 0 && hw < 3) st_ag(reinterpret_cast<unsigned*>(csf) + hw * 1024 + n0 + (lane & 15) * 4 + (lane >> 4), __float_as_uint(cs));
      unsigned c0, c1, c2;
      split3(cs, c0, c1, c2);
      tw = u32x4{c0 | (c1 << 16), c0 | (c2 << 16), c1 | (c0 << 16), 0u};
      best = rmd = 0u;
      wsum = 0.f;
      asm volatile("" : "=v"(xq), KEPT("="));
#pragma unroll
      for (int i = 0; i < 8; ++i) asm volatile("" : "=v"(sl[i]));
    } else {
      asm volatile("; kept registers" : KEPT("="));
      PIN();
      ARGS_HERE;
      const int lane = fresh_lane(), t = hw * 64 + lane;
      best = rmd = 0u;
      tag = __builtin_amdgcn_readfirstlane(*LDS_AT(const volatile unsigned, lbase + FLG + 20));
#pragma unroll
      for (int i = 0; i < 8; ++i) asm volatile("" : "=v"(sl[i]));
      unsigned sad;
      auto tickc = [&]() __attribute__((always_inline)) {
        tick();
        if (nb == kcap + 1) {
#pragma unroll
          for (int i = 0; i < 8; ++i) {
            const unsigned a = (sad ^ ((i >> 2) * 64) ^ (((i >> 1) & 1) * 16)) + (i & 1) * 12288;
            sl[i] = __builtin_shufflevector(__builtin_amdgcn_ds_read_tr16_b64_v4i16(LDS_AT(s16x4, a)), __builtin_amdgcn_ds_read_tr16_b64_v4i16(LDS_AT(s16x4, a + 1536)), 0, 1, 2, 3, 4, 5, 6, 7);
          }
        }
        PIN();
      };
      static_assert(CAP0 + 8 <= KPOLL, "the stage is taken before that");
      if (hw == 1) STAMP(4, 4);
      if (hw == 0) {
        __builtin_amdgcn_s_sleep(TPRE);
        tile_at(3u);
        __builtin_amdgcn_s_sleep(XGATE);
        STAMP(4, 3);
#pragma unroll 1
        for (int spin = 0; spin < SPIN_MAX; ++spin) {
          if (none(ld_ag(gran + GR_X + (tmi * RX + tni % RX) * 128 + 64 + lane) != tag)) break;
          __builtin_amdgcn_s_sleep(1);
        }
      }
      if (hw == 0) STAMP(4, 5);
      asm volatile("" ::: "memory");
      PIN();
      __builtin_amdgcn_s_barrier();
      PIN();
      if (hw == 1) STAMP(4, 6);
      {
        const int ln = fresh_lane(), g4 = ln >> 4, tp = ln & 3, k = 8 * g4 + ((ln & 15) >> 2);
        sad = lbase + (kcap % NST) * STB + ABYTES + k * 384 + (((2 * (tp >> 1)) ^ (4 * ((k >> 1) & 1)) ^ (g4 & 1)) << 4) + 8 * (tp & 1) + (hw % 3) * 128;
      }
      wsum = __uint_as_float(__builtin_amdgcn_readfirstlane(*LDS_AT(const volatile unsigned, lbase + FLG + 60)));
      asm volatile("" : "+v"(sad), "=v"(tw), "=v"(alpha), "=v"(m));
      PIN();
      const int row = m0 + (t & 127);
      const __amdgpu_buffer_rsrc_t rsg = __builtin_amdgcn_make_buffer_rsrc(hand, 0, GRAN_N * 4, 0x00020000);
      if (hw < 2 || tmi == tni) {
      while (nb < HS) tickc();
#pragma unroll 1
      for (int spin = 0; spin < SPIN_MAX; ++spin) {
        const unsigned v = ld_ag(gran + GR_X + (tmi * RX + tni % RX) * 128 + lane);
        tickc();
        if (none(v != tag)) break;
      }
      asm volatile("" ::: "memory");
      PIN();
      const double s1 = ld_ag(stats + row), s2 = ld_ag(stats + S_ + row), sw = ld_ag(stats + 2 * S_ + row);
      tickc();
      float mean, var, mb, den;
      ln_from_sums(s1, s2, mean, var, mb, den);
      const float rin = frcp(den), g1 = 1.0f + rin;
      const float mb2 = rbf(mean + (mean - mb) * rin);
      const float den2 = rbf(__builtin_amdgcn_sqrtf(rbf(rbf(var * g1 * g1) + 1e-5f)));
      alpha = g1 * frcp(den2); m = (mb * rin + mb2) * frcp(g1);
      if (tmi == tni && hw >= 2) {
        const float r = (float)fma(-(double)mb, (double)wsum, sw) * rin;
        const unsigned rb = pack2(r, r);
        unsigned rkey = (rb ^ ((unsigned)((int)rb >> 31) | 0x80000000u)) & 0xffff0000u;
        if (r != r) rkey = 0u;
        rkey = wave_umax(rkey | (unsigned)(2047 - row));
        const unsigned md = __builtin_amdgcn_readlane(f2word(__uint_as_float((__float_as_uint(mb) >> 16) | (__float_as_uint(den) & 0xffff0000u))), (2047 - (int)(rkey & 0xffffu)) & 63);
        __builtin_amdgcn_raw_buffer_store_b64(u32x2{rkey, md}, rsg, lane < NREP ? lane * 256 + (tmi * 2 + hw - 2) * 8 : GRAN_N * 4, 0, 16);
      }
      }
      while (nb < KPOLL) tickc();
#pragma unroll 1
      for (int spin = 0; spin < SPIN_MAX; ++spin) {
        const u32x2 v = __builtin_amdgcn_raw_buffer_load_b64(rsg, (bid % NREP) * 256 + (lane & 31) * 8, 0, 16);
        tick(); tick();
        if (none(umax(v[0], v[1]) == SENT)) {
          best = wave_umax(v[0]);
          rmd = __builtin_amdgcn_readlane(v[1], ((2047 - (int)(best & 0xffffu)) >> 6) & 31);
          break;
        }
      }
      const int ix = (2047 - (int)(best & 0xffffu)) & 2047;
      if (row == ix) alpha = 0.f;
      xq = __builtin_amdgcn_raw_buffer_load_b64(__builtin_amdgcn_make_buffer_rsrc(const_cast<u16*>(A), 0, S_ * D_ * 2, 0x00020000), (ix * D_ + hw * 256 + (lane & 3) * 64 + (lane >> 2) * 4) * 2, 0, 16);
      PIN();
    }
    tag = __builtin_amdgcn_readfirstlane(tag);
    wsum = __uint_as_float(__builtin_amdgcn_readfirstlane(__float_as_uint(wsum)));
    const int lane = fresh_lane(), t = hw * 64 + lane;
    {
      unsigned a0, a1, a2;
      split3(-m, a0, a1, a2);
      u32x4 ta = {a0 | (a0 << 16), a1 | (a0 << 16), a1 | (a2 << 16), 0u};
      asm volatile("" : "+v"(tw), "+v"(ta), "+v"(alpha));
      __builtin_amdgcn_s_setprio(TPRIO);
      if (nb < lim) {
        while (nb < lim) tick();
        __builtin_amdgcn_s_sleep(TSLP);
      }
      if (!LAY2 && hw < 3) *LDS_AT(u32x4, lbase + TABW + (hw * 64 + (lane & 15) * 4 + (lane >> 4)) * 16) = tw;
      if (t < 128) {
        *LDS_AT(u32x4, lbase + TABA + t * 16) = ta;
        *LDS_AT(float, lbase + ALPHA + t * 4) = alpha;
      }
    }
    if (t >= 192 && t < 196) *LDS_AT(u32x4, lbase + CRED + ((t >> 1) & 1) * 512 + (t & 1) * 128) = u32x4{0u, 0u, 0u, 0u};
    asm volatile("s_waitcnt lgkmcnt(0)" ::: "memory");
    PIN();
    __builtin_amdgcn_s_barrier();
    PIN();
    *LDS_AT(u32x4, lbase + XML + t * 16) = u32x4{SENT, SENT, SENT, SENT};
    *LDS_AT(u32x4, lbase + SPM + (t & 127) * 16) = u32x4{SENT, SENT, SENT, SENT};
    if (hw == 0) {
      unsigned v;
      asm volatile("v_mov_b32 %0, 0\n\tv_writelane_b32 %0, 2, 0\n\tv_writelane_b32 %0, %1, 5\n\tv_writelane_b32 %0, %2, 7"
                   : "=&v"(v) : "s"(tag), "s"(best));
      lds_wr(lbase + FLG + lane * 4, v);
    }
    const int idx = LAY2 ? (2047 - (int)(best & 0xffffu)) & 2047 : -64;
    const bool p1blk = (oni & 7) != 7, fast = tmi < 8, own = fast && (loc & 1);
    const int fg = xcd * 28 + (p1blk ? loc - (loc >> 3) : 0), ow = (xcd & 3) * 16 + (loc >> 1);
    static_assert(OMX == 4, "oni % 8 = loc % 8");
#define GLD(a, b) "global_load_dwordx4 v[" #a ":" #b "], %0, off\n\tv_lshl_add_u64 %0, %0, 0, %1\n\t"
#define GLD4(...) asm volatile(__VA_ARGS__ : "+v"(p) : "s"((unsigned long long)(F_ * 2)), KEPT() : "memory")
    auto gptr = [&](const u16* wg, const u16* wu) {
      const int ln = fresh_lane();
      return ((ln & 2) ? wu : wg) + (size_t)(hw * 256 + (ln >> 2) * 4) * F_ + fg * 16 + (ln & 1) * 8;
    };
    auto gather = [&](const u16*& p, int e) __attribute__((always_inline)) {
      if (e == 0) GLD4(GLD(104, 107) GLD(108, 111) GLD(112, 115) GLD(116, 119));
      else if (e == 1) GLD4(GLD(120, 123) GLD(124, 127) GLD(128, 131) GLD(132, 135));
      else if (e == 2) GLD4(GLD(136, 139) GLD(140, 143) GLD(144, 147) GLD(148, 151));
      else GLD4(GLD(152, 155) GLD(156, 159) GLD(160, 163) GLD(164, 167));
      p += 60 * F_;
    };
    auto dmask = [&](int q) { const int ln = fresh_lane(); return ((ln >> 1) & 1) == q ? ((ln & 1) ? 0xffff0000u : 0x0000ffffu) : 0u; };
    unsigned dm0, dm1, dad;
    auto gu_setup = [&]() __attribute__((always_inline)) {
      const int ln = fresh_lane();
      dm0 = dmask(0); dm1 = dmask(1);
      dad = lbase + SACT + ((ln & 3) * 8 + (ln >> 5) * 4 + ((ln >> 4) & 1)) * 16 + hw * 4;
      asm volatile("" : "+v"(dm0), "+v"(dm1), "+v"(dad));
    };
    auto gate_up = [&](u32x2 x, unsigned md) __attribute__((always_inline)) {
      const unsigned rin = __builtin_amdgcn_readfirstlane(__float_as_uint(frcp(__uint_as_float(md & 0xffff0000u))));
      asm volatile("s_getpc_b64 s[90:91]\n\ts_add_u32 s90, s90, gate_up_code@rel32@lo+4\n\ts_addc_u32 s91, s91, gate_up_code@rel32@hi+12\n\ts_swappc_b64 s[92:93], s[90:91]"
                   : "+{v[100:101]}"(x) : "{s84}"(md << 16), "{s85}"(md & 0xffff0000u), "{s86}"(rin), "{v99}"(dad), "{v102}"(dm0), "{v103}"(dm1), KEPT()
                   : "v84", "v85", "v86", "v87", "v88", "v89", "v90", "v91", "v92", "v93", "v94", "v95", "v96", "v97", "s88", "s89", "s90", "s91", "s92", "s93", "scc", "memory");
    };
    auto act_diag = [&](int so = 0) __attribute__((always_inline)) {
      const int ln = fresh_lane();
      const f32x4 g4 = *LDS_AT(const volatile f32x4, lbase + SACT + so + (ln >> 2) * 16), u4 = *LDS_AT(const volatile f32x4, lbase + SACT + so + 256 + (ln >> 2) * 16);
      float gv = fadd(fadd(g4[0], g4[1]), fadd(g4[2], g4[3])), uv = fadd(fadd(u4[0], u4[1]), fadd(u4[2], u4[3]));
      rbf2(gv, uv);
      const float av = rbf(gv * frcp(1.0f + __expf(-gv))) * uv;
      const unsigned ap2 = pack2(av, av);
      return __builtin_bit_cast(s16x4, u32x2{ap2 & dm0, ap2 & dm1});
    };
    auto down = [&](const s16x4& a, const u32x2 (&w)[16]) __attribute__((always_inline)) {
      f32x4 d[2] = {{0.f, 0.f, 0.f, 0.f}, {0.f, 0.f, 0.f, 0.f}};
      static_for<0, 16>([&](auto fc) __attribute__((always_inline)) {
        constexpr int f = decltype(fc)::value;
        d[f & 1] = __builtin_amdgcn_mfma_f32_4x4x4bf16_1k(a, __builtin_bit_cast(s16x4, w[f]), d[f & 1], 4, f, 0);
      });
      f32x4 z;
#pragma unroll
      for (int e = 0; e < 4; ++e) { z[e] = d[0][e] + d[1][e]; asm volatile("" : "+v"(z[e])); }
      return u32x4{f2word(z[0]), f2word(z[1]), f2word(z[2]), f2word(z[3])};
    };
    auto fold8 = [&](f32x2 y0, f32x2 y1) __attribute__((always_inline)) { return dpp_add<0x128, 0xf>(fold16(y0, y1)); };
    auto fold4 = [&](f32x2 y0, f32x2 y1) __attribute__((always_inline)) { return dpp_add<0x124, 0xf>(fold8(y0, y1)); };
    auto y_sum = [&](const __amdgpu_buffer_rsrc_t rs, auto nbc) __attribute__((always_inline)) {
      constexpr int NB = decltype(nbc)::value, N = 14 / NB;
      const int ln = fresh_lane(), src = (GRAN_N + (ln >> 2) * D_ + ow * 16 + (ln & 3) * 4) * 4;
      f32x2 y0 = {0.f, 0.f}, y1 = {0.f, 0.f};
#pragma unroll 1
      for (int bt = 0; bt < NB; ++bt) {
        f32x2 z0, z1;
#pragma unroll 1
        for (int spin = 0; spin < SPIN_MAX; ++spin) {
          u32x4 v[N];
#pragma unroll
          for (int i = 0; i < N; ++i) v[i] = __builtin_amdgcn_raw_buffer_load_b128(rs, src, (bt * N + i) * (16 * D_ * 4), 16);
          z0 = y0; z1 = y1;
#pragma unroll
          for (int i = 0; i < N; ++i) {
            z0 = f32x2{fadd(z0[0], __uint_as_float(v[i][0])), fadd(z0[1], __uint_as_float(v[i][1]))};
            z1 = f32x2{fadd(z1[0], __uint_as_float(v[i][2])), fadd(z1[1], __uint_as_float(v[i][3]))};
          }
          const float chk = fadd(fadd(z0[0], z0[1]), fadd(z1[0], z1[1]));
          if (none(chk != chk)) break;
          unsigned mx = 0u;
#pragma unroll
          for (int i = 0; i < N; ++i) mx = umax(umax(umax(mx, v[i][0]), v[i][1]), umax(v[i][2], v[i][3]));
          if (none(mx == SENT)) break;
          __builtin_amdgcn_s_sleep(YSLP);
          asm volatile("" ::: "memory");
        }
        y0 = z0; y1 = z1;
      }
      return fold4(y0, y1);
    };
    [&]() __attribute__((always_inline)) {
    ARGS_HERE;
    const unsigned l16 = lane * 16;
    unsigned* kvflag = gran + GR_KV + ((LAY2 ? 16 : 0) + tni) * (32 * RK);
    auto wait_sig = [&](int sh) {
#pragma unroll 1
      for (int i = 0; i < 4 * SPIN_MAX; ++i) {
        if (((__builtin_amdgcn_readfirstlane(*LDS_AT(const volatile unsigned, lbase + FLG + 32)) >> sh) & 7u) >= 4u) break;
        __builtin_amdgcn_s_sleep(SSLP);
      }
      asm volatile("" ::: "memory");
      PIN();
    };
    PIN();
    asm volatile("s_waitcnt lgkmcnt(0)" ::: "memory");
    if (!LAY2) asm volatile("s_waitcnt vmcnt(0)" ::: "memory");
    __builtin_amdgcn_s_barrier();
    PIN();
    __builtin_amdgcn_s_setprio(HPRIO);
    if (hw < 3) {
      __builtin_amdgcn_s_sleep(SPRE);
      wait_sig(0);
      __builtin_amdgcn_raw_buffer_store_b32(tag, __builtin_amdgcn_make_buffer_rsrc(kvflag, 0, hw == 0 ? RK * 128 : 0, 0x00020000), lane * 128 + tmi * 4, 0, 16);
      if (hw == 0) STAMP(LAY2 ? 5 : 1, 16);
    }
    if (hw == 3) {
      __builtin_amdgcn_s_sleep(KVS);
      const unsigned kv0 = ld_ag(kvflag + (tmi % RK) * 32 + (lane & 15));
      PIN();
      const char* cnt = reinterpret_cast<const char*>(gran + GR_N + bid * 64 + 2);
      unsigned vk[8], vp = lbase + FLG + 8 + (lane & 7), pw = 0u;
#pragma unroll
      for (int j = 0; j < 8; ++j) { vk[j] = l16 + (j >> 2) * 131072u + ((j >> 1) & 1) * 4096u + (j & 1) * (VOFF - 2048u); asm volatile("" : "+v"(vk[j])); }
#define G4(a, b, c, d, sc) "s_nop 0\n\tglobal_load_lds_dwordx4 %" #a ", %8" sc "\n\tglobal_load_lds_dwordx4 %" #b ", %8 offset:1024" sc "\n\t"             \
                       "global_load_lds_dwordx4 %" #c ", %8 offset:2048" sc "\n\tglobal_load_lds_dwordx4 %" #d ", %8 offset:3072" sc "\n\t"
#define M4 "s_addk_i32 m0, 0x1000\n\t"
      auto stage = [&](const char* sp, unsigned dst) __attribute__((always_inline)) {
        asm volatile("s_mov_b32 m0, %9\n\t" G4(0, 0, 1, 1, " sc1") M4 G4(2, 2, 3, 3, " sc1") M4 G4(4, 4, 5, 5, " sc1") M4 G4(6, 6, 7, 7, " sc1")
                     :: "v"(vk[0]), "v"(vk[1]), "v"(vk[2]), "v"(vk[3]), "v"(vk[4]), "v"(vk[5]), "v"(vk[6]), "v"(vk[7]), "s"(sp), "s"(dst) : "m0", "scc", "memory");
      };
      auto count = [&]() __attribute__((always_inline)) {
        asm volatile("s_mov_b32 m0, %2\n\ts_nop 0\n\tglobal_load_lds_dword %0, %1 sc1" :: "v"(0u), "s"(cnt), "s"(lbase + FLG - 252) : "m0", "memory");
        cnt += 8;
      };
      auto freed = [&](unsigned n) __attribute__((always_inline)) {
        if (!none(pw < n)) {
#pragma unroll 1
          for (int i = 0; i < 4 * SPIN_MAX; ++i) {
            pw = *LDS_AT(const volatile unsigned char, vp);
            if (none(pw < n)) break;
            __builtin_amdgcn_s_sleep(W3SLP);
          }
        }
        asm volatile("" ::: "memory");
      };
      unsigned long long ord = (0xfedcba9876543210ull >> 4 & ~((1ull << 4 * (tmi & 7)) - 1ull)) | (0xfedcba9876543210ull & ((1ull << 4 * (tmi & 7)) - 1ull));
      ord ^= (tmi & 8) * 0x1111111111111111ull;
      unsigned seen;
      const char* kvb = reinterpret_cast<const char*>(KV) + (size_t)tni * (128 * 2048);
      auto panel = [&]() __attribute__((always_inline)) {
        const unsigned pn = (unsigned)ord & 15u;
        ord >>= 4;
        if (__builtin_expect(!((seen >> pn) & 1u), 0)) {
#pragma unroll 1
          for (int i = 0; i < SPIN_MAX && !((seen >> pn) & 1u); ++i) seen = (unsigned)__ballot(ld_ag(kvflag + (tmi % RK) * 32 + (lane & 15)) == tag);
        }
        asm volatile("" ::: "memory");
        return kvb + pn * 8192u;
      };
      asm volatile("" : "+s"(ord), "+s"(kvb), "+s"(cnt));
      asm volatile("" : "+v"(vp), "+v"(pw));
      PIN();
      seen = (unsigned)__ballot(kv0 == tag);
      const char* sp = panel();
      wait_sig(16);
      stage(sp, lbase + 2 * ASB);
      count();
      const int ksp = LAY2 ? PNS / 2 : -1, ksg = LAY2 ? 64 : GF0;
      const u16* gp = gptr(wg, wu);
#pragma unroll 1
      for (int k = 1; k < 15; ++k) {
        if (__builtin_expect(k == ksp, 0)) {
          gu_setup();
          gate_up(xq, rmd);
          PIN();
          *LDS_AT(volatile unsigned char, lbase + FLG + 7) = (unsigned char)0x7f;
        }
        freed((unsigned)(0xba9876543211000ull >> 4 * k) & 15u);
        stage(sp + 2048, lbase + ((2 * k + 1) & 7) * ASB);
        sp = panel();
        stage(sp, lbase + ((2 * k + 2) & 7) * ASB);
        count();
        if (__builtin_expect((unsigned)(k - ksg) < 4u, 0)) gather(gp, k - ksg);
      }
      freed(12u);
      glds16s<true>(csf + 4096 + tni * 128, l16, lbase + UGL);
      stage(sp + 2048, lbase + 7 * ASB);
      count();
      const unsigned vwo = (unsigned)((lane >> 3) * D_ + oni * 64 + ((lane & 7) ^ ((lane >> 3) & 6)) * 8) * 2u;
      constexpr unsigned vs = 8u * D_ * 2u - 1024u;
#pragma unroll 1
      for (int ns = 0; ns < 6; ++ns) {
        freed((unsigned)(ns + 25) >> 1);
        const char* wp = reinterpret_cast<const char*>(wo) + (size_t)ns * (128 * D_ * 2);
#pragma unroll
        for (int j = 0; j < 4; ++j)
          glds16x4<false>(wp + j * (32u * D_ * 2u), vwo, vwo + vs, vwo + 2 * vs, vwo + 3 * vs, lbase + ns * ASB + j * 4096);
      }
      asm volatile("s_waitcnt vmcnt(0)" ::: "memory");
      return;
    }
    if (!LAY2) {
      auto until = [&](unsigned n) {
        while ((__builtin_amdgcn_readfirstlane(*LDS_AT(const volatile unsigned, lbase + FLG + 8)) & 255u) < n) __builtin_amdgcn_s_sleep(16);
        asm volatile("" ::: "memory");
        PIN();
      };
      if (hw != 1) {
        constexpr int N2 = HAND_N + Y_N + 3 * D_ / 2;
        const __amdgpu_buffer_rsrc_t rsi = __builtin_amdgcn_make_buffer_rsrc(hand + (hw ? GRAN_N : 2 * HAND_N), 0, (hw ? N2 : PS_N) * 4, 0x00020000);
        until(IAT);
#pragma unroll 1
        for (int i = 0; i < ((hw ? N2 : PS_N) / 4 + 16383) / 16384; ++i) {
          __builtin_amdgcn_raw_buffer_store_b128(u32x4{SENT, SENT, SENT, SENT}, rsi, ((i * 256 + bid) * 64 + lane) * 16, 0, 16);
          __builtin_amdgcn_s_sleep(ISLP);
        }
      }
      const int ng = loc & 15, fc = xcd * 2 + (loc >> 4);
      const size_t og = (size_t)((fg - xcd * 28) / 7 * 256 + lane) * F_ + (xcd * 7 + fg % 7) * 64;
      const u16* pd = wd + (size_t)(fc * 224 + (lane < 56 ? lane : 0)) * D_ + ng * 64;
      const u16* p = gptr(wg, wu);
#pragma unroll 1
      for (unsigned n = T0; n < T0 + 13u; ++n) {
        until(n);
        if (hw == 1) {
          const unsigned i = n - T0;
          const u16* q = i < 8u ? ((i & 1u) ? wu : wg) + og + (size_t)(i >> 1) * (64 * F_) : pd + (size_t)((i - 8u - (i > TW)) & 3u) * (56 * D_);
          if (i == TW) {
            q = wo + (size_t)(768 + (loc / (32 / OMX)) * (256 / OMX) + (lane & (256 / OMX - 1) & 63)) * D_ + oni * 64;
            unsigned long long a0, a1;
            CODE_ADDR("layers_kernel", a0); CODE_ADDR("end_marker_kernel", a1);
            if (loc < 8) { const unsigned p1 = touch_code(a0, a1, 3 + loc, lane); asm volatile("" :: "v"(p1)); }
          }
          const unsigned p0 = touch(q);
          asm volatile("" :: "v"(p0));
        }
        if (n >= G0 && (n - G0) % GS == 0u && (n - G0) / GS < 4u) gather(p, (n - G0) / GS);
        if (n == UAT && hw == UHW) {
          u32x4 a[4][2], c[2];
#pragma unroll
          for (int i = 0; i < 4; ++i)
#pragma unroll
            for (int h = 0; h < 2; ++h) a[i][h] = *reinterpret_cast<const u32x4*>(wo + (size_t)(tni * 64 + tmi * 4 + i) * D_ + h * 512 + lane * 8);
#pragma unroll
          for (int h = 0; h < 2; ++h) c[h] = *reinterpret_cast<const u32x4*>(w_r + h * 512 + lane * 8);
          auto dot = [&](unsigned x, unsigned y, float z) { return __builtin_amdgcn_fdot2_f32_bf16(__builtin_bit_cast(bf16x2, x), __builtin_bit_cast(bf16x2, y), z, false); };
          PIN();
          __builtin_amdgcn_s_setprio(0);
#pragma unroll 1
          for (int i = 0; i < SPIN_MAX; ++i) {
            if (__builtin_amdgcn_readfirstlane(*LDS_AT(const volatile unsigned, lbase + FLG + 16)) >= UARR) break;
            __builtin_amdgcn_s_sleep(4);
          }
          PIN();
          float wm = 0.f;
#pragma unroll
          for (int e = 0; e < 8; ++e) wm = dot(c[e >> 2][e & 3], 0x3f803f80u, wm);
          wm = wave_sum(wm);
          lds_wr(lbase + FLG + 60, __float_as_uint(wm));
          wm *= -1.0f / 1024.0f;
          f32x2 u[2], g[2];
#pragma unroll
          for (int i = 0; i < 4; ++i) {
            float p1 = 0.f, pw = 0.f, p2 = 0.f;
#pragma unroll
            for (int e = 0; e < 8; ++e) {
              const unsigned x = a[i][e >> 2][e & 3];
              p1 = dot(x, 0x3f803f80u, p1); pw = dot(x, c[e >> 2][e & 3], pw); p2 = dot(x, x, p2);
            }
            u[i >> 1][i & 1] = fmaf(p1, wm, pw); g[i >> 1][i & 1] = p2;
          }
          float us = fold4(u[0], u[1]), gs = fold4(g[0], g[1]);
          us = dpp_add<0x121, 0xf>(dpp_add<0x122, 0xf>(us)); gs = dpp_add<0x121, 0xf>(dpp_add<0x122, 0xf>(gs));
          __builtin_amdgcn_raw_buffer_store_b32(__float_as_uint((lane & 1) ? __builtin_amdgcn_sqrtf(gs) * (1.0f / 32.0f) : us),
              __builtin_amdgcn_make_buffer_rsrc(csf + 4096 + tni * 128, 0, 512, 0x00020000), (lane & 14) ? 512 : ((lane & 1) * 64 + tmi * 4 + (lane >> 4)) * 4, 0, 16);
        }
      }
      if (hw == 2 && bid == 0) {
        const __amdgpu_buffer_rsrc_t rsc = __builtin_amdgcn_make_buffer_rsrc(gran, 0, GR_N * 4, 0x00020000);
#pragma unroll 1
        for (int spin = 0; spin < SPIN_MAX; ++spin) {
          bool ok = true;
#pragma unroll
          for (int i = 0; i < 4; ++i) ok &= __builtin_amdgcn_raw_buffer_load_b32(rsc, (GR_ROW + (i * 64 + lane) * 32 + 1) * 4, 0, 16) == tag;
          if (__all(ok)) break;
          __builtin_amdgcn_s_sleep(8);
          asm volatile("" ::: "memory");
        }
        if (lane == 0) st_ag(gran, tag);
      }
      return;
    }
    unsigned* X2 = hand + GRAN_N + Y_N;
    unsigned* XS = X2 + X2_N;
    unsigned* P4 = XS + XS_N;
    unsigned* PA = P4 + P4_N;
    const __amdgpu_buffer_rsrc_t rsh = __builtin_amdgcn_make_buffer_rsrc(hand, 0, HAND_N * 4, 0x00020000);
    auto signal = [&](unsigned ph) {
      asm volatile("" ::: "memory");
      PIN();
      *LDS_AT(volatile unsigned char, lbase + FLG + 4 + hw) = (unsigned char)ph;
    };
    auto wait_all = [&](unsigned ph) {
#pragma unroll 1
      for (int i = 0; i < SPIN_MAX; ++i) {
        if (all_ge(__builtin_amdgcn_readfirstlane(*LDS_AT(const volatile unsigned, lbase + FLG + 4)), ph)) break;
        __builtin_amdgcn_s_sleep(WSLP);
      }
      asm volatile("" ::: "memory");
      PIN();
    };
    gu_setup();
    gate_up(xq, rmd);
    signal(hw == 2 ? 2u : 0x7fu);
    if (wid == 8) STAMP(dk, 2);
    if (hw < 2) {
#pragma unroll 1
      for (int i = 0; i < SPIN_MAX; ++i) {
        if (__builtin_amdgcn_readfirstlane(*LDS_AT(const volatile unsigned, lbase + FLG)) >= FSTART) break;
        __builtin_amdgcn_s_sleep(WSLP);
      }
      asm volatile("" ::: "memory");
      PIN();
      if (wid == 8) STAMP(dk, 10);
      u32x2 w[16];
      const __amdgpu_buffer_rsrc_t rd_ = __builtin_amdgcn_make_buffer_rsrc(const_cast<u16*>(wd), 0, D_ * F_ * 2, 0x00020000);
      auto ldw = [&](int c) __attribute__((always_inline)) {
#pragma unroll
        for (int f = 0; f < 16; ++f) w[f] = __builtin_amdgcn_raw_buffer_load_b64(rd_, lane * 8 + c * 512 + (f & 1) * 2048, (fg * 16 + (f & ~1)) * (D_ * 2) + hw * 1024, 0);
        PIN();
      };
      ldw(0);
      const unsigned pt = __builtin_amdgcn_raw_buffer_load_b32(rd_, (lane >> 2) * (D_ * 2) + (lane & 3) * 128 + 512, fg * 16 * (D_ * 2) + hw * 1024, 0);
      int yo = p1blk ? (GRAN_N + fg * D_ + hw * 512) * 4 + lane * 16 : HAND_N * 4;
      asm volatile("" : "+v"(yo));
      wait_all(2u);
      if (wid == 8) STAMP(dk, 3);
      const s16x4 a = act_diag();
      const u32x4 y0 = down(a, w);
      PIN();
      ldw(1);
      __builtin_amdgcn_raw_buffer_store_b128(y0, rsh, yo, 0, 16);
      PIN();
      __builtin_amdgcn_raw_buffer_store_b128(down(a, w), rsh, yo + 1024, 0, 16);
      asm volatile("" :: "v"(pt));
      if (wid == 8) STAMP(dk, 4);
    }
    PIN();
    [&]() __attribute__((always_inline)) {
    const int lane = fresh_lane();
    const float rmb = __uint_as_float(rmd << 16);
    auto first_words = [&](int src, auto nc) __attribute__((always_inline)) {
#pragma unroll 1
      for (int spin = 0; spin < SPIN_MAX; ++spin) {
        unsigned mx = 0u;
#pragma unroll
        for (int i = 0; i < decltype(nc)::value; ++i) mx = umax(mx, __builtin_amdgcn_raw_buffer_load_b32(rsh, src, i * (56 * D_ * 4), 16));
        if (none(mx == SENT)) break;
        __builtin_amdgcn_s_sleep(PSLP);
        asm volatile("" ::: "memory");
      }
    };
    if (hw == 2) {
      wait_all(2u);
      if (own) {
        const float rden = __uint_as_float(rmd & 0xffff0000u), rrin = frcp(rden);
        const int col = ow * 16 + (lane & 3) * 4 + (lane >> 4);
        const unsigned xo2 = ld_ag(reinterpret_cast<const unsigned*>(A + (size_t)idx * D_ + (col & ~1)));
        __builtin_amdgcn_s_sleep(PRE1);
        first_words((GRAN_N + (lane < 56 ? lane : 0) * D_ + ow * 16) * 4, std::integral_constant<int, 4>{});
        const float y = y_sum(rsh, std::integral_constant<int, 2>{});
        const unsigned rk = best >> 16;
        const float rv = __uint_as_float(((rk & 0x8000u) ? (rk & 0x7fffu) : (~rk & 0xffffu)) << 16);
        const float rs = rbf(frcp(1.0f + __expf(-rv)));
        const float x1v = __uint_as_float((col & 1) ? xo2 & 0xffff0000u : xo2 << 16);
        float h0 = x1v, h1 = x1v;
        ln_apply2(h0, h1, rmb, rden, rrin);
        const float x2v = rbf(x1v + rbf(h0 + rbf(rbf(y) * rs)));
        const auto pr = __builtin_amdgcn_permlane16_swap(__float_as_uint(x2v), __float_as_uint(x2v), false, false);
        __builtin_amdgcn_raw_buffer_store_b32(f2word(__uint_as_float((pr[0] >> 16) | pr[1])), rsh, (lane & 28) ? HAND_N * 4 : (int)(X2 - hand) * 4 + col * 2, 0, 16);
        const float d = x2v - rmb;
        const float sd = wave_sum(d), sq = wave_sum(d * d);
        if (lane < 2) st_ag(XS + ow * 2 + lane, f2word(lane ? sq : sd));
        STAMP(dk, 5);
      }
    }
    {
      const int ks = kcap ^ (tmi & 8), g4 = lane >> 4;
      const int src = (int)(X2 - hand) * 4 + ks * 128 + g4 * 16;
      int dst = (lane & 12) ? HAND_N * 4 : (int)((P4 - hand) + (tni * 16 + ks) * 192 + hw * 64 + (lane & 2) * 16 + (lane & 1) * 8 + (g4 >> 1) * 16 + (g4 & 1) * 4) * 4;
      asm volatile("" : "+v"(dst));
      u32x4 x0, x1;
      if (!(hw == 2 && own)) __builtin_amdgcn_s_sleep(PRE4);
#pragma unroll 1
      for (int spin = 0; spin < SPIN_MAX; ++spin) {
        x0 = __builtin_amdgcn_raw_buffer_load_b128(rsh, src, 0, 16); x1 = __builtin_amdgcn_raw_buffer_load_b128(rsh, src + 64, 0, 16);
        if (none(umax(umax(umax(x0[0], x0[1]), umax(x0[2], x0[3])), umax(umax(x1[0], x1[1]), umax(x1[2], x1[3]))) == SENT)) break;
        __builtin_amdgcn_s_sleep(PSLP);
        asm volatile("" ::: "memory");
      }
      f32x4 d[4];
#pragma unroll
      for (int i = 0; i < 4; ++i) d[i] = MFMA16(sl[2 * i], __builtin_bit_cast(s16x8, x0), (f32x4{0.f, 0.f, 0.f, 0.f}));
#pragma unroll
      for (int i = 0; i < 4; ++i) d[i] = MFMA16(sl[2 * i + 1], __builtin_bit_cast(s16x8, x1), d[i]);
      u32x4 o;
#pragma unroll
      for (int e = 0; e < 4; ++e) o[e] = f2word((lane & 2) ? ((lane & 1) ? d[3][e] : d[2][e]) : ((lane & 1) ? d[1][e] : d[0][e]));
      __builtin_amdgcn_raw_buffer_store_b128(o, rsh, dst, 0, 16);
      PIN();
      if (wid == 8) STAMP(dk, 6);
    }
    if (hw == 2) {
      const int col = (lane & 15) * 4 + (lane >> 4), src = (int)((P4 - hand) + (tni * 16 + (lane >> 4)) * 192 + (lane & 15) * 4) * 4;
      const __amdgpu_buffer_rsrc_t rsq = __builtin_amdgcn_make_buffer_rsrc(hand, 0, fast ? HAND_N * 4 : 0, 0x00020000);
      float cs[3], val[3];
#pragma unroll
      for (int p = 0; p < 3; ++p) cs[p] = __uint_as_float(ld_ag(reinterpret_cast<const unsigned*>(csf) + p * 1024 + n0 + col));
      u32x2 xs;
      __builtin_amdgcn_s_sleep(PRE7);
      first_words((int)((P4 - hand) + (tni * 16 + (lane & 15)) * 192 + ((lane >> 4) % 3) * 64) * 4, std::integral_constant<int, 1>{});
#pragma unroll 1
      for (int spin = 0; spin < SPIN_MAX; ++spin) {
        u32x4 v[3][4];
        xs = __builtin_amdgcn_raw_buffer_load_b64(rsh, (int)((XS - hand) + lane * 2) * 4, 0, 16);
#pragma unroll
        for (int p = 2; p >= 0; --p)
#pragma unroll
          for (int i = 0; i < 4; ++i) v[p][i] = __builtin_amdgcn_raw_buffer_load_b128(p ? rsh : rsq, src + p * 256, i * (4 * 192 * 4), 16);
        auto part = [&](int p) __attribute__((always_inline)) {
          float z[4];
#pragma unroll
          for (int e = 0; e < 4; ++e) {
            z[e] = fadd(fadd(__uint_as_float(v[p][0][e]), __uint_as_float(v[p][1][e])), fadd(__uint_as_float(v[p][2][e]), __uint_as_float(v[p][3][e])));
          }
          val[p] = fold16(f32x2{z[0], z[1]}, f32x2{z[2], z[3]});
        };
        part(2); part(1);
        val[0] = 0.f;
        if (fast) part(0);
        const float chk = fadd(fadd(fadd(val[0], val[1]), fadd(val[2], __uint_as_float(xs[0]))), __uint_as_float(xs[1]));
        if (none(chk != chk)) break;
        unsigned mx = umax(xs[0], xs[1]);
#pragma unroll
        for (int i = 0; i < 12; ++i) mx = umax(umax(umax(mx, v[i >> 2][i & 3][0]), v[i >> 2][i & 3][1]), umax(v[i >> 2][i & 3][2], v[i >> 2][i & 3][3]));
        if (none(mx == SENT)) break;
        __builtin_amdgcn_s_sleep(PSLP);
        asm volatile("" ::: "memory");
      }
      const auto sw = __builtin_amdgcn_permlane32_swap(xs[0], xs[1], false, false);
      float sm = __uint_as_float(sw[0]) + __uint_as_float(sw[1]);
      sm = dpp_add<0x142, 0xa>(dpp_add<0x118, 0xf>(dpp_add<0x114, 0xf>(dpp_add<0x112, 0xf>(dpp_add<0x111, 0xf>(sm)))));
      const float dm = __int_as_float(__builtin_amdgcn_readlane(__float_as_int(sm), 31)) * (0.25f / 1024.0f);
      const float var = fmaxf(__int_as_float(__builtin_amdgcn_readlane(__float_as_int(sm), 63)) * (0.25f / 1024.0f) - dm * dm, 0.f);
      const float mb = rbf(rmb + dm);
      const float rin = frcp(rbf(__builtin_amdgcn_sqrtf(rbf(rbf(var) + 1e-5f))));
      const unsigned kv = pack2((val[1] - mb * cs[1]) * rin, (val[2] - mb * cs[2]) * rin);
      *LDS_AT(u16, lbase + TAB + 128 + kpos(col) * 2) = (u16)kv;
      *LDS_AT(u16, lbase + TAB + 256 + col * 2) = (u16)(kv >> 16);
      if (fast) *LDS_AT(u16, lbase + TAB + kpos(col) * 2) = f2bf((val[0] - mb * cs[0]) * rin * QSCALE);
      signal(7u);
      STAMP(dk, 7);
      if (own) {
#pragma unroll
        for (int i = 0; i < 14; ++i)
          __builtin_amdgcn_raw_buffer_store_b128(u32x4{SENT, SENT, SENT, SENT}, rsh, (GRAN_N + (i * 16 + (lane >> 2)) * D_ + ow * 16 + (lane & 3) * 4) * 4, 0, 16);
      }
      return;
    }
    if (!fast) return;
    const int head = tni * 2 + hw;
    const unsigned tb = lbase + TAB + hw * 64;
    {
      const __amdgpu_buffer_rsrc_t rs = __builtin_amdgcn_make_buffer_rsrc(KV, 0, 2 * S_ * D_ * 2, 0x00020000);
      const int src = (head * 64 + tmi * 4) * 2048 + lane * 16;
      auto ld = [&](int i, unsigned off) { return __builtin_bit_cast(s16x8, __builtin_amdgcn_raw_buffer_load_b128(rs, src + (i & 3) * 1024, (i >> 3) * 65536 + ((i >> 2) & 1) * 4096 + off, 16)); };
#pragma unroll 1
      for (int i = 0; i < SPIN_MAX; ++i)
        if (__builtin_amdgcn_readfirstlane(ld_ag(kvflag + (tmi % RK) * 32 + tmi + 8)) == tag) break;
      asm volatile("" ::: "memory");
      s16x8 f[16];
#pragma unroll
      for (int i = 0; i < 16; ++i) f[i] = ld(i, 0u);
      PIN();
      const bool on = ((lane ^ (lane >> 4)) & 1) == 0;
      const unsigned pl = lbase + RED + hw * 1024;
      unsigned pw = pl + (lane >> 4) * 64 + (lane & 15) * 2, pr = pl + (on ? (lane >> 5) * 16 : 512), qr = on ? tb + (lane >> 5) * 16 : pl + 512;
      const int gone = (((idx >> 7) & 7) == tmi) ? ((idx >> 10) * 2 + (kpos(idx & 31) >> 4)) * 64 + ((idx >> 5) & 3) * 16 + (kpos(idx & 31) & 15) : -1;
      asm volatile("" : "+v"(pw), "+v"(pr), "+v"(qr));
      *LDS_AT(u32x2, pl + 512 + lane * 8) = u32x2{0u, 0u};
      wait_all(7u);
      u32x4 q[2];
#pragma unroll
      for (int sx = 0; sx < 2; ++sx) {
        q[sx] = *LDS_AT(const u32x4, qr + sx * 32);
      }
      float sc[4];
#pragma unroll
      for (int i = 0; i < 8; ++i) {
        f32x4 d = MFMA16(__builtin_bit_cast(s16x8, q[0]), f[2 * i], (f32x4{0.f, 0.f, 0.f, 0.f}));
        d = MFMA16(__builtin_bit_cast(s16x8, q[1]), f[2 * i + 1], d);
        PIN();
        f[2 * i] = ld(2 * i, VOFF); f[2 * i + 1] = ld(2 * i + 1, VOFF);
        PIN();
#pragma unroll
        for (int r = 0; r < 2; ++r) sc[(i >> 2) * 2 + r] = (i & 3) == 0 || (lane >> 4) == (i & 3) ? d[r] : sc[(i >> 2) * 2 + r];
      }
      if (gone >= 0) {
#pragma unroll
        for (int r = 0; r < 4; ++r) sc[r] = r * 64 + lane == gone ? -INFINITY : sc[r];
      }
      const float mx = wave_fmax(fmaxf(fmaxf(sc[0], sc[1]), fmaxf(sc[2], sc[3])));
      const unsigned p0 = pack2(__builtin_amdgcn_exp2f(sc[0] - mx), __builtin_amdgcn_exp2f(sc[1] - mx)), p1 = pack2(__builtin_amdgcn_exp2f(sc[2] - mx), __builtin_amdgcn_exp2f(sc[3] - mx));
      *LDS_AT(u16, pw) = (u16)p0; *LDS_AT(u16, pw + 32) = (u16)(p0 >> 16);
      *LDS_AT(u16, pw + 256) = (u16)p1; *LDS_AT(u16, pw + 288) = (u16)(p1 >> 16);
      asm volatile("" ::: "memory");
      const float ls = wave_sum((__uint_as_float(p0 << 16) + __uint_as_float(p0 & 0xffff0000u)) + (__uint_as_float(p1 << 16) + __uint_as_float(p1 & 0xffff0000u)));
      f32x4 o = {0.f, 0.f, 0.f, 0.f};
#pragma unroll
      for (int i = 0; i < 16; ++i) o = MFMA16(*LDS_AT(const s16x8, pr + i * 32), f[i], o);
      int pv = __float_as_int((lane & 16) ? o[1] : o[0]);
      asm volatile("s_nop 0\n\tv_writelane_b32 %0, %1, 32\n\tv_writelane_b32 %0, %2, 33" : "+v"(pv) : "s"(ls), "s"(mx));
      __builtin_amdgcn_raw_buffer_store_b32(f2word(__int_as_float(pv)), rsh, lane < 34 ? (int)((PA - hand) + (head * 8 + tmi) * 64 + lane) * 4 : HAND_N * 4, 0, 16);
    }
    if (wid == 8) STAMP(dk, 8);
    if (tmi != ((idx >> 7) & 7)) return;
    {
      const int src = (int)((PA - hand) + head * 512 + (lane < 34 ? lane : 0)) * 4;
      const unsigned ta = tb + (lane & 31) * 2;
      const float so = wave_sum(lane < 32 ? bf2f(*LDS_AT(const u16, ta)) * bf2f(*LDS_AT(const u16, ta + 128)) : 0.f);
      const float vo = lane < 32 ? bf2f(*LDS_AT(const u16, ta + 256)) : 1.0f;
      unsigned v[8];
      __builtin_amdgcn_s_sleep(PRE5);
#pragma unroll 1
      for (int spin = 0; spin < SPIN_MAX; ++spin) {
        unsigned mx = 0u;
#pragma unroll
        for (int i = 0; i < 8; ++i) v[i] = __builtin_amdgcn_raw_buffer_load_b32(rsh, src + i * 256, 0, 16);
#pragma unroll
        for (int i = 0; i < 8; ++i) mx = umax(mx, v[i]);
        if (none(mx == SENT)) break;
        __builtin_amdgcn_s_sleep(PSLP);
        asm volatile("" ::: "memory");
      }
      float m8[8], mx = so;
#pragma unroll
      for (int i = 0; i < 8; ++i) { m8[i] = __int_as_float(__builtin_amdgcn_readlane((int)v[i], 33)); mx = fmaxf(mx, m8[i]); }
      float acc = __builtin_amdgcn_exp2f(so - mx) * vo;
#pragma unroll
      for (int i = 0; i < 8; ++i) acc += __builtin_amdgcn_exp2f(m8[i] - mx) * __uint_as_float(v[i]);
      const float l = __int_as_float(__builtin_amdgcn_readlane(__float_as_int(acc), 32));
      const float ov = acc / l;
      const float o1 = __int_as_float(__builtin_amdgcn_update_dpp(0, __float_as_int(ov), 0x55, 0xf, 0xf, false));
      const float o2 = __int_as_float(__builtin_amdgcn_update_dpp(0, __float_as_int(ov), 0xaa, 0xf, 0xf, false));
      const float o3 = __int_as_float(__builtin_amdgcn_update_dpp(0, __float_as_int(ov), 0xff, 0xf, 0xf, false));
      const __amdgpu_buffer_rsrc_t rso = __builtin_amdgcn_make_buffer_rsrc(Of, 0, S_ * D_ * 2, 0x00020000);
      if (lane < 32 && (lane & 3) == 0)
        __builtin_amdgcn_raw_buffer_store_b64(u32x2{pack2(ov, o1), pack2(o2, o3)}, rso,
            (((idx >> 5) * 32 + head) * 1024 + (lane >> 4) * 512 + (((lane >> 2) & 1) * 32 + (idx & 31)) * 8 + ((lane >> 3) & 1) * 4) * 2, 0, 16);
      asm volatile("s_waitcnt vmcnt(0)" ::: "memory");
      if (lane < RO) st_ag(gran + GR_O + ((16 + (idx >> 7)) * RO + lane) * 64 + 16 + head, tag);
      STAMP(dk, 9);
    }
    }();
    }();
    __builtin_amdgcn_s_setprio(0);
    asm volatile("s_waitcnt vmcnt(0) lgkmcnt(0)" ::: "memory");
    PIN();
    ARGS_HERE;
    u32x2 w[16];
    auto ld_wd = [&]() __attribute__((always_inline)) {
      const __amdgpu_buffer_rsrc_t rd_ = __builtin_amdgcn_make_buffer_rsrc(const_cast<u16*>(wd), 0, D_ * F_ * 2, 0x00020000);
#pragma unroll
      for (int f = 0; f < 16; ++f) w[f] = __builtin_amdgcn_raw_buffer_load_b64(rd_, t * 8 + (f & 1) * 2048, (fg * 16 + (f & ~1)) * (D_ * 2), 0);
      PIN();
    };
    float fnum = 0.f, fvar = 0.f;
    u32x2 wq = {0u, 0u};
    const int rrow = omi * 128 + (oni >> 3) * 64 + (hw & 1) * 32;
    if (LAY2) {
      ld_wd();
      wq = *reinterpret_cast<const u32x2*>(w_r + hw * 256 + (lane & 3) * 64 + (lane >> 2) * 4);
      if (!p1blk) {
        const int fr = rrow + (lane & 7) * 4 + (lane >> 4);
        const double s1 = ld_ag(stats + fr), s2 = ld_ag(stats + S_ + fr), sw = ld_ag(stats + 2 * S_ + fr);
        lds_wr(lbase + SPM + 132, __float_as_uint(wsum));
        float mean, var, mb, den;
        ln_from_sums(s1, s2, mean, var, mb, den);
        const float g1 = 1.0f + frcp(den);
        fnum = fr == idx ? __uint_as_float(0x7fc00000u) : fmaf(-mean, wsum, (float)sw) * g1;
        fvar = var * g1 * g1;
      }
      asm volatile("" : "+v"(fnum), "+v"(fvar));
      PIN();
    }
    if (hw == 3) {
      const int nfl = (LAY2 && (idx >> 7) == omi) ? 48 : 16;
      auto pfl = (__attribute__((address_space(1))) const unsigned*)(gran + GR_O + (((LAY2 ? 16 : 0) + omi) * RO + oni % RO) * 64 + (lane < nfl ? lane : 0));
      constexpr unsigned vs = 8u * D_ * 2u - 1024u;
      unsigned vw0 = (unsigned)((lane >> 3) * D_ + oni * 64 + ((lane & 7) ^ ((lane >> 3) & 6)) * 8) * 2u, vw1 = vw0 + vs, vw2 = vw0 + 2 * vs, vw3 = vw0 + 3 * vs;
      asm volatile("" : "+v"(pfl), "+v"(vw0), "+v"(vw1), "+v"(vw2), "+v"(vw3));
      auto wait_arr = [&](unsigned n) {
#pragma unroll 1
        for (int i = 0; i < 4 * SPIN_MAX; ++i) {
          if (__builtin_amdgcn_readfirstlane(*LDS_AT(const volatile unsigned, lbase + FLG + 16)) >= n) break;
          __builtin_amdgcn_s_sleep(1);
        }
        asm volatile("" ::: "memory");
        PIN();
      };
      wait_arr(12u);
      unsigned fl;
      asm volatile("global_load_dword %0, %1, off sc1" : "=v"(fl) : "v"(pfl) : "memory");
#pragma unroll 1
      for (int i = 0; i < 4 * SPIN_MAX; ++i) {
        if (__builtin_amdgcn_readfirstlane(*LDS_AT(const volatile unsigned, lbase + FLG + 24)) >= 8u) break;
        __builtin_amdgcn_s_sleep(1);
      }
      asm volatile("" ::: "memory");
      PIN();
#pragma unroll
      for (int j = 24; j < 32; ++j)
        glds16x4<false>(reinterpret_cast<const char*>(wo) + j * (32u * D_ * 2u), vw0, vw1, vw2, vw3, lbase + j * 4096);
      unsigned keep;
      asm volatile("s_mov_b32 %0, m0\n\ts_mov_b32 m0, %3\n\ts_nop 2\n\tglobal_load_lds_dword %1, %2 sc1\n\ts_mov_b32 m0, %0"
                   : "=&s"(keep) : "v"(0u), "s"(reinterpret_cast<const char*>(gran + GR_N + bid * 64 + 39)), "s"(lbase + FLG - 252) : "memory");
      asm volatile("s_waitcnt vmcnt(33)" : "+v"(fl) :: "memory");
      if (__builtin_amdgcn_ballot_w64(fl != tag)) {
        __builtin_amdgcn_s_sleep(CSL1);
#pragma unroll 1
        for (int spin = 0; spin < SPIN_MAX; ++spin) {
          if (!__builtin_amdgcn_ballot_w64(__hip_atomic_load(pfl, __ATOMIC_RELAXED, __HIP_MEMORY_SCOPE_AGENT) != tag)) break;
          __builtin_amdgcn_s_sleep(CSLP);
        }
      }
      asm volatile("" ::: "memory");
      PIN();
    }
    u32x4 pv[4];
    asm volatile("" : "=v"(pv[0]), "=v"(pv[1]), "=v"(pv[2]), "=v"(pv[3]));
    if (LAY2 && !p1blk && hw < 2) {
      const __amdgpu_buffer_rsrc_t rsp = __builtin_amdgcn_make_buffer_rsrc(hand + 2 * HAND_N + PP_O, 0, H_ * S_ * 4, 0x00020000);
      const int src = ((lane >> 3) * S_ + rrow + (lane & 7) * 4) * 4;
#pragma unroll 1
      for (int i = 0; i < 4 * SPIN_MAX; ++i) {
        if (__builtin_amdgcn_readfirstlane(*LDS_AT(const volatile unsigned, lbase + FLG + 24)) >= 8u) break;
        __builtin_amdgcn_s_sleep(2);
      }
#pragma unroll 1
      for (int spin = 0; spin < SPIN_MAX; ++spin) {
        asm volatile("" ::: "memory");
        unsigned mx = 0u;
#pragma unroll
        for (int i = 0; i < 4; ++i) pv[i] = __builtin_amdgcn_raw_buffer_load_b128(rsp, src, i * (8 * S_ * 4), 16);
#pragma unroll
        for (int i = 0; i < 4; ++i) mx = umax(umax(umax(mx, pv[i][0]), pv[i][1]), umax(pv[i][2], pv[i][3]));
        if (none(mx == SENT)) break;
        __builtin_amdgcn_s_sleep(PPSLP);
      }
    }
    asm volatile("s_waitcnt lgkmcnt(0)" ::: "memory");
    __builtin_amdgcn_s_barrier();
    if (!LAY2) {
      PIN();
      asm volatile(AGAIN "\ngate_up_code:\n\t"
                 "v_lshlrev_b32 v84, 16, v100\n\tv_and_b32 v85, 0xffff0000, v100\n\tv_lshlrev_b32 v86, 16, v101\n\tv_and_b32 v87, 0xffff0000, v101\n\t"
                 "v_subrev_f32 v84, s84, v84\n\tv_subrev_f32 v85, s84, v85\n\tv_subrev_f32 v86, s84, v86\n\tv_subrev_f32 v87, s84, v87\n\t"
                 "v_cvt_pk_bf16_f32 v85, v84, v85\n\tv_cvt_pk_bf16_f32 v87, v86, v87\n\ts_mov_b32 s88, 0x01000100\n\ts_mov_b32 s89, 0x03020302\n\t"
                 "v_lshlrev_b32 v84, 16, v85\n\tv_and_b32 v85, 0xffff0000, v85\n\tv_lshlrev_b32 v86, 16, v87\n\tv_and_b32 v87, 0xffff0000, v87\n\t"
                 "v_mul_f32 v88, s86, v84\n\tv_mul_f32 v89, s86, v85\n\tv_mul_f32 v90, s86, v86\n\tv_mul_f32 v91, s86, v87\n\tv_fma_f32 v84, -v88, s85, v84\n\t"
                 "v_fma_f32 v85, -v89, s85, v85\n\tv_fma_f32 v86, -v90, s85, v86\n\tv_fma_f32 v87, -v91, s85, v87\n\tv_fmac_f32 v88, s86, v84\n\t"
                 "v_fmac_f32 v89, s86, v85\n\tv_fmac_f32 v90, s86, v86\n\tv_fmac_f32 v91, s86, v87\n\tv_cvt_pk_bf16_f32 v88, v88, v89\n\t"
                 "v_cvt_pk_bf16_f32 v90, v90, v91\n\tv_perm_b32 v92, v88, v88, s88\n\tv_perm_b32 v93, v88, v88, s89\n\tv_perm_b32 v94, v90, v90, s88\n\t"
                 "v_perm_b32 v95, v90, v90, s89\n\ts_nop 0\n\tv_and_b32_dpp v96, v92, v102 quad_perm:[0,0,0,0] row_mask:0xf bank_mask:0xf\n\t"
                 "v_and_b32_dpp v97, v92, v103 quad_perm:[0,0,0,0] row_mask:0xf bank_mask:0xf\n\t"
                 "v_and_b32_dpp v100, v93, v102 quad_perm:[0,0,0,0] row_mask:0xf bank_mask:0xf\n\t"
                 "v_and_b32_dpp v101, v93, v103 quad_perm:[0,0,0,0] row_mask:0xf bank_mask:0xf\n\tv_mfma_f32_4x4x4_16b_bf16 v[84:87], v[96:97], v[104:105], 0\n\t"
                 "v_mfma_f32_4x4x4_16b_bf16 v[88:91], v[96:97], v[106:107], 0\n\tv_and_b32_dpp v96, v94, v102 quad_perm:[0,0,0,0] row_mask:0xf bank_mask:0xf\n\t"
                 "v_and_b32_dpp v97, v94, v103 quad_perm:[0,0,0,0] row_mask:0xf bank_mask:0xf\n\t"
                 "v_mfma_f32_4x4x4_16b_bf16 v[84:87], v[100:101], v[108:109], v[84:87]\n\tv_mfma_f32_4x4x4_16b_bf16 v[88:91], v[100:101], v[110:111], v[88:91]\n\t"
                 "v_and_b32_dpp v100, v95, v102 quad_perm:[0,0,0,0] row_mask:0xf bank_mask:0xf\n\t"
                 "v_and_b32_dpp v101, v95, v103 quad_perm:[0,0,0,0] row_mask:0xf bank_mask:0xf\n\tv_mfma_f32_4x4x4_16b_bf16 v[84:87], v[96:97], v[112:113], v[84:87]\n\t"
                 "v_mfma_f32_4x4x4_16b_bf16 v[88:91], v[96:97], v[114:115], v[88:91]\n\tv_and_b32_dpp v96, v92, v102 quad_perm:[1,1,1,1] row_mask:0xf bank_mask:0xf\n\t"
                 "v_and_b32_dpp v97, v92, v103 quad_perm:[1,1,1,1] row_mask:0xf bank_mask:0xf\n\t"
                 "v_mfma_f32_4x4x4_16b_bf16 v[84:87], v[100:101], v[116:117], v[84:87]\n\tv_mfma_f32_4x4x4_16b_bf16 v[88:91], v[100:101], v[118:119], v[88:91]\n\t"
                 "v_and_b32_dpp v100, v93, v102 quad_perm:[1,1,1,1] row_mask:0xf bank_mask:0xf\n\t"
                 "v_and_b32_dpp v101, v93, v103 quad_perm:[1,1,1,1] row_mask:0xf bank_mask:0xf\n\tv_mfma_f32_4x4x4_16b_bf16 v[84:87], v[96:97], v[120:121], v[84:87]\n\t"
                 "v_mfma_f32_4x4x4_16b_bf16 v[88:91], v[96:97], v[122:123], v[88:91]\n\tv_and_b32_dpp v96, v94, v102 quad_perm:[1,1,1,1] row_mask:0xf bank_mask:0xf\n\t"
                 "v_and_b32_dpp v97, v94, v103 quad_perm:[1,1,1,1] row_mask:0xf bank_mask:0xf\n\t"
                 "v_mfma_f32_4x4x4_16b_bf16 v[84:87], v[100:101], v[124:125], v[84:87]\n\tv_mfma_f32_4x4x4_16b_bf16 v[88:91], v[100:101], v[126:127], v[88:91]\n\t"
                 "v_and_b32_dpp v100, v95, v102 quad_perm:[1,1,1,1] row_mask:0xf bank_mask:0xf\n\t"
                 "v_and_b32_dpp v101, v95, v103 quad_perm:[1,1,1,1] row_mask:0xf bank_mask:0xf\n\tv_mfma_f32_4x4x4_16b_bf16 v[84:87], v[96:97], v[128:129], v[84:87]\n\t"
                 "v_mfma_f32_4x4x4_16b_bf16 v[88:91], v[96:97], v[130:131], v[88:91]\n\tv_and_b32_dpp v96, v92, v102 quad_perm:[2,2,2,2] row_mask:0xf bank_mask:0xf\n\t"
                 "v_and_b32_dpp v97, v92, v103 quad_perm:[2,2,2,2] row_mask:0xf bank_mask:0xf\n\t"
                 "v_mfma_f32_4x4x4_16b_bf16 v[84:87], v[100:101], v[132:133], v[84:87]\n\tv_mfma_f32_4x4x4_16b_bf16 v[88:91], v[100:101], v[134:135], v[88:91]\n\t"
                 "v_and_b32_dpp v100, v93, v102 quad_perm:[2,2,2,2] row_mask:0xf bank_mask:0xf\n\t"
                 "v_and_b32_dpp v101, v93, v103 quad_perm:[2,2,2,2] row_mask:0xf bank_mask:0xf\n\tv_mfma_f32_4x4x4_16b_bf16 v[84:87], v[96:97], v[136:137], v[84:87]\n\t"
                 "v_mfma_f32_4x4x4_16b_bf16 v[88:91], v[96:97], v[138:139], v[88:91]\n\tv_and_b32_dpp v96, v94, v102 quad_perm:[2,2,2,2] row_mask:0xf bank_mask:0xf\n\t"
                 "v_and_b32_dpp v97, v94, v103 quad_perm:[2,2,2,2] row_mask:0xf bank_mask:0xf\n\t"
                 "v_mfma_f32_4x4x4_16b_bf16 v[84:87], v[100:101], v[140:141], v[84:87]\n\tv_mfma_f32_4x4x4_16b_bf16 v[88:91], v[100:101], v[142:143], v[88:91]\n\t"
                 "v_and_b32_dpp v100, v95, v102 quad_perm:[2,2,2,2] row_mask:0xf bank_mask:0xf\n\t"
                 "v_and_b32_dpp v101, v95, v103 quad_perm:[2,2,2,2] row_mask:0xf bank_mask:0xf\n\tv_mfma_f32_4x4x4_16b_bf16 v[84:87], v[96:97], v[144:145], v[84:87]\n\t"
                 "v_mfma_f32_4x4x4_16b_bf16 v[88:91], v[96:97], v[146:147], v[88:91]\n\tv_and_b32_dpp v96, v92, v102 quad_perm:[3,3,3,3] row_mask:0xf bank_mask:0xf\n\t"
                 "v_and_b32_dpp v97, v92, v103 quad_perm:[3,3,3,3] row_mask:0xf bank_mask:0xf\n\t"
                 "v_mfma_f32_4x4x4_16b_bf16 v[84:87], v[100:101], v[148:149], v[84:87]\n\tv_mfma_f32_4x4x4_16b_bf16 v[88:91], v[100:101], v[150:151], v[88:91]\n\t"
                 "v_and_b32_dpp v100, v93, v102 quad_perm:[3,3,3,3] row_mask:0xf bank_mask:0xf\n\t"
                 "v_and_b32_dpp v101, v93, v103 quad_perm:[3,3,3,3] row_mask:0xf bank_mask:0xf\n\tv_mfma_f32_4x4x4_16b_bf16 v[84:87], v[96:97], v[152:153], v[84:87]\n\t"
                 "v_mfma_f32_4x4x4_16b_bf16 v[88:91], v[96:97], v[154:155], v[88:91]\n\tv_and_b32_dpp v96, v94, v102 quad_perm:[3,3,3,3] row_mask:0xf bank_mask:0xf\n\t"
                 "v_and_b32_dpp v97, v94, v103 quad_perm:[3,3,3,3] row_mask:0xf bank_mask:0xf\n\t"
                 "v_mfma_f32_4x4x4_16b_bf16 v[84:87], v[100:101], v[156:157], v[84:87]\n\tv_mfma_f32_4x4x4_16b_bf16 v[88:91], v[100:101], v[158:159], v[88:91]\n\t"
                 "v_and_b32_dpp v100, v95, v102 quad_perm:[3,3,3,3] row_mask:0xf bank_mask:0xf\n\t"
                 "v_and_b32_dpp v101, v95, v103 quad_perm:[3,3,3,3] row_mask:0xf bank_mask:0xf\n\tv_mfma_f32_4x4x4_16b_bf16 v[84:87], v[96:97], v[160:161], v[84:87]\n\t"
                 "v_mfma_f32_4x4x4_16b_bf16 v[88:91], v[96:97], v[162:163], v[88:91]\n\ts_nop 1\n\t"
                 "v_mfma_f32_4x4x4_16b_bf16 v[84:87], v[100:101], v[164:165], v[84:87]\n\tv_mfma_f32_4x4x4_16b_bf16 v[88:91], v[100:101], v[166:167], v[88:91]\n\t"
                 "s_nop 4\n\tv_permlane32_swap_b32 v84, v88\n\tv_permlane32_swap_b32 v85, v89\n\tv_permlane32_swap_b32 v86, v90\n\tv_permlane32_swap_b32 v87, v91\n\t"
                 "v_add_f32 v84, v84, v88\n\tv_add_f32 v85, v85, v89\n\tv_add_f32 v86, v86, v90\n\tv_add_f32 v87, v87, v91\n\ts_nop 0\n\t"
                 "v_permlane16_swap_b32 v84, v85\n\tv_permlane16_swap_b32 v86, v87\n\ts_nop 0\n\tv_add_f32 v84, v84, v85\n\tv_add_f32 v86, v86, v87\n\ts_nop 1\n\t"
                 "v_add_f32_dpp v84, v84, v84 row_ror:8 row_mask:0xf bank_mask:0xf\n\tv_add_f32_dpp v86, v86, v86 row_ror:8 row_mask:0xf bank_mask:0xf\n\ts_nop 0\n\t"
                 "v_add_f32_dpp v84, v84, v84 row_ror:4 row_mask:0xf bank_mask:0xf\n\tv_add_f32_dpp v86, v86, v86 row_ror:4 row_mask:0xf bank_mask:0xf\n\t"
                 "ds_write2_b32 v99, v84, v86 offset1:8\n\ts_setpc_b64 s[92:93]\n\t"
                   :: "{s[0:1]}"(ap + 1), "{s2}"(bid), "{v0}"(wid * 64), KEPT() : "s90", "s91", "scc", "memory");
      __builtin_unreachable();
    }
    {
    constexpr int dk = 8;
    constexpr int BEST = FLG + 40, MEET = FLG + 48;
    __builtin_amdgcn_s_setprio(HPRIO);
    if (hw == 0) STAMP(dk, 0);
    const unsigned tb1 = (unsigned)__builtin_amdgcn_s_memrealtime();
    unsigned* hand2 = hand + HAND_N;
    const __amdgpu_buffer_rsrc_t rsh = __builtin_amdgcn_make_buffer_rsrc(hand2, 0, (HAND_N + PS_N) * 4, 0x00020000);
    const __amdgpu_buffer_rsrc_t rsx = __builtin_amdgcn_make_buffer_rsrc(xout, 0, 3 * D_ * 2, 0x00020000);
    constexpr int OOR = (HAND_N + PS_N) * 4;
    if (!p1blk && hw < 2) {
      f32x2 a[2], c[2];
#pragma unroll
      for (int e = 0; e < 4; ++e) {
        a[e >> 1][e & 1] = (__uint_as_float(pv[0][e] << 16) + __uint_as_float(pv[1][e] << 16)) + (__uint_as_float(pv[2][e] << 16) + __uint_as_float(pv[3][e] << 16));
        c[e >> 1][e & 1] = (__uint_as_float(pv[0][e] & 0xffff0000u) + __uint_as_float(pv[1][e] & 0xffff0000u)) + (__uint_as_float(pv[2][e] & 0xffff0000u) + __uint_as_float(pv[3][e] & 0xffff0000u));
      }
      const float r = (fnum + fold8(a[0], a[1])) * __builtin_amdgcn_rsqf(fvar + fold8(c[0], c[1]));
      unsigned key = (__float_as_uint(r) ^ ((unsigned)(__float_as_int(r) >> 31) | 0x80000000u)) & 0xfffff800u;
      if (r != r) key = 0u;
      key = wave_umax(key | (unsigned)(2047 - (rrow + (lane & 7) * 4 + (lane >> 4))));
      __builtin_amdgcn_raw_buffer_store_b64(u32x2{key, tb1 & 0x7fffffffu}, rsh, lane < NREP ? (HAND_N + PK_O + lane * 128 + (rrow >> 5) * 2) * 4 : OOR, 0, 16);
      if (hw == 1) STAMP(dk, 2);
    }
    gu_setup();
    bool got = true;
    unsigned fra = lbase;
    int fro = S_ * D_ * 2;
    auto get_fc = [&]() __attribute__((always_inline)) {
      const u32x2 fw = __builtin_amdgcn_raw_buffer_load_b64(rsh, (HAND_N + PK_O + (bid % NREP) * 128 + lane * 2) * 4, 0, 16);
      got = none(umax(fw[0], fw[1]) == SENT);
      if (got) {
        const unsigned c1 = wave_umax(fw[0]), c2 = wave_umax(fw[0] == c1 ? 0u : fw[0]);
#define BADFC 0
        const unsigned r1 = (2047u - (c1 & 0x7ffu)) ^ BADFC, r2 = 2047u - (c2 & 0x7ffu);
        const unsigned r = (lane & 8) ? r2 : r1;
        fra = lbase + QOFF + (r & 127u) * 128 + (((r ^ lane) & 7u) << 4);
        fro = (r >> 7) == (unsigned)omi && lane < 16 ? ((lane >> 3) * D_ + oni * 64 + (lane & 7) * 8) * 2 : S_ * D_ * 2;
        unsigned dt = (unsigned)((int)((fw[1] - tb1) << 1) >> 1) + 512u;
        dt = dt < 1024u ? dt : 512u;
        *LDS_AT(volatile u32x2, lbase + SPM + 144) = u32x2{r1 | (r2 << 16), (unsigned)__builtin_amdgcn_readlane(dt, r1 >> 5) | (wave_umax(dt) << 16)};
      }
    };
    if (hw == 0) {
      __builtin_amdgcn_s_sleep(FCPRE);
#pragma unroll 1
      for (int i = 0; i < SPIN_MAX; ++i) {
        if (__builtin_amdgcn_readfirstlane(*LDS_AT(const volatile unsigned, lbase + FLG + 28)) - best >= 4u) break;
        __builtin_amdgcn_s_sleep(FCSLP);
      }
      asm volatile("" ::: "memory");
      get_fc();
    } else __builtin_amdgcn_s_sleep(TPRE - 10);
    PIN();
    tile_at(2u);
    const unsigned tb2 = (unsigned)__builtin_amdgcn_s_memrealtime() - 512u;
    LOGDEF;
    LOG();
#pragma unroll 1
    for (int spin = 0; spin < SPIN_MAX && !got; ++spin) { get_fc(); asm volatile("" ::: "memory"); }
    if (hw == 0) __builtin_amdgcn_raw_buffer_store_b128(lds_rd4(fra), rsx, fro, 0, XAUX);
    PIN();
    u32x2 fm = {SENT, 0u};
#pragma unroll 1
    for (int i = 0; i < 4 * SPIN_MAX && fm[0] == SENT; ++i) {
      const u32x2 q = *LDS_AT(const volatile u32x2, lbase + SPM + 144);
      fm = u32x2{(unsigned)__builtin_amdgcn_readfirstlane(q[0]), (unsigned)__builtin_amdgcn_readfirstlane(q[1])};
    }
    const int fr1 = fm[0] & 0x7ffu, fr2 = (fm[0] >> 16) & 0x7ffu;
    const unsigned t1 = tb2 + (fm[1] & 1023u), tmx = tb2 + ((fm[1] >> 16) & 1023u);
    auto until = [&](unsigned te) { while ((int)((unsigned)__builtin_amdgcn_s_memrealtime() - te) < 0) __builtin_amdgcn_s_sleep(1); asm volatile("" ::: "memory"); };
    lds_wr(lbase + SPM + 136, tmx);
    PIN();
    if (hw == 0) STAMP(dk, 3);
    const int xoff = (hw * 256 + (lane & 3) * 64 + (lane >> 2) * 4) * 2;
    const int oc = (omi * 2 + (oni >> 3)) * 32, col = oc + (lane & 7) * 4 + (lane >> 4);
    const int xoo = !p1blk && hw == 3 ? (col & ~1) * 2 : S_ * D_ * 2;
    auto get_row = [&](int r, int sl, u32x2& xr, unsigned& xo) __attribute__((always_inline)) {
      if ((r >> 7) == omi) {
#pragma unroll 1
        for (int i = 0; i < SPIN_MAX; ++i) {
          if (__builtin_amdgcn_readfirstlane(*LDS_AT(const volatile unsigned, lbase + FLG + 24)) >= 16u) break;
          __builtin_amdgcn_s_sleep(1);
        }
        asm volatile("" ::: "memory");
      }
#pragma unroll 1
      for (int spin = 0; spin < SPIN_MAX; ++spin) {
        LOG();
        xr = __builtin_amdgcn_raw_buffer_load_b64(rsx, xoff, sl * (D_ * 2), 16);
        xo = __builtin_amdgcn_raw_buffer_load_b32(rsx, xoo, sl * (D_ * 2), 16);
        LOG();
        if (none(umax(umax(xr[0], xr[1]), xo) == SENT)) break;
        __builtin_amdgcn_s_sleep(XRSLP);
        asm volatile("" ::: "memory");
      }
      PIN();
    };
    unsigned rk1;
    auto row_md = [&](const u32x2& xr, int k) __attribute__((always_inline)) {
      auto dot = [&](unsigned x, unsigned y, float z) { return __builtin_amdgcn_fdot2_f32_bf16(__builtin_bit_cast(bf16x2, x), __builtin_bit_cast(bf16x2, y), z, false); };
      const float p1 = dot(xr[1], 0x3f803f80u, dot(xr[0], 0x3f803f80u, 0.f)), p2 = dot(xr[1], xr[1], dot(xr[0], xr[0], 0.f)), p3 = dot(xr[1], wq[1], dot(xr[0], wq[0], 0.f));
      const auto q = __builtin_amdgcn_permlane32_swap(__float_as_uint(p1), __float_as_uint(p2), false, false);
      const auto q3 = __builtin_amdgcn_permlane32_swap(__float_as_uint(p3), __float_as_uint(p3), false, false);
      const auto q2 = __builtin_amdgcn_permlane16_swap(__float_as_uint(__uint_as_float(q[0]) + __uint_as_float(q[1])), __float_as_uint(__uint_as_float(q3[0]) + __uint_as_float(q3[1])), false, false);
      float sv = __uint_as_float(q2[0]) + __uint_as_float(q2[1]);
      sv = dpp_add<0x121, 0xf>(dpp_add<0x122, 0xf>(dpp_add<0x124, 0xf>(dpp_add<0x128, 0xf>(sv))));
      lds_wr(lbase + SPM + k * 64 + (lane >> 4) * 16 + hw * 4, f2word(sv));
      u32x4 a, c, e;
#pragma unroll 1
      for (int i = 0; i < SPIN_MAX; ++i) {
        a = lds_rd4(lbase + SPM + k * 64); c = lds_rd4(lbase + SPM + k * 64 + 32); e = lds_rd4(lbase + SPM + k * 64 + 16);
        if (__builtin_amdgcn_readfirstlane(umax(umax(a[0], a[1]), umax(a[2], a[3]))) != SENT) break;
      }
      auto sum4 = [&](const u32x4& v) { return (__uint_as_float(v[0]) + __uint_as_float(v[1])) + (__uint_as_float(v[2]) + __uint_as_float(v[3])); };
      const float mean = sum4(a) * (1.0f / 1024.0f), msq = sum4(c) * (1.0f / 1024.0f);
      const float den = rbf(__builtin_amdgcn_sqrtf(rbf(rbf(fmaxf(fmaf(-mean, mean, msq), 0.f)) + 1e-5f)));
      if (k == 0) { const float r = fmaf(-rbf(mean), wsum, sum4(e)) * frcp(den); rk1 = __builtin_amdgcn_readfirstlane(pack2(r, r)) >> 16; }
      return (unsigned)__builtin_amdgcn_readfirstlane((pack2(mean, mean) & 0xffffu) | (__float_as_uint(den) & 0xffff0000u));
    };
    auto ffn = [&](const u32x2& xr, unsigned md, unsigned n, const __amdgpu_buffer_rsrc_t rs, int yo, auto&& off) __attribute__((always_inline)) {
      gate_up(xr, md);
      if (hw == 0 && n == 2u) STAMP(dk, 8);
      asm volatile("" ::: "memory");
      PIN();
      lds_wr(lbase + MEET + hw * 4, n + off());
      u32x4 mt;
#pragma unroll 1
      for (int i = 0; i < SPIN_MAX; ++i) {
        mt = lds_rd4(lbase + MEET);
        if (__builtin_amdgcn_readfirstlane(min4(mt)) >= n) break;
      }
      asm volatile("" ::: "memory");
      PIN();
      if (hw == 0 && n == 2u) STAMP(dk, 9);
      if (__builtin_amdgcn_readfirstlane(mt[0]) != n + 1u)
        __builtin_amdgcn_raw_buffer_store_b128(down(act_diag(n == 4u ? 512 : 0), w), rs, (yo + fg * D_) * 4 + t * 16, 0, 16);
      dad += n == 2u ? 512 : -512;
      if (hw == 0 && n == 2u) STAMP(dk, 4);
    };
    auto y_sum4 = [&](const __amdgpu_buffer_rsrc_t rs, int yo, int k) __attribute__((always_inline)) {
      const int src = (yo + (hw * 56 + (lane >> 3)) * D_ + oc + (lane & 7) * 4) * 4;
      u32x4 v[7];
#pragma unroll
      for (int i = 0; i < 7; ++i) asm volatile("" : "=v"(v[i]));
      unsigned todo = 0x7fu;
#pragma unroll 1
      for (int spin = 0; spin < SPIN_MAX; ++spin) {
        LOG();
#pragma unroll
        for (int i = 0; i < 7; ++i) if ((todo >> i) & 1u) v[i] = __builtin_amdgcn_raw_buffer_load_b128(rs, src, i * (8 * D_ * 4), 16);
#pragma unroll
        for (int i = 0; i < 7; ++i) if (none(umax(umax(v[i][0], v[i][1]), umax(v[i][2], v[i][3])) == SENT)) todo &= ~(1u << i);
        if (!todo) break;
        __builtin_amdgcn_s_sleep(YSLP);
        asm volatile("" ::: "memory");
      }
      f32x2 z0 = {0.f, 0.f}, z1 = {0.f, 0.f};
#pragma unroll
      for (int i = 0; i < 7; ++i) {
        z0 += f32x2{__uint_as_float(v[i][0]), __uint_as_float(v[i][1])};
        z1 += f32x2{__uint_as_float(v[i][2]), __uint_as_float(v[i][3])};
      }
      const unsigned ya = lbase + SPM + 256 + k * 512 + ((lane & 7) * 4 + (lane >> 4)) * 16;
      lds_wr((lane & 8) ? lbase + DUM + lane * 4 : ya + hw * 4, f2word(fold8(z0, z1)));
      u32x4 q = {0u, 0u, 0u, 0u};
      if (hw == 3) {
#pragma unroll 1
        for (int i = 0; i < 4 * SPIN_MAX; ++i) {
          q = lds_rd4(ya);
          if (none(umax(umax(q[0], q[1]), umax(q[2], q[3])) == SENT)) break;
        }
      }
      return (__uint_as_float(q[0]) + __uint_as_float(q[1])) + (__uint_as_float(q[2]) + __uint_as_float(q[3]));
    };
    const __amdgpu_buffer_rsrc_t rs1 = __builtin_amdgcn_make_buffer_rsrc(hand, 0, HAND_N * 4, 0x00020000);
    u32x2 xr;
    unsigned xo, xo2;
    until(t1 + XDT);
    get_row(fr1, 0, xr, xo);
    if (hw == 0) STAMP(dk, 7);
#define FAILFC 0
    const unsigned md1 = row_md(xr, 0) ^ (FAILFC & 1);
    if (hw == 0) STAMP(dk, 10);
    LOGAT(20);
    u32x2 xr2;
    unsigned md2 = 0u;
    if (p1blk) {
      xr2 = __builtin_amdgcn_raw_buffer_load_b64(rsx, (fr2 >> 7) == omi ? S_ * D_ * 2 : xoff, D_ * 2, 16);
      PIN();
      ffn(xr, md1, 2u, rsh, GRAN_N, [&]() { return 0u; });
      __builtin_amdgcn_s_setprio(R2PRIO);
      LOG();
      if ((fr2 >> 7) == omi || !none(umax(xr2[0], xr2[1]) == SENT)) get_row(fr2, 1, xr2, xo2);
      md2 = row_md(xr2, 1) ^ (FAILFC & 2);
      ffn(xr2, md2, 4u, rs1, GRAN_N, [&]() {
        const u32x2 v = *LDS_AT(const volatile u32x2, lbase + BEST);
        return v[0] != 0u && (2047 - (int)(v[0] & 0x7ffu) != fr2 || v[1] != md2) ? 1u : 0u; });
    }
    if (hw == 0) STAMP(dk, 5);
    LOG();
    float y = 0.f;
    auto put_row = [&](int r, unsigned md, unsigned rb) __attribute__((always_inline)) {
      const float rden = __uint_as_float(md & 0xffff0000u), rs = rbf(frcp(1.0f + __expf(-__uint_as_float(rb << 16))));
      const float xa = __uint_as_float((col & 1) ? xo & 0xffff0000u : xo << 16);
      float ha = xa, hb = xa;
      ln_apply2(ha, hb, __uint_as_float(md << 16), rden, frcp(rden));
      const float o = xa + rbf(ha + rbf(rbf(y) * rs));
      __builtin_amdgcn_raw_buffer_store_b16((unsigned short)(pack2(o, o) & 0xffffu), __builtin_amdgcn_make_buffer_rsrc(out, 0, hw == 3 ? S_ * D_ * 2 : 0, 0x00020000),
                                            (lane & 8) ? S_ * D_ * 2 : (r * D_ + col) * 2, 0, OAUX);
    };
    if (!p1blk) {
      until(t1 + XDT + YD1); until(tmx + YD2);
      if (hw == 3) STAMP(dk, 13);
      LOGAT(8);
      y = y_sum4(rsh, GRAN_N, 0);
      put_row(fr1, md1, rk1);
      if (hw == 3) STAMP(dk, 14);
      LOGAT(24);
    }
    unsigned bmd = 0u;
    best = 0u;
#pragma unroll 1
    for (int spin = 0; spin < 4 * SPIN_MAX; ++spin) {
      const u32x2 v = *LDS_AT(const volatile u32x2, lbase + BEST);
      best = __builtin_amdgcn_readfirstlane(v[0]); bmd = __builtin_amdgcn_readfirstlane(v[1]);
      if (best) break;
      __builtin_amdgcn_s_sleep(1);
    }
    asm volatile("" ::: "memory");
    const int idx = 2047 - (int)(best & 0x7ffu);
    const unsigned rk = best >> 16, rb = (rk & 0x8000u) ? (rk & 0x7fffu) : (~rk & 0xffffu);
    if (hw == 0) STAMP(dk, 1);
    LOGAT(25);
    LOGOUT(hw);
    if (__builtin_expect(idx != fr1 || bmd != md1 || rb != rk1, 0)) {
      const __amdgpu_buffer_rsrc_t rsf = __builtin_amdgcn_make_buffer_rsrc(gran + GR_F, 0, !p1blk && hw == 3 && idx != fr1 ? 48 * 128 : 0, 0x00020000);
      asm volatile("s_waitcnt vmcnt(0)" ::: "memory");
      __builtin_amdgcn_raw_buffer_store_b32(tag, rsf, lane == 0 ? (16 + (oc >> 5)) * 128 : 48 * 128, 0, 16);
      if (idx != fr1 || bmd != md1) {
        if (!p1blk || idx != fr2 || bmd != md2) get_row(idx, idx == fr1 ? 0 : (idx == fr2 ? 1 : 2), xr, xo);
        if (!p1blk && idx == fr2) md2 = row_md(xr, 1) ^ (FAILFC & 2);
        if (idx == fr2 && bmd == md2) {
          if (!p1blk) y = y_sum4(rs1, GRAN_N, 1);
        } else {
          if (p1blk) ffn(xr, bmd, 6u, rsh, HAND_N + Y3_O, [&]() { return 0u; });
          else y = y_sum4(rsh, HAND_N + Y3_O, 2);
        }
      }
      if (p1blk || hw != 3) return;
#pragma unroll 1
      for (int spin = 0; spin < SPIN_MAX && idx != fr1; ++spin) {
        if (__builtin_amdgcn_readfirstlane(__builtin_amdgcn_raw_buffer_load_b32(rsf, (oc >> 6) * 128, 0, 16)) == tag) break;
        asm volatile("" ::: "memory");
      }
      put_row(idx, bmd, rb);
    }
    if (p1blk || hw != 3) return;
    }
    return;
  }
  if (wid >= 4) {
    const int lane = fresh_lane();
    const int lw = wid - 4;
    const long long lay_ = ap->lay;
    const u16* A = ap->a; const u16* W = ap->W; const u16* wc = ap->wc;
#define LPRIO 3
#define LP0 3
    __builtin_amdgcn_s_setprio(LP0);
    unsigned va[4], vw[6];
    const unsigned ul = lane;
    va[0] = (unsigned)(m0 + lw * 32) * (K * 2) + (ul >> 3) * (K * 2) + (((ul & 7u) ^ (ul >> 4)) << 4);
    va[1] = va[0] + (8 * K * 2 - 1024);
    va[2] = (va[0] ^ 64u) + 2 * (8 * K * 2 - 1024);
    va[3] = va[2] + (8 * K * 2 - 1024);
#pragma unroll
    for (int i = 0; i < 3; ++i) {
      const unsigned n = ul + 64u * i, k = __umul24(n, 171u) >> 12, cp = n - __umul24(k, 24u);
      vw[i] = __umul24(k, ldw * 2) + (cp >> 3) * 2048u + (((cp & 7u) ^ ((k & 2u) << 1)) << 4) + ((unsigned)(lw * 16 * ldw + n0) * 2u - i * 1024u);
      vw[i + 3] = (vw[i] ^ 16u) + 8 * ldw * 2;
    }
    PIN();
    asm volatile("" : "+v"(va[0]), "+v"(va[1]), "+v"(va[2]), "+v"(va[3]), "+v"(vw[0]), "+v"(vw[1]), "+v"(vw[2]), "+v"(vw[3]), "+v"(vw[4]), "+v"(vw[5]));
    const bool LAY2 = lay_ != 0;
    const int krot = tmi & 8;
    if (LAY2 && slow_cols(W, tni)) W = wc;
    auto stage_a = [&](int kt, int slot) {
      const char* pa = reinterpret_cast<const char*>(A) + (size_t)(kt ^ krot) * 128;
      asm volatile("s_mov_b32 m0, %5\n\ts_nop 0\n\tglobal_load_lds_dwordx4 %0, %4 sc1\n\tglobal_load_lds_dwordx4 %1, %4 offset:1024 sc1\n\t"
                   "global_load_lds_dwordx4 %2, %4 offset:2048 sc1\n\tglobal_load_lds_dwordx4 %3, %4 offset:3072 sc1"
                   :: "v"(va[0]), "v"(va[1]), "v"(va[2]), "v"(va[3]), "s"(pa), "s"(lbase + slot * STB + lw * 4096) : "m0", "memory");
    };
    auto stage_w = [&](int kt, int slot) {
      const char* pw = reinterpret_cast<const char*>(W) + (size_t)(kt ^ krot) * 128 * ldw;
      asm volatile("s_mov_b32 m0, %7\n\ts_nop 0\n\tglobal_load_lds_dwordx4 %0, %6 sc1\n\tglobal_load_lds_dwordx4 %1, %6 offset:1024 sc1\n\t"
                   "global_load_lds_dwordx4 %2, %6 offset:2048 sc1\n\ts_addk_i32 m0, 0xc00\n\ts_nop 0\n\tglobal_load_lds_dwordx4 %3, %6 sc1\n\t"
                   "global_load_lds_dwordx4 %4, %6 offset:1024 sc1\n\tglobal_load_lds_dwordx4 %5, %6 offset:2048 sc1"
                   :: "v"(vw[0]), "v"(vw[1]), "v"(vw[2]), "v"(vw[3]), "v"(vw[4]), "v"(vw[5]), "s"(pw), "s"(lbase + slot * STB + ABYTES + lw * 6144) : "m0", "scc", "memory");
    };
    if (LAY2) {
      __builtin_amdgcn_s_setprio(0);
      __builtin_amdgcn_s_sleep(TPRE - 10);
      tile_at(3u);
      __builtin_amdgcn_s_setprio(LPRIO);
#pragma unroll
      for (int st = 0; st < NST; ++st) stage_w(st, st);
      PIN();
      __builtin_amdgcn_s_barrier();
      PIN();
#pragma unroll
      for (int st = 0; st < NST; ++st) stage_a(st, st);
      asm volatile("s_waitcnt vmcnt(8)" ::: "memory");
    } else {
      __builtin_amdgcn_s_setprio(LPRIO);
      stage_a(0, 0); stage_w(0, 0);
      if (PRO > 1) { stage_a(1, 1); stage_w(1, 1); }
      if (PRO > 2) { stage_a(2, 2); stage_w(2, 2); }
      asm volatile("s_waitcnt vmcnt(%0)" :: "n"(10 * PRO - 10) : "memory");
    }
    int slot = 0;
    __builtin_amdgcn_s_barrier();
    if (!LAY2) {
      if (PRO < 2) { stage_a(1, 1); stage_w(1, 1); }
      if (PRO < 3) { stage_a(2, 2); stage_w(2, 2); }
    }
    for (int kt = 0; kt + 1 < NKT; ++kt) {
      if (LAY2 && kt == 0) asm volatile("s_waitcnt vmcnt(4)" ::: "memory");
      else if (kt + 2 >= NKT) asm volatile("s_waitcnt vmcnt(0)" ::: "memory");
      else asm volatile("s_waitcnt vmcnt(10)" ::: "memory");
      __builtin_amdgcn_s_barrier();
      if (kt + NST < NKT) { stage_a(kt + NST, slot); stage_w(kt + NST, slot); }
      slot = slot == NST - 1 ? 0 : slot + 1;
    }
    __builtin_amdgcn_s_setprio(0);
    __builtin_amdgcn_s_barrier();
  } else {
  LAYER_HERE;
  const int dk = LAY2 ? 5 : 1;
  const int lane = fresh_lane(), wm = wid & 1, wn = wid >> 1;
  if (wid == 0) STAMP(dk, 0);
  f32x4 acc[4][6];
#pragma unroll
  for (int i = 0; i < 4; ++i)
#pragma unroll
    for (int j = 0; j < 6; ++j) acc[i][j] = f32x4{0.f, 0.f, 0.f, 0.f};
  const int q16 = lane & 15, g4 = lane >> 4;
  const int e0 = 16 * (q16 >> 3) + (q16 & 7);
  s16x8 af[2][4];
  unsigned aad[2], wad[2];
  {
    const int row = wm * 64 + e0, swz = ((row >> 1) & 3) | (((row >> 4) & 1) << 2);
#pragma unroll
    for (int h = 0; h < 2; ++h) aad[h] = lbase + row * 128 + (((4 * h + g4) ^ swz) << 4);
    const int tq = (lane & 15) >> 2, tp = lane & 3, k = 8 * g4 + tq, ch = wn * 4 + 2 * (tp >> 1);
    wad[0] = lbase + ABYTES + k * 384 + ((ch ^ (4 * ((k >> 1) & 1)) ^ (g4 & 1)) << 4) + 8 * (tp & 1);
    wad[1] = wad[0] ^ 16;
  }
  auto lda = [&](unsigned a, int i) { return *LDS_AT(const s16x8, a + (i >> 1) * 4096 + (i & 1) * 1024); };
  auto ldw_ = [&](int h, int j) {
    const unsigned w = wad[j & 1] + h * 12288 + (j >> 1) * 128;
    s16x4 lo = __builtin_amdgcn_ds_read_tr16_b64_v4i16(LDS_AT(s16x4, w));
    s16x4 h4 = __builtin_amdgcn_ds_read_tr16_b64_v4i16(LDS_AT(s16x4, w + 1536));
    return (s16x8)__builtin_shufflevector(lo, h4, 0, 1, 2, 3, 4, 5, 6, 7);
  };
  ARGS_HERE;
  unsigned lto = lbase + wid * 4096;
  char* kvb = reinterpret_cast<char*>(KV) + ((tni * 2 + wn) * 64 + tmi * 4 + wm * 2) * 2048 - lto;
  {
    const unsigned ato = lbase + ALPHA + wm * 256, dum = lbase + DUM + lane * 4;
    *LDS_AT(u32x4, lto + QOFF + lane * 16) = u32x4{lane >= 16 ? lbase + CRED : lbase + TABA + (wm * 64 + e0) * 16, lbase + TABW + (wn * 32 + e0) * 16,
                                                   ato + e0 * 4, lto + (lane >> 5) * 1024 + ((lane & 28) * 2 + (lane & 3)) * 16};
    *LDS_AT(u32x2, lto + QOFF + 2048 + lane * 16) = u32x2{aad[0], aad[1]};
    *LDS_AT(u32x4, lto + QOFF + 1024 + lane * 16) = u32x4{ato + g4 * 16 + (lane >> 5) * 32, lto + (lane >> 5) * 1024 + ((lane & 24) * 2 + (lane & 7)) * 16,
                                                          lane == 0 ? lbase + FLG + 32 : dum, lane == 0 ? lbase + FLG + 16 : dum};
  }
  asm volatile("" : "+s"(lto), "+s"(kvb) :: "memory");
#pragma unroll
  for (int i = 0; i < 4; ++i)
#pragma unroll
    for (int j = 0; j < 6; ++j) asm volatile("" : "+v"(acc[i][j]));
  asm volatile("" : "+v"(aad[0]), "+v"(aad[1]), "+v"(wad[0]), "+v"(wad[1]));
  if (LAY2) __builtin_amdgcn_s_barrier();
  __builtin_amdgcn_s_barrier();
  PIN();
  if (wid == 0) STAMP(dk, 1);
  s16x8 wq[4][2];
  wq[0][0] = ldw_(0, 0); af[0][0] = lda(aad[0], 0); wq[0][1] = ldw_(1, 0); af[1][0] = lda(aad[1], 0); wq[1][0] = ldw_(0, 1); wq[1][1] = ldw_(1, 1);
  af[0][1] = lda(aad[0], 1); af[1][1] = lda(aad[1], 1); af[0][2] = lda(aad[0], 2); af[1][2] = lda(aad[1], 2);
  PIN();
  s16x4 wlo;
  auto rw = [&](s16x8& d, int h, int j, int hi, int off = 0) __attribute__((always_inline)) {
    const s16x4 v = __builtin_amdgcn_ds_read_tr16_b64_v4i16(LDS_AT(s16x4, wad[j & 1] + h * 12288 + (j >> 1) * 128 + hi * 1536 + off));
    if (hi) d = (s16x8)__builtin_shufflevector(wlo, v, 0, 1, 2, 3, 4, 5, 6, 7);
    else wlo = v;
  };
  auto at = [&](s16x8& x, int r) __attribute__((always_inline)) {
    if (r == 0) asm volatile("" :: "{v[104:107]}"(x));
    if (r == 1) asm volatile("" :: "{v[108:111]}"(x));
    if (r == 2) asm volatile("" :: "{v[112:115]}"(x));
    if (r == 3) asm volatile("" :: "{v[116:119]}"(x));
    if (r == 4) asm volatile("" :: "{v[120:123]}"(x));
    if (r == 5) asm volatile("" :: "{v[124:127]}"(x));
    if (r == 6) asm volatile("" :: "{v[128:131]}"(x));
    if (r == 7) asm volatile("" :: "{v[132:135]}"(x));
    if (r == 8) asm volatile("" :: "{v[136:139]}"(x));
    if (r == 9) asm volatile("" :: "{v[140:143]}"(x));
    if (r == 10) asm volatile("" :: "{v[144:147]}"(x));
    if (r == 11) asm volatile("" :: "{v[148:151]}"(x));
    if (r == 12) asm volatile("" :: "{v[152:155]}"(x));
    if (r == 13) asm volatile("" :: "{v[156:159]}"(x));
    if (r == 14) asm volatile("" :: "{v[160:163]}"(x));
    if (r == 15) asm volatile("" :: "{v[164:167]}"(x));
  };
  auto mm = [&](int i, int j, int h, int sl, int hw_) __attribute__((always_inline)) {
    s16x8 &a = af[h][i], &w = wq[sl][hw_];
    at(a, h * 4 + i); at(w, 8 + sl * 2 + hw_);
    if (j >= 4) asm volatile("v_mfma_f32_16x16x32_bf16 %0, %1, %2, %0" : "+v"(acc[i][j]) : "v"(a), "v"(w));
    else asm volatile("v_mfma_f32_16x16x32_bf16 %0, %1, %2, %0" : "+v"(acc[i][j]) : "v"(w), "v"(a));
    PIN();
  };
  auto next = [&](int g, int J0, int s0, int s1, int off) __attribute__((always_inline)) {
    if (g == 2 || g == 3) rw(wq[s0][0], 0, J0, g & 1, off);
    if (g == 4 || g == 5) af[g & 1][0] = lda(aad[g & 1] + off, 0);
    if (g == 6 || g == 7) rw(wq[s0][1], 1, J0, g & 1, off);
    if (g >= 8 && g < 12) rw(wq[s1][(g - 8) >> 1], (g - 8) >> 1, J0 + 1, g & 1, off);
    if (g >= 12) af[g & 1][(g - 10) >> 1] = lda(aad[g & 1] + off, (g - 10) >> 1);
  };
  auto third = [&](int p, int J0, int delta) __attribute__((always_inline)) {
    auto mv = [&](unsigned& x) __attribute__((always_inline)) { asm volatile("v_add_u32 %0, %1, %0" : "+v"(x) : "s"(delta)); };
#pragma unroll
    for (int g = 0; g < 16; ++g) {
      const int h = g & 1, i = p == 1 ? (g >> 1) & 3 : g >> 2, t = p == 1 ? g >> 3 : (g >> 1) & 1, j = 2 * p + t, sl = p == 1 ? 2 - 2 * t : p + t;
      mm(i, j, h, sl, h);
      if (p == 0) {
        if (g < 2) af[g][3] = lda(aad[g], 3);
        else if (g < 6) rw(wq[2][(g - 2) >> 1], (g - 2) >> 1, 2, g & 1);
        else if (g < 10) rw(wq[3][(g - 6) >> 1], (g - 6) >> 1, 5, g & 1);
        else if (g < 12) mv(aad[g - 10]);
      }
      if (p == 1) {
        if (g < 4) rw(wq[0][g >> 1], g >> 1, 3, g & 1);
        if (g == 4) mv(wad[1]);
        if (g >= 8 && g < 12) rw(wq[2][(g - 8) >> 1], (g - 8) >> 1, 4, g & 1);
        if (g == 12) mv(wad[0]);
      }
      if (p == 2) {
        if (g == BARG) {
          asm volatile("" ::: "memory");
          PIN();
          __builtin_amdgcn_s_barrier();
          asm volatile("" ::: "memory");
        }
        next(g, J0, 0, 1, 0);
      }
      PIN();
      {
        if ((p == 2 && g == 15) || (p == 0 && g == 7)) __builtin_amdgcn_s_waitcnt(0xc67f);
        if (p == 0 && g == 2) __builtin_amdgcn_s_waitcnt(0xc57f);
        if ((p == 0 && g == 14) || (p == 1 && (g & 7) == 7)) __builtin_amdgcn_s_waitcnt(0xc07f);
        PIN();
      }
    }
  };
  third(0, 0, STB); third(1, 0, STB);
  {
    static_assert((NKT - NST - 1) % NST == 0, "");
#pragma unroll 1
    for (int kt = 0; kt < NKT - NST - 1; kt += NST) {
#pragma unroll
      for (int u = 0; u < NST; ++u) {
        const int delta = u == 1 ? -(NST - 1) * STB : STB;
        third(2, 0, 0); third(0, 0, delta); third(1, 0, delta);
      }
    }
  }
  static_assert(NKT % NST == 1, "slots of the last stages");
#define TT(i)
  auto bar = [&]() { PIN(); __builtin_amdgcn_s_barrier(); asm volatile("" ::: "memory"); PIN(); };
  TT(0);
  third(2, 2, 0);
  const __amdgpu_buffer_rsrc_t rsk = __builtin_amdgcn_make_buffer_rsrc(kvb, 0, 0x7fffffff, 0x00020000), rsv = __builtin_amdgcn_make_buffer_rsrc(kvb + VOFF, 0, 0x7fffffff, 0x00020000);
  unsigned pk;
  u32x2 t0, t1, t2, t3;
  auto park = [&](int i) { return *LDS_AT(const volatile u32x2, pk + (i >> 1) * 1024 + (i & 1) * 8); };
  float al[4];
  f32x4 av[4];
  u32x4 kvp[12];
  auto cacc = [&](int p, int x, int t) -> f32x4& { return p < 2 ? acc[x][2 * p + t] : acc[(x >> 1) * 2 + t][4 + (x & 1)]; };
  auto signal = [&](unsigned v) { __hip_atomic_fetch_add(LDS_AT(unsigned, t3[0]), v, __ATOMIC_RELAXED, __HIP_MEMORY_SCOPE_WORKGROUP); PIN(); };
  auto outop = [&](int p, int k) {
    if (k >= 0 && k < 16) {
      const int x = k >> 2, t = (k >> 1) & 1;
      f32x4& c = cacc(p, x, t);
#pragma unroll
      for (int o = 2 * (k & 1); o < 2 * (k & 1) + 2; ++o) {
        float r;
        asm volatile("v_mul_f32 %0, %1, %2" : "=v"(r) : "v"(c[o]), "v"(p < 2 ? al[x] : av[(x >> 1) * 2 + t][o]));
        c[o] = r;
      }
      PIN();
    }
    if (k >= 1 && k <= 16) {
      const int x = (k - 1) >> 2, t = ((k - 1) >> 1) & 1, o = (k - 1) & 1;
      kvp[p * 4 + x][2 * t + o] = pack2(cacc(p, x, t)[2 * o], cacc(p, x, t)[2 * o + 1]);
      PIN();
      if (t && o) {
        if (p) __builtin_amdgcn_raw_buffer_store_b128(kvp[p * 4 + x], p == 1 ? rsk : rsv, (p == 1 ? t1[1] : t2[1]) + (x >> 1) * 2048 + (x & 1) * (p == 1 ? 64 : 128), 0, 16);
        else *LDS_AT(u32x4, t2[1] + (x >> 1) * 2048 + (x & 1) * 128) = kvp[x];
        PIN();
      }
    }
  };
  static_for<0, 168>([&](auto nc) __attribute__((always_inline)) {
    constexpr int n = decltype(nc)::value, sec = n < 80 ? n >> 4 : (n < 104 ? 5 : 6 + ((n - 104) >> 4)), g = n < 104 ? n - 16 * sec : (n - 104) & 15;
    auto taba = [&](int i) { af[0][i] = *LDS_AT(const s16x8, t0[0] + (i >> 1) * 512 + (i & 1) * 128); };
    auto tabw = [&](int j) { wq[(2 + (j >> 1)) & 3][j & 1] = *LDS_AT(const s16x8, t0[1] + ((j >> 1) * 64 + (j & 1) * 8) * 16); };
    auto rwn = [&](int e, int sl, int j, int off) { if (e >= 0 && e < 4) rw(wq[sl][e >> 1], e >> 1, j, e & 1, off); };
    if (g == 0) TT(1 + sec);
    if (sec == 5) {
      const int i = (g / 12) * 2 + (g & 1), j = (g % 12) >> 1;
      mm(i, j, 0, (2 + (j >> 1)) & 3, j & 1);
    } else {
      constexpr int part[10] = {1, 2, 1, 2, 0, 0, 1, 2, 0, 0}, s0[10] = {0, 2, 0, 2, 0, 0, 1, 3, 1, 3};
      const int h = g & 1, i = sec == 7 ? (g >> 3) * 2 + ((g >> 1) & 1) : g >> 2, t = sec == 7 ? (g >> 2) & 1 : (g >> 1) & 1;
      mm(i, 2 * part[sec] + t, h, (s0[sec] + t) & 3, h);
    }
    if (sec == 0 || sec == 2) {
      if (g < 2) af[g][3] = lda(aad[g] + sec / 2 * STB, 3);
      rwn(g - 2, 2, 4, sec / 2 * STB); rwn(g - 6, 3, 5, sec / 2 * STB);
    }
    if (sec == 1) {
      if (g == 1) bar();
      next(g, 2, 0, 1, STB);
    }
    if ((sec == 3 && g == 12) || (sec == 6 && g == 13)) { pk = lto + QOFF + fresh_lane() * 16; asm volatile("" : "+v"(pk)); }
    if (sec == 3) {
      rwn(g, 0, 0, STB); rwn(g - 4, 1, 1, STB);
      if (g >= 8 && g < 12) asm volatile("v_subrev_u32 %0, %1, %0" : "+v"(g < 10 ? aad[g & 1] : wad[g & 1]) : "s"(STB));
      if (g == 13) t1 = park(1);
      if (g == 14) t0 = park(0);
    }
    if (sec == 4) {
      if (g == B15) bar();
      if (g == 4) taba(0);
      if (g == 5 || g == 6) tabw(g - 5);
      if (g == 7) taba(1);
      if (g == 8 || g == 9) tabw(g - 6);
      if (g == 10 || g == 11) af[1][g - 10] = lda(aad[1], g - 10);
      if (g == 12) taba(2);
      if (g == 15) taba(3);
    }
    if (sec == 5) {
      if (g < 2) tabw(4 + g);
      rwn(g - 2, 1, 2, 0);
      if (g == 12 || g == 13) af[0][g - 12] = lda(aad[0], g - 12);
      if (g == 22) { al[0] = *LDS_AT(const float, t1[0]); al[1] = *LDS_AT(const float, t1[0] + 32); }
      rwn(g - 16, 2, 3, 0);
      if (g == 20 || g == 21) af[1][g - 18] = lda(aad[1], g - 18);
    }
    if (sec == 6) {
      if (g < 2) af[0][2 + g] = lda(aad[0], 2 + g);
      rwn(g - 2, 3, 4, 0);
      if (g == 9) { al[2] = *LDS_AT(const float, t1[0] + 128); al[3] = *LDS_AT(const float, t1[0] + 160); }
      if (g == 14) t2 = park(2);
      if ((g & 3) >= 2) rwn((g - 10) / 4 * 2 + (g & 1) - (g < 10) * 8, 0, 5, 0);
    }
    if (sec == 7) {
      if (g == 2 || g == 3 || g == 10 || g == 11) av[(g >> 3) * 2 + (g & 1)] = *LDS_AT(const f32x4, t2[0] + ((g >> 3) * 32 + (g & 1) * 8) * 4);
      rwn(g < 2 ? g : (g < 4 ? -1 : g - 2), 1, 0, 0); rwn(g - 6, 2, 1, 0);
      if (g == 12) t3 = park(3);
      if (g == 13) { const u32x2 a = park(4); aad[0] = a[0]; aad[1] = a[1]; }
      if (g == 15) {
        asm volatile("" ::: "memory");
        __builtin_amdgcn_s_waitcnt(0xc07f);
        bar();
      }
    }
    if (sec == 8) next(g, 0, 3, 0, STB);
    if (sec == 9) {
      if (g < 2) af[g][3] = lda(aad[g] + STB, 3);
      if (g == 2) signal(0x10000u);
    }
    PIN();
    outop(1, n - 104 - KO);
    outop(2, n - 120 - KO);
    {
      constexpr int wg[8] = WGAPS;
#pragma unroll
      for (int e = 0; e < 8; ++e)
        if (n - 136 == wg[e]) {
          *LDS_AT(u32x4, (e < 4 ? t1[1] + (e & 1) * 64 : t2[1] + 2048 + (e & 1) * 128) + ((e >> 1) & 1) * ASB) = kvp[4 + e];
          PIN();
        }
    }
    if (n == 152 + 3 || n == 152 + 4) {
      al[2 * (n & 1)] *= QSCALE; al[2 * (n & 1) + 1] *= QSCALE;
      asm volatile("" : "+v"(al[2 * (n & 1)]), "+v"(al[2 * (n & 1) + 1]));
      if (n & 1) asm volatile("v_add_u32 %0, %1, %0" : "+v"(t2[1]) : "s"(QOFF));
      PIN();
    }
    outop(0, n - 152 - KO);
  });
  TT(13);
  if (wid == 0) STAMP(dk, 18);
#pragma unroll
  for (int k = 16 - KO; k <= 16; ++k) outop(0, k);
  __hip_atomic_fetch_add(LDS_AT(unsigned, t3[1]), 1u, __ATOMIC_RELAXED, __HIP_MEMORY_SCOPE_WORKGROUP);
  TT(14);
  asm volatile("s_waitcnt vmcnt(0)" ::: "memory");
  signal(1u);
  TT(15);
  if (wid == 0) STAMP(dk, 19);
  }
  PIN();
  {
  const int lane = fresh_lane(), tid = wid * 64 + lane;
  LAYER_HERE;
  const int dk = LAY2 ? 6 : 2;
  const int g = wid >> 2, hh = (wid >> 1) & 1, qg = wid & 1;
  const int head = tni * 2 + hh, qt = tmi * 4 + qg * 2;
  unsigned qaa = lbase + QOFF + (hh * 2 + qg) * 4096 + lane * 16 + (g ? 0 : 2048), qab = qaa ^ 2048;
  const unsigned wbase = lbase + (hh * 2 + g) * 4096 + lane * 16;
  auto sptr = [&](int sl) { unsigned a = wbase + sl * ASB; asm volatile("" : "+v"(a)); return LDS_AT(const s16x8, a); };
  auto rd = [&](__attribute__((address_space(3))) const s16x8* sp, int off) { return sp[off >> 4]; };
  auto sl0 = sptr(0);
  const unsigned arr_a = lane == 0 ? lbase + FLG + 16 : lbase + DUM + lane * 4;
  auto arrive = [&]() { __hip_atomic_fetch_add(LDS_AT(unsigned, arr_a), 1u, __ATOMIC_RELAXED, __HIP_MEMORY_SCOPE_WORKGROUP); };
  unsigned arr_w = lane == 0 ? lbase + FLG + 8 + g * 4 : lbase + DUM + lane * 4;
  unsigned arr_i = 1u << (8 * (wid & 3));
  auto arrive_w = [&]() { __hip_atomic_fetch_add(LDS_AT(unsigned, arr_w), arr_i, __ATOMIC_RELAXED, __HIP_MEMORY_SCOPE_WORKGROUP); };
  s16x8 sel;
#pragma unroll
  for (int e = 0; e < 8; ++e) sel[e] = ((lane & 7) == (e & 4)) ? (short)0x3F80 : (short)0;
  int sidx = 0xDDDDDDDD;
  f32x16 oa, ob, nn, sa, sb;
  f32x4 la = {0.f, 0.f, 0.f, 0.f}, lb = {0.f, 0.f, 0.f, 0.f};
#pragma unroll
  for (int e = 0; e < 16; ++e) { oa[e] = 0.f; ob[e] = 0.f; sa[e] = 0.f; sb[e] = 0.f; }
  asm volatile("" : "+v"(sidx), "+v"(sel), "+v"(oa), "+v"(ob), "+v"(la), "+v"(lb), "+v"(qaa), "+v"(qab), "+v"(arr_w), "+v"(arr_i));
  PIN();
  s16x8 qa0, qa1, qb0, qb1, k00, k01, v00, v01, v10, v11, k10, k11;
  u32x4 fw;
  auto first = [&]() {
    fw = lds_rd4(lbase + FLG + 16);
    asm volatile("" ::: "memory");
    k00 = rd(sl0, 0); qa0 = *LDS_AT(const s16x8, qaa); qb0 = *LDS_AT(const s16x8, qab);
    k01 = rd(sl0, 1024); qa1 = *LDS_AT(const s16x8, qaa + 1024); qb1 = *LDS_AT(const s16x8, qab + 1024);
    v00 = rd(sl0, 2048); v01 = rd(sl0, 3072);
    PIN();
  };
  first();
  if (__builtin_amdgcn_readfirstlane(fw[0]) < 4u) {
#pragma unroll 1
    for (int i = 0; i < SPIN_MAX; ++i) {
      if (__builtin_amdgcn_readfirstlane(*LDS_AT(const volatile unsigned, lbase + FLG + 16)) >= 4u) break;
      __builtin_amdgcn_s_sleep(ASLP);
    }
    asm volatile("" ::: "memory");
    first();
  }
  const unsigned tag = __builtin_amdgcn_readfirstlane(fw[1]);
  const int idx = LAY2 ? (2047 - (int)(__builtin_amdgcn_readfirstlane(fw[3]) & 0xffffu)) & 2047 : -64;
  float ma;
  const float zoff = __uint_as_float(__builtin_amdgcn_readfirstlane(LAY2 && g == ((idx >> 6) & 1) ? 0u : 0xff800000u));
  {
    sa = MFMA32(k00, qa0, sa); sb = MFMA32(k00, qb0, sb);
    sa = MFMA32(k01, qa1, sa); sb = MFMA32(k01, qb1, sb);
    float mb = sb[0];
    ma = sa[0];
#pragma unroll
    for (int e = 1; e < 16; ++e) { ma = fmaxf(ma, sa[e]); mb = fmaxf(mb, sb[e]); }
    ma = fmaxf(ma, mb);
    const auto sw = __builtin_amdgcn_permlane32_swap(__float_as_uint(ma), __float_as_uint(ma), false, false);
    ma = fmaxf(__uint_as_float(sw[0]), __uint_as_float(sw[1]));
    f32x2 n2 = {-ma, -ma};
    asm volatile("" : "+v"(n2));
#pragma unroll
    for (int e = 0; e < 16; ++e) nn[e] = n2[e & 1];
    sa = MFMA32(k00, qa0, nn); sa = MFMA32(k01, qa1, sa);
    la[0] = lb[0] = -__uint_as_float(__float_as_uint(__builtin_amdgcn_exp2f(zoff - ma)) & 0xffff0000u);
  }
#define SB() __builtin_amdgcn_sched_barrier(0)
#define EX(i) sc[i] = __builtin_amdgcn_exp2f(sc[i])
  auto unit = [&](f32x16& sc, f32x16& sn, const s16x8& kn0, const s16x8& kn1, const s16x8& qn0, const s16x8& qn1,
                  f32x16& op, f32x4& lp, const s16x8& vp,
                  f32x16& oc, f32x4& lc, const s16x8& vc, auto&& mid) {
    lp = SMF16(sel, PHI(sn), lp); SB();
    op = SMF32(vp, PHI(sn), op); SB();
    EX(0); EX(1); EX(2); EX(3); SB();
    sn = MFMA32(kn0, qn0, nn); SB();
    EX(4); EX(5); EX(6); EX(7); SB();
    sn = MFMA32(kn1, qn1, sn); SB();
    mid();
    EX(8); EX(9); EX(10); EX(11); SB();
    lc = SMF16(sel, PLO(sc), lc); SB();
    oc = SMF32(vc, PLO(sc), oc); SB();
    EX(12); EX(13); EX(14); EX(15); SB();
    asm volatile("" : "+v"(sc));
  };
  sl0 = sptr(1);
  unsigned f0 = 0u;
  auto check = [&](unsigned need) {
    if (__builtin_expect(__builtin_amdgcn_readfirstlane(f0) < need, 0)) {
#pragma unroll 1
      for (int i = 0; i < SPIN_MAX; ++i) {
        if (__builtin_amdgcn_readfirstlane(*LDS_AT(const volatile unsigned, lbase + FLG)) >= need) break;
        __builtin_amdgcn_s_sleep(1);
      }
    }
    asm volatile("" ::: "memory");
  };
#define PAT 0x22221110
#define SAW8(i) do { if (((PAT >> (28 - 4 * (i))) & 3) != ((PAT >> ((32 - 4 * (i)) & 31)) & 3)) { __builtin_amdgcn_s_setprio((PAT >> (28 - 4 * (i))) & 3); SB(); } } while (0)
#define SAW(i) do { if (odd) SAW8(4 + (i)); else SAW8(i); } while (0)
  auto three = [&](int st, bool odd) {
    SAW(0);
    unit(sa, sb, k00, k01, qb0, qb1, ob, lb, v11, oa, la, v00, [&]() { k10 = rd(sl0, 0); k11 = rd(sl0, 1024); SB(); });
    SAW(1);
    unit(sb, sa, k10, k11, qa0, qa1, oa, la, v01, ob, lb, v00, [&]() { v10 = rd(sl0, 2048); v11 = rd(sl0, 3072); SB(); });
    sl0 = sptr((2 * st + 2) & 7);
    SB();
    SAW(2);
    unit(sa, sb, k10, k11, qb0, qb1, ob, lb, v01, oa, la, v10, [&]() { k00 = rd(sl0, 0); k01 = rd(sl0, 1024); SB(); });
  };
  auto fourth = [&](int st, bool odd) {
    SAW(3);
    unit(sb, sa, k00, k01, qa0, qa1, oa, la, v11, ob, lb, v10, [&]() {
      v00 = rd(sl0, 2048); v01 = rd(sl0, 3072);
      f0 = *LDS_AT(const volatile unsigned, lbase + FLG);
      SB();
      arrive_w();
      SB(); });
    check(st >= 14 ? 32u : 2u * st + 5u);
    sl0 = sptr((2 * st + 3) & 7);
    SB();
  };
  SB();
  SAW8(0);
  {
    f32x16& sc = sa;
    sb = MFMA32(k00, qb0, nn); SB();
    k10 = rd(sl0, 0); k11 = rd(sl0, 1024); SB();
    sb = MFMA32(k01, qb1, sb); SB();
    EX(0); EX(1); EX(2); EX(3); EX(4); EX(5); EX(6); EX(7); EX(8); SB();
    oa = SMF32(v00, PLO(sa), oa); SB();
    EX(9); EX(10); EX(11); SB();
    la = SMF16(sel, PLO(sa), la); SB();
    EX(12); EX(13); EX(14); EX(15); SB();
    asm volatile("" : "+v"(sa));
  }
  SAW8(1);
  unit(sb, sa, k10, k11, qa0, qa1, oa, la, v01, ob, lb, v00, [&]() { v10 = rd(sl0, 2048); v11 = rd(sl0, 3072); SB(); });
  sl0 = sptr(2);
  SB();
  SAW8(2);
  unit(sa, sb, k10, k11, qb0, qb1, ob, lb, v01, oa, la, v10, [&]() {
    f0 = *LDS_AT(const volatile unsigned, lbase + FLG);
    asm volatile("" ::: "memory");
    k00 = rd(sl0, 0); k01 = rd(sl0, 1024); SB(); });
  if (__builtin_amdgcn_readfirstlane(f0) < 3u) { check(3u); k00 = rd(sl0, 0); k01 = rd(sl0, 1024); }
  SB();
  SAW8(3);
  unit(sb, sa, k00, k01, qa0, qa1, oa, la, v11, ob, lb, v10, [&]() {
    v00 = rd(sl0, 2048); v01 = rd(sl0, 3072); f0 = *LDS_AT(const volatile unsigned, lbase + FLG); SB(); arrive_w(); SB(); });
  check(5u);
  sl0 = sptr(3);
  SB();
  three(1, true);
#pragma unroll 1
  for (int st = 2; st < 16; st += 2) { fourth(st - 1, true); three(st, false); fourth(st, false); three(st + 1, true); }
  SAW8(7);
  unsigned vr, tk, tv;
  u32x4 f4;
  auto table = [&]() {
    f4 = lds_rd4(lbase + FLG);
    k00 = *LDS_AT(const volatile s16x8, tk); k01 = *LDS_AT(const volatile s16x8, tk + 32);
    vr = *LDS_AT(const volatile u16, tv);
  };
  unsigned xw, xm;
  u32x2 ml;
  u32x4 o2[4];
  auto take = [&]() {
    ml = *LDS_AT(const volatile u32x2, xm);
#pragma unroll
    for (int e = 0; e < 4; ++e) o2[e] = lds_rd4(xw + e * 1024);
  };
  float pzt;
  int dso;
  f32x16& sc = sb;
#define EXP() do { asm volatile("" : "+v"(sc)); SB(); } while (0)
  oa = SMF32(v11, PHI(sa), oa); SB();
  tk = lbase + TAB + 128 + hh * 64 + (lane >> 5) * 16; tv = lbase + TAB + 256 + hh * 64 + (lane & 31) * 2;
  table();
  SB();
  EX(0); EX(1); EXP();
  la = SMF16(sel, PHI(sa), la); SB();
  EX(2); EX(3); EX(4); EXP();
  xw = wbase + (qg ? QOFF : 6 * ASB); xm = lbase + XML + wid * 512 + lane * 8;
  pzt = __builtin_amdgcn_exp2f(nn[0] + (zoff - 8.f));
  asm volatile("" : "+v"(xw), "+v"(xm), "+v"(pzt), "+v"(f4));
  SB();
  {
    const unsigned ph = LAY2 ? 0x79797979u : 0x80808080u, pg = qg ? 0u : 0x80u << (16 * hh + 8);
    auto ready = [&]() {
      return (((__builtin_amdgcn_readfirstlane(f4[1]) + ph) & 0x80808080u) == 0x80808080u) & (((__builtin_amdgcn_readfirstlane(g ? f4[3] : f4[2]) + 0x71717171u) & pg) == pg);
    };
    if (__builtin_expect(!ready(), 0)) {
#pragma unroll 1
      for (int i = 0; i < SPIN_MAX; ++i) {
        __builtin_amdgcn_s_sleep(1);
        table();
        if (ready()) break;
      }
    }
    asm volatile("" ::: "memory");
  }
  SB();
  sa = MFMA32(k00, qb0, nn); asm volatile("" : "+v"(sa)); SB();
#pragma unroll
  for (int e = 0; e < 4; ++e) *LDS_AT(volatile f32x4, xw + e * 1024) = f32x4{oa[4 * e], oa[4 * e + 1], oa[4 * e + 2], oa[4 * e + 3]};
  *LDS_AT(volatile u32x2, xm) = u32x2{__float_as_uint(ma), f2word(pzt <= la[0] ? la[0] : 0.f)};
  SB();
  EX(5); EX(6); EX(7); EXP();
  sa = MFMA32(k01, qb1, sa); asm volatile("" : "+v"(sa), "+v"(vr)); SB();
  ARGS_HERE;
  EX(8); EX(9); EX(10); EXP();
  ob = SMF32(v10, PLO(sb), ob); SB();
  EX(11); EX(12); EX(13); EXP();
  lb = SMF16(sel, PLO(sb), lb); SB();
  const bool used = !LAY2 || (qt + g) * 32 + (lane & 31) != idx;
  dso = used ? (((qt + g) * 32 + head) * 1024 + lane * 8) * 2 : S_ * D_ * 2;
  xw += g ? -4096 : 4096; xm += g ? -2048 : 2048;
  asm volatile("" : "+v"(dso), "+v"(xw), "+v"(xm));
  SB();
  EX(14); EX(15); EXP();
  take();
  SB();
  lb = SMF16(sel, PHI(sb), lb); SB();
  ob = SMF32(v11, PHI(sb), ob); SB();
  __builtin_amdgcn_s_setprio(EPRIO);
  TE(0, 0); TE(1, 0); TE(2, 0); TE(3, 0); TE(7, 0);
  float lsb = lb[0];
  if (LAY2) {
    sa[0] = __builtin_amdgcn_exp2f(sa[0]);
    ob = SMF32((u32x4{lane < 32 ? vr : 0u, 0u, 0u, 0u}), PLO(sa), ob);
    lsb += __uint_as_float(__float_as_uint(sa[0]) & 0xffff0000u);
    lsb = pzt <= lsb ? lsb : 0.f;
  }
  const __amdgpu_buffer_rsrc_t rso = __builtin_amdgcn_make_buffer_rsrc(Of, 0, S_ * D_ * 2, 0x00020000);
  TE(0, 1);
  if (__builtin_amdgcn_ballot_w64(ml[1] == SENT) != 0ull) {
#pragma unroll 1
    for (int i = 0; i < SPIN_MAX; ++i) {
      __builtin_amdgcn_s_sleep(XSLP);
      if (!__builtin_amdgcn_ballot_w64(*LDS_AT(const volatile unsigned, xm + 4) == SENT)) break;
    }
    asm volatile("s_waitcnt lgkmcnt(0)" ::: "memory");
    take();
  }
  arrive();
  TE(0, 2);
  TE(0, 3); TE(1, 1);
  {
    const float m2 = __uint_as_float(ml[0]), l2 = __uint_as_float(ml[1]);
    const float mx = fmaxf(ma, m2);
    const float f1 = __builtin_amdgcn_exp2f(ma - mx), f2 = __builtin_amdgcn_exp2f(m2 - mx);
    const float inv = frcp(lsb * f1 + l2 * f2);
    const float c1 = f1 * inv, c2 = f2 * inv;
    f32x2 r[8];
#pragma unroll
    for (int e = 0; e < 8; ++e) {
      const unsigned x0 = o2[e >> 1][(e & 1) * 2], x1 = o2[e >> 1][(e & 1) * 2 + 1];
      r[e] = f32x2{__uint_as_float(x0) * c2, __uint_as_float(x1) * c2};
    }
#pragma unroll
    for (int e = 0; e < 8; ++e) r[e] = f32x2{ob[2 * e], ob[2 * e + 1]} * f32x2{c1, c1} + r[e];
    auto put = [&]() {
#pragma unroll
      for (int sx = 0; sx < 2; ++sx)
        __builtin_amdgcn_raw_buffer_store_b128(u32x4{pack2(r[4 * sx][0], r[4 * sx][1]), pack2(r[4 * sx + 1][0], r[4 * sx + 1][1]),
            pack2(r[4 * sx + 2][0], r[4 * sx + 2][1]), pack2(r[4 * sx + 3][0], r[4 * sx + 3][1])}, rso, dso + sx * 1024, 0, 16);
    };
    put();
    PIN();
    __builtin_amdgcn_s_setprio(0);
    PIN();
    f32x2 chk = {lsb - __builtin_amdgcn_fmed3f(lsb, 8.6736174e-19f, 1.2676506e30f), l2 - __builtin_amdgcn_fmed3f(l2, 8.6736174e-19f, 1.2676506e30f)};
#pragma unroll
    for (int e = 0; e < 8; ++e) chk = r[e] * f32x2{0.f, 0.f} + chk;
    if (__builtin_expect(__builtin_amdgcn_ballot_w64(used && !(chk[0] + chk[1] == 0.f)) != 0ull, 0)) {
      float l;
      attn_safe(KV, head * 64, qb0, qb1, ob, l, LAY2 ? head * 64 + (idx >> 5) : -1, idx & 31, lbase + TAB + 128 + hh * 64);
      const float inv2 = 1.0f / l;
#pragma unroll
      for (int e = 0; e < 8; ++e) r[e] = f32x2{ob[2 * e] * inv2, ob[2 * e + 1] * inv2};
      asm volatile("s_waitcnt vmcnt(0)" ::: "memory");
      put();
    }
    unsigned pp = 0u;
    if (LAY2) {
      const unsigned ua = lbase + UGL + hh * 128 + (lane >> 5) * 16;
      f32x2 du = {0.f, 0.f}, dn = {0.f, 0.f};
#pragma unroll
      for (int q = 0; q < 4; ++q) {
        const f32x4 uu = *LDS_AT(const f32x4, ua + q * 32), gg = *LDS_AT(const f32x4, ua + 256 + q * 32);
        du = r[2 * q] * f32x2{uu[0], uu[1]} + du; du = r[2 * q + 1] * f32x2{uu[2], uu[3]} + du;
        const f32x2 t0 = r[2 * q] * f32x2{gg[0], gg[1]}, t1 = r[2 * q + 1] * f32x2{gg[2], gg[3]};
        dn = t0 * t0 + dn; dn = t1 * t1 + dn;
      }
      const auto a = __builtin_amdgcn_permlane32_swap(__float_as_uint(du[0] + du[1]), __float_as_uint(du[0] + du[1]), false, false);
      const auto c = __builtin_amdgcn_permlane32_swap(__float_as_uint(dn[0] + dn[1]), __float_as_uint(dn[0] + dn[1]), false, false);
      pp = f2word(__uint_as_float(pack2(__uint_as_float(a[0]) + __uint_as_float(a[1]), __uint_as_float(c[0]) + __uint_as_float(c[1]))));
    }
    __builtin_amdgcn_raw_buffer_store_b32(pp, __builtin_amdgcn_make_buffer_rsrc(hand + 2 * HAND_N + PP_O, 0, LAY2 ? H_ * S_ * 4 : 0, 0x00020000),
                                          lane < 32 ? (head * S_ + (qt + g) * 32 + lane) * 4 : H_ * S_ * 4, 0, 16);
    PIN();
  }
  TE(1, 2);
  u32x4 xres[2];
  oproj_tail(LAY2, tid, lbase, lbase + FLG, omi, oni, idx, gran + GR_O + ((LAY2 ? 16 : 0) + tmi) * (64 * RO) + tni,
             gran + GR_X + omi * (128 * RX) + oni * 4, tag, Of, xout, A, w_r, stats_out, stats, hand, xres TEPASS);
  if (LAY2) {
    const int row = omi * 128 + (wid & 3) * 32 + (wid >> 2) * 16 + (lane >> 3), col = oni * 64 + (lane & 7) * 8;
    unsigned md[2], best = 0u;
    const __amdgpu_buffer_rsrc_t rsk = __builtin_amdgcn_make_buffer_rsrc(hand + HAND_N, 0, (HAND_N + PS_N) * 4, 0x00020000);
    f32x2 xf[2][4];
#pragma unroll
    for (int p = 0; p < 2; ++p)
#pragma unroll
      for (int e = 0; e < 4; ++e) {
        xf[p][e] = f32x2{__uint_as_float(xres[p][e] << 16), __uint_as_float(xres[p][e] & 0xffff0000u)};
        asm volatile("" : "+v"(xf[p][e]));
      }
    LOGDEF;
    LOG();
    const bool rtr = (oni & 7) == 7;
    {
      const int rl = (wid & 3) * 32 + (wid >> 2) * 16 + (lane & 15), rr = omi * 128 + rl;
      int src = (HAND_N + ((omi * 16 + (lane >> 4) * 4) * 128 + rl) * 4) * 4;
      int ko = rtr && (wid >> 2) == (oni >> 3) && lane < NREP ? lane * 1024 + (rr >> 4) * 8 : (HAND_N + PS_N) * 4;
      int bp = (lane >> 3) * 4;
      const float wsum = *LDS_AT(const volatile float, lbase + SPM + 132);
      asm volatile("" : "+v"(src), "+v"(ko), "+v"(bp));
      u32x4 v[4];
      __builtin_amdgcn_s_sleep(MPRE);
#pragma unroll 1
      for (int spin = 0; spin < SPIN_MAX; ++spin) {
        LOG();
#pragma unroll
        for (int i = 0; i < 4; ++i) v[i] = __builtin_amdgcn_raw_buffer_load_b128(rsk, src + (i & 1) * 2048, (i >> 1) * 4096, 16);
        unsigned mx = 0u;
#pragma unroll
        for (int i = 0; i < 4; ++i) mx = umax(umax(umax(mx, v[i][0]), v[i][1]), v[i][2]);
        if (none(mx == SENT)) break;
        __builtin_amdgcn_s_sleep(MSLP);
        asm volatile("" ::: "memory");
      }
      LOGAT(12);
      if (rtr) __builtin_amdgcn_s_setprio(3); else __builtin_amdgcn_s_setprio(LNPRIO);
      double sm[3];
#pragma unroll
      for (int c = 0; c < 3; ++c) {
        const double a = ((double)__uint_as_float(v[0][c]) + (double)__uint_as_float(v[1][c])) + ((double)__uint_as_float(v[2][c]) + (double)__uint_as_float(v[3][c]));
        u32x2 q = __builtin_bit_cast(u32x2, a);
        const auto lo = __builtin_amdgcn_permlane32_swap(q[0], q[0], false, false), hi = __builtin_amdgcn_permlane32_swap(q[1], q[1], false, false);
        q = __builtin_bit_cast(u32x2, __builtin_bit_cast(double, u32x2{lo[0], hi[0]}) + __builtin_bit_cast(double, u32x2{lo[1], hi[1]}));
        const auto l2 = __builtin_amdgcn_permlane16_swap(q[0], q[0], false, false), h2 = __builtin_amdgcn_permlane16_swap(q[1], q[1], false, false);
        sm[c] = __builtin_bit_cast(double, u32x2{l2[0], h2[0]}) + __builtin_bit_cast(double, u32x2{l2[1], h2[1]});
      }
      float mean, var, mb, den;
      ln_from_sums(sm[0], sm[1], mean, var, mb, den);
      const unsigned mdr = f2word(__uint_as_float((__float_as_uint(mb) >> 16) | (__float_as_uint(den) & 0xffff0000u)));
      md[0] = __builtin_amdgcn_ds_bpermute(bp, mdr); md[1] = __builtin_amdgcn_ds_bpermute(bp + 32, mdr);
      if (rtr) {
        const float r = (float)fma(-(double)mb, (double)wsum, sm[2]) * frcp(den);
        const unsigned rb = pack2(r, r);
        unsigned rkey = (rb ^ ((unsigned)((int)rb >> 31) | 0x80000000u)) & 0xffff0000u;
        if (r != r) rkey = 0u;
        const unsigned top = wave_umax(rkey | (unsigned)(2047 - rr));
        const unsigned mdt = __builtin_amdgcn_readlane(mdr, (2047 - (int)(top & 0xffffu)) & 15);
        __builtin_amdgcn_raw_buffer_store_b64(u32x2{top, mdt}, rsk, ko, 0, 16);
        PIN();
        __builtin_amdgcn_s_setprio(LNPRIO);
        __hip_atomic_fetch_add(LDS_AT(unsigned, lane == 0 ? lbase + SPM + 128 : lbase + DUM + lane * 4), 1u, __ATOMIC_RELAXED, __HIP_MEMORY_SCOPE_WORKGROUP);
        if (wid == 2 || wid == 5) STAMP(8, 6);
      }
      LOGAT(13);
    }
    auto keys = [&](const u32x4& v) __attribute__((always_inline)) {
      if (none(umax(umax(v[0], v[1]), umax(v[2], v[3])) == SENT)) {
        best = wave_umax(umax(v[0], v[2])) | 0x8000u;
        *LDS_AT(volatile u32x2, lbase + FLG + 40) = u32x2{best, (unsigned)__builtin_amdgcn_readlane(v[0] > v[2] ? v[1] : v[3], ((2047 - (int)(best & 0x7ffu)) >> 5) & 63)};
        STAMP(8, 11);
      }
    };
    auto ln_rows = [&]() __attribute__((always_inline)) {
#pragma unroll
      for (int p = 0; p < 2; ++p) {
        const float mb_ = __uint_as_float(md[p] << 16), rin = frcp(__uint_as_float(md[p] & 0xffff0000u));
#pragma unroll
        for (int e = 0; e < 4; ++e) {
          const f32x2 h = (xf[p][e] - f32x2{mb_, mb_}) * f32x2{rin, rin} + xf[p][e];
          xres[p][e] = pack2(h[0], h[1]);
        }
      }
      asm volatile("" : "+v"(xres[0]), "+v"(xres[1]));
    };
    if (wid == 0) STAMP(8, 12);
    ln_rows();
    LOGAT(14);
    const __amdgpu_buffer_rsrc_t rsu = __builtin_amdgcn_make_buffer_rsrc(out, 0, S_ * D_ * 2, 0x00020000);
    unsigned fr1 = SENT;
#pragma unroll 1
    for (int i = 0; i < 4 * SPIN_MAX && fr1 == SENT; ++i) fr1 = __builtin_amdgcn_readfirstlane(*LDS_AT(const volatile unsigned, lbase + SPM + 144));
    fr1 &= 0x7ffu;
    unsigned tmx = __builtin_amdgcn_readfirstlane(*LDS_AT(const volatile unsigned, lbase + SPM + 136)) + KDT;
    if (tmx - (unsigned)__builtin_amdgcn_s_memrealtime() > 1024u) tmx = 0u;
    auto due = [&]() { return tmx == 0u || (int)((unsigned)__builtin_amdgcn_s_memrealtime() - tmx) >= 0; };
    lk_set(15);
    if (wid == 6) {
      if (rtr) {
#pragma unroll 1
        for (int i = 0; i < SPIN_MAX; ++i) {
          if (__builtin_amdgcn_readfirstlane(*LDS_AT(const volatile unsigned, lbase + SPM + 128)) == 7u) break;
          __builtin_amdgcn_s_sleep(1);
        }
        asm volatile("" ::: "memory");
      }
    }
#pragma unroll 1
    for (int i = 0; i < OSLP && !best; ++i) {
      if (wid == 6 && due()) {
        LOG();
        keys(__builtin_amdgcn_raw_buffer_load_b128(rsk, (bid % NREP) * 1024 + lane * 16, 0, 16));
        i += 2;
      } else {
        if (__builtin_amdgcn_readfirstlane(*LDS_AT(const volatile unsigned, lbase + FLG + 40))) break;
        __builtin_amdgcn_s_sleep(2);
      }
      asm volatile("" ::: "memory");
    }
    u32x4 kv = {SENT, SENT, SENT, SENT};
    if (wid == 6 && !best && due()) kv = __builtin_amdgcn_raw_buffer_load_b128(rsk, (bid % NREP) * 1024 + lane * 16, 0, 16);
    PIN();
#pragma unroll
    for (int p = 0; p < 2; ++p) __builtin_amdgcn_raw_buffer_store_b128(xres[p], rsu, row + 8 * p != (int)fr1 ? ((row + 8 * p) * D_ + col) * 2 : S_ * D_ * 2, 0, OAUX);
    PIN();
    if (wid == 6 && !best) { keys(kv); while (!due()) __builtin_amdgcn_s_sleep(1); }
#pragma unroll 1
    for (int i = 0; i < 4 * SPIN_MAX && !best; ++i) {
      if (wid == 6) {
        LOG();
        keys(__builtin_amdgcn_raw_buffer_load_b128(rsk, (bid % NREP) * 1024 + lane * 16, 0, 16));
        if (!best) __builtin_amdgcn_s_sleep(KSLP);
      } else {
        best = __builtin_amdgcn_readfirstlane(*LDS_AT(const volatile unsigned, lbase + FLG + 40));
        if (!best) __builtin_amdgcn_s_sleep(1);
      }
      asm volatile("" ::: "memory");
    }
    const int idx2 = 2047 - (int)(best & 0x7ffu);
    if (__builtin_expect(idx2 != (int)fr1, 0)) {
      const __amdgpu_buffer_rsrc_t rsf = __builtin_amdgcn_make_buffer_rsrc(gran + GR_F, 0, 48 * 128, 0x00020000);
      if (!none((row | 8) == (idx2 | 8))) {
#pragma unroll
        for (int p = 0; p < 2; ++p)
          __builtin_amdgcn_raw_buffer_store_b128(u32x4{pack2(xf[p][0][0], xf[p][0][1]), pack2(xf[p][1][0], xf[p][1][1]), pack2(xf[p][2][0], xf[p][2][1]), pack2(xf[p][3][0], xf[p][3][1])},
              __builtin_amdgcn_make_buffer_rsrc(xout, 0, 3 * D_ * 2, 0x00020000), row + 8 * p == idx2 ? (2 * D_ + col) * 2 : S_ * D_ * 2, 0, 16);
        asm volatile("s_waitcnt vmcnt(0)" ::: "memory");
        __builtin_amdgcn_raw_buffer_store_b32(tag, rsf, lane == 0 ? oni * 128 : 48 * 128, 0, 16);
      }
      if (!none((row | 8) == ((int)fr1 | 8))) {
#pragma unroll 1
        for (int spin = 0; spin < SPIN_MAX; ++spin) {
          if (none(__builtin_amdgcn_raw_buffer_load_b32(rsf, (16 + oni * 2 + (lane & 1)) * 128, 0, 16) != tag)) break;
          asm volatile("" ::: "memory");
        }
#pragma unroll
        for (int p = 0; p < 2; ++p) __builtin_amdgcn_raw_buffer_store_b128(xres[p], rsu, row + 8 * p == (int)fr1 ? ((row + 8 * p) * D_ + col) * 2 : S_ * D_ * 2, 0, OAUX);
      }
    }
  }
  }
}
struct Args2 { Args l[2]; };
extern "C" __global__ __launch_bounds__(768, 3) void layers_kernel(const Args2 args) {
  __shared__ __attribute__((aligned(128))) char smem[163840];
  const unsigned long long t_entry = 0;
  const int tid = threadIdx.x, bid = blockIdx.x;
  int wid = __builtin_amdgcn_readfirstlane(tid >> 6);
  asm volatile("" : "+s"(wid));
  const ArgP ap = (ArgP)__builtin_amdgcn_kernarg_segment_ptr();
  layer_body(wid, bid, (unsigned)(uintptr_t)smem, ap, t_entry);
  LAYER_HERE;
  if (!LAY2) {
    asm volatile(AGAIN :: "{s[0:1]}"(ap + 1), "{s2}"(bid), "{v0}"(wid * 64) : "s90", "s91", "scc", "memory");
    __builtin_unreachable();
  }
}

extern "C" __global__ void end_marker_kernel() {}

extern "C" void kernel_launch(void* const* d_in, const int* in_sizes, int n_in, void* d_out, int out_size,
                              void* d_ws, size_t ws_size, hipStream_t stream) {
  const u16* x0 = (const u16*)d_in[0];
  const u16* wqkv = (const u16*)d_in[1];
  const u16* wo = (const u16*)d_in[2];
  const u16* w_r = (const u16*)d_in[4];
  const u16* wg = (const u16*)d_in[5];
  const u16* wu = (const u16*)d_in[6];
  const u16* wd = (const u16*)d_in[7];
  u16* out = (u16*)d_out;

  char* ws = (char*)d_ws;
  const size_t MB4 = (size_t)S_ * D_ * 2;
  u16* kv1 = (u16*)(ws);
  u16* kv2 = (u16*)(ws + 2 * MB4);
  u16* o1 = (u16*)(ws + 4 * MB4);
  u16* x1 = (u16*)(ws + 5 * MB4);
  unsigned* hand = (unsigned*)(ws + 7 * MB4);
  u16* x3 = (u16*)(hand + HAND_N + GRAN_N + Y_N);
  double* stats = (double*)(ws + 8 * MB4);
  unsigned* gran = (unsigned*)(ws + 9 * MB4);
  float* csf = (float*)(ws + 9 * MB4 + (1 << 20));
  u16* o2 = (u16*)(ws + 10 * MB4);
  u16* wc = (u16*)((((uintptr_t)ws + 11 * MB4 + 2047) & ~(uintptr_t)2047) + (((uintptr_t)wqkv + 128) & 2047));
  Args2 a;
  for (int l = 0; l < 2; ++l) {
    Args& r = a.l[l];
    r.lay = l; r.a = l ? x1 : x0; r.kv = l ? kv2 : kv1; r.o = l ? o2 : o1; r.xo = l ? x3 : x1; r.so = l ? stats + STAT_N : stats;
    r.W = wqkv; r.wc = wc; r.wg = wg; r.wu = wu; r.wd = wd; r.wo = wo; r.w_r = w_r; r.gran = gran; r.csf = csf; r.stats = stats; r.hand = hand; r.out = out;
  }
  layers_kernel<<<256, 768, 0, stream>>>(a);
}
```

```cpp
#include <hip/hip_runtime.h>
#include <hip/hip_bf16.h>
#include <stdint.h>
#include <type_traits>
#include <utility>

typedef unsigned short u16;
typedef short s16x4 __attribute__((ext_vector_type(4)));
typedef short s16x8 __attribute__((ext_vector_type(8)));
typedef float f32x16 __attribute__((ext_vector_type(16)));
typedef float f32x4 __attribute__((ext_vector_type(4)));
typedef unsigned int u32x4 __attribute__((ext_vector_type(4)));
typedef unsigned int u32x2 __attribute__((ext_vector_type(2)));
typedef __bf16 bf16x8 __attribute__((ext_vector_type(8)));
typedef __bf16 bf16x16 __attribute__((ext_vector_type(16)));

__device__ __forceinline__ int fresh_lane() {
  int l;
  asm volatile("v_mbcnt_lo_u32_b32 %0, -1, 0\n\tv_mbcnt_hi_u32_b32 %0, -1, %0" : "=v"(l));
  return l;
}
#define STAMP(k, i)
#define STAMPV(k, i, v)
#define LOGDEF
#define LOG()
#define LOGAT(k)
#define lk_set(k)
#define LOGOUT(slot)
#define TE(grp, k)
#define TEARG
#define TEPASS
constexpr int S_ = 2048, D_ = 1024, H_ = 32, F_ = 3584;

__device__ __forceinline__ int kpos(int r) { return (r & ~12) | ((r & 4) << 1) | ((r & 8) >> 1); }
__device__ __forceinline__ float bf2f(u16 v) { return __uint_as_float(((unsigned)v) << 16); }
__device__ __forceinline__ u16 f2bf(float f) {
  __hip_bfloat16 b = __float2bfloat16(f);
  return *reinterpret_cast<u16*>(&b);
}
typedef float f32x2 __attribute__((ext_vector_type(2)));
typedef __bf16 bf16x2 __attribute__((ext_vector_type(2)));
__device__ __forceinline__ unsigned pack2(float lo, float hi) {
  f32x2 v = {lo, hi};
  return __builtin_bit_cast(unsigned, __builtin_convertvector(v, bf16x2));
}
__device__ __forceinline__ float rbf(float f) { return __uint_as_float(pack2(f, f) << 16); }
__device__ __forceinline__ void rbf2(float& a, float& b) {
  const unsigned p = pack2(a, b);
  a = __uint_as_float(p << 16); b = __uint_as_float(p & 0xffff0000u);
}

__device__ __forceinline__ float fadd(float a, float b) { float r = a + b; asm("" : "+v"(r)); return r; }
template <int CTRL, int RMASK>
__device__ __forceinline__ float dpp_add(float v) {
  const int t = __builtin_amdgcn_update_dpp(0, __float_as_int(v), CTRL, RMASK, 0xf, false);
  return v + __int_as_float(t);
}
__device__ __forceinline__ float wave_sum(float v) {
  v = dpp_add<0x111, 0xf>(v);
  v = dpp_add<0x112, 0xf>(v);
  v = dpp_add<0x114, 0xf>(v);
  v = dpp_add<0x118, 0xf>(v);
  v = dpp_add<0x142, 0xa>(v);
  v = dpp_add<0x143, 0xc>(v);
  return __int_as_float(__builtin_amdgcn_readlane(__float_as_int(v), 63));
}

template <int CTRL, int RMASK>
__device__ __forceinline__ unsigned dpp_umax(unsigned v) {
  const unsigned t = (unsigned)__builtin_amdgcn_update_dpp(0, (int)v, CTRL, RMASK, 0xf, false);
  return v > t ? v : t;
}
__device__ __forceinline__ unsigned wave_umax(unsigned v) {
  v = dpp_umax<0x111, 0xf>(v); v = dpp_umax<0x112, 0xf>(v); v = dpp_umax<0x114, 0xf>(v); v = dpp_umax<0x118, 0xf>(v);
  v = dpp_umax<0x142, 0xa>(v); v = dpp_umax<0x143, 0xc>(v);
  return (unsigned)__builtin_amdgcn_readlane((int)v, 63);
}

#define NREP 32
constexpr unsigned SENT = 0xFFFFFFFFu;
constexpr int GRAN_N = 256 * NREP;
constexpr int Y_N = F_ / 16 * D_;
constexpr int X2_N = D_ / 2;
constexpr int XS_N = 128;
constexpr int P4_N = 3 * D_ * 16;
constexpr int PA_N = H_ * 8 * 64;
constexpr int HAND_N = GRAN_N + Y_N + X2_N + XS_N + P4_N + PA_N;
constexpr int MD_O = 16 * 16 * 128 * 4;
constexpr int PK_O = MD_O;
constexpr int PP_O = PK_O + 128 * NREP;
constexpr int Y3_O = PP_O + H_ * S_;
constexpr int PS_N = Y3_O + Y_N;
constexpr int GR_ROW = 64, GR_COL = GR_ROW + 4 * S_, GR_KV = GR_COL + 16 * 16 * 192;
#define RO 16
#define RK 16
#define RX 1
constexpr int GR_O = GR_KV + 2 * 16 * 32 * RK, GR_X = GR_O + 2 * 16 * 64 * RO, GR_N = GR_X + 16 * 128 * RX;
constexpr int GR_F = GR_N + 256 * 64;
constexpr int STAT_N = 3 * S_;

__device__ __forceinline__ void st_ag(unsigned* p, unsigned v) { __hip_atomic_store(p, v, __ATOMIC_RELAXED, __HIP_MEMORY_SCOPE_AGENT); }
__device__ __forceinline__ unsigned ld_ag(const unsigned* p) { return __hip_atomic_load(p, __ATOMIC_RELAXED, __HIP_MEMORY_SCOPE_AGENT); }
__device__ __forceinline__ double ld_ag(const double* p) { return __hip_atomic_load(p, __ATOMIC_RELAXED, __HIP_MEMORY_SCOPE_AGENT); }
__device__ __forceinline__ unsigned f2word(float f) { const unsigned u = __float_as_uint(f); return u < 0xFFFFFFFEu ? u : 0xFFFFFFFEu; }
constexpr int SPIN_MAX = 1 << 18;
__device__ __forceinline__ unsigned umax(unsigned a, unsigned b) { return a > b ? a : b; }
__device__ __forceinline__ bool none(bool p) { return __builtin_amdgcn_ballot_w64(p) == 0ull; }

__device__ __forceinline__ void ln_from_sums(double s1, double s2, float& mean, float& var, float& mb, float& den) {
  const double md = s1 * (1.0 / 1024.0);
  const double vd = fma(-md, md, s2 * (1.0 / 1024.0));
  mean = (float)md; var = fmaxf((float)vd, 0.f);
  mb = rbf(mean);
  den = rbf(__builtin_amdgcn_sqrtf(rbf(rbf(var) + 1e-5f)));
}
__device__ __forceinline__ float frcp(float x) { return __builtin_amdgcn_rcpf(x); }
__device__ __forceinline__ void ln_apply2(float& a, float& b, float mb, float den, float rin) {
  a -= mb; b -= mb;
  rbf2(a, b);
  float qa = a * rin, qb = b * rin;
  qa = fmaf(fmaf(-qa, den, a), rin, qa);
  qb = fmaf(fmaf(-qb, den, b), rin, qb);
  rbf2(qa, qb);
  a = qa; b = qb;
}

#define CODE_ADDR(SYM, out64)                                                                                          \
  do {                                                                                                                 \
    unsigned lo_, hi_;                                                                                                 \
    asm volatile("s_getpc_b64 s[90:91]\n\ts_add_u32 s90, s90, " SYM "@rel32@lo+4\n\ts_addc_u32 s91, s91, " SYM          \
                 "@rel32@hi+12\n\ts_mov_b32 %0, s90\n\ts_mov_b32 %1, s91"                                             \
                 : "=s"(lo_), "=s"(hi_) :: "s90", "s91", "scc");                                                        \
    out64 = ((unsigned long long)hi_ << 32) | lo_;                                                                     \
  } while (0)

#define QNX 4
constexpr int QMX = 32 / QNX, QNG = 16 / QNX, QMG = 8 / QNG;

__device__ __forceinline__ unsigned touch(const void* p) { return *reinterpret_cast<const unsigned*>(p); }

__device__ __forceinline__ unsigned touch_code(unsigned long long lo, unsigned long long hi, int pc, int lane) {
  const unsigned long long p = lo + (unsigned)(pc * 8192 + lane * 128);
  return p + 4 <= hi ? *reinterpret_cast<__attribute__((address_space(1))) const unsigned*>(p) : 0u;
}

template <bool SC = false>
__device__ __forceinline__ void glds16s(const void* sbase, unsigned voff, unsigned lds_dst) {
  unsigned keep;
  if (SC)
    asm volatile("s_mov_b32 %0, m0\n\ts_mov_b32 m0, %3\n\ts_nop 2\n\tglobal_load_lds_dwordx4 %1, %2 sc1\n\ts_mov_b32 m0, %0"
                 : "=&s"(keep) : "v"(voff), "s"(sbase), "s"(lds_dst) : "memory");
  else
  asm volatile("s_mov_b32 %0, m0\n\ts_mov_b32 m0, %3\n\ts_nop 2\n\tglobal_load_lds_dwordx4 %1, %2\n\ts_mov_b32 m0, %0"
               : "=&s"(keep) : "v"(voff), "s"(sbase), "s"(lds_dst) : "memory");
}
template <bool SC>
__device__ __forceinline__ void glds16x4(const void* sbase, unsigned v0, unsigned v1, unsigned v2, unsigned v3, unsigned lds_dst) {
  unsigned keep;
  if (SC)
    asm volatile("s_mov_b32 %0, m0\n\ts_mov_b32 m0, %6\n\ts_nop 2\n\tglobal_load_lds_dwordx4 %1, %5 sc1\n\tglobal_load_lds_dwordx4 %2, %5 offset:1024 sc1\n\t"
                 "global_load_lds_dwordx4 %3, %5 offset:2048 sc1\n\tglobal_load_lds_dwordx4 %4, %5 offset:3072 sc1\n\ts_mov_b32 m0, %0"
                 : "=&s"(keep) : "v"(v0), "v"(v1), "v"(v2), "v"(v3), "s"(sbase), "s"(__builtin_amdgcn_readfirstlane(lds_dst)) : "memory");
  else
    asm volatile("s_mov_b32 %0, m0\n\ts_mov_b32 m0, %6\n\ts_nop 2\n\tglobal_load_lds_dwordx4 %1, %5\n\tglobal_load_lds_dwordx4 %2, %5 offset:1024\n\t"
                 "global_load_lds_dwordx4 %3, %5 offset:2048\n\tglobal_load_lds_dwordx4 %4, %5 offset:3072\n\ts_mov_b32 m0, %0"
                 : "=&s"(keep) : "v"(v0), "v"(v1), "v"(v2), "v"(v3), "s"(sbase), "s"(__builtin_amdgcn_readfirstlane(lds_dst)) : "memory");
}
constexpr float QSCALE = 0.17677669529663687f * 1.4426950408889634f;

__device__ __forceinline__ unsigned min4(u32x4 v) { const unsigned a = v[0] < v[1] ? v[0] : v[1], b = v[2] < v[3] ? v[2] : v[3]; return a < b ? a : b; }
#define LDS_AT(T, a) ((__attribute__((address_space(3))) T*)(uintptr_t)(a))
__device__ __forceinline__ u32x4 lds_rd4(unsigned a) { return *LDS_AT(const volatile u32x4, a); }
__device__ __forceinline__ void lds_wr(unsigned a, unsigned v) { *LDS_AT(volatile unsigned, a) = v; }
__device__ __forceinline__ void split3(float v, unsigned& p0, unsigned& p1, unsigned& p2) {
  p0 = pack2(v, v) & 0xffffu; v -= __uint_as_float(p0 << 16);
  p1 = pack2(v, v) & 0xffffu; v -= __uint_as_float(p1 << 16);
  p2 = pack2(v, v) & 0xffffu;
}

#define PIN() __builtin_amdgcn_sched_barrier(0)
template <int B, class F, int... I>
__device__ __forceinline__ void static_for_(F&& f, std::integer_sequence<int, I...>) { (f(std::integral_constant<int, B + I>{}), ...); }
template <int B, int E, class F>
__device__ __forceinline__ void static_for(F&& f) { static_for_<B>(f, std::make_integer_sequence<int, E - B>{}); }

#define TPRE 60
#define TPSL 0
#define FCPRE 44
#define FCSLP 2
#define SIGAT 24
#define XPRIO 1
#define OPD 6
#define LAUX 16
#define CSLP 2
#define EPRIO 3
#define OPRIO 1
#define XSLP 1
#define CSL1 8
#define XAUX 16
__device__ __forceinline__ void oproj_tail(const bool RES2, const int tid, const unsigned lbase, const unsigned flg, const int tmi, const int tni, const int idx,
                                           unsigned* osig, unsigned* xsig, const unsigned tag,
                                           const u16* A, u16* out,
                                           const u16* res, const u16* __restrict__ w_r, double* stats,
                                           const double* stats_in, unsigned* hand, u32x4 (&xres)[2] TEARG) {
  constexpr int ldw = D_, PD = OPD;
  const int lane = tid & 63, wid = __builtin_amdgcn_readfirstlane(tid >> 6);
  const int m0 = tmi * 128, n0 = tni * 64;
  const int dk = RES2 ? 7 : 3;
  if (wid == 0) STAMP(dk, 0);
  TE(1, 3);
  asm volatile("" : "+s"(A) :: "memory");
  PIN();
  const int row = m0 + (wid & 3) * 32 + (wid >> 2) * 16 + (lane >> 3), col = n0 + (lane & 7) * 8;
  const unsigned tb = lbase + 147456;
  typedef __attribute__((address_space(1))) double* gdp;
  typedef __attribute__((address_space(1))) unsigned* gup;
  unsigned tr, oad;
  gdp pst;
  gup pps;
  int xo;
  auto setup = [&]() __attribute__((always_inline)) {
    tr = tb + (row - m0) * 128 + (((lane ^ (lane >> 3)) & 7) << 4);
    pst = (gdp)(stats + ((lane & 3) == 3 ? 0 : (lane & 3)) * S_ + row + 8 * ((lane >> 2) & 1));
    pps = (gup)(hand + 2 * HAND_N + ((tmi * 16 + tni) * 128 + row - m0 + 8 * ((lane >> 2) & 1)) * 4 + (lane & 3));
    xo = (row * ldw + col) * 2;
    asm volatile("" : "+v"(tr), "+v"(pst), "+v"(pps), "+v"(xo));
    PIN();
  };
  {
    oad = lane == 0 ? flg + 24 : flg + 64 + lane * 4;
    gup ps = (gup)(osig + lane * 64);
    asm volatile("" : "+v"(oad), "+v"(ps));
    asm volatile("s_waitcnt vmcnt(1)" ::: "memory");
    TE(2, 1);
    const unsigned old = __hip_atomic_fetch_add(LDS_AT(unsigned, oad), 1u, __ATOMIC_RELAXED, __HIP_MEMORY_SCOPE_WORKGROUP);
    if (__builtin_amdgcn_readfirstlane(old) == 7u && lane < RO) __hip_atomic_store(ps, tag, __ATOMIC_RELAXED, __HIP_MEMORY_SCOPE_AGENT);
    PIN();
    TE(2, 2);
    if (wid == 0) STAMP(dk, 12);
  }
  auto announce = [&]() __attribute__((always_inline)) {
    asm volatile("" ::: "memory");
    PIN();
    __builtin_amdgcn_s_barrier();
    PIN();
    if (wid == 0) STAMP(dk, 1);
    TE(2, 3); TE(4, 0); TE(7, 1);
  };
  u32x4 rwr;
  auto sums = [&](const u32x4& x0, const u32x4& x1) __attribute__((always_inline)) {
    float f[2][3];
#pragma unroll
    for (int p = 0; p < 2; ++p) {
      f[p][0] = f[p][1] = f[p][2] = 0.f;
#pragma unroll
      for (int e = 0; e < 4; ++e) {
        const unsigned xu = p ? x1[e] : x0[e], wu = rwr[e];
        const bf16x2 x = __builtin_bit_cast(bf16x2, xu);
        f[p][0] = __builtin_amdgcn_fdot2_f32_bf16(x, __builtin_bit_cast(bf16x2, 0x3f803f80u), f[p][0], false);
        f[p][1] = __builtin_amdgcn_fdot2_f32_bf16(x, x, f[p][1], false);
        f[p][2] = __builtin_amdgcn_fdot2_f32_bf16(x, __builtin_bit_cast(bf16x2, wu), f[p][2], false);
      }
    }
    auto addx = [&](float keep, float give, auto ctl) {
      return keep + __int_as_float(__builtin_amdgcn_update_dpp(0, __float_as_int(give), decltype(ctl)::value, 0xf, 0xf, false));
    };
    const bool b2 = lane & 4, b1 = lane & 2, z0 = (lane & 3) == 0, z1 = (lane & 3) == 1;
    float g[3];
#pragma unroll
    for (int c = 0; c < 3; ++c) g[c] = addx(b2 ? f[1][c] : f[0][c], b2 ? f[0][c] : f[1][c], std::integral_constant<int, 0x141>{});
    const float u1 = addx(b1 ? g[2] : g[0], b1 ? g[0] : g[2], std::integral_constant<int, 0x4e>{});
    const float u2 = addx(g[1], g[1], std::integral_constant<int, 0x4e>{});
    return addx(z1 ? u2 : u1, z0 ? u2 : u1, std::integral_constant<int, 0xb1>{});
  };
  if (wid < 4) {
    const __amdgpu_buffer_rsrc_t rsa = __builtin_amdgcn_make_buffer_rsrc(const_cast<u16*>(A), 0, S_ * D_ * 2, 0x00020000);
    int sa0 = (tmi * 4 + wid) * 65536, va0 = (lane >> 4) * 512 + (lane & 15) * 16;
    asm volatile("" : "+v"(va0));
    asm volatile("" : "+s"(sa0));
    auto lda = [&](int i, int h) { return __builtin_amdgcn_raw_buffer_load_b128(rsa, va0 + (i & 1) * 2048 + h * 256, sa0 + (i >> 1) * 4096, LAUX); };
    unsigned wad[4], tw[4];
    f32x4 acc[2][4];
    const int k = (lane >> 5) * 16 + ((lane >> 2) & 7);
#pragma unroll
    for (int j = 0; j < 4; ++j) {
      wad[j] = lbase + k * 128 + (((2 * j) ^ (k & 6)) << 4) + 8 * (lane & 3);
      tw[j] = tb + wid * 4096 + (lane & 15) * 128 + (((2 * j + (lane >> 5)) ^ (lane & 7)) << 4) + ((lane >> 4) & 1) * 8;
      asm volatile("" : "+v"(wad[j]), "+v"(tw[j]));
    }
    auto ldw_ = [&](int i, int j) {
      const unsigned a = wad[j] + (i & 3) * 4096;
      s16x4 lo = __builtin_amdgcn_ds_read_tr16_b64_v4i16(LDS_AT(s16x4, a));
      s16x4 h4 = __builtin_amdgcn_ds_read_tr16_b64_v4i16(LDS_AT(s16x4, a + 1024));
      return (s16x8)__builtin_shufflevector(lo, h4, 0, 1, 2, 3, 4, 5, 6, 7);
    };
    u32x4 id[2];
#pragma unroll
    for (int j = 0; j < 2; ++j) {
#pragma unroll
      for (int e = 0; e < 4; ++e) id[j][e] = (lane >> 4) == 2 * j + ((lane >> 3) & 1) && ((lane & 7) >> 1) == e ? ((lane & 1) ? 0x3f800000u : 0x3f80u) : 0u;
      asm volatile("" : "+v"(id[j]));
    }
    unsigned rad = tb + wid * 4096 + (lane & 15) * 128 + (((lane >> 4) ^ (lane & 7)) << 4), rfa = flg + 36 + wid;
    asm volatile("" : "+v"(rad), "+v"(rfa));
    u32x4 rb[2][2];
    rwr = *reinterpret_cast<const u32x4*>(w_r + col);
    setup();
    asm volatile("" : "+v"(rwr));
    announce();
    u32x4 af[PD][2];
    s16x8 wf[2][4];
#pragma unroll
    for (int i = 0; i < PD; ++i) { af[i][0] = lda(i, 0); af[i][1] = lda(i, 1); }
    PIN();
    __builtin_amdgcn_s_setprio(OPRIO);
#pragma unroll
    for (int q = 0; q < 4; ++q) rb[q >> 1][q & 1] = lds_rd4((rad ^ ((q & 1) * 64)) + (q >> 1) * 2048);
    PIN();
#pragma unroll
    for (int j = 0; j < 4; ++j)
#pragma unroll
      for (int h = 0; h < 2; ++h) { asm volatile("v_mfma_f32_16x16x32_bf16 %0, %1, %2, 0" : "=&v"(acc[h][j]) : "v"(id[j & 1]), "v"(rb[h][j >> 1])); PIN(); }
#pragma unroll 1
    for (int spin = 0; spin < SPIN_MAX; ++spin)
      if (__builtin_amdgcn_readfirstlane(*LDS_AT(const volatile unsigned, flg)) >= 40u) break;
    asm volatile("" : "+v"(va0) :: "memory");
    PIN();
#pragma unroll
    for (int j = 0; j < 4; ++j) wf[0][j] = ldw_(0, j);
    s16x4 wlo;
    static_for<0, 32>([&](auto ic) __attribute__((always_inline)) {
      constexpr int i = decltype(ic)::value;
      if (i == SIGAT) { __hip_atomic_fetch_add(LDS_AT(unsigned, oad + 4), 1u, __ATOMIC_RELAXED, __HIP_MEMORY_SCOPE_WORKGROUP); PIN(); }
#pragma unroll
      for (int g = 0; g < 8; ++g) {
        const int j = g >> 1, h = g & 1;
        asm volatile("v_mfma_f32_16x16x32_bf16 %0, %1, %2, %0" : "+v"(acc[h][j]) : "v"(wf[i & 1][j]), "v"(af[i % PD][h]));
        PIN();
        if (i == 15 && !(g & 1)) { wad[g >> 1] += 65536; asm volatile("" : "+v"(wad[g >> 1])); }
        if (i + 1 < 32) {
          const s16x4 v = __builtin_amdgcn_ds_read_tr16_b64_v4i16(LDS_AT(s16x4, wad[g >> 1] + ((i + 1) & 15) * 4096 + (g & 1) * 1024));
          if (g & 1) wf[(i + 1) & 1][g >> 1] = (s16x8)__builtin_shufflevector(wlo, v, 0, 1, 2, 3, 4, 5, 6, 7);
          else wlo = v;
          PIN();
        }
        if (g >= 6 && i + PD < 32) { af[i % PD][h] = lda(i + PD, h); PIN(); }
        if (i + 1 < 32 && i != SIGAT && i + 1 != SIGAT) {
          if (g == 3) { __builtin_amdgcn_s_waitcnt(0xc47f); PIN(); }
          if (g == 7 && i + PD < 32) { __builtin_amdgcn_s_waitcnt(0x0470 | (2 * PD - 2)); PIN(); }
          if (g == 7 && i + PD >= 32) { __builtin_amdgcn_s_waitcnt(0xc47f); PIN(); }
        }
      }
    });
    asm volatile("s_nop 3" : "+v"(acc[0][0]), "+v"(acc[1][0]));
    TE(4, 1); TE(7, 2);
    if (wid == 0) STAMP(dk, 17);
    if (wid == 1 && !RES2) STAMP(4, 0);
#pragma unroll
    for (int j = 0; j < 4; ++j)
#pragma unroll
      for (int h = 0; h < 2; ++h) {
        *LDS_AT(u32x2, tw[j] + h * 2048) = u32x2{pack2(acc[h][j][0], acc[h][j][1]), pack2(acc[h][j][2], acc[h][j][3])};
        PIN();
      }
    asm volatile("" ::: "memory");
    *LDS_AT(volatile unsigned char, rfa) = (unsigned char)2;
#pragma unroll
    for (int p = 0; p < 2; ++p) xres[p] = lds_rd4(tr + p * 1024);
    PIN();
    if (!RES2) {
      const __amdgpu_buffer_rsrc_t rso = __builtin_amdgcn_make_buffer_rsrc(out, 0, S_ * D_ * 2, 0x00020000);
      const __amdgpu_buffer_rsrc_t rsf = __builtin_amdgcn_make_buffer_rsrc(xsig + wid, 0, RX * 512, 0x00020000);
      const u32x4 x2 = lds_rd4(tr + 2048), x3 = lds_rd4(tr + 3072);
      PIN();
#pragma unroll
      for (int p = 0; p < 4; ++p) { __builtin_amdgcn_raw_buffer_store_b128(p < 2 ? xres[p] : (p < 3 ? x2 : x3), rso, xo + p * (8 * ldw * 2), 0, XAUX); PIN(); }
      TE(4, 2); TE(5, 0);
      const float s0 = sums(xres[0], xres[1]), s1 = sums(x2, x3);
      if ((lane & 3) != 3) { __builtin_amdgcn_global_atomic_fadd_f64(pst, (double)s0); __builtin_amdgcn_global_atomic_fadd_f64(pst + 16, (double)s1); }
      PIN();
      TE(4, 3); TE(5, 1);
      asm volatile("s_waitcnt vmcnt(2)" ::: "memory");
      __builtin_amdgcn_raw_buffer_store_b32(tag, rsf, lane * 512 + 256, 0, 16);
      *LDS_AT(volatile unsigned char, rfa) = (unsigned char)3;
      PIN();
      TE(5, 2);
      asm volatile("s_waitcnt vmcnt(1)" ::: "memory");
      __builtin_amdgcn_raw_buffer_store_b32(tag, rsf, lane * 512, 0, 16);
      PIN();
      TE(5, 3);
      __builtin_amdgcn_s_setprio(0);
      return;
    }
    TE(4, 2); TE(5, 0);
  } else {
    u32x4 rres[4];
    double is1[4], is2[4];
#pragma unroll
    for (int p = 0; p < 4; ++p)
      rres[p] = __builtin_amdgcn_raw_buffer_load_b128(__builtin_amdgcn_make_buffer_rsrc(const_cast<u16*>(res), 0, S_ * D_ * 2, 0x00020000), ((row - 16 + 8 * p) * ldw + col) * 2, 0, 16);
    rwr = *reinterpret_cast<const u32x4*>(w_r + col);
#pragma unroll
    for (int p = 0; p < 4; ++p) { is1[p] = ld_ag(stats_in + row - 16 + 8 * p); is2[p] = ld_ag(stats_in + S_ + row - 16 + 8 * p); }
    PIN();
    setup();
    if (RES2) {
#pragma unroll
      for (int p = 0; p < 4; ++p) {
        float mean, var, mb, den;
        ln_from_sums(is1[p], is2[p], mean, var, mb, den);
        const float rin = frcp(den);
#pragma unroll
        for (int e = 0; e < 4; ++e) {
          const f32x2 x = {__uint_as_float(rres[p][e] << 16), __uint_as_float(rres[p][e] & 0xffff0000u)};
          const f32x2 h = (x - f32x2{mb, mb}) * f32x2{rin, rin} + x;
          rres[p][e] = pack2(h[0], h[1]);
        }
        asm volatile("" : "+v"(rres[p]));
      }
      PIN();
    }
    if ((idx >> 5) == tmi * 4 + wid - 4) {
      const int dr = idx - (row - 16);
      u32x4 qq;
#pragma unroll 1
      for (int i = 0; i < SPIN_MAX; ++i) {
        qq = __builtin_amdgcn_raw_buffer_load_b128(__builtin_amdgcn_make_buffer_rsrc(hand, 0, HAND_N * 4, 0x00020000), (GRAN_N + Y_N) * 4 + col * 2, 0, 16);
        if (none(umax(umax(qq[0], qq[1]), umax(qq[2], qq[3])) == SENT)) break;
        asm volatile("" ::: "memory");
      }
#pragma unroll
      for (int p = 0; p < 4; ++p)
#pragma unroll
        for (int e = 0; e < 4; ++e) rres[p][e] = dr == 8 * p ? qq[e] : rres[p][e];
    }
#pragma unroll 1
    for (int i = 0; i < 4 * SPIN_MAX; ++i)
      if (__builtin_amdgcn_readfirstlane(*LDS_AT(const volatile unsigned, flg + 16)) >= 12u) break;
#pragma unroll
    for (int p = 0; p < 4; ++p) *LDS_AT(volatile u32x4, tr - 2048 + p * 1024) = rres[p];
    asm volatile("s_waitcnt lgkmcnt(0)" ::: "memory");
    announce();
    TE(4, 1); TE(7, 2);
    asm volatile("s_waitcnt vmcnt(0)" : "+v"(rwr) :: "memory");
    if (!RES2) return;
    __builtin_amdgcn_s_sleep(TPRE);
#pragma unroll 1
    for (int i = 0; i < 4 * SPIN_MAX; ++i) {
      if (__builtin_amdgcn_readfirstlane(*LDS_AT(const volatile unsigned char, flg + 32 + wid)) == 2u) break;
      __builtin_amdgcn_s_sleep(TPSL);
    }
    asm volatile("" ::: "memory");
    __builtin_amdgcn_s_setprio(XPRIO);
    PIN();
    TE(4, 2); TE(5, 0);
#pragma unroll
    for (int p = 0; p < 2; ++p) xres[p] = lds_rd4(tr + p * 1024);
  }
  TE(4, 3); TE(7, 3);
  const float sv = sums(xres[0], xres[1]);
  if (wid == 0) STAMP(dk, 14);
  if ((lane & 3) != 3) __hip_atomic_store(pps, f2word(sv), __ATOMIC_RELAXED, __HIP_MEMORY_SCOPE_AGENT);
  PIN();
  __builtin_amdgcn_s_setprio(0);
  __hip_atomic_fetch_add(LDS_AT(unsigned, oad), 1u, __ATOMIC_RELAXED, __HIP_MEMORY_SCOPE_WORKGROUP);
  TE(5, 1); TE(5, 2);
  PIN();
}

#define MFMA32(a, b, c) __builtin_amdgcn_mfma_f32_32x32x16_bf16(a, b, c, 0, 0, 0)
#define MFMA16(a, b, c) __builtin_amdgcn_mfma_f32_16x16x32_bf16(a, b, c, 0, 0, 0)

#define PLO(x) __builtin_bit_cast(bf16x16, __builtin_shufflevector(x, x, 0, 1, 2, 3, 4, 5, 6, 7))
#define PHI(x) __builtin_bit_cast(bf16x16, __builtin_shufflevector(x, x, 8, 9, 10, 11, 12, 13, 14, 15))
#define SMF32(a, b, c) __builtin_amdgcn_smfmac_f32_32x32x32_bf16(__builtin_bit_cast(bf16x8, a), b, c, sidx, 0, 0)
#define SMF16(a, b, c) __builtin_amdgcn_smfmac_f32_16x16x64_bf16(__builtin_bit_cast(bf16x8, a), b, c, sidx, 0, 0)

__device__ __forceinline__ void patch_kv(unsigned tab, int r, s16x8& k0, s16x8& k1, s16x8& v0, s16x8& v1, bool on = true) {
  const int lane = fresh_lane(), hi = lane >> 5;
  if (on && (lane & 31) == kpos(r)) {
    k0 = *LDS_AT(const s16x8, tab + hi * 16); k1 = *LDS_AT(const s16x8, tab + 32 + hi * 16);
  }
  const short vv = *LDS_AT(const short, tab + 128 + (lane & 31) * 2);
  const int js = (on && hi == ((r >> 2) & 1)) ? (r >> 4) * 8 + ((r >> 3) & 1) * 4 + (r & 3) : -1;
#pragma unroll
  for (int e = 0; e < 8; ++e) { v0[e] = js == e ? vv : v0[e]; v1[e] = js == 8 + e ? vv : v1[e]; }
}

__device__ __forceinline__ void attn_safe(const u16* KV, int tile0, s16x8 q0, s16x8 q1,
                                       f32x16& o, float& l_out, int ptile, int prow, unsigned ptab) {
  const int lane = fresh_lane();
  float m = -INFINITY, l = 0.f;
  const int sidx = 0xDDDDDDDD;
  const __amdgpu_buffer_rsrc_t rs = __builtin_amdgcn_make_buffer_rsrc(const_cast<u16*>(KV), 0, 2 * S_ * D_ * 2, 0x00020000);
#pragma unroll
  for (int e = 0; e < 16; ++e) o[e] = 0.f;
  for (int tt = 0; tt < 64; ++tt) {
    const int t = tile0 + tt, off = t * 2048 + lane * 16;
    s16x8 k0 = __builtin_bit_cast(s16x8, __builtin_amdgcn_raw_buffer_load_b128(rs, off, 0, 16));
    s16x8 k1 = __builtin_bit_cast(s16x8, __builtin_amdgcn_raw_buffer_load_b128(rs, off + 1024, 0, 16));
    s16x8 v0 = __builtin_bit_cast(s16x8, __builtin_amdgcn_raw_buffer_load_b128(rs, off + S_ * D_ * 2, 0, 16));
    s16x8 v1 = __builtin_bit_cast(s16x8, __builtin_amdgcn_raw_buffer_load_b128(rs, off + S_ * D_ * 2 + 1024, 0, 16));
    if (t == ptile) patch_kv(ptab, prow, k0, k1, v0, v1);
    f32x16 sc;
#pragma unroll
    for (int e = 0; e < 16; ++e) sc[e] = 0.f;
    sc = MFMA32(k0, q0, sc); sc = MFMA32(k1, q1, sc);
    float mt = sc[0];
#pragma unroll
    for (int e = 1; e < 16; ++e) mt = fmaxf(mt, sc[e]);
    mt = fmaxf(mt, __shfl_xor(mt, 32, 64));
    const float mn = fmaxf(m, mt);
    const float alpha = __builtin_amdgcn_exp2f(m - mn);
    m = mn;
#pragma unroll
    for (int e = 0; e < 16; ++e) sc[e] = __builtin_amdgcn_exp2f(sc[e] - mn);
    float ls = 0.f;
#pragma unroll
    for (int e = 0; e < 16; ++e) ls += __uint_as_float(__float_as_uint(sc[e]) & 0xffff0000u);
    l = l * alpha + ls;
#pragma unroll
    for (int e = 0; e < 16; ++e) o[e] *= alpha;
    o = SMF32(v0, PLO(sc), o); o = SMF32(v1, PHI(sc), o);
  }
  l_out = l + __shfl_xor(l, 32, 64);
}

__device__ __forceinline__ float wave_fmax(float v) {
  asm volatile("s_nop 1\n\tv_max_f32_dpp %0, %0, %0 row_shr:1 row_mask:0xf bank_mask:0xf\n\ts_nop 1\n\tv_max_f32_dpp %0, %0, %0 row_shr:2 row_mask:0xf bank_mask:0xf\n\t"
               "s_nop 1\n\tv_max_f32_dpp %0, %0, %0 row_shr:4 row_mask:0xf bank_mask:0xf\n\ts_nop 1\n\tv_max_f32_dpp %0, %0, %0 row_shr:8 row_mask:0xf bank_mask:0xf\n\t"
               "s_nop 1\n\tv_max_f32_dpp %0, %0, %0 row_bcast:15 row_mask:0xa bank_mask:0xf\n\ts_nop 1\n\tv_max_f32_dpp %0, %0, %0 row_bcast:31 row_mask:0xc bank_mask:0xf\n\ts_nop 0"
               : "+v"(v));
  return __int_as_float(__builtin_amdgcn_readlane(__float_as_int(v), 63));
}

#define PSLP 8
#define WSLP 3
#define PRE1 8
#define PRE5 8
#define W3SLP 4
#define FSTART 10
#define OAUX 16
#define OMX 4
#define T0 2u
#define TW 10
#define HW 2
#define HP1 1
#define POLLAT 11
#define X2AT 2
#define HPRIO 3
#define SSLP 1
#define SPRE 6
#define WGAPS {0, 1, 7, 8, 9, 10, 11, 12}
#define KVS 4
#define PNS 12
#define G0 5u
#define GS 2u
#define IAT 1u
#define YSLP 2
#define ISLP 8
#define KSLP 2
#define UAT 13u
#define UHW 0
#define PPSLP 2
#define XRSLP 2
#define MPRE 20
#define OSLP 2
#define YD1 150
#define YD2 180
#define KDT 200
#define XDT 65
#define R2PRIO 0
#define LNPRIO 1
#define MSLP 4
#define BARG 0
struct Args {
  long long lay;
  const u16* a; u16* kv; u16* o; u16* xo; double* so;
  const u16* W; u16* wc;
  const u16 *wg, *wu, *wd, *wo, *w_r;
  unsigned* gran; float* csf; double* stats; unsigned* hand; u16* out;
};
typedef __attribute__((address_space(4))) const Args* ArgP;
#define WCAT 3
#define CAP0 1
#define PRE4 50
#define PRE7 20
#define UARR 12u
#define GF0 10
#define XGATE 8
#define ASLP 1
#define B15 3
#define KO 5
#define PRO 2
#define HS 2
#define WMAP 0x32100333
#define SMAP 0
#define STCH 6
#define KPOLL 10
#define TPRIO 2
#define TSLP 9
__device__ __forceinline__ bool slow_cols(const u16* W, int tni) { return ((((unsigned)(uintptr_t)W >> 7) + tni) & 7) == 3; }
#define ARGS_HERE                                                                                                      \
  ArgP ap_ = ap; asm volatile("" : "+s"(ap_));                                                                          \
  const u16* A = ap_->a; const u16* __restrict__ W = ap_->W; u16* KV = ap_->kv; u16* Of = ap_->o;        \
  const u16* __restrict__ wg = ap_->wg; const u16* __restrict__ wu = ap_->wu; const u16* __restrict__ wd = ap_->wd;       \
  const u16* __restrict__ wo = ap_->wo; const u16* __restrict__ w_r = ap_->w_r; unsigned* gran = ap_->gran;              \
  float* csf = ap_->csf; const double* stats = ap_->stats; double* stats_out = ap_->so; unsigned* hand = ap_->hand; \
  u16* xout = ap_->xo; u16* __restrict__ out = ap_->out
#define AGAIN "s_getpc_b64 s[90:91]\n\ts_add_u32 s90, s90, layers_kernel@rel32@lo+4\n\ts_addc_u32 s91, s91, layers_kernel@rel32@hi+12\n\ts_setpc_b64 s[90:91]"
#define KEPT(c)                                                                                                        \
  c "{v[104:107]}"(gu[0]), c "{v[108:111]}"(gu[1]), c "{v[112:115]}"(gu[2]), c "{v[116:119]}"(gu[3]), c "{v[120:123]}"(gu[4]),      \
  c "{v[124:127]}"(gu[5]), c "{v[128:131]}"(gu[6]), c "{v[132:135]}"(gu[7]), c "{v[136:139]}"(gu[8]), c "{v[140:143]}"(gu[9]),      \
  c "{v[144:147]}"(gu[10]), c "{v[148:151]}"(gu[11]), c "{v[152:155]}"(gu[12]), c "{v[156:159]}"(gu[13]), c "{v[160:163]}"(gu[14]), \
  c "{v[164:167]}"(gu[15])
#define LAYER_HERE ArgP apl_ = ap; asm volatile("" : "+s"(apl_)); const bool LAY2 = apl_->lay != 0
__device__ __forceinline__ void layer_body(const int wid, const int bid, const unsigned lbase, const ArgP ap, const unsigned long long t_entry) {
  constexpr int K = 1024, NKT = 16, NST = 3, ABYTES = 16384, WBYTES = 24576, STB = ABYTES + WBYTES, ldw = 3 * D_;
  constexpr int TABA = NST * STB, ALPHA = TABA + 2048, CRED = ALPHA + 512;
  constexpr int ASB = 16384, FLG = 8 * ASB + 256;
  constexpr int DUM = FLG + 64, TAB = DUM + 256, RED = TAB + 512, SACT = RED + 4096, XML = SACT + 3584, UGL = XML + 4096, SPM = UGL + 1024, QOFF = 9 * ASB;
  constexpr int TABW = RED;
  static_assert(SPM + 2048 <= QOFF && CRED <= 8 * ASB && RED + 8192 <= QOFF && RED % 16 == 0, "lds");
  const int xcd = (bid & 1) * 4 + ((bid >> 1) & 3), loc = bid >> 3;
  const int tmi = (xcd / QNG) * QMX + loc / QNX, tni = (xcd % QNG) * QNX + loc % QNX;
  const int m0 = tmi * 128, n0 = tni * 64;
  const int omi = (xcd / (OMX / 2)) * OMX + loc / (32 / OMX), oni = (xcd % (OMX / 2)) * (32 / OMX) + loc % (32 / OMX);
  constexpr unsigned VOFF = (unsigned)S_ * D_ * 2;
  auto tile_at = [&](unsigned n) __attribute__((always_inline)) {
#pragma unroll 1
    for (int i = 0; i < 4 * SPIN_MAX; ++i) {
      if (__builtin_amdgcn_readfirstlane(*LDS_AT(const volatile unsigned, lbase + FLG + 36)) == n * 0x01010101u) break;
      __builtin_amdgcn_s_sleep(1);
    }
    asm volatile("" ::: "memory");
    PIN();
  };
  auto all_ge = [&](unsigned w, unsigned n) { return ((w + (128u - n) * 0x01010101u) & 0x80808080u) == 0x80808080u; };
  if (wid >= 8) {
    u32x4 gu[16];
    LAYER_HERE;
    constexpr int dk = 6;
    const int hw = wid - 8, b = bid;
    int nb = 0;
    constexpr int lim = NKT - 1;
    auto tick_nw = [&]() {
      PIN();
      if (nb < lim) { __builtin_amdgcn_s_barrier(); ++nb; } else __builtin_amdgcn_s_sleep(2);
      asm volatile("" ::: "memory");
      PIN();
    };
    auto tick = [&]() {
      asm volatile("s_waitcnt lgkmcnt(0)" ::: "memory");
      tick_nw();
    };
    auto fold16 = [&](f32x2 y0, f32x2 y1) __attribute__((always_inline)) {
      const auto q0 = __builtin_amdgcn_permlane32_swap(__float_as_uint(y0[0]), __float_as_uint(y1[0]), false, false);
      const auto q1 = __builtin_amdgcn_permlane32_swap(__float_as_uint(y0[1]), __float_as_uint(y1[1]), false, false);
      const auto q = __builtin_amdgcn_permlane16_swap(__float_as_uint(__uint_as_float(q0[0]) + __uint_as_float(q0[1])),
                                                      __float_as_uint(__uint_as_float(q1[0]) + __uint_as_float(q1[1])), false, false);
      return __uint_as_float(q[0]) + __uint_as_float(q[1]);
    };
    unsigned best, tag, rmd;
    float alpha, m, wsum;
    u32x4 tw;
    u32x2 xq;
    s16x8 sl[8];
    const int kcap = CAP0 + (tmi & 7);
    if (!LAY2) {
      ARGS_HERE;
      const int lane = fresh_lane(), t = hw * 64 + lane;
      const unsigned ep = ld_ag(gran);
      const int srow = m0 + tni * 8 + hw * 2;
      u32x4 xa[2][2], wv[8];
      const int wr_ = (3 - ((unsigned)(uintptr_t)W >> 7) - (tni & ~3)) & 7;
      const bool cpy = hw == 3 && wr_ < 4 && wr_ != (tni & 3), wld = hw < 3 ? wr_ != (tni & 3) : cpy;
      const int wcn = cpy ? (tni & ~3) + wr_ : tni, wcp = cpy ? (tni - wr_ - 1) & 3 : hw;
      const int wof = ((tmi * 64 + (lane >> 3)) * ldw + wcp * D_ + wcn * 64 + (lane & 7) * 8) * 2;
      auto ld_w = [&]() {
        const __amdgpu_buffer_rsrc_t rw = __builtin_amdgcn_make_buffer_rsrc(const_cast<u16*>(W), 0, wld ? D_ * ldw * 2 : 0, 0x00020000);
#pragma unroll
        for (int r = 0; r < 8; ++r) wv[r] = __builtin_amdgcn_raw_buffer_load_b128(rw, wof, r * 8 * ldw * 2, 0);
        PIN();
      };
      auto wcopy = [&]() __attribute__((always_inline)) {
        if (cpy) {
          const __amdgpu_buffer_rsrc_t rw = __builtin_amdgcn_make_buffer_rsrc(ap_->wc, 0, D_ * ldw * 2, 0x00020000);
#pragma unroll
          for (int r = 0; r < 8; ++r) __builtin_amdgcn_raw_buffer_store_b128(wv[r], rw, wof + r * 8 * ldw * 2, 0, 16);
        }
        PIN();
      };
      const int wat = ((hw == 3 ? SMAP : WMAP) >> (4 * (tmi & 7))) & 3;
      unsigned ps = 0u;
      auto stouch = [&](int at) __attribute__((always_inline)) {
        if (at == STCH && cpy) ps = __builtin_amdgcn_raw_buffer_load_b32(__builtin_amdgcn_make_buffer_rsrc(const_cast<u16*>(W), 0, D_ * ldw * 2, 0x00020000),
                                                                        (((tmi ^ 8) * 64 + lane) * ldw + wcp * D_ + wcn * 64) * 2, 0, 0);
        PIN();
      };
      auto ld_x = [&](int h) {
#pragma unroll
        for (int r = 0; r < 2; ++r) xa[r][h] = *reinterpret_cast<const u32x4*>(A + (size_t)(srow + r) * D_ + (h ^ (tmi >> 3)) * 512 + lane * 8);
        PIN();
      };
      ld_x(0);
      if (X2AT == 0) ld_x(1);
      if (wat == 0) ld_w();
      unsigned pc = 0u;
      if (hw == 3) {
        unsigned long long a0, a1;
        CODE_ADDR("layers_kernel", a0);
        CODE_ADDR("end_marker_kernel", a1);
        pc = touch_code(a0, a1, loc, lane);
      }
      if (hw == 2) st_ag(gran + GR_N + bid * 64 + lane, lane + 1u);
      {
        const int gi = bid * 256 + t;
        if (gi < GRAN_N / 4) __builtin_amdgcn_raw_buffer_store_b128(u32x4{SENT, SENT, SENT, SENT}, __builtin_amdgcn_make_buffer_rsrc(hand, 0, GRAN_N * 4, 0x00020000), gi * 16, 0, 16);
        if (t < 24) __hip_atomic_store(stats_out + (t >> 3) * S_ + m0 + tni * 8 + (t & 7), 0.0, __ATOMIC_RELAXED, __HIP_MEMORY_SCOPE_AGENT);
      }
      tick();
      if (wat == 1) ld_w();
      if (X2AT == 1) ld_x(1);
      tick();
      if (wat == 2) ld_w();
      if (WCAT == 2) wcopy();
      if (X2AT == 2) ld_x(1);
      asm volatile("" :: "v"(ep));
      tick();
      if (wat == 3) ld_w();
      if (WCAT == 3) wcopy();
      if (X2AT == 3) ld_x(1);
      stouch(3);
#pragma unroll
      for (int i = 0; i < HW; ++i) { tick(); stouch(4 + i); }
      __builtin_amdgcn_s_setprio(HP1);
      PIN();
      tag = ep + 1u;
      const __amdgpu_buffer_rsrc_t rsc = __builtin_amdgcn_make_buffer_rsrc(gran, 0, GR_N * 4, 0x00020000);
      {
        u32x4 od;
#pragma unroll
        for (int q = 0; q < 4; ++q) od[q] = ((lane & 7) >> 1) == q ? ((lane & 1) ? 0x3f800000u : 0x00003f80u) : 0u;
        f32x4 d[2] = {{0.f, 0.f, 0.f, 0.f}, {0.f, 0.f, 0.f, 0.f}};
#pragma unroll
        for (int r = 0; r < 8; ++r) d[r & 1] = __builtin_amdgcn_mfma_f32_16x16x32_bf16(__builtin_bit_cast(s16x8, od), __builtin_bit_cast(s16x8, wv[r]), d[r & 1], 0, 0, 0);
        u32x4 gr;
#pragma unroll
        for (int q = 0; q < 4; ++q) gr[q] = ((__float_as_uint(dpp_add<0x128, 0xf>(fadd(d[0][q], d[1][q]))) + 0x80u) & ~0xffu) | ((tag >> (8 * q)) & 0xffu);
        asm volatile("s_waitcnt vmcnt(0)" ::: "memory");
        __builtin_amdgcn_raw_buffer_store_b128(gr, rsc, (lane & 40) || !wld ? GR_N * 4 : (GR_COL + (wcn * 16 + tmi) * 192 + wcp * 64 + (lane & 7) * 8 + (lane >> 4) * 4) * 4, 0, 16);
      }
      asm volatile("" :: "v"(pc));
      tick_nw();
      stouch(6);
      {
        float xv[2][16], mean[2];
#pragma unroll
        for (int r = 0; r < 2; ++r) {
          float sm = 0.f;
#pragma unroll
          for (int i = 0; i < 4; ++i) {
            xv[r][2 * i] = __uint_as_float(xa[r][0][i] << 16); xv[r][2 * i + 1] = __uint_as_float(xa[r][0][i] & 0xffff0000u);
            xv[r][8 + 2 * i] = __uint_as_float(xa[r][1][i] << 16); xv[r][8 + 2 * i + 1] = __uint_as_float(xa[r][1][i] & 0xffff0000u);
          }
#pragma unroll
          for (int i = 0; i < 16; ++i) sm += xv[r][i];
          mean[r] = wave_sum(sm) * (1.0f / 1024.0f);
          tick();
          stouch(7 + r);
        }
        float vr[2];
#pragma unroll
        for (int r = 0; r < 2; ++r) {
          float v = 0.f;
#pragma unroll
          for (int i = 0; i < 16; ++i) { const float d = xv[r][i] - mean[r]; v += d * d; }
          vr[r] = wave_sum(v) * (1.0f / 1024.0f);
          if (r == 0) tick();
        }
        const float vv = lane & 2 ? vr[1] : vr[0], mm = lane & 2 ? mean[1] : mean[0];
        const float rin = 1.0f / rbf(sqrtf(rbf(rbf(vv) + 1e-5f)));
        if (lane < 4) __builtin_amdgcn_raw_buffer_store_b64(u32x2{__float_as_uint((lane & 1) ? rbf(mm) : rin), tag}, rsc, (GR_ROW + srow * 4 + lane * 2) * 4, 0, 16);
      }
      tick();
      float cs;
      while (nb < POLLAT) tick();
      asm volatile("" :: "v"(ps));
#pragma unroll 1
      for (int spin = 0;; ++spin) {
        u32x4 csv[4];
        const __amdgpu_buffer_rsrc_t rsw = __builtin_amdgcn_make_buffer_rsrc(gran, 0, hw < 3 ? GR_N * 4 : 0, 0x00020000);
#pragma unroll
        for (int i = 0; i < 4; ++i) csv[i] = __builtin_amdgcn_raw_buffer_load_b128(rsw, (GR_COL + (tni * 16 + (lane >> 4)) * 192 + hw * 64 + (lane & 15) * 4) * 4, i * (4 * 192 * 4), 16);
        const u32x4 rp = __builtin_amdgcn_raw_buffer_load_b128(rsc, (GR_ROW + (m0 + (t & 127)) * 4) * 4, 0, 16);
        PIN();
        tick(); tick();
        unsigned bad = 0u;
        float z[4];
#pragma unroll
        for (int q = 0; q < 4; ++q) {
          const unsigned tq = tag >> (8 * q);
          bad |= ((csv[0][q] ^ tq) | (csv[1][q] ^ tq)) | ((csv[2][q] ^ tq) | (csv[3][q] ^ tq));
          z[q] = fadd(fadd(__uint_as_float(csv[0][q] & ~0xffu), __uint_as_float(csv[1][q] & ~0xffu)), fadd(__uint_as_float(csv[2][q] & ~0xffu), __uint_as_float(csv[3][q] & ~0xffu)));
        }
        cs = fold16(f32x2{z[0], z[1]}, f32x2{z[2], z[3]});
        bad = (hw < 3 ? bad & 0xffu : 0u) | (rp[1] ^ tag) | (rp[3] ^ tag);
        alpha = __uint_as_float(rp[0]); m = __uint_as_float(rp[2]);
        if (none(bad != 0u) || spin >= SPIN_MAX) break;
      }
      if (tmi == 0 && hw < 3) st_ag(reinterpret_cast<unsigned*>(csf) + hw * 1024 + n0 + (lane & 15) * 4 + (lane >> 4), __float_as_uint(cs));
      unsigned c0, c1, c2;
      split3(cs, c0, c1, c2);
      tw = u32x4{c0 | (c1 << 16), c0 | (c2 << 16), c1 | (c0 << 16), 0u};
      best = rmd = 0u;
      wsum = 0.f;
      asm volatile("" : "=v"(xq), KEPT("="));
#pragma unroll
      for (int i = 0; i < 8; ++i) asm volatile("" : "=v"(sl[i]));
    } else {
      asm volatile("; kept registers" : KEPT("="));
      PIN();
      ARGS_HERE;
      const int lane = fresh_lane(), t = hw * 64 + lane;
      best = rmd = 0u;
      tag = __builtin_amdgcn_readfirstlane(*LDS_AT(const volatile unsigned, lbase + FLG + 20));
#pragma unroll
      for (int i = 0; i < 8; ++i) asm volatile("" : "=v"(sl[i]));
      unsigned sad;
      auto tickc = [&]() __attribute__((always_inline)) {
        tick();
        if (nb == kcap + 1) {
#pragma unroll
          for (int i = 0; i < 8; ++i) {
            const unsigned a = (sad ^ ((i >> 2) * 64) ^ (((i >> 1) & 1) * 16)) + (i & 1) * 12288;
            sl[i] = __builtin_shufflevector(__builtin_amdgcn_ds_read_tr16_b64_v4i16(LDS_AT(s16x4, a)), __builtin_amdgcn_ds_read_tr16_b64_v4i16(LDS_AT(s16x4, a + 1536)), 0, 1, 2, 3, 4, 5, 6, 7);
          }
        }
        PIN();
      };
      static_assert(CAP0 + 8 <= KPOLL, "the stage is taken before that");
      if (hw == 1) STAMP(4, 4);
      if (hw == 0) {
        __builtin_amdgcn_s_sleep(TPRE);
        tile_at(3u);
        __builtin_amdgcn_s_sleep(XGATE);
        STAMP(4, 3);
#pragma unroll 1
        for (int spin = 0; spin < SPIN_MAX; ++spin) {
          if (none(ld_ag(gran + GR_X + (tmi * RX + tni % RX) * 128 + 64 + lane) != tag)) break;
          __builtin_amdgcn_s_sleep(1);
        }
      }
      if (hw == 0) STAMP(4, 5);
      asm volatile("" ::: "memory");
      PIN();
      __builtin_amdgcn_s_barrier();
      PIN();
      if (hw == 1) STAMP(4, 6);
      {
        const int ln = fresh_lane(), g4 = ln >> 4, tp = ln & 3, k = 8 * g4 + ((ln & 15) >> 2);
        sad = lbase + (kcap % NST) * STB + ABYTES + k * 384 + (((2 * (tp >> 1)) ^ (4 * ((k >> 1) & 1)) ^ (g4 & 1)) << 4) + 8 * (tp & 1) + (hw % 3) * 128;
      }
      wsum = __uint_as_float(__builtin_amdgcn_readfirstlane(*LDS_AT(const volatile unsigned, lbase + FLG + 60)));
      asm volatile("" : "+v"(sad), "=v"(tw), "=v"(alpha), "=v"(m));
      PIN();
      const int row = m0 + (t & 127);
      const __amdgpu_buffer_rsrc_t rsg = __builtin_amdgcn_make_buffer_rsrc(hand, 0, GRAN_N * 4, 0x00020000);
      if (hw < 2 || tmi == tni) {
      while (nb < HS) tickc();
#pragma unroll 1
      for (int spin = 0; spin < SPIN_MAX; ++spin) {
        const unsigned v = ld_ag(gran + GR_X + (tmi * RX + tni % RX) * 128 + lane);
        tickc();
        if (none(v != tag)) break;
      }
      asm volatile("" ::: "memory");
      PIN();
      const double s1 = ld_ag(stats + row), s2 = ld_ag(stats + S_ + row), sw = ld_ag(stats + 2 * S_ + row);
      tickc();
      float mean, var, mb, den;
      ln_from_sums(s1, s2, mean, var, mb, den);
      const float rin = frcp(den), g1 = 1.0f + rin;
      const float mb2 = rbf(mean + (mean - mb) * rin);
      const float den2 = rbf(__builtin_amdgcn_sqrtf(rbf(rbf(var * g1 * g1) + 1e-5f)));
      alpha = g1 * frcp(den2); m = (mb * rin + mb2) * frcp(g1);
      if (tmi == tni && hw >= 2) {
        const float r = (float)fma(-(double)mb, (double)wsum, sw) * rin;
        const unsigned rb = pack2(r, r);
        unsigned rkey = (rb ^ ((unsigned)((int)rb >> 31) | 0x80000000u)) & 0xffff0000u;
        if (r != r) rkey = 0u;
        rkey = wave_umax(rkey | (unsigned)(2047 - row));
        const unsigned md = __builtin_amdgcn_readlane(f2word(__uint_as_float((__float_as_uint(mb) >> 16) | (__float_as_uint(den) & 0xffff0000u))), (2047 - (int)(rkey & 0xffffu)) & 63);
        __builtin_amdgcn_raw_buffer_store_b64(u32x2{rkey, md}, rsg, lane < NREP ? lane * 256 + (tmi * 2 + hw - 2) * 8 : GRAN_N * 4, 0, 16);
      }
      }
      while (nb < KPOLL) tickc();
#pragma unroll 1
      for (int spin = 0; spin < SPIN_MAX; ++spin) {
        const u32x2 v = __builtin_amdgcn_raw_buffer_load_b64(rsg, (bid % NREP) * 256 + (lane & 31) * 8, 0, 16);
        tick(); tick();
        if (none(umax(v[0], v[1]) == SENT)) {
          best = wave_umax(v[0]);
          rmd = __builtin_amdgcn_readlane(v[1], ((2047 - (int)(best & 0xffffu)) >> 6) & 31);
          break;
        }
      }
      const int ix = (2047 - (int)(best & 0xffffu)) & 2047;
      if (row == ix) alpha = 0.f;
      xq = __builtin_amdgcn_raw_buffer_load_b64(__builtin_amdgcn_make_buffer_rsrc(const_cast<u16*>(A), 0, S_ * D_ * 2, 0x00020000), (ix * D_ + hw * 256 + (lane & 3) * 64 + (lane >> 2) * 4) * 2, 0, 16);
      PIN();
    }
    tag = __builtin_amdgcn_readfirstlane(tag);
    wsum = __uint_as_float(__builtin_amdgcn_readfirstlane(__float_as_uint(wsum)));
    const int lane = fresh_lane(), t = hw * 64 + lane;
    {
      unsigned a0, a1, a2;
      split3(-m, a0, a1, a2);
      u32x4 ta = {a0 | (a0 << 16), a1 | (a0 << 16), a1 | (a2 << 16), 0u};
      asm volatile("" : "+v"(tw), "+v"(ta), "+v"(alpha));
      __builtin_amdgcn_s_setprio(TPRIO);
      if (nb < lim) {
        while (nb < lim) tick();
        __builtin_amdgcn_s_sleep(TSLP);
      }
      if (!LAY2 && hw < 3) *LDS_AT(u32x4, lbase + TABW + (hw * 64 + (lane & 15) * 4 + (lane >> 4)) * 16) = tw;
      if (t < 128) {
        *LDS_AT(u32x4, lbase + TABA + t * 16) = ta;
        *LDS_AT(float, lbase + ALPHA + t * 4) = alpha;
      }
    }
    if (t >= 192 && t < 196) *LDS_AT(u32x4, lbase + CRED + ((t >> 1) & 1) * 512 + (t & 1) * 128) = u32x4{0u, 0u, 0u, 0u};
    asm volatile("s_waitcnt lgkmcnt(0)" ::: "memory");
    PIN();
    __builtin_amdgcn_s_barrier();
    PIN();
    *LDS_AT(u32x4, lbase + XML + t * 16) = u32x4{SENT, SENT, SENT, SENT};
    *LDS_AT(u32x4, lbase + SPM + (t & 127) * 16) = u32x4{SENT, SENT, SENT, SENT};
    if (hw == 0) {
      unsigned v;
      asm volatile("v_mov_b32 %0, 0\n\tv_writelane_b32 %0, 2, 0\n\tv_writelane_b32 %0, %1, 5\n\tv_writelane_b32 %0, %2, 7"
                   : "=&v"(v) : "s"(tag), "s"(best));
      lds_wr(lbase + FLG + lane * 4, v);
    }
    const int idx = LAY2 ? (2047 - (int)(best & 0xffffu)) & 2047 : -64;
    const bool p1blk = (oni & 7) != 7, fast = tmi < 8, own = fast && (loc & 1);
    const int fg = xcd * 28 + (p1blk ? loc - (loc >> 3) : 0), ow = (xcd & 3) * 16 + (loc >> 1);
    static_assert(OMX == 4, "oni % 8 = loc % 8");
#define GLD(a, b) "global_load_dwordx4 v[" #a ":" #b "], %0, off\n\tv_lshl_add_u64 %0, %0, 0, %1\n\t"
#define GLD4(...) asm volatile(__VA_ARGS__ : "+v"(p) : "s"((unsigned long long)(F_ * 2)), KEPT() : "memory")
    auto gptr = [&](const u16* wg, const u16* wu) {
      const int ln = fresh_lane();
      return ((ln & 2) ? wu : wg) + (size_t)(hw * 256 + (ln >> 2) * 4) * F_ + fg * 16 + (ln & 1) * 8;
    };
    auto gather = [&](const u16*& p, int e) __attribute__((always_inline)) {
      if (e == 0) GLD4(GLD(104, 107) GLD(108, 111) GLD(112, 115) GLD(116, 119));
      else if (e == 1) GLD4(GLD(120, 123) GLD(124, 127) GLD(128, 131) GLD(132, 135));
      else if (e == 2) GLD4(GLD(136, 139) GLD(140, 143) GLD(144, 147) GLD(148, 151));
      else GLD4(GLD(152, 155) GLD(156, 159) GLD(160, 163) GLD(164, 167));
      p += 60 * F_;
    };
    auto dmask = [&](int q) { const int ln = fresh_lane(); return ((ln >> 1) & 1) == q ? ((ln & 1) ? 0xffff0000u : 0x0000ffffu) : 0u; };
    unsigned dm0, dm1, dad;
    auto gu_setup = [&]() __attribute__((always_inline)) {
      const int ln = fresh_lane();
      dm0 = dmask(0); dm1 = dmask(1);
      dad = lbase + SACT + ((ln & 3) * 8 + (ln >> 5) * 4 + ((ln >> 4) & 1)) * 16 + hw * 4;
      asm volatile("" : "+v"(dm0), "+v"(dm1), "+v"(dad));
    };
    auto gate_up = [&](u32x2 x, unsigned md) __attribute__((always_inline)) {
      const unsigned rin = __builtin_amdgcn_readfirstlane(__float_as_uint(frcp(__uint_as_float(md & 0xffff0000u))));
      asm volatile("s_getpc_b64 s[90:91]\n\ts_add_u32 s90, s90, gate_up_code@rel32@lo+4\n\ts_addc_u32 s91, s91, gate_up_code@rel32@hi+12\n\ts_swappc_b64 s[92:93], s[90:91]"
                   : "+{v[100:101]}"(x) : "{s84}"(md << 16), "{s85}"(md & 0xffff0000u), "{s86}"(rin), "{v99}"(dad), "{v102}"(dm0), "{v103}"(dm1), KEPT()
                   : "v84", "v85", "v86", "v87", "v88", "v89", "v90", "v91", "v92", "v93", "v94", "v95", "v96", "v97", "s88", "s89", "s90", "s91", "s92", "s93", "scc", "memory");
    };
    auto act_diag = [&](int so = 0) __attribute__((always_inline)) {
      const int ln = fresh_lane();
      const f32x4 g4 = *LDS_AT(const volatile f32x4, lbase + SACT + so + (ln >> 2) * 16), u4 = *LDS_AT(const volatile f32x4, lbase + SACT + so + 256 + (ln >> 2) * 16);
      float gv = fadd(fadd(g4[0], g4[1]), fadd(g4[2], g4[3])), uv = fadd(fadd(u4[0], u4[1]), fadd(u4[2], u4[3]));
      rbf2(gv, uv);
      const float av = rbf(gv * frcp(1.0f + __expf(-gv))) * uv;
      const unsigned ap2 = pack2(av, av);
      return __builtin_bit_cast(s16x4, u32x2{ap2 & dm0, ap2 & dm1});
    };
    auto down = [&](const s16x4& a, const u32x2 (&w)[16]) __attribute__((always_inline)) {
      f32x4 d[2] = {{0.f, 0.f, 0.f, 0.f}, {0.f, 0.f, 0.f, 0.f}};
      static_for<0, 16>([&](auto fc) __attribute__((always_inline)) {
        constexpr int f = decltype(fc)::value;
        d[f & 1] = __builtin_amdgcn_mfma_f32_4x4x4bf16_1k(a, __builtin_bit_cast(s16x4, w[f]), d[f & 1], 4, f, 0);
      });
      f32x4 z;
#pragma unroll
      for (int e = 0; e < 4; ++e) { z[e] = d[0][e] + d[1][e]; asm volatile("" : "+v"(z[e])); }
      return u32x4{f2word(z[0]), f2word(z[1]), f2word(z[2]), f2word(z[3])};
    };
    auto fold8 = [&](f32x2 y0, f32x2 y1) __attribute__((always_inline)) { return dpp_add<0x128, 0xf>(fold16(y0, y1)); };
    auto fold4 = [&](f32x2 y0, f32x2 y1) __attribute__((always_inline)) { return dpp_add<0x124, 0xf>(fold8(y0, y1)); };
    auto y_sum = [&](const __amdgpu_buffer_rsrc_t rs, auto nbc) __attribute__((always_inline)) {
      constexpr int NB = decltype(nbc)::value, N = 14 / NB;
      const int ln = fresh_lane(), src = (GRAN_N + (ln >> 2) * D_ + ow * 16 + (ln & 3) * 4) * 4;
      f32x2 y0 = {0.f, 0.f}, y1 = {0.f, 0.f};
#pragma unroll 1
      for (int bt = 0; bt < NB; ++bt) {
        f32x2 z0, z1;
#pragma unroll 1
        for (int spin = 0; spin < SPIN_MAX; ++spin) {
          u32x4 v[N];
#pragma unroll
          for (int i = 0; i < N; ++i) v[i] = __builtin_amdgcn_raw_buffer_load_b128(rs, src, (bt * N + i) * (16 * D_ * 4), 16);
          z0 = y0; z1 = y1;
#pragma unroll
          for (int i = 0; i < N; ++i) {
            z0 = f32x2{fadd(z0[0], __uint_as_float(v[i][0])), fadd(z0[1], __uint_as_float(v[i][1]))};
            z1 = f32x2{fadd(z1[0], __uint_as_float(v[i][2])), fadd(z1[1], __uint_as_float(v[i][3]))};
          }
          const float chk = fadd(fadd(z0[0], z0[1]), fadd(z1[0], z1[1]));
          if (none(chk != chk)) break;
          unsigned mx = 0u;
#pragma unroll
          for (int i = 0; i < N; ++i) mx = umax(umax(umax(mx, v[i][0]), v[i][1]), umax(v[i][2], v[i][3]));
          if (none(mx == SENT)) break;
          __builtin_amdgcn_s_sleep(YSLP);
          asm volatile("" ::: "memory");
        }
        y0 = z0; y1 = z1;
      }
      return fold4(y0, y1);
    };
    [&]() __attribute__((always_inline)) {
    ARGS_HERE;
    const unsigned l16 = lane * 16;
    unsigned* kvflag = gran + GR_KV + ((LAY2 ? 16 : 0) + tni) * (32 * RK);
    auto wait_sig = [&](int sh) {
#pragma unroll 1
      for (int i = 0; i < 4 * SPIN_MAX; ++i) {
        if (((__builtin_amdgcn_readfirstlane(*LDS_AT(const volatile unsigned, lbase + FLG + 32)) >> sh) & 7u) >= 4u) break;
        __builtin_amdgcn_s_sleep(SSLP);
      }
      asm volatile("" ::: "memory");
      PIN();
    };
    PIN();
    asm volatile("s_waitcnt lgkmcnt(0)" ::: "memory");
    if (!LAY2) asm volatile("s_waitcnt vmcnt(0)" ::: "memory");
    __builtin_amdgcn_s_barrier();
    PIN();
    __builtin_amdgcn_s_setprio(HPRIO);
    if (hw < 3) {
      __builtin_amdgcn_s_sleep(SPRE);
      wait_sig(0);
      __builtin_amdgcn_raw_buffer_store_b32(tag, __builtin_amdgcn_make_buffer_rsrc(kvflag, 0, hw == 0 ? RK * 128 : 0, 0x00020000), lane * 128 + tmi * 4, 0, 16);
      if (hw == 0) STAMP(LAY2 ? 5 : 1, 16);
    }
    if (hw == 3) {
      __builtin_amdgcn_s_sleep(KVS);
      const unsigned kv0 = ld_ag(kvflag + (tmi % RK) * 32 + (lane & 15));
      PIN();
      const char* cnt = reinterpret_cast<const char*>(gran + GR_N + bid * 64 + 2);
      unsigned vk[8], vp = lbase + FLG + 8 + (lane & 7), pw = 0u;
#pragma unroll
      for (int j = 0; j < 8; ++j) { vk[j] = l16 + (j >> 2) * 131072u + ((j >> 1) & 1) * 4096u + (j & 1) * (VOFF - 2048u); asm volatile("" : "+v"(vk[j])); }
#define G4(a, b, c, d, sc) "s_nop 0\n\tglobal_load_lds_dwordx4 %" #a ", %8" sc "\n\tglobal_load_lds_dwordx4 %" #b ", %8 offset:1024" sc "\n\t"             \
                       "global_load_lds_dwordx4 %" #c ", %8 offset:2048" sc "\n\tglobal_load_lds_dwordx4 %" #d ", %8 offset:3072" sc "\n\t"
#define M4 "s_addk_i32 m0, 0x1000\n\t"
      auto stage = [&](const char* sp, unsigned dst) __attribute__((always_inline)) {
        asm volatile("s_mov_b32 m0, %9\n\t" G4(0, 0, 1, 1, " sc1") M4 G4(2, 2, 3, 3, " sc1") M4 G4(4, 4, 5, 5, " sc1") M4 G4(6, 6, 7, 7, " sc1")
                     :: "v"(vk[0]), "v"(vk[1]), "v"(vk[2]), "v"(vk[3]), "v"(vk[4]), "v"(vk[5]), "v"(vk[6]), "v"(vk[7]), "s"(sp), "s"(dst) : "m0", "scc", "memory");
      };
      auto count = [&]() __attribute__((always_inline)) {
        asm volatile("s_mov_b32 m0, %2\n\ts_nop 0\n\tglobal_load_lds_dword %0, %1 sc1" :: "v"(0u), "s"(cnt), "s"(lbase + FLG - 252) : "m0", "memory");
        cnt += 8;
      };
      auto freed = [&](unsigned n) __attribute__((always_inline)) {
        if (!none(pw < n)) {
#pragma unroll 1
          for (int i = 0; i < 4 * SPIN_MAX; ++i) {
            pw = *LDS_AT(const volatile unsigned char, vp);
            if (none(pw < n)) break;
            __builtin_amdgcn_s_sleep(W3SLP);
          }
        }
        asm volatile("" ::: "memory");
      };
      unsigned long long ord = (0xfedcba9876543210ull >> 4 & ~((1ull << 4 * (tmi & 7)) - 1ull)) | (0xfedcba9876543210ull & ((1ull << 4 * (tmi & 7)) - 1ull));
      ord ^= (tmi & 8) * 0x1111111111111111ull;
      unsigned seen;
      const char* kvb = reinterpret_cast<const char*>(KV) + (size_t)tni * (128 * 2048);
      auto panel = [&]() __attribute__((always_inline)) {
        const unsigned pn = (unsigned)ord & 15u;
        ord >>= 4;
        if (__builtin_expect(!((seen >> pn) & 1u), 0)) {
#pragma unroll 1
          for (int i = 0; i < SPIN_MAX && !((seen >> pn) & 1u); ++i) seen = (unsigned)__ballot(ld_ag(kvflag + (tmi % RK) * 32 + (lane & 15)) == tag);
        }
        asm volatile("" ::: "memory");
        return kvb + pn * 8192u;
      };
      asm volatile("" : "+s"(ord), "+s"(kvb), "+s"(cnt));
      asm volatile("" : "+v"(vp), "+v"(pw));
      PIN();
      seen = (unsigned)__ballot(kv0 == tag);
      const char* sp = panel();
      wait_sig(16);
      stage(sp, lbase + 2 * ASB);
      count();
      const int ksp = LAY2 ? PNS / 2 : -1, ksg = LAY2 ? 64 : GF0;
      const u16* gp = gptr(wg, wu);
#pragma unroll 1
      for (int k = 1; k < 15; ++k) {
        if (__builtin_expect(k == ksp, 0)) {
          gu_setup();
          gate_up(xq, rmd);
          PIN();
          *LDS_AT(volatile unsigned char, lbase + FLG + 7) = (unsigned char)0x7f;
        }
        freed((unsigned)(0xba9876543211000ull >> 4 * k) & 15u);
        stage(sp + 2048, lbase + ((2 * k + 1) & 7) * ASB);
        sp = panel();
        stage(sp, lbase + ((2 * k + 2) & 7) * ASB);
        count();
        if (__builtin_expect((unsigned)(k - ksg) < 4u, 0)) gather(gp, k - ksg);
      }
      freed(12u);
      glds16s<true>(csf + 4096 + tni * 128, l16, lbase + UGL);
      stage(sp + 2048, lbase + 7 * ASB);
      count();
      const unsigned vwo = (unsigned)((lane >> 3) * D_ + oni * 64 + ((lane & 7) ^ ((lane >> 3) & 6)) * 8) * 2u;
      constexpr unsigned vs = 8u * D_ * 2u - 1024u;
#pragma unroll 1
      for (int ns = 0; ns < 6; ++ns) {
        freed((unsigned)(ns + 25) >> 1);
        const char* wp = reinterpret_cast<const char*>(wo) + (size_t)ns * (128 * D_ * 2);
#pragma unroll
        for (int j = 0; j < 4; ++j)
          glds16x4<false>(wp + j * (32u * D_ * 2u), vwo, vwo + vs, vwo + 2 * vs, vwo + 3 * vs, lbase + ns * ASB + j * 4096);
      }
      asm volatile("s_waitcnt vmcnt(0)" ::: "memory");
      return;
    }
    if (!LAY2) {
      auto until = [&](unsigned n) {
        while ((__builtin_amdgcn_readfirstlane(*LDS_AT(const volatile unsigned, lbase + FLG + 8)) & 255u) < n) __builtin_amdgcn_s_sleep(16);
        asm volatile("" ::: "memory");
        PIN();
      };
      if (hw != 1) {
        constexpr int N2 = HAND_N + Y_N + 3 * D_ / 2;
        const __amdgpu_buffer_rsrc_t rsi = __builtin_amdgcn_make_buffer_rsrc(hand + (hw ? GRAN_N : 2 * HAND_N), 0, (hw ? N2 : PS_N) * 4, 0x00020000);
        until(IAT);
#pragma unroll 1
        for (int i = 0; i < ((hw ? N2 : PS_N) / 4 + 16383) / 16384; ++i) {
          __builtin_amdgcn_raw_buffer_store_b128(u32x4{SENT, SENT, SENT, SENT}, rsi, ((i * 256 + bid) * 64 + lane) * 16, 0, 16);
          __builtin_amdgcn_s_sleep(ISLP);
        }
      }
      const int ng = loc & 15, fc = xcd * 2 + (loc >> 4);
      const size_t og = (size_t)((fg - xcd * 28) / 7 * 256 + lane) * F_ + (xcd * 7 + fg % 7) * 64;
      const u16* pd = wd + (size_t)(fc * 224 + (lane < 56 ? lane : 0)) * D_ + ng * 64;
      const u16* p = gptr(wg, wu);
#pragma unroll 1
      for (unsigned n = T0; n < T0 + 13u; ++n) {
        until(n);
        if (hw == 1) {
          const unsigned i = n - T0;
          const u16* q = i < 8u ? ((i & 1u) ? wu : wg) + og + (size_t)(i >> 1) * (64 * F_) : pd + (size_t)((i - 8u - (i > TW)) & 3u) * (56 * D_);
          if (i == TW) {
            q = wo + (size_t)(768 + (loc / (32 / OMX)) * (256 / OMX) + (lane & (256 / OMX - 1) & 63)) * D_ + oni * 64;
            unsigned long long a0, a1;
            CODE_ADDR("layers_kernel", a0); CODE_ADDR("end_marker_kernel", a1);
            if (loc < 8) { const unsigned p1 = touch_code(a0, a1, 3 + loc, lane); asm volatile("" :: "v"(p1)); }
          }
          const unsigned p0 = touch(q);
          asm volatile("" :: "v"(p0));
        }
        if (n >= G0 && (n - G0) % GS == 0u && (n - G0) / GS < 4u) gather(p, (n - G0) / GS);
        if (n == UAT && hw == UHW) {
          u32x4 a[4][2], c[2];
#pragma unroll
          for (int i = 0; i < 4; ++i)
#pragma unroll
            for (int h = 0; h < 2; ++h) a[i][h] = *reinterpret_cast<const u32x4*>(wo + (size_t)(tni * 64 + tmi * 4 + i) * D_ + h * 512 + lane * 8);
#pragma unroll
          for (int h = 0; h < 2; ++h) c[h] = *reinterpret_cast<const u32x4*>(w_r + h * 512 + lane * 8);
          auto dot = [&](unsigned x, unsigned y, float z) { return __builtin_amdgcn_fdot2_f32_bf16(__builtin_bit_cast(bf16x2, x), __builtin_bit_cast(bf16x2, y), z, false); };
          PIN();
          __builtin_amdgcn_s_setprio(0);
#pragma unroll 1
          for (int i = 0; i < SPIN_MAX; ++i) {
            if (__builtin_amdgcn_readfirstlane(*LDS_AT(const volatile unsigned, lbase + FLG + 16)) >= UARR) break;
            __builtin_amdgcn_s_sleep(4);
          }
          PIN();
          float wm = 0.f;
#pragma unroll
          for (int e = 0; e < 8; ++e) wm = dot(c[e >> 2][e & 3], 0x3f803f80u, wm);
          wm = wave_sum(wm);
          lds_wr(lbase + FLG + 60, __float_as_uint(wm));
          wm *= -1.0f / 1024.0f;
          f32x2 u[2], g[2];
#pragma unroll
          for (int i = 0; i < 4; ++i) {
            float p1 = 0.f, pw = 0.f, p2 = 0.f;
#pragma unroll
            for (int e = 0; e < 8; ++e) {
              const unsigned x = a[i][e >> 2][e & 3];
              p1 = dot(x, 0x3f803f80u, p1); pw = dot(x, c[e >> 2][e & 3], pw); p2 = dot(x, x, p2);
            }
            u[i >> 1][i & 1] = fmaf(p1, wm, pw); g[i >> 1][i & 1] = p2;
          }
          float us = fold4(u[0], u[1]), gs = fold4(g[0], g[1]);
          us = dpp_add<0x121, 0xf>(dpp_add<0x122, 0xf>(us)); gs = dpp_add<0x121, 0xf>(dpp_add<0x122, 0xf>(gs));
          __builtin_amdgcn_raw_buffer_store_b32(__float_as_uint((lane & 1) ? __builtin_amdgcn_sqrtf(gs) * (1.0f / 32.0f) : us),
              __builtin_amdgcn_make_buffer_rsrc(csf + 4096 + tni * 128, 0, 512, 0x00020000), (lane & 14) ? 512 : ((lane & 1) * 64 + tmi * 4 + (lane >> 4)) * 4, 0, 16);
        }
      }
      if (hw == 2 && bid == 0) {
        const __amdgpu_buffer_rsrc_t rsc = __builtin_amdgcn_make_buffer_rsrc(gran, 0, GR_N * 4, 0x00020000);
#pragma unroll 1
        for (int spin = 0; spin < SPIN_MAX; ++spin) {
          bool ok = true;
#pragma unroll
          for (int i = 0; i < 4; ++i) ok &= __builtin_amdgcn_raw_buffer_load_b32(rsc, (GR_ROW + (i * 64 + lane) * 32 + 1) * 4, 0, 16) == tag;
          if (__all(ok)) break;
          __builtin_amdgcn_s_sleep(8);
          asm volatile("" ::: "memory");
        }
        if (lane == 0) st_ag(gran, tag);
      }
      return;
    }
    unsigned* X2 = hand + GRAN_N + Y_N;
    unsigned* XS = X2 + X2_N;
    unsigned* P4 = XS + XS_N;
    unsigned* PA = P4 + P4_N;
    const __amdgpu_buffer_rsrc_t rsh = __builtin_amdgcn_make_buffer_rsrc(hand, 0, HAND_N * 4, 0x00020000);
    auto signal = [&](unsigned ph) {
      asm volatile("" ::: "memory");
      PIN();
      *LDS_AT(volatile unsigned char, lbase + FLG + 4 + hw) = (unsigned char)ph;
    };
    auto wait_all = [&](unsigned ph) {
#pragma unroll 1
      for (int i = 0; i < SPIN_MAX; ++i) {
        if (all_ge(__builtin_amdgcn_readfirstlane(*LDS_AT(const volatile unsigned, lbase + FLG + 4)), ph)) break;
        __builtin_amdgcn_s_sleep(WSLP);
      }
      asm volatile("" ::: "memory");
      PIN();
    };
    gu_setup();
    gate_up(xq, rmd);
    signal(hw == 2 ? 2u : 0x7fu);
    if (wid == 8) STAMP(dk, 2);
    if (hw < 2) {
#pragma unroll 1
      for (int i = 0; i < SPIN_MAX; ++i) {
        if (__builtin_amdgcn_readfirstlane(*LDS_AT(const volatile unsigned, lbase + FLG)) >= FSTART) break;
        __builtin_amdgcn_s_sleep(WSLP);
      }
      asm volatile("" ::: "memory");
      PIN();
      if (wid == 8) STAMP(dk, 10);
      u32x2 w[16];
      const __amdgpu_buffer_rsrc_t rd_ = __builtin_amdgcn_make_buffer_rsrc(const_cast<u16*>(wd), 0, D_ * F_ * 2, 0x00020000);
      auto ldw = [&](int c) __attribute__((always_inline)) {
#pragma unroll
        for (int f = 0; f < 16; ++f) w[f] = __builtin_amdgcn_raw_buffer_load_b64(rd_, lane * 8 + c * 512 + (f & 1) * 2048, (fg * 16 + (f & ~1)) * (D_ * 2) + hw * 1024, 0);
        PIN();
      };
      ldw(0);
      const unsigned pt = __builtin_amdgcn_raw_buffer_load_b32(rd_, (lane >> 2) * (D_ * 2) + (lane & 3) * 128 + 512, fg * 16 * (D_ * 2) + hw * 1024, 0);
      int yo = p1blk ? (GRAN_N + fg * D_ + hw * 512) * 4 + lane * 16 : HAND_N * 4;
      asm volatile("" : "+v"(yo));
      wait_all(2u);
      if (wid == 8) STAMP(dk, 3);
      const s16x4 a = act_diag();
      const u32x4 y0 = down(a, w);
      PIN();
      ldw(1);
      __builtin_amdgcn_raw_buffer_store_b128(y0, rsh, yo, 0, 16);
      PIN();
      __builtin_amdgcn_raw_buffer_store_b128(down(a, w), rsh, yo + 1024, 0, 16);
      asm volatile("" :: "v"(pt));
      if (wid == 8) STAMP(dk, 4);
    }
    PIN();
    [&]() __attribute__((always_inline)) {
    const int lane = fresh_lane();
    const float rmb = __uint_as_float(rmd << 16);
    auto first_words = [&](int src, auto nc) __attribute__((always_inline)) {
#pragma unroll 1
      for (int spin = 0; spin < SPIN_MAX; ++spin) {
        unsigned mx = 0u;
#pragma unroll
        for (int i = 0; i < decltype(nc)::value; ++i) mx = umax(mx, __builtin_amdgcn_raw_buffer_load_b32(rsh, src, i * (56 * D_ * 4), 16));
        if (none(mx == SENT)) break;
        __builtin_amdgcn_s_sleep(PSLP);
        asm volatile("" ::: "memory");
      }
    };
    if (hw == 2) {
      wait_all(2u);
      if (own) {
        const float rden = __uint_as_float(rmd & 0xffff0000u), rrin = frcp(rden);
        const int col = ow * 16 + (lane & 3) * 4 + (lane >> 4);
        const unsigned xo2 = ld_ag(reinterpret_cast<const unsigned*>(A + (size_t)idx * D_ + (col & ~1)));
        __builtin_amdgcn_s_sleep(PRE1);
        first_words((GRAN_N + (lane < 56 ? lane : 0) * D_ + ow * 16) * 4, std::integral_constant<int, 4>{});
        const float y = y_sum(rsh, std::integral_constant<int, 2>{});
        const unsigned rk = best >> 16;
        const float rv = __uint_as_float(((rk & 0x8000u) ? (rk & 0x7fffu) : (~rk & 0xffffu)) << 16);
        const float rs = rbf(frcp(1.0f + __expf(-rv)));
        const float x1v = __uint_as_float((col & 1) ? xo2 & 0xffff0000u : xo2 << 16);
        float h0 = x1v, h1 = x1v;
        ln_apply2(h0, h1, rmb, rden, rrin);
        const float x2v = rbf(x1v + rbf(h0 + rbf(rbf(y) * rs)));
        const auto pr = __builtin_amdgcn_permlane16_swap(__float_as_uint(x2v), __float_as_uint(x2v), false, false);
        __builtin_amdgcn_raw_buffer_store_b32(f2word(__uint_as_float((pr[0] >> 16) | pr[1])), rsh, (lane & 28) ? HAND_N * 4 : (int)(X2 - hand) * 4 + col * 2, 0, 16);
        const float d = x2v - rmb;
        const float sd = wave_sum(d), sq = wave_sum(d * d);
        if (lane < 2) st_ag(XS + ow * 2 + lane, f2word(lane ? sq : sd));
        STAMP(dk, 5);
      }
    }
    {
      const int ks = kcap ^ (tmi & 8), g4 = lane >> 4;
      const int src = (int)(X2 - hand) * 4 + ks * 128 + g4 * 16;
      int dst = (lane & 12) ? HAND_N * 4 : (int)((P4 - hand) + (tni * 16 + ks) * 192 + hw * 64 + (lane & 2) * 16 + (lane & 1) * 8 + (g4 >> 1) * 16 + (g4 & 1) * 4) * 4;
      asm volatile("" : "+v"(dst));
      u32x4 x0, x1;
      if (!(hw == 2 && own)) __builtin_amdgcn_s_sleep(PRE4);
#pragma unroll 1
      for (int spin = 0; spin < SPIN_MAX; ++spin) {
        x0 = __builtin_amdgcn_raw_buffer_load_b128(rsh, src, 0, 16); x1 = __builtin_amdgcn_raw_buffer_load_b128(rsh, src + 64, 0, 16);
        if (none(umax(umax(umax(x0[0], x0[1]), umax(x0[2], x0[3])), umax(umax(x1[0], x1[1]), umax(x1[2], x1[3]))) == SENT)) break;
        __builtin_amdgcn_s_sleep(PSLP);
        asm volatile("" ::: "memory");
      }
      f32x4 d[4];
#pragma unroll
      for (int i = 0; i < 4; ++i) d[i] = MFMA16(sl[2 * i], __builtin_bit_cast(s16x8, x0), (f32x4{0.f, 0.f, 0.f, 0.f}));
#pragma unroll
      for (int i = 0; i < 4; ++i) d[i] = MFMA16(sl[2 * i + 1], __builtin_bit_cast(s16x8, x1), d[i]);
      u32x4 o;
#pragma unroll
      for (int e = 0; e < 4; ++e) o[e] = f2word((lane & 2) ? ((lane & 1) ? d[3][e] : d[2][e]) : ((lane & 1) ? d[1][e] : d[0][e]));
      __builtin_amdgcn_raw_buffer_store_b128(o, rsh, dst, 0, 16);
      PIN();
      if (wid == 8) STAMP(dk, 6);
    }
    if (hw == 2) {
      const int col = (lane & 15) * 4 + (lane >> 4), src = (int)((P4 - hand) + (tni * 16 + (lane >> 4)) * 192 + (lane & 15) * 4) * 4;
      const __amdgpu_buffer_rsrc_t rsq = __builtin_amdgcn_make_buffer_rsrc(hand, 0, fast ? HAND_N * 4 : 0, 0x00020000);
      float cs[3], val[3];
#pragma unroll
      for (int p = 0; p < 3; ++p) cs[p] = __uint_as_float(ld_ag(reinterpret_cast<const unsigned*>(csf) + p * 1024 + n0 + col));
      u32x2 xs;
      __builtin_amdgcn_s_sleep(PRE7);
      first_words((int)((P4 - hand) + (tni * 16 + (lane & 15)) * 192 + ((lane >> 4) % 3) * 64) * 4, std::integral_constant<int, 1>{});
#pragma unroll 1
      for (int spin = 0; spin < SPIN_MAX; ++spin) {
        u32x4 v[3][4];
        xs = __builtin_amdgcn_raw_buffer_load_b64(rsh, (int)((XS - hand) + lane * 2) * 4, 0, 16);
#pragma unroll
        for (int p = 2; p >= 0; --p)
#pragma unroll
          for (int i = 0; i < 4; ++i) v[p][i] = __builtin_amdgcn_raw_buffer_load_b128(p ? rsh : rsq, src + p * 256, i * (4 * 192 * 4), 16);
        auto part = [&](int p) __attribute__((always_inline)) {
          float z[4];
#pragma unroll
          for (int e = 0; e < 4; ++e) {
            z[e] = fadd(fadd(__uint_as_float(v[p][0][e]), __uint_as_float(v[p][1][e])), fadd(__uint_as_float(v[p][2][e]), __uint_as_float(v[p][3][e])));
          }
          val[p] = fold16(f32x2{z[0], z[1]}, f32x2{z[2], z[3]});
        };
        part(2); part(1);
        val[0] = 0.f;
        if (fast) part(0);
        const float chk = fadd(fadd(fadd(val[0], val[1]), fadd(val[2], __uint_as_float(xs[0]))), __uint_as_float(xs[1]));
        if (none(chk != chk)) break;
        unsigned mx = umax(xs[0], xs[1]);
#pragma unroll
        for (int i = 0; i < 12; ++i) mx = umax(umax(umax(mx, v[i >> 2][i & 3][0]), v[i >> 2][i & 3][1]), umax(v[i >> 2][i & 3][2], v[i >> 2][i & 3][3]));
        if (none(mx == SENT)) break;
        __builtin_amdgcn_s_sleep(PSLP);
        asm volatile("" ::: "memory");
      }
      const auto sw = __builtin_amdgcn_permlane32_swap(xs[0], xs[1], false, false);
      float sm = __uint_as_float(sw[0]) + __uint_as_float(sw[1]);
      sm = dpp_add<0x142, 0xa>(dpp_add<0x118, 0xf>(dpp_add<0x114, 0xf>(dpp_add<0x112, 0xf>(dpp_add<0x111, 0xf>(sm)))));
      const float dm = __int_as_float(__builtin_amdgcn_readlane(__float_as_int(sm), 31)) * (0.25f / 1024.0f);
      const float var = fmaxf(__int_as_float(__builtin_amdgcn_readlane(__float_as_int(sm), 63)) * (0.25f / 1024.0f) - dm * dm, 0.f);
      const float mb = rbf(rmb + dm);
      const float rin = frcp(rbf(__builtin_amdgcn_sqrtf(rbf(rbf(var) + 1e-5f))));
      const unsigned kv = pack2((val[1] - mb * cs[1]) * rin, (val[2] - mb * cs[2]) * rin);
      *LDS_AT(u16, lbase + TAB + 128 + kpos(col) * 2) = (u16)kv;
      *LDS_AT(u16, lbase + TAB + 256 + col * 2) = (u16)(kv >> 16);
      if (fast) *LDS_AT(u16, lbase + TAB + kpos(col) * 2) = f2bf((val[0] - mb * cs[0]) * rin * QSCALE);
      signal(7u);
      STAMP(dk, 7);
      if (own) {
#pragma unroll
        for (int i = 0; i < 14; ++i)
          __builtin_amdgcn_raw_buffer_store_b128(u32x4{SENT, SENT, SENT, SENT}, rsh, (GRAN_N + (i * 16 + (lane >> 2)) * D_ + ow * 16 + (lane & 3) * 4) * 4, 0, 16);
      }
      return;
    }
    if (!fast) return;
    const int head = tni * 2 + hw;
    const unsigned tb = lbase + TAB + hw * 64;
    {
      const __amdgpu_buffer_rsrc_t rs = __builtin_amdgcn_make_buffer_rsrc(KV, 0, 2 * S_ * D_ * 2, 0x00020000);
      const int src = (head * 64 + tmi * 4) * 2048 + lane * 16;
      auto ld = [&](int i, unsigned off) { return __builtin_bit_cast(s16x8, __builtin_amdgcn_raw_buffer_load_b128(rs, src + (i & 3) * 1024, (i >> 3) * 65536 + ((i >> 2) & 1) * 4096 + off, 16)); };
#pragma unroll 1
      for (int i = 0; i < SPIN_MAX; ++i)
        if (__builtin_amdgcn_readfirstlane(ld_ag(kvflag + (tmi % RK) * 32 + tmi + 8)) == tag) break;
      asm volatile("" ::: "memory");
      s16x8 f[16];
#pragma unroll
      for (int i = 0; i < 16; ++i) f[i] = ld(i, 0u);
      PIN();
      const bool on = ((lane ^ (lane >> 4)) & 1) == 0;
      const unsigned pl = lbase + RED + hw * 1024;
      unsigned pw = pl + (lane >> 4) * 64 + (lane & 15) * 2, pr = pl + (on ? (lane >> 5) * 16 : 512), qr = on ? tb + (lane >> 5) * 16 : pl + 512;
      const int gone = (((idx >> 7) & 7) == tmi) ? ((idx >> 10) * 2 + (kpos(idx & 31) >> 4)) * 64 + ((idx >> 5) & 3) * 16 + (kpos(idx & 31) & 15) : -1;
      asm volatile("" : "+v"(pw), "+v"(pr), "+v"(qr));
      *LDS_AT(u32x2, pl + 512 + lane * 8) = u32x2{0u, 0u};
      wait_all(7u);
      u32x4 q[2];
#pragma unroll
      for (int sx = 0; sx < 2; ++sx) {
        q[sx] = *LDS_AT(const u32x4, qr + sx * 32);
      }
      float sc[4];
#pragma unroll
      for (int i = 0; i < 8; ++i) {
        f32x4 d = MFMA16(__builtin_bit_cast(s16x8, q[0]), f[2 * i], (f32x4{0.f, 0.f, 0.f, 0.f}));
        d = MFMA16(__builtin_bit_cast(s16x8, q[1]), f[2 * i + 1], d);
        PIN();
        f[2 * i] = ld(2 * i, VOFF); f[2 * i + 1] = ld(2 * i + 1, VOFF);
        PIN();
#pragma unroll
        for (int r = 0; r < 2; ++r) sc[(i >> 2) * 2 + r] = (i & 3) == 0 || (lane >> 4) == (i & 3) ? d[r] : sc[(i >> 2) * 2 + r];
      }
      if (gone >= 0) {
#pragma unroll
        for (int r = 0; r < 4; ++r) sc[r] = r * 64 + lane == gone ? -INFINITY : sc[r];
      }
      const float mx = wave_fmax(fmaxf(fmaxf(sc[0], sc[1]), fmaxf(sc[2], sc[3])));
      const unsigned p0 = pack2(__builtin_amdgcn_exp2f(sc[0] - mx), __builtin_amdgcn_exp2f(sc[1] - mx)), p1 = pack2(__builtin_amdgcn_exp2f(sc[2] - mx), __builtin_amdgcn_exp2f(sc[3] - mx));
      *LDS_AT(u16, pw) = (u16)p0; *LDS_AT(u16, pw + 32) = (u16)(p0 >> 16);
      *LDS_AT(u16, pw + 256) = (u16)p1; *LDS_AT(u16, pw + 288) = (u16)(p1 >> 16);
      asm volatile("" ::: "memory");
      const float ls = wave_sum((__uint_as_float(p0 << 16) + __uint_as_float(p0 & 0xffff0000u)) + (__uint_as_float(p1 << 16) + __uint_as_float(p1 & 0xffff0000u)));
      f32x4 o = {0.f, 0.f, 0.f, 0.f};
#pragma unroll
      for (int i = 0; i < 16; ++i) o = MFMA16(*LDS_AT(const s16x8, pr + i * 32), f[i], o);
      int pv = __float_as_int((lane & 16) ? o[1] : o[0]);
      asm volatile("s_nop 0\n\tv_writelane_b32 %0, %1, 32\n\tv_writelane_b32 %0, %2, 33" : "+v"(pv) : "s"(ls), "s"(mx));
      __builtin_amdgcn_raw_buffer_store_b32(f2word(__int_as_float(pv)), rsh, lane < 34 ? (int)((PA - hand) + (head * 8 + tmi) * 64 + lane) * 4 : HAND_N * 4, 0, 16);
    }
    if (wid == 8) STAMP(dk, 8);
    if (tmi != ((idx >> 7) & 7)) return;
    {
      const int src = (int)((PA - hand) + head * 512 + (lane < 34 ? lane : 0)) * 4;
      const unsigned ta = tb + (lane & 31) * 2;
      const float so = wave_sum(lane < 32 ? bf2f(*LDS_AT(const u16, ta)) * bf2f(*LDS_AT(const u16, ta + 128)) : 0.f);
      const float vo = lane < 32 ? bf2f(*LDS_AT(const u16, ta + 256)) : 1.0f;
      unsigned v[8];
      __builtin_amdgcn_s_sleep(PRE5);
#pragma unroll 1
      for (int spin = 0; spin < SPIN_MAX; ++spin) {
        unsigned mx = 0u;
#pragma unroll
        for (int i = 0; i < 8; ++i) v[i] = __builtin_amdgcn_raw_buffer_load_b32(rsh, src + i * 256, 0, 16);
#pragma unroll
        for (int i = 0; i < 8; ++i) mx = umax(mx, v[i]);
        if (none(mx == SENT)) break;
        __builtin_amdgcn_s_sleep(PSLP);
        asm volatile("" ::: "memory");
      }
      float m8[8], mx = so;
#pragma unroll
      for (int i = 0; i < 8; ++i) { m8[i] = __int_as_float(__builtin_amdgcn_readlane((int)v[i], 33)); mx = fmaxf(mx, m8[i]); }
      float acc = __builtin_amdgcn_exp2f(so - mx) * vo;
#pragma unroll
      for (int i = 0; i < 8; ++i) acc += __builtin_amdgcn_exp2f(m8[i] - mx) * __uint_as_float(v[i]);
      const float l = __int_as_float(__builtin_amdgcn_readlane(__float_as_int(acc), 32));
      const float ov = acc / l;
      const float o1 = __int_as_float(__builtin_amdgcn_update_dpp(0, __float_as_int(ov), 0x55, 0xf, 0xf, false));
      const float o2 = __int_as_float(__builtin_amdgcn_update_dpp(0, __float_as_int(ov), 0xaa, 0xf, 0xf, false));
      const float o3 = __int_as_float(__builtin_amdgcn_update_dpp(0, __float_as_int(ov), 0xff, 0xf, 0xf, false));
      const __amdgpu_buffer_rsrc_t rso = __builtin_amdgcn_make_buffer_rsrc(Of, 0, S_ * D_ * 2, 0x00020000);
      if (lane < 32 && (lane & 3) == 0)
        __builtin_amdgcn_raw_buffer_store_b64(u32x2{pack2(ov, o1), pack2(o2, o3)}, rso,
            (((idx >> 5) * 32 + head) * 1024 + (lane >> 4) * 512 + (((lane >> 2) & 1) * 32 + (idx & 31)) * 8 + ((lane >> 3) & 1) * 4) * 2, 0, 16);
      asm volatile("s_waitcnt vmcnt(0)" ::: "memory");
      if (lane < RO) st_ag(gran + GR_O + ((16 + (idx >> 7)) * RO + lane) * 64 + 16 + head, tag);
      STAMP(dk, 9);
    }
    }();
    }();
    __builtin_amdgcn_s_setprio(0);
    asm volatile("s_waitcnt vmcnt(0) lgkmcnt(0)" ::: "memory");
    PIN();
    ARGS_HERE;
    u32x2 w[16];
    auto ld_wd = [&]() __attribute__((always_inline)) {
      const __amdgpu_buffer_rsrc_t rd_ = __builtin_amdgcn_make_buffer_rsrc(const_cast<u16*>(wd), 0, D_ * F_ * 2, 0x00020000);
#pragma unroll
      for (int f = 0; f < 16; ++f) w[f] = __builtin_amdgcn_raw_buffer_load_b64(rd_, t * 8 + (f & 1) * 2048, (fg * 16 + (f & ~1)) * (D_ * 2), 0);
      PIN();
    };
    float fnum = 0.f, fvar = 0.f;
    u32x2 wq = {0u, 0u};
    const int rrow = omi * 128 + (oni >> 3) * 64 + (hw & 1) * 32;
    if (LAY2) {
      ld_wd();
      wq = *reinterpret_cast<const u32x2*>(w_r + hw * 256 + (lane & 3) * 64 + (lane >> 2) * 4);
      if (!p1blk) {
        const int fr = rrow + (lane & 7) * 4 + (lane >> 4);
        const double s1 = ld_ag(stats + fr), s2 = ld_ag(stats + S_ + fr), sw = ld_ag(stats + 2 * S_ + fr);
        lds_wr(lbase + SPM + 132, __float_as_uint(wsum));
        float mean, var, mb, den;
        ln_from_sums(s1, s2, mean, var, mb, den);
        const float g1 = 1.0f + frcp(den);
        fnum = fr == idx ? __uint_as_float(0x7fc00000u) : fmaf(-mean, wsum, (float)sw) * g1;
        fvar = var * g1 * g1;
      }
      asm volatile("" : "+v"(fnum), "+v"(fvar));
      PIN();
    }
    if (hw == 3) {
      const int nfl = (LAY2 && (idx >> 7) == omi) ? 48 : 16;
      auto pfl = (__attribute__((address_space(1))) const unsigned*)(gran + GR_O + (((LAY2 ? 16 : 0) + omi) * RO + oni % RO) * 64 + (lane < nfl ? lane : 0));
      constexpr unsigned vs = 8u * D_ * 2u - 1024u;
      unsigned vw0 = (unsigned)((lane >> 3) * D_ + oni * 64 + ((lane & 7) ^ ((lane >> 3) & 6)) * 8) * 2u, vw1 = vw0 + vs, vw2 = vw0 + 2 * vs, vw3 = vw0 + 3 * vs;
      asm volatile("" : "+v"(pfl), "+v"(vw0), "+v"(vw1), "+v"(vw2), "+v"(vw3));
      auto wait_arr = [&](unsigned n) {
#pragma unroll 1
        for (int i = 0; i < 4 * SPIN_MAX; ++i) {
          if (__builtin_amdgcn_readfirstlane(*LDS_AT(const volatile unsigned, lbase + FLG + 16)) >= n) break;
          __builtin_amdgcn_s_sleep(1);
        }
        asm volatile("" ::: "memory");
        PIN();
      };
      wait_arr(12u);
      unsigned fl;
      asm volatile("global_load_dword %0, %1, off sc1" : "=v"(fl) : "v"(pfl) : "memory");
#pragma unroll 1
      for (int i = 0; i < 4 * SPIN_MAX; ++i) {
        if (__builtin_amdgcn_readfirstlane(*LDS_AT(const volatile unsigned, lbase + FLG + 24)) >= 8u) break;
        __builtin_amdgcn_s_sleep(1);
      }
      asm volatile("" ::: "memory");
      PIN();
#pragma unroll
      for (int j = 24; j < 32; ++j)
        glds16x4<false>(reinterpret_cast<const char*>(wo) + j * (32u * D_ * 2u), vw0, vw1, vw2, vw3, lbase + j * 4096);
      unsigned keep;
      asm volatile("s_mov_b32 %0, m0\n\ts_mov_b32 m0, %3\n\ts_nop 2\n\tglobal_load_lds_dword %1, %2 sc1\n\ts_mov_b32 m0, %0"
                   : "=&s"(keep) : "v"(0u), "s"(reinterpret_cast<const char*>(gran + GR_N + bid * 64 + 39)), "s"(lbase + FLG - 252) : "memory");
      asm volatile("s_waitcnt vmcnt(33)" : "+v"(fl) :: "memory");
      if (__builtin_amdgcn_ballot_w64(fl != tag)) {
        __builtin_amdgcn_s_sleep(CSL1);
#pragma unroll 1
        for (int spin = 0; spin < SPIN_MAX; ++spin) {
          if (!__builtin_amdgcn_ballot_w64(__hip_atomic_load(pfl, __ATOMIC_RELAXED, __HIP_MEMORY_SCOPE_AGENT) != tag)) break;
          __builtin_amdgcn_s_sleep(CSLP);
        }
      }
      asm volatile("" ::: "memory");
      PIN();
    }
    u32x4 pv[4];
    asm volatile("" : "=v"(pv[0]), "=v"(pv[1]), "=v"(pv[2]), "=v"(pv[3]));
    if (LAY2 && !p1blk && hw < 2) {
      const __amdgpu_buffer_rsrc_t rsp = __builtin_amdgcn_make_buffer_rsrc(hand + 2 * HAND_N + PP_O, 0, H_ * S_ * 4, 0x00020000);
      const int src = ((lane >> 3) * S_ + rrow + (lane & 7) * 4) * 4;
#pragma unroll 1
      for (int i = 0; i < 4 * SPIN_MAX; ++i) {
        if (__builtin_amdgcn_readfirstlane(*LDS_AT(const volatile unsigned, lbase + FLG + 24)) >= 8u) break;
        __builtin_amdgcn_s_sleep(2);
      }
#pragma unroll 1
      for (int spin = 0; spin < SPIN_MAX; ++spin) {
        asm volatile("" ::: "memory");
        unsigned mx = 0u;
#pragma unroll
        for (int i = 0; i < 4; ++i) pv[i] = __builtin_amdgcn_raw_buffer_load_b128(rsp, src, i * (8 * S_ * 4), 16);
#pragma unroll
        for (int i = 0; i < 4; ++i) mx = umax(umax(umax(mx, pv[i][0]), pv[i][1]), umax(pv[i][2], pv[i][3]));
        if (none(mx == SENT)) break;
        __builtin_amdgcn_s_sleep(PPSLP);
      }
    }
    asm volatile("s_waitcnt lgkmcnt(0)" ::: "memory");
    __builtin_amdgcn_s_barrier();
    if (!LAY2) {
      PIN();
      asm volatile(AGAIN "\ngate_up_code:\n\t"
                 "v_lshlrev_b32 v84, 16, v100\n\tv_and_b32 v85, 0xffff0000, v100\n\tv_lshlrev_b32 v86, 16, v101\n\tv_and_b32 v87, 0xffff0000, v101\n\t"
                 "v_subrev_f32 v84, s84, v84\n\tv_subrev_f32 v85, s84, v85\n\tv_subrev_f32 v86, s84, v86\n\tv_subrev_f32 v87, s84, v87\n\t"
                 "v_cvt_pk_bf16_f32 v85, v84, v85\n\tv_cvt_pk_bf16_f32 v87, v86, v87\n\ts_mov_b32 s88, 0x01000100\n\ts_mov_b32 s89, 0x03020302\n\t"
                 "v_lshlrev_b32 v84, 16, v85\n\tv_and_b32 v85, 0xffff0000, v85\n\tv_lshlrev_b32 v86, 16, v87\n\tv_and_b32 v87, 0xffff0000, v87\n\t"
                 "v_mul_f32 v88, s86, v84\n\tv_mul_f32 v89, s86, v85\n\tv_mul_f32 v90, s86, v86\n\tv_mul_f32 v91, s86, v87\n\tv_fma_f32 v84, -v88, s85, v84\n\t"
                 "v_fma_f32 v85, -v89, s85, v85\n\tv_fma_f32 v86, -v90, s85, v86\n\tv_fma_f32 v87, -v91, s85, v87\n\tv_fmac_f32 v88, s86, v84\n\t"
                 "v_fmac_f32 v89, s86, v85\n\tv_fmac_f32 v90, s86, v86\n\tv_fmac_f32 v91, s86, v87\n\tv_cvt_pk_bf16_f32 v88, v88, v89\n\t"
                 "v_cvt_pk_bf16_f32 v90, v90, v91\n\tv_perm_b32 v92, v88, v88, s88\n\tv_perm_b32 v93, v88, v88, s89\n\tv_perm_b32 v94, v90, v90, s88\n\t"
                 "v_perm_b32 v95, v90, v90, s89\n\ts_nop 0\n\tv_and_b32_dpp v96, v92, v102 quad_perm:[0,0,0,0] row_mask:0xf bank_mask:0xf\n\t"
                 "v_and_b32_dpp v97, v92, v103 quad_perm:[0,0,0,0] row_mask:0xf bank_mask:0xf\n\t"
                 "v_and_b32_dpp v100, v93, v102 quad_perm:[0,0,0,0] row_mask:0xf bank_mask:0xf\n\t"
                 "v_and_b32_dpp v101, v93, v103 quad_perm:[0,0,0,0] row_mask:0xf bank_mask:0xf\n\tv_mfma_f32_4x4x4_16b_bf16 v[84:87], v[96:97], v[104:105], 0\n\t"
                 "v_mfma_f32_4x4x4_16b_bf16 v[88:91], v[96:97], v[106:107], 0\n\tv_and_b32_dpp v96, v94, v102 quad_perm:[0,0,0,0] row_mask:0xf bank_mask:0xf\n\t"
                 "v_and_b32_dpp v97, v94, v103 quad_perm:[0,0,0,0] row_mask:0xf bank_mask:0xf\n\t"
                 "v_mfma_f32_4x4x4_16b_bf16 v[84:87], v[100:101], v[108:109], v[84:87]\n\tv_mfma_f32_4x4x4_16b_bf16 v[88:91], v[100:101], v[110:111], v[88:91]\n\t"
                 "v_and_b32_dpp v100, v95, v102 quad_perm:[0,0,0,0] row_mask:0xf bank_mask:0xf\n\t"
                 "v_and_b32_dpp v101, v95, v103 quad_perm:[0,0,0,0] row_mask:0xf bank_mask:0xf\n\tv_mfma_f32_4x4x4_16b_bf16 v[84:87], v[96:97], v[112:113], v[84:87]\n\t"
                 "v_mfma_f32_4x4x4_16b_bf16 v[88:91], v[96:97], v[114:115], v[88:91]\n\tv_and_b32_dpp v96, v92, v102 quad_perm:[1,1,1,1] row_mask:0xf bank_mask:0xf\n\t"
                 "v_and_b32_dpp v97, v92, v103 quad_perm:[1,1,1,1] row_mask:0xf bank_mask:0xf\n\t"
                 "v_mfma_f32_4x4x4_16b_bf16 v[84:87], v[100:101], v[116:117], v[84:87]\n\tv_mfma_f32_4x4x4_16b_bf16 v[88:91], v[100:101], v[118:119], v[88:91]\n\t"
                 "v_and_b32_dpp v100, v93, v102 quad_perm:[1,1,1,1] row_mask:0xf bank_mask:0xf\n\t"
                 "v_and_b32_dpp v101, v93, v103 quad_perm:[1,1,1,1] row_mask:0xf bank_mask:0xf\n\tv_mfma_f32_4x4x4_16b_bf16 v[84:87], v[96:97], v[120:121], v[84:87]\n\t"
                 "v_mfma_f32_4x4x4_16b_bf16 v[88:91], v[96:97], v[122:123], v[88:91]\n\tv_and_b32_dpp v96, v94, v102 quad_perm:[1,1,1,1] row_mask:0xf bank_mask:0xf\n\t"
                 "v_and_b32_dpp v97, v94, v103 quad_perm:[1,1,1,1] row_mask:0xf bank_mask:0xf\n\t"
                 "v_mfma_f32_4x4x4_16b_bf16 v[84:87], v[100:101], v[124:125], v[84:87]\n\tv_mfma_f32_4x4x4_16b_bf16 v[88:91], v[100:101], v[126:127], v[88:91]\n\t"
                 "v_and_b32_dpp v100, v95, v102 quad_perm:[1,1,1,1] row_mask:0xf bank_mask:0xf\n\t"
                 "v_and_b32_dpp v101, v95, v103 quad_perm:[1,1,1,1] row_mask:0xf bank_mask:0xf\n\tv_mfma_f32_4x4x4_16b_bf16 v[84:87], v[96:97], v[128:129], v[84:87]\n\t"
                 "v_mfma_f32_4x4x4_16b_bf16 v[88:91], v[96:97], v[130:131], v[88:91]\n\tv_and_b32_dpp v96, v92, v102 quad_perm:[2,2,2,2] row_mask:0xf bank_mask:0xf\n\t"
                 "v_and_b32_dpp v97, v92, v103 quad_perm:[2,2,2,2] row_mask:0xf bank_mask:0xf\n\t"
                 "v_mfma_f32_4x4x4_16b_bf16 v[84:87], v[100:101], v[132:133], v[84:87]\n\tv_mfma_f32_4x4x4_16b_bf16 v[88:91], v[100:101], v[134:135], v[88:91]\n\t"
                 "v_and_b32_dpp v100, v93, v102 quad_perm:[2,2,2,2] row_mask:0xf bank_mask:0xf\n\t"
                 "v_and_b32_dpp v101, v93, v103 quad_perm:[2,2,2,2] row_mask:0xf bank_mask:0xf\n\tv_mfma_f32_4x4x4_16b_bf16 v[84:87], v[96:97], v[136:137], v[84:87]\n\t"
                 "v_mfma_f32_4x4x4_16b_bf16 v[88:91], v[96:97], v[138:139], v[88:91]\n\tv_and_b32_dpp v96, v94, v102 quad_perm:[2,2,2,2] row_mask:0xf bank_mask:0xf\n\t"
                 "v_and_b32_dpp v97, v94, v103 quad_perm:[2,2,2,2] row_mask:0xf bank_mask:0xf\n\t"
                 "v_mfma_f32_4x4x4_16b_bf16 v[84:87], v[100:101], v[140:141], v[84:87]\n\tv_mfma_f32_4x4x4_16b_bf16 v[88:91], v[100:101], v[142:143], v[88:91]\n\t"
                 "v_and_b32_dpp v100, v95, v102 quad_perm:[2,2,2,2] row_mask:0xf bank_mask:0xf\n\t"
                 "v_and_b32_dpp v101, v95, v103 quad_perm:[2,2,2,2] row_mask:0xf bank_mask:0xf\n\tv_mfma_f32_4x4x4_16b_bf16 v[84:87], v[96:97], v[144:145], v[84:87]\n\t"
                 "v_mfma_f32_4x4x4_16b_bf16 v[88:91], v[96:97], v[146:147], v[88:91]\n\tv_and_b32_dpp v96, v92, v102 quad_perm:[3,3,3,3] row_mask:0xf bank_mask:0xf\n\t"
                 "v_and_b32_dpp v97, v92, v103 quad_perm:[3,3,3,3] row_mask:0xf bank_mask:0xf\n\t"
                 "v_mfma_f32_4x4x4_16b_bf16 v[84:87], v[100:101], v[148:149], v[84:87]\n\tv_mfma_f32_4x4x4_16b_bf16 v[88:91], v[100:101], v[150:151], v[88:91]\n\t"
                 "v_and_b32_dpp v100, v93, v102 quad_perm:[3,3,3,3] row_mask:0xf bank_mask:0xf\n\t"
                 "v_and_b32_dpp v101, v93, v103 quad_perm:[3,3,3,3] row_mask:0xf bank_mask:0xf\n\tv_mfma_f32_4x4x4_16b_bf16 v[84:87], v[96:97], v[152:153], v[84:87]\n\t"
                 "v_mfma_f32_4x4x4_16b_bf16 v[88:91], v[96:97], v[154:155], v[88:91]\n\tv_and_b32_dpp v96, v94, v102 quad_perm:[3,3,3,3] row_mask:0xf bank_mask:0xf\n\t"
                 "v_and_b32_dpp v97, v94, v103 quad_perm:[3,3,3,3] row_mask:0xf bank_mask:0xf\n\t"
                 "v_mfma_f32_4x4x4_16b_bf16 v[84:87], v[100:101], v[156:157], v[84:87]\n\tv_mfma_f32_4x4x4_16b_bf16 v[88:91], v[100:101], v[158:159], v[88:91]\n\t"
                 "v_and_b32_dpp v100, v95, v102 quad_perm:[3,3,3,3] row_mask:0xf bank_mask:0xf\n\t"
                 "v_and_b32_dpp v101, v95, v103 quad_perm:[3,3,3,3] row_mask:0xf bank_mask:0xf\n\tv_mfma_f32_4x4x4_16b_bf16 v[84:87], v[96:97], v[160:161], v[84:87]\n\t"
                 "v_mfma_f32_4x4x4_16b_bf16 v[88:91], v[96:97], v[162:163], v[88:91]\n\ts_nop 1\n\t"
                 "v_mfma_f32_4x4x4_16b_bf16 v[84:87], v[100:101], v[164:165], v[84:87]\n\tv_mfma_f32_4x4x4_16b_bf16 v[88:91], v[100:101], v[166:167], v[88:91]\n\t"
                 "s_nop 4\n\tv_permlane32_swap_b32 v84, v88\n\tv_permlane32_swap_b32 v85, v89\n\tv_permlane32_swap_b32 v86, v90\n\tv_permlane32_swap_b32 v87, v91\n\t"
                 "v_add_f32 v84, v84, v88\n\tv_add_f32 v85, v85, v89\n\tv_add_f32 v86, v86, v90\n\tv_add_f32 v87, v87, v91\n\ts_nop 0\n\t"
                 "v_permlane16_swap_b32 v84, v85\n\tv_permlane16_swap_b32 v86, v87\n\ts_nop 0\n\tv_add_f32 v84, v84, v85\n\tv_add_f32 v86, v86, v87\n\ts_nop 1\n\t"
                 "v_add_f32_dpp v84, v84, v84 row_ror:8 row_mask:0xf bank_mask:0xf\n\tv_add_f32_dpp v86, v86, v86 row_ror:8 row_mask:0xf bank_mask:0xf\n\ts_nop 0\n\t"
                 "v_add_f32_dpp v84, v84, v84 row_ror:4 row_mask:0xf bank_mask:0xf\n\tv_add_f32_dpp v86, v86, v86 row_ror:4 row_mask:0xf bank_mask:0xf\n\t"
                 "ds_write2_b32 v99, v84, v86 offset1:8\n\ts_setpc_b64 s[92:93]\n\t"
                   :: "{s[0:1]}"(ap + 1), "{s2}"(bid), "{v0}"(wid * 64), KEPT() : "s90", "s91", "scc", "memory");
      __builtin_unreachable();
    }
    {
    constexpr int dk = 8;
    constexpr int BEST = FLG + 40, MEET = FLG + 48;
    __builtin_amdgcn_s_setprio(HPRIO);
    if (hw == 0) STAMP(dk, 0);
    const unsigned tb1 = (unsigned)__builtin_amdgcn_s_memrealtime();
    unsigned* hand2 = hand + HAND_N;
    const __amdgpu_buffer_rsrc_t rsh = __builtin_amdgcn_make_buffer_rsrc(hand2, 0, (HAND_N + PS_N) * 4, 0x00020000);
    const __amdgpu_buffer_rsrc_t rsx = __builtin_amdgcn_make_buffer_rsrc(xout, 0, 3 * D_ * 2, 0x00020000);
    constexpr int OOR = (HAND_N + PS_N) * 4;
    if (!p1blk && hw < 2) {
      f32x2 a[2], c[2];
#pragma unroll
      for (int e = 0; e < 4; ++e) {
        a[e >> 1][e & 1] = (__uint_as_float(pv[0][e] << 16) + __uint_as_float(pv[1][e] << 16)) + (__uint_as_float(pv[2][e] << 16) + __uint_as_float(pv[3][e] << 16));
        c[e >> 1][e & 1] = (__uint_as_float(pv[0][e] & 0xffff0000u) + __uint_as_float(pv[1][e] & 0xffff0000u)) + (__uint_as_float(pv[2][e] & 0xffff0000u) + __uint_as_float(pv[3][e] & 0xffff0000u));
      }
      const float r = (fnum + fold8(a[0], a[1])) * __builtin_amdgcn_rsqf(fvar + fold8(c[0], c[1]));
      unsigned key = (__float_as_uint(r) ^ ((unsigned)(__float_as_int(r) >> 31) | 0x80000000u)) & 0xfffff800u;
      if (r != r) key = 0u;
      key = wave_umax(key | (unsigned)(2047 - (rrow + (lane & 7) * 4 + (lane >> 4))));
      __builtin_amdgcn_raw_buffer_store_b64(u32x2{key, tb1 & 0x7fffffffu}, rsh, lane < NREP ? (HAND_N + PK_O + lane * 128 + (rrow >> 5) * 2) * 4 : OOR, 0, 16);
      if (hw == 1) STAMP(dk, 2);
    }
    gu_setup();
    bool got = true;
    unsigned fra = lbase;
    int fro = S_ * D_ * 2;
    auto get_fc = [&]() __attribute__((always_inline)) {
      const u32x2 fw = __builtin_amdgcn_raw_buffer_load_b64(rsh, (HAND_N + PK_O + (bid % NREP) * 128 + lane * 2) * 4, 0, 16);
      got = none(umax(fw[0], fw[1]) == SENT);
      if (got) {
        const unsigned c1 = wave_umax(fw[0]), c2 = wave_umax(fw[0] == c1 ? 0u : fw[0]);
#define BADFC 0
        const unsigned r1 = (2047u - (c1 & 0x7ffu)) ^ BADFC, r2 = 2047u - (c2 & 0x7ffu);
        const unsigned r = (lane & 8) ? r2 : r1;
        fra = lbase + QOFF + (r & 127u) * 128 + (((r ^ lane) & 7u) << 4);
        fro = (r >> 7) == (unsigned)omi && lane < 16 ? ((lane >> 3) * D_ + oni * 64 + (lane & 7) * 8) * 2 : S_ * D_ * 2;
        unsigned dt = (unsigned)((int)((fw[1] - tb1) << 1) >> 1) + 512u;
        dt = dt < 1024u ? dt : 512u;
        *LDS_AT(volatile u32x2, lbase + SPM + 144) = u32x2{r1 | (r2 << 16), (unsigned)__builtin_amdgcn_readlane(dt, r1 >> 5) | (wave_umax(dt) << 16)};
      }
    };
    if (hw == 0) {
      __builtin_amdgcn_s_sleep(FCPRE);
#pragma unroll 1
      for (int i = 0; i < SPIN_MAX; ++i) {
        if (__builtin_amdgcn_readfirstlane(*LDS_AT(const volatile unsigned, lbase + FLG + 28)) - best >= 4u) break;
        __builtin_amdgcn_s_sleep(FCSLP);
      }
      asm volatile("" ::: "memory");
      get_fc();
    } else __builtin_amdgcn_s_sleep(TPRE - 10);
    PIN();
    tile_at(2u);
    const unsigned tb2 = (unsigned)__builtin_amdgcn_s_memrealtime() - 512u;
    LOGDEF;
    LOG();
#pragma unroll 1
    for (int spin = 0; spin < SPIN_MAX && !got; ++spin) { get_fc(); asm volatile("" ::: "memory"); }
    if (hw == 0) __builtin_amdgcn_raw_buffer_store_b128(lds_rd4(fra), rsx, fro, 0, XAUX);
    PIN();
    u32x2 fm = {SENT, 0u};
#pragma unroll 1
    for (int i = 0; i < 4 * SPIN_MAX && fm[0] == SENT; ++i) {
      const u32x2 q = *LDS_AT(const volatile u32x2, lbase + SPM + 144);
      fm = u32x2{(unsigned)__builtin_amdgcn_readfirstlane(q[0]), (unsigned)__builtin_amdgcn_readfirstlane(q[1])};
    }
    const int fr1 = fm[0] & 0x7ffu, fr2 = (fm[0] >> 16) & 0x7ffu;
    const unsigned t1 = tb2 + (fm[1] & 1023u), tmx = tb2 + ((fm[1] >> 16) & 1023u);
    auto until = [&](unsigned te) { while ((int)((unsigned)__builtin_amdgcn_s_memrealtime() - te) < 0) __builtin_amdgcn_s_sleep(1); asm volatile("" ::: "memory"); };
    lds_wr(lbase + SPM + 136, tmx);
    PIN();
    if (hw == 0) STAMP(dk, 3);
    const int xoff = (hw * 256 + (lane & 3) * 64 + (lane >> 2) * 4) * 2;
    const int oc = (omi * 2 + (oni >> 3)) * 32, col = oc + (lane & 7) * 4 + (lane >> 4);
    const int xoo = !p1blk && hw == 3 ? (col & ~1) * 2 : S_ * D_ * 2;
    auto get_row = [&](int r, int sl, u32x2& xr, unsigned& xo) __attribute__((always_inline)) {
      if ((r >> 7) == omi) {
#pragma unroll 1
        for (int i = 0; i < SPIN_MAX; ++i) {
          if (__builtin_amdgcn_readfirstlane(*LDS_AT(const volatile unsigned, lbase + FLG + 24)) >= 16u) break;
          __builtin_amdgcn_s_sleep(1);
        }
        asm volatile("" ::: "memory");
      }
#pragma unroll 1
      for (int spin = 0; spin < SPIN_MAX; ++spin) {
        LOG();
        xr = __builtin_amdgcn_raw_buffer_load_b64(rsx, xoff, sl * (D_ * 2), 16);
        xo = __builtin_amdgcn_raw_buffer_load_b32(rsx, xoo, sl * (D_ * 2), 16);
        LOG();
        if (none(umax(umax(xr[0], xr[1]), xo) == SENT)) break;
        __builtin_amdgcn_s_sleep(XRSLP);
        asm volatile("" ::: "memory");
      }
      PIN();
    };
    unsigned rk1;
    auto row_md = [&](const u32x2& xr, int k) __attribute__((always_inline)) {
      auto dot = [&](unsigned x, unsigned y, float z) { return __builtin_amdgcn_fdot2_f32_bf16(__builtin_bit_cast(bf16x2, x), __builtin_bit_cast(bf16x2, y), z, false); };
      const float p1 = dot(xr[1], 0x3f803f80u, dot(xr[0], 0x3f803f80u, 0.f)), p2 = dot(xr[1], xr[1], dot(xr[0], xr[0], 0.f)), p3 = dot(xr[1], wq[1], dot(xr[0], wq[0], 0.f));
      const auto q = __builtin_amdgcn_permlane32_swap(__float_as_uint(p1), __float_as_uint(p2), false, false);
      const auto q3 = __builtin_amdgcn_permlane32_swap(__float_as_uint(p3), __float_as_uint(p3), false, false);
      const auto q2 = __builtin_amdgcn_permlane16_swap(__float_as_uint(__uint_as_float(q[0]) + __uint_as_float(q[1])), __float_as_uint(__uint_as_float(q3[0]) + __uint_as_float(q3[1])), false, false);
      float sv = __uint_as_float(q2[0]) + __uint_as_float(q2[1]);
      sv = dpp_add<0x121, 0xf>(dpp_add<0x122, 0xf>(dpp_add<0x124, 0xf>(dpp_add<0x128, 0xf>(sv))));
      lds_wr(lbase + SPM + k * 64 + (lane >> 4) * 16 + hw * 4, f2word(sv));
      u32x4 a, c, e;
#pragma unroll 1
      for (int i = 0; i < SPIN_MAX; ++i) {
        a = lds_rd4(lbase + SPM + k * 64); c = lds_rd4(lbase + SPM + k * 64 + 32); e = lds_rd4(lbase + SPM + k * 64 + 16);
        if (__builtin_amdgcn_readfirstlane(umax(umax(a[0], a[1]), umax(a[2], a[3]))) != SENT) break;
      }
      auto sum4 = [&](const u32x4& v) { return (__uint_as_float(v[0]) + __uint_as_float(v[1])) + (__uint_as_float(v[2]) + __uint_as_float(v[3])); };
      const float mean = sum4(a) * (1.0f / 1024.0f), msq = sum4(c) * (1.0f / 1024.0f);
      const float den = rbf(__builtin_amdgcn_sqrtf(rbf(rbf(fmaxf(fmaf(-mean, mean, msq), 0.f)) + 1e-5f)));
      if (k == 0) { const float r = fmaf(-rbf(mean), wsum, sum4(e)) * frcp(den); rk1 = __builtin_amdgcn_readfirstlane(pack2(r, r)) >> 16; }
      return (unsigned)__builtin_amdgcn_readfirstlane((pack2(mean, mean) & 0xffffu) | (__float_as_uint(den) & 0xffff0000u));
    };
    auto ffn = [&](const u32x2& xr, unsigned md, unsigned n, const __amdgpu_buffer_rsrc_t rs, int yo, auto&& off) __attribute__((always_inline)) {
      gate_up(xr, md);
      if (hw == 0 && n == 2u) STAMP(dk, 8);
      asm volatile("" ::: "memory");
      PIN();
      lds_wr(lbase + MEET + hw * 4, n + off());
      u32x4 mt;
#pragma unroll 1
      for (int i = 0; i < SPIN_MAX; ++i) {
        mt = lds_rd4(lbase + MEET);
        if (__builtin_amdgcn_readfirstlane(min4(mt)) >= n) break;
      }
      asm volatile("" ::: "memory");
      PIN();
      if (hw == 0 && n == 2u) STAMP(dk, 9);
      if (__builtin_amdgcn_readfirstlane(mt[0]) != n + 1u)
        __builtin_amdgcn_raw_buffer_store_b128(down(act_diag(n == 4u ? 512 : 0), w), rs, (yo + fg * D_) * 4 + t * 16, 0, 16);
      dad += n == 2u ? 512 : -512;
      if (hw == 0 && n == 2u) STAMP(dk, 4);
    };
    auto y_sum4 = [&](const __amdgpu_buffer_rsrc_t rs, int yo, int k) __attribute__((always_inline)) {
      const int src = (yo + (hw * 56 + (lane >> 3)) * D_ + oc + (lane & 7) * 4) * 4;
      u32x4 v[7];
#pragma unroll
      for (int i = 0; i < 7; ++i) asm volatile("" : "=v"(v[i]));
      unsigned todo = 0x7fu;
#pragma unroll 1
      for (int spin = 0; spin < SPIN_MAX; ++spin) {
        LOG();
#pragma unroll
        for (int i = 0; i < 7; ++i) if ((todo >> i) & 1u) v[i] = __builtin_amdgcn_raw_buffer_load_b128(rs, src, i * (8 * D_ * 4), 16);
#pragma unroll
        for (int i = 0; i < 7; ++i) if (none(umax(umax(v[i][0], v[i][1]), umax(v[i][2], v[i][3])) == SENT)) todo &= ~(1u << i);
        if (!todo) break;
        __builtin_amdgcn_s_sleep(YSLP);
        asm volatile("" ::: "memory");
      }
      f32x2 z0 = {0.f, 0.f}, z1 = {0.f, 0.f};
#pragma unroll
      for (int i = 0; i < 7; ++i) {
        z0 += f32x2{__uint_as_float(v[i][0]), __uint_as_float(v[i][1])};
        z1 += f32x2{__uint_as_float(v[i][2]), __uint_as_float(v[i][3])};
      }
      const unsigned ya = lbase + SPM + 256 + k * 512 + ((lane & 7) * 4 + (lane >> 4)) * 16;
      lds_wr((lane & 8) ? lbase + DUM + lane * 4 : ya + hw * 4, f2word(fold8(z0, z1)));
      u32x4 q = {0u, 0u, 0u, 0u};
      if (hw == 3) {
#pragma unroll 1
        for (int i = 0; i < 4 * SPIN_MAX; ++i) {
          q = lds_rd4(ya);
          if (none(umax(umax(q[0], q[1]), umax(q[2], q[3])) == SENT)) break;
        }
      }
      return (__uint_as_float(q[0]) + __uint_as_float(q[1])) + (__uint_as_float(q[2]) + __uint_as_float(q[3]));
    };
    const __amdgpu_buffer_rsrc_t rs1 = __builtin_amdgcn_make_buffer_rsrc(hand, 0, HAND_N * 4, 0x00020000);
    u32x2 xr;
    unsigned xo, xo2;
    until(t1 + XDT);
    get_row(fr1, 0, xr, xo);
    if (hw == 0) STAMP(dk, 7);
#define FAILFC 0
    const unsigned md1 = row_md(xr, 0) ^ (FAILFC & 1);
    if (hw == 0) STAMP(dk, 10);
    LOGAT(20);
    u32x2 xr2;
    unsigned md2 = 0u;
    if (p1blk) {
      xr2 = __builtin_amdgcn_raw_buffer_load_b64(rsx, (fr2 >> 7) == omi ? S_ * D_ * 2 : xoff, D_ * 2, 16);
      PIN();
      ffn(xr, md1, 2u, rsh, GRAN_N, [&]() { return 0u; });
      __builtin_amdgcn_s_setprio(R2PRIO);
      LOG();
      if ((fr2 >> 7) == omi || !none(umax(xr2[0], xr2[1]) == SENT)) get_row(fr2, 1, xr2, xo2);
      md2 = row_md(xr2, 1) ^ (FAILFC & 2);
      ffn(xr2, md2, 4u, rs1, GRAN_N, [&]() {
        const u32x2 v = *LDS_AT(const volatile u32x2, lbase + BEST);
        return v[0] != 0u && (2047 - (int)(v[0] & 0x7ffu) != fr2 || v[1] != md2) ? 1u : 0u; });
    }
    if (hw == 0) STAMP(dk, 5);
    LOG();
    float y = 0.f;
    auto put_row = [&](int r, unsigned md, unsigned rb) __attribute__((always_inline)) {
      const float rden = __uint_as_float(md & 0xffff0000u), rs = rbf(frcp(1.0f + __expf(-__uint_as_float(rb << 16))));
      const float xa = __uint_as_float((col & 1) ? xo & 0xffff0000u : xo << 16);
      float ha = xa, hb = xa;
      ln_apply2(ha, hb, __uint_as_float(md << 16), rden, frcp(rden));
      const float o = xa + rbf(ha + rbf(rbf(y) * rs));
      __builtin_amdgcn_raw_buffer_store_b16((unsigned short)(pack2(o, o) & 0xffffu), __builtin_amdgcn_make_buffer_rsrc(out, 0, hw == 3 ? S_ * D_ * 2 : 0, 0x00020000),
                                            (lane & 8) ? S_ * D_ * 2 : (r * D_ + col) * 2, 0, OAUX);
    };
    if (!p1blk) {
      until(t1 + XDT + YD1); until(tmx + YD2);
      if (hw == 3) STAMP(dk, 13);
      LOGAT(8);
      y = y_sum4(rsh, GRAN_N, 0);
      put_row(fr1, md1, rk1);
      if (hw == 3) STAMP(dk, 14);
      LOGAT(24);
    }
    unsigned bmd = 0u;
    best = 0u;
#pragma unroll 1
    for (int spin = 0; spin < 4 * SPIN_MAX; ++spin) {
      const u32x2 v = *LDS_AT(const volatile u32x2, lbase + BEST);
      best = __builtin_amdgcn_readfirstlane(v[0]); bmd = __builtin_amdgcn_readfirstlane(v[1]);
      if (best) break;
      __builtin_amdgcn_s_sleep(1);
    }
    asm volatile("" ::: "memory");
    const int idx = 2047 - (int)(best & 0x7ffu);
    const unsigned rk = best >> 16, rb = (rk & 0x8000u) ? (rk & 0x7fffu) : (~rk & 0xffffu);
    if (hw == 0) STAMP(dk, 1);
    LOGAT(25);
    LOGOUT(hw);
    if (__builtin_expect(idx != fr1 || bmd != md1 || rb != rk1, 0)) {
      const __amdgpu_buffer_rsrc_t rsf = __builtin_amdgcn_make_buffer_rsrc(gran + GR_F, 0, !p1blk && hw == 3 && idx != fr1 ? 48 * 128 : 0, 0x00020000);
      asm volatile("s_waitcnt vmcnt(0)" ::: "memory");
      __builtin_amdgcn_raw_buffer_store_b32(tag, rsf, lane == 0 ? (16 + (oc >> 5)) * 128 : 48 * 128, 0, 16);
      if (idx != fr1 || bmd != md1) {
        if (!p1blk || idx != fr2 || bmd != md2) get_row(idx, idx == fr1 ? 0 : (idx == fr2 ? 1 : 2), xr, xo);
        if (!p1blk && idx == fr2) md2 = row_md(xr, 1) ^ (FAILFC & 2);
        if (idx == fr2 && bmd == md2) {
          if (!p1blk) y = y_sum4(rs1, GRAN_N, 1);
        } else {
          if (p1blk) ffn(xr, bmd, 6u, rsh, HAND_N + Y3_O, [&]() { return 0u; });
          else y = y_sum4(rsh, HAND_N + Y3_O, 2);
        }
      }
      if (p1blk || hw != 3) return;
#pragma unroll 1
      for (int spin = 0; spin < SPIN_MAX && idx != fr1; ++spin) {
        if (__builtin_amdgcn_readfirstlane(__builtin_amdgcn_raw_buffer_load_b32(rsf, (oc >> 6) * 128, 0, 16)) == tag) break;
        asm volatile("" ::: "memory");
      }
      put_row(idx, bmd, rb);
    }
    if (p1blk || hw != 3) return;
    }
    return;
  }
  if (wid >= 4) {
    const int lane = fresh_lane();
    const int lw = wid - 4;
    const long long lay_ = ap->lay;
    const u16* A = ap->a; const u16* W = ap->W; const u16* wc = ap->wc;
#define LPRIO 3
#define LP0 3
    __builtin_amdgcn_s_setprio(LP0);
    unsigned va[4], vw[6];
    const unsigned ul = lane;
    va[0] = (unsigned)(m0 + lw * 32) * (K * 2) + (ul >> 3) * (K * 2) + (((ul & 7u) ^ (ul >> 4)) << 4);
    va[1] = va[0] + (8 * K * 2 - 1024);
    va[2] = (va[0] ^ 64u) + 2 * (8 * K * 2 - 1024);
    va[3] = va[2] + (8 * K * 2 - 1024);
#pragma unroll
    for (int i = 0; i < 3; ++i) {
      const unsigned n = ul + 64u * i, k = __umul24(n, 171u) >> 12, cp = n - __umul24(k, 24u);
      vw[i] = __umul24(k, ldw * 2) + (cp >> 3) * 2048u + (((cp & 7u) ^ ((k & 2u) << 1)) << 4) + ((unsigned)(lw * 16 * ldw + n0) * 2u - i * 1024u);
      vw[i + 3] = (vw[i] ^ 16u) + 8 * ldw * 2;
    }
    PIN();
    asm volatile("" : "+v"(va[0]), "+v"(va[1]), "+v"(va[2]), "+v"(va[3]), "+v"(vw[0]), "+v"(vw[1]), "+v"(vw[2]), "+v"(vw[3]), "+v"(vw[4]), "+v"(vw[5]));
    const bool LAY2 = lay_ != 0;
    const int krot = tmi & 8;
    if (LAY2 && slow_cols(W, tni)) W = wc;
    auto stage_a = [&](int kt, int slot) {
      const char* pa = reinterpret_cast<const char*>(A) + (size_t)(kt ^ krot) * 128;
      asm volatile("s_mov_b32 m0, %5\n\ts_nop 0\n\tglobal_load_lds_dwordx4 %0, %4 sc1\n\tglobal_load_lds_dwordx4 %1, %4 offset:1024 sc1\n\t"
                   "global_load_lds_dwordx4 %2, %4 offset:2048 sc1\n\tglobal_load_lds_dwordx4 %3, %4 offset:3072 sc1"
                   :: "v"(va[0]), "v"(va[1]), "v"(va[2]), "v"(va[3]), "s"(pa), "s"(lbase + slot * STB + lw * 4096) : "m0", "memory");
    };
    auto stage_w = [&](int kt, int slot) {
      const char* pw = reinterpret_cast<const char*>(W) + (size_t)(kt ^ krot) * 128 * ldw;
      asm volatile("s_mov_b32 m0, %7\n\ts_nop 0\n\tglobal_load_lds_dwordx4 %0, %6 sc1\n\tglobal_load_lds_dwordx4 %1, %6 offset:1024 sc1\n\t"
                   "global_load_lds_dwordx4 %2, %6 offset:2048 sc1\n\ts_addk_i32 m0, 0xc00\n\ts_nop 0\n\tglobal_load_lds_dwordx4 %3, %6 sc1\n\t"
                   "global_load_lds_dwordx4 %4, %6 offset:1024 sc1\n\tglobal_load_lds_dwordx4 %5, %6 offset:2048 sc1"
                   :: "v"(vw[0]), "v"(vw[1]), "v"(vw[2]), "v"(vw[3]), "v"(vw[4]), "v"(vw[5]), "s"(pw), "s"(lbase + slot * STB + ABYTES + lw * 6144) : "m0", "scc", "memory");
    };
    if (LAY2) {
      __builtin_amdgcn_s_setprio(0);
      __builtin_amdgcn_s_sleep(TPRE - 10);
      tile_at(3u);
      __builtin_amdgcn_s_setprio(LPRIO);
#pragma unroll
      for (int st = 0; st < NST; ++st) stage_w(st, st);
      PIN();
      __builtin_amdgcn_s_barrier();
      PIN();
#pragma unroll
      for (int st = 0; st < NST; ++st) stage_a(st, st);
      asm volatile("s_waitcnt vmcnt(8)" ::: "memory");
    } else {
      __builtin_amdgcn_s_setprio(LPRIO);
      stage_a(0, 0); stage_w(0, 0);
      if (PRO > 1) { stage_a(1, 1); stage_w(1, 1); }
      if (PRO > 2) { stage_a(2, 2); stage_w(2, 2); }
      asm volatile("s_waitcnt vmcnt(%0)" :: "n"(10 * PRO - 10) : "memory");
    }
    int slot = 0;
    __builtin_amdgcn_s_barrier();
    if (!LAY2) {
      if (PRO < 2) { stage_a(1, 1); stage_w(1, 1); }
      if (PRO < 3) { stage_a(2, 2); stage_w(2, 2); }
    }
    for (int kt = 0; kt + 1 < NKT; ++kt) {
      if (LAY2 && kt == 0) asm volatile("s_waitcnt vmcnt(4)" ::: "memory");
      else if (kt + 2 >= NKT) asm volatile("s_waitcnt vmcnt(0)" ::: "memory");
      else asm volatile("s_waitcnt vmcnt(10)" ::: "memory");
      __builtin_amdgcn_s_barrier();
      if (kt + NST < NKT) { stage_a(kt + NST, slot); stage_w(kt + NST, slot); }
      slot = slot == NST - 1 ? 0 : slot + 1;
    }
    __builtin_amdgcn_s_setprio(0);
    __builtin_amdgcn_s_barrier();
  } else {
  LAYER_HERE;
  const int dk = LAY2 ? 5 : 1;
  const int lane = fresh_lane(), wm = wid & 1, wn = wid >> 1;
  if (wid == 0) STAMP(dk, 0);
  f32x4 acc[4][6];
#pragma unroll
  for (int i = 0; i < 4; ++i)
#pragma unroll
    for (int j = 0; j < 6; ++j) acc[i][j] = f32x4{0.f, 0.f, 0.f, 0.f};
  const int q16 = lane & 15, g4 = lane >> 4;
  const int e0 = 16 * (q16 >> 3) + (q16 & 7);
  s16x8 af[2][4];
  unsigned aad[2], wad[2];
  {
    const int row = wm * 64 + e0, swz = ((row >> 1) & 3) | (((row >> 4) & 1) << 2);
#pragma unroll
    for (int h = 0; h < 2; ++h) aad[h] = lbase + row * 128 + (((4 * h + g4) ^ swz) << 4);
    const int tq = (lane & 15) >> 2, tp = lane & 3, k = 8 * g4 + tq, ch = wn * 4 + 2 * (tp >> 1);
    wad[0] = lbase + ABYTES + k * 384 + ((ch ^ (4 * ((k >> 1) & 1)) ^ (g4 & 1)) << 4) + 8 * (tp & 1);
    wad[1] = wad[0] ^ 16;
  }
  auto lda = [&](unsigned a, int i) { return *LDS_AT(const s16x8, a + (i >> 1) * 4096 + (i & 1) * 1024); };
  auto ldw_ = [&](int h, int j) {
    const unsigned w = wad[j & 1] + h * 12288 + (j >> 1) * 128;
    s16x4 lo = __builtin_amdgcn_ds_read_tr16_b64_v4i16(LDS_AT(s16x4, w));
    s16x4 h4 = __builtin_amdgcn_ds_read_tr16_b64_v4i16(LDS_AT(s16x4, w + 1536));
    return (s16x8)__builtin_shufflevector(lo, h4, 0, 1, 2, 3, 4, 5, 6, 7);
  };
  ARGS_HERE;
  unsigned lto = lbase + wid * 4096;
  char* kvb = reinterpret_cast<char*>(KV) + ((tni * 2 + wn) * 64 + tmi * 4 + wm * 2) * 2048 - lto;
  {
    const unsigned ato = lbase + ALPHA + wm * 256, dum = lbase + DUM + lane * 4;
    *LDS_AT(u32x4, lto + QOFF + lane * 16) = u32x4{lane >= 16 ? lbase + CRED : lbase + TABA + (wm * 64 + e0) * 16, lbase + TABW + (wn * 32 + e0) * 16,
                                                   ato + e0 * 4, lto + (lane >> 5) * 1024 + ((lane & 28) * 2 + (lane & 3)) * 16};
    *LDS_AT(u32x2, lto + QOFF + 2048 + lane * 16) = u32x2{aad[0], aad[1]};
    *LDS_AT(u32x4, lto + QOFF + 1024 + lane * 16) = u32x4{ato + g4 * 16 + (lane >> 5) * 32, lto + (lane >> 5) * 1024 + ((lane & 24) * 2 + (lane & 7)) * 16,
                                                          lane == 0 ? lbase + FLG + 32 : dum, lane == 0 ? lbase + FLG + 16 : dum};
  }
  asm volatile("" : "+s"(lto), "+s"(kvb) :: "memory");
#pragma unroll
  for (int i = 0; i < 4; ++i)
#pragma unroll
    for (int j = 0; j < 6; ++j) asm volatile("" : "+v"(acc[i][j]));
  asm volatile("" : "+v"(aad[0]), "+v"(aad[1]), "+v"(wad[0]), "+v"(wad[1]));
  if (LAY2) __builtin_amdgcn_s_barrier();
  __builtin_amdgcn_s_barrier();
  PIN();
  if (wid == 0) STAMP(dk, 1);
  s16x8 wq[4][2];
  wq[0][0] = ldw_(0, 0); af[0][0] = lda(aad[0], 0); wq[0][1] = ldw_(1, 0); af[1][0] = lda(aad[1], 0); wq[1][0] = ldw_(0, 1); wq[1][1] = ldw_(1, 1);
  af[0][1] = lda(aad[0], 1); af[1][1] = lda(aad[1], 1); af[0][2] = lda(aad[0], 2); af[1][2] = lda(aad[1], 2);
  PIN();
  s16x4 wlo;
  auto rw = [&](s16x8& d, int h, int j, int hi, int off = 0) __attribute__((always_inline)) {
    const s16x4 v = __builtin_amdgcn_ds_read_tr16_b64_v4i16(LDS_AT(s16x4, wad[j & 1] + h * 12288 + (j >> 1) * 128 + hi * 1536 + off));
    if (hi) d = (s16x8)__builtin_shufflevector(wlo, v, 0, 1, 2, 3, 4, 5, 6, 7);
    else wlo = v;
  };
  auto at = [&](s16x8& x, int r) __attribute__((always_inline)) {
    if (r == 0) asm volatile("" :: "{v[104:107]}"(x));
    if (r == 1) asm volatile("" :: "{v[108:111]}"(x));
    if (r == 2) asm volatile("" :: "{v[112:115]}"(x));
    if (r == 3) asm volatile("" :: "{v[116:119]}"(x));
    if (r == 4) asm volatile("" :: "{v[120:123]}"(x));
    if (r == 5) asm volatile("" :: "{v[124:127]}"(x));
    if (r == 6) asm volatile("" :: "{v[128:131]}"(x));
    if (r == 7) asm volatile("" :: "{v[132:135]}"(x));
    if (r == 8) asm volatile("" :: "{v[136:139]}"(x));
    if (r == 9) asm volatile("" :: "{v[140:143]}"(x));
    if (r == 10) asm volatile("" :: "{v[144:147]}"(x));
    if (r == 11) asm volatile("" :: "{v[148:151]}"(x));
    if (r == 12) asm volatile("" :: "{v[152:155]}"(x));
    if (r == 13) asm volatile("" :: "{v[156:159]}"(x));
    if (r == 14) asm volatile("" :: "{v[160:163]}"(x));
    if (r == 15) asm volatile("" :: "{v[164:167]}"(x));
  };
  auto mm = [&](int i, int j, int h, int sl, int hw_) __attribute__((always_inline)) {
    s16x8 &a = af[h][i], &w = wq[sl][hw_];
    at(a, h * 4 + i); at(w, 8 + sl * 2 + hw_);
    if (j >= 4) asm volatile("v_mfma_f32_16x16x32_bf16 %0, %1, %2, %0" : "+v"(acc[i][j]) : "v"(a), "v"(w));
    else asm volatile("v_mfma_f32_16x16x32_bf16 %0, %1, %2, %0" : "+v"(acc[i][j]) : "v"(w), "v"(a));
    PIN();
  };
  auto next = [&](int g, int J0, int s0, int s1, int off) __attribute__((always_inline)) {
    if (g == 2 || g == 3) rw(wq[s0][0], 0, J0, g & 1, off);
    if (g == 4 || g == 5) af[g & 1][0] = lda(aad[g & 1] + off, 0);
    if (g == 6 || g == 7) rw(wq[s0][1], 1, J0, g & 1, off);
    if (g >= 8 && g < 12) rw(wq[s1][(g - 8) >> 1], (g - 8) >> 1, J0 + 1, g & 1, off);
    if (g >= 12) af[g & 1][(g - 10) >> 1] = lda(aad[g & 1] + off, (g - 10) >> 1);
  };
  auto third = [&](int p, int J0, int delta) __attribute__((always_inline)) {
    auto mv = [&](unsigned& x) __attribute__((always_inline)) { asm volatile("v_add_u32 %0, %1, %0" : "+v"(x) : "s"(delta)); };
#pragma unroll
    for (int g = 0; g < 16; ++g) {
      const int h = g & 1, i = p == 1 ? (g >> 1) & 3 : g >> 2, t = p == 1 ? g >> 3 : (g >> 1) & 1, j = 2 * p + t, sl = p == 1 ? 2 - 2 * t : p + t;
      mm(i, j, h, sl, h);
      if (p == 0) {
        if (g < 2) af[g][3] = lda(aad[g], 3);
        else if (g < 6) rw(wq[2][(g - 2) >> 1], (g - 2) >> 1, 2, g & 1);
        else if (g < 10) rw(wq[3][(g - 6) >> 1], (g - 6) >> 1, 5, g & 1);
        else if (g < 12) mv(aad[g - 10]);
      }
      if (p == 1) {
        if (g < 4) rw(wq[0][g >> 1], g >> 1, 3, g & 1);
        if (g == 4) mv(wad[1]);
        if (g >= 8 && g < 12) rw(wq[2][(g - 8) >> 1], (g - 8) >> 1, 4, g & 1);
        if (g == 12) mv(wad[0]);
      }
      if (p == 2) {
        if (g == BARG) {
          asm volatile("" ::: "memory");
          PIN();
          __builtin_amdgcn_s_barrier();
          asm volatile("" ::: "memory");
        }
        next(g, J0, 0, 1, 0);
      }
      PIN();
      {
        if ((p == 2 && g == 15) || (p == 0 && g == 7)) __builtin_amdgcn_s_waitcnt(0xc67f);
        if (p == 0 && g == 2) __builtin_amdgcn_s_waitcnt(0xc57f);
        if ((p == 0 && g == 14) || (p == 1 && (g & 7) == 7)) __builtin_amdgcn_s_waitcnt(0xc07f);
        PIN();
      }
    }
  };
  third(0, 0, STB); third(1, 0, STB);
  {
    static_assert((NKT - NST - 1) % NST == 0, "");
#pragma unroll 1
    for (int kt = 0; kt < NKT - NST - 1; kt += NST) {
#pragma unroll
      for (int u = 0; u < NST; ++u) {
        const int delta = u == 1 ? -(NST - 1) * STB : STB;
        third(2, 0, 0); third(0, 0, delta); third(1, 0, delta);
      }
    }
  }
  static_assert(NKT % NST == 1, "slots of the last stages");
#define TT(i)
  auto bar = [&]() { PIN(); __builtin_amdgcn_s_barrier(); asm volatile("" ::: "memory"); PIN(); };
  TT(0);
  third(2, 2, 0);
  const __amdgpu_buffer_rsrc_t rsk = __builtin_amdgcn_make_buffer_rsrc(kvb, 0, 0x7fffffff, 0x00020000), rsv = __builtin_amdgcn_make_buffer_rsrc(kvb + VOFF, 0, 0x7fffffff, 0x00020000);
  unsigned pk;
  u32x2 t0, t1, t2, t3;
  auto park = [&](int i) { return *LDS_AT(const volatile u32x2, pk + (i >> 1) * 1024 + (i & 1) * 8); };
  float al[4];
  f32x4 av[4];
  u32x4 kvp[12];
  auto cacc = [&](int p, int x, int t) -> f32x4& { return p < 2 ? acc[x][2 * p + t] : acc[(x >> 1) * 2 + t][4 + (x & 1)]; };
  auto signal = [&](unsigned v) { __hip_atomic_fetch_add(LDS_AT(unsigned, t3[0]), v, __ATOMIC_RELAXED, __HIP_MEMORY_SCOPE_WORKGROUP); PIN(); };
  auto outop = [&](int p, int k) {
    if (k >= 0 && k < 16) {
      const int x = k >> 2, t = (k >> 1) & 1;
      f32x4& c = cacc(p, x, t);
#pragma unroll
      for (int o = 2 * (k & 1); o < 2 * (k & 1) + 2; ++o) {
        float r;
        asm volatile("v_mul_f32 %0, %1, %2" : "=v"(r) : "v"(c[o]), "v"(p < 2 ? al[x] : av[(x >> 1) * 2 + t][o]));
        c[o] = r;
      }
      PIN();
    }
    if (k >= 1 && k <= 16) {
      const int x = (k - 1) >> 2, t = ((k - 1) >> 1) & 1, o = (k - 1) & 1;
      kvp[p * 4 + x][2 * t + o] = pack2(cacc(p, x, t)[2 * o], cacc(p, x, t)[2 * o + 1]);
      PIN();
      if (t && o) {
        if (p) __builtin_amdgcn_raw_buffer_store_b128(kvp[p * 4 + x], p == 1 ? rsk : rsv, (p == 1 ? t1[1] : t2[1]) + (x >> 1) * 2048 + (x & 1) * (p == 1 ? 64 : 128), 0, 16);
        else *LDS_AT(u32x4, t2[1] + (x >> 1) * 2048 + (x & 1) * 128) = kvp[x];
        PIN();
      }
    }
  };
  static_for<0, 168>([&](auto nc) __attribute__((always_inline)) {
    constexpr int n = decltype(nc)::value, sec = n < 80 ? n >> 4 : (n < 104 ? 5 : 6 + ((n - 104) >> 4)), g = n < 104 ? n - 16 * sec : (n - 104) & 15;
    auto taba = [&](int i) { af[0][i] = *LDS_AT(const s16x8, t0[0] + (i >> 1) * 512 + (i & 1) * 128); };
    auto tabw = [&](int j) { wq[(2 + (j >> 1)) & 3][j & 1] = *LDS_AT(const s16x8, t0[1] + ((j >> 1) * 64 + (j & 1) * 8) * 16); };
    auto rwn = [&](int e, int sl, int j, int off) { if (e >= 0 && e < 4) rw(wq[sl][e >> 1], e >> 1, j, e & 1, off); };
    if (g == 0) TT(1 + sec);
    if (sec == 5) {
      const int i = (g / 12) * 2 + (g & 1), j = (g % 12) >> 1;
      mm(i, j, 0, (2 + (j >> 1)) & 3, j & 1);
    } else {
      constexpr int part[10] = {1, 2, 1, 2, 0, 0, 1, 2, 0, 0}, s0[10] = {0, 2, 0, 2, 0, 0, 1, 3, 1, 3};
      const int h = g & 1, i = sec == 7 ? (g >> 3) * 2 + ((g >> 1) & 1) : g >> 2, t = sec == 7 ? (g >> 2) & 1 : (g >> 1) & 1;
      mm(i, 2 * part[sec] + t, h, (s0[sec] + t) & 3, h);
    }
    if (sec == 0 || sec == 2) {
      if (g < 2) af[g][3] = lda(aad[g] + sec / 2 * STB, 3);
      rwn(g - 2, 2, 4, sec / 2 * STB); rwn(g - 6, 3, 5, sec / 2 * STB);
    }
    if (sec == 1) {
      if (g == 1) bar();
      next(g, 2, 0, 1, STB);
    }
    if ((sec == 3 && g == 12) || (sec == 6 && g == 13)) { pk = lto + QOFF + fresh_lane() * 16; asm volatile("" : "+v"(pk)); }
    if (sec == 3) {
      rwn(g, 0, 0, STB); rwn(g - 4, 1, 1, STB);
      if (g >= 8 && g < 12) asm volatile("v_subrev_u32 %0, %1, %0" : "+v"(g < 10 ? aad[g & 1] : wad[g & 1]) : "s"(STB));
      if (g == 13) t1 = park(1);
      if (g == 14) t0 = park(0);
    }
    if (sec == 4) {
      if (g == B15) bar();
      if (g == 4) taba(0);
      if (g == 5 || g == 6) tabw(g - 5);
      if (g == 7) taba(1);
      if (g == 8 || g == 9) tabw(g - 6);
      if (g == 10 || g == 11) af[1][g - 10] = lda(aad[1], g - 10);
      if (g == 12) taba(2);
      if (g == 15) taba(3);
    }
    if (sec == 5) {
      if (g < 2) tabw(4 + g);
      rwn(g - 2, 1, 2, 0);
      if (g == 12 || g == 13) af[0][g - 12] = lda(aad[0], g - 12);
      if (g == 22) { al[0] = *LDS_AT(const float, t1[0]); al[1] = *LDS_AT(const float, t1[0] + 32); }
      rwn(g - 16, 2, 3, 0);
      if (g == 20 || g == 21) af[1][g - 18] = lda(aad[1], g - 18);
    }
    if (sec == 6) {
      if (g < 2) af[0][2 + g] = lda(aad[0], 2 + g);
      rwn(g - 2, 3, 4, 0);
      if (g == 9) { al[2] = *LDS_AT(const float, t1[0] + 128); al[3] = *LDS_AT(const float, t1[0] + 160); }
      if (g == 14) t2 = park(2);
      if ((g & 3) >= 2) rwn((g - 10) / 4 * 2 + (g & 1) - (g < 10) * 8, 0, 5, 0);
    }
    if (sec == 7) {
      if (g == 2 || g == 3 || g == 10 || g == 11) av[(g >> 3) * 2 + (g & 1)] = *LDS_AT(const f32x4, t2[0] + ((g >> 3) * 32 + (g & 1) * 8) * 4);
      rwn(g < 2 ? g : (g < 4 ? -1 : g - 2), 1, 0, 0); rwn(g - 6, 2, 1, 0);
      if (g == 12) t3 = park(3);
      if (g == 13) { const u32x2 a = park(4); aad[0] = a[0]; aad[1] = a[1]; }
      if (g == 15) {
        asm volatile("" ::: "memory");
        __builtin_amdgcn_s_waitcnt(0xc07f);
        bar();
      }
    }
    if (sec == 8) next(g, 0, 3, 0, STB);
    if (sec == 9) {
      if (g < 2) af[g][3] = lda(aad[g] + STB, 3);
      if (g == 2) signal(0x10000u);
    }
    PIN();
    outop(1, n - 104 - KO);
    outop(2, n - 120 - KO);
    {
      constexpr int wg[8] = WGAPS;
#pragma unroll
      for (int e = 0; e < 8; ++e)
        if (n - 136 == wg[e]) {
          *LDS_AT(u32x4, (e < 4 ? t1[1] + (e & 1) * 64 : t2[1] + 2048 + (e & 1) * 128) + ((e >> 1) & 1) * ASB) = kvp[4 + e];
          PIN();
        }
    }
    if (n == 152 + 3 || n == 152 + 4) {
      al[2 * (n & 1)] *= QSCALE; al[2 * (n & 1) + 1] *= QSCALE;
      asm volatile("" : "+v"(al[2 * (n & 1)]), "+v"(al[2 * (n & 1) + 1]));
      if (n & 1) asm volatile("v_add_u32 %0, %1, %0" : "+v"(t2[1]) : "s"(QOFF));
      PIN();
    }
    outop(0, n - 152 - KO);
  });
  TT(13);
  if (wid == 0) STAMP(dk, 18);
#pragma unroll
  for (int k = 16 - KO; k <= 16; ++k) outop(0, k);
  __hip_atomic_fetch_add(LDS_AT(unsigned, t3[1]), 1u, __ATOMIC_RELAXED, __HIP_MEMORY_SCOPE_WORKGROUP);
  TT(14);
  asm volatile("s_waitcnt vmcnt(0)" ::: "memory");
  signal(1u);
  TT(15);
  if (wid == 0) STAMP(dk, 19);
  }
  PIN();
  {
  const int lane = fresh_lane(), tid = wid * 64 + lane;
  LAYER_HERE;
  const int dk = LAY2 ? 6 : 2;
  const int g = wid >> 2, hh = (wid >> 1) & 1, qg = wid & 1;
  const int head = tni * 2 + hh, qt = tmi * 4 + qg * 2;
  unsigned qaa = lbase + QOFF + (hh * 2 + qg) * 4096 + lane * 16 + (g ? 0 : 2048), qab = qaa ^ 2048;
  const unsigned wbase = lbase + (hh * 2 + g) * 4096 + lane * 16;
  auto sptr = [&](int sl) { unsigned a = wbase + sl * ASB; asm volatile("" : "+v"(a)); return LDS_AT(const s16x8, a); };
  auto rd = [&](__attribute__((address_space(3))) const s16x8* sp, int off) { return sp[off >> 4]; };
  auto sl0 = sptr(0);
  const unsigned arr_a = lane == 0 ? lbase + FLG + 16 : lbase + DUM + lane * 4;
  auto arrive = [&]() { __hip_atomic_fetch_add(LDS_AT(unsigned, arr_a), 1u, __ATOMIC_RELAXED, __HIP_MEMORY_SCOPE_WORKGROUP); };
  unsigned arr_w = lane == 0 ? lbase + FLG + 8 + g * 4 : lbase + DUM + lane * 4;
  unsigned arr_i = 1u << (8 * (wid & 3));
  auto arrive_w = [&]() { __hip_atomic_fetch_add(LDS_AT(unsigned, arr_w), arr_i, __ATOMIC_RELAXED, __HIP_MEMORY_SCOPE_WORKGROUP); };
  s16x8 sel;
#pragma unroll
  for (int e = 0; e < 8; ++e) sel[e] = ((lane & 7) == (e & 4)) ? (short)0x3F80 : (short)0;
  int sidx = 0xDDDDDDDD;
  f32x16 oa, ob, nn, sa, sb;
  f32x4 la = {0.f, 0.f, 0.f, 0.f}, lb = {0.f, 0.f, 0.f, 0.f};
#pragma unroll
  for (int e = 0; e < 16; ++e) { oa[e] = 0.f; ob[e] = 0.f; sa[e] = 0.f; sb[e] = 0.f; }
  asm volatile("" : "+v"(sidx), "+v"(sel), "+v"(oa), "+v"(ob), "+v"(la), "+v"(lb), "+v"(qaa), "+v"(qab), "+v"(arr_w), "+v"(arr_i));
  PIN();
  s16x8 qa0, qa1, qb0, qb1, k00, k01, v00, v01, v10, v11, k10, k11;
  u32x4 fw;
  auto first = [&]() {
    fw = lds_rd4(lbase + FLG + 16);
    asm volatile("" ::: "memory");
    k00 = rd(sl0, 0); qa0 = *LDS_AT(const s16x8, qaa); qb0 = *LDS_AT(const s16x8, qab);
    k01 = rd(sl0, 1024); qa1 = *LDS_AT(const s16x8, qaa + 1024); qb1 = *LDS_AT(const s16x8, qab + 1024);
    v00 = rd(sl0, 2048); v01 = rd(sl0, 3072);
    PIN();
  };
  first();
  if (__builtin_amdgcn_readfirstlane(fw[0]) < 4u) {
#pragma unroll 1
    for (int i = 0; i < SPIN_MAX; ++i) {
      if (__builtin_amdgcn_readfirstlane(*LDS_AT(const volatile unsigned, lbase + FLG + 16)) >= 4u) break;
      __builtin_amdgcn_s_sleep(ASLP);
    }
    asm volatile("" ::: "memory");
    first();
  }
  const unsigned tag = __builtin_amdgcn_readfirstlane(fw[1]);
  const int idx = LAY2 ? (2047 - (int)(__builtin_amdgcn_readfirstlane(fw[3]) & 0xffffu)) & 2047 : -64;
  float ma;
  const float zoff = __uint_as_float(__builtin_amdgcn_readfirstlane(LAY2 && g == ((idx >> 6) & 1) ? 0u : 0xff800000u));
  {
    sa = MFMA32(k00, qa0, sa); sb = MFMA32(k00, qb0, sb);
    sa = MFMA32(k01, qa1, sa); sb = MFMA32(k01, qb1, sb);
    float mb = sb[0];
    ma = sa[0];
#pragma unroll
    for (int e = 1; e < 16; ++e) { ma = fmaxf(ma, sa[e]); mb = fmaxf(mb, sb[e]); }
    ma = fmaxf(ma, mb);
    const auto sw = __builtin_amdgcn_permlane32_swap(__float_as_uint(ma), __float_as_uint(ma), false, false);
    ma = fmaxf(__uint_as_float(sw[0]), __uint_as_float(sw[1]));
    f32x2 n2 = {-ma, -ma};
    asm volatile("" : "+v"(n2));
#pragma unroll
    for (int e = 0; e < 16; ++e) nn[e] = n2[e & 1];
    sa = MFMA32(k00, qa0, nn); sa = MFMA32(k01, qa1, sa);
    la[0] = lb[0] = -__uint_as_float(__float_as_uint(__builtin_amdgcn_exp2f(zoff - ma)) & 0xffff0000u);
  }
#define SB() __builtin_amdgcn_sched_barrier(0)
#define EX(i) sc[i] = __builtin_amdgcn_exp2f(sc[i])
  auto unit = [&](f32x16& sc, f32x16& sn, const s16x8& kn0, const s16x8& kn1, const s16x8& qn0, const s16x8& qn1,
                  f32x16& op, f32x4& lp, const s16x8& vp,
                  f32x16& oc, f32x4& lc, const s16x8& vc, auto&& mid) {
    lp = SMF16(sel, PHI(sn), lp); SB();
    op = SMF32(vp, PHI(sn), op); SB();
    EX(0); EX(1); EX(2); EX(3); SB();
    sn = MFMA32(kn0, qn0, nn); SB();
    EX(4); EX(5); EX(6); EX(7); SB();
    sn = MFMA32(kn1, qn1, sn); SB();
    mid();
    EX(8); EX(9); EX(10); EX(11); SB();
    lc = SMF16(sel, PLO(sc), lc); SB();
    oc = SMF32(vc, PLO(sc), oc); SB();
    EX(12); EX(13); EX(14); EX(15); SB();
    asm volatile("" : "+v"(sc));
  };
  sl0 = sptr(1);
  unsigned f0 = 0u;
  auto check = [&](unsigned need) {
    if (__builtin_expect(__builtin_amdgcn_readfirstlane(f0) < need, 0)) {
#pragma unroll 1
      for (int i = 0; i < SPIN_MAX; ++i) {
        if (__builtin_amdgcn_readfirstlane(*LDS_AT(const volatile unsigned, lbase + FLG)) >= need) break;
        __builtin_amdgcn_s_sleep(1);
      }
    }
    asm volatile("" ::: "memory");
  };
#define PAT 0x22221110
#define SAW8(i) do { if (((PAT >> (28 - 4 * (i))) & 3) != ((PAT >> ((32 - 4 * (i)) & 31)) & 3)) { __builtin_amdgcn_s_setprio((PAT >> (28 - 4 * (i))) & 3); SB(); } } while (0)
#define SAW(i) do { if (odd) SAW8(4 + (i)); else SAW8(i); } while (0)
  auto three = [&](int st, bool odd) {
    SAW(0);
    unit(sa, sb, k00, k01, qb0, qb1, ob, lb, v11, oa, la, v00, [&]() { k10 = rd(sl0, 0); k11 = rd(sl0, 1024); SB(); });
    SAW(1);
    unit(sb, sa, k10, k11, qa0, qa1, oa, la, v01, ob, lb, v00, [&]() { v10 = rd(sl0, 2048); v11 = rd(sl0, 3072); SB(); });
    sl0 = sptr((2 * st + 2) & 7);
    SB();
    SAW(2);
    unit(sa, sb, k10, k11, qb0, qb1, ob, lb, v01, oa, la, v10, [&]() { k00 = rd(sl0, 0); k01 = rd(sl0, 1024); SB(); });
  };
  auto fourth = [&](int st, bool odd) {
    SAW(3);
    unit(sb, sa, k00, k01, qa0, qa1, oa, la, v11, ob, lb, v10, [&]() {
      v00 = rd(sl0, 2048); v01 = rd(sl0, 3072);
      f0 = *LDS_AT(const volatile unsigned, lbase + FLG);
      SB();
      arrive_w();
      SB(); });
    check(st >= 14 ? 32u : 2u * st + 5u);
    sl0 = sptr((2 * st + 3) & 7);
    SB();
  };
  SB();
  SAW8(0);
  {
    f32x16& sc = sa;
    sb = MFMA32(k00, qb0, nn); SB();
    k10 = rd(sl0, 0); k11 = rd(sl0, 1024); SB();
    sb = MFMA32(k01, qb1, sb); SB();
    EX(0); EX(1); EX(2); EX(3); EX(4); EX(5); EX(6); EX(7); EX(8); SB();
    oa = SMF32(v00, PLO(sa), oa); SB();
    EX(9); EX(10); EX(11); SB();
    la = SMF16(sel, PLO(sa), la); SB();
    EX(12); EX(13); EX(14); EX(15); SB();
    asm volatile("" : "+v"(sa));
  }
  SAW8(1);
  unit(sb, sa, k10, k11, qa0, qa1, oa, la, v01, ob, lb, v00, [&]() { v10 = rd(sl0, 2048); v11 = rd(sl0, 3072); SB(); });
  sl0 = sptr(2);
  SB();
  SAW8(2);
  unit(sa, sb, k10, k11, qb0, qb1, ob, lb, v01, oa, la, v10, [&]() {
    f0 = *LDS_AT(const volatile unsigned, lbase + FLG);
    asm volatile("" ::: "memory");
    k00 = rd(sl0, 0); k01 = rd(sl0, 1024); SB(); });
  if (__builtin_amdgcn_readfirstlane(f0) < 3u) { check(3u); k00 = rd(sl0, 0); k01 = rd(sl0, 1024); }
  SB();
  SAW8(3);
  unit(sb, sa, k00, k01, qa0, qa1, oa, la, v11, ob, lb, v10, [&]() {
    v00 = rd(sl0, 2048); v01 = rd(sl0, 3072); f0 = *LDS_AT(const volatile unsigned, lbase + FLG); SB(); arrive_w(); SB(); });
  check(5u);
  sl0 = sptr(3);
  SB();
  three(1, true);
#pragma unroll 1
  for (int st = 2; st < 16; st += 2) { fourth(st - 1, true); three(st, false); fourth(st, false); three(st + 1, true); }
  SAW8(7);
  unsigned vr, tk, tv;
  u32x4 f4;
  auto table = [&]() {
    f4 = lds_rd4(lbase + FLG);
    k00 = *LDS_AT(const volatile s16x8, tk); k01 = *LDS_AT(const volatile s16x8, tk + 32);
    vr = *LDS_AT(const volatile u16, tv);
  };
  unsigned xw, xm;
  u32x2 ml;
  u32x4 o2[4];
  auto take = [&]() {
    ml = *LDS_AT(const volatile u32x2, xm);
#pragma unroll
    for (int e = 0; e < 4; ++e) o2[e] = lds_rd4(xw + e * 1024);
  };
  float pzt;
  int dso;
  f32x16& sc = sb;
#define EXP() do { asm volatile("" : "+v"(sc)); SB(); } while (0)
  oa = SMF32(v11, PHI(sa), oa); SB();
  tk = lbase + TAB + 128 + hh * 64 + (lane >> 5) * 16; tv = lbase + TAB + 256 + hh * 64 + (lane & 31) * 2;
  table();
  SB();
  EX(0); EX(1); EXP();
  la = SMF16(sel, PHI(sa), la); SB();
  EX(2); EX(3); EX(4); EXP();
  xw = wbase + (qg ? QOFF : 6 * ASB); xm = lbase + XML + wid * 512 + lane * 8;
  pzt = __builtin_amdgcn_exp2f(nn[0] + (zoff - 8.f));
  asm volatile("" : "+v"(xw), "+v"(xm), "+v"(pzt), "+v"(f4));
  SB();
  {
    const unsigned ph = LAY2 ? 0x79797979u : 0x80808080u, pg = qg ? 0u : 0x80u << (16 * hh + 8);
    auto ready = [&]() {
      return (((__builtin_amdgcn_readfirstlane(f4[1]) + ph) & 0x80808080u) == 0x80808080u) & (((__builtin_amdgcn_readfirstlane(g ? f4[3] : f4[2]) + 0x71717171u) & pg) == pg);
    };
    if (__builtin_expect(!ready(), 0)) {
#pragma unroll 1
      for (int i = 0; i < SPIN_MAX; ++i) {
        __builtin_amdgcn_s_sleep(1);
        table();
        if (ready()) break;
      }
    }
    asm volatile("" ::: "memory");
  }
  SB();
  sa = MFMA32(k00, qb0, nn); asm volatile("" : "+v"(sa)); SB();
#pragma unroll
  for (int e = 0; e < 4; ++e) *LDS_AT(volatile f32x4, xw + e * 1024) = f32x4{oa[4 * e], oa[4 * e + 1], oa[4 * e + 2], oa[4 * e + 3]};
  *LDS_AT(volatile u32x2, xm) = u32x2{__float_as_uint(ma), f2word(pzt <= la[0] ? la[0] : 0.f)};
  SB();
  EX(5); EX(6); EX(7); EXP();
  sa = MFMA32(k01, qb1, sa); asm volatile("" : "+v"(sa), "+v"(vr)); SB();
  ARGS_HERE;
  EX(8); EX(9); EX(10); EXP();
  ob = SMF32(v10, PLO(sb), ob); SB();
  EX(11); EX(12); EX(13); EXP();
  lb = SMF16(sel, PLO(sb), lb); SB();
  const bool used = !LAY2 || (qt + g) * 32 + (lane & 31) != idx;
  dso = used ? (((qt + g) * 32 + head) * 1024 + lane * 8) * 2 : S_ * D_ * 2;
  xw += g ? -4096 : 4096; xm += g ? -2048 : 2048;
  asm volatile("" : "+v"(dso), "+v"(xw), "+v"(xm));
  SB();
  EX(14); EX(15); EXP();
  take();
  SB();
  lb = SMF16(sel, PHI(sb), lb); SB();
  ob = SMF32(v11, PHI(sb), ob); SB();
  __builtin_amdgcn_s_setprio(EPRIO);
  TE(0, 0); TE(1, 0); TE(2, 0); TE(3, 0); TE(7, 0);
  float lsb = lb[0];
  if (LAY2) {
    sa[0] = __builtin_amdgcn_exp2f(sa[0]);
    ob = SMF32((u32x4{lane < 32 ? vr : 0u, 0u, 0u, 0u}), PLO(sa), ob);
    lsb += __uint_as_float(__float_as_uint(sa[0]) & 0xffff0000u);
    lsb = pzt <= lsb ? lsb : 0.f;
  }
  const __amdgpu_buffer_rsrc_t rso = __builtin_amdgcn_make_buffer_rsrc(Of, 0, S_ * D_ * 2, 0x00020000);
  TE(0, 1);
  if (__builtin_amdgcn_ballot_w64(ml[1] == SENT) != 0ull) {
#pragma unroll 1
    for (int i = 0; i < SPIN_MAX; ++i) {
      __builtin_amdgcn_s_sleep(XSLP);
      if (!__builtin_amdgcn_ballot_w64(*LDS_AT(const volatile unsigned, xm + 4) == SENT)) break;
    }
    asm volatile("s_waitcnt lgkmcnt(0)" ::: "memory");
    take();
  }
  arrive();
  TE(0, 2);
  TE(0, 3); TE(1, 1);
  {
    const float m2 = __uint_as_float(ml[0]), l2 = __uint_as_float(ml[1]);
    const float mx = fmaxf(ma, m2);
    const float f1 = __builtin_amdgcn_exp2f(ma - mx), f2 = __builtin_amdgcn_exp2f(m2 - mx);
    const float inv = frcp(lsb * f1 + l2 * f2);
    const float c1 = f1 * inv, c2 = f2 * inv;
    f32x2 r[8];
#pragma unroll
    for (int e = 0; e < 8; ++e) {
      const unsigned x0 = o2[e >> 1][(e & 1) * 2], x1 = o2[e >> 1][(e & 1) * 2 + 1];
      r[e] = f32x2{__uint_as_float(x0) * c2, __uint_as_float(x1) * c2};
    }
#pragma unroll
    for (int e = 0; e < 8; ++e) r[e] = f32x2{ob[2 * e], ob[2 * e + 1]} * f32x2{c1, c1} + r[e];
    auto put = [&]() {
#pragma unroll
      for (int sx = 0; sx < 2; ++sx)
        __builtin_amdgcn_raw_buffer_store_b128(u32x4{pack2(r[4 * sx][0], r[4 * sx][1]), pack2(r[4 * sx + 1][0], r[4 * sx + 1][1]),
            pack2(r[4 * sx + 2][0], r[4 * sx + 2][1]), pack2(r[4 * sx + 3][0], r[4 * sx + 3][1])}, rso, dso + sx * 1024, 0, 16);
    };
    put();
    PIN();
    __builtin_amdgcn_s_setprio(0);
    PIN();
    f32x2 chk = {lsb - __builtin_amdgcn_fmed3f(lsb, 8.6736174e-19f, 1.2676506e30f), l2 - __builtin_amdgcn_fmed3f(l2, 8.6736174e-19f, 1.2676506e30f)};
#pragma unroll
    for (int e = 0; e < 8; ++e) chk = r[e] * f32x2{0.f, 0.f} + chk;
    if (__builtin_expect(__builtin_amdgcn_ballot_w64(used && !(chk[0] + chk[1] == 0.f)) != 0ull, 0)) {
      float l;
      attn_safe(KV, head * 64, qb0, qb1, ob, l, LAY2 ? head * 64 + (idx >> 5) : -1, idx & 31, lbase + TAB + 128 + hh * 64);
      const float inv2 = 1.0f / l;
#pragma unroll
      for (int e = 0; e < 8; ++e) r[e] = f32x2{ob[2 * e] * inv2, ob[2 * e + 1] * inv2};
      asm volatile("s_waitcnt vmcnt(0)" ::: "memory");
      put();
    }
    unsigned pp = 0u;
    if (LAY2) {
      const unsigned ua = lbase + UGL + hh * 128 + (lane >> 5) * 16;
      f32x2 du = {0.f, 0.f}, dn = {0.f, 0.f};
#pragma unroll
      for (int q = 0; q < 4; ++q) {
        const f32x4 uu = *LDS_AT(const f32x4, ua + q * 32), gg = *LDS_AT(const f32x4, ua + 256 + q * 32);
        du = r[2 * q] * f32x2{uu[0], uu[1]} + du; du = r[2 * q + 1] * f32x2{uu[2], uu[3]} + du;
        const f32x2 t0 = r[2 * q] * f32x2{gg[0], gg[1]}, t1 = r[2 * q + 1] * f32x2{gg[2], gg[3]};
        dn = t0 * t0 + dn; dn = t1 * t1 + dn;
      }
      const auto a = __builtin_amdgcn_permlane32_swap(__float_as_uint(du[0] + du[1]), __float_as_uint(du[0] + du[1]), false, false);
      const auto c = __builtin_amdgcn_permlane32_swap(__float_as_uint(dn[0] + dn[1]), __float_as_uint(dn[0] + dn[1]), false, false);
      pp = f2word(__uint_as_float(pack2(__uint_as_float(a[0]) + __uint_as_float(a[1]), __uint_as_float(c[0]) + __uint_as_float(c[1]))));
    }
    __builtin_amdgcn_raw_buffer_store_b32(pp, __builtin_amdgcn_make_buffer_rsrc(hand + 2 * HAND_N + PP_O, 0, LAY2 ? H_ * S_ * 4 : 0, 0x00020000),
                                          lane < 32 ? (head * S_ + (qt + g) * 32 + lane) * 4 : H_ * S_ * 4, 0, 16);
    PIN();
  }
  TE(1, 2);
  u32x4 xres[2];
  oproj_tail(LAY2, tid, lbase, lbase + FLG, omi, oni, idx, gran + GR_O + ((LAY2 ? 16 : 0) + tmi) * (64 * RO) + tni,
             gran + GR_X + omi * (128 * RX) + oni * 4, tag, Of, xout, A, w_r, stats_out, stats, hand, xres TEPASS);
  if (LAY2) {
    const int row = omi * 128 + (wid & 3) * 32 + (wid >> 2) * 16 + (lane >> 3), col = oni * 64 + (lane & 7) * 8;
    unsigned md[2], best = 0u;
    const __amdgpu_buffer_rsrc_t rsk = __builtin_amdgcn_make_buffer_rsrc(hand + HAND_N, 0, (HAND_N + PS_N) * 4, 0x00020000);
    f32x2 xf[2][4];
#pragma unroll
    for (int p = 0; p < 2; ++p)
#pragma unroll
      for (int e = 0; e < 4; ++e) {
        xf[p][e] = f32x2{__uint_as_float(xres[p][e] << 16), __uint_as_float(xres[p][e] & 0xffff0000u)};
        asm volatile("" : "+v"(xf[p][e]));
      }
    LOGDEF;
    LOG();
    const bool rtr = (oni & 7) == 7;
    {
      const int rl = (wid & 3) * 32 + (wid >> 2) * 16 + (lane & 15), rr = omi * 128 + rl;
      int src = (HAND_N + ((omi * 16 + (lane >> 4) * 4) * 128 + rl) * 4) * 4;
      int ko = rtr && (wid >> 2) == (oni >> 3) && lane < NREP ? lane * 1024 + (rr >> 4) * 8 : (HAND_N + PS_N) * 4;
      int bp = (lane >> 3) * 4;
      const float wsum = *LDS_AT(const volatile float, lbase + SPM + 132);
      asm volatile("" : "+v"(src), "+v"(ko), "+v"(bp));
      u32x4 v[4];
      __builtin_amdgcn_s_sleep(MPRE);
#pragma unroll 1
      for (int spin = 0; spin < SPIN_MAX; ++spin) {
        LOG();
#pragma unroll
        for (int i = 0; i < 4; ++i) v[i] = __builtin_amdgcn_raw_buffer_load_b128(rsk, src + (i & 1) * 2048, (i >> 1) * 4096, 16);
        unsigned mx = 0u;
#pragma unroll
        for (int i = 0; i < 4; ++i) mx = umax(umax(umax(mx, v[i][0]), v[i][1]), v[i][2]);
        if (none(mx == SENT)) break;
        __builtin_amdgcn_s_sleep(MSLP);
        asm volatile("" ::: "memory");
      }
      LOGAT(12);
      if (rtr) __builtin_amdgcn_s_setprio(3); else __builtin_amdgcn_s_setprio(LNPRIO);
      double sm[3];
#pragma unroll
      for (int c = 0; c < 3; ++c) {
        const double a = ((double)__uint_as_float(v[0][c]) + (double)__uint_as_float(v[1][c])) + ((double)__uint_as_float(v[2][c]) + (double)__uint_as_float(v[3][c]));
        u32x2 q = __builtin_bit_cast(u32x2, a);
        const auto lo = __builtin_amdgcn_permlane32_swap(q[0], q[0], false, false), hi = __builtin_amdgcn_permlane32_swap(q[1], q[1], false, false);
        q = __builtin_bit_cast(u32x2, __builtin_bit_cast(double, u32x2{lo[0], hi[0]}) + __builtin_bit_cast(double, u32x2{lo[1], hi[1]}));
        const auto l2 = __builtin_amdgcn_permlane16_swap(q[0], q[0], false, false), h2 = __builtin_amdgcn_permlane16_swap(q[1], q[1], false, false);
        sm[c] = __builtin_bit_cast(double, u32x2{l2[0], h2[0]}) + __builtin_bit_cast(double, u32x2{l2[1], h2[1]});
      }
      float mean, var, mb, den;
      ln_from_sums(sm[0], sm[1], mean, var, mb, den);
      const unsigned mdr = f2word(__uint_as_float((__float_as_uint(mb) >> 16) | (__float_as_uint(den) & 0xffff0000u)));
      md[0] = __builtin_amdgcn_ds_bpermute(bp, mdr); md[1] = __builtin_amdgcn_ds_bpermute(bp + 32, mdr);
      if (rtr) {
        const float r = (float)fma(-(double)mb, (double)wsum, sm[2]) * frcp(den);
        const unsigned rb = pack2(r, r);
        unsigned rkey = (rb ^ ((unsigned)((int)rb >> 31) | 0x80000000u)) & 0xffff0000u;
        if (r != r) rkey = 0u;
        const unsigned top = wave_umax(rkey | (unsigned)(2047 - rr));
        const unsigned mdt = __builtin_amdgcn_readlane(mdr, (2047 - (int)(top & 0xffffu)) & 15);
        __builtin_amdgcn_raw_buffer_store_b64(u32x2{top, mdt}, rsk, ko, 0, 16);
        PIN();
        __builtin_amdgcn_s_setprio(LNPRIO);
        __hip_atomic_fetch_add(LDS_AT(unsigned, lane == 0 ? lbase + SPM + 128 : lbase + DUM + lane * 4), 1u, __ATOMIC_RELAXED, __HIP_MEMORY_SCOPE_WORKGROUP);
        if (wid == 2 || wid == 5) STAMP(8, 6);
      }
      LOGAT(13);
    }
    auto keys = [&](const u32x4& v) __attribute__((always_inline)) {
      if (none(umax(umax(v[0], v[1]), umax(v[2], v[3])) == SENT)) {
        best = wave_umax(umax(v[0], v[2])) | 0x8000u;
        *LDS_AT(volatile u32x2, lbase + FLG + 40) = u32x2{best, (unsigned)__builtin_amdgcn_readlane(v[0] > v[2] ? v[1] : v[3], ((2047 - (int)(best & 0x7ffu)) >> 5) & 63)};
        STAMP(8, 11);
      }
    };
    auto ln_rows = [&]() __attribute__((always_inline)) {
#pragma unroll
      for (int p = 0; p < 2; ++p) {
        const float mb_ = __uint_as_float(md[p] << 16), rin = frcp(__uint_as_float(md[p] & 0xffff0000u));
#pragma unroll
        for (int e = 0; e < 4; ++e) {
          const f32x2 h = (xf[p][e] - f32x2{mb_, mb_}) * f32x2{rin, rin} + xf[p][e];
          xres[p][e] = pack2(h[0], h[1]);
        }
      }
      asm volatile("" : "+v"(xres[0]), "+v"(xres[1]));
    };
    if (wid == 0) STAMP(8, 12);
    ln_rows();
    LOGAT(14);
    const __amdgpu_buffer_rsrc_t rsu = __builtin_amdgcn_make_buffer_rsrc(out, 0, S_ * D_ * 2, 0x00020000);
    unsigned fr1 = SENT;
#pragma unroll 1
    for (int i = 0; i < 4 * SPIN_MAX && fr1 == SENT; ++i) fr1 = __builtin_amdgcn_readfirstlane(*LDS_AT(const volatile unsigned, lbase + SPM + 144));
    fr1 &= 0x7ffu;
    unsigned tmx = __builtin_amdgcn_readfirstlane(*LDS_AT(const volatile unsigned, lbase + SPM + 136)) + KDT;
    if (tmx - (unsigned)__builtin_amdgcn_s_memrealtime() > 1024u) tmx = 0u;
    auto due = [&]() { return tmx == 0u || (int)((unsigned)__builtin_amdgcn_s_memrealtime() - tmx) >= 0; };
    lk_set(15);
    if (wid == 6) {
      if (rtr) {
#pragma unroll 1
        for (int i = 0; i < SPIN_MAX; ++i) {
          if (__builtin_amdgcn_readfirstlane(*LDS_AT(const volatile unsigned, lbase + SPM + 128)) == 7u) break;
          __builtin_amdgcn_s_sleep(1);
        }
        asm volatile("" ::: "memory");
      }
    }
#pragma unroll 1
    for (int i = 0; i < OSLP && !best; ++i) {
      if (wid == 6 && due()) {
        LOG();
        keys(__builtin_amdgcn_raw_buffer_load_b128(rsk, (bid % NREP) * 1024 + lane * 16, 0, 16));
        i += 2;
      } else {
        if (__builtin_amdgcn_readfirstlane(*LDS_AT(const volatile unsigned, lbase + FLG + 40))) break;
        __builtin_amdgcn_s_sleep(2);
      }
      asm volatile("" ::: "memory");
    }
    u32x4 kv = {SENT, SENT, SENT, SENT};
    if (wid == 6 && !best && due()) kv = __builtin_amdgcn_raw_buffer_load_b128(rsk, (bid % NREP) * 1024 + lane * 16, 0, 16);
    PIN();
#pragma unroll
    for (int p = 0; p < 2; ++p) __builtin_amdgcn_raw_buffer_store_b128(xres[p], rsu, row + 8 * p != (int)fr1 ? ((row + 8 * p) * D_ + col) * 2 : S_ * D_ * 2, 0, OAUX);
    PIN();
    if (wid == 6 && !best) { keys(kv); while (!due()) __builtin_amdgcn_s_sleep(1); }
#pragma unroll 1
    for (int i = 0; i < 4 * SPIN_MAX && !best; ++i) {
      if (wid == 6) {
        LOG();
        keys(__builtin_amdgcn_raw_buffer_load_b128(rsk, (bid % NREP) * 1024 + lane * 16, 0, 16));
        if (!best) __builtin_amdgcn_s_sleep(KSLP);
      } else {
        best = __builtin_amdgcn_readfirstlane(*LDS_AT(const volatile unsigned, lbase + FLG + 40));
        if (!best) __builtin_amdgcn_s_sleep(1);
      }
      asm volatile("" ::: "memory");
    }
    const int idx2 = 2047 - (int)(best & 0x7ffu);
    if (__builtin_expect(idx2 != (int)fr1, 0)) {
      const __amdgpu_buffer_rsrc_t rsf = __builtin_amdgcn_make_buffer_rsrc(gran + GR_F, 0, 48 * 128, 0x00020000);
      if (!none((row | 8) == (idx2 | 8))) {
#pragma unroll
        for (int p = 0; p < 2; ++p)
          __builtin_amdgcn_raw_buffer_store_b128(u32x4{pack2(xf[p][0][0], xf[p][0][1]), pack2(xf[p][1][0], xf[p][1][1]), pack2(xf[p][2][0], xf[p][2][1]), pack2(xf[p][3][0], xf[p][3][1])},
              __builtin_amdgcn_make_buffer_rsrc(xout, 0, 3 * D_ * 2, 0x00020000), row + 8 * p == idx2 ? (2 * D_ + col) * 2 : S_ * D_ * 2, 0, 16);
        asm volatile("s_waitcnt vmcnt(0)" ::: "memory");
        __builtin_amdgcn_raw_buffer_store_b32(tag, rsf, lane == 0 ? oni * 128 : 48 * 128, 0, 16);
      }
      if (!none((row | 8) == ((int)fr1 | 8))) {
#pragma unroll 1
        for (int spin = 0; spin < SPIN_MAX; ++spin) {
          if (none(__builtin_amdgcn_raw_buffer_load_b32(rsf, (16 + oni * 2 + (lane & 1)) * 128, 0, 16) != tag)) break;
          asm volatile("" ::: "memory");
        }
#pragma unroll
        for (int p = 0; p < 2; ++p) __builtin_amdgcn_raw_buffer_store_b128(xres[p], rsu, row + 8 * p == (int)fr1 ? ((row + 8 * p) * D_ + col) * 2 : S_ * D_ * 2, 0, OAUX);
      }
    }
  }
  }
}
struct Args2 { Args l[2]; };
extern "C" __global__ __launch_bounds__(768, 3) void layers_kernel(const Args2 args) {
  __shared__ __attribute__((aligned(128))) char smem[163840];
  const unsigned long long t_entry = 0;
  const int tid = threadIdx.x, bid = blockIdx.x;
  int wid = __builtin_amdgcn_readfirstlane(tid >> 6);
  asm volatile("" : "+s"(wid));
  const ArgP ap = (ArgP)__builtin_amdgcn_kernarg_segment_ptr();
  layer_body(wid, bid, (unsigned)(uintptr_t)smem, ap, t_entry);
  LAYER_HERE;
  if (!LAY2) {
    asm volatile(AGAIN :: "{s[0:1]}"(ap + 1), "{s2}"(bid), "{v0}"(wid * 64) : "s90", "s91", "scc", "memory");
    __builtin_unreachable();
  }
}

extern "C" __global__ void end_marker_kernel() {}

extern "C" void kernel_launch(void* const* d_in, const int* in_sizes, int n_in, void* d_out, int out_size,
                              void* d_ws, size_t ws_size, hipStream_t stream) {
  const u16* x0 = (const u16*)d_in[0];
  const u16* wqkv = (const u16*)d_in[1];
  const u16* wo = (const u16*)d_in[2];
  const u16* w_r = (const u16*)d_in[4];
  const u16* wg = (const u16*)d_in[5];
  const u16* wu = (const u16*)d_in[6];
  const u16* wd = (const u16*)d_in[7];
  u16* out = (u16*)d_out;

  char* ws = (char*)d_ws;
  const size_t MB4 = (size_t)S_ * D_ * 2;
#define ALIAS 3
  u16* kv1 = (u16*)(ws);
  u16* o1 = (u16*)(ws + 2 * MB4);
  u16* x1 = (u16*)(ws + 3 * MB4);
  unsigned* gran = (unsigned*)(ws + 4 * MB4);
  float* csf = (float*)(gran + GR_F + 48 * 32);
  double* stats = (double*)(csf + 8192);
  unsigned* hand = (unsigned*)(stats + 2 * STAT_N);
  static_assert((GR_F + 48 * 32) % 32 == 0 && (size_t)(GR_F + 48 * 32 + 8192 + 4 * STAT_N + 2 * HAND_N + PS_N) * 4 <= 6u << 20, "");
  u16* x3 = (u16*)(hand + HAND_N + GRAN_N + Y_N);
  u16* wc = (u16*)((((uintptr_t)ws + 4 * MB4 + (6u << 20) + 2047) & ~(uintptr_t)2047) + (((uintptr_t)wqkv + 128) & 2047));
  u16* kv2 = (ALIAS & 1) ? kv1 : (u16*)(ws + 8 * MB4);
  u16* o2 = (ALIAS & 2) ? o1 : (u16*)(ws + 10 * MB4);
  Args2 a;
  for (int l = 0; l < 2; ++l) {
    Args& r = a.l[l];
    r.lay = l; r.a = l ? x1 : x0; r.kv = l ? kv2 : kv1; r.o = l ? o2 : o1; r.xo = l ? x3 : x1; r.so = l ? stats + STAT_N : stats;
    r.W = wqkv; r.wc = wc; r.wg = wg; r.wu = wu; r.wd = wd; r.wo = wo; r.w_r = w_r; r.gran = gran; r.csf = csf; r.stats = stats; r.hand = hand; r.out = out;
  }
  layers_kernel<<<256, 768, 0, stream>>>(a);
}
```

```cpp
#include <hip/hip_runtime.h>
#include <hip/hip_bf16.h>
#include <stdint.h>
#include <type_traits>
#include <utility>

typedef unsigned short u16;
typedef short s16x4 __attribute__((ext_vector_type(4)));
typedef short s16x8 __attribute__((ext_vector_type(8)));
typedef float f32x16 __attribute__((ext_vector_type(16)));
typedef float f32x4 __attribute__((ext_vector_type(4)));
typedef unsigned int u32x4 __attribute__((ext_vector_type(4)));
typedef unsigned int u32x2 __attribute__((ext_vector_type(2)));
typedef __bf16 bf16x8 __attribute__((ext_vector_type(8)));
typedef __bf16 bf16x16 __attribute__((ext_vector_type(16)));

__device__ __forceinline__ int fresh_lane() {
  int l;
  asm volatile("v_mbcnt_lo_u32_b32 %0, -1, 0\n\tv_mbcnt_hi_u32_b32 %0, -1, %0" : "=v"(l));
  return l;
}
#define STAMP(k, i)
#define STAMPV(k, i, v)
#define LOGDEF
#define LOG()
#define LOGAT(k)
#define lk_set(k)
#define LOGOUT(slot)
#define TE(grp, k)
#define TEARG
#define TEPASS
constexpr int S_ = 2048, D_ = 1024, H_ = 32, F_ = 3584;

__device__ __forceinline__ int kpos(int r) { return (r & ~12) | ((r & 4) << 1) | ((r & 8) >> 1); }
__device__ __forceinline__ float bf2f(u16 v) { return __uint_as_float(((unsigned)v) << 16); }
__device__ __forceinline__ u16 f2bf(float f) {
  __hip_bfloat16 b = __float2bfloat16(f);
  return *reinterpret_cast<u16*>(&b);
}
typedef float f32x2 __attribute__((ext_vector_type(2)));
typedef __bf16 bf16x2 __attribute__((ext_vector_type(2)));
__device__ __forceinline__ unsigned pack2(float lo, float hi) {
  f32x2 v = {lo, hi};
  return __builtin_bit_cast(unsigned, __builtin_convertvector(v, bf16x2));
}
__device__ __forceinline__ float rbf(float f) { return __uint_as_float(pack2(f, f) << 16); }
__device__ __forceinline__ void rbf2(float& a, float& b) {
  const unsigned p = pack2(a, b);
  a = __uint_as_float(p << 16); b = __uint_as_float(p & 0xffff0000u);
}

__device__ __forceinline__ float fadd(float a, float b) { float r = a + b; asm("" : "+v"(r)); return r; }
template <int CTRL, int RMASK>
__device__ __forceinline__ float dpp_add(float v) {
  const int t = __builtin_amdgcn_update_dpp(0, __float_as_int(v), CTRL, RMASK, 0xf, false);
  return v + __int_as_float(t);
}
__device__ __forceinline__ float wave_sum(float v) {
  v = dpp_add<0x111, 0xf>(v);
  v = dpp_add<0x112, 0xf>(v);
  v = dpp_add<0x114, 0xf>(v);
  v = dpp_add<0x118, 0xf>(v);
  v = dpp_add<0x142, 0xa>(v);
  v = dpp_add<0x143, 0xc>(v);
  return __int_as_float(__builtin_amdgcn_readlane(__float_as_int(v), 63));
}

template <int CTRL, int RMASK>
__device__ __forceinline__ unsigned dpp_umax(unsigned v) {
  const unsigned t = (unsigned)__builtin_amdgcn_update_dpp(0, (int)v, CTRL, RMASK, 0xf, false);
  return v > t ? v : t;
}
__device__ __forceinline__ unsigned wave_umax(unsigned v) {
  v = dpp_umax<0x111, 0xf>(v); v = dpp_umax<0x112, 0xf>(v); v = dpp_umax<0x114, 0xf>(v); v = dpp_umax<0x118, 0xf>(v);
  v = dpp_umax<0x142, 0xa>(v); v = dpp_umax<0x143, 0xc>(v);
  return (unsigned)__builtin_amdgcn_readlane((int)v, 63);
}

#define NREP 32
constexpr unsigned SENT = 0xFFFFFFFFu;
constexpr int GRAN_N = 256 * NREP;
constexpr int Y_N = F_ / 16 * D_;
constexpr int X2_N = D_ / 2;
constexpr int XS_N = 128;
constexpr int P4_N = 3 * D_ * 16;
constexpr int PA_N = H_ * 8 * 64;
constexpr int HAND_N = GRAN_N + Y_N + X2_N + XS_N + P4_N + PA_N;
constexpr int MD_O = 16 * 16 * 128 * 4;
constexpr int PK_O = MD_O;
constexpr int PP_O = PK_O + 128 * NREP;
constexpr int Y3_O = PP_O + H_ * S_;
constexpr int PS_N = Y3_O + Y_N;
constexpr int GR_ROW = 64, GR_COL = GR_ROW + 4 * S_, GR_KV = GR_COL + 16 * 16 * 192;
#define RO 16
#define RK 16
#define RX 1
constexpr int GR_O = GR_KV + 2 * 16 * 32 * RK, GR_X = GR_O + 2 * 16 * 64 * RO, GR_N = GR_X + 16 * 128 * RX;
constexpr int GR_F = GR_N + 256 * 64;
constexpr int STAT_N = 3 * S_;

__device__ __forceinline__ void st_ag(unsigned* p, unsigned v) { __hip_atomic_store(p, v, __ATOMIC_RELAXED, __HIP_MEMORY_SCOPE_AGENT); }
__device__ __forceinline__ unsigned ld_ag(const unsigned* p) { return __hip_atomic_load(p, __ATOMIC_RELAXED, __HIP_MEMORY_SCOPE_AGENT); }
__device__ __forceinline__ double ld_ag(const double* p) { return __hip_atomic_load(p, __ATOMIC_RELAXED, __HIP_MEMORY_SCOPE_AGENT); }
__device__ __forceinline__ unsigned f2word(float f) { const unsigned u = __float_as_uint(f); return u < 0xFFFFFFFEu ? u : 0xFFFFFFFEu; }
constexpr int SPIN_MAX = 1 << 18;
__device__ __forceinline__ unsigned umax(unsigned a, unsigned b) { return a > b ? a : b; }
__device__ __forceinline__ bool none(bool p) { return __builtin_amdgcn_ballot_w64(p) == 0ull; }

__device__ __forceinline__ void ln_from_sums(double s1, double s2, float& mean, float& var, float& mb, float& den) {
  const double md = s1 * (1.0 / 1024.0);
  const double vd = fma(-md, md, s2 * (1.0 / 1024.0));
  mean = (float)md; var = fmaxf((float)vd, 0.f);
  mb = rbf(mean);
  den = rbf(__builtin_amdgcn_sqrtf(rbf(rbf(var) + 1e-5f)));
}
__device__ __forceinline__ float frcp(float x) { return __builtin_amdgcn_rcpf(x); }
__device__ __forceinline__ void ln_apply2(float& a, float& b, float mb, float den, float rin) {
  a -= mb; b -= mb;
  rbf2(a, b);
  float qa = a * rin, qb = b * rin;
  qa = fmaf(fmaf(-qa, den, a), rin, qa);
  qb = fmaf(fmaf(-qb, den, b), rin, qb);
  rbf2(qa, qb);
  a = qa; b = qb;
}

#define CODE_ADDR(SYM, out64)                                                                                          \
  do {                                                                                                                 \
    unsigned lo_, hi_;                                                                                                 \
    asm volatile("s_getpc_b64 s[90:91]\n\ts_add_u32 s90, s90, " SYM "@rel32@lo+4\n\ts_addc_u32 s91, s91, " SYM          \
                 "@rel32@hi+12\n\ts_mov_b32 %0, s90\n\ts_mov_b32 %1, s91"                                             \
                 : "=s"(lo_), "=s"(hi_) :: "s90", "s91", "scc");                                                        \
    out64 = ((unsigned long long)hi_ << 32) | lo_;                                                                     \
  } while (0)

#define QNX 4
constexpr int QMX = 32 / QNX, QNG = 16 / QNX, QMG = 8 / QNG;

__device__ __forceinline__ unsigned touch(const void* p) { return *reinterpret_cast<const unsigned*>(p); }

__device__ __forceinline__ unsigned touch_code(unsigned long long lo, unsigned long long hi, int pc, int lane) {
  const unsigned long long p = lo + (unsigned)(pc * 8192 + lane * 128);
  return p + 4 <= hi ? *reinterpret_cast<__attribute__((address_space(1))) const unsigned*>(p) : 0u;
}

template <bool SC = false>
__device__ __forceinline__ void glds16s(const void* sbase, unsigned voff, unsigned lds_dst) {
  unsigned keep;
  if (SC)
    asm volatile("s_mov_b32 %0, m0\n\ts_mov_b32 m0, %3\n\ts_nop 2\n\tglobal_load_lds_dwordx4 %1, %2 sc1\n\ts_mov_b32 m0, %0"
                 : "=&s"(keep) : "v"(voff), "s"(sbase), "s"(lds_dst) : "memory");
  else
  asm volatile("s_mov_b32 %0, m0\n\ts_mov_b32 m0, %3\n\ts_nop 2\n\tglobal_load_lds_dwordx4 %1, %2\n\ts_mov_b32 m0, %0"
               : "=&s"(keep) : "v"(voff), "s"(sbase), "s"(lds_dst) : "memory");
}
template <bool SC>
__device__ __forceinline__ void glds16x4(const void* sbase, unsigned v0, unsigned v1, unsigned v2, unsigned v3, unsigned lds_dst) {
  unsigned keep;
  if (SC)
    asm volatile("s_mov_b32 %0, m0\n\ts_mov_b32 m0, %6\n\ts_nop 2\n\tglobal_load_lds_dwordx4 %1, %5 sc1\n\tglobal_load_lds_dwordx4 %2, %5 offset:1024 sc1\n\t"
                 "global_load_lds_dwordx4 %3, %5 offset:2048 sc1\n\tglobal_load_lds_dwordx4 %4, %5 offset:3072 sc1\n\ts_mov_b32 m0, %0"
                 : "=&s"(keep) : "v"(v0), "v"(v1), "v"(v2), "v"(v3), "s"(sbase), "s"(__builtin_amdgcn_readfirstlane(lds_dst)) : "memory");
  else
    asm volatile("s_mov_b32 %0, m0\n\ts_mov_b32 m0, %6\n\ts_nop 2\n\tglobal_load_lds_dwordx4 %1, %5\n\tglobal_load_lds_dwordx4 %2, %5 offset:1024\n\t"
                 "global_load_lds_dwordx4 %3, %5 offset:2048\n\tglobal_load_lds_dwordx4 %4, %5 offset:3072\n\ts_mov_b32 m0, %0"
                 : "=&s"(keep) : "v"(v0), "v"(v1), "v"(v2), "v"(v3), "s"(sbase), "s"(__builtin_amdgcn_readfirstlane(lds_dst)) : "memory");
}
constexpr float QSCALE = 0.17677669529663687f * 1.4426950408889634f;

__device__ __forceinline__ unsigned min4(u32x4 v) { const unsigned a = v[0] < v[1] ? v[0] : v[1], b = v[2] < v[3] ? v[2] : v[3]; return a < b ? a : b; }
#define LDS_AT(T, a) ((__attribute__((address_space(3))) T*)(uintptr_t)(a))
__device__ __forceinline__ u32x4 lds_rd4(unsigned a) { return *LDS_AT(const volatile u32x4, a); }
__device__ __forceinline__ void lds_wr(unsigned a, unsigned v) { *LDS_AT(volatile unsigned, a) = v; }
__device__ __forceinline__ void split3(float v, unsigned& p0, unsigned& p1, unsigned& p2) {
  p0 = pack2(v, v) & 0xffffu; v -= __uint_as_float(p0 << 16);
  p1 = pack2(v, v) & 0xffffu; v -= __uint_as_float(p1 << 16);
  p2 = pack2(v, v) & 0xffffu;
}

#define PIN() __builtin_amdgcn_sched_barrier(0)
template <int B, class F, int... I>
__device__ __forceinline__ void static_for_(F&& f, std::integer_sequence<int, I...>) { (f(std::integral_constant<int, B + I>{}), ...); }
template <int B, int E, class F>
__device__ __forceinline__ void static_for(F&& f) { static_for_<B>(f, std::make_integer_sequence<int, E - B>{}); }

#define TPRE 60
#define TPSL 0
#define FCPRE 44
#define FCSLP 2
#define SIGAT 24
#define XPRIO 1
#define OPD 6
#define LAUX 16
#define CSLP 2
#define EPRIO 3
#define OPRIO 1
#define XSLP 1
#define CSL1 8
#define XAUX 16
__device__ __forceinline__ void oproj_tail(const bool RES2, const int tid, const unsigned lbase, const unsigned flg, const int tmi, const int tni, const int idx,
                                           unsigned* osig, unsigned* xsig, const unsigned tag,
                                           const u16* A, u16* out,
                                           const u16* res, const u16* __restrict__ w_r, double* stats,
                                           const double* stats_in, unsigned* hand, u32x4 (&xres)[2] TEARG) {
  constexpr int ldw = D_, PD = OPD;
  const int lane = tid & 63, wid = __builtin_amdgcn_readfirstlane(tid >> 6);
  const int m0 = tmi * 128, n0 = tni * 64;
  const int dk = RES2 ? 7 : 3;
  if (wid == 0) STAMP(dk, 0);
  TE(1, 3);
  asm volatile("" : "+s"(A) :: "memory");
  PIN();
  const int row = m0 + (wid & 3) * 32 + (wid >> 2) * 16 + (lane >> 3), col = n0 + (lane & 7) * 8;
  const unsigned tb = lbase + 147456;
  typedef __attribute__((address_space(1))) double* gdp;
  typedef __attribute__((address_space(1))) unsigned* gup;
  unsigned tr, oad;
  gdp pst;
  gup pps;
  int xo;
  auto setup = [&]() __attribute__((always_inline)) {
    tr = tb + (row - m0) * 128 + (((lane ^ (lane >> 3)) & 7) << 4);
    pst = (gdp)(stats + ((lane & 3) == 3 ? 0 : (lane & 3)) * S_ + row + 8 * ((lane >> 2) & 1));
    pps = (gup)(hand + 2 * HAND_N + ((tmi * 16 + tni) * 128 + row - m0 + 8 * ((lane >> 2) & 1)) * 4 + (lane & 3));
    xo = (row * ldw + col) * 2;
    asm volatile("" : "+v"(tr), "+v"(pst), "+v"(pps), "+v"(xo));
    PIN();
  };
  {
    oad = lane == 0 ? flg + 24 : flg + 64 + lane * 4;
    gup ps = (gup)(osig + lane * 64);
    asm volatile("" : "+v"(oad), "+v"(ps));
    asm volatile("s_waitcnt vmcnt(1)" ::: "memory");
    TE(2, 1);
    const unsigned old = __hip_atomic_fetch_add(LDS_AT(unsigned, oad), 1u, __ATOMIC_RELAXED, __HIP_MEMORY_SCOPE_WORKGROUP);
    if (__builtin_amdgcn_readfirstlane(old) == 7u && lane < RO) __hip_atomic_store(ps, tag, __ATOMIC_RELAXED, __HIP_MEMORY_SCOPE_AGENT);
    PIN();
    TE(2, 2);
    if (wid == 0) STAMP(dk, 12);
  }
  auto announce = [&]() __attribute__((always_inline)) {
    asm volatile("" ::: "memory");
    PIN();
    __builtin_amdgcn_s_barrier();
    PIN();
    if (wid == 0) STAMP(dk, 1);
    TE(2, 3); TE(4, 0); TE(7, 1);
  };
  u32x4 rwr;
  auto sums = [&](const u32x4& x0, const u32x4& x1) __attribute__((always_inline)) {
    float f[2][3];
#pragma unroll
    for (int p = 0; p < 2; ++p) {
      f[p][0] = f[p][1] = f[p][2] = 0.f;
#pragma unroll
      for (int e = 0; e < 4; ++e) {
        const unsigned xu = p ? x1[e] : x0[e], wu = rwr[e];
        const bf16x2 x = __builtin_bit_cast(bf16x2, xu);
        f[p][0] = __builtin_amdgcn_fdot2_f32_bf16(x, __builtin_bit_cast(bf16x2, 0x3f803f80u), f[p][0], false);
        f[p][1] = __builtin_amdgcn_fdot2_f32_bf16(x, x, f[p][1], false);
        f[p][2] = __builtin_amdgcn_fdot2_f32_bf16(x, __builtin_bit_cast(bf16x2, wu), f[p][2], false);
      }
    }
    auto addx = [&](float keep, float give, auto ctl) {
      return keep + __int_as_float(__builtin_amdgcn_update_dpp(0, __float_as_int(give), decltype(ctl)::value, 0xf, 0xf, false));
    };
    const bool b2 = lane & 4, b1 = lane & 2, z0 = (lane & 3) == 0, z1 = (lane & 3) == 1;
    float g[3];
#pragma unroll
    for (int c = 0; c < 3; ++c) g[c] = addx(b2 ? f[1][c] : f[0][c], b2 ? f[0][c] : f[1][c], std::integral_constant<int, 0x141>{});
    const float u1 = addx(b1 ? g[2] : g[0], b1 ? g[0] : g[2], std::integral_constant<int, 0x4e>{});
    const float u2 = addx(g[1], g[1], std::integral_constant<int, 0x4e>{});
    return addx(z1 ? u2 : u1, z0 ? u2 : u1, std::integral_constant<int, 0xb1>{});
  };
  if (wid < 4) {
    const __amdgpu_buffer_rsrc_t rsa = __builtin_amdgcn_make_buffer_rsrc(const_cast<u16*>(A), 0, S_ * D_ * 2, 0x00020000);
    int sa0 = (tmi * 4 + wid) * 65536, va0 = (lane >> 4) * 512 + (lane & 15) * 16;
    asm volatile("" : "+v"(va0));
    asm volatile("" : "+s"(sa0));
    auto lda = [&](int i, int h) { return __builtin_amdgcn_raw_buffer_load_b128(rsa, va0 + (i & 1) * 2048 + h * 256, sa0 + (i >> 1) * 4096, LAUX); };
    unsigned wad[4], tw[4];
    f32x4 acc[2][4];
    const int k = (lane >> 5) * 16 + ((lane >> 2) & 7);
#pragma unroll
    for (int j = 0; j < 4; ++j) {
      wad[j] = lbase + k * 128 + (((2 * j) ^ (k & 6)) << 4) + 8 * (lane & 3);
      tw[j] = tb + wid * 4096 + (lane & 15) * 128 + (((2 * j + (lane >> 5)) ^ (lane & 7)) << 4) + ((lane >> 4) & 1) * 8;
      asm volatile("" : "+v"(wad[j]), "+v"(tw[j]));
    }
    auto ldw_ = [&](int i, int j) {
      const unsigned a = wad[j] + (i & 3) * 4096;
      s16x4 lo = __builtin_amdgcn_ds_read_tr16_b64_v4i16(LDS_AT(s16x4, a));
      s16x4 h4 = __builtin_amdgcn_ds_read_tr16_b64_v4i16(LDS_AT(s16x4, a + 1024));
      return (s16x8)__builtin_shufflevector(lo, h4, 0, 1, 2, 3, 4, 5, 6, 7);
    };
    u32x4 id[2];
#pragma unroll
    for (int j = 0; j < 2; ++j) {
#pragma unroll
      for (int e = 0; e < 4; ++e) id[j][e] = (lane >> 4) == 2 * j + ((lane >> 3) & 1) && ((lane & 7) >> 1) == e ? ((lane & 1) ? 0x3f800000u : 0x3f80u) : 0u;
      asm volatile("" : "+v"(id[j]));
    }
    unsigned rad = tb + wid * 4096 + (lane & 15) * 128 + (((lane >> 4) ^ (lane & 7)) << 4), rfa = flg + 36 + wid;
    asm volatile("" : "+v"(rad), "+v"(rfa));
    u32x4 rb[2][2];
    rwr = *reinterpret_cast<const u32x4*>(w_r + col);
    setup();
    asm volatile("" : "+v"(rwr));
    announce();
    u32x4 af[PD][2];
    s16x8 wf[2][4];
#pragma unroll
    for (int i = 0; i < PD; ++i) { af[i][0] = lda(i, 0); af[i][1] = lda(i, 1); }
    PIN();
    __builtin_amdgcn_s_setprio(OPRIO);
#pragma unroll
    for (int q = 0; q < 4; ++q) rb[q >> 1][q & 1] = lds_rd4((rad ^ ((q & 1) * 64)) + (q >> 1) * 2048);
    PIN();
#pragma unroll
    for (int j = 0; j < 4; ++j)
#pragma unroll
      for (int h = 0; h < 2; ++h) { asm volatile("v_mfma_f32_16x16x32_bf16 %0, %1, %2, 0" : "=&v"(acc[h][j]) : "v"(id[j & 1]), "v"(rb[h][j >> 1])); PIN(); }
#pragma unroll 1
    for (int spin = 0; spin < SPIN_MAX; ++spin)
      if (__builtin_amdgcn_readfirstlane(*LDS_AT(const volatile unsigned, flg)) >= 40u) break;
    asm volatile("" : "+v"(va0) :: "memory");
    PIN();
#pragma unroll
    for (int j = 0; j < 4; ++j) wf[0][j] = ldw_(0, j);
    s16x4 wlo;
    static_for<0, 32>([&](auto ic) __attribute__((always_inline)) {
      constexpr int i = decltype(ic)::value;
      if (i == SIGAT) { __hip_atomic_fetch_add(LDS_AT(unsigned, oad + 4), 1u, __ATOMIC_RELAXED, __HIP_MEMORY_SCOPE_WORKGROUP); PIN(); }
#pragma unroll
      for (int g = 0; g < 8; ++g) {
        const int j = g >> 1, h = g & 1;
        asm volatile("v_mfma_f32_16x16x32_bf16 %0, %1, %2, %0" : "+v"(acc[h][j]) : "v"(wf[i & 1][j]), "v"(af[i % PD][h]));
        PIN();
        if (i == 15 && !(g & 1)) { wad[g >> 1] += 65536; asm volatile("" : "+v"(wad[g >> 1])); }
        if (i + 1 < 32) {
          const s16x4 v = __builtin_amdgcn_ds_read_tr16_b64_v4i16(LDS_AT(s16x4, wad[g >> 1] + ((i + 1) & 15) * 4096 + (g & 1) * 1024));
          if (g & 1) wf[(i + 1) & 1][g >> 1] = (s16x8)__builtin_shufflevector(wlo, v, 0, 1, 2, 3, 4, 5, 6, 7);
          else wlo = v;
          PIN();
        }
        if (g >= 6 && i + PD < 32) { af[i % PD][h] = lda(i + PD, h); PIN(); }
        if (i + 1 < 32 && i != SIGAT && i + 1 != SIGAT) {
          if (g == 3) { __builtin_amdgcn_s_waitcnt(0xc47f); PIN(); }
          if (g == 7 && i + PD < 32) { __builtin_amdgcn_s_waitcnt(0x0470 | (2 * PD - 2)); PIN(); }
          if (g == 7 && i + PD >= 32) { __builtin_amdgcn_s_waitcnt(0xc47f); PIN(); }
        }
      }
    });
    asm volatile("s_nop 3" : "+v"(acc[0][0]), "+v"(acc[1][0]));
    TE(4, 1); TE(7, 2);
    if (wid == 0) STAMP(dk, 17);
    if (wid == 1 && !RES2) STAMP(4, 0);
#pragma unroll
    for (int j = 0; j < 4; ++j)
#pragma unroll
      for (int h = 0; h < 2; ++h) {
        *LDS_AT(u32x2, tw[j] + h * 2048) = u32x2{pack2(acc[h][j][0], acc[h][j][1]), pack2(acc[h][j][2], acc[h][j][3])};
        PIN();
      }
    asm volatile("" ::: "memory");
    *LDS_AT(volatile unsigned char, rfa) = (unsigned char)2;
#pragma unroll
    for (int p = 0; p < 2; ++p) xres[p] = lds_rd4(tr + p * 1024);
    PIN();
    if (!RES2) {
      const __amdgpu_buffer_rsrc_t rso = __builtin_amdgcn_make_buffer_rsrc(out, 0, S_ * D_ * 2, 0x00020000);
      const __amdgpu_buffer_rsrc_t rsf = __builtin_amdgcn_make_buffer_rsrc(xsig + wid, 0, RX * 512, 0x00020000);
      const u32x4 x2 = lds_rd4(tr + 2048), x3 = lds_rd4(tr + 3072);
      PIN();
#pragma unroll
      for (int p = 0; p < 4; ++p) { __builtin_amdgcn_raw_buffer_store_b128(p < 2 ? xres[p] : (p < 3 ? x2 : x3), rso, xo + p * (8 * ldw * 2), 0, XAUX); PIN(); }
      TE(4, 2); TE(5, 0);
      const float s0 = sums(xres[0], xres[1]), s1 = sums(x2, x3);
      if ((lane & 3) != 3) { __builtin_amdgcn_global_atomic_fadd_f64(pst, (double)s0); __builtin_amdgcn_global_atomic_fadd_f64(pst + 16, (double)s1); }
      PIN();
      TE(4, 3); TE(5, 1);
      asm volatile("s_waitcnt vmcnt(2)" ::: "memory");
      __builtin_amdgcn_raw_buffer_store_b32(tag, rsf, lane * 512 + 256, 0, 16);
      *LDS_AT(volatile unsigned char, rfa) = (unsigned char)3;
      PIN();
      TE(5, 2);
      asm volatile("s_waitcnt vmcnt(1)" ::: "memory");
      __builtin_amdgcn_raw_buffer_store_b32(tag, rsf, lane * 512, 0, 16);
      PIN();
      TE(5, 3);
      __builtin_amdgcn_s_setprio(0);
      return;
    }
    TE(4, 2); TE(5, 0);
  } else {
    u32x4 rres[4];
    double is1[4], is2[4];
#pragma unroll
    for (int p = 0; p < 4; ++p)
      rres[p] = __builtin_amdgcn_raw_buffer_load_b128(__builtin_amdgcn_make_buffer_rsrc(const_cast<u16*>(res), 0, S_ * D_ * 2, 0x00020000), ((row - 16 + 8 * p) * ldw + col) * 2, 0, 16);
    rwr = *reinterpret_cast<const u32x4*>(w_r + col);
#pragma unroll
    for (int p = 0; p < 4; ++p) { is1[p] = ld_ag(stats_in + row - 16 + 8 * p); is2[p] = ld_ag(stats_in + S_ + row - 16 + 8 * p); }
    PIN();
    setup();
    if (RES2) {
#pragma unroll
      for (int p = 0; p < 4; ++p) {
        float mean, var, mb, den;
        ln_from_sums(is1[p], is2[p], mean, var, mb, den);
        const float rin = frcp(den);
#pragma unroll
        for (int e = 0; e < 4; ++e) {
          const f32x2 x = {__uint_as_float(rres[p][e] << 16), __uint_as_float(rres[p][e] & 0xffff0000u)};
          const f32x2 h = (x - f32x2{mb, mb}) * f32x2{rin, rin} + x;
          rres[p][e] = pack2(h[0], h[1]);
        }
        asm volatile("" : "+v"(rres[p]));
      }
      PIN();
    }
    if ((idx >> 5) == tmi * 4 + wid - 4) {
      const int dr = idx - (row - 16);
      u32x4 qq;
#pragma unroll 1
      for (int i = 0; i < SPIN_MAX; ++i) {
        qq = __builtin_amdgcn_raw_buffer_load_b128(__builtin_amdgcn_make_buffer_rsrc(hand, 0, HAND_N * 4, 0x00020000), (GRAN_N + Y_N) * 4 + col * 2, 0, 16);
        if (none(umax(umax(qq[0], qq[1]), umax(qq[2], qq[3])) == SENT)) break;
        asm volatile("" ::: "memory");
      }
#pragma unroll
      for (int p = 0; p < 4; ++p)
#pragma unroll
        for (int e = 0; e < 4; ++e) rres[p][e] = dr == 8 * p ? qq[e] : rres[p][e];
    }
#pragma unroll 1
    for (int i = 0; i < 4 * SPIN_MAX; ++i)
      if (__builtin_amdgcn_readfirstlane(*LDS_AT(const volatile unsigned, flg + 16)) >= 12u) break;
#pragma unroll
    for (int p = 0; p < 4; ++p) *LDS_AT(volatile u32x4, tr - 2048 + p * 1024) = rres[p];
    asm volatile("s_waitcnt lgkmcnt(0)" ::: "memory");
    announce();
    TE(4, 1); TE(7, 2);
    asm volatile("s_waitcnt vmcnt(0)" : "+v"(rwr) :: "memory");
    if (!RES2) return;
    __builtin_amdgcn_s_sleep(TPRE);
#pragma unroll 1
    for (int i = 0; i < 4 * SPIN_MAX; ++i) {
      if (__builtin_amdgcn_readfirstlane(*LDS_AT(const volatile unsigned char, flg + 32 + wid)) == 2u) break;
      __builtin_amdgcn_s_sleep(TPSL);
    }
    asm volatile("" ::: "memory");
    __builtin_amdgcn_s_setprio(XPRIO);
    PIN();
    TE(4, 2); TE(5, 0);
#pragma unroll
    for (int p = 0; p < 2; ++p) xres[p] = lds_rd4(tr + p * 1024);
  }
  TE(4, 3); TE(7, 3);
  const float sv = sums(xres[0], xres[1]);
  if (wid == 0) STAMP(dk, 14);
  if ((lane & 3) != 3) __hip_atomic_store(pps, f2word(sv), __ATOMIC_RELAXED, __HIP_MEMORY_SCOPE_AGENT);
  PIN();
  __builtin_amdgcn_s_setprio(0);
  __hip_atomic_fetch_add(LDS_AT(unsigned, oad), 1u, __ATOMIC_RELAXED, __HIP_MEMORY_SCOPE_WORKGROUP);
  TE(5, 1); TE(5, 2);
  PIN();
}

#define MFMA32(a, b, c) __builtin_amdgcn_mfma_f32_32x32x16_bf16(a, b, c, 0, 0, 0)
#define MFMA16(a, b, c) __builtin_amdgcn_mfma_f32_16x16x32_bf16(a, b, c, 0, 0, 0)

#define PLO(x) __builtin_bit_cast(bf16x16, __builtin_shufflevector(x, x, 0, 1, 2, 3, 4, 5, 6, 7))
#define PHI(x) __builtin_bit_cast(bf16x16, __builtin_shufflevector(x, x, 8, 9, 10, 11, 12, 13, 14, 15))
#define SMF32(a, b, c) __builtin_amdgcn_smfmac_f32_32x32x32_bf16(__builtin_bit_cast(bf16x8, a), b, c, sidx, 0, 0)
#define SMF16(a, b, c) __builtin_amdgcn_smfmac_f32_16x16x64_bf16(__builtin_bit_cast(bf16x8, a), b, c, sidx, 0, 0)

__device__ __forceinline__ void patch_kv(unsigned tab, int r, s16x8& k0, s16x8& k1, s16x8& v0, s16x8& v1, bool on = true) {
  const int lane = fresh_lane(), hi = lane >> 5;
  if (on && (lane & 31) == kpos(r)) {
    k0 = *LDS_AT(const s16x8, tab + hi * 16); k1 = *LDS_AT(const s16x8, tab + 32 + hi * 16);
  }
  const short vv = *LDS_AT(const short, tab + 128 + (lane & 31) * 2);
  const int js = (on && hi == ((r >> 2) & 1)) ? (r >> 4) * 8 + ((r >> 3) & 1) * 4 + (r & 3) : -1;
#pragma unroll
  for (int e = 0; e < 8; ++e) { v0[e] = js == e ? vv : v0[e]; v1[e] = js == 8 + e ? vv : v1[e]; }
}

__device__ __forceinline__ void attn_safe(const u16* KV, int tile0, s16x8 q0, s16x8 q1,
                                       f32x16& o, float& l_out, int ptile, int prow, unsigned ptab) {
  const int lane = fresh_lane();
  float m = -INFINITY, l = 0.f;
  const int sidx = 0xDDDDDDDD;
  const __amdgpu_buffer_rsrc_t rs = __builtin_amdgcn_make_buffer_rsrc(const_cast<u16*>(KV), 0, 2 * S_ * D_ * 2, 0x00020000);
#pragma unroll
  for (int e = 0; e < 16; ++e) o[e] = 0.f;
  for (int tt = 0; tt < 64; ++tt) {
    const int t = tile0 + tt, off = t * 2048 + lane * 16;
    s16x8 k0 = __builtin_bit_cast(s16x8, __builtin_amdgcn_raw_buffer_load_b128(rs, off, 0, 16));
    s16x8 k1 = __builtin_bit_cast(s16x8, __builtin_amdgcn_raw_buffer_load_b128(rs, off + 1024, 0, 16));
    s16x8 v0 = __builtin_bit_cast(s16x8, __builtin_amdgcn_raw_buffer_load_b128(rs, off + S_ * D_ * 2, 0, 16));
    s16x8 v1 = __builtin_bit_cast(s16x8, __builtin_amdgcn_raw_buffer_load_b128(rs, off + S_ * D_ * 2 + 1024, 0, 16));
    if (t == ptile) patch_kv(ptab, prow, k0, k1, v0, v1);
    f32x16 sc;
#pragma unroll
    for (int e = 0; e < 16; ++e) sc[e] = 0.f;
    sc = MFMA32(k0, q0, sc); sc = MFMA32(k1, q1, sc);
    float mt = sc[0];
#pragma unroll
    for (int e = 1; e < 16; ++e) mt = fmaxf(mt, sc[e]);
    mt = fmaxf(mt, __shfl_xor(mt, 32, 64));
    const float mn = fmaxf(m, mt);
    const float alpha = __builtin_amdgcn_exp2f(m - mn);
    m = mn;
#pragma unroll
    for (int e = 0; e < 16; ++e) sc[e] = __builtin_amdgcn_exp2f(sc[e] - mn);
    float ls = 0.f;
#pragma unroll
    for (int e = 0; e < 16; ++e) ls += __uint_as_float(__float_as_uint(sc[e]) & 0xffff0000u);
    l = l * alpha + ls;
#pragma unroll
    for (int e = 0; e < 16; ++e) o[e] *= alpha;
    o = SMF32(v0, PLO(sc), o); o = SMF32(v1, PHI(sc), o);
  }
  l_out = l + __shfl_xor(l, 32, 64);
}

__device__ __forceinline__ float wave_fmax(float v) {
  asm volatile("s_nop 1\n\tv_max_f32_dpp %0, %0, %0 row_shr:1 row_mask:0xf bank_mask:0xf\n\ts_nop 1\n\tv_max_f32_dpp %0, %0, %0 row_shr:2 row_mask:0xf bank_mask:0xf\n\t"
               "s_nop 1\n\tv_max_f32_dpp %0, %0, %0 row_shr:4 row_mask:0xf bank_mask:0xf\n\ts_nop 1\n\tv_max_f32_dpp %0, %0, %0 row_shr:8 row_mask:0xf bank_mask:0xf\n\t"
               "s_nop 1\n\tv_max_f32_dpp %0, %0, %0 row_bcast:15 row_mask:0xa bank_mask:0xf\n\ts_nop 1\n\tv_max_f32_dpp %0, %0, %0 row_bcast:31 row_mask:0xc bank_mask:0xf\n\ts_nop 0"
               : "+v"(v));
  return __int_as_float(__builtin_amdgcn_readlane(__float_as_int(v), 63));
}

#define PSLP 8
#define WSLP 3
#define PRE1 8
#define PRE5 8
#define W3SLP 4
#define FSTART 10
#define OAUX 16
#define OMX 4
#define T0 2u
#define TW 10
#define HW 2
#define HP1 1
#define POLLAT 11
#define X2AT 2
#define HPRIO 3
#define SSLP 1
#define SPRE 6
#define WGAPS {0, 1, 7, 8, 9, 10, 11, 12}
#define KVS 4
#define PNS 12
#define G0 5u
#define GS 2u
#define IAT 1u
#define YSLP 2
#define ISLP 8
#define KSLP 2
#define UAT 13u
#define UHW 0
#define PPSLP 2
#define XRSLP 2
#define MPRE 20
#define YD1 150
#define YD2 180
#define KDT 200
#define XDT 45
#define R2PRIO 0
#define LNPRIO 1
#define MSLP 4
#define BARG 0
struct Args {
  long long lay;
  const u16* a; u16* kv; u16* o; u16* xo; double* so;
  const u16* W; u16* wc;
  const u16 *wg, *wu, *wd, *wo, *w_r;
  unsigned* gran; float* csf; double* stats; unsigned* hand; u16* out;
};
typedef __attribute__((address_space(4))) const Args* ArgP;
#define WCAT 3
#define CAP0 1
#define PRE4 50
#define PRE7 20
#define UARR 12u
#define GF0 10
#define XGATE 8
#define ASLP 1
#define B15 3
#define KO 5
#define PRO 2
#define HS 2
#define XD2 90
#define WMAP 0x32100333
#define SMAP 0
#define STCH 6
#define KPOLL 10
#define TPRIO 2
#define TSLP 9
__device__ __forceinline__ bool slow_cols(const u16* W, int tni) { return ((((unsigned)(uintptr_t)W >> 7) + tni) & 7) == 3; }
#define ARGS_HERE                                                                                                      \
  ArgP ap_ = ap; asm volatile("" : "+s"(ap_));                                                                          \
  const u16* A = ap_->a; const u16* __restrict__ W = ap_->W; u16* KV = ap_->kv; u16* Of = ap_->o;        \
  const u16* __restrict__ wg = ap_->wg; const u16* __restrict__ wu = ap_->wu; const u16* __restrict__ wd = ap_->wd;       \
  const u16* __restrict__ wo = ap_->wo; const u16* __restrict__ w_r = ap_->w_r; unsigned* gran = ap_->gran;              \
  float* csf = ap_->csf; const double* stats = ap_->stats; double* stats_out = ap_->so; unsigned* hand = ap_->hand; \
  u16* xout = ap_->xo; u16* __restrict__ out = ap_->out
#define AGAIN "s_getpc_b64 s[90:91]\n\ts_add_u32 s90, s90, layers_kernel@rel32@lo+4\n\ts_addc_u32 s91, s91, layers_kernel@rel32@hi+12\n\ts_setpc_b64 s[90:91]"
#define KEPT(c)                                                                                                        \
  c "{v[104:107]}"(gu[0]), c "{v[108:111]}"(gu[1]), c "{v[112:115]}"(gu[2]), c "{v[116:119]}"(gu[3]), c "{v[120:123]}"(gu[4]),      \
  c "{v[124:127]}"(gu[5]), c "{v[128:131]}"(gu[6]), c "{v[132:135]}"(gu[7]), c "{v[136:139]}"(gu[8]), c "{v[140:143]}"(gu[9]),      \
  c "{v[144:147]}"(gu[10]), c "{v[148:151]}"(gu[11]), c "{v[152:155]}"(gu[12]), c "{v[156:159]}"(gu[13]), c "{v[160:163]}"(gu[14]), \
  c "{v[164:167]}"(gu[15])
#define LAYER_HERE ArgP apl_ = ap; asm volatile("" : "+s"(apl_)); const bool LAY2 = apl_->lay != 0
__device__ __forceinline__ void layer_body(const int wid, const int bid, const unsigned lbase, const ArgP ap, const unsigned long long t_entry) {
  constexpr int K = 1024, NKT = 16, NST = 3, ABYTES = 16384, WBYTES = 24576, STB = ABYTES + WBYTES, ldw = 3 * D_;
  constexpr int TABA = NST * STB, ALPHA = TABA + 2048, CRED = ALPHA + 512;
  constexpr int ASB = 16384, FLG = 8 * ASB + 256;
  constexpr int DUM = FLG + 64, TAB = DUM + 256, RED = TAB + 512, SACT = RED + 4096, XML = SACT + 3584, UGL = XML + 4096, SPM = UGL + 1024, QOFF = 9 * ASB;
  constexpr int TABW = RED;
  static_assert(SPM + 2048 <= QOFF && CRED <= 8 * ASB && RED + 8192 <= QOFF && RED % 16 == 0, "lds");
  const int xcd = (bid & 1) * 4 + ((bid >> 1) & 3), loc = bid >> 3;
  const int tmi = (xcd / QNG) * QMX + loc / QNX, tni = (xcd % QNG) * QNX + loc % QNX;
  const int m0 = tmi * 128, n0 = tni * 64;
  const int omi = (xcd / (OMX / 2)) * OMX + loc / (32 / OMX), oni = (xcd % (OMX / 2)) * (32 / OMX) + loc % (32 / OMX);
  constexpr unsigned VOFF = (unsigned)S_ * D_ * 2;
  auto tile_at = [&](unsigned n) __attribute__((always_inline)) {
#pragma unroll 1
    for (int i = 0; i < 4 * SPIN_MAX; ++i) {
      if (__builtin_amdgcn_readfirstlane(*LDS_AT(const volatile unsigned, lbase + FLG + 36)) == n * 0x01010101u) break;
      __builtin_amdgcn_s_sleep(1);
    }
    asm volatile("" ::: "memory");
    PIN();
  };
  auto all_ge = [&](unsigned w, unsigned n) { return ((w + (128u - n) * 0x01010101u) & 0x80808080u) == 0x80808080u; };
  if (wid >= 8) {
    u32x4 gu[16];
    LAYER_HERE;
    constexpr int dk = 6;
    const int hw = wid - 8, b = bid;
    int nb = 0;
    constexpr int lim = NKT - 1;
    auto tick_nw = [&]() {
      PIN();
      if (nb < lim) { __builtin_amdgcn_s_barrier(); ++nb; } else __builtin_amdgcn_s_sleep(2);
      asm volatile("" ::: "memory");
      PIN();
    };
    auto tick = [&]() {
      asm volatile("s_waitcnt lgkmcnt(0)" ::: "memory");
      tick_nw();
    };
    auto fold16 = [&](f32x2 y0, f32x2 y1) __attribute__((always_inline)) {
      const auto q0 = __builtin_amdgcn_permlane32_swap(__float_as_uint(y0[0]), __float_as_uint(y1[0]), false, false);
      const auto q1 = __builtin_amdgcn_permlane32_swap(__float_as_uint(y0[1]), __float_as_uint(y1[1]), false, false);
      const auto q = __builtin_amdgcn_permlane16_swap(__float_as_uint(__uint_as_float(q0[0]) + __uint_as_float(q0[1])),
                                                      __float_as_uint(__uint_as_float(q1[0]) + __uint_as_float(q1[1])), false, false);
      return __uint_as_float(q[0]) + __uint_as_float(q[1]);
    };
    unsigned best, tag, rmd;
    float alpha, m, wsum;
    u32x4 tw;
    u32x2 xq;
    s16x8 sl[8];
    const int kcap = CAP0 + (tmi & 7);
    if (!LAY2) {
      ARGS_HERE;
      const int lane = fresh_lane(), t = hw * 64 + lane;
      const unsigned ep = ld_ag(gran);
      const int srow = m0 + tni * 8 + hw * 2;
      u32x4 xa[2][2], wv[8];
      const int wr_ = (3 - ((unsigned)(uintptr_t)W >> 7) - (tni & ~3)) & 7;
      const bool cpy = hw == 3 && wr_ < 4 && wr_ != (tni & 3), wld = hw < 3 ? wr_ != (tni & 3) : cpy;
      const int wcn = cpy ? (tni & ~3) + wr_ : tni, wcp = cpy ? (tni - wr_ - 1) & 3 : hw;
      const int wof = ((tmi * 64 + (lane >> 3)) * ldw + wcp * D_ + wcn * 64 + (lane & 7) * 8) * 2;
      auto ld_w = [&]() {
        const __amdgpu_buffer_rsrc_t rw = __builtin_amdgcn_make_buffer_rsrc(const_cast<u16*>(W), 0, wld ? D_ * ldw * 2 : 0, 0x00020000);
#pragma unroll
        for (int r = 0; r < 8; ++r) wv[r] = __builtin_amdgcn_raw_buffer_load_b128(rw, wof, r * 8 * ldw * 2, 0);
        PIN();
      };
      auto wcopy = [&]() __attribute__((always_inline)) {
        if (cpy) {
          const __amdgpu_buffer_rsrc_t rw = __builtin_amdgcn_make_buffer_rsrc(ap_->wc, 0, D_ * ldw * 2, 0x00020000);
#pragma unroll
          for (int r = 0; r < 8; ++r) __builtin_amdgcn_raw_buffer_store_b128(wv[r], rw, wof + r * 8 * ldw * 2, 0, 16);
        }
        PIN();
      };
      const int wat = ((hw == 3 ? SMAP : WMAP) >> (4 * (tmi & 7))) & 3;
      unsigned ps = 0u;
      auto stouch = [&](int at) __attribute__((always_inline)) {
        if (at == STCH && cpy) ps = __builtin_amdgcn_raw_buffer_load_b32(__builtin_amdgcn_make_buffer_rsrc(const_cast<u16*>(W), 0, D_ * ldw * 2, 0x00020000),
                                                                        (((tmi ^ 8) * 64 + lane) * ldw + wcp * D_ + wcn * 64) * 2, 0, 0);
        PIN();
      };
      auto ld_x = [&](int h) {
#pragma unroll
        for (int r = 0; r < 2; ++r) xa[r][h] = *reinterpret_cast<const u32x4*>(A + (size_t)(srow + r) * D_ + (h ^ (tmi >> 3)) * 512 + lane * 8);
        PIN();
      };
      ld_x(0);
      if (X2AT == 0) ld_x(1);
      if (wat == 0) ld_w();
      unsigned pc = 0u;
      if (hw == 3) {
        unsigned long long a0, a1;
        CODE_ADDR("layers_kernel", a0);
        CODE_ADDR("end_marker_kernel", a1);
        pc = touch_code(a0, a1, loc, lane);
      }
      if (hw == 2) st_ag(gran + GR_N + bid * 64 + lane, lane + 1u);
      {
        const int gi = bid * 256 + t;
        if (gi < GRAN_N / 4) __builtin_amdgcn_raw_buffer_store_b128(u32x4{SENT, SENT, SENT, SENT}, __builtin_amdgcn_make_buffer_rsrc(hand, 0, GRAN_N * 4, 0x00020000), gi * 16, 0, 16);
        if (t < 24) __hip_atomic_store(stats_out + (t >> 3) * S_ + m0 + tni * 8 + (t & 7), 0.0, __ATOMIC_RELAXED, __HIP_MEMORY_SCOPE_AGENT);
      }
      tick();
      if (wat == 1) ld_w();
      if (X2AT == 1) ld_x(1);
      tick();
      if (wat == 2) ld_w();
      if (WCAT == 2) wcopy();
      if (X2AT == 2) ld_x(1);
      asm volatile("" :: "v"(ep));
      tick();
      if (wat == 3) ld_w();
      if (WCAT == 3) wcopy();
      if (X2AT == 3) ld_x(1);
      stouch(3);
#pragma unroll
      for (int i = 0; i < HW; ++i) { tick(); stouch(4 + i); }
      __builtin_amdgcn_s_setprio(HP1);
      PIN();
      tag = ep + 1u;
      const __amdgpu_buffer_rsrc_t rsc = __builtin_amdgcn_make_buffer_rsrc(gran, 0, GR_N * 4, 0x00020000);
      {
        u32x4 od;
#pragma unroll
        for (int q = 0; q < 4; ++q) od[q] = ((lane & 7) >> 1) == q ? ((lane & 1) ? 0x3f800000u : 0x00003f80u) : 0u;
        f32x4 d[2] = {{0.f, 0.f, 0.f, 0.f}, {0.f, 0.f, 0.f, 0.f}};
#pragma unroll
        for (int r = 0; r < 8; ++r) d[r & 1] = __builtin_amdgcn_mfma_f32_16x16x32_bf16(__builtin_bit_cast(s16x8, od), __builtin_bit_cast(s16x8, wv[r]), d[r & 1], 0, 0, 0);
        u32x4 gr;
#pragma unroll
        for (int q = 0; q < 4; ++q) gr[q] = ((__float_as_uint(dpp_add<0x128, 0xf>(fadd(d[0][q], d[1][q]))) + 0x80u) & ~0xffu) | ((tag >> (8 * q)) & 0xffu);
        asm volatile("s_waitcnt vmcnt(0)" ::: "memory");
        __builtin_amdgcn_raw_buffer_store_b128(gr, rsc, (lane & 40) || !wld ? GR_N * 4 : (GR_COL + (wcn * 16 + tmi) * 192 + wcp * 64 + (lane & 7) * 8 + (lane >> 4) * 4) * 4, 0, 16);
      }
      asm volatile("" :: "v"(pc));
      tick_nw();
      stouch(6);
      {
        float xv[2][16], mean[2];
#pragma unroll
        for (int r = 0; r < 2; ++r) {
          float sm = 0.f;
#pragma unroll
          for (int i = 0; i < 4; ++i) {
            xv[r][2 * i] = __uint_as_float(xa[r][0][i] << 16); xv[r][2 * i + 1] = __uint_as_float(xa[r][0][i] & 0xffff0000u);
            xv[r][8 + 2 * i] = __uint_as_float(xa[r][1][i] << 16); xv[r][8 + 2 * i + 1] = __uint_as_float(xa[r][1][i] & 0xffff0000u);
          }
#pragma unroll
          for (int i = 0; i < 16; ++i) sm += xv[r][i];
          mean[r] = wave_sum(sm) * (1.0f / 1024.0f);
          tick();
          stouch(7 + r);
        }
        float vr[2];
#pragma unroll
        for (int r = 0; r < 2; ++r) {
          float v = 0.f;
#pragma unroll
          for (int i = 0; i < 16; ++i) { const float d = xv[r][i] - mean[r]; v += d * d; }
          vr[r] = wave_sum(v) * (1.0f / 1024.0f);
          if (r == 0) tick();
        }
        const float vv = lane & 2 ? vr[1] : vr[0], mm = lane & 2 ? mean[1] : mean[0];
        const float rin = 1.0f / rbf(sqrtf(rbf(rbf(vv) + 1e-5f)));
        if (lane < 4) __builtin_amdgcn_raw_buffer_store_b64(u32x2{__float_as_uint((lane & 1) ? rbf(mm) : rin), tag}, rsc, (GR_ROW + srow * 4 + lane * 2) * 4, 0, 16);
      }
      tick();
      float cs;
      while (nb < POLLAT) tick();
      asm volatile("" :: "v"(ps));
#pragma unroll 1
      for (int spin = 0;; ++spin) {
        u32x4 csv[4];
        const __amdgpu_buffer_rsrc_t rsw = __builtin_amdgcn_make_buffer_rsrc(gran, 0, hw < 3 ? GR_N * 4 : 0, 0x00020000);
#pragma unroll
        for (int i = 0; i < 4; ++i) csv[i] = __builtin_amdgcn_raw_buffer_load_b128(rsw, (GR_COL + (tni * 16 + (lane >> 4)) * 192 + hw * 64 + (lane & 15) * 4) * 4, i * (4 * 192 * 4), 16);
        const u32x4 rp = __builtin_amdgcn_raw_buffer_load_b128(rsc, (GR_ROW + (m0 + (t & 127)) * 4) * 4, 0, 16);
        PIN();
        tick(); tick();
        unsigned bad = 0u;
        float z[4];
#pragma unroll
        for (int q = 0; q < 4; ++q) {
          const unsigned tq = tag >> (8 * q);
          bad |= ((csv[0][q] ^ tq) | (csv[1][q] ^ tq)) | ((csv[2][q] ^ tq) | (csv[3][q] ^ tq));
          z[q] = fadd(fadd(__uint_as_float(csv[0][q] & ~0xffu), __uint_as_float(csv[1][q] & ~0xffu)), fadd(__uint_as_float(csv[2][q] & ~0xffu), __uint_as_float(csv[3][q] & ~0xffu)));
        }
        cs = fold16(f32x2{z[0], z[1]}, f32x2{z[2], z[3]});
        bad = (hw < 3 ? bad & 0xffu : 0u) | (rp[1] ^ tag) | (rp[3] ^ tag);
        alpha = __uint_as_float(rp[0]); m = __uint_as_float(rp[2]);
        if (none(bad != 0u) || spin >= SPIN_MAX) break;
      }
      if (tmi == 0 && hw < 3) st_ag(reinterpret_cast<unsigned*>(csf) + hw * 1024 + n0 + (lane & 15) * 4 + (lane >> 4), __float_as_uint(cs));
      unsigned c0, c1, c2;
      split3(cs, c0, c1, c2);
      tw = u32x4{c0 | (c1 << 16), c0 | (c2 << 16), c1 | (c0 << 16), 0u};
      best = rmd = 0u;
      wsum = 0.f;
      asm volatile("" : "=v"(xq), KEPT("="));
#pragma unroll
      for (int i = 0; i < 8; ++i) asm volatile("" : "=v"(sl[i]));
    } else {
      asm volatile("; kept registers" : KEPT("="));
      PIN();
      ARGS_HERE;
      const int lane = fresh_lane(), t = hw * 64 + lane;
      best = rmd = 0u;
      tag = __builtin_amdgcn_readfirstlane(*LDS_AT(const volatile unsigned, lbase + FLG + 20));
#pragma unroll
      for (int i = 0; i < 8; ++i) asm volatile("" : "=v"(sl[i]));
      unsigned sad;
      auto tickc = [&]() __attribute__((always_inline)) {
        tick();
        if (nb == kcap + 1) {
#pragma unroll
          for (int i = 0; i < 8; ++i) {
            const unsigned a = (sad ^ ((i >> 2) * 64) ^ (((i >> 1) & 1) * 16)) + (i & 1) * 12288;
            sl[i] = __builtin_shufflevector(__builtin_amdgcn_ds_read_tr16_b64_v4i16(LDS_AT(s16x4, a)), __builtin_amdgcn_ds_read_tr16_b64_v4i16(LDS_AT(s16x4, a + 1536)), 0, 1, 2, 3, 4, 5, 6, 7);
          }
        }
        PIN();
      };
      static_assert(CAP0 + 8 <= KPOLL, "the stage is taken before that");
      if (hw == 1) STAMP(4, 4);
      if (hw == 0) {
        __builtin_amdgcn_s_sleep(TPRE);
        tile_at(3u);
        __builtin_amdgcn_s_sleep(XGATE);
        STAMP(4, 3);
#pragma unroll 1
        for (int spin = 0; spin < SPIN_MAX; ++spin) {
          if (none(ld_ag(gran + GR_X + (tmi * RX + tni % RX) * 128 + 64 + lane) != tag)) break;
          __builtin_amdgcn_s_sleep(1);
        }
      }
      if (hw == 0) STAMP(4, 5);
      asm volatile("" ::: "memory");
      PIN();
      __builtin_amdgcn_s_barrier();
      PIN();
      if (hw == 1) STAMP(4, 6);
      {
        const int ln = fresh_lane(), g4 = ln >> 4, tp = ln & 3, k = 8 * g4 + ((ln & 15) >> 2);
        sad = lbase + (kcap % NST) * STB + ABYTES + k * 384 + (((2 * (tp >> 1)) ^ (4 * ((k >> 1) & 1)) ^ (g4 & 1)) << 4) + 8 * (tp & 1) + (hw % 3) * 128;
      }
      wsum = __uint_as_float(__builtin_amdgcn_readfirstlane(*LDS_AT(const volatile unsigned, lbase + FLG + 60)));
      asm volatile("" : "+v"(sad), "=v"(tw), "=v"(alpha), "=v"(m));
      PIN();
      const int row = m0 + (t & 127);
      const __amdgpu_buffer_rsrc_t rsg = __builtin_amdgcn_make_buffer_rsrc(hand, 0, GRAN_N * 4, 0x00020000);
      if (hw < 2 || tmi == tni) {
      while (nb < HS) tickc();
#pragma unroll 1
      for (int spin = 0; spin < SPIN_MAX; ++spin) {
        const unsigned v = ld_ag(gran + GR_X + (tmi * RX + tni % RX) * 128 + lane);
        tickc();
        if (none(v != tag)) break;
      }
      asm volatile("" ::: "memory");
      PIN();
      const double s1 = ld_ag(stats + row), s2 = ld_ag(stats + S_ + row), sw = ld_ag(stats + 2 * S_ + row);
      tickc();
      float mean, var, mb, den;
      ln_from_sums(s1, s2, mean, var, mb, den);
      const float rin = frcp(den), g1 = 1.0f + rin;
      const float mb2 = rbf(mean + (mean - mb) * rin);
      const float den2 = rbf(__builtin_amdgcn_sqrtf(rbf(rbf(var * g1 * g1) + 1e-5f)));
      alpha = g1 * frcp(den2); m = (mb * rin + mb2) * frcp(g1);
      if (tmi == tni && hw >= 2) {
        const float r = (float)fma(-(double)mb, (double)wsum, sw) * rin;
        const unsigned rb = pack2(r, r);
        unsigned rkey = (rb ^ ((unsigned)((int)rb >> 31) | 0x80000000u)) & 0xffff0000u;
        if (r != r) rkey = 0u;
        rkey = wave_umax(rkey | (unsigned)(2047 - row));
        const unsigned md = __builtin_amdgcn_readlane(f2word(__uint_as_float((__float_as_uint(mb) >> 16) | (__float_as_uint(den) & 0xffff0000u))), (2047 - (int)(rkey & 0xffffu)) & 63);
        __builtin_amdgcn_raw_buffer_store_b64(u32x2{rkey, md}, rsg, lane < NREP ? lane * 256 + (tmi * 2 + hw - 2) * 8 : GRAN_N * 4, 0, 16);
      }
      }
      while (nb < KPOLL) tickc();
#pragma unroll 1
      for (int spin = 0; spin < SPIN_MAX; ++spin) {
        const u32x2 v = __builtin_amdgcn_raw_buffer_load_b64(rsg, (bid % NREP) * 256 + (lane & 31) * 8, 0, 16);
        tick(); tick();
        if (none(umax(v[0], v[1]) == SENT)) {
          best = wave_umax(v[0]);
          rmd = __builtin_amdgcn_readlane(v[1], ((2047 - (int)(best & 0xffffu)) >> 6) & 31);
          break;
        }
      }
      const int ix = (2047 - (int)(best & 0xffffu)) & 2047;
      if (row == ix) alpha = 0.f;
      xq = __builtin_amdgcn_raw_buffer_load_b64(__builtin_amdgcn_make_buffer_rsrc(const_cast<u16*>(A), 0, S_ * D_ * 2, 0x00020000), (ix * D_ + hw * 256 + (lane & 3) * 64 + (lane >> 2) * 4) * 2, 0, 16);
      PIN();
    }
    tag = __builtin_amdgcn_readfirstlane(tag);
    wsum = __uint_as_float(__builtin_amdgcn_readfirstlane(__float_as_uint(wsum)));
    const int lane = fresh_lane(), t = hw * 64 + lane;
    {
      unsigned a0, a1, a2;
      split3(-m, a0, a1, a2);
      u32x4 ta = {a0 | (a0 << 16), a1 | (a0 << 16), a1 | (a2 << 16), 0u};
      asm volatile("" : "+v"(tw), "+v"(ta), "+v"(alpha));
      __builtin_amdgcn_s_setprio(TPRIO);
      if (nb < lim) {
        while (nb < lim) tick();
        __builtin_amdgcn_s_sleep(TSLP);
      }
      if (!LAY2 && hw < 3) *LDS_AT(u32x4, lbase + TABW + (hw * 64 + (lane & 15) * 4 + (lane >> 4)) * 16) = tw;
      if (t < 128) {
        *LDS_AT(u32x4, lbase + TABA + t * 16) = ta;
        *LDS_AT(float, lbase + ALPHA + t * 4) = alpha;
      }
    }
    if (t >= 192 && t < 196) *LDS_AT(u32x4, lbase + CRED + ((t >> 1) & 1) * 512 + (t & 1) * 128) = u32x4{0u, 0u, 0u, 0u};
    asm volatile("s_waitcnt lgkmcnt(0)" ::: "memory");
    PIN();
    __builtin_amdgcn_s_barrier();
    PIN();
    *LDS_AT(u32x4, lbase + XML + t * 16) = u32x4{SENT, SENT, SENT, SENT};
    *LDS_AT(u32x4, lbase + SPM + (t & 127) * 16) = u32x4{SENT, SENT, SENT, SENT};
    if (hw == 0) {
      unsigned v;
      asm volatile("v_mov_b32 %0, 0\n\tv_writelane_b32 %0, 2, 0\n\tv_writelane_b32 %0, %1, 5\n\tv_writelane_b32 %0, %2, 7"
                   : "=&v"(v) : "s"(tag), "s"(best));
      lds_wr(lbase + FLG + lane * 4, v);
    }
    const int idx = LAY2 ? (2047 - (int)(best & 0xffffu)) & 2047 : -64;
    const bool p1blk = (oni & 7) != 7, fast = tmi < 8, own = fast && (loc & 1);
    const int fg = xcd * 28 + (p1blk ? loc - (loc >> 3) : 0), ow = (xcd & 3) * 16 + (loc >> 1);
    static_assert(OMX == 4, "oni % 8 = loc % 8");
#define GLD(a, b) "global_load_dwordx4 v[" #a ":" #b "], %0, off\n\tv_lshl_add_u64 %0, %0, 0, %1\n\t"
#define GLD4(...) asm volatile(__VA_ARGS__ : "+v"(p) : "s"((unsigned long long)(F_ * 2)), KEPT() : "memory")
    auto gptr = [&](const u16* wg, const u16* wu) {
      const int ln = fresh_lane();
      return ((ln & 2) ? wu : wg) + (size_t)(hw * 256 + (ln >> 2) * 4) * F_ + fg * 16 + (ln & 1) * 8;
    };
    auto gather = [&](const u16*& p, int e) __attribute__((always_inline)) {
      if (e == 0) GLD4(GLD(104, 107) GLD(108, 111) GLD(112, 115) GLD(116, 119));
      else if (e == 1) GLD4(GLD(120, 123) GLD(124, 127) GLD(128, 131) GLD(132, 135));
      else if (e == 2) GLD4(GLD(136, 139) GLD(140, 143) GLD(144, 147) GLD(148, 151));
      else GLD4(GLD(152, 155) GLD(156, 159) GLD(160, 163) GLD(164, 167));
      p += 60 * F_;
    };
    auto dmask = [&](int q) { const int ln = fresh_lane(); return ((ln >> 1) & 1) == q ? ((ln & 1) ? 0xffff0000u : 0x0000ffffu) : 0u; };
    unsigned dm0, dm1, dad;
    auto gu_setup = [&]() __attribute__((always_inline)) {
      const int ln = fresh_lane();
      dm0 = dmask(0); dm1 = dmask(1);
      dad = lbase + SACT + ((ln & 3) * 8 + (ln >> 5) * 4 + ((ln >> 4) & 1)) * 16 + hw * 4;
      asm volatile("" : "+v"(dm0), "+v"(dm1), "+v"(dad));
    };
    auto gate_up = [&](u32x2 x, unsigned md) __attribute__((always_inline)) {
      const unsigned rin = __builtin_amdgcn_readfirstlane(__float_as_uint(frcp(__uint_as_float(md & 0xffff0000u))));
      asm volatile("s_getpc_b64 s[90:91]\n\ts_add_u32 s90, s90, gate_up_code@rel32@lo+4\n\ts_addc_u32 s91, s91, gate_up_code@rel32@hi+12\n\ts_swappc_b64 s[92:93], s[90:91]"
                   : "+{v[100:101]}"(x) : "{s84}"(md << 16), "{s85}"(md & 0xffff0000u), "{s86}"(rin), "{v99}"(dad), "{v102}"(dm0), "{v103}"(dm1), KEPT()
                   : "v84", "v85", "v86", "v87", "v88", "v89", "v90", "v91", "v92", "v93", "v94", "v95", "v96", "v97", "s88", "s89", "s90", "s91", "s92", "s93", "scc", "memory");
    };
    auto act_diag = [&](int so = 0) __attribute__((always_inline)) {
      const int ln = fresh_lane();
      const f32x4 g4 = *LDS_AT(const volatile f32x4, lbase + SACT + so + (ln >> 2) * 16), u4 = *LDS_AT(const volatile f32x4, lbase + SACT + so + 256 + (ln >> 2) * 16);
      float gv = fadd(fadd(g4[0], g4[1]), fadd(g4[2], g4[3])), uv = fadd(fadd(u4[0], u4[1]), fadd(u4[2], u4[3]));
      rbf2(gv, uv);
      const float av = rbf(gv * frcp(1.0f + __expf(-gv))) * uv;
      const unsigned ap2 = pack2(av, av);
      return __builtin_bit_cast(s16x4, u32x2{ap2 & dm0, ap2 & dm1});
    };
    auto down = [&](const s16x4& a, const u32x2 (&w)[16]) __attribute__((always_inline)) {
      f32x4 d[2] = {{0.f, 0.f, 0.f, 0.f}, {0.f, 0.f, 0.f, 0.f}};
      static_for<0, 16>([&](auto fc) __attribute__((always_inline)) {
        constexpr int f = decltype(fc)::value;
        d[f & 1] = __builtin_amdgcn_mfma_f32_4x4x4bf16_1k(a, __builtin_bit_cast(s16x4, w[f]), d[f & 1], 4, f, 0);
      });
      f32x4 z;
#pragma unroll
      for (int e = 0; e < 4; ++e) { z[e] = d[0][e] + d[1][e]; asm volatile("" : "+v"(z[e])); }
      return u32x4{f2word(z[0]), f2word(z[1]), f2word(z[2]), f2word(z[3])};
    };
    auto fold8 = [&](f32x2 y0, f32x2 y1) __attribute__((always_inline)) { return dpp_add<0x128, 0xf>(fold16(y0, y1)); };
    auto fold4 = [&](f32x2 y0, f32x2 y1) __attribute__((always_inline)) { return dpp_add<0x124, 0xf>(fold8(y0, y1)); };
    auto y_sum = [&](const __amdgpu_buffer_rsrc_t rs, auto nbc) __attribute__((always_inline)) {
      constexpr int NB = decltype(nbc)::value, N = 14 / NB;
      const int ln = fresh_lane(), src = (GRAN_N + (ln >> 2) * D_ + ow * 16 + (ln & 3) * 4) * 4;
      f32x2 y0 = {0.f, 0.f}, y1 = {0.f, 0.f};
#pragma unroll 1
      for (int bt = 0; bt < NB; ++bt) {
        f32x2 z0, z1;
#pragma unroll 1
        for (int spin = 0; spin < SPIN_MAX; ++spin) {
          u32x4 v[N];
#pragma unroll
          for (int i = 0; i < N; ++i) v[i] = __builtin_amdgcn_raw_buffer_load_b128(rs, src, (bt * N + i) * (16 * D_ * 4), 16);
          z0 = y0; z1 = y1;
#pragma unroll
          for (int i = 0; i < N; ++i) {
            z0 = f32x2{fadd(z0[0], __uint_as_float(v[i][0])), fadd(z0[1], __uint_as_float(v[i][1]))};
            z1 = f32x2{fadd(z1[0], __uint_as_float(v[i][2])), fadd(z1[1], __uint_as_float(v[i][3]))};
          }
          const float chk = fadd(fadd(z0[0], z0[1]), fadd(z1[0], z1[1]));
          if (none(chk != chk)) break;
          unsigned mx = 0u;
#pragma unroll
          for (int i = 0; i < N; ++i) mx = umax(umax(umax(mx, v[i][0]), v[i][1]), umax(v[i][2], v[i][3]));
          if (none(mx == SENT)) break;
          __builtin_amdgcn_s_sleep(YSLP);
          asm volatile("" ::: "memory");
        }
        y0 = z0; y1 = z1;
      }
      return fold4(y0, y1);
    };
    [&]() __attribute__((always_inline)) {
    ARGS_HERE;
    const unsigned l16 = lane * 16;
    unsigned* kvflag = gran + GR_KV + ((LAY2 ? 16 : 0) + tni) * (32 * RK);
    auto wait_sig = [&](int sh) {
#pragma unroll 1
      for (int i = 0; i < 4 * SPIN_MAX; ++i) {
        if (((__builtin_amdgcn_readfirstlane(*LDS_AT(const volatile unsigned, lbase + FLG + 32)) >> sh) & 7u) >= 4u) break;
        __builtin_amdgcn_s_sleep(SSLP);
      }
      asm volatile("" ::: "memory");
      PIN();
    };
    PIN();
    asm volatile("s_waitcnt lgkmcnt(0)" ::: "memory");
    if (!LAY2) asm volatile("s_waitcnt vmcnt(0)" ::: "memory");
    __builtin_amdgcn_s_barrier();
    PIN();
    __builtin_amdgcn_s_setprio(HPRIO);
    if (hw < 3) {
      __builtin_amdgcn_s_sleep(SPRE);
      wait_sig(0);
      __builtin_amdgcn_raw_buffer_store_b32(tag, __builtin_amdgcn_make_buffer_rsrc(kvflag, 0, hw == 0 ? RK * 128 : 0, 0x00020000), lane * 128 + tmi * 4, 0, 16);
      if (hw == 0) STAMP(LAY2 ? 5 : 1, 16);
    }
    if (hw == 3) {
      __builtin_amdgcn_s_sleep(KVS);
      const unsigned kv0 = ld_ag(kvflag + (tmi % RK) * 32 + (lane & 15));
      PIN();
      const char* cnt = reinterpret_cast<const char*>(gran + GR_N + bid * 64 + 2);
      unsigned vk[8], vp = lbase + FLG + 8 + (lane & 7), pw = 0u;
#pragma unroll
      for (int j = 0; j < 8; ++j) { vk[j] = l16 + (j >> 2) * 131072u + ((j >> 1) & 1) * 4096u + (j & 1) * (VOFF - 2048u); asm volatile("" : "+v"(vk[j])); }
#define G4(a, b, c, d, sc) "s_nop 0\n\tglobal_load_lds_dwordx4 %" #a ", %8" sc "\n\tglobal_load_lds_dwordx4 %" #b ", %8 offset:1024" sc "\n\t"             \
                       "global_load_lds_dwordx4 %" #c ", %8 offset:2048" sc "\n\tglobal_load_lds_dwordx4 %" #d ", %8 offset:3072" sc "\n\t"
#define M4 "s_addk_i32 m0, 0x1000\n\t"
      auto stage = [&](const char* sp, unsigned dst) __attribute__((always_inline)) {
        asm volatile("s_mov_b32 m0, %9\n\t" G4(0, 0, 1, 1, " sc1") M4 G4(2, 2, 3, 3, " sc1") M4 G4(4, 4, 5, 5, " sc1") M4 G4(6, 6, 7, 7, " sc1")
                     :: "v"(vk[0]), "v"(vk[1]), "v"(vk[2]), "v"(vk[3]), "v"(vk[4]), "v"(vk[5]), "v"(vk[6]), "v"(vk[7]), "s"(sp), "s"(dst) : "m0", "scc", "memory");
      };
      auto count = [&]() __attribute__((always_inline)) {
        asm volatile("s_mov_b32 m0, %2\n\ts_nop 0\n\tglobal_load_lds_dword %0, %1 sc1" :: "v"(0u), "s"(cnt), "s"(lbase + FLG - 252) : "m0", "memory");
        cnt += 8;
      };
      auto freed = [&](unsigned n) __attribute__((always_inline)) {
        if (!none(pw < n)) {
#pragma unroll 1
          for (int i = 0; i < 4 * SPIN_MAX; ++i) {
            pw = *LDS_AT(const volatile unsigned char, vp);
            if (none(pw < n)) break;
            __builtin_amdgcn_s_sleep(W3SLP);
          }
        }
        asm volatile("" ::: "memory");
      };
      unsigned long long ord = (0xfedcba9876543210ull >> 4 & ~((1ull << 4 * (tmi & 7)) - 1ull)) | (0xfedcba9876543210ull & ((1ull << 4 * (tmi & 7)) - 1ull));
      ord ^= (tmi & 8) * 0x1111111111111111ull;
      unsigned seen;
      const char* kvb = reinterpret_cast<const char*>(KV) + (size_t)tni * (128 * 2048);
      auto panel = [&]() __attribute__((always_inline)) {
        const unsigned pn = (unsigned)ord & 15u;
        ord >>= 4;
        if (__builtin_expect(!((seen >> pn) & 1u), 0)) {
#pragma unroll 1
          for (int i = 0; i < SPIN_MAX && !((seen >> pn) & 1u); ++i) seen = (unsigned)__ballot(ld_ag(kvflag + (tmi % RK) * 32 + (lane & 15)) == tag);
        }
        asm volatile("" ::: "memory");
        return kvb + pn * 8192u;
      };
      asm volatile("" : "+s"(ord), "+s"(kvb), "+s"(cnt));
      asm volatile("" : "+v"(vp), "+v"(pw));
      PIN();
      seen = (unsigned)__ballot(kv0 == tag);
      const char* sp = panel();
      wait_sig(16);
      stage(sp, lbase + 2 * ASB);
      count();
      const int ksp = LAY2 ? PNS / 2 : -1, ksg = LAY2 ? 64 : GF0;
      const u16* gp = gptr(wg, wu);
#pragma unroll 1
      for (int k = 1; k < 15; ++k) {
        if (__builtin_expect(k == ksp, 0)) {
          gu_setup();
          gate_up(xq, rmd);
          PIN();
          *LDS_AT(volatile unsigned char, lbase + FLG + 7) = (unsigned char)0x7f;
        }
        freed((unsigned)(0xba9876543211000ull >> 4 * k) & 15u);
        stage(sp + 2048, lbase + ((2 * k + 1) & 7) * ASB);
        sp = panel();
        stage(sp, lbase + ((2 * k + 2) & 7) * ASB);
        count();
        if (__builtin_expect((unsigned)(k - ksg) < 4u, 0)) gather(gp, k - ksg);
      }
      freed(12u);
      glds16s<true>(csf + 4096 + tni * 128, l16, lbase + UGL);
      stage(sp + 2048, lbase + 7 * ASB);
      count();
      const unsigned vwo = (unsigned)((lane >> 3) * D_ + oni * 64 + ((lane & 7) ^ ((lane >> 3) & 6)) * 8) * 2u;
      constexpr unsigned vs = 8u * D_ * 2u - 1024u;
#pragma unroll 1
      for (int ns = 0; ns < 6; ++ns) {
        freed((unsigned)(ns + 25) >> 1);
        const char* wp = reinterpret_cast<const char*>(wo) + (size_t)ns * (128 * D_ * 2);
#pragma unroll
        for (int j = 0; j < 4; ++j)
          glds16x4<false>(wp + j * (32u * D_ * 2u), vwo, vwo + vs, vwo + 2 * vs, vwo + 3 * vs, lbase + ns * ASB + j * 4096);
      }
      asm volatile("s_waitcnt vmcnt(0)" ::: "memory");
      return;
    }
    if (!LAY2) {
      auto until = [&](unsigned n) {
        while ((__builtin_amdgcn_readfirstlane(*LDS_AT(const volatile unsigned, lbase + FLG + 8)) & 255u) < n) __builtin_amdgcn_s_sleep(16);
        asm volatile("" ::: "memory");
        PIN();
      };
      if (hw != 1) {
        constexpr int N2 = HAND_N + Y_N + 3 * D_ / 2;
        const __amdgpu_buffer_rsrc_t rsi = __builtin_amdgcn_make_buffer_rsrc(hand + (hw ? GRAN_N : 2 * HAND_N), 0, (hw ? N2 : PS_N) * 4, 0x00020000);
        until(IAT);
#pragma unroll 1
        for (int i = 0; i < ((hw ? N2 : PS_N) / 4 + 16383) / 16384; ++i) {
          __builtin_amdgcn_raw_buffer_store_b128(u32x4{SENT, SENT, SENT, SENT}, rsi, ((i * 256 + bid) * 64 + lane) * 16, 0, 16);
          __builtin_amdgcn_s_sleep(ISLP);
        }
      }
      const int ng = loc & 15, fc = xcd * 2 + (loc >> 4);
      const size_t og = (size_t)((fg - xcd * 28) / 7 * 256 + lane) * F_ + (xcd * 7 + fg % 7) * 64;
      const u16* pd = wd + (size_t)(fc * 224 + (lane < 56 ? lane : 0)) * D_ + ng * 64;
      const u16* p = gptr(wg, wu);
#pragma unroll 1
      for (unsigned n = T0; n < T0 + 13u; ++n) {
        until(n);
        if (hw == 1) {
          const unsigned i = n - T0;
          const u16* q = i < 8u ? ((i & 1u) ? wu : wg) + og + (size_t)(i >> 1) * (64 * F_) : pd + (size_t)((i - 8u - (i > TW)) & 3u) * (56 * D_);
          if (i == TW) {
            q = wo + (size_t)(768 + (loc / (32 / OMX)) * (256 / OMX) + (lane & (256 / OMX - 1) & 63)) * D_ + oni * 64;
            unsigned long long a0, a1;
            CODE_ADDR("layers_kernel", a0); CODE_ADDR("end_marker_kernel", a1);
            if (loc < 8) { const unsigned p1 = touch_code(a0, a1, 3 + loc, lane); asm volatile("" :: "v"(p1)); }
          }
          const unsigned p0 = touch(q);
          asm volatile("" :: "v"(p0));
        }
        if (n >= G0 && (n - G0) % GS == 0u && (n - G0) / GS < 4u) gather(p, (n - G0) / GS);
        if (n == UAT && hw == UHW) {
          u32x4 a[4][2], c[2];
#pragma unroll
          for (int i = 0; i < 4; ++i)
#pragma unroll
            for (int h = 0; h < 2; ++h) a[i][h] = *reinterpret_cast<const u32x4*>(wo + (size_t)(tni * 64 + tmi * 4 + i) * D_ + h * 512 + lane * 8);
#pragma unroll
          for (int h = 0; h < 2; ++h) c[h] = *reinterpret_cast<const u32x4*>(w_r + h * 512 + lane * 8);
          auto dot = [&](unsigned x, unsigned y, float z) { return __builtin_amdgcn_fdot2_f32_bf16(__builtin_bit_cast(bf16x2, x), __builtin_bit_cast(bf16x2, y), z, false); };
          PIN();
          __builtin_amdgcn_s_setprio(0);
#pragma unroll 1
          for (int i = 0; i < SPIN_MAX; ++i) {
            if (__builtin_amdgcn_readfirstlane(*LDS_AT(const volatile unsigned, lbase + FLG + 16)) >= UARR) break;
            __builtin_amdgcn_s_sleep(4);
          }
          PIN();
          float wm = 0.f;
#pragma unroll
          for (int e = 0; e < 8; ++e) wm = dot(c[e >> 2][e & 3], 0x3f803f80u, wm);
          wm = wave_sum(wm);
          lds_wr(lbase + FLG + 60, __float_as_uint(wm));
          wm *= -1.0f / 1024.0f;
          f32x2 u[2], g[2];
#pragma unroll
          for (int i = 0; i < 4; ++i) {
            float p1 = 0.f, pw = 0.f, p2 = 0.f;
#pragma unroll
            for (int e = 0; e < 8; ++e) {
              const unsigned x = a[i][e >> 2][e & 3];
              p1 = dot(x, 0x3f803f80u, p1); pw = dot(x, c[e >> 2][e & 3], pw); p2 = dot(x, x, p2);
            }
            u[i >> 1][i & 1] = fmaf(p1, wm, pw); g[i >> 1][i & 1] = p2;
          }
          float us = fold4(u[0], u[1]), gs = fold4(g[0], g[1]);
          us = dpp_add<0x121, 0xf>(dpp_add<0x122, 0xf>(us)); gs = dpp_add<0x121, 0xf>(dpp_add<0x122, 0xf>(gs));
          __builtin_amdgcn_raw_buffer_store_b32(__float_as_uint((lane & 1) ? __builtin_amdgcn_sqrtf(gs) * (1.0f / 32.0f) : us),
              __builtin_amdgcn_make_buffer_rsrc(csf + 4096 + tni * 128, 0, 512, 0x00020000), (lane & 14) ? 512 : ((lane & 1) * 64 + tmi * 4 + (lane >> 4)) * 4, 0, 16);
        }
      }
      if (hw == 2 && bid == 0) {
        const __amdgpu_buffer_rsrc_t rsc = __builtin_amdgcn_make_buffer_rsrc(gran, 0, GR_N * 4, 0x00020000);
#pragma unroll 1
        for (int spin = 0; spin < SPIN_MAX; ++spin) {
          bool ok = true;
#pragma unroll
          for (int i = 0; i < 4; ++i) ok &= __builtin_amdgcn_raw_buffer_load_b32(rsc, (GR_ROW + (i * 64 + lane) * 32 + 1) * 4, 0, 16) == tag;
          if (__all(ok)) break;
          __builtin_amdgcn_s_sleep(8);
          asm volatile("" ::: "memory");
        }
        if (lane == 0) st_ag(gran, tag);
      }
      return;
    }
    unsigned* X2 = hand + GRAN_N + Y_N;
    unsigned* XS = X2 + X2_N;
    unsigned* P4 = XS + XS_N;
    unsigned* PA = P4 + P4_N;
    const __amdgpu_buffer_rsrc_t rsh = __builtin_amdgcn_make_buffer_rsrc(hand, 0, HAND_N * 4, 0x00020000);
    auto signal = [&](unsigned ph) {
      asm volatile("" ::: "memory");
      PIN();
      *LDS_AT(volatile unsigned char, lbase + FLG + 4 + hw) = (unsigned char)ph;
    };
    auto wait_all = [&](unsigned ph) {
#pragma unroll 1
      for (int i = 0; i < SPIN_MAX; ++i) {
        if (all_ge(__builtin_amdgcn_readfirstlane(*LDS_AT(const volatile unsigned, lbase + FLG + 4)), ph)) break;
        __builtin_amdgcn_s_sleep(WSLP);
      }
      asm volatile("" ::: "memory");
      PIN();
    };
    gu_setup();
    gate_up(xq, rmd);
    signal(hw == 2 ? 2u : 0x7fu);
    if (wid == 8) STAMP(dk, 2);
    if (hw < 2) {
#pragma unroll 1
      for (int i = 0; i < SPIN_MAX; ++i) {
        if (__builtin_amdgcn_readfirstlane(*LDS_AT(const volatile unsigned, lbase + FLG)) >= FSTART) break;
        __builtin_amdgcn_s_sleep(WSLP);
      }
      asm volatile("" ::: "memory");
      PIN();
      if (wid == 8) STAMP(dk, 10);
      u32x2 w[16];
      const __amdgpu_buffer_rsrc_t rd_ = __builtin_amdgcn_make_buffer_rsrc(const_cast<u16*>(wd), 0, D_ * F_ * 2, 0x00020000);
      auto ldw = [&](int c) __attribute__((always_inline)) {
#pragma unroll
        for (int f = 0; f < 16; ++f) w[f] = __builtin_amdgcn_raw_buffer_load_b64(rd_, lane * 8 + c * 512 + (f & 1) * 2048, (fg * 16 + (f & ~1)) * (D_ * 2) + hw * 1024, 0);
        PIN();
      };
      ldw(0);
      const unsigned pt = __builtin_amdgcn_raw_buffer_load_b32(rd_, (lane >> 2) * (D_ * 2) + (lane & 3) * 128 + 512, fg * 16 * (D_ * 2) + hw * 1024, 0);
      int yo = p1blk ? (GRAN_N + fg * D_ + hw * 512) * 4 + lane * 16 : HAND_N * 4;
      asm volatile("" : "+v"(yo));
      wait_all(2u);
      if (wid == 8) STAMP(dk, 3);
      const s16x4 a = act_diag();
      const u32x4 y0 = down(a, w);
      PIN();
      ldw(1);
      __builtin_amdgcn_raw_buffer_store_b128(y0, rsh, yo, 0, 16);
      PIN();
      __builtin_amdgcn_raw_buffer_store_b128(down(a, w), rsh, yo + 1024, 0, 16);
      asm volatile("" :: "v"(pt));
      if (wid == 8) STAMP(dk, 4);
    }
    PIN();
    [&]() __attribute__((always_inline)) {
    const int lane = fresh_lane();
    const float rmb = __uint_as_float(rmd << 16);
    auto first_words = [&](int src, auto nc) __attribute__((always_inline)) {
#pragma unroll 1
      for (int spin = 0; spin < SPIN_MAX; ++spin) {
        unsigned mx = 0u;
#pragma unroll
        for (int i = 0; i < decltype(nc)::value; ++i) mx = umax(mx, __builtin_amdgcn_raw_buffer_load_b32(rsh, src, i * (56 * D_ * 4), 16));
        if (none(mx == SENT)) break;
        __builtin_amdgcn_s_sleep(PSLP);
        asm volatile("" ::: "memory");
      }
    };
    if (hw == 2) {
      wait_all(2u);
      if (own) {
        const float rden = __uint_as_float(rmd & 0xffff0000u), rrin = frcp(rden);
        const int col = ow * 16 + (lane & 3) * 4 + (lane >> 4);
        const unsigned xo2 = ld_ag(reinterpret_cast<const unsigned*>(A + (size_t)idx * D_ + (col & ~1)));
        __builtin_amdgcn_s_sleep(PRE1);
        first_words((GRAN_N + (lane < 56 ? lane : 0) * D_ + ow * 16) * 4, std::integral_constant<int, 4>{});
        const float y = y_sum(rsh, std::integral_constant<int, 2>{});
        const unsigned rk = best >> 16;
        const float rv = __uint_as_float(((rk & 0x8000u) ? (rk & 0x7fffu) : (~rk & 0xffffu)) << 16);
        const float rs = rbf(frcp(1.0f + __expf(-rv)));
        const float x1v = __uint_as_float((col & 1) ? xo2 & 0xffff0000u : xo2 << 16);
        float h0 = x1v, h1 = x1v;
        ln_apply2(h0, h1, rmb, rden, rrin);
        const float x2v = rbf(x1v + rbf(h0 + rbf(rbf(y) * rs)));
        const auto pr = __builtin_amdgcn_permlane16_swap(__float_as_uint(x2v), __float_as_uint(x2v), false, false);
        __builtin_amdgcn_raw_buffer_store_b32(f2word(__uint_as_float((pr[0] >> 16) | pr[1])), rsh, (lane & 28) ? HAND_N * 4 : (int)(X2 - hand) * 4 + col * 2, 0, 16);
        const float d = x2v - rmb;
        const float sd = wave_sum(d), sq = wave_sum(d * d);
        if (lane < 2) st_ag(XS + ow * 2 + lane, f2word(lane ? sq : sd));
        STAMP(dk, 5);
      }
    }
    {
      const int ks = kcap ^ (tmi & 8), g4 = lane >> 4;
      const int src = (int)(X2 - hand) * 4 + ks * 128 + g4 * 16;
      int dst = (lane & 12) ? HAND_N * 4 : (int)((P4 - hand) + (tni * 16 + ks) * 192 + hw * 64 + (lane & 2) * 16 + (lane & 1) * 8 + (g4 >> 1) * 16 + (g4 & 1) * 4) * 4;
      asm volatile("" : "+v"(dst));
      u32x4 x0, x1;
      if (!(hw == 2 && own)) __builtin_amdgcn_s_sleep(PRE4);
#pragma unroll 1
      for (int spin = 0; spin < SPIN_MAX; ++spin) {
        x0 = __builtin_amdgcn_raw_buffer_load_b128(rsh, src, 0, 16); x1 = __builtin_amdgcn_raw_buffer_load_b128(rsh, src + 64, 0, 16);
        if (none(umax(umax(umax(x0[0], x0[1]), umax(x0[2], x0[3])), umax(umax(x1[0], x1[1]), umax(x1[2], x1[3]))) == SENT)) break;
        __builtin_amdgcn_s_sleep(PSLP);
        asm volatile("" ::: "memory");
      }
      f32x4 d[4];
#pragma unroll
      for (int i = 0; i < 4; ++i) d[i] = MFMA16(sl[2 * i], __builtin_bit_cast(s16x8, x0), (f32x4{0.f, 0.f, 0.f, 0.f}));
#pragma unroll
      for (int i = 0; i < 4; ++i) d[i] = MFMA16(sl[2 * i + 1], __builtin_bit_cast(s16x8, x1), d[i]);
      u32x4 o;
#pragma unroll
      for (int e = 0; e < 4; ++e) o[e] = f2word((lane & 2) ? ((lane & 1) ? d[3][e] : d[2][e]) : ((lane & 1) ? d[1][e] : d[0][e]));
      __builtin_amdgcn_raw_buffer_store_b128(o, rsh, dst, 0, 16);
      PIN();
      if (wid == 8) STAMP(dk, 6);
    }
    if (hw == 2) {
      const int col = (lane & 15) * 4 + (lane >> 4), src = (int)((P4 - hand) + (tni * 16 + (lane >> 4)) * 192 + (lane & 15) * 4) * 4;
      const __amdgpu_buffer_rsrc_t rsq = __builtin_amdgcn_make_buffer_rsrc(hand, 0, fast ? HAND_N * 4 : 0, 0x00020000);
      float cs[3], val[3];
#pragma unroll
      for (int p = 0; p < 3; ++p) cs[p] = __uint_as_float(ld_ag(reinterpret_cast<const unsigned*>(csf) + p * 1024 + n0 + col));
      u32x2 xs;
      __builtin_amdgcn_s_sleep(PRE7);
      first_words((int)((P4 - hand) + (tni * 16 + (lane & 15)) * 192 + ((lane >> 4) % 3) * 64) * 4, std::integral_constant<int, 1>{});
#pragma unroll 1
      for (int spin = 0; spin < SPIN_MAX; ++spin) {
        u32x4 v[3][4];
        xs = __builtin_amdgcn_raw_buffer_load_b64(rsh, (int)((XS - hand) + lane * 2) * 4, 0, 16);
#pragma unroll
        for (int p = 2; p >= 0; --p)
#pragma unroll
          for (int i = 0; i < 4; ++i) v[p][i] = __builtin_amdgcn_raw_buffer_load_b128(p ? rsh : rsq, src + p * 256, i * (4 * 192 * 4), 16);
        auto part = [&](int p) __attribute__((always_inline)) {
          float z[4];
#pragma unroll
          for (int e = 0; e < 4; ++e) {
            z[e] = fadd(fadd(__uint_as_float(v[p][0][e]), __uint_as_float(v[p][1][e])), fadd(__uint_as_float(v[p][2][e]), __uint_as_float(v[p][3][e])));
          }
          val[p] = fold16(f32x2{z[0], z[1]}, f32x2{z[2], z[3]});
        };
        part(2); part(1);
        val[0] = 0.f;
        if (fast) part(0);
        const float chk = fadd(fadd(fadd(val[0], val[1]), fadd(val[2], __uint_as_float(xs[0]))), __uint_as_float(xs[1]));
        if (none(chk != chk)) break;
        unsigned mx = umax(xs[0], xs[1]);
#pragma unroll
        for (int i = 0; i < 12; ++i) mx = umax(umax(umax(mx, v[i >> 2][i & 3][0]), v[i >> 2][i & 3][1]), umax(v[i >> 2][i & 3][2], v[i >> 2][i & 3][3]));
        if (none(mx == SENT)) break;
        __builtin_amdgcn_s_sleep(PSLP);
        asm volatile("" ::: "memory");
      }
      const auto sw = __builtin_amdgcn_permlane32_swap(xs[0], xs[1], false, false);
      float sm = __uint_as_float(sw[0]) + __uint_as_float(sw[1]);
      sm = dpp_add<0x142, 0xa>(dpp_add<0x118, 0xf>(dpp_add<0x114, 0xf>(dpp_add<0x112, 0xf>(dpp_add<0x111, 0xf>(sm)))));
      const float dm = __int_as_float(__builtin_amdgcn_readlane(__float_as_int(sm), 31)) * (0.25f / 1024.0f);
      const float var = fmaxf(__int_as_float(__builtin_amdgcn_readlane(__float_as_int(sm), 63)) * (0.25f / 1024.0f) - dm * dm, 0.f);
      const float mb = rbf(rmb + dm);
      const float rin = frcp(rbf(__builtin_amdgcn_sqrtf(rbf(rbf(var) + 1e-5f))));
      const unsigned kv = pack2((val[1] - mb * cs[1]) * rin, (val[2] - mb * cs[2]) * rin);
      *LDS_AT(u16, lbase + TAB + 128 + kpos(col) * 2) = (u16)kv;
      *LDS_AT(u16, lbase + TAB + 256 + col * 2) = (u16)(kv >> 16);
      if (fast) *LDS_AT(u16, lbase + TAB + kpos(col) * 2) = f2bf((val[0] - mb * cs[0]) * rin * QSCALE);
      signal(7u);
      STAMP(dk, 7);
      if (own) {
#pragma unroll
        for (int i = 0; i < 14; ++i)
          __builtin_amdgcn_raw_buffer_store_b128(u32x4{SENT, SENT, SENT, SENT}, rsh, (GRAN_N + (i * 16 + (lane >> 2)) * D_ + ow * 16 + (lane & 3) * 4) * 4, 0, 16);
      }
      return;
    }
    if (!fast) return;
    const int head = tni * 2 + hw;
    const unsigned tb = lbase + TAB + hw * 64;
    {
      const __amdgpu_buffer_rsrc_t rs = __builtin_amdgcn_make_buffer_rsrc(KV, 0, 2 * S_ * D_ * 2, 0x00020000);
      const int src = (head * 64 + tmi * 4) * 2048 + lane * 16;
      auto ld = [&](int i, unsigned off) { return __builtin_bit_cast(s16x8, __builtin_amdgcn_raw_buffer_load_b128(rs, src + (i & 3) * 1024, (i >> 3) * 65536 + ((i >> 2) & 1) * 4096 + off, 16)); };
#pragma unroll 1
      for (int i = 0; i < SPIN_MAX; ++i)
        if (__builtin_amdgcn_readfirstlane(ld_ag(kvflag + (tmi % RK) * 32 + tmi + 8)) == tag) break;
      asm volatile("" ::: "memory");
      s16x8 f[16];
#pragma unroll
      for (int i = 0; i < 16; ++i) f[i] = ld(i, 0u);
      PIN();
      const bool on = ((lane ^ (lane >> 4)) & 1) == 0;
      const unsigned pl = lbase + RED + hw * 1024;
      unsigned pw = pl + (lane >> 4) * 64 + (lane & 15) * 2, pr = pl + (on ? (lane >> 5) * 16 : 512), qr = on ? tb + (lane >> 5) * 16 : pl + 512;
      const int gone = (((idx >> 7) & 7) == tmi) ? ((idx >> 10) * 2 + (kpos(idx & 31) >> 4)) * 64 + ((idx >> 5) & 3) * 16 + (kpos(idx & 31) & 15) : -1;
      asm volatile("" : "+v"(pw), "+v"(pr), "+v"(qr));
      *LDS_AT(u32x2, pl + 512 + lane * 8) = u32x2{0u, 0u};
      wait_all(7u);
      u32x4 q[2];
#pragma unroll
      for (int sx = 0; sx < 2; ++sx) {
        q[sx] = *LDS_AT(const u32x4, qr + sx * 32);
      }
      float sc[4];
#pragma unroll
      for (int i = 0; i < 8; ++i) {
        f32x4 d = MFMA16(__builtin_bit_cast(s16x8, q[0]), f[2 * i], (f32x4{0.f, 0.f, 0.f, 0.f}));
        d = MFMA16(__builtin_bit_cast(s16x8, q[1]), f[2 * i + 1], d);
        PIN();
        f[2 * i] = ld(2 * i, VOFF); f[2 * i + 1] = ld(2 * i + 1, VOFF);
        PIN();
#pragma unroll
        for (int r = 0; r < 2; ++r) sc[(i >> 2) * 2 + r] = (i & 3) == 0 || (lane >> 4) == (i & 3) ? d[r] : sc[(i >> 2) * 2 + r];
      }
      if (gone >= 0) {
#pragma unroll
        for (int r = 0; r < 4; ++r) sc[r] = r * 64 + lane == gone ? -INFINITY : sc[r];
      }
      const float mx = wave_fmax(fmaxf(fmaxf(sc[0], sc[1]), fmaxf(sc[2], sc[3])));
      const unsigned p0 = pack2(__builtin_amdgcn_exp2f(sc[0] - mx), __builtin_amdgcn_exp2f(sc[1] - mx)), p1 = pack2(__builtin_amdgcn_exp2f(sc[2] - mx), __builtin_amdgcn_exp2f(sc[3] - mx));
      *LDS_AT(u16, pw) = (u16)p0; *LDS_AT(u16, pw + 32) = (u16)(p0 >> 16);
      *LDS_AT(u16, pw + 256) = (u16)p1; *LDS_AT(u16, pw + 288) = (u16)(p1 >> 16);
      asm volatile("" ::: "memory");
      const float ls = wave_sum((__uint_as_float(p0 << 16) + __uint_as_float(p0 & 0xffff0000u)) + (__uint_as_float(p1 << 16) + __uint_as_float(p1 & 0xffff0000u)));
      f32x4 o = {0.f, 0.f, 0.f, 0.f};
#pragma unroll
      for (int i = 0; i < 16; ++i) o = MFMA16(*LDS_AT(const s16x8, pr + i * 32), f[i], o);
      int pv = __float_as_int((lane & 16) ? o[1] : o[0]);
      asm volatile("s_nop 0\n\tv_writelane_b32 %0, %1, 32\n\tv_writelane_b32 %0, %2, 33" : "+v"(pv) : "s"(ls), "s"(mx));
      __builtin_amdgcn_raw_buffer_store_b32(f2word(__int_as_float(pv)), rsh, lane < 34 ? (int)((PA - hand) + (head * 8 + tmi) * 64 + lane) * 4 : HAND_N * 4, 0, 16);
    }
    if (wid == 8) STAMP(dk, 8);
    if (tmi != ((idx >> 7) & 7)) return;
    {
      const int src = (int)((PA - hand) + head * 512 + (lane < 34 ? lane : 0)) * 4;
      const unsigned ta = tb + (lane & 31) * 2;
      const float so = wave_sum(lane < 32 ? bf2f(*LDS_AT(const u16, ta)) * bf2f(*LDS_AT(const u16, ta + 128)) : 0.f);
      const float vo = lane < 32 ? bf2f(*LDS_AT(const u16, ta + 256)) : 1.0f;
      unsigned v[8];
      __builtin_amdgcn_s_sleep(PRE5);
#pragma unroll 1
      for (int spin = 0; spin < SPIN_MAX; ++spin) {
        unsigned mx = 0u;
#pragma unroll
        for (int i = 0; i < 8; ++i) v[i] = __builtin_amdgcn_raw_buffer_load_b32(rsh, src + i * 256, 0, 16);
#pragma unroll
        for (int i = 0; i < 8; ++i) mx = umax(mx, v[i]);
        if (none(mx == SENT)) break;
        __builtin_amdgcn_s_sleep(PSLP);
        asm volatile("" ::: "memory");
      }
      float m8[8], mx = so;
#pragma unroll
      for (int i = 0; i < 8; ++i) { m8[i] = __int_as_float(__builtin_amdgcn_readlane((int)v[i], 33)); mx = fmaxf(mx, m8[i]); }
      float acc = __builtin_amdgcn_exp2f(so - mx) * vo;
#pragma unroll
      for (int i = 0; i < 8; ++i) acc += __builtin_amdgcn_exp2f(m8[i] - mx) * __uint_as_float(v[i]);
      const float l = __int_as_float(__builtin_amdgcn_readlane(__float_as_int(acc), 32));
      const float ov = acc / l;
      const float o1 = __int_as_float(__builtin_amdgcn_update_dpp(0, __float_as_int(ov), 0x55, 0xf, 0xf, false));
      const float o2 = __int_as_float(__builtin_amdgcn_update_dpp(0, __float_as_int(ov), 0xaa, 0xf, 0xf, false));
      const float o3 = __int_as_float(__builtin_amdgcn_update_dpp(0, __float_as_int(ov), 0xff, 0xf, 0xf, false));
      const __amdgpu_buffer_rsrc_t rso = __builtin_amdgcn_make_buffer_rsrc(Of, 0, S_ * D_ * 2, 0x00020000);
      if (lane < 32 && (lane & 3) == 0)
        __builtin_amdgcn_raw_buffer_store_b64(u32x2{pack2(ov, o1), pack2(o2, o3)}, rso,
            (((idx >> 5) * 32 + head) * 1024 + (lane >> 4) * 512 + (((lane >> 2) & 1) * 32 + (idx & 31)) * 8 + ((lane >> 3) & 1) * 4) * 2, 0, 16);
      asm volatile("s_waitcnt vmcnt(0)" ::: "memory");
      if (lane < RO) st_ag(gran + GR_O + ((16 + (idx >> 7)) * RO + lane) * 64 + 16 + head, tag);
      STAMP(dk, 9);
    }
    }();
    }();
    __builtin_amdgcn_s_setprio(0);
    asm volatile("s_waitcnt vmcnt(0) lgkmcnt(0)" ::: "memory");
    PIN();
    ARGS_HERE;
    u32x2 w[16];
    auto ld_wd = [&]() __attribute__((always_inline)) {
      const __amdgpu_buffer_rsrc_t rd_ = __builtin_amdgcn_make_buffer_rsrc(const_cast<u16*>(wd), 0, D_ * F_ * 2, 0x00020000);
#pragma unroll
      for (int f = 0; f < 16; ++f) w[f] = __builtin_amdgcn_raw_buffer_load_b64(rd_, t * 8 + (f & 1) * 2048, (fg * 16 + (f & ~1)) * (D_ * 2), 0);
      PIN();
    };
    float fnum = 0.f, fvar = 0.f;
    const int rrow = omi * 128 + (oni >> 3) * 64 + (hw & 1) * 32;
    if (LAY2) {
      ld_wd();
      if (!p1blk) {
        const int fr = rrow + (lane & 7) * 4 + (lane >> 4);
        const double s1 = ld_ag(stats + fr), s2 = ld_ag(stats + S_ + fr), sw = ld_ag(stats + 2 * S_ + fr);
        lds_wr(lbase + SPM + 132, __float_as_uint(wsum));
        float mean, var, mb, den;
        ln_from_sums(s1, s2, mean, var, mb, den);
        const float g1 = 1.0f + frcp(den);
        fnum = fr == idx ? __uint_as_float(0x7fc00000u) : fmaf(-mean, wsum, (float)sw) * g1;
        fvar = var * g1 * g1;
      }
      asm volatile("" : "+v"(fnum), "+v"(fvar));
      PIN();
    }
    if (hw == 3) {
      const int nfl = (LAY2 && (idx >> 7) == omi) ? 48 : 16;
      auto pfl = (__attribute__((address_space(1))) const unsigned*)(gran + GR_O + (((LAY2 ? 16 : 0) + omi) * RO + oni % RO) * 64 + (lane < nfl ? lane : 0));
      constexpr unsigned vs = 8u * D_ * 2u - 1024u;
      unsigned vw0 = (unsigned)((lane >> 3) * D_ + oni * 64 + ((lane & 7) ^ ((lane >> 3) & 6)) * 8) * 2u, vw1 = vw0 + vs, vw2 = vw0 + 2 * vs, vw3 = vw0 + 3 * vs;
      asm volatile("" : "+v"(pfl), "+v"(vw0), "+v"(vw1), "+v"(vw2), "+v"(vw3));
      auto wait_arr = [&](unsigned n) {
#pragma unroll 1
        for (int i = 0; i < 4 * SPIN_MAX; ++i) {
          if (__builtin_amdgcn_readfirstlane(*LDS_AT(const volatile unsigned, lbase + FLG + 16)) >= n) break;
          __builtin_amdgcn_s_sleep(1);
        }
        asm volatile("" ::: "memory");
        PIN();
      };
      wait_arr(12u);
      unsigned fl;
      asm volatile("global_load_dword %0, %1, off sc1" : "=v"(fl) : "v"(pfl) : "memory");
#pragma unroll 1
      for (int i = 0; i < 4 * SPIN_MAX; ++i) {
        if (__builtin_amdgcn_readfirstlane(*LDS_AT(const volatile unsigned, lbase + FLG + 24)) >= 8u) break;
        __builtin_amdgcn_s_sleep(1);
      }
      asm volatile("" ::: "memory");
      PIN();
#pragma unroll
      for (int j = 24; j < 32; ++j)
        glds16x4<false>(reinterpret_cast<const char*>(wo) + j * (32u * D_ * 2u), vw0, vw1, vw2, vw3, lbase + j * 4096);
      unsigned keep;
      asm volatile("s_mov_b32 %0, m0\n\ts_mov_b32 m0, %3\n\ts_nop 2\n\tglobal_load_lds_dword %1, %2 sc1\n\ts_mov_b32 m0, %0"
                   : "=&s"(keep) : "v"(0u), "s"(reinterpret_cast<const char*>(gran + GR_N + bid * 64 + 39)), "s"(lbase + FLG - 252) : "memory");
      asm volatile("s_waitcnt vmcnt(33)" : "+v"(fl) :: "memory");
      if (__builtin_amdgcn_ballot_w64(fl != tag)) {
        __builtin_amdgcn_s_sleep(CSL1);
#pragma unroll 1
        for (int spin = 0; spin < SPIN_MAX; ++spin) {
          if (!__builtin_amdgcn_ballot_w64(__hip_atomic_load(pfl, __ATOMIC_RELAXED, __HIP_MEMORY_SCOPE_AGENT) != tag)) break;
          __builtin_amdgcn_s_sleep(CSLP);
        }
      }
      asm volatile("" ::: "memory");
      PIN();
    }
    u32x4 pv[4];
    asm volatile("" : "=v"(pv[0]), "=v"(pv[1]), "=v"(pv[2]), "=v"(pv[3]));
    if (LAY2 && !p1blk && hw < 2) {
      const __amdgpu_buffer_rsrc_t rsp = __builtin_amdgcn_make_buffer_rsrc(hand + 2 * HAND_N + PP_O, 0, H_ * S_ * 4, 0x00020000);
      const int src = ((lane >> 3) * S_ + rrow + (lane & 7) * 4) * 4;
#pragma unroll 1
      for (int i = 0; i < 4 * SPIN_MAX; ++i) {
        if (__builtin_amdgcn_readfirstlane(*LDS_AT(const volatile unsigned, lbase + FLG + 24)) >= 8u) break;
        __builtin_amdgcn_s_sleep(2);
      }
#pragma unroll 1
      for (int spin = 0; spin < SPIN_MAX; ++spin) {
        asm volatile("" ::: "memory");
        unsigned mx = 0u;
#pragma unroll
        for (int i = 0; i < 4; ++i) pv[i] = __builtin_amdgcn_raw_buffer_load_b128(rsp, src, i * (8 * S_ * 4), 16);
#pragma unroll
        for (int i = 0; i < 4; ++i) mx = umax(umax(umax(mx, pv[i][0]), pv[i][1]), umax(pv[i][2], pv[i][3]));
        if (none(mx == SENT)) break;
        __builtin_amdgcn_s_sleep(PPSLP);
      }
    }
    asm volatile("s_waitcnt lgkmcnt(0)" ::: "memory");
    __builtin_amdgcn_s_barrier();
    if (!LAY2) {
      PIN();
      asm volatile(AGAIN "\ngate_up_code:\n\t"
                 "v_lshlrev_b32 v84, 16, v100\n\tv_and_b32 v85, 0xffff0000, v100\n\tv_lshlrev_b32 v86, 16, v101\n\tv_and_b32 v87, 0xffff0000, v101\n\t"
                 "v_subrev_f32 v84, s84, v84\n\tv_subrev_f32 v85, s84, v85\n\tv_subrev_f32 v86, s84, v86\n\tv_subrev_f32 v87, s84, v87\n\t"
                 "v_cvt_pk_bf16_f32 v85, v84, v85\n\tv_cvt_pk_bf16_f32 v87, v86, v87\n\ts_mov_b32 s88, 0x01000100\n\ts_mov_b32 s89, 0x03020302\n\t"
                 "v_lshlrev_b32 v84, 16, v85\n\tv_and_b32 v85, 0xffff0000, v85\n\tv_lshlrev_b32 v86, 16, v87\n\tv_and_b32 v87, 0xffff0000, v87\n\t"
                 "v_mul_f32 v88, s86, v84\n\tv_mul_f32 v89, s86, v85\n\tv_mul_f32 v90, s86, v86\n\tv_mul_f32 v91, s86, v87\n\tv_fma_f32 v84, -v88, s85, v84\n\t"
                 "v_fma_f32 v85, -v89, s85, v85\n\tv_fma_f32 v86, -v90, s85, v86\n\tv_fma_f32 v87, -v91, s85, v87\n\tv_fmac_f32 v88, s86, v84\n\t"
                 "v_fmac_f32 v89, s86, v85\n\tv_fmac_f32 v90, s86, v86\n\tv_fmac_f32 v91, s86, v87\n\tv_cvt_pk_bf16_f32 v88, v88, v89\n\t"
                 "v_cvt_pk_bf16_f32 v90, v90, v91\n\tv_perm_b32 v92, v88, v88, s88\n\tv_perm_b32 v93, v88, v88, s89\n\tv_perm_b32 v94, v90, v90, s88\n\t"
                 "v_perm_b32 v95, v90, v90, s89\n\ts_nop 0\n\tv_and_b32_dpp v96, v92, v102 quad_perm:[0,0,0,0] row_mask:0xf bank_mask:0xf\n\t"
                 "v_and_b32_dpp v97, v92, v103 quad_perm:[0,0,0,0] row_mask:0xf bank_mask:0xf\n\t"
                 "v_and_b32_dpp v100, v93, v102 quad_perm:[0,0,0,0] row_mask:0xf bank_mask:0xf\n\t"
                 "v_and_b32_dpp v101, v93, v103 quad_perm:[0,0,0,0] row_mask:0xf bank_mask:0xf\n\tv_mfma_f32_4x4x4_16b_bf16 v[84:87], v[96:97], v[104:105], 0\n\t"
                 "v_mfma_f32_4x4x4_16b_bf16 v[88:91], v[96:97], v[106:107], 0\n\tv_and_b32_dpp v96, v94, v102 quad_perm:[0,0,0,0] row_mask:0xf bank_mask:0xf\n\t"
                 "v_and_b32_dpp v97, v94, v103 quad_perm:[0,0,0,0] row_mask:0xf bank_mask:0xf\n\t"
                 "v_mfma_f32_4x4x4_16b_bf16 v[84:87], v[100:101], v[108:109], v[84:87]\n\tv_mfma_f32_4x4x4_16b_bf16 v[88:91], v[100:101], v[110:111], v[88:91]\n\t"
                 "v_and_b32_dpp v100, v95, v102 quad_perm:[0,0,0,0] row_mask:0xf bank_mask:0xf\n\t"
                 "v_and_b32_dpp v101, v95, v103 quad_perm:[0,0,0,0] row_mask:0xf bank_mask:0xf\n\tv_mfma_f32_4x4x4_16b_bf16 v[84:87], v[96:97], v[112:113], v[84:87]\n\t"
                 "v_mfma_f32_4x4x4_16b_bf16 v[88:91], v[96:97], v[114:115], v[88:91]\n\tv_and_b32_dpp v96, v92, v102 quad_perm:[1,1,1,1] row_mask:0xf bank_mask:0xf\n\t"
                 "v_and_b32_dpp v97, v92, v103 quad_perm:[1,1,1,1] row_mask:0xf bank_mask:0xf\n\t"
                 "v_mfma_f32_4x4x4_16b_bf16 v[84:87], v[100:101], v[116:117], v[84:87]\n\tv_mfma_f32_4x4x4_16b_bf16 v[88:91], v[100:101], v[118:119], v[88:91]\n\t"
                 "v_and_b32_dpp v100, v93, v102 quad_perm:[1,1,1,1] row_mask:0xf bank_mask:0xf\n\t"
                 "v_and_b32_dpp v101, v93, v103 quad_perm:[1,1,1,1] row_mask:0xf bank_mask:0xf\n\tv_mfma_f32_4x4x4_16b_bf16 v[84:87], v[96:97], v[120:121], v[84:87]\n\t"
                 "v_mfma_f32_4x4x4_16b_bf16 v[88:91], v[96:97], v[122:123], v[88:91]\n\tv_and_b32_dpp v96, v94, v102 quad_perm:[1,1,1,1] row_mask:0xf bank_mask:0xf\n\t"
                 "v_and_b32_dpp v97, v94, v103 quad_perm:[1,1,1,1] row_mask:0xf bank_mask:0xf\n\t"
                 "v_mfma_f32_4x4x4_16b_bf16 v[84:87], v[100:101], v[124:125], v[84:87]\n\tv_mfma_f32_4x4x4_16b_bf16 v[88:91], v[100:101], v[126:127], v[88:91]\n\t"
                 "v_and_b32_dpp v100, v95, v102 quad_perm:[1,1,1,1] row_mask:0xf bank_mask:0xf\n\t"
                 "v_and_b32_dpp v101, v95, v103 quad_perm:[1,1,1,1] row_mask:0xf bank_mask:0xf\n\tv_mfma_f32_4x4x4_16b_bf16 v[84:87], v[96:97], v[128:129], v[84:87]\n\t"
                 "v_mfma_f32_4x4x4_16b_bf16 v[88:91], v[96:97], v[130:131], v[88:91]\n\tv_and_b32_dpp v96, v92, v102 quad_perm:[2,2,2,2] row_mask:0xf bank_mask:0xf\n\t"
                 "v_and_b32_dpp v97, v92, v103 quad_perm:[2,2,2,2] row_mask:0xf bank_mask:0xf\n\t"
                 "v_mfma_f32_4x4x4_16b_bf16 v[84:87], v[100:101], v[132:133], v[84:87]\n\tv_mfma_f32_4x4x4_16b_bf16 v[88:91], v[100:101], v[134:135], v[88:91]\n\t"
                 "v_and_b32_dpp v100, v93, v102 quad_perm:[2,2,2,2] row_mask:0xf bank_mask:0xf\n\t"
                 "v_and_b32_dpp v101, v93, v103 quad_perm:[2,2,2,2] row_mask:0xf bank_mask:0xf\n\tv_mfma_f32_4x4x4_16b_bf16 v[84:87], v[96:97], v[136:137], v[84:87]\n\t"
                 "v_mfma_f32_4x4x4_16b_bf16 v[88:91], v[96:97], v[138:139], v[88:91]\n\tv_and_b32_dpp v96, v94, v102 quad_perm:[2,2,2,2] row_mask:0xf bank_mask:0xf\n\t"
                 "v_and_b32_dpp v97, v94, v103 quad_perm:[2,2,2,2] row_mask:0xf bank_mask:0xf\n\t"
                 "v_mfma_f32_4x4x4_16b_bf16 v[84:87], v[100:101], v[140:141], v[84:87]\n\tv_mfma_f32_4x4x4_16b_bf16 v[88:91], v[100:101], v[142:143], v[88:91]\n\t"
                 "v_and_b32_dpp v100, v95, v102 quad_perm:[2,2,2,2] row_mask:0xf bank_mask:0xf\n\t"
                 "v_and_b32_dpp v101, v95, v103 quad_perm:[2,2,2,2] row_mask:0xf bank_mask:0xf\n\tv_mfma_f32_4x4x4_16b_bf16 v[84:87], v[96:97], v[144:145], v[84:87]\n\t"
                 "v_mfma_f32_4x4x4_16b_bf16 v[88:91], v[96:97], v[146:147], v[88:91]\n\tv_and_b32_dpp v96, v92, v102 quad_perm:[3,3,3,3] row_mask:0xf bank_mask:0xf\n\t"
                 "v_and_b32_dpp v97, v92, v103 quad_perm:[3,3,3,3] row_mask:0xf bank_mask:0xf\n\t"
                 "v_mfma_f32_4x4x4_16b_bf16 v[84:87], v[100:101], v[148:149], v[84:87]\n\tv_mfma_f32_4x4x4_16b_bf16 v[88:91], v[100:101], v[150:151], v[88:91]\n\t"
                 "v_and_b32_dpp v100, v93, v102 quad_perm:[3,3,3,3] row_mask:0xf bank_mask:0xf\n\t"
                 "v_and_b32_dpp v101, v93, v103 quad_perm:[3,3,3,3] row_mask:0xf bank_mask:0xf\n\tv_mfma_f32_4x4x4_16b_bf16 v[84:87], v[96:97], v[152:153], v[84:87]\n\t"
                 "v_mfma_f32_4x4x4_16b_bf16 v[88:91], v[96:97], v[154:155], v[88:91]\n\tv_and_b32_dpp v96, v94, v102 quad_perm:[3,3,3,3] row_mask:0xf bank_mask:0xf\n\t"
                 "v_and_b32_dpp v97, v94, v103 quad_perm:[3,3,3,3] row_mask:0xf bank_mask:0xf\n\t"
                 "v_mfma_f32_4x4x4_16b_bf16 v[84:87], v[100:101], v[156:157], v[84:87]\n\tv_mfma_f32_4x4x4_16b_bf16 v[88:91], v[100:101], v[158:159], v[88:91]\n\t"
                 "v_and_b32_dpp v100, v95, v102 quad_perm:[3,3,3,3] row_mask:0xf bank_mask:0xf\n\t"
                 "v_and_b32_dpp v101, v95, v103 quad_perm:[3,3,3,3] row_mask:0xf bank_mask:0xf\n\tv_mfma_f32_4x4x4_16b_bf16 v[84:87], v[96:97], v[160:161], v[84:87]\n\t"
                 "v_mfma_f32_4x4x4_16b_bf16 v[88:91], v[96:97], v[162:163], v[88:91]\n\ts_nop 1\n\t"
                 "v_mfma_f32_4x4x4_16b_bf16 v[84:87], v[100:101], v[164:165], v[84:87]\n\tv_mfma_f32_4x4x4_16b_bf16 v[88:91], v[100:101], v[166:167], v[88:91]\n\t"
                 "s_nop 4\n\tv_permlane32_swap_b32 v84, v88\n\tv_permlane32_swap_b32 v85, v89\n\tv_permlane32_swap_b32 v86, v90\n\tv_permlane32_swap_b32 v87, v91\n\t"
                 "v_add_f32 v84, v84, v88\n\tv_add_f32 v85, v85, v89\n\tv_add_f32 v86, v86, v90\n\tv_add_f32 v87, v87, v91\n\ts_nop 0\n\t"
                 "v_permlane16_swap_b32 v84, v85\n\tv_permlane16_swap_b32 v86, v87\n\ts_nop 0\n\tv_add_f32 v84, v84, v85\n\tv_add_f32 v86, v86, v87\n\ts_nop 1\n\t"
                 "v_add_f32_dpp v84, v84, v84 row_ror:8 row_mask:0xf bank_mask:0xf\n\tv_add_f32_dpp v86, v86, v86 row_ror:8 row_mask:0xf bank_mask:0xf\n\ts_nop 0\n\t"
                 "v_add_f32_dpp v84, v84, v84 row_ror:4 row_mask:0xf bank_mask:0xf\n\tv_add_f32_dpp v86, v86, v86 row_ror:4 row_mask:0xf bank_mask:0xf\n\t"
                 "ds_write2_b32 v99, v84, v86 offset1:8\n\ts_setpc_b64 s[92:93]\n\t"
                   :: "{s[0:1]}"(ap + 1), "{s2}"(bid), "{v0}"(wid * 64), KEPT() : "s90", "s91", "scc", "memory");
      __builtin_unreachable();
    }
    {
    constexpr int dk = 8;
    constexpr int BEST = FLG + 40, MEET = FLG + 48;
    __builtin_amdgcn_s_setprio(HPRIO);
    if (hw == 0) STAMP(dk, 0);
    const unsigned tb1 = (unsigned)__builtin_amdgcn_s_memrealtime();
    unsigned* hand2 = hand + HAND_N;
    const __amdgpu_buffer_rsrc_t rsh = __builtin_amdgcn_make_buffer_rsrc(hand2, 0, (HAND_N + PS_N) * 4, 0x00020000);
    const __amdgpu_buffer_rsrc_t rsx = __builtin_amdgcn_make_buffer_rsrc(xout, 0, 3 * D_ * 2, 0x00020000);
    constexpr int OOR = (HAND_N + PS_N) * 4;
    if (!p1blk && hw < 2) {
      f32x2 a[2], c[2];
#pragma unroll
      for (int e = 0; e < 4; ++e) {
        a[e >> 1][e & 1] = (__uint_as_float(pv[0][e] << 16) + __uint_as_float(pv[1][e] << 16)) + (__uint_as_float(pv[2][e] << 16) + __uint_as_float(pv[3][e] << 16));
        c[e >> 1][e & 1] = (__uint_as_float(pv[0][e] & 0xffff0000u) + __uint_as_float(pv[1][e] & 0xffff0000u)) + (__uint_as_float(pv[2][e] & 0xffff0000u) + __uint_as_float(pv[3][e] & 0xffff0000u));
      }
      const float r = (fnum + fold8(a[0], a[1])) * __builtin_amdgcn_rsqf(fvar + fold8(c[0], c[1]));
      unsigned key = (__float_as_uint(r) ^ ((unsigned)(__float_as_int(r) >> 31) | 0x80000000u)) & 0xfffff800u;
      if (r != r) key = 0u;
      key = wave_umax(key | (unsigned)(2047 - (rrow + (lane & 7) * 4 + (lane >> 4))));
      __builtin_amdgcn_raw_buffer_store_b64(u32x2{key, tb1 & 0x7fffffffu}, rsh, lane < NREP ? (HAND_N + PK_O + lane * 128 + (rrow >> 5) * 2) * 4 : OOR, 0, 16);
      if (hw == 1) STAMP(dk, 2);
    }
    gu_setup();
    bool got = true;
    unsigned fra = lbase;
    int fro = S_ * D_ * 2;
    auto get_fc = [&]() __attribute__((always_inline)) {
      const u32x2 fw = __builtin_amdgcn_raw_buffer_load_b64(rsh, (HAND_N + PK_O + (bid % NREP) * 128 + lane * 2) * 4, 0, 16);
      got = none(umax(fw[0], fw[1]) == SENT);
      if (got) {
        const unsigned c1 = wave_umax(fw[0]), c2 = wave_umax(fw[0] == c1 ? 0u : fw[0]);
#define BADFC 0
        const unsigned r1 = (2047u - (c1 & 0x7ffu)) ^ BADFC, r2 = 2047u - (c2 & 0x7ffu);
        const unsigned r = (lane & 8) ? r2 : r1;
        fra = lbase + QOFF + (r & 127u) * 128 + (((r ^ lane) & 7u) << 4);
        fro = (r >> 7) == (unsigned)omi && lane < 16 ? ((lane >> 3) * D_ + oni * 64 + (lane & 7) * 8) * 2 : S_ * D_ * 2;
        unsigned dt = (unsigned)((int)((fw[1] - tb1) << 1) >> 1) + 512u;
        dt = dt < 1024u ? dt : 512u;
        *LDS_AT(volatile u32x2, lbase + SPM + 144) = u32x2{r1 | (r2 << 16), (unsigned)__builtin_amdgcn_readlane(dt, r1 >> 5) | (wave_umax(dt) << 16)};
      }
    };
    if (hw == 0) {
      __builtin_amdgcn_s_sleep(FCPRE);
#pragma unroll 1
      for (int i = 0; i < SPIN_MAX; ++i) {
        if (__builtin_amdgcn_readfirstlane(*LDS_AT(const volatile unsigned, lbase + FLG + 28)) - best >= 4u) break;
        __builtin_amdgcn_s_sleep(FCSLP);
      }
      asm volatile("" ::: "memory");
      get_fc();
    } else __builtin_amdgcn_s_sleep(TPRE - 10);
    PIN();
    tile_at(2u);
    const unsigned tb2 = (unsigned)__builtin_amdgcn_s_memrealtime() - 512u;
    LOGDEF;
    LOG();
#pragma unroll 1
    for (int spin = 0; spin < SPIN_MAX && !got; ++spin) { get_fc(); asm volatile("" ::: "memory"); }
    if (hw == 0) __builtin_amdgcn_raw_buffer_store_b128(lds_rd4(fra), rsx, fro, 0, XAUX);
    PIN();
    u32x2 fm = {SENT, 0u};
#pragma unroll 1
    for (int i = 0; i < 4 * SPIN_MAX && fm[0] == SENT; ++i) {
      const u32x2 q = *LDS_AT(const volatile u32x2, lbase + SPM + 144);
      fm = u32x2{(unsigned)__builtin_amdgcn_readfirstlane(q[0]), (unsigned)__builtin_amdgcn_readfirstlane(q[1])};
    }
    const int fr1 = fm[0] & 0x7ffu, fr2 = (fm[0] >> 16) & 0x7ffu;
    const unsigned t1 = tb2 + (fm[1] & 1023u), tmx = tb2 + ((fm[1] >> 16) & 1023u);
    auto until = [&](unsigned te) { while ((int)((unsigned)__builtin_amdgcn_s_memrealtime() - te) < 0) __builtin_amdgcn_s_sleep(1); asm volatile("" ::: "memory"); };
    lds_wr(lbase + SPM + 136, tmx);
    PIN();
    if (hw == 0) STAMP(dk, 3);
    const int xoff = (hw * 256 + (lane & 3) * 64 + (lane >> 2) * 4) * 2;
    const int oc = (omi * 2 + (oni >> 3)) * 32, col = oc + (lane & 7) * 4 + (lane >> 4);
    const int xoo = !p1blk && hw == 3 ? (col & ~1) * 2 : S_ * D_ * 2;
    auto get_row = [&](int r, int sl, u32x2& xr, unsigned& xo) __attribute__((always_inline)) {
      if ((r >> 7) == omi) {
#pragma unroll 1
        for (int i = 0; i < SPIN_MAX; ++i) {
          if (__builtin_amdgcn_readfirstlane(*LDS_AT(const volatile unsigned, lbase + FLG + 24)) >= 16u) break;
          __builtin_amdgcn_s_sleep(1);
        }
        asm volatile("" ::: "memory");
      }
#pragma unroll 1
      for (int spin = 0; spin < SPIN_MAX; ++spin) {
        LOG();
        xr = __builtin_amdgcn_raw_buffer_load_b64(rsx, xoff, sl * (D_ * 2), 16);
        xo = __builtin_amdgcn_raw_buffer_load_b32(rsx, xoo, sl * (D_ * 2), 16);
        LOG();
        if (none(umax(umax(xr[0], xr[1]), xo) == SENT)) break;
        __builtin_amdgcn_s_sleep(XRSLP);
        asm volatile("" ::: "memory");
      }
      PIN();
    };
    auto row_md = [&](const u32x2& xr, int k) __attribute__((always_inline)) {
      auto dot = [&](unsigned x, unsigned y, float z) { return __builtin_amdgcn_fdot2_f32_bf16(__builtin_bit_cast(bf16x2, x), __builtin_bit_cast(bf16x2, y), z, false); };
      const float p1 = dot(xr[1], 0x3f803f80u, dot(xr[0], 0x3f803f80u, 0.f)), p2 = dot(xr[1], xr[1], dot(xr[0], xr[0], 0.f));
      const auto q = __builtin_amdgcn_permlane32_swap(__float_as_uint(p1), __float_as_uint(p2), false, false);
      const float qs = __uint_as_float(q[0]) + __uint_as_float(q[1]);
      const auto q2 = __builtin_amdgcn_permlane16_swap(__float_as_uint(qs), __float_as_uint(qs), false, false);
      float sv = __uint_as_float(q2[0]) + __uint_as_float(q2[1]);
      sv = dpp_add<0x121, 0xf>(dpp_add<0x122, 0xf>(dpp_add<0x124, 0xf>(dpp_add<0x128, 0xf>(sv))));
      lds_wr(lbase + SPM + k * 64 + (lane >> 4) * 16 + hw * 4, f2word(sv));
      u32x4 a, c;
#pragma unroll 1
      for (int i = 0; i < SPIN_MAX; ++i) {
        a = lds_rd4(lbase + SPM + k * 64); c = lds_rd4(lbase + SPM + k * 64 + 32);
        if (__builtin_amdgcn_readfirstlane(umax(umax(a[0], a[1]), umax(a[2], a[3]))) != SENT) break;
      }
      auto sum4 = [&](const u32x4& v) { return (__uint_as_float(v[0]) + __uint_as_float(v[1])) + (__uint_as_float(v[2]) + __uint_as_float(v[3])); };
      const float mean = sum4(a) * (1.0f / 1024.0f), msq = sum4(c) * (1.0f / 1024.0f);
      const float den = rbf(__builtin_amdgcn_sqrtf(rbf(rbf(fmaxf(fmaf(-mean, mean, msq), 0.f)) + 1e-5f)));
      return (unsigned)__builtin_amdgcn_readfirstlane((pack2(mean, mean) & 0xffffu) | (__float_as_uint(den) & 0xffff0000u));
    };
    auto ffn = [&](const u32x2& xr, unsigned md, unsigned n, const __amdgpu_buffer_rsrc_t rs, int yo, auto&& off) __attribute__((always_inline)) {
      gate_up(xr, md);
      if (hw == 0 && n == 2u) STAMP(dk, 8);
      asm volatile("" ::: "memory");
      PIN();
      lds_wr(lbase + MEET + hw * 4, n + off());
      u32x4 mt;
#pragma unroll 1
      for (int i = 0; i < SPIN_MAX; ++i) {
        mt = lds_rd4(lbase + MEET);
        if (__builtin_amdgcn_readfirstlane(min4(mt)) >= n) break;
      }
      asm volatile("" ::: "memory");
      PIN();
      if (hw == 0 && n == 2u) STAMP(dk, 9);
      if (__builtin_amdgcn_readfirstlane(mt[0]) != n + 1u)
        __builtin_amdgcn_raw_buffer_store_b128(down(act_diag(n == 4u ? 512 : 0), w), rs, (yo + fg * D_) * 4 + t * 16, 0, 16);
      dad += n == 2u ? 512 : -512;
      if (hw == 0 && n == 2u) STAMP(dk, 4);
    };
    auto y_sum4 = [&](const __amdgpu_buffer_rsrc_t rs, int yo, int k) __attribute__((always_inline)) {
      const int src = (yo + (hw * 56 + (lane >> 3)) * D_ + oc + (lane & 7) * 4) * 4;
      u32x4 v[7];
#pragma unroll
      for (int i = 0; i < 7; ++i) asm volatile("" : "=v"(v[i]));
      unsigned todo = 0x7fu;
#pragma unroll 1
      for (int spin = 0; spin < SPIN_MAX; ++spin) {
        LOG();
#pragma unroll
        for (int i = 0; i < 7; ++i) if ((todo >> i) & 1u) v[i] = __builtin_amdgcn_raw_buffer_load_b128(rs, src, i * (8 * D_ * 4), 16);
#pragma unroll
        for (int i = 0; i < 7; ++i) if (none(umax(umax(v[i][0], v[i][1]), umax(v[i][2], v[i][3])) == SENT)) todo &= ~(1u << i);
        if (!todo) break;
        __builtin_amdgcn_s_sleep(YSLP);
        asm volatile("" ::: "memory");
      }
      f32x2 z0 = {0.f, 0.f}, z1 = {0.f, 0.f};
#pragma unroll
      for (int i = 0; i < 7; ++i) {
        z0 += f32x2{__uint_as_float(v[i][0]), __uint_as_float(v[i][1])};
        z1 += f32x2{__uint_as_float(v[i][2]), __uint_as_float(v[i][3])};
      }
      const unsigned ya = lbase + SPM + 256 + k * 512 + ((lane & 7) * 4 + (lane >> 4)) * 16;
      lds_wr((lane & 8) ? lbase + DUM + lane * 4 : ya + hw * 4, f2word(fold8(z0, z1)));
      u32x4 q = {0u, 0u, 0u, 0u};
      if (hw == 3) {
#pragma unroll 1
        for (int i = 0; i < 4 * SPIN_MAX; ++i) {
          q = lds_rd4(ya);
          if (none(umax(umax(q[0], q[1]), umax(q[2], q[3])) == SENT)) break;
        }
      }
      return (__uint_as_float(q[0]) + __uint_as_float(q[1])) + (__uint_as_float(q[2]) + __uint_as_float(q[3]));
    };
    const __amdgpu_buffer_rsrc_t rs1 = __builtin_amdgcn_make_buffer_rsrc(hand, 0, HAND_N * 4, 0x00020000);
    u32x2 xr;
    unsigned xo, xo2;
    until(t1 + XDT);
    get_row(fr1, 0, xr, xo);
    if (hw == 0) STAMP(dk, 7);
#define FAILFC 0
    const unsigned md1 = row_md(xr, 0) ^ (FAILFC & 1);
    if (hw == 0) STAMP(dk, 10);
    LOGAT(20);
    u32x2 xr2;
    unsigned md2 = 0u;
    if (p1blk) {
      xr2 = __builtin_amdgcn_raw_buffer_load_b64(rsx, (fr2 >> 7) == omi ? S_ * D_ * 2 : xoff, D_ * 2, 16);
      PIN();
      ffn(xr, md1, 2u, rsh, GRAN_N, [&]() { return 0u; });
      __builtin_amdgcn_s_setprio(R2PRIO);
      LOG();
      if ((fr2 >> 7) == omi || !none(umax(xr2[0], xr2[1]) == SENT)) get_row(fr2, 1, xr2, xo2);
      md2 = row_md(xr2, 1) ^ (FAILFC & 2);
      ffn(xr2, md2, 4u, rs1, GRAN_N, [&]() {
        const u32x2 v = *LDS_AT(const volatile u32x2, lbase + BEST);
        return v[0] != 0u && (2047 - (int)(v[0] & 0x7ffu) != fr2 || v[1] != md2) ? 1u : 0u; });
    }
    if (hw == 0) STAMP(dk, 5);
    LOG();
    float y = 0.f;
    auto put_row = [&](int r, unsigned md, unsigned rb) __attribute__((always_inline)) {
      const float rden = __uint_as_float(md & 0xffff0000u), rs = rbf(frcp(1.0f + __expf(-__uint_as_float(rb << 16))));
      const float xa = __uint_as_float((col & 1) ? xo & 0xffff0000u : xo << 16);
      float ha = xa, hb = xa;
      ln_apply2(ha, hb, __uint_as_float(md << 16), rden, frcp(rden));
      const float o = xa + rbf(ha + rbf(rbf(y) * rs));
      __builtin_amdgcn_raw_buffer_store_b16((unsigned short)(pack2(o, o) & 0xffffu), __builtin_amdgcn_make_buffer_rsrc(out, 0, S_ * D_ * 2, 0x00020000),
                                            (lane & 8) ? S_ * D_ * 2 : (r * D_ + col) * 2, 0, OAUX);
    };
    if (!p1blk) {
      until(tmx + XD2);
      get_row(fr2, 1, xr2, xo2);
      md2 = row_md(xr2, 1) ^ (FAILFC & 2);
      until(t1 + XDT + YD1); until(tmx + YD2);
      if (hw == 3) STAMP(dk, 13);
      LOGAT(8);
      y = y_sum4(rsh, GRAN_N, 0);
      if (hw == 3) STAMP(dk, 14);
      LOGAT(24);
    }
    unsigned bmd = 0u;
    best = 0u;
#pragma unroll 1
    for (int spin = 0; spin < 4 * SPIN_MAX; ++spin) {
      const u32x2 v = *LDS_AT(const volatile u32x2, lbase + BEST);
      best = __builtin_amdgcn_readfirstlane(v[0]); bmd = __builtin_amdgcn_readfirstlane(v[1]);
      if (best) break;
      __builtin_amdgcn_s_sleep(1);
    }
    asm volatile("" ::: "memory");
    const int idx = 2047 - (int)(best & 0x7ffu);
    const unsigned rk = best >> 16, rb = (rk & 0x8000u) ? (rk & 0x7fffu) : (~rk & 0xffffu);
    if (hw == 0) STAMP(dk, 1);
    LOGAT(25);
    LOGOUT(hw);
    if (__builtin_expect(idx != fr1 || bmd != md1, 0)) {
      if (idx == fr2 && bmd == md2) {
        if (p1blk) return;
        y = y_sum4(rs1, GRAN_N, 1);
        xo = xo2;
      } else {
        get_row(idx, idx == fr1 ? 0 : (idx == fr2 ? 1 : 2), xr, xo);
        if (p1blk) ffn(xr, bmd, 6u, rsh, HAND_N + Y3_O, [&]() { return 0u; });
        else y = y_sum4(rsh, HAND_N + Y3_O, 2);
        if (p1blk || hw != 3) return;
#pragma unroll 1
        for (int spin = 0; spin < SPIN_MAX && idx != fr1 && idx != fr2; ++spin) {
          if (__builtin_amdgcn_readfirstlane(ld_ag(gran + GR_F + (oc >> 6) * 32)) == tag) break;
          asm volatile("" ::: "memory");
        }
      }
    }
    if (p1blk || hw != 3) return;
    put_row(idx, bmd, rb);
    }
    return;
  }
  if (wid >= 4) {
    const int lane = fresh_lane();
    const int lw = wid - 4;
    const long long lay_ = ap->lay;
    const u16* A = ap->a; const u16* W = ap->W; const u16* wc = ap->wc;
#define LPRIO 3
#define LP0 3
    __builtin_amdgcn_s_setprio(LP0);
    unsigned va[4], vw[6];
    const unsigned ul = lane;
    va[0] = (unsigned)(m0 + lw * 32) * (K * 2) + (ul >> 3) * (K * 2) + (((ul & 7u) ^ (ul >> 4)) << 4);
    va[1] = va[0] + (8 * K * 2 - 1024);
    va[2] = (va[0] ^ 64u) + 2 * (8 * K * 2 - 1024);
    va[3] = va[2] + (8 * K * 2 - 1024);
#pragma unroll
    for (int i = 0; i < 3; ++i) {
      const unsigned n = ul + 64u * i, k = __umul24(n, 171u) >> 12, cp = n - __umul24(k, 24u);
      vw[i] = __umul24(k, ldw * 2) + (cp >> 3) * 2048u + (((cp & 7u) ^ ((k & 2u) << 1)) << 4) + ((unsigned)(lw * 16 * ldw + n0) * 2u - i * 1024u);
      vw[i + 3] = (vw[i] ^ 16u) + 8 * ldw * 2;
    }
    PIN();
    asm volatile("" : "+v"(va[0]), "+v"(va[1]), "+v"(va[2]), "+v"(va[3]), "+v"(vw[0]), "+v"(vw[1]), "+v"(vw[2]), "+v"(vw[3]), "+v"(vw[4]), "+v"(vw[5]));
    const bool LAY2 = lay_ != 0;
    const int krot = tmi & 8;
    if (LAY2 && slow_cols(W, tni)) W = wc;
    auto stage_a = [&](int kt, int slot) {
      const char* pa = reinterpret_cast<const char*>(A) + (size_t)(kt ^ krot) * 128;
      asm volatile("s_mov_b32 m0, %5\n\ts_nop 0\n\tglobal_load_lds_dwordx4 %0, %4 sc1\n\tglobal_load_lds_dwordx4 %1, %4 offset:1024 sc1\n\t"
                   "global_load_lds_dwordx4 %2, %4 offset:2048 sc1\n\tglobal_load_lds_dwordx4 %3, %4 offset:3072 sc1"
                   :: "v"(va[0]), "v"(va[1]), "v"(va[2]), "v"(va[3]), "s"(pa), "s"(lbase + slot * STB + lw * 4096) : "m0", "memory");
    };
    auto stage_w = [&](int kt, int slot) {
      const char* pw = reinterpret_cast<const char*>(W) + (size_t)(kt ^ krot) * 128 * ldw;
      asm volatile("s_mov_b32 m0, %7\n\ts_nop 0\n\tglobal_load_lds_dwordx4 %0, %6 sc1\n\tglobal_load_lds_dwordx4 %1, %6 offset:1024 sc1\n\t"
                   "global_load_lds_dwordx4 %2, %6 offset:2048 sc1\n\ts_addk_i32 m0, 0xc00\n\ts_nop 0\n\tglobal_load_lds_dwordx4 %3, %6 sc1\n\t"
                   "global_load_lds_dwordx4 %4, %6 offset:1024 sc1\n\tglobal_load_lds_dwordx4 %5, %6 offset:2048 sc1"
                   :: "v"(vw[0]), "v"(vw[1]), "v"(vw[2]), "v"(vw[3]), "v"(vw[4]), "v"(vw[5]), "s"(pw), "s"(lbase + slot * STB + ABYTES + lw * 6144) : "m0", "scc", "memory");
    };
    if (LAY2) {
      __builtin_amdgcn_s_setprio(0);
      __builtin_amdgcn_s_sleep(TPRE - 10);
      tile_at(3u);
      __builtin_amdgcn_s_setprio(LPRIO);
#pragma unroll
      for (int st = 0; st < NST; ++st) stage_w(st, st);
      PIN();
      __builtin_amdgcn_s_barrier();
      PIN();
#pragma unroll
      for (int st = 0; st < NST; ++st) stage_a(st, st);
      asm volatile("s_waitcnt vmcnt(8)" ::: "memory");
    } else {
      __builtin_amdgcn_s_setprio(LPRIO);
      stage_a(0, 0); stage_w(0, 0);
      if (PRO > 1) { stage_a(1, 1); stage_w(1, 1); }
      if (PRO > 2) { stage_a(2, 2); stage_w(2, 2); }
      asm volatile("s_waitcnt vmcnt(%0)" :: "n"(10 * PRO - 10) : "memory");
    }
    int slot = 0;
    __builtin_amdgcn_s_barrier();
    if (!LAY2) {
      if (PRO < 2) { stage_a(1, 1); stage_w(1, 1); }
      if (PRO < 3) { stage_a(2, 2); stage_w(2, 2); }
    }
    for (int kt = 0; kt + 1 < NKT; ++kt) {
      if (LAY2 && kt == 0) asm volatile("s_waitcnt vmcnt(4)" ::: "memory");
      else if (kt + 2 >= NKT) asm volatile("s_waitcnt vmcnt(0)" ::: "memory");
      else asm volatile("s_waitcnt vmcnt(10)" ::: "memory");
      __builtin_amdgcn_s_barrier();
      if (kt + NST < NKT) { stage_a(kt + NST, slot); stage_w(kt + NST, slot); }
      slot = slot == NST - 1 ? 0 : slot + 1;
    }
    __builtin_amdgcn_s_setprio(0);
    __builtin_amdgcn_s_barrier();
  } else {
  LAYER_HERE;
  const int dk = LAY2 ? 5 : 1;
  const int lane = fresh_lane(), wm = wid & 1, wn = wid >> 1;
  if (wid == 0) STAMP(dk, 0);
  f32x4 acc[4][6];
#pragma unroll
  for (int i = 0; i < 4; ++i)
#pragma unroll
    for (int j = 0; j < 6; ++j) acc[i][j] = f32x4{0.f, 0.f, 0.f, 0.f};
  const int q16 = lane & 15, g4 = lane >> 4;
  const int e0 = 16 * (q16 >> 3) + (q16 & 7);
  s16x8 af[2][4];
  unsigned aad[2], wad[2];
  {
    const int row = wm * 64 + e0, swz = ((row >> 1) & 3) | (((row >> 4) & 1) << 2);
#pragma unroll
    for (int h = 0; h < 2; ++h) aad[h] = lbase + row * 128 + (((4 * h + g4) ^ swz) << 4);
    const int tq = (lane & 15) >> 2, tp = lane & 3, k = 8 * g4 + tq, ch = wn * 4 + 2 * (tp >> 1);
    wad[0] = lbase + ABYTES + k * 384 + ((ch ^ (4 * ((k >> 1) & 1)) ^ (g4 & 1)) << 4) + 8 * (tp & 1);
    wad[1] = wad[0] ^ 16;
  }
  auto lda = [&](unsigned a, int i) { return *LDS_AT(const s16x8, a + (i >> 1) * 4096 + (i & 1) * 1024); };
  auto ldw_ = [&](int h, int j) {
    const unsigned w = wad[j & 1] + h * 12288 + (j >> 1) * 128;
    s16x4 lo = __builtin_amdgcn_ds_read_tr16_b64_v4i16(LDS_AT(s16x4, w));
    s16x4 h4 = __builtin_amdgcn_ds_read_tr16_b64_v4i16(LDS_AT(s16x4, w + 1536));
    return (s16x8)__builtin_shufflevector(lo, h4, 0, 1, 2, 3, 4, 5, 6, 7);
  };
  ARGS_HERE;
  unsigned lto = lbase + wid * 4096;
  char* kvb = reinterpret_cast<char*>(KV) + ((tni * 2 + wn) * 64 + tmi * 4 + wm * 2) * 2048 - lto;
  {
    const unsigned ato = lbase + ALPHA + wm * 256, dum = lbase + DUM + lane * 4;
    *LDS_AT(u32x4, lto + QOFF + lane * 16) = u32x4{lane >= 16 ? lbase + CRED : lbase + TABA + (wm * 64 + e0) * 16, lbase + TABW + (wn * 32 + e0) * 16,
                                                   ato + e0 * 4, lto + (lane >> 5) * 1024 + ((lane & 28) * 2 + (lane & 3)) * 16};
    *LDS_AT(u32x2, lto + QOFF + 2048 + lane * 16) = u32x2{aad[0], aad[1]};
    *LDS_AT(u32x4, lto + QOFF + 1024 + lane * 16) = u32x4{ato + g4 * 16 + (lane >> 5) * 32, lto + (lane >> 5) * 1024 + ((lane & 24) * 2 + (lane & 7)) * 16,
                                                          lane == 0 ? lbase + FLG + 32 : dum, lane == 0 ? lbase + FLG + 16 : dum};
  }
  asm volatile("" : "+s"(lto), "+s"(kvb) :: "memory");
#pragma unroll
  for (int i = 0; i < 4; ++i)
#pragma unroll
    for (int j = 0; j < 6; ++j) asm volatile("" : "+v"(acc[i][j]));
  asm volatile("" : "+v"(aad[0]), "+v"(aad[1]), "+v"(wad[0]), "+v"(wad[1]));
  if (LAY2) __builtin_amdgcn_s_barrier();
  __builtin_amdgcn_s_barrier();
  PIN();
  if (wid == 0) STAMP(dk, 1);
  s16x8 wq[4][2];
  wq[0][0] = ldw_(0, 0); af[0][0] = lda(aad[0], 0); wq[0][1] = ldw_(1, 0); af[1][0] = lda(aad[1], 0); wq[1][0] = ldw_(0, 1); wq[1][1] = ldw_(1, 1);
  af[0][1] = lda(aad[0], 1); af[1][1] = lda(aad[1], 1); af[0][2] = lda(aad[0], 2); af[1][2] = lda(aad[1], 2);
  PIN();
  s16x4 wlo;
  auto rw = [&](s16x8& d, int h, int j, int hi, int off = 0) __attribute__((always_inline)) {
    const s16x4 v = __builtin_amdgcn_ds_read_tr16_b64_v4i16(LDS_AT(s16x4, wad[j & 1] + h * 12288 + (j >> 1) * 128 + hi * 1536 + off));
    if (hi) d = (s16x8)__builtin_shufflevector(wlo, v, 0, 1, 2, 3, 4, 5, 6, 7);
    else wlo = v;
  };
  auto at = [&](s16x8& x, int r) __attribute__((always_inline)) {
    if (r == 0) asm volatile("" :: "{v[104:107]}"(x));
    if (r == 1) asm volatile("" :: "{v[108:111]}"(x));
    if (r == 2) asm volatile("" :: "{v[112:115]}"(x));
    if (r == 3) asm volatile("" :: "{v[116:119]}"(x));
    if (r == 4) asm volatile("" :: "{v[120:123]}"(x));
    if (r == 5) asm volatile("" :: "{v[124:127]}"(x));
    if (r == 6) asm volatile("" :: "{v[128:131]}"(x));
    if (r == 7) asm volatile("" :: "{v[132:135]}"(x));
    if (r == 8) asm volatile("" :: "{v[136:139]}"(x));
    if (r == 9) asm volatile("" :: "{v[140:143]}"(x));
    if (r == 10) asm volatile("" :: "{v[144:147]}"(x));
    if (r == 11) asm volatile("" :: "{v[148:151]}"(x));
    if (r == 12) asm volatile("" :: "{v[152:155]}"(x));
    if (r == 13) asm volatile("" :: "{v[156:159]}"(x));
    if (r == 14) asm volatile("" :: "{v[160:163]}"(x));
    if (r == 15) asm volatile("" :: "{v[164:167]}"(x));
  };
  auto mm = [&](int i, int j, int h, int sl, int hw_) __attribute__((always_inline)) {
    s16x8 &a = af[h][i], &w = wq[sl][hw_];
    at(a, h * 4 + i); at(w, 8 + sl * 2 + hw_);
    if (j >= 4) asm volatile("v_mfma_f32_16x16x32_bf16 %0, %1, %2, %0" : "+v"(acc[i][j]) : "v"(a), "v"(w));
    else asm volatile("v_mfma_f32_16x16x32_bf16 %0, %1, %2, %0" : "+v"(acc[i][j]) : "v"(w), "v"(a));
    PIN();
  };
  auto next = [&](int g, int J0, int s0, int s1, int off) __attribute__((always_inline)) {
    if (g == 2 || g == 3) rw(wq[s0][0], 0, J0, g & 1, off);
    if (g == 4 || g == 5) af[g & 1][0] = lda(aad[g & 1] + off, 0);
    if (g == 6 || g == 7) rw(wq[s0][1], 1, J0, g & 1, off);
    if (g >= 8 && g < 12) rw(wq[s1][(g - 8) >> 1], (g - 8) >> 1, J0 + 1, g & 1, off);
    if (g >= 12) af[g & 1][(g - 10) >> 1] = lda(aad[g & 1] + off, (g - 10) >> 1);
  };
  auto third = [&](int p, int J0, int delta) __attribute__((always_inline)) {
    auto mv = [&](unsigned& x) __attribute__((always_inline)) { asm volatile("v_add_u32 %0, %1, %0" : "+v"(x) : "s"(delta)); };
#pragma unroll
    for (int g = 0; g < 16; ++g) {
      const int h = g & 1, i = p == 1 ? (g >> 1) & 3 : g >> 2, t = p == 1 ? g >> 3 : (g >> 1) & 1, j = 2 * p + t, sl = p == 1 ? 2 - 2 * t : p + t;
      mm(i, j, h, sl, h);
      if (p == 0) {
        if (g < 2) af[g][3] = lda(aad[g], 3);
        else if (g < 6) rw(wq[2][(g - 2) >> 1], (g - 2) >> 1, 2, g & 1);
        else if (g < 10) rw(wq[3][(g - 6) >> 1], (g - 6) >> 1, 5, g & 1);
        else if (g < 12) mv(aad[g - 10]);
      }
      if (p == 1) {
        if (g < 4) rw(wq[0][g >> 1], g >> 1, 3, g & 1);
        if (g == 4) mv(wad[1]);
        if (g >= 8 && g < 12) rw(wq[2][(g - 8) >> 1], (g - 8) >> 1, 4, g & 1);
        if (g == 12) mv(wad[0]);
      }
      if (p == 2) {
        if (g == BARG) {
          asm volatile("" ::: "memory");
          PIN();
          __builtin_amdgcn_s_barrier();
          asm volatile("" ::: "memory");
        }
        next(g, J0, 0, 1, 0);
      }
      PIN();
      {
        if ((p == 2 && g == 15) || (p == 0 && g == 7)) __builtin_amdgcn_s_waitcnt(0xc67f);
        if (p == 0 && g == 2) __builtin_amdgcn_s_waitcnt(0xc57f);
        if ((p == 0 && g == 14) || (p == 1 && (g & 7) == 7)) __builtin_amdgcn_s_waitcnt(0xc07f);
        PIN();
      }
    }
  };
  third(0, 0, STB); third(1, 0, STB);
  {
    static_assert((NKT - NST - 1) % NST == 0, "");
#pragma unroll 1
    for (int kt = 0; kt < NKT - NST - 1; kt += NST) {
#pragma unroll
      for (int u = 0; u < NST; ++u) {
        const int delta = u == 1 ? -(NST - 1) * STB : STB;
        third(2, 0, 0); third(0, 0, delta); third(1, 0, delta);
      }
    }
  }
  static_assert(NKT % NST == 1, "slots of the last stages");
#define TT(i)
  auto bar = [&]() { PIN(); __builtin_amdgcn_s_barrier(); asm volatile("" ::: "memory"); PIN(); };
  TT(0);
  third(2, 2, 0);
  const __amdgpu_buffer_rsrc_t rsk = __builtin_amdgcn_make_buffer_rsrc(kvb, 0, 0x7fffffff, 0x00020000), rsv = __builtin_amdgcn_make_buffer_rsrc(kvb + VOFF, 0, 0x7fffffff, 0x00020000);
  unsigned pk;
  u32x2 t0, t1, t2, t3;
  auto park = [&](int i) { return *LDS_AT(const volatile u32x2, pk + (i >> 1) * 1024 + (i & 1) * 8); };
  float al[4];
  f32x4 av[4];
  u32x4 kvp[12];
  auto cacc = [&](int p, int x, int t) -> f32x4& { return p < 2 ? acc[x][2 * p + t] : acc[(x >> 1) * 2 + t][4 + (x & 1)]; };
  auto signal = [&](unsigned v) { __hip_atomic_fetch_add(LDS_AT(unsigned, t3[0]), v, __ATOMIC_RELAXED, __HIP_MEMORY_SCOPE_WORKGROUP); PIN(); };
  auto outop = [&](int p, int k) {
    if (k >= 0 && k < 16) {
      const int x = k >> 2, t = (k >> 1) & 1;
      f32x4& c = cacc(p, x, t);
#pragma unroll
      for (int o = 2 * (k & 1); o < 2 * (k & 1) + 2; ++o) {
        float r;
        asm volatile("v_mul_f32 %0, %1, %2" : "=v"(r) : "v"(c[o]), "v"(p < 2 ? al[x] : av[(x >> 1) * 2 + t][o]));
        c[o] = r;
      }
      PIN();
    }
    if (k >= 1 && k <= 16) {
      const int x = (k - 1) >> 2, t = ((k - 1) >> 1) & 1, o = (k - 1) & 1;
      kvp[p * 4 + x][2 * t + o] = pack2(cacc(p, x, t)[2 * o], cacc(p, x, t)[2 * o + 1]);
      PIN();
      if (t && o) {
        if (p) __builtin_amdgcn_raw_buffer_store_b128(kvp[p * 4 + x], p == 1 ? rsk : rsv, (p == 1 ? t1[1] : t2[1]) + (x >> 1) * 2048 + (x & 1) * (p == 1 ? 64 : 128), 0, 16);
        else *LDS_AT(u32x4, t2[1] + (x >> 1) * 2048 + (x & 1) * 128) = kvp[x];
        PIN();
      }
    }
  };
  static_for<0, 168>([&](auto nc) __attribute__((always_inline)) {
    constexpr int n = decltype(nc)::value, sec = n < 80 ? n >> 4 : (n < 104 ? 5 : 6 + ((n - 104) >> 4)), g = n < 104 ? n - 16 * sec : (n - 104) & 15;
    auto taba = [&](int i) { af[0][i] = *LDS_AT(const s16x8, t0[0] + (i >> 1) * 512 + (i & 1) * 128); };
    auto tabw = [&](int j) { wq[(2 + (j >> 1)) & 3][j & 1] = *LDS_AT(const s16x8, t0[1] + ((j >> 1) * 64 + (j & 1) * 8) * 16); };
    auto rwn = [&](int e, int sl, int j, int off) { if (e >= 0 && e < 4) rw(wq[sl][e >> 1], e >> 1, j, e & 1, off); };
    if (g == 0) TT(1 + sec);
    if (sec == 5) {
      const int i = (g / 12) * 2 + (g & 1), j = (g % 12) >> 1;
      mm(i, j, 0, (2 + (j >> 1)) & 3, j & 1);
    } else {
      constexpr int part[10] = {1, 2, 1, 2, 0, 0, 1, 2, 0, 0}, s0[10] = {0, 2, 0, 2, 0, 0, 1, 3, 1, 3};
      const int h = g & 1, i = sec == 7 ? (g >> 3) * 2 + ((g >> 1) & 1) : g >> 2, t = sec == 7 ? (g >> 2) & 1 : (g >> 1) & 1;
      mm(i, 2 * part[sec] + t, h, (s0[sec] + t) & 3, h);
    }
    if (sec == 0 || sec == 2) {
      if (g < 2) af[g][3] = lda(aad[g] + sec / 2 * STB, 3);
      rwn(g - 2, 2, 4, sec / 2 * STB); rwn(g - 6, 3, 5, sec / 2 * STB);
    }
    if (sec == 1) {
      if (g == 1) bar();
      next(g, 2, 0, 1, STB);
    }
    if ((sec == 3 && g == 12) || (sec == 6 && g == 13)) { pk = lto + QOFF + fresh_lane() * 16; asm volatile("" : "+v"(pk)); }
    if (sec == 3) {
      rwn(g, 0, 0, STB); rwn(g - 4, 1, 1, STB);
      if (g >= 8 && g < 12) asm volatile("v_subrev_u32 %0, %1, %0" : "+v"(g < 10 ? aad[g & 1] : wad[g & 1]) : "s"(STB));
      if (g == 13) t1 = park(1);
      if (g == 14) t0 = park(0);
    }
    if (sec == 4) {
      if (g == B15) bar();
      if (g == 4) taba(0);
      if (g == 5 || g == 6) tabw(g - 5);
      if (g == 7) taba(1);
      if (g == 8 || g == 9) tabw(g - 6);
      if (g == 10 || g == 11) af[1][g - 10] = lda(aad[1], g - 10);
      if (g == 12) taba(2);
      if (g == 15) taba(3);
    }
    if (sec == 5) {
      if (g < 2) tabw(4 + g);
      rwn(g - 2, 1, 2, 0);
      if (g == 12 || g == 13) af[0][g - 12] = lda(aad[0], g - 12);
      if (g == 22) { al[0] = *LDS_AT(const float, t1[0]); al[1] = *LDS_AT(const float, t1[0] + 32); }
      rwn(g - 16, 2, 3, 0);
      if (g == 20 || g == 21) af[1][g - 18] = lda(aad[1], g - 18);
    }
    if (sec == 6) {
      if (g < 2) af[0][2 + g] = lda(aad[0], 2 + g);
      rwn(g - 2, 3, 4, 0);
      if (g == 9) { al[2] = *LDS_AT(const float, t1[0] + 128); al[3] = *LDS_AT(const float, t1[0] + 160); }
      if (g == 14) t2 = park(2);
      if ((g & 3) >= 2) rwn((g - 10) / 4 * 2 + (g & 1) - (g < 10) * 8, 0, 5, 0);
    }
    if (sec == 7) {
      if (g == 2 || g == 3 || g == 10 || g == 11) av[(g >> 3) * 2 + (g & 1)] = *LDS_AT(const f32x4, t2[0] + ((g >> 3) * 32 + (g & 1) * 8) * 4);
      rwn(g < 2 ? g : (g < 4 ? -1 : g - 2), 1, 0, 0); rwn(g - 6, 2, 1, 0);
      if (g == 12) t3 = park(3);
      if (g == 13) { const u32x2 a = park(4); aad[0] = a[0]; aad[1] = a[1]; }
      if (g == 15) {
        asm volatile("" ::: "memory");
        __builtin_amdgcn_s_waitcnt(0xc07f);
        bar();
      }
    }
    if (sec == 8) next(g, 0, 3, 0, STB);
    if (sec == 9) {
      if (g < 2) af[g][3] = lda(aad[g] + STB, 3);
      if (g == 2) signal(0x10000u);
    }
    PIN();
    outop(1, n - 104 - KO);
    outop(2, n - 120 - KO);
    {
      constexpr int wg[8] = WGAPS;
#pragma unroll
      for (int e = 0; e < 8; ++e)
        if (n - 136 == wg[e]) {
          *LDS_AT(u32x4, (e < 4 ? t1[1] + (e & 1) * 64 : t2[1] + 2048 + (e & 1) * 128) + ((e >> 1) & 1) * ASB) = kvp[4 + e];
          PIN();
        }
    }
    if (n == 152 + 3 || n == 152 + 4) {
      al[2 * (n & 1)] *= QSCALE; al[2 * (n & 1) + 1] *= QSCALE;
      asm volatile("" : "+v"(al[2 * (n & 1)]), "+v"(al[2 * (n & 1) + 1]));
      if (n & 1) asm volatile("v_add_u32 %0, %1, %0" : "+v"(t2[1]) : "s"(QOFF));
      PIN();
    }
    outop(0, n - 152 - KO);
  });
  TT(13);
  if (wid == 0) STAMP(dk, 18);
#pragma unroll
  for (int k = 16 - KO; k <= 16; ++k) outop(0, k);
  __hip_atomic_fetch_add(LDS_AT(unsigned, t3[1]), 1u, __ATOMIC_RELAXED, __HIP_MEMORY_SCOPE_WORKGROUP);
  TT(14);
  asm volatile("s_waitcnt vmcnt(0)" ::: "memory");
  signal(1u);
  TT(15);
  if (wid == 0) STAMP(dk, 19);
  }
  PIN();
  {
  const int lane = fresh_lane(), tid = wid * 64 + lane;
  LAYER_HERE;
  const int dk = LAY2 ? 6 : 2;
  const int g = wid >> 2, hh = (wid >> 1) & 1, qg = wid & 1;
  const int head = tni * 2 + hh, qt = tmi * 4 + qg * 2;
  unsigned qaa = lbase + QOFF + (hh * 2 + qg) * 4096 + lane * 16 + (g ? 0 : 2048), qab = qaa ^ 2048;
  const unsigned wbase = lbase + (hh * 2 + g) * 4096 + lane * 16;
  auto sptr = [&](int sl) { unsigned a = wbase + sl * ASB; asm volatile("" : "+v"(a)); return LDS_AT(const s16x8, a); };
  auto rd = [&](__attribute__((address_space(3))) const s16x8* sp, int off) { return sp[off >> 4]; };
  auto sl0 = sptr(0);
  const unsigned arr_a = lane == 0 ? lbase + FLG + 16 : lbase + DUM + lane * 4;
  auto arrive = [&]() { __hip_atomic_fetch_add(LDS_AT(unsigned, arr_a), 1u, __ATOMIC_RELAXED, __HIP_MEMORY_SCOPE_WORKGROUP); };
  unsigned arr_w = lane == 0 ? lbase + FLG + 8 + g * 4 : lbase + DUM + lane * 4;
  unsigned arr_i = 1u << (8 * (wid & 3));
  auto arrive_w = [&]() { __hip_atomic_fetch_add(LDS_AT(unsigned, arr_w), arr_i, __ATOMIC_RELAXED, __HIP_MEMORY_SCOPE_WORKGROUP); };
  s16x8 sel;
#pragma unroll
  for (int e = 0; e < 8; ++e) sel[e] = ((lane & 7) == (e & 4)) ? (short)0x3F80 : (short)0;
  int sidx = 0xDDDDDDDD;
  f32x16 oa, ob, nn, sa, sb;
  f32x4 la = {0.f, 0.f, 0.f, 0.f}, lb = {0.f, 0.f, 0.f, 0.f};
#pragma unroll
  for (int e = 0; e < 16; ++e) { oa[e] = 0.f; ob[e] = 0.f; sa[e] = 0.f; sb[e] = 0.f; }
  asm volatile("" : "+v"(sidx), "+v"(sel), "+v"(oa), "+v"(ob), "+v"(la), "+v"(lb), "+v"(qaa), "+v"(qab), "+v"(arr_w), "+v"(arr_i));
  PIN();
  s16x8 qa0, qa1, qb0, qb1, k00, k01, v00, v01, v10, v11, k10, k11;
  u32x4 fw;
  auto first = [&]() {
    fw = lds_rd4(lbase + FLG + 16);
    asm volatile("" ::: "memory");
    k00 = rd(sl0, 0); qa0 = *LDS_AT(const s16x8, qaa); qb0 = *LDS_AT(const s16x8, qab);
    k01 = rd(sl0, 1024); qa1 = *LDS_AT(const s16x8, qaa + 1024); qb1 = *LDS_AT(const s16x8, qab + 1024);
    v00 = rd(sl0, 2048); v01 = rd(sl0, 3072);
    PIN();
  };
  first();
  if (__builtin_amdgcn_readfirstlane(fw[0]) < 4u) {
#pragma unroll 1
    for (int i = 0; i < SPIN_MAX; ++i) {
      if (__builtin_amdgcn_readfirstlane(*LDS_AT(const volatile unsigned, lbase + FLG + 16)) >= 4u) break;
      __builtin_amdgcn_s_sleep(ASLP);
    }
    asm volatile("" ::: "memory");
    first();
  }
  const unsigned tag = __builtin_amdgcn_readfirstlane(fw[1]);
  const int idx = LAY2 ? (2047 - (int)(__builtin_amdgcn_readfirstlane(fw[3]) & 0xffffu)) & 2047 : -64;
  float ma;
  const float zoff = __uint_as_float(__builtin_amdgcn_readfirstlane(LAY2 && g == ((idx >> 6) & 1) ? 0u : 0xff800000u));
  {
    sa = MFMA32(k00, qa0, sa); sb = MFMA32(k00, qb0, sb);
    sa = MFMA32(k01, qa1, sa); sb = MFMA32(k01, qb1, sb);
    float mb = sb[0];
    ma = sa[0];
#pragma unroll
    for (int e = 1; e < 16; ++e) { ma = fmaxf(ma, sa[e]); mb = fmaxf(mb, sb[e]); }
    ma = fmaxf(ma, mb);
    const auto sw = __builtin_amdgcn_permlane32_swap(__float_as_uint(ma), __float_as_uint(ma), false, false);
    ma = fmaxf(__uint_as_float(sw[0]), __uint_as_float(sw[1]));
    f32x2 n2 = {-ma, -ma};
    asm volatile("" : "+v"(n2));
#pragma unroll
    for (int e = 0; e < 16; ++e) nn[e] = n2[e & 1];
    sa = MFMA32(k00, qa0, nn); sa = MFMA32(k01, qa1, sa);
    la[0] = lb[0] = -__uint_as_float(__float_as_uint(__builtin_amdgcn_exp2f(zoff - ma)) & 0xffff0000u);
  }
#define SB() __builtin_amdgcn_sched_barrier(0)
#define EX(i) sc[i] = __builtin_amdgcn_exp2f(sc[i])
  auto unit = [&](f32x16& sc, f32x16& sn, const s16x8& kn0, const s16x8& kn1, const s16x8& qn0, const s16x8& qn1,
                  f32x16& op, f32x4& lp, const s16x8& vp,
                  f32x16& oc, f32x4& lc, const s16x8& vc, auto&& mid) {
    lp = SMF16(sel, PHI(sn), lp); SB();
    op = SMF32(vp, PHI(sn), op); SB();
    EX(0); EX(1); EX(2); EX(3); SB();
    sn = MFMA32(kn0, qn0, nn); SB();
    EX(4); EX(5); EX(6); EX(7); SB();
    sn = MFMA32(kn1, qn1, sn); SB();
    mid();
    EX(8); EX(9); EX(10); EX(11); SB();
    lc = SMF16(sel, PLO(sc), lc); SB();
    oc = SMF32(vc, PLO(sc), oc); SB();
    EX(12); EX(13); EX(14); EX(15); SB();
    asm volatile("" : "+v"(sc));
  };
  sl0 = sptr(1);
  unsigned f0 = 0u;
  auto check = [&](unsigned need) {
    if (__builtin_expect(__builtin_amdgcn_readfirstlane(f0) < need, 0)) {
#pragma unroll 1
      for (int i = 0; i < SPIN_MAX; ++i) {
        if (__builtin_amdgcn_readfirstlane(*LDS_AT(const volatile unsigned, lbase + FLG)) >= need) break;
        __builtin_amdgcn_s_sleep(1);
      }
    }
    asm volatile("" ::: "memory");
  };
#define PAT 0x22221110
#define SAW8(i) do { if (((PAT >> (28 - 4 * (i))) & 3) != ((PAT >> ((32 - 4 * (i)) & 31)) & 3)) { __builtin_amdgcn_s_setprio((PAT >> (28 - 4 * (i))) & 3); SB(); } } while (0)
#define SAW(i) do { if (odd) SAW8(4 + (i)); else SAW8(i); } while (0)
  auto three = [&](int st, bool odd) {
    SAW(0);
    unit(sa, sb, k00, k01, qb0, qb1, ob, lb, v11, oa, la, v00, [&]() { k10 = rd(sl0, 0); k11 = rd(sl0, 1024); SB(); });
    SAW(1);
    unit(sb, sa, k10, k11, qa0, qa1, oa, la, v01, ob, lb, v00, [&]() { v10 = rd(sl0, 2048); v11 = rd(sl0, 3072); SB(); });
    sl0 = sptr((2 * st + 2) & 7);
    SB();
    SAW(2);
    unit(sa, sb, k10, k11, qb0, qb1, ob, lb, v01, oa, la, v10, [&]() { k00 = rd(sl0, 0); k01 = rd(sl0, 1024); SB(); });
  };
  auto fourth = [&](int st, bool odd) {
    SAW(3);
    unit(sb, sa, k00, k01, qa0, qa1, oa, la, v11, ob, lb, v10, [&]() {
      v00 = rd(sl0, 2048); v01 = rd(sl0, 3072);
      f0 = *LDS_AT(const volatile unsigned, lbase + FLG);
      SB();
      arrive_w();
      SB(); });
    check(st >= 14 ? 32u : 2u * st + 5u);
    sl0 = sptr((2 * st + 3) & 7);
    SB();
  };
  SB();
  SAW8(0);
  {
    f32x16& sc = sa;
    sb = MFMA32(k00, qb0, nn); SB();
    k10 = rd(sl0, 0); k11 = rd(sl0, 1024); SB();
    sb = MFMA32(k01, qb1, sb); SB();
    EX(0); EX(1); EX(2); EX(3); EX(4); EX(5); EX(6); EX(7); EX(8); SB();
    oa = SMF32(v00, PLO(sa), oa); SB();
    EX(9); EX(10); EX(11); SB();
    la = SMF16(sel, PLO(sa), la); SB();
    EX(12); EX(13); EX(14); EX(15); SB();
    asm volatile("" : "+v"(sa));
  }
  SAW8(1);
  unit(sb, sa, k10, k11, qa0, qa1, oa, la, v01, ob, lb, v00, [&]() { v10 = rd(sl0, 2048); v11 = rd(sl0, 3072); SB(); });
  sl0 = sptr(2);
  SB();
  SAW8(2);
  unit(sa, sb, k10, k11, qb0, qb1, ob, lb, v01, oa, la, v10, [&]() {
    f0 = *LDS_AT(const volatile unsigned, lbase + FLG);
    asm volatile("" ::: "memory");
    k00 = rd(sl0, 0); k01 = rd(sl0, 1024); SB(); });
  if (__builtin_amdgcn_readfirstlane(f0) < 3u) { check(3u); k00 = rd(sl0, 0); k01 = rd(sl0, 1024); }
  SB();
  SAW8(3);
  unit(sb, sa, k00, k01, qa0, qa1, oa, la, v11, ob, lb, v10, [&]() {
    v00 = rd(sl0, 2048); v01 = rd(sl0, 3072); f0 = *LDS_AT(const volatile unsigned, lbase + FLG); SB(); arrive_w(); SB(); });
  check(5u);
  sl0 = sptr(3);
  SB();
  three(1, true);
#pragma unroll 1
  for (int st = 2; st < 16; st += 2) { fourth(st - 1, true); three(st, false); fourth(st, false); three(st + 1, true); }
  SAW8(7);
  unsigned vr, tk, tv;
  u32x4 f4;
  auto table = [&]() {
    f4 = lds_rd4(lbase + FLG);
    k00 = *LDS_AT(const volatile s16x8, tk); k01 = *LDS_AT(const volatile s16x8, tk + 32);
    vr = *LDS_AT(const volatile u16, tv);
  };
  unsigned xw, xm;
  u32x2 ml;
  u32x4 o2[4];
  auto take = [&]() {
    ml = *LDS_AT(const volatile u32x2, xm);
#pragma unroll
    for (int e = 0; e < 4; ++e) o2[e] = lds_rd4(xw + e * 1024);
  };
  float pzt;
  int dso;
  f32x16& sc = sb;
#define EXP() do { asm volatile("" : "+v"(sc)); SB(); } while (0)
  oa = SMF32(v11, PHI(sa), oa); SB();
  tk = lbase + TAB + 128 + hh * 64 + (lane >> 5) * 16; tv = lbase + TAB + 256 + hh * 64 + (lane & 31) * 2;
  table();
  SB();
  EX(0); EX(1); EXP();
  la = SMF16(sel, PHI(sa), la); SB();
  EX(2); EX(3); EX(4); EXP();
  xw = wbase + (qg ? QOFF : 6 * ASB); xm = lbase + XML + wid * 512 + lane * 8;
  pzt = __builtin_amdgcn_exp2f(nn[0] + (zoff - 8.f));
  asm volatile("" : "+v"(xw), "+v"(xm), "+v"(pzt), "+v"(f4));
  SB();
  {
    const unsigned ph = LAY2 ? 0x79797979u : 0x80808080u, pg = qg ? 0u : 0x80u << (16 * hh + 8);
    auto ready = [&]() {
      return (((__builtin_amdgcn_readfirstlane(f4[1]) + ph) & 0x80808080u) == 0x80808080u) & (((__builtin_amdgcn_readfirstlane(g ? f4[3] : f4[2]) + 0x71717171u) & pg) == pg);
    };
    if (__builtin_expect(!ready(), 0)) {
#pragma unroll 1
      for (int i = 0; i < SPIN_MAX; ++i) {
        __builtin_amdgcn_s_sleep(1);
        table();
        if (ready()) break;
      }
    }
    asm volatile("" ::: "memory");
  }
  SB();
  sa = MFMA32(k00, qb0, nn); asm volatile("" : "+v"(sa)); SB();
#pragma unroll
  for (int e = 0; e < 4; ++e) *LDS_AT(volatile f32x4, xw + e * 1024) = f32x4{oa[4 * e], oa[4 * e + 1], oa[4 * e + 2], oa[4 * e + 3]};
  *LDS_AT(volatile u32x2, xm) = u32x2{__float_as_uint(ma), f2word(pzt <= la[0] ? la[0] : 0.f)};
  SB();
  EX(5); EX(6); EX(7); EXP();
  sa = MFMA32(k01, qb1, sa); asm volatile("" : "+v"(sa), "+v"(vr)); SB();
  ARGS_HERE;
  EX(8); EX(9); EX(10); EXP();
  ob = SMF32(v10, PLO(sb), ob); SB();
  EX(11); EX(12); EX(13); EXP();
  lb = SMF16(sel, PLO(sb), lb); SB();
  const bool used = !LAY2 || (qt + g) * 32 + (lane & 31) != idx;
  dso = used ? (((qt + g) * 32 + head) * 1024 + lane * 8) * 2 : S_ * D_ * 2;
  xw += g ? -4096 : 4096; xm += g ? -2048 : 2048;
  asm volatile("" : "+v"(dso), "+v"(xw), "+v"(xm));
  SB();
  EX(14); EX(15); EXP();
  take();
  SB();
  lb = SMF16(sel, PHI(sb), lb); SB();
  ob = SMF32(v11, PHI(sb), ob); SB();
  __builtin_amdgcn_s_setprio(EPRIO);
  TE(0, 0); TE(1, 0); TE(2, 0); TE(3, 0); TE(7, 0);
  float lsb = lb[0];
  if (LAY2) {
    sa[0] = __builtin_amdgcn_exp2f(sa[0]);
    ob = SMF32((u32x4{lane < 32 ? vr : 0u, 0u, 0u, 0u}), PLO(sa), ob);
    lsb += __uint_as_float(__float_as_uint(sa[0]) & 0xffff0000u);
    lsb = pzt <= lsb ? lsb : 0.f;
  }
  const __amdgpu_buffer_rsrc_t rso = __builtin_amdgcn_make_buffer_rsrc(Of, 0, S_ * D_ * 2, 0x00020000);
  TE(0, 1);
  if (__builtin_amdgcn_ballot_w64(ml[1] == SENT) != 0ull) {
#pragma unroll 1
    for (int i = 0; i < SPIN_MAX; ++i) {
      __builtin_amdgcn_s_sleep(XSLP);
      if (!__builtin_amdgcn_ballot_w64(*LDS_AT(const volatile unsigned, xm + 4) == SENT)) break;
    }
    asm volatile("s_waitcnt lgkmcnt(0)" ::: "memory");
    take();
  }
  arrive();
  TE(0, 2);
  TE(0, 3); TE(1, 1);
  {
    const float m2 = __uint_as_float(ml[0]), l2 = __uint_as_float(ml[1]);
    const float mx = fmaxf(ma, m2);
    const float f1 = __builtin_amdgcn_exp2f(ma - mx), f2 = __builtin_amdgcn_exp2f(m2 - mx);
    const float inv = frcp(lsb * f1 + l2 * f2);
    const float c1 = f1 * inv, c2 = f2 * inv;
    f32x2 r[8];
#pragma unroll
    for (int e = 0; e < 8; ++e) {
      const unsigned x0 = o2[e >> 1][(e & 1) * 2], x1 = o2[e >> 1][(e & 1) * 2 + 1];
      r[e] = f32x2{__uint_as_float(x0) * c2, __uint_as_float(x1) * c2};
    }
#pragma unroll
    for (int e = 0; e < 8; ++e) r[e] = f32x2{ob[2 * e], ob[2 * e + 1]} * f32x2{c1, c1} + r[e];
    auto put = [&]() {
#pragma unroll
      for (int sx = 0; sx < 2; ++sx)
        __builtin_amdgcn_raw_buffer_store_b128(u32x4{pack2(r[4 * sx][0], r[4 * sx][1]), pack2(r[4 * sx + 1][0], r[4 * sx + 1][1]),
            pack2(r[4 * sx + 2][0], r[4 * sx + 2][1]), pack2(r[4 * sx + 3][0], r[4 * sx + 3][1])}, rso, dso + sx * 1024, 0, 16);
    };
    put();
    PIN();
    __builtin_amdgcn_s_setprio(0);
    PIN();
    f32x2 chk = {lsb - __builtin_amdgcn_fmed3f(lsb, 8.6736174e-19f, 1.2676506e30f), l2 - __builtin_amdgcn_fmed3f(l2, 8.6736174e-19f, 1.2676506e30f)};
#pragma unroll
    for (int e = 0; e < 8; ++e) chk = r[e] * f32x2{0.f, 0.f} + chk;
    if (__builtin_expect(__builtin_amdgcn_ballot_w64(used && !(chk[0] + chk[1] == 0.f)) != 0ull, 0)) {
      float l;
      attn_safe(KV, head * 64, qb0, qb1, ob, l, LAY2 ? head * 64 + (idx >> 5) : -1, idx & 31, lbase + TAB + 128 + hh * 64);
      const float inv2 = 1.0f / l;
#pragma unroll
      for (int e = 0; e < 8; ++e) r[e] = f32x2{ob[2 * e] * inv2, ob[2 * e + 1] * inv2};
      asm volatile("s_waitcnt vmcnt(0)" ::: "memory");
      put();
    }
    unsigned pp = 0u;
    if (LAY2) {
      const unsigned ua = lbase + UGL + hh * 128 + (lane >> 5) * 16;
      f32x2 du = {0.f, 0.f}, dn = {0.f, 0.f};
#pragma unroll
      for (int q = 0; q < 4; ++q) {
        const f32x4 uu = *LDS_AT(const f32x4, ua + q * 32), gg = *LDS_AT(const f32x4, ua + 256 + q * 32);
        du = r[2 * q] * f32x2{uu[0], uu[1]} + du; du = r[2 * q + 1] * f32x2{uu[2], uu[3]} + du;
        const f32x2 t0 = r[2 * q] * f32x2{gg[0], gg[1]}, t1 = r[2 * q + 1] * f32x2{gg[2], gg[3]};
        dn = t0 * t0 + dn; dn = t1 * t1 + dn;
      }
      const auto a = __builtin_amdgcn_permlane32_swap(__float_as_uint(du[0] + du[1]), __float_as_uint(du[0] + du[1]), false, false);
      const auto c = __builtin_amdgcn_permlane32_swap(__float_as_uint(dn[0] + dn[1]), __float_as_uint(dn[0] + dn[1]), false, false);
      pp = f2word(__uint_as_float(pack2(__uint_as_float(a[0]) + __uint_as_float(a[1]), __uint_as_float(c[0]) + __uint_as_float(c[1]))));
    }
    __builtin_amdgcn_raw_buffer_store_b32(pp, __builtin_amdgcn_make_buffer_rsrc(hand + 2 * HAND_N + PP_O, 0, LAY2 ? H_ * S_ * 4 : 0, 0x00020000),
                                          lane < 32 ? (head * S_ + (qt + g) * 32 + lane) * 4 : H_ * S_ * 4, 0, 16);
    PIN();
  }
  TE(1, 2);
  u32x4 xres[2];
  oproj_tail(LAY2, tid, lbase, lbase + FLG, omi, oni, idx, gran + GR_O + ((LAY2 ? 16 : 0) + tmi) * (64 * RO) + tni,
             gran + GR_X + omi * (128 * RX) + oni * 4, tag, Of, xout, A, w_r, stats_out, stats, hand, xres TEPASS);
  if (LAY2) {
    const int row = omi * 128 + (wid & 3) * 32 + (wid >> 2) * 16 + (lane >> 3), col = oni * 64 + (lane & 7) * 8;
    unsigned md[2], best = 0u;
    const __amdgpu_buffer_rsrc_t rsk = __builtin_amdgcn_make_buffer_rsrc(hand + HAND_N, 0, (HAND_N + PS_N) * 4, 0x00020000);
    f32x2 xf[2][4];
#pragma unroll
    for (int p = 0; p < 2; ++p)
#pragma unroll
      for (int e = 0; e < 4; ++e) {
        xf[p][e] = f32x2{__uint_as_float(xres[p][e] << 16), __uint_as_float(xres[p][e] & 0xffff0000u)};
        asm volatile("" : "+v"(xf[p][e]));
      }
    LOGDEF;
    LOG();
    const bool rtr = (oni & 7) == 7;
    {
      const int rl = (wid & 3) * 32 + (wid >> 2) * 16 + (lane & 15), rr = omi * 128 + rl;
      int src = (HAND_N + ((omi * 16 + (lane >> 4) * 4) * 128 + rl) * 4) * 4;
      int ko = rtr && (wid >> 2) == (oni >> 3) && lane < NREP ? lane * 1024 + (rr >> 4) * 8 : (HAND_N + PS_N) * 4;
      int bp = (lane >> 3) * 4;
      const float wsum = *LDS_AT(const volatile float, lbase + SPM + 132);
      asm volatile("" : "+v"(src), "+v"(ko), "+v"(bp));
      u32x4 v[4];
      __builtin_amdgcn_s_sleep(MPRE);
#pragma unroll 1
      for (int spin = 0; spin < SPIN_MAX; ++spin) {
        LOG();
#pragma unroll
        for (int i = 0; i < 4; ++i) v[i] = __builtin_amdgcn_raw_buffer_load_b128(rsk, src + (i & 1) * 2048, (i >> 1) * 4096, 16);
        unsigned mx = 0u;
#pragma unroll
        for (int i = 0; i < 4; ++i) mx = umax(umax(umax(mx, v[i][0]), v[i][1]), v[i][2]);
        if (none(mx == SENT)) break;
        __builtin_amdgcn_s_sleep(MSLP);
        asm volatile("" ::: "memory");
      }
      LOGAT(12);
      if (rtr) __builtin_amdgcn_s_setprio(3); else __builtin_amdgcn_s_setprio(LNPRIO);
      double sm[3];
#pragma unroll
      for (int c = 0; c < 3; ++c) {
        const double a = ((double)__uint_as_float(v[0][c]) + (double)__uint_as_float(v[1][c])) + ((double)__uint_as_float(v[2][c]) + (double)__uint_as_float(v[3][c]));
        u32x2 q = __builtin_bit_cast(u32x2, a);
        const auto lo = __builtin_amdgcn_permlane32_swap(q[0], q[0], false, false), hi = __builtin_amdgcn_permlane32_swap(q[1], q[1], false, false);
        q = __builtin_bit_cast(u32x2, __builtin_bit_cast(double, u32x2{lo[0], hi[0]}) + __builtin_bit_cast(double, u32x2{lo[1], hi[1]}));
        const auto l2 = __builtin_amdgcn_permlane16_swap(q[0], q[0], false, false), h2 = __builtin_amdgcn_permlane16_swap(q[1], q[1], false, false);
        sm[c] = __builtin_bit_cast(double, u32x2{l2[0], h2[0]}) + __builtin_bit_cast(double, u32x2{l2[1], h2[1]});
      }
      float mean, var, mb, den;
      ln_from_sums(sm[0], sm[1], mean, var, mb, den);
      const unsigned mdr = f2word(__uint_as_float((__float_as_uint(mb) >> 16) | (__float_as_uint(den) & 0xffff0000u)));
      md[0] = __builtin_amdgcn_ds_bpermute(bp, mdr); md[1] = __builtin_amdgcn_ds_bpermute(bp + 32, mdr);
      if (rtr) {
        const float r = (float)fma(-(double)mb, (double)wsum, sm[2]) * frcp(den);
        const unsigned rb = pack2(r, r);
        unsigned rkey = (rb ^ ((unsigned)((int)rb >> 31) | 0x80000000u)) & 0xffff0000u;
        if (r != r) rkey = 0u;
        const unsigned top = wave_umax(rkey | (unsigned)(2047 - rr));
        const unsigned mdt = __builtin_amdgcn_readlane(mdr, (2047 - (int)(top & 0xffffu)) & 15);
        __builtin_amdgcn_raw_buffer_store_b64(u32x2{top, mdt}, rsk, ko, 0, 16);
        PIN();
        __builtin_amdgcn_s_setprio(LNPRIO);
        __hip_atomic_fetch_add(LDS_AT(unsigned, lane == 0 ? lbase + SPM + 128 : lbase + DUM + lane * 4), 1u, __ATOMIC_RELAXED, __HIP_MEMORY_SCOPE_WORKGROUP);
        if (wid == 2 || wid == 5) STAMP(8, 6);
      }
      LOGAT(13);
    }
    auto keys = [&](const u32x4& v) __attribute__((always_inline)) {
      if (none(umax(umax(v[0], v[1]), umax(v[2], v[3])) == SENT)) {
        best = wave_umax(umax(v[0], v[2])) | 0x8000u;
        *LDS_AT(volatile u32x2, lbase + FLG + 40) = u32x2{best, (unsigned)__builtin_amdgcn_readlane(v[0] > v[2] ? v[1] : v[3], ((2047 - (int)(best & 0x7ffu)) >> 5) & 63)};
        STAMP(8, 11);
      }
    };
    auto ln_rows = [&]() __attribute__((always_inline)) {
#pragma unroll
      for (int p = 0; p < 2; ++p) {
        const float mb_ = __uint_as_float(md[p] << 16), rin = frcp(__uint_as_float(md[p] & 0xffff0000u));
#pragma unroll
        for (int e = 0; e < 4; ++e) {
          const f32x2 h = (xf[p][e] - f32x2{mb_, mb_}) * f32x2{rin, rin} + xf[p][e];
          xres[p][e] = pack2(h[0], h[1]);
        }
      }
      asm volatile("" : "+v"(xres[0]), "+v"(xres[1]));
    };
    if (wid == 0) STAMP(8, 12);
    ln_rows();
    LOGAT(14);
    const __amdgpu_buffer_rsrc_t rsu = __builtin_amdgcn_make_buffer_rsrc(out, 0, S_ * D_ * 2, 0x00020000);
    unsigned fr1 = SENT;
#pragma unroll 1
    for (int i = 0; i < 4 * SPIN_MAX && fr1 == SENT; ++i) fr1 = __builtin_amdgcn_readfirstlane(*LDS_AT(const volatile unsigned, lbase + SPM + 144));
    const int fr2 = (fr1 >> 16) & 0x7ffu;
    fr1 &= 0x7ffu;
    unsigned tmx = __builtin_amdgcn_readfirstlane(*LDS_AT(const volatile unsigned, lbase + SPM + 136)) + KDT;
    if (tmx - (unsigned)__builtin_amdgcn_s_memrealtime() > 1024u) tmx = 0u;
    auto due = [&]() { return tmx == 0u || (int)((unsigned)__builtin_amdgcn_s_memrealtime() - tmx) >= 0; };
    lk_set(15);
    const bool held = !none((row | 8) == ((int)fr1 | 8) || (row | 8) == (fr2 | 8));
#pragma unroll
    for (int p = 0; p < 2; ++p) __builtin_amdgcn_raw_buffer_store_b128(xres[p], rsu, row + 8 * p != (int)fr1 && row + 8 * p != fr2 ? ((row + 8 * p) * D_ + col) * 2 : S_ * D_ * 2, 0, OAUX);
    PIN();
    if (wid == 6) {
      if (rtr) {
#pragma unroll 1
        for (int i = 0; i < SPIN_MAX; ++i) {
          if (__builtin_amdgcn_readfirstlane(*LDS_AT(const volatile unsigned, lbase + SPM + 128)) == 7u) break;
          __builtin_amdgcn_s_sleep(1);
        }
        asm volatile("" ::: "memory");
      }
      while (!due()) __builtin_amdgcn_s_sleep(1);
    }
#pragma unroll 1
    for (int i = 0; i < 4 * SPIN_MAX && !best; ++i) {
      if (wid == 6) {
        LOG();
        keys(__builtin_amdgcn_raw_buffer_load_b128(rsk, (bid % NREP) * 1024 + lane * 16, 0, 16));
        if (!best) __builtin_amdgcn_s_sleep(KSLP);
      } else {
        best = __builtin_amdgcn_readfirstlane(*LDS_AT(const volatile unsigned, lbase + FLG + 40));
        if (!best) __builtin_amdgcn_s_sleep(1);
      }
      asm volatile("" ::: "memory");
    }
    const int idx2 = 2047 - (int)(best & 0x7ffu);
    if (held) {
#pragma unroll
      for (int p = 0; p < 2; ++p) {
        const int r = row + 8 * p;
        __builtin_amdgcn_raw_buffer_store_b128(xres[p], rsu, (r == (int)fr1 || r == fr2) && r != idx2 ? (r * D_ + col) * 2 : S_ * D_ * 2, 0, OAUX);
      }
    }
    if (__builtin_expect(idx2 != (int)fr1 && idx2 != fr2, 0) && !none((row | 8) == (idx2 | 8))) {
#pragma unroll
      for (int p = 0; p < 2; ++p)
        __builtin_amdgcn_raw_buffer_store_b128(u32x4{pack2(xf[p][0][0], xf[p][0][1]), pack2(xf[p][1][0], xf[p][1][1]), pack2(xf[p][2][0], xf[p][2][1]), pack2(xf[p][3][0], xf[p][3][1])},
            __builtin_amdgcn_make_buffer_rsrc(xout, 0, 3 * D_ * 2, 0x00020000), row + 8 * p == idx2 ? (2 * D_ + col) * 2 : S_ * D_ * 2, 0, 16);
      asm volatile("s_waitcnt vmcnt(0)" ::: "memory");
      if (lane == 0) st_ag(gran + GR_F + oni * 32, tag);
    }
  }
  }
}
struct Args2 { Args l[2]; };
extern "C" __global__ __launch_bounds__(768, 3) void layers_kernel(const Args2 args) {
  __shared__ __attribute__((aligned(128))) char smem[163840];
  const unsigned long long t_entry = 0;
  const int tid = threadIdx.x, bid = blockIdx.x;
  int wid = __builtin_amdgcn_readfirstlane(tid >> 6);
  asm volatile("" : "+s"(wid));
  const ArgP ap = (ArgP)__builtin_amdgcn_kernarg_segment_ptr();
  layer_body(wid, bid, (unsigned)(uintptr_t)smem, ap, t_entry);
  LAYER_HERE;
  if (!LAY2) {
    asm volatile(AGAIN :: "{s[0:1]}"(ap + 1), "{s2}"(bid), "{v0}"(wid * 64) : "s90", "s91", "scc", "memory");
    __builtin_unreachable();
  }
}

extern "C" __global__ void end_marker_kernel() {}

extern "C" void kernel_launch(void* const* d_in, const int* in_sizes, int n_in, void* d_out, int out_size,
                              void* d_ws, size_t ws_size, hipStream_t stream) {
  const u16* x0 = (const u16*)d_in[0];
  const u16* wqkv = (const u16*)d_in[1];
  const u16* wo = (const u16*)d_in[2];
  const u16* w_r = (const u16*)d_in[4];
  const u16* wg = (const u16*)d_in[5];
  const u16* wu = (const u16*)d_in[6];
  const u16* wd = (const u16*)d_in[7];
  u16* out = (u16*)d_out;

  char* ws = (char*)d_ws;
  const size_t MB4 = (size_t)S_ * D_ * 2;
  u16* kv1 = (u16*)(ws);
  u16* o1 = (u16*)(ws + 2 * MB4);
  u16* x1 = (u16*)(ws + 3 * MB4);
  unsigned* gran = (unsigned*)(ws + 4 * MB4);
  float* csf = (float*)(gran + GR_F + 16 * 32);
  double* stats = (double*)(csf + 8192);
  unsigned* hand = (unsigned*)(stats + 2 * STAT_N);
  static_assert((GR_F + 16 * 32) % 32 == 0 && (size_t)(GR_F + 16 * 32 + 8192 + 4 * STAT_N + 2 * HAND_N + PS_N) * 4 <= 6u << 20, "");
  u16* x3 = (u16*)(hand + HAND_N + GRAN_N + Y_N);
  u16* wc = (u16*)((((uintptr_t)ws + 4 * MB4 + (6u << 20) + 2047) & ~(uintptr_t)2047) + (((uintptr_t)wqkv + 128) & 2047));
  u16* kv2 = (u16*)(ws + 8 * MB4);
  u16* o2 = (u16*)(ws + 10 * MB4);
  Args2 a;
  for (int l = 0; l < 2; ++l) {
    Args& r = a.l[l];
    r.lay = l; r.a = l ? x1 : x0; r.kv = l ? kv2 : kv1; r.o = l ? o2 : o1; r.xo = l ? x3 : x1; r.so = l ? stats + STAT_N : stats;
    r.W = wqkv; r.wc = wc; r.wg = wg; r.wu = wu; r.wd = wd; r.wo = wo; r.w_r = w_r; r.gran = gran; r.csf = csf; r.stats = stats; r.hand = hand; r.out = out;
  }
  layers_kernel<<<256, 768, 0, stream>>>(a);
}
```

```cpp
#include <hip/hip_runtime.h>
#include <hip/hip_bf16.h>
#include <stdint.h>
#include <type_traits>
#include <utility>

typedef unsigned short u16;
typedef short s16x4 __attribute__((ext_vector_type(4)));
typedef short s16x8 __attribute__((ext_vector_type(8)));
typedef float f32x16 __attribute__((ext_vector_type(16)));
typedef float f32x4 __attribute__((ext_vector_type(4)));
typedef unsigned int u32x4 __attribute__((ext_vector_type(4)));
typedef unsigned int u32x2 __attribute__((ext_vector_type(2)));
typedef __bf16 bf16x8 __attribute__((ext_vector_type(8)));
typedef __bf16 bf16x16 __attribute__((ext_vector_type(16)));

__device__ __forceinline__ int fresh_lane() {
  int l;
  asm volatile("v_mbcnt_lo_u32_b32 %0, -1, 0\n\tv_mbcnt_hi_u32_b32 %0, -1, %0" : "=v"(l));
  return l;
}
#define STAMP(k, i)
#define STAMPV(k, i, v)
#define LOGDEF
#define LOG()
#define LOGAT(k)
#define lk_set(k)
#define LOGOUT(slot)
#define TE(grp, k)
#define TEARG
#define TEPASS
constexpr int S_ = 2048, D_ = 1024, H_ = 32, F_ = 3584;

__device__ __forceinline__ int kpos(int r) { return (r & ~12) | ((r & 4) << 1) | ((r & 8) >> 1); }
__device__ __forceinline__ float bf2f(u16 v) { return __uint_as_float(((unsigned)v) << 16); }
__device__ __forceinline__ u16 f2bf(float f) {
  __hip_bfloat16 b = __float2bfloat16(f);
  return *reinterpret_cast<u16*>(&b);
}
typedef float f32x2 __attribute__((ext_vector_type(2)));
typedef __bf16 bf16x2 __attribute__((ext_vector_type(2)));
__device__ __forceinline__ unsigned pack2(float lo, float hi) {
  f32x2 v = {lo, hi};
  return __builtin_bit_cast(unsigned, __builtin_convertvector(v, bf16x2));
}
__device__ __forceinline__ float rbf(float f) { return __uint_as_float(pack2(f, f) << 16); }
__device__ __forceinline__ void rbf2(float& a, float& b) {
  const unsigned p = pack2(a, b);
  a = __uint_as_float(p << 16); b = __uint_as_float(p & 0xffff0000u);
}

__device__ __forceinline__ float fadd(float a, float b) { float r = a + b; asm("" : "+v"(r)); return r; }
template <int CTRL, int RMASK>
__device__ __forceinline__ float dpp_add(float v) {
  const int t = __builtin_amdgcn_update_dpp(0, __float_as_int(v), CTRL, RMASK, 0xf, false);
  return v + __int_as_float(t);
}
__device__ __forceinline__ float wave_sum(float v) {
  v = dpp_add<0x111, 0xf>(v);
  v = dpp_add<0x112, 0xf>(v);
  v = dpp_add<0x114, 0xf>(v);
  v = dpp_add<0x118, 0xf>(v);
  v = dpp_add<0x142, 0xa>(v);
  v = dpp_add<0x143, 0xc>(v);
  return __int_as_float(__builtin_amdgcn_readlane(__float_as_int(v), 63));
}

template <int CTRL, int RMASK>
__device__ __forceinline__ unsigned dpp_umax(unsigned v) {
  const unsigned t = (unsigned)__builtin_amdgcn_update_dpp(0, (int)v, CTRL, RMASK, 0xf, false);
  return v > t ? v : t;
}
__device__ __forceinline__ unsigned wave_umax(unsigned v) {
  v = dpp_umax<0x111, 0xf>(v); v = dpp_umax<0x112, 0xf>(v); v = dpp_umax<0x114, 0xf>(v); v = dpp_umax<0x118, 0xf>(v);
  v = dpp_umax<0x142, 0xa>(v); v = dpp_umax<0x143, 0xc>(v);
  return (unsigned)__builtin_amdgcn_readlane((int)v, 63);
}

#define NREP 32
constexpr unsigned SENT = 0xFFFFFFFFu;
constexpr int GRAN_N = 256 * NREP;
constexpr int Y_N = F_ / 16 * D_;
constexpr int X2_N = D_ / 2;
constexpr int XS_N = 128;
constexpr int P4_N = 3 * D_ * 16;
constexpr int PA_N = H_ * 8 * 64;
constexpr int HAND_N = GRAN_N + Y_N + X2_N + XS_N + P4_N + PA_N;
constexpr int MD_O = 16 * 16 * 128 * 4;
constexpr int PK_O = MD_O;
constexpr int PP_O = PK_O + 128 * NREP;
constexpr int Y3_O = PP_O + H_ * S_;
constexpr int PS_N = Y3_O + Y_N;
constexpr int GR_ROW = 64, GR_COL = GR_ROW + 4 * S_, GR_KV = GR_COL + 16 * 16 * 192;
#define RO 16
#define RK 16
#define RX 1
constexpr int GR_O = GR_KV + 2 * 16 * 32 * RK, GR_X = GR_O + 2 * 16 * 64 * RO, GR_N = GR_X + 16 * 128 * RX;
constexpr int GR_F = GR_N + 256 * 64;
constexpr int STAT_N = 3 * S_;

__device__ __forceinline__ void st_ag(unsigned* p, unsigned v) { __hip_atomic_store(p, v, __ATOMIC_RELAXED, __HIP_MEMORY_SCOPE_AGENT); }
__device__ __forceinline__ unsigned ld_ag(const unsigned* p) { return __hip_atomic_load(p, __ATOMIC_RELAXED, __HIP_MEMORY_SCOPE_AGENT); }
__device__ __forceinline__ double ld_ag(const double* p) { return __hip_atomic_load(p, __ATOMIC_RELAXED, __HIP_MEMORY_SCOPE_AGENT); }
__device__ __forceinline__ unsigned f2word(float f) { const unsigned u = __float_as_uint(f); return u < 0xFFFFFFFEu ? u : 0xFFFFFFFEu; }
constexpr int SPIN_MAX = 1 << 18;
__device__ __forceinline__ unsigned umax(unsigned a, unsigned b) { return a > b ? a : b; }
__device__ __forceinline__ bool none(bool p) { return __builtin_amdgcn_ballot_w64(p) == 0ull; }

__device__ __forceinline__ void ln_from_sums(double s1, double s2, float& mean, float& var, float& mb, float& den) {
  const double md = s1 * (1.0 / 1024.0);
  const double vd = fma(-md, md, s2 * (1.0 / 1024.0));
  mean = (float)md; var = fmaxf((float)vd, 0.f);
  mb = rbf(mean);
  den = rbf(__builtin_amdgcn_sqrtf(rbf(rbf(var) + 1e-5f)));
}
__device__ __forceinline__ float frcp(float x) { return __builtin_amdgcn_rcpf(x); }
__device__ __forceinline__ void ln_apply2(float& a, float& b, float mb, float den, float rin) {
  a -= mb; b -= mb;
  rbf2(a, b);
  float qa = a * rin, qb = b * rin;
  qa = fmaf(fmaf(-qa, den, a), rin, qa);
  qb = fmaf(fmaf(-qb, den, b), rin, qb);
  rbf2(qa, qb);
  a = qa; b = qb;
}

#define CODE_ADDR(SYM, out64)                                                                                          \
  do {                                                                                                                 \
    unsigned lo_, hi_;                                                                                                 \
    asm volatile("s_getpc_b64 s[90:91]\n\ts_add_u32 s90, s90, " SYM "@rel32@lo+4\n\ts_addc_u32 s91, s91, " SYM          \
                 "@rel32@hi+12\n\ts_mov_b32 %0, s90\n\ts_mov_b32 %1, s91"                                             \
                 : "=s"(lo_), "=s"(hi_) :: "s90", "s91", "scc");                                                        \
    out64 = ((unsigned long long)hi_ << 32) | lo_;                                                                     \
  } while (0)

#define QNX 4
constexpr int QMX = 32 / QNX, QNG = 16 / QNX, QMG = 8 / QNG;

__device__ __forceinline__ unsigned touch(const void* p) { return *reinterpret_cast<const unsigned*>(p); }

__device__ __forceinline__ unsigned touch_code(unsigned long long lo, unsigned long long hi, int pc, int lane) {
  const unsigned long long p = lo + (unsigned)(pc * 8192 + lane * 128);
  return p + 4 <= hi ? *reinterpret_cast<__attribute__((address_space(1))) const unsigned*>(p) : 0u;
}

template <bool SC = false>
__device__ __forceinline__ void glds16s(const void* sbase, unsigned voff, unsigned lds_dst) {
  unsigned keep;
  if (SC)
    asm volatile("s_mov_b32 %0, m0\n\ts_mov_b32 m0, %3\n\ts_nop 2\n\tglobal_load_lds_dwordx4 %1, %2 sc1\n\ts_mov_b32 m0, %0"
                 : "=&s"(keep) : "v"(voff), "s"(sbase), "s"(lds_dst) : "memory");
  else
  asm volatile("s_mov_b32 %0, m0\n\ts_mov_b32 m0, %3\n\ts_nop 2\n\tglobal_load_lds_dwordx4 %1, %2\n\ts_mov_b32 m0, %0"
               : "=&s"(keep) : "v"(voff), "s"(sbase), "s"(lds_dst) : "memory");
}
template <bool SC>
__device__ __forceinline__ void glds16x4(const void* sbase, unsigned v0, unsigned v1, unsigned v2, unsigned v3, unsigned lds_dst) {
  unsigned keep;
  if (SC)
    asm volatile("s_mov_b32 %0, m0\n\ts_mov_b32 m0, %6\n\ts_nop 2\n\tglobal_load_lds_dwordx4 %1, %5 sc1\n\tglobal_load_lds_dwordx4 %2, %5 offset:1024 sc1\n\t"
                 "global_load_lds_dwordx4 %3, %5 offset:2048 sc1\n\tglobal_load_lds_dwordx4 %4, %5 offset:3072 sc1\n\ts_mov_b32 m0, %0"
                 : "=&s"(keep) : "v"(v0), "v"(v1), "v"(v2), "v"(v3), "s"(sbase), "s"(__builtin_amdgcn_readfirstlane(lds_dst)) : "memory");
  else
    asm volatile("s_mov_b32 %0, m0\n\ts_mov_b32 m0, %6\n\ts_nop 2\n\tglobal_load_lds_dwordx4 %1, %5\n\tglobal_load_lds_dwordx4 %2, %5 offset:1024\n\t"
                 "global_load_lds_dwordx4 %3, %5 offset:2048\n\tglobal_load_lds_dwordx4 %4, %5 offset:3072\n\ts_mov_b32 m0, %0"
                 : "=&s"(keep) : "v"(v0), "v"(v1), "v"(v2), "v"(v3), "s"(sbase), "s"(__builtin_amdgcn_readfirstlane(lds_dst)) : "memory");
}
constexpr float QSCALE = 0.17677669529663687f * 1.4426950408889634f;

__device__ __forceinline__ unsigned min4(u32x4 v) { const unsigned a = v[0] < v[1] ? v[0] : v[1], b = v[2] < v[3] ? v[2] : v[3]; return a < b ? a : b; }
#define LDS_AT(T, a) ((__attribute__((address_space(3))) T*)(uintptr_t)(a))
__device__ __forceinline__ u32x4 lds_rd4(unsigned a) { return *LDS_AT(const volatile u32x4, a); }
__device__ __forceinline__ void lds_wr(unsigned a, unsigned v) { *LDS_AT(volatile unsigned, a) = v; }
__device__ __forceinline__ void split3(float v, unsigned& p0, unsigned& p1, unsigned& p2) {
  p0 = pack2(v, v) & 0xffffu; v -= __uint_as_float(p0 << 16);
  p1 = pack2(v, v) & 0xffffu; v -= __uint_as_float(p1 << 16);
  p2 = pack2(v, v) & 0xffffu;
}

#define PIN() __builtin_amdgcn_sched_barrier(0)
template <int B, class F, int... I>
__device__ __forceinline__ void static_for_(F&& f, std::integer_sequence<int, I...>) { (f(std::integral_constant<int, B + I>{}), ...); }
template <int B, int E, class F>
__device__ __forceinline__ void static_for(F&& f) { static_for_<B>(f, std::make_integer_sequence<int, E - B>{}); }

#define TPRE 60
#define TPSL 0
#define FCPRE 44
#define FCSLP 2
#define SIGAT 24
#define XPRIO 1
#define OPD 6
#define LAUX 16
#define CSLP 2
#define EPRIO 3
#define OPRIO 1
#define XSLP 1
#define CSL1 8
#define XAUX 16
__device__ __forceinline__ void oproj_tail(const bool RES2, const int tid, const unsigned lbase, const unsigned flg, const int tmi, const int tni, const int idx,
                                           unsigned* osig, unsigned* xsig, const unsigned tag,
                                           const u16* A, u16* out,
                                           const u16* res, const u16* __restrict__ w_r, double* stats,
                                           const double* stats_in, unsigned* hand, u32x4 (&xres)[2] TEARG) {
  constexpr int ldw = D_, PD = OPD;
  const int lane = tid & 63, wid = __builtin_amdgcn_readfirstlane(tid >> 6);
  const int m0 = tmi * 128, n0 = tni * 64;
  const int dk = RES2 ? 7 : 3;
  if (wid == 0) STAMP(dk, 0);
  TE(1, 3);
  asm volatile("" : "+s"(A) :: "memory");
  PIN();
  const int row = m0 + (wid & 3) * 32 + (wid >> 2) * 16 + (lane >> 3), col = n0 + (lane & 7) * 8;
  const unsigned tb = lbase + 147456;
  typedef __attribute__((address_space(1))) double* gdp;
  typedef __attribute__((address_space(1))) unsigned* gup;
  unsigned tr, oad;
  gdp pst;
  gup pps;
  int xo;
  auto setup = [&]() __attribute__((always_inline)) {
    tr = tb + (row - m0) * 128 + (((lane ^ (lane >> 3)) & 7) << 4);
    pst = (gdp)(stats + ((lane & 3) == 3 ? 0 : (lane & 3)) * S_ + row + 8 * ((lane >> 2) & 1));
    pps = (gup)(hand + 2 * HAND_N + ((tmi * 16 + tni) * 128 + row - m0 + 8 * ((lane >> 2) & 1)) * 4 + (lane & 3));
    xo = (row * ldw + col) * 2;
    asm volatile("" : "+v"(tr), "+v"(pst), "+v"(pps), "+v"(xo));
    PIN();
  };
  {
    oad = lane == 0 ? flg + 24 : flg + 64 + lane * 4;
    gup ps = (gup)(osig + lane * 64);
    asm volatile("" : "+v"(oad), "+v"(ps));
    asm volatile("s_waitcnt vmcnt(1)" ::: "memory");
    TE(2, 1);
    const unsigned old = __hip_atomic_fetch_add(LDS_AT(unsigned, oad), 1u, __ATOMIC_RELAXED, __HIP_MEMORY_SCOPE_WORKGROUP);
    if (__builtin_amdgcn_readfirstlane(old) == 7u && lane < RO) __hip_atomic_store(ps, tag, __ATOMIC_RELAXED, __HIP_MEMORY_SCOPE_AGENT);
    PIN();
    TE(2, 2);
    if (wid == 0) STAMP(dk, 12);
  }
  auto announce = [&]() __attribute__((always_inline)) {
    asm volatile("" ::: "memory");
    PIN();
    __builtin_amdgcn_s_barrier();
    PIN();
    if (wid == 0) STAMP(dk, 1);
    TE(2, 3); TE(4, 0); TE(7, 1);
  };
  u32x4 rwr;
  auto sums = [&](const u32x4& x0, const u32x4& x1) __attribute__((always_inline)) {
    float f[2][3];
#pragma unroll
    for (int p = 0; p < 2; ++p) {
      f[p][0] = f[p][1] = f[p][2] = 0.f;
#pragma unroll
      for (int e = 0; e < 4; ++e) {
        const unsigned xu = p ? x1[e] : x0[e], wu = rwr[e];
        const bf16x2 x = __builtin_bit_cast(bf16x2, xu);
        f[p][0] = __builtin_amdgcn_fdot2_f32_bf16(x, __builtin_bit_cast(bf16x2, 0x3f803f80u), f[p][0], false);
        f[p][1] = __builtin_amdgcn_fdot2_f32_bf16(x, x, f[p][1], false);
        f[p][2] = __builtin_amdgcn_fdot2_f32_bf16(x, __builtin_bit_cast(bf16x2, wu), f[p][2], false);
      }
    }
    auto addx = [&](float keep, float give, auto ctl) {
      return keep + __int_as_float(__builtin_amdgcn_update_dpp(0, __float_as_int(give), decltype(ctl)::value, 0xf, 0xf, false));
    };
    const bool b2 = lane & 4, b1 = lane & 2, z0 = (lane & 3) == 0, z1 = (lane & 3) == 1;
    float g[3];
#pragma unroll
    for (int c = 0; c < 3; ++c) g[c] = addx(b2 ? f[1][c] : f[0][c], b2 ? f[0][c] : f[1][c], std::integral_constant<int, 0x141>{});
    const float u1 = addx(b1 ? g[2] : g[0], b1 ? g[0] : g[2], std::integral_constant<int, 0x4e>{});
    const float u2 = addx(g[1], g[1], std::integral_constant<int, 0x4e>{});
    return addx(z1 ? u2 : u1, z0 ? u2 : u1, std::integral_constant<int, 0xb1>{});
  };
  if (wid < 4) {
    const __amdgpu_buffer_rsrc_t rsa = __builtin_amdgcn_make_buffer_rsrc(const_cast<u16*>(A), 0, S_ * D_ * 2, 0x00020000);
    int sa0 = (tmi * 4 + wid) * 65536, va0 = (lane >> 4) * 512 + (lane & 15) * 16;
    asm volatile("" : "+v"(va0));
    asm volatile("" : "+s"(sa0));
    auto lda = [&](int i, int h) { return __builtin_amdgcn_raw_buffer_load_b128(rsa, va0 + (i & 1) * 2048 + h * 256, sa0 + (i >> 1) * 4096, LAUX); };
    unsigned wad[4], tw[4];
    f32x4 acc[2][4];
    const int k = (lane >> 5) * 16 + ((lane >> 2) & 7);
#pragma unroll
    for (int j = 0; j < 4; ++j) {
      wad[j] = lbase + k * 128 + (((2 * j) ^ (k & 6)) << 4) + 8 * (lane & 3);
      tw[j] = tb + wid * 4096 + (lane & 15) * 128 + (((2 * j + (lane >> 5)) ^ (lane & 7)) << 4) + ((lane >> 4) & 1) * 8;
      asm volatile("" : "+v"(wad[j]), "+v"(tw[j]));
    }
    auto ldw_ = [&](int i, int j) {
      const unsigned a = wad[j] + (i & 3) * 4096;
      s16x4 lo = __builtin_amdgcn_ds_read_tr16_b64_v4i16(LDS_AT(s16x4, a));
      s16x4 h4 = __builtin_amdgcn_ds_read_tr16_b64_v4i16(LDS_AT(s16x4, a + 1024));
      return (s16x8)__builtin_shufflevector(lo, h4, 0, 1, 2, 3, 4, 5, 6, 7);
    };
    u32x4 id[2];
#pragma unroll
    for (int j = 0; j < 2; ++j) {
#pragma unroll
      for (int e = 0; e < 4; ++e) id[j][e] = (lane >> 4) == 2 * j + ((lane >> 3) & 1) && ((lane & 7) >> 1) == e ? ((lane & 1) ? 0x3f800000u : 0x3f80u) : 0u;
      asm volatile("" : "+v"(id[j]));
    }
    unsigned rad = tb + wid * 4096 + (lane & 15) * 128 + (((lane >> 4) ^ (lane & 7)) << 4), rfa = flg + 36 + wid;
    asm volatile("" : "+v"(rad), "+v"(rfa));
    u32x4 rb[2][2];
    rwr = *reinterpret_cast<const u32x4*>(w_r + col);
    setup();
    asm volatile("" : "+v"(rwr));
    announce();
    u32x4 af[PD][2];
    s16x8 wf[2][4];
#pragma unroll
    for (int i = 0; i < PD; ++i) { af[i][0] = lda(i, 0); af[i][1] = lda(i, 1); }
    PIN();
    __builtin_amdgcn_s_setprio(OPRIO);
#pragma unroll
    for (int q = 0; q < 4; ++q) rb[q >> 1][q & 1] = lds_rd4((rad ^ ((q & 1) * 64)) + (q >> 1) * 2048);
    PIN();
#pragma unroll
    for (int j = 0; j < 4; ++j)
#pragma unroll
      for (int h = 0; h < 2; ++h) { asm volatile("v_mfma_f32_16x16x32_bf16 %0, %1, %2, 0" : "=&v"(acc[h][j]) : "v"(id[j & 1]), "v"(rb[h][j >> 1])); PIN(); }
#pragma unroll 1
    for (int spin = 0; spin < SPIN_MAX; ++spin)
      if (__builtin_amdgcn_readfirstlane(*LDS_AT(const volatile unsigned, flg)) >= 40u) break;
    asm volatile("" : "+v"(va0) :: "memory");
    PIN();
#pragma unroll
    for (int j = 0; j < 4; ++j) wf[0][j] = ldw_(0, j);
    s16x4 wlo;
    static_for<0, 32>([&](auto ic) __attribute__((always_inline)) {
      constexpr int i = decltype(ic)::value;
      if (i == SIGAT) { __hip_atomic_fetch_add(LDS_AT(unsigned, oad + 4), 1u, __ATOMIC_RELAXED, __HIP_MEMORY_SCOPE_WORKGROUP); PIN(); }
#pragma unroll
      for (int g = 0; g < 8; ++g) {
        const int j = g >> 1, h = g & 1;
        asm volatile("v_mfma_f32_16x16x32_bf16 %0, %1, %2, %0" : "+v"(acc[h][j]) : "v"(wf[i & 1][j]), "v"(af[i % PD][h]));
        PIN();
        if (i == 15 && !(g & 1)) { wad[g >> 1] += 65536; asm volatile("" : "+v"(wad[g >> 1])); }
        if (i + 1 < 32) {
          const s16x4 v = __builtin_amdgcn_ds_read_tr16_b64_v4i16(LDS_AT(s16x4, wad[g >> 1] + ((i + 1) & 15) * 4096 + (g & 1) * 1024));
          if (g & 1) wf[(i + 1) & 1][g >> 1] = (s16x8)__builtin_shufflevector(wlo, v, 0, 1, 2, 3, 4, 5, 6, 7);
          else wlo = v;
          PIN();
        }
        if (g >= 6 && i + PD < 32) { af[i % PD][h] = lda(i + PD, h); PIN(); }
        if (i + 1 < 32 && i != SIGAT && i + 1 != SIGAT) {
          if (g == 3) { __builtin_amdgcn_s_waitcnt(0xc47f); PIN(); }
          if (g == 7 && i + PD < 32) { __builtin_amdgcn_s_waitcnt(0x0470 | (2 * PD - 2)); PIN(); }
          if (g == 7 && i + PD >= 32) { __builtin_amdgcn_s_waitcnt(0xc47f); PIN(); }
        }
      }
    });
    asm volatile("s_nop 3" : "+v"(acc[0][0]), "+v"(acc[1][0]));
    TE(4, 1); TE(7, 2);
    if (wid == 0) STAMP(dk, 17);
    if (wid == 1 && !RES2) STAMP(4, 0);
#pragma unroll
    for (int j = 0; j < 4; ++j)
#pragma unroll
      for (int h = 0; h < 2; ++h) {
        *LDS_AT(u32x2, tw[j] + h * 2048) = u32x2{pack2(acc[h][j][0], acc[h][j][1]), pack2(acc[h][j][2], acc[h][j][3])};
        PIN();
      }
    asm volatile("" ::: "memory");
    *LDS_AT(volatile unsigned char, rfa) = (unsigned char)2;
#pragma unroll
    for (int p = 0; p < 2; ++p) xres[p] = lds_rd4(tr + p * 1024);
    PIN();
    if (!RES2) {
      const __amdgpu_buffer_rsrc_t rso = __builtin_amdgcn_make_buffer_rsrc(out, 0, S_ * D_ * 2, 0x00020000);
      const __amdgpu_buffer_rsrc_t rsf = __builtin_amdgcn_make_buffer_rsrc(xsig + wid, 0, RX * 512, 0x00020000);
      const u32x4 x2 = lds_rd4(tr + 2048), x3 = lds_rd4(tr + 3072);
      PIN();
#pragma unroll
      for (int p = 0; p < 4; ++p) { __builtin_amdgcn_raw_buffer_store_b128(p < 2 ? xres[p] : (p < 3 ? x2 : x3), rso, xo + p * (8 * ldw * 2), 0, XAUX); PIN(); }
      TE(4, 2); TE(5, 0);
      const float s0 = sums(xres[0], xres[1]), s1 = sums(x2, x3);
      if ((lane & 3) != 3) { __builtin_amdgcn_global_atomic_fadd_f64(pst, (double)s0); __builtin_amdgcn_global_atomic_fadd_f64(pst + 16, (double)s1); }
      PIN();
      TE(4, 3); TE(5, 1);
      asm volatile("s_waitcnt vmcnt(2)" ::: "memory");
      __builtin_amdgcn_raw_buffer_store_b32(tag, rsf, lane * 512 + 256, 0, 16);
      *LDS_AT(volatile unsigned char, rfa) = (unsigned char)3;
      PIN();
      TE(5, 2);
      asm volatile("s_waitcnt vmcnt(1)" ::: "memory");
      __builtin_amdgcn_raw_buffer_store_b32(tag, rsf, lane * 512, 0, 16);
      PIN();
      TE(5, 3);
      __builtin_amdgcn_s_setprio(0);
      return;
    }
    TE(4, 2); TE(5, 0);
  } else {
    u32x4 rres[4];
    double is1[4], is2[4];
#pragma unroll
    for (int p = 0; p < 4; ++p)
      rres[p] = __builtin_amdgcn_raw_buffer_load_b128(__builtin_amdgcn_make_buffer_rsrc(const_cast<u16*>(res), 0, S_ * D_ * 2, 0x00020000), ((row - 16 + 8 * p) * ldw + col) * 2, 0, 16);
    rwr = *reinterpret_cast<const u32x4*>(w_r + col);
#pragma unroll
    for (int p = 0; p < 4; ++p) { is1[p] = ld_ag(stats_in + row - 16 + 8 * p); is2[p] = ld_ag(stats_in + S_ + row - 16 + 8 * p); }
    PIN();
    setup();
    if (RES2) {
#pragma unroll
      for (int p = 0; p < 4; ++p) {
        float mean, var, mb, den;
        ln_from_sums(is1[p], is2[p], mean, var, mb, den);
        const float rin = frcp(den);
#pragma unroll
        for (int e = 0; e < 4; ++e) {
          const f32x2 x = {__uint_as_float(rres[p][e] << 16), __uint_as_float(rres[p][e] & 0xffff0000u)};
          const f32x2 h = (x - f32x2{mb, mb}) * f32x2{rin, rin} + x;
          rres[p][e] = pack2(h[0], h[1]);
        }
        asm volatile("" : "+v"(rres[p]));
      }
      PIN();
    }
    if ((idx >> 5) == tmi * 4 + wid - 4) {
      const int dr = idx - (row - 16);
      u32x4 qq;
#pragma unroll 1
      for (int i = 0; i < SPIN_MAX; ++i) {
        qq = __builtin_amdgcn_raw_buffer_load_b128(__builtin_amdgcn_make_buffer_rsrc(hand, 0, HAND_N * 4, 0x00020000), (GRAN_N + Y_N) * 4 + col * 2, 0, 16);
        if (none(umax(umax(qq[0], qq[1]), umax(qq[2], qq[3])) == SENT)) break;
        asm volatile("" ::: "memory");
      }
#pragma unroll
      for (int p = 0; p < 4; ++p)
#pragma unroll
        for (int e = 0; e < 4; ++e) rres[p][e] = dr == 8 * p ? qq[e] : rres[p][e];
    }
#pragma unroll 1
    for (int i = 0; i < 4 * SPIN_MAX; ++i)
      if (__builtin_amdgcn_readfirstlane(*LDS_AT(const volatile unsigned, flg + 16)) >= 12u) break;
#pragma unroll
    for (int p = 0; p < 4; ++p) *LDS_AT(volatile u32x4, tr - 2048 + p * 1024) = rres[p];
    asm volatile("s_waitcnt lgkmcnt(0)" ::: "memory");
    announce();
    TE(4, 1); TE(7, 2);
    asm volatile("s_waitcnt vmcnt(0)" : "+v"(rwr) :: "memory");
    if (!RES2) return;
    __builtin_amdgcn_s_sleep(TPRE);
#pragma unroll 1
    for (int i = 0; i < 4 * SPIN_MAX; ++i) {
      if (__builtin_amdgcn_readfirstlane(*LDS_AT(const volatile unsigned char, flg + 32 + wid)) == 2u) break;
      __builtin_amdgcn_s_sleep(TPSL);
    }
    asm volatile("" ::: "memory");
    __builtin_amdgcn_s_setprio(XPRIO);
    PIN();
    TE(4, 2); TE(5, 0);
#pragma unroll
    for (int p = 0; p < 2; ++p) xres[p] = lds_rd4(tr + p * 1024);
  }
  TE(4, 3); TE(7, 3);
  const float sv = sums(xres[0], xres[1]);
  if (wid == 0) STAMP(dk, 14);
  if ((lane & 3) != 3) __hip_atomic_store(pps, f2word(sv), __ATOMIC_RELAXED, __HIP_MEMORY_SCOPE_AGENT);
  PIN();
  __builtin_amdgcn_s_setprio(0);
  __hip_atomic_fetch_add(LDS_AT(unsigned, oad), 1u, __ATOMIC_RELAXED, __HIP_MEMORY_SCOPE_WORKGROUP);
  TE(5, 1); TE(5, 2);
  PIN();
}

#define MFMA32(a, b, c) __builtin_amdgcn_mfma_f32_32x32x16_bf16(a, b, c, 0, 0, 0)
#define MFMA16(a, b, c) __builtin_amdgcn_mfma_f32_16x16x32_bf16(a, b, c, 0, 0, 0)

#define PLO(x) __builtin_bit_cast(bf16x16, __builtin_shufflevector(x, x, 0, 1, 2, 3, 4, 5, 6, 7))
#define PHI(x) __builtin_bit_cast(bf16x16, __builtin_shufflevector(x, x, 8, 9, 10, 11, 12, 13, 14, 15))
#define SMF32(a, b, c) __builtin_amdgcn_smfmac_f32_32x32x32_bf16(__builtin_bit_cast(bf16x8, a), b, c, sidx, 0, 0)
#define SMF16(a, b, c) __builtin_amdgcn_smfmac_f32_16x16x64_bf16(__builtin_bit_cast(bf16x8, a), b, c, sidx, 0, 0)

__device__ __forceinline__ void patch_kv(unsigned tab, int r, s16x8& k0, s16x8& k1, s16x8& v0, s16x8& v1, bool on = true) {
  const int lane = fresh_lane(), hi = lane >> 5;
  if (on && (lane & 31) == kpos(r)) {
    k0 = *LDS_AT(const s16x8, tab + hi * 16); k1 = *LDS_AT(const s16x8, tab + 32 + hi * 16);
  }
  const short vv = *LDS_AT(const short, tab + 128 + (lane & 31) * 2);
  const int js = (on && hi == ((r >> 2) & 1)) ? (r >> 4) * 8 + ((r >> 3) & 1) * 4 + (r & 3) : -1;
#pragma unroll
  for (int e = 0; e < 8; ++e) { v0[e] = js == e ? vv : v0[e]; v1[e] = js == 8 + e ? vv : v1[e]; }
}

__device__ __forceinline__ void attn_safe(const u16* KV, int tile0, s16x8 q0, s16x8 q1,
                                       f32x16& o, float& l_out, int ptile, int prow, unsigned ptab) {
  const int lane = fresh_lane();
  float m = -INFINITY, l = 0.f;
  const int sidx = 0xDDDDDDDD;
  const __amdgpu_buffer_rsrc_t rs = __builtin_amdgcn_make_buffer_rsrc(const_cast<u16*>(KV), 0, 2 * S_ * D_ * 2, 0x00020000);
#pragma unroll
  for (int e = 0; e < 16; ++e) o[e] = 0.f;
  for (int tt = 0; tt < 64; ++tt) {
    const int t = tile0 + tt, off = t * 2048 + lane * 16;
    s16x8 k0 = __builtin_bit_cast(s16x8, __builtin_amdgcn_raw_buffer_load_b128(rs, off, 0, 16));
    s16x8 k1 = __builtin_bit_cast(s16x8, __builtin_amdgcn_raw_buffer_load_b128(rs, off + 1024, 0, 16));
    s16x8 v0 = __builtin_bit_cast(s16x8, __builtin_amdgcn_raw_buffer_load_b128(rs, off + S_ * D_ * 2, 0, 16));
    s16x8 v1 = __builtin_bit_cast(s16x8, __builtin_amdgcn_raw_buffer_load_b128(rs, off + S_ * D_ * 2 + 1024, 0, 16));
    if (t == ptile) patch_kv(ptab, prow, k0, k1, v0, v1);
    f32x16 sc;
#pragma unroll
    for (int e = 0; e < 16; ++e) sc[e] = 0.f;
    sc = MFMA32(k0, q0, sc); sc = MFMA32(k1, q1, sc);
    float mt = sc[0];
#pragma unroll
    for (int e = 1; e < 16; ++e) mt = fmaxf(mt, sc[e]);
    mt = fmaxf(mt, __shfl_xor(mt, 32, 64));
    const float mn = fmaxf(m, mt);
    const float alpha = __builtin_amdgcn_exp2f(m - mn);
    m = mn;
#pragma unroll
    for (int e = 0; e < 16; ++e) sc[e] = __builtin_amdgcn_exp2f(sc[e] - mn);
    float ls = 0.f;
#pragma unroll
    for (int e = 0; e < 16; ++e) ls += __uint_as_float(__float_as_uint(sc[e]) & 0xffff0000u);
    l = l * alpha + ls;
#pragma unroll
    for (int e = 0; e < 16; ++e) o[e] *= alpha;
    o = SMF32(v0, PLO(sc), o); o = SMF32(v1, PHI(sc), o);
  }
  l_out = l + __shfl_xor(l, 32, 64);
}

__device__ __forceinline__ float wave_fmax(float v) {
  asm volatile("s_nop 1\n\tv_max_f32_dpp %0, %0, %0 row_shr:1 row_mask:0xf bank_mask:0xf\n\ts_nop 1\n\tv_max_f32_dpp %0, %0, %0 row_shr:2 row_mask:0xf bank_mask:0xf\n\t"
               "s_nop 1\n\tv_max_f32_dpp %0, %0, %0 row_shr:4 row_mask:0xf bank_mask:0xf\n\ts_nop 1\n\tv_max_f32_dpp %0, %0, %0 row_shr:8 row_mask:0xf bank_mask:0xf\n\t"
               "s_nop 1\n\tv_max_f32_dpp %0, %0, %0 row_bcast:15 row_mask:0xa bank_mask:0xf\n\ts_nop 1\n\tv_max_f32_dpp %0, %0, %0 row_bcast:31 row_mask:0xc bank_mask:0xf\n\ts_nop 0"
               : "+v"(v));
  return __int_as_float(__builtin_amdgcn_readlane(__float_as_int(v), 63));
}

#define PSLP 8
#define WSLP 3
#define PRE1 8
#define PRE5 8
#define W3SLP 4
#define FSTART 10
#define OAUX 16
#define OMX 4
#define T0 2u
#define TW 10
#define HW 2
#define HP1 1
#define POLLAT 11
#define X2AT 2
#define HPRIO 3
#define SSLP 1
#define SPRE 6
#define WGAPS {0, 1, 7, 8, 9, 10, 11, 12}
#define KVS 4
#define PNS 12
#define G0 5u
#define GS 2u
#define IAT 1u
#define YSLP 2
#define ISLP 8
#define KSLP 2
#define UAT 13u
#define UHW 0
#define PPSLP 2
#define XRSLP 2
#define MPRE 20
#define OSLP 2
#define YD1 150
#define YD2 180
#define KDT 200
#define XDT 65
#define R2PRIO 0
#define LNPRIO 1
#define MSLP 4
#define BARG 0
struct Args {
  long long lay;
  const u16* a; u16* kv; u16* o; u16* xo; double* so;
  const u16* W; u16* wc;
  const u16 *wg, *wu, *wd, *wo, *w_r;
  unsigned* gran; float* csf; double* stats; unsigned* hand; u16* out;
};
typedef __attribute__((address_space(4))) const Args* ArgP;
#define WCAT 3
#define CAP0 1
#define PRE4 50
#define PRE7 20
#define UARR 12u
#define GF0 10
#define XGATE 8
#define ASLP 1
#define B15 3
#define KO 5
#define PRO 2
#define HS 2
#define WMAP 0x32100333
#define SMAP 0
#define STCH 6
#define KPOLL 10
#define TPRIO 2
#define TSLP 9
__device__ __forceinline__ bool slow_cols(const u16* W, int tni) { return ((((unsigned)(uintptr_t)W >> 7) + tni) & 7) == 3; }
#define ARGS_HERE                                                                                                      \
  ArgP ap_ = ap; asm volatile("" : "+s"(ap_));                                                                          \
  const u16* A = ap_->a; const u16* __restrict__ W = ap_->W; u16* KV = ap_->kv; u16* Of = ap_->o;        \
  const u16* __restrict__ wg = ap_->wg; const u16* __restrict__ wu = ap_->wu; const u16* __restrict__ wd = ap_->wd;       \
  const u16* __restrict__ wo = ap_->wo; const u16* __restrict__ w_r = ap_->w_r; unsigned* gran = ap_->gran;              \
  float* csf = ap_->csf; const double* stats = ap_->stats; double* stats_out = ap_->so; unsigned* hand = ap_->hand; \
  u16* xout = ap_->xo; u16* __restrict__ out = ap_->out
#define AGAIN "s_getpc_b64 s[90:91]\n\ts_add_u32 s90, s90, layers_kernel@rel32@lo+4\n\ts_addc_u32 s91, s91, layers_kernel@rel32@hi+12\n\ts_setpc_b64 s[90:91]"
#define KEPT(c)                                                                                                        \
  c "{v[104:107]}"(gu[0]), c "{v[108:111]}"(gu[1]), c "{v[112:115]}"(gu[2]), c "{v[116:119]}"(gu[3]), c "{v[120:123]}"(gu[4]),      \
  c "{v[124:127]}"(gu[5]), c "{v[128:131]}"(gu[6]), c "{v[132:135]}"(gu[7]), c "{v[136:139]}"(gu[8]), c "{v[140:143]}"(gu[9]),      \
  c "{v[144:147]}"(gu[10]), c "{v[148:151]}"(gu[11]), c "{v[152:155]}"(gu[12]), c "{v[156:159]}"(gu[13]), c "{v[160:163]}"(gu[14]), \
  c "{v[164:167]}"(gu[15])
#define LAYER_HERE ArgP apl_ = ap; asm volatile("" : "+s"(apl_)); const bool LAY2 = apl_->lay != 0
__device__ __forceinline__ void layer_body(const int wid, const int bid, const unsigned lbase, const ArgP ap, const unsigned long long t_entry) {
  constexpr int K = 1024, NKT = 16, NST = 3, ABYTES = 16384, WBYTES = 24576, STB = ABYTES + WBYTES, ldw = 3 * D_;
  constexpr int TABA = NST * STB, ALPHA = TABA + 2048, CRED = ALPHA + 512;
  constexpr int ASB = 16384, FLG = 8 * ASB + 256;
  constexpr int DUM = FLG + 64, TAB = DUM + 256, RED = TAB + 512, SACT = RED + 4096, XML = SACT + 3584, UGL = XML + 4096, SPM = UGL + 1024, QOFF = 9 * ASB;
  constexpr int TABW = RED;
  static_assert(SPM + 2048 <= QOFF && CRED <= 8 * ASB && RED + 8192 <= QOFF && RED % 16 == 0, "lds");
  const int xcd = (bid & 1) * 4 + ((bid >> 1) & 3), loc = bid >> 3;
  const int tmi = (xcd / QNG) * QMX + loc / QNX, tni = (xcd % QNG) * QNX + loc % QNX;
  const int m0 = tmi * 128, n0 = tni * 64;
  const int omi = (xcd / (OMX / 2)) * OMX + loc / (32 / OMX), oni = (xcd % (OMX / 2)) * (32 / OMX) + loc % (32 / OMX);
  constexpr unsigned VOFF = (unsigned)S_ * D_ * 2;
  auto tile_at = [&](unsigned n) __attribute__((always_inline)) {
#pragma unroll 1
    for (int i = 0; i < 4 * SPIN_MAX; ++i) {
      if (__builtin_amdgcn_readfirstlane(*LDS_AT(const volatile unsigned, lbase + FLG + 36)) == n * 0x01010101u) break;
      __builtin_amdgcn_s_sleep(1);
    }
    asm volatile("" ::: "memory");
    PIN();
  };
  auto all_ge = [&](unsigned w, unsigned n) { return ((w + (128u - n) * 0x01010101u) & 0x80808080u) == 0x80808080u; };
  if (wid >= 8) {
    u32x4 gu[16];
    LAYER_HERE;
    constexpr int dk = 6;
    const int hw = wid - 8, b = bid;
    int nb = 0;
    constexpr int lim = NKT - 1;
    auto tick_nw = [&]() {
      PIN();
      if (nb < lim) { __builtin_amdgcn_s_barrier(); ++nb; } else __builtin_amdgcn_s_sleep(2);
      asm volatile("" ::: "memory");
      PIN();
    };
    auto tick = [&]() {
      asm volatile("s_waitcnt lgkmcnt(0)" ::: "memory");
      tick_nw();
    };
    auto fold16 = [&](f32x2 y0, f32x2 y1) __attribute__((always_inline)) {
      const auto q0 = __builtin_amdgcn_permlane32_swap(__float_as_uint(y0[0]), __float_as_uint(y1[0]), false, false);
      const auto q1 = __builtin_amdgcn_permlane32_swap(__float_as_uint(y0[1]), __float_as_uint(y1[1]), false, false);
      const auto q = __builtin_amdgcn_permlane16_swap(__float_as_uint(__uint_as_float(q0[0]) + __uint_as_float(q0[1])),
                                                      __float_as_uint(__uint_as_float(q1[0]) + __uint_as_float(q1[1])), false, false);
      return __uint_as_float(q[0]) + __uint_as_float(q[1]);
    };
    unsigned best, tag, rmd;
    float alpha, m, wsum;
    u32x4 tw;
    u32x2 xq;
    s16x8 sl[8];
    const int kcap = CAP0 + (tmi & 7);
    if (!LAY2) {
      ARGS_HERE;
      const int lane = fresh_lane(), t = hw * 64 + lane;
      const unsigned ep = ld_ag(gran);
      const int srow = m0 + tni * 8 + hw * 2;
      u32x4 xa[2][2], wv[8];
      const int wr_ = (3 - ((unsigned)(uintptr_t)W >> 7) - (tni & ~3)) & 7;
      const bool cpy = hw == 3 && wr_ < 4 && wr_ != (tni & 3), wld = hw < 3 ? wr_ != (tni & 3) : cpy;
      const int wcn = cpy ? (tni & ~3) + wr_ : tni, wcp = cpy ? (tni - wr_ - 1) & 3 : hw;
      const int wof = ((tmi * 64 + (lane >> 3)) * ldw + wcp * D_ + wcn * 64 + (lane & 7) * 8) * 2;
      auto ld_w = [&]() {
        const __amdgpu_buffer_rsrc_t rw = __builtin_amdgcn_make_buffer_rsrc(const_cast<u16*>(W), 0, wld ? D_ * ldw * 2 : 0, 0x00020000);
#pragma unroll
        for (int r = 0; r < 8; ++r) wv[r] = __builtin_amdgcn_raw_buffer_load_b128(rw, wof, r * 8 * ldw * 2, 0);
        PIN();
      };
      auto wcopy = [&]() __attribute__((always_inline)) {
        if (cpy) {
          const __amdgpu_buffer_rsrc_t rw = __builtin_amdgcn_make_buffer_rsrc(ap_->wc, 0, D_ * ldw * 2, 0x00020000);
#pragma unroll
          for (int r = 0; r < 8; ++r) __builtin_amdgcn_raw_buffer_store_b128(wv[r], rw, wof + r * 8 * ldw * 2, 0, 16);
        }
        PIN();
      };
      const int wat = ((hw == 3 ? SMAP : WMAP) >> (4 * (tmi & 7))) & 3;
      unsigned ps = 0u;
      auto stouch = [&](int at) __attribute__((always_inline)) {
        if (at == STCH && cpy) ps = __builtin_amdgcn_raw_buffer_load_b32(__builtin_amdgcn_make_buffer_rsrc(const_cast<u16*>(W), 0, D_ * ldw * 2, 0x00020000),
                                                                        (((tmi ^ 8) * 64 + lane) * ldw + wcp * D_ + wcn * 64) * 2, 0, 0);
        PIN();
      };
      auto ld_x = [&](int h) {
#pragma unroll
        for (int r = 0; r < 2; ++r) xa[r][h] = *reinterpret_cast<const u32x4*>(A + (size_t)(srow + r) * D_ + (h ^ (tmi >> 3)) * 512 + lane * 8);
        PIN();
      };
      ld_x(0);
      if (X2AT == 0) ld_x(1);
      if (wat == 0) ld_w();
      unsigned pc = 0u;
      if (hw == 3) {
        unsigned long long a0, a1;
        CODE_ADDR("layers_kernel", a0);
        CODE_ADDR("end_marker_kernel", a1);
        pc = touch_code(a0, a1, loc, lane);
      }
      if (hw == 2) st_ag(gran + GR_N + bid * 64 + lane, lane + 1u);
      {
        const int gi = bid * 256 + t;
        if (gi < GRAN_N / 4) __builtin_amdgcn_raw_buffer_store_b128(u32x4{SENT, SENT, SENT, SENT}, __builtin_amdgcn_make_buffer_rsrc(hand, 0, GRAN_N * 4, 0x00020000), gi * 16, 0, 16);
        if (t < 24) __hip_atomic_store(stats_out + (t >> 3) * S_ + m0 + tni * 8 + (t & 7), 0.0, __ATOMIC_RELAXED, __HIP_MEMORY_SCOPE_AGENT);
      }
      tick();
      if (wat == 1) ld_w();
      if (X2AT == 1) ld_x(1);
      tick();
      if (wat == 2) ld_w();
      if (WCAT == 2) wcopy();
      if (X2AT == 2) ld_x(1);
      asm volatile("" :: "v"(ep));
      tick();
      if (wat == 3) ld_w();
      if (WCAT == 3) wcopy();
      if (X2AT == 3) ld_x(1);
      stouch(3);
#pragma unroll
      for (int i = 0; i < HW; ++i) { tick(); stouch(4 + i); }
      __builtin_amdgcn_s_setprio(HP1);
      PIN();
      tag = ep + 1u;
      const __amdgpu_buffer_rsrc_t rsc = __builtin_amdgcn_make_buffer_rsrc(gran, 0, GR_N * 4, 0x00020000);
      {
        u32x4 od;
#pragma unroll
        for (int q = 0; q < 4; ++q) od[q] = ((lane & 7) >> 1) == q ? ((lane & 1) ? 0x3f800000u : 0x00003f80u) : 0u;
        f32x4 d[2] = {{0.f, 0.f, 0.f, 0.f}, {0.f, 0.f, 0.f, 0.f}};
#pragma unroll
        for (int r = 0; r < 8; ++r) d[r & 1] = __builtin_amdgcn_mfma_f32_16x16x32_bf16(__builtin_bit_cast(s16x8, od), __builtin_bit_cast(s16x8, wv[r]), d[r & 1], 0, 0, 0);
        u32x4 gr;
#pragma unroll
        for (int q = 0; q < 4; ++q) gr[q] = ((__float_as_uint(dpp_add<0x128, 0xf>(fadd(d[0][q], d[1][q]))) + 0x80u) & ~0xffu) | ((tag >> (8 * q)) & 0xffu);
        asm volatile("s_waitcnt vmcnt(0)" ::: "memory");
        __builtin_amdgcn_raw_buffer_store_b128(gr, rsc, (lane & 40) || !wld ? GR_N * 4 : (GR_COL + (wcn * 16 + tmi) * 192 + wcp * 64 + (lane & 7) * 8 + (lane >> 4) * 4) * 4, 0, 16);
      }
      asm volatile("" :: "v"(pc));
      tick_nw();
      stouch(6);
      {
        float xv[2][16], mean[2];
#pragma unroll
        for (int r = 0; r < 2; ++r) {
          float sm = 0.f;
#pragma unroll
          for (int i = 0; i < 4; ++i) {
            xv[r][2 * i] = __uint_as_float(xa[r][0][i] << 16); xv[r][2 * i + 1] = __uint_as_float(xa[r][0][i] & 0xffff0000u);
            xv[r][8 + 2 * i] = __uint_as_float(xa[r][1][i] << 16); xv[r][8 + 2 * i + 1] = __uint_as_float(xa[r][1][i] & 0xffff0000u);
          }
#pragma unroll
          for (int i = 0; i < 16; ++i) sm += xv[r][i];
          mean[r] = wave_sum(sm) * (1.0f / 1024.0f);
          tick();
          stouch(7 + r);
        }
        float vr[2];
#pragma unroll
        for (int r = 0; r < 2; ++r) {
          float v = 0.f;
#pragma unroll
          for (int i = 0; i < 16; ++i) { const float d = xv[r][i] - mean[r]; v += d * d; }
          vr[r] = wave_sum(v) * (1.0f / 1024.0f);
          if (r == 0) tick();
        }
        const float vv = lane & 2 ? vr[1] : vr[0], mm = lane & 2 ? mean[1] : mean[0];
        const float rin = 1.0f / rbf(sqrtf(rbf(rbf(vv) + 1e-5f)));
        if (lane < 4) __builtin_amdgcn_raw_buffer_store_b64(u32x2{__float_as_uint((lane & 1) ? rbf(mm) : rin), tag}, rsc, (GR_ROW + srow * 4 + lane * 2) * 4, 0, 16);
      }
      tick();
      float cs;
      while (nb < POLLAT) tick();
      asm volatile("" :: "v"(ps));
#pragma unroll 1
      for (int spin = 0;; ++spin) {
        u32x4 csv[4];
        const __amdgpu_buffer_rsrc_t rsw = __builtin_amdgcn_make_buffer_rsrc(gran, 0, hw < 3 ? GR_N * 4 : 0, 0x00020000);
#pragma unroll
        for (int i = 0; i < 4; ++i) csv[i] = __builtin_amdgcn_raw_buffer_load_b128(rsw, (GR_COL + (tni * 16 + (lane >> 4)) * 192 + hw * 64 + (lane & 15) * 4) * 4, i * (4 * 192 * 4), 16);
        const u32x4 rp = __builtin_amdgcn_raw_buffer_load_b128(rsc, (GR_ROW + (m0 + (t & 127)) * 4) * 4, 0, 16);
        PIN();
        tick(); tick();
        unsigned bad = 0u;
        float z[4];
#pragma unroll
        for (int q = 0; q < 4; ++q) {
          const unsigned tq = tag >> (8 * q);
          bad |= ((csv[0][q] ^ tq) | (csv[1][q] ^ tq)) | ((csv[2][q] ^ tq) | (csv[3][q] ^ tq));
          z[q] = fadd(fadd(__uint_as_float(csv[0][q] & ~0xffu), __uint_as_float(csv[1][q] & ~0xffu)), fadd(__uint_as_float(csv[2][q] & ~0xffu), __uint_as_float(csv[3][q] & ~0xffu)));
        }
        cs = fold16(f32x2{z[0], z[1]}, f32x2{z[2], z[3]});
        bad = (hw < 3 ? bad & 0xffu : 0u) | (rp[1] ^ tag) | (rp[3] ^ tag);
        alpha = __uint_as_float(rp[0]); m = __uint_as_float(rp[2]);
        if (none(bad != 0u) || spin >= SPIN_MAX) break;
      }
      if (tmi == 0 && hw < 3) st_ag(reinterpret_cast<unsigned*>(csf) + hw * 1024 + n0 + (lane & 15) * 4 + (lane >> 4), __float_as_uint(cs));
      unsigned c0, c1, c2;
      split3(cs, c0, c1, c2);
      tw = u32x4{c0 | (c1 << 16), c0 | (c2 << 16), c1 | (c0 << 16), 0u};
      best = rmd = 0u;
      wsum = 0.f;
      asm volatile("" : "=v"(xq), KEPT("="));
#pragma unroll
      for (int i = 0; i < 8; ++i) asm volatile("" : "=v"(sl[i]));
    } else {
      asm volatile("; kept registers" : KEPT("="));
      PIN();
      ARGS_HERE;
      const int lane = fresh_lane(), t = hw * 64 + lane;
      best = rmd = 0u;
      tag = __builtin_amdgcn_readfirstlane(*LDS_AT(const volatile unsigned, lbase + FLG + 20));
#pragma unroll
      for (int i = 0; i < 8; ++i) asm volatile("" : "=v"(sl[i]));
      unsigned sad;
      auto tickc = [&]() __attribute__((always_inline)) {
        tick();
        if (nb == kcap + 1) {
#pragma unroll
          for (int i = 0; i < 8; ++i) {
            const unsigned a = (sad ^ ((i >> 2) * 64) ^ (((i >> 1) & 1) * 16)) + (i & 1) * 12288;
            sl[i] = __builtin_shufflevector(__builtin_amdgcn_ds_read_tr16_b64_v4i16(LDS_AT(s16x4, a)), __builtin_amdgcn_ds_read_tr16_b64_v4i16(LDS_AT(s16x4, a + 1536)), 0, 1, 2, 3, 4, 5, 6, 7);
          }
        }
        PIN();
      };
      static_assert(CAP0 + 8 <= KPOLL, "the stage is taken before that");
      if (hw == 1) STAMP(4, 4);
      if (hw == 0) {
        __builtin_amdgcn_s_sleep(TPRE);
        tile_at(3u);
        __builtin_amdgcn_s_sleep(XGATE);
        STAMP(4, 3);
#pragma unroll 1
        for (int spin = 0; spin < SPIN_MAX; ++spin) {
          if (none(ld_ag(gran + GR_X + (tmi * RX + tni % RX) * 128 + 64 + lane) != tag)) break;
          __builtin_amdgcn_s_sleep(1);
        }
      }
      if (hw == 0) STAMP(4, 5);
      asm volatile("" ::: "memory");
      PIN();
      __builtin_amdgcn_s_barrier();
      PIN();
      if (hw == 1) STAMP(4, 6);
      {
        const int ln = fresh_lane(), g4 = ln >> 4, tp = ln & 3, k = 8 * g4 + ((ln & 15) >> 2);
        sad = lbase + (kcap % NST) * STB + ABYTES + k * 384 + (((2 * (tp >> 1)) ^ (4 * ((k >> 1) & 1)) ^ (g4 & 1)) << 4) + 8 * (tp & 1) + (hw % 3) * 128;
      }
      wsum = __uint_as_float(__builtin_amdgcn_readfirstlane(*LDS_AT(const volatile unsigned, lbase + FLG + 60)));
      asm volatile("" : "+v"(sad), "=v"(tw), "=v"(alpha), "=v"(m));
      PIN();
      const int row = m0 + (t & 127);
      const __amdgpu_buffer_rsrc_t rsg = __builtin_amdgcn_make_buffer_rsrc(hand, 0, GRAN_N * 4, 0x00020000);
      if (hw < 2 || tmi == tni) {
      while (nb < HS) tickc();
#pragma unroll 1
      for (int spin = 0; spin < SPIN_MAX; ++spin) {
        const unsigned v = ld_ag(gran + GR_X + (tmi * RX + tni % RX) * 128 + lane);
        tickc();
        if (none(v != tag)) break;
      }
      asm volatile("" ::: "memory");
      PIN();
      const double s1 = ld_ag(stats + row), s2 = ld_ag(stats + S_ + row), sw = ld_ag(stats + 2 * S_ + row);
      tickc();
      float mean, var, mb, den;
      ln_from_sums(s1, s2, mean, var, mb, den);
      const float rin = frcp(den), g1 = 1.0f + rin;
      const float mb2 = rbf(mean + (mean - mb) * rin);
      const float den2 = rbf(__builtin_amdgcn_sqrtf(rbf(rbf(var * g1 * g1) + 1e-5f)));
      alpha = g1 * frcp(den2); m = (mb * rin + mb2) * frcp(g1);
      if (tmi == tni && hw >= 2) {
        const float r = (float)fma(-(double)mb, (double)wsum, sw) * rin;
        const unsigned rb = pack2(r, r);
        unsigned rkey = (rb ^ ((unsigned)((int)rb >> 31) | 0x80000000u)) & 0xffff0000u;
        if (r != r) rkey = 0u;
        rkey = wave_umax(rkey | (unsigned)(2047 - row));
        const unsigned md = __builtin_amdgcn_readlane(f2word(__uint_as_float((__float_as_uint(mb) >> 16) | (__float_as_uint(den) & 0xffff0000u))), (2047 - (int)(rkey & 0xffffu)) & 63);
        __builtin_amdgcn_raw_buffer_store_b64(u32x2{rkey, md}, rsg, lane < NREP ? lane * 256 + (tmi * 2 + hw - 2) * 8 : GRAN_N * 4, 0, 16);
      }
      }
      while (nb < KPOLL) tickc();
#pragma unroll 1
      for (int spin = 0; spin < SPIN_MAX; ++spin) {
        const u32x2 v = __builtin_amdgcn_raw_buffer_load_b64(rsg, (bid % NREP) * 256 + (lane & 31) * 8, 0, 16);
        tick(); tick();
        if (none(umax(v[0], v[1]) == SENT)) {
          best = wave_umax(v[0]);
          rmd = __builtin_amdgcn_readlane(v[1], ((2047 - (int)(best & 0xffffu)) >> 6) & 31);
          break;
        }
      }
      const int ix = (2047 - (int)(best & 0xffffu)) & 2047;
      if (row == ix) alpha = 0.f;
      xq = __builtin_amdgcn_raw_buffer_load_b64(__builtin_amdgcn_make_buffer_rsrc(const_cast<u16*>(A), 0, S_ * D_ * 2, 0x00020000), (ix * D_ + hw * 256 + (lane & 3) * 64 + (lane >> 2) * 4) * 2, 0, 16);
      PIN();
    }
    tag = __builtin_amdgcn_readfirstlane(tag);
    wsum = __uint_as_float(__builtin_amdgcn_readfirstlane(__float_as_uint(wsum)));
    const int lane = fresh_lane(), t = hw * 64 + lane;
    {
      unsigned a0, a1, a2;
      split3(-m, a0, a1, a2);
      u32x4 ta = {a0 | (a0 << 16), a1 | (a0 << 16), a1 | (a2 << 16), 0u};
      asm volatile("" : "+v"(tw), "+v"(ta), "+v"(alpha));
      __builtin_amdgcn_s_setprio(TPRIO);
      if (nb < lim) {
        while (nb < lim) tick();
        __builtin_amdgcn_s_sleep(TSLP);
      }
      if (!LAY2 && hw < 3) *LDS_AT(u32x4, lbase + TABW + (hw * 64 + (lane & 15) * 4 + (lane >> 4)) * 16) = tw;
      if (t < 128) {
        *LDS_AT(u32x4, lbase + TABA + t * 16) = ta;
        *LDS_AT(float, lbase + ALPHA + t * 4) = alpha;
      }
    }
    if (t >= 192 && t < 196) *LDS_AT(u32x4, lbase + CRED + ((t >> 1) & 1) * 512 + (t & 1) * 128) = u32x4{0u, 0u, 0u, 0u};
    asm volatile("s_waitcnt lgkmcnt(0)" ::: "memory");
    PIN();
    __builtin_amdgcn_s_barrier();
    PIN();
    *LDS_AT(u32x4, lbase + XML + t * 16) = u32x4{SENT, SENT, SENT, SENT};
    *LDS_AT(u32x4, lbase + SPM + (t & 127) * 16) = u32x4{SENT, SENT, SENT, SENT};
    if (hw == 0) {
      unsigned v;
      asm volatile("v_mov_b32 %0, 0\n\tv_writelane_b32 %0, 2, 0\n\tv_writelane_b32 %0, %1, 5\n\tv_writelane_b32 %0, %2, 7"
                   : "=&v"(v) : "s"(tag), "s"(best));
      lds_wr(lbase + FLG + lane * 4, v);
    }
    const int idx = LAY2 ? (2047 - (int)(best & 0xffffu)) & 2047 : -64;
    const bool p1blk = (oni & 7) != 7, fast = tmi < 8, own = fast && (loc & 1);
    const int fg = xcd * 28 + (p1blk ? loc - (loc >> 3) : 0), ow = (xcd & 3) * 16 + (loc >> 1);
    static_assert(OMX == 4, "oni % 8 = loc % 8");
#define GLD(a, b) "global_load_dwordx4 v[" #a ":" #b "], %0, off\n\tv_lshl_add_u64 %0, %0, 0, %1\n\t"
#define GLD4(...) asm volatile(__VA_ARGS__ : "+v"(p) : "s"((unsigned long long)(F_ * 2)), KEPT() : "memory")
    auto gptr = [&](const u16* wg, const u16* wu) {
      const int ln = fresh_lane();
      return ((ln & 2) ? wu : wg) + (size_t)(hw * 256 + (ln >> 2) * 4) * F_ + fg * 16 + (ln & 1) * 8;
    };
    auto gather = [&](const u16*& p, int e) __attribute__((always_inline)) {
      if (e == 0) GLD4(GLD(104, 107) GLD(108, 111) GLD(112, 115) GLD(116, 119));
      else if (e == 1) GLD4(GLD(120, 123) GLD(124, 127) GLD(128, 131) GLD(132, 135));
      else if (e == 2) GLD4(GLD(136, 139) GLD(140, 143) GLD(144, 147) GLD(148, 151));
      else GLD4(GLD(152, 155) GLD(156, 159) GLD(160, 163) GLD(164, 167));
      p += 60 * F_;
    };
    auto dmask = [&](int q) { const int ln = fresh_lane(); return ((ln >> 1) & 1) == q ? ((ln & 1) ? 0xffff0000u : 0x0000ffffu) : 0u; };
    unsigned dm0, dm1, dad;
    auto gu_setup = [&]() __attribute__((always_inline)) {
      const int ln = fresh_lane();
      dm0 = dmask(0); dm1 = dmask(1);
      dad = lbase + SACT + ((ln & 3) * 8 + (ln >> 5) * 4 + ((ln >> 4) & 1)) * 16 + hw * 4;
      asm volatile("" : "+v"(dm0), "+v"(dm1), "+v"(dad));
    };
    auto gate_up = [&](u32x2 x, unsigned md) __attribute__((always_inline)) {
      const unsigned rin = __builtin_amdgcn_readfirstlane(__float_as_uint(frcp(__uint_as_float(md & 0xffff0000u))));
      asm volatile("s_getpc_b64 s[90:91]\n\ts_add_u32 s90, s90, gate_up_code@rel32@lo+4\n\ts_addc_u32 s91, s91, gate_up_code@rel32@hi+12\n\ts_swappc_b64 s[92:93], s[90:91]"
                   : "+{v[100:101]}"(x) : "{s84}"(md << 16), "{s85}"(md & 0xffff0000u), "{s86}"(rin), "{v99}"(dad), "{v102}"(dm0), "{v103}"(dm1), KEPT()
                   : "v84", "v85", "v86", "v87", "v88", "v89", "v90", "v91", "v92", "v93", "v94", "v95", "v96", "v97", "s88", "s89", "s90", "s91", "s92", "s93", "scc", "memory");
    };
    auto act_diag = [&](int so = 0) __attribute__((always_inline)) {
      const int ln = fresh_lane();
      const f32x4 g4 = *LDS_AT(const volatile f32x4, lbase + SACT + so + (ln >> 2) * 16), u4 = *LDS_AT(const volatile f32x4, lbase + SACT + so + 256 + (ln >> 2) * 16);
      float gv = fadd(fadd(g4[0], g4[1]), fadd(g4[2], g4[3])), uv = fadd(fadd(u4[0], u4[1]), fadd(u4[2], u4[3]));
      rbf2(gv, uv);
      const float av = rbf(gv * frcp(1.0f + __expf(-gv))) * uv;
      const unsigned ap2 = pack2(av, av);
      return __builtin_bit_cast(s16x4, u32x2{ap2 & dm0, ap2 & dm1});
    };
    auto down = [&](const s16x4& a, const u32x2 (&w)[16]) __attribute__((always_inline)) {
      f32x4 d[2] = {{0.f, 0.f, 0.f, 0.f}, {0.f, 0.f, 0.f, 0.f}};
      static_for<0, 16>([&](auto fc) __attribute__((always_inline)) {
        constexpr int f = decltype(fc)::value;
        d[f & 1] = __builtin_amdgcn_mfma_f32_4x4x4bf16_1k(a, __builtin_bit_cast(s16x4, w[f]), d[f & 1], 4, f, 0);
      });
      f32x4 z;
#pragma unroll
      for (int e = 0; e < 4; ++e) { z[e] = d[0][e] + d[1][e]; asm volatile("" : "+v"(z[e])); }
      return u32x4{f2word(z[0]), f2word(z[1]), f2word(z[2]), f2word(z[3])};
    };
    auto fold8 = [&](f32x2 y0, f32x2 y1) __attribute__((always_inline)) { return dpp_add<0x128, 0xf>(fold16(y0, y1)); };
    auto fold4 = [&](f32x2 y0, f32x2 y1) __attribute__((always_inline)) { return dpp_add<0x124, 0xf>(fold8(y0, y1)); };
    auto y_sum = [&](const __amdgpu_buffer_rsrc_t rs, auto nbc) __attribute__((always_inline)) {
      constexpr int NB = decltype(nbc)::value, N = 14 / NB;
      const int ln = fresh_lane(), src = (GRAN_N + (ln >> 2) * D_ + ow * 16 + (ln & 3) * 4) * 4;
      f32x2 y0 = {0.f, 0.f}, y1 = {0.f, 0.f};
#pragma unroll 1
      for (int bt = 0; bt < NB; ++bt) {
        f32x2 z0, z1;
#pragma unroll 1
        for (int spin = 0; spin < SPIN_MAX; ++spin) {
          u32x4 v[N];
#pragma unroll
          for (int i = 0; i < N; ++i) v[i] = __builtin_amdgcn_raw_buffer_load_b128(rs, src, (bt * N + i) * (16 * D_ * 4), 16);
          z0 = y0; z1 = y1;
#pragma unroll
          for (int i = 0; i < N; ++i) {
            z0 = f32x2{fadd(z0[0], __uint_as_float(v[i][0])), fadd(z0[1], __uint_as_float(v[i][1]))};
            z1 = f32x2{fadd(z1[0], __uint_as_float(v[i][2])), fadd(z1[1], __uint_as_float(v[i][3]))};
          }
          const float chk = fadd(fadd(z0[0], z0[1]), fadd(z1[0], z1[1]));
          if (none(chk != chk)) break;
          unsigned mx = 0u;
#pragma unroll
          for (int i = 0; i < N; ++i) mx = umax(umax(umax(mx, v[i][0]), v[i][1]), umax(v[i][2], v[i][3]));
          if (none(mx == SENT)) break;
          __builtin_amdgcn_s_sleep(YSLP);
          asm volatile("" ::: "memory");
        }
        y0 = z0; y1 = z1;
      }
      return fold4(y0, y1);
    };
    [&]() __attribute__((always_inline)) {
    ARGS_HERE;
    const unsigned l16 = lane * 16;
    unsigned* kvflag = gran + GR_KV + ((LAY2 ? 16 : 0) + tni) * (32 * RK);
    auto wait_sig = [&](int sh) {
#pragma unroll 1
      for (int i = 0; i < 4 * SPIN_MAX; ++i) {
        if (((__builtin_amdgcn_readfirstlane(*LDS_AT(const volatile unsigned, lbase + FLG + 32)) >> sh) & 7u) >= 4u) break;
        __builtin_amdgcn_s_sleep(SSLP);
      }
      asm volatile("" ::: "memory");
      PIN();
    };
    PIN();
    asm volatile("s_waitcnt lgkmcnt(0)" ::: "memory");
    if (!LAY2) asm volatile("s_waitcnt vmcnt(0)" ::: "memory");
    __builtin_amdgcn_s_barrier();
    PIN();
    __builtin_amdgcn_s_setprio(HPRIO);
    if (hw < 3) {
      __builtin_amdgcn_s_sleep(SPRE);
      wait_sig(0);
      __builtin_amdgcn_raw_buffer_store_b32(tag, __builtin_amdgcn_make_buffer_rsrc(kvflag, 0, hw == 0 ? RK * 128 : 0, 0x00020000), lane * 128 + tmi * 4, 0, 16);
      if (hw == 0) STAMP(LAY2 ? 5 : 1, 16);
    }
    if (hw == 3) {
      __builtin_amdgcn_s_sleep(KVS);
      const unsigned kv0 = ld_ag(kvflag + (tmi % RK) * 32 + (lane & 15));
      PIN();
      const char* cnt = reinterpret_cast<const char*>(gran + GR_N + bid * 64 + 2);
      unsigned vk[8], vp = lbase + FLG + 8 + (lane & 7), pw = 0u;
#pragma unroll
      for (int j = 0; j < 8; ++j) { vk[j] = l16 + (j >> 2) * 131072u + ((j >> 1) & 1) * 4096u + (j & 1) * (VOFF - 2048u); asm volatile("" : "+v"(vk[j])); }
#define G4(a, b, c, d, sc) "s_nop 0\n\tglobal_load_lds_dwordx4 %" #a ", %8" sc "\n\tglobal_load_lds_dwordx4 %" #b ", %8 offset:1024" sc "\n\t"             \
                       "global_load_lds_dwordx4 %" #c ", %8 offset:2048" sc "\n\tglobal_load_lds_dwordx4 %" #d ", %8 offset:3072" sc "\n\t"
#define M4 "s_addk_i32 m0, 0x1000\n\t"
      auto stage = [&](const char* sp, unsigned dst) __attribute__((always_inline)) {
        asm volatile("s_mov_b32 m0, %9\n\t" G4(0, 0, 1, 1, " sc1") M4 G4(2, 2, 3, 3, " sc1") M4 G4(4, 4, 5, 5, " sc1") M4 G4(6, 6, 7, 7, " sc1")
                     :: "v"(vk[0]), "v"(vk[1]), "v"(vk[2]), "v"(vk[3]), "v"(vk[4]), "v"(vk[5]), "v"(vk[6]), "v"(vk[7]), "s"(sp), "s"(dst) : "m0", "scc", "memory");
      };
      auto count = [&]() __attribute__((always_inline)) {
        asm volatile("s_mov_b32 m0, %2\n\ts_nop 0\n\tglobal_load_lds_dword %0, %1 sc1" :: "v"(0u), "s"(cnt), "s"(lbase + FLG - 252) : "m0", "memory");
        cnt += 8;
      };
      auto freed = [&](unsigned n) __attribute__((always_inline)) {
        if (!none(pw < n)) {
#pragma unroll 1
          for (int i = 0; i < 4 * SPIN_MAX; ++i) {
            pw = *LDS_AT(const volatile unsigned char, vp);
            if (none(pw < n)) break;
            __builtin_amdgcn_s_sleep(W3SLP);
          }
        }
        asm volatile("" ::: "memory");
      };
      unsigned long long ord = (0xfedcba9876543210ull >> 4 & ~((1ull << 4 * (tmi & 7)) - 1ull)) | (0xfedcba9876543210ull & ((1ull << 4 * (tmi & 7)) - 1ull));
      ord ^= (tmi & 8) * 0x1111111111111111ull;
      unsigned seen;
      const char* kvb = reinterpret_cast<const char*>(KV) + (size_t)tni * (128 * 2048);
      auto panel = [&]() __attribute__((always_inline)) {
        const unsigned pn = (unsigned)ord & 15u;
        ord >>= 4;
        if (__builtin_expect(!((seen >> pn) & 1u), 0)) {
#pragma unroll 1
          for (int i = 0; i < SPIN_MAX && !((seen >> pn) & 1u); ++i) seen = (unsigned)__ballot(ld_ag(kvflag + (tmi % RK) * 32 + (lane & 15)) == tag);
        }
        asm volatile("" ::: "memory");
        return kvb + pn * 8192u;
      };
      asm volatile("" : "+s"(ord), "+s"(kvb), "+s"(cnt));
      asm volatile("" : "+v"(vp), "+v"(pw));
      PIN();
      seen = (unsigned)__ballot(kv0 == tag);
      const char* sp = panel();
      wait_sig(16);
      stage(sp, lbase + 2 * ASB);
      count();
      const int ksp = LAY2 ? PNS / 2 : -1, ksg = LAY2 ? 64 : GF0;
      const u16* gp = gptr(wg, wu);
#pragma unroll 1
      for (int k = 1; k < 15; ++k) {
        if (__builtin_expect(k == ksp, 0)) {
          gu_setup();
          gate_up(xq, rmd);
          PIN();
          *LDS_AT(volatile unsigned char, lbase + FLG + 7) = (unsigned char)0x7f;
        }
        freed((unsigned)(0xba9876543211000ull >> 4 * k) & 15u);
        stage(sp + 2048, lbase + ((2 * k + 1) & 7) * ASB);
        sp = panel();
        stage(sp, lbase + ((2 * k + 2) & 7) * ASB);
        count();
        if (__builtin_expect((unsigned)(k - ksg) < 4u, 0)) gather(gp, k - ksg);
      }
      freed(12u);
      glds16s<true>(csf + 4096 + tni * 128, l16, lbase + UGL);
      stage(sp + 2048, lbase + 7 * ASB);
      count();
      const unsigned vwo = (unsigned)((lane >> 3) * D_ + oni * 64 + ((lane & 7) ^ ((lane >> 3) & 6)) * 8) * 2u;
      constexpr unsigned vs = 8u * D_ * 2u - 1024u;
#pragma unroll 1
      for (int ns = 0; ns < 6; ++ns) {
        freed((unsigned)(ns + 25) >> 1);
        const char* wp = reinterpret_cast<const char*>(wo) + (size_t)ns * (128 * D_ * 2);
#pragma unroll
        for (int j = 0; j < 4; ++j)
          glds16x4<false>(wp + j * (32u * D_ * 2u), vwo, vwo + vs, vwo + 2 * vs, vwo + 3 * vs, lbase + ns * ASB + j * 4096);
      }
      asm volatile("s_waitcnt vmcnt(0)" ::: "memory");
      return;
    }
    if (!LAY2) {
      auto until = [&](unsigned n) {
        while ((__builtin_amdgcn_readfirstlane(*LDS_AT(const volatile unsigned, lbase + FLG + 8)) & 255u) < n) __builtin_amdgcn_s_sleep(16);
        asm volatile("" ::: "memory");
        PIN();
      };
      if (hw != 1) {
        constexpr int N2 = HAND_N + Y_N + 3 * D_ / 2;
        const __amdgpu_buffer_rsrc_t rsi = __builtin_amdgcn_make_buffer_rsrc(hand + (hw ? GRAN_N : 2 * HAND_N), 0, (hw ? N2 : PS_N) * 4, 0x00020000);
        until(IAT);
#pragma unroll 1
        for (int i = 0; i < ((hw ? N2 : PS_N) / 4 + 16383) / 16384; ++i) {
          __builtin_amdgcn_raw_buffer_store_b128(u32x4{SENT, SENT, SENT, SENT}, rsi, ((i * 256 + bid) * 64 + lane) * 16, 0, 16);
          __builtin_amdgcn_s_sleep(ISLP);
        }
      }
      const int ng = loc & 15, fc = xcd * 2 + (loc >> 4);
      const size_t og = (size_t)((fg - xcd * 28) / 7 * 256 + lane) * F_ + (xcd * 7 + fg % 7) * 64;
      const u16* pd = wd + (size_t)(fc * 224 + (lane < 56 ? lane : 0)) * D_ + ng * 64;
      const u16* p = gptr(wg, wu);
#pragma unroll 1
      for (unsigned n = T0; n < T0 + 13u; ++n) {
        until(n);
        if (hw == 1) {
          const unsigned i = n - T0;
          const u16* q = i < 8u ? ((i & 1u) ? wu : wg) + og + (size_t)(i >> 1) * (64 * F_) : pd + (size_t)((i - 8u - (i > TW)) & 3u) * (56 * D_);
          if (i == TW) {
            q = wo + (size_t)(768 + (loc / (32 / OMX)) * (256 / OMX) + (lane & (256 / OMX - 1) & 63)) * D_ + oni * 64;
            unsigned long long a0, a1;
            CODE_ADDR("layers_kernel", a0); CODE_ADDR("end_marker_kernel", a1);
            if (loc < 8) { const unsigned p1 = touch_code(a0, a1, 3 + loc, lane); asm volatile("" :: "v"(p1)); }
          }
          const unsigned p0 = touch(q);
          asm volatile("" :: "v"(p0));
        }
        if (n >= G0 && (n - G0) % GS == 0u && (n - G0) / GS < 4u) gather(p, (n - G0) / GS);
        if (n == UAT && hw == UHW) {
          u32x4 a[4][2], c[2];
#pragma unroll
          for (int i = 0; i < 4; ++i)
#pragma unroll
            for (int h = 0; h < 2; ++h) a[i][h] = *reinterpret_cast<const u32x4*>(wo + (size_t)(tni * 64 + tmi * 4 + i) * D_ + h * 512 + lane * 8);
#pragma unroll
          for (int h = 0; h < 2; ++h) c[h] = *reinterpret_cast<const u32x4*>(w_r + h * 512 + lane * 8);
          auto dot = [&](unsigned x, unsigned y, float z) { return __builtin_amdgcn_fdot2_f32_bf16(__builtin_bit_cast(bf16x2, x), __builtin_bit_cast(bf16x2, y), z, false); };
          PIN();
          __builtin_amdgcn_s_setprio(0);
#pragma unroll 1
          for (int i = 0; i < SPIN_MAX; ++i) {
            if (__builtin_amdgcn_readfirstlane(*LDS_AT(const volatile unsigned, lbase + FLG + 16)) >= UARR) break;
            __builtin_amdgcn_s_sleep(4);
          }
          PIN();
          float wm = 0.f;
#pragma unroll
          for (int e = 0; e < 8; ++e) wm = dot(c[e >> 2][e & 3], 0x3f803f80u, wm);
          wm = wave_sum(wm);
          lds_wr(lbase + FLG + 60, __float_as_uint(wm));
          wm *= -1.0f / 1024.0f;
          f32x2 u[2], g[2];
#pragma unroll
          for (int i = 0; i < 4; ++i) {
            float p1 = 0.f, pw = 0.f, p2 = 0.f;
#pragma unroll
            for (int e = 0; e < 8; ++e) {
              const unsigned x = a[i][e >> 2][e & 3];
              p1 = dot(x, 0x3f803f80u, p1); pw = dot(x, c[e >> 2][e & 3], pw); p2 = dot(x, x, p2);
            }
            u[i >> 1][i & 1] = fmaf(p1, wm, pw); g[i >> 1][i & 1] = p2;
          }
          float us = fold4(u[0], u[1]), gs = fold4(g[0], g[1]);
          us = dpp_add<0x121, 0xf>(dpp_add<0x122, 0xf>(us)); gs = dpp_add<0x121, 0xf>(dpp_add<0x122, 0xf>(gs));
          __builtin_amdgcn_raw_buffer_store_b32(__float_as_uint((lane & 1) ? __builtin_amdgcn_sqrtf(gs) * (1.0f / 32.0f) : us),
              __builtin_amdgcn_make_buffer_rsrc(csf + 4096 + tni * 128, 0, 512, 0x00020000), (lane & 14) ? 512 : ((lane & 1) * 64 + tmi * 4 + (lane >> 4)) * 4, 0, 16);
        }
      }
      if (hw == 2 && bid == 0) {
        const __amdgpu_buffer_rsrc_t rsc = __builtin_amdgcn_make_buffer_rsrc(gran, 0, GR_N * 4, 0x00020000);
#pragma unroll 1
        for (int spin = 0; spin < SPIN_MAX; ++spin) {
          bool ok = true;
#pragma unroll
          for (int i = 0; i < 4; ++i) ok &= __builtin_amdgcn_raw_buffer_load_b32(rsc, (GR_ROW + (i * 64 + lane) * 32 + 1) * 4, 0, 16) == tag;
          if (__all(ok)) break;
          __builtin_amdgcn_s_sleep(8);
          asm volatile("" ::: "memory");
        }
        if (lane == 0) st_ag(gran, tag);
      }
      return;
    }
    unsigned* X2 = hand + GRAN_N + Y_N;
    unsigned* XS = X2 + X2_N;
    unsigned* P4 = XS + XS_N;
    unsigned* PA = P4 + P4_N;
    const __amdgpu_buffer_rsrc_t rsh = __builtin_amdgcn_make_buffer_rsrc(hand, 0, HAND_N * 4, 0x00020000);
    auto signal = [&](unsigned ph) {
      asm volatile("" ::: "memory");
      PIN();
      *LDS_AT(volatile unsigned char, lbase + FLG + 4 + hw) = (unsigned char)ph;
    };
    auto wait_all = [&](unsigned ph) {
#pragma unroll 1
      for (int i = 0; i < SPIN_MAX; ++i) {
        if (all_ge(__builtin_amdgcn_readfirstlane(*LDS_AT(const volatile unsigned, lbase + FLG + 4)), ph)) break;
        __builtin_amdgcn_s_sleep(WSLP);
      }
      asm volatile("" ::: "memory");
      PIN();
    };
    gu_setup();
    gate_up(xq, rmd);
    signal(hw == 2 ? 2u : 0x7fu);
    if (wid == 8) STAMP(dk, 2);
    if (hw < 2) {
#pragma unroll 1
      for (int i = 0; i < SPIN_MAX; ++i) {
        if (__builtin_amdgcn_readfirstlane(*LDS_AT(const volatile unsigned, lbase + FLG)) >= FSTART) break;
        __builtin_amdgcn_s_sleep(WSLP);
      }
      asm volatile("" ::: "memory");
      PIN();
      if (wid == 8) STAMP(dk, 10);
      u32x2 w[16];
      const __amdgpu_buffer_rsrc_t rd_ = __builtin_amdgcn_make_buffer_rsrc(const_cast<u16*>(wd), 0, D_ * F_ * 2, 0x00020000);
      auto ldw = [&](int c) __attribute__((always_inline)) {
#pragma unroll
        for (int f = 0; f < 16; ++f) w[f] = __builtin_amdgcn_raw_buffer_load_b64(rd_, lane * 8 + c * 512 + (f & 1) * 2048, (fg * 16 + (f & ~1)) * (D_ * 2) + hw * 1024, 0);
        PIN();
      };
      ldw(0);
      const unsigned pt = __builtin_amdgcn_raw_buffer_load_b32(rd_, (lane >> 2) * (D_ * 2) + (lane & 3) * 128 + 512, fg * 16 * (D_ * 2) + hw * 1024, 0);
      int yo = p1blk ? (GRAN_N + fg * D_ + hw * 512) * 4 + lane * 16 : HAND_N * 4;
      asm volatile("" : "+v"(yo));
      wait_all(2u);
      if (wid == 8) STAMP(dk, 3);
      const s16x4 a = act_diag();
      const u32x4 y0 = down(a, w);
      PIN();
      ldw(1);
      __builtin_amdgcn_raw_buffer_store_b128(y0, rsh, yo, 0, 16);
      PIN();
      __builtin_amdgcn_raw_buffer_store_b128(down(a, w), rsh, yo + 1024, 0, 16);
      asm volatile("" :: "v"(pt));
      if (wid == 8) STAMP(dk, 4);
    }
    PIN();
    [&]() __attribute__((always_inline)) {
    const int lane = fresh_lane();
    const float rmb = __uint_as_float(rmd << 16);
    auto first_words = [&](int src, auto nc) __attribute__((always_inline)) {
#pragma unroll 1
      for (int spin = 0; spin < SPIN_MAX; ++spin) {
        unsigned mx = 0u;
#pragma unroll
        for (int i = 0; i < decltype(nc)::value; ++i) mx = umax(mx, __builtin_amdgcn_raw_buffer_load_b32(rsh, src, i * (56 * D_ * 4), 16));
        if (none(mx == SENT)) break;
        __builtin_amdgcn_s_sleep(PSLP);
        asm volatile("" ::: "memory");
      }
    };
    if (hw == 2) {
      wait_all(2u);
      if (own) {
        const float rden = __uint_as_float(rmd & 0xffff0000u), rrin = frcp(rden);
        const int col = ow * 16 + (lane & 3) * 4 + (lane >> 4);
        const unsigned xo2 = ld_ag(reinterpret_cast<const unsigned*>(A + (size_t)idx * D_ + (col & ~1)));
        __builtin_amdgcn_s_sleep(PRE1);
        first_words((GRAN_N + (lane < 56 ? lane : 0) * D_ + ow * 16) * 4, std::integral_constant<int, 4>{});
        const float y = y_sum(rsh, std::integral_constant<int, 2>{});
        const unsigned rk = best >> 16;
        const float rv = __uint_as_float(((rk & 0x8000u) ? (rk & 0x7fffu) : (~rk & 0xffffu)) << 16);
        const float rs = rbf(frcp(1.0f + __expf(-rv)));
        const float x1v = __uint_as_float((col & 1) ? xo2 & 0xffff0000u : xo2 << 16);
        float h0 = x1v, h1 = x1v;
        ln_apply2(h0, h1, rmb, rden, rrin);
        const float x2v = rbf(x1v + rbf(h0 + rbf(rbf(y) * rs)));
        const auto pr = __builtin_amdgcn_permlane16_swap(__float_as_uint(x2v), __float_as_uint(x2v), false, false);
        __builtin_amdgcn_raw_buffer_store_b32(f2word(__uint_as_float((pr[0] >> 16) | pr[1])), rsh, (lane & 28) ? HAND_N * 4 : (int)(X2 - hand) * 4 + col * 2, 0, 16);
        const float d = x2v - rmb;
        const float sd = wave_sum(d), sq = wave_sum(d * d);
        if (lane < 2) st_ag(XS + ow * 2 + lane, f2word(lane ? sq : sd));
        STAMP(dk, 5);
      }
    }
    {
      const int ks = kcap ^ (tmi & 8), g4 = lane >> 4;
      const int src = (int)(X2 - hand) * 4 + ks * 128 + g4 * 16;
      int dst = (lane & 12) ? HAND_N * 4 : (int)((P4 - hand) + (tni * 16 + ks) * 192 + hw * 64 + (lane & 2) * 16 + (lane & 1) * 8 + (g4 >> 1) * 16 + (g4 & 1) * 4) * 4;
      asm volatile("" : "+v"(dst));
      u32x4 x0, x1;
      if (!(hw == 2 && own)) __builtin_amdgcn_s_sleep(PRE4);
#pragma unroll 1
      for (int spin = 0; spin < SPIN_MAX; ++spin) {
        x0 = __builtin_amdgcn_raw_buffer_load_b128(rsh, src, 0, 16); x1 = __builtin_amdgcn_raw_buffer_load_b128(rsh, src + 64, 0, 16);
        if (none(umax(umax(umax(x0[0], x0[1]), umax(x0[2], x0[3])), umax(umax(x1[0], x1[1]), umax(x1[2], x1[3]))) == SENT)) break;
        __builtin_amdgcn_s_sleep(PSLP);
        asm volatile("" ::: "memory");
      }
      f32x4 d[4];
#pragma unroll
      for (int i = 0; i < 4; ++i) d[i] = MFMA16(sl[2 * i], __builtin_bit_cast(s16x8, x0), (f32x4{0.f, 0.f, 0.f, 0.f}));
#pragma unroll
      for (int i = 0; i < 4; ++i) d[i] = MFMA16(sl[2 * i + 1], __builtin_bit_cast(s16x8, x1), d[i]);
      u32x4 o;
#pragma unroll
      for (int e = 0; e < 4; ++e) o[e] = f2word((lane & 2) ? ((lane & 1) ? d[3][e] : d[2][e]) : ((lane & 1) ? d[1][e] : d[0][e]));
      __builtin_amdgcn_raw_buffer_store_b128(o, rsh, dst, 0, 16);
      PIN();
      if (wid == 8) STAMP(dk, 6);
    }
    if (hw == 2) {
      const int col = (lane & 15) * 4 + (lane >> 4), src = (int)((P4 - hand) + (tni * 16 + (lane >> 4)) * 192 + (lane & 15) * 4) * 4;
      const __amdgpu_buffer_rsrc_t rsq = __builtin_amdgcn_make_buffer_rsrc(hand, 0, fast ? HAND_N * 4 : 0, 0x00020000);
      float cs[3], val[3];
#pragma unroll
      for (int p = 0; p < 3; ++p) cs[p] = __uint_as_float(ld_ag(reinterpret_cast<const unsigned*>(csf) + p * 1024 + n0 + col));
      u32x2 xs;
      __builtin_amdgcn_s_sleep(PRE7);
      first_words((int)((P4 - hand) + (tni * 16 + (lane & 15)) * 192 + ((lane >> 4) % 3) * 64) * 4, std::integral_constant<int, 1>{});
#pragma unroll 1
      for (int spin = 0; spin < SPIN_MAX; ++spin) {
        u32x4 v[3][4];
        xs = __builtin_amdgcn_raw_buffer_load_b64(rsh, (int)((XS - hand) + lane * 2) * 4, 0, 16);
#pragma unroll
        for (int p = 2; p >= 0; --p)
#pragma unroll
          for (int i = 0; i < 4; ++i) v[p][i] = __builtin_amdgcn_raw_buffer_load_b128(p ? rsh : rsq, src + p * 256, i * (4 * 192 * 4), 16);
        auto part = [&](int p) __attribute__((always_inline)) {
          float z[4];
#pragma unroll
          for (int e = 0; e < 4; ++e) {
            z[e] = fadd(fadd(__uint_as_float(v[p][0][e]), __uint_as_float(v[p][1][e])), fadd(__uint_as_float(v[p][2][e]), __uint_as_float(v[p][3][e])));
          }
          val[p] = fold16(f32x2{z[0], z[1]}, f32x2{z[2], z[3]});
        };
        part(2); part(1);
        val[0] = 0.f;
        if (fast) part(0);
        const float chk = fadd(fadd(fadd(val[0], val[1]), fadd(val[2], __uint_as_float(xs[0]))), __uint_as_float(xs[1]));
        if (none(chk != chk)) break;
        unsigned mx = umax(xs[0], xs[1]);
#pragma unroll
        for (int i = 0; i < 12; ++i) mx = umax(umax(umax(mx, v[i >> 2][i & 3][0]), v[i >> 2][i & 3][1]), umax(v[i >> 2][i & 3][2], v[i >> 2][i & 3][3]));
        if (none(mx == SENT)) break;
        __builtin_amdgcn_s_sleep(PSLP);
        asm volatile("" ::: "memory");
      }
      const auto sw = __builtin_amdgcn_permlane32_swap(xs[0], xs[1], false, false);
      float sm = __uint_as_float(sw[0]) + __uint_as_float(sw[1]);
      sm = dpp_add<0x142, 0xa>(dpp_add<0x118, 0xf>(dpp_add<0x114, 0xf>(dpp_add<0x112, 0xf>(dpp_add<0x111, 0xf>(sm)))));
      const float dm = __int_as_float(__builtin_amdgcn_readlane(__float_as_int(sm), 31)) * (0.25f / 1024.0f);
      const float var = fmaxf(__int_as_float(__builtin_amdgcn_readlane(__float_as_int(sm), 63)) * (0.25f / 1024.0f) - dm * dm, 0.f);
      const float mb = rbf(rmb + dm);
      const float rin = frcp(rbf(__builtin_amdgcn_sqrtf(rbf(rbf(var) + 1e-5f))));
      const unsigned kv = pack2((val[1] - mb * cs[1]) * rin, (val[2] - mb * cs[2]) * rin);
      *LDS_AT(u16, lbase + TAB + 128 + kpos(col) * 2) = (u16)kv;
      *LDS_AT(u16, lbase + TAB + 256 + col * 2) = (u16)(kv >> 16);
      if (fast) *LDS_AT(u16, lbase + TAB + kpos(col) * 2) = f2bf((val[0] - mb * cs[0]) * rin * QSCALE);
      signal(7u);
      STAMP(dk, 7);
      if (own) {
#pragma unroll
        for (int i = 0; i < 14; ++i)
          __builtin_amdgcn_raw_buffer_store_b128(u32x4{SENT, SENT, SENT, SENT}, rsh, (GRAN_N + (i * 16 + (lane >> 2)) * D_ + ow * 16 + (lane & 3) * 4) * 4, 0, 16);
      }
      return;
    }
    if (!fast) return;
    const int head = tni * 2 + hw;
    const unsigned tb = lbase + TAB + hw * 64;
    {
      const __amdgpu_buffer_rsrc_t rs = __builtin_amdgcn_make_buffer_rsrc(KV, 0, 2 * S_ * D_ * 2, 0x00020000);
      const int src = (head * 64 + tmi * 4) * 2048 + lane * 16;
      auto ld = [&](int i, unsigned off) { return __builtin_bit_cast(s16x8, __builtin_amdgcn_raw_buffer_load_b128(rs, src + (i & 3) * 1024, (i >> 3) * 65536 + ((i >> 2) & 1) * 4096 + off, 16)); };
#pragma unroll 1
      for (int i = 0; i < SPIN_MAX; ++i)
        if (__builtin_amdgcn_readfirstlane(ld_ag(kvflag + (tmi % RK) * 32 + tmi + 8)) == tag) break;
      asm volatile("" ::: "memory");
      s16x8 f[16];
#pragma unroll
      for (int i = 0; i < 16; ++i) f[i] = ld(i, 0u);
      PIN();
      const bool on = ((lane ^ (lane >> 4)) & 1) == 0;
      const unsigned pl = lbase + RED + hw * 1024;
      unsigned pw = pl + (lane >> 4) * 64 + (lane & 15) * 2, pr = pl + (on ? (lane >> 5) * 16 : 512), qr = on ? tb + (lane >> 5) * 16 : pl + 512;
      const int gone = (((idx >> 7) & 7) == tmi) ? ((idx >> 10) * 2 + (kpos(idx & 31) >> 4)) * 64 + ((idx >> 5) & 3) * 16 + (kpos(idx & 31) & 15) : -1;
      asm volatile("" : "+v"(pw), "+v"(pr), "+v"(qr));
      *LDS_AT(u32x2, pl + 512 + lane * 8) = u32x2{0u, 0u};
      wait_all(7u);
      u32x4 q[2];
#pragma unroll
      for (int sx = 0; sx < 2; ++sx) {
        q[sx] = *LDS_AT(const u32x4, qr + sx * 32);
      }
      float sc[4];
#pragma unroll
      for (int i = 0; i < 8; ++i) {
        f32x4 d = MFMA16(__builtin_bit_cast(s16x8, q[0]), f[2 * i], (f32x4{0.f, 0.f, 0.f, 0.f}));
        d = MFMA16(__builtin_bit_cast(s16x8, q[1]), f[2 * i + 1], d);
        PIN();
        f[2 * i] = ld(2 * i, VOFF); f[2 * i + 1] = ld(2 * i + 1, VOFF);
        PIN();
#pragma unroll
        for (int r = 0; r < 2; ++r) sc[(i >> 2) * 2 + r] = (i & 3) == 0 || (lane >> 4) == (i & 3) ? d[r] : sc[(i >> 2) * 2 + r];
      }
      if (gone >= 0) {
#pragma unroll
        for (int r = 0; r < 4; ++r) sc[r] = r * 64 + lane == gone ? -INFINITY : sc[r];
      }
      const float mx = wave_fmax(fmaxf(fmaxf(sc[0], sc[1]), fmaxf(sc[2], sc[3])));
      const unsigned p0 = pack2(__builtin_amdgcn_exp2f(sc[0] - mx), __builtin_amdgcn_exp2f(sc[1] - mx)), p1 = pack2(__builtin_amdgcn_exp2f(sc[2] - mx), __builtin_amdgcn_exp2f(sc[3] - mx));
      *LDS_AT(u16, pw) = (u16)p0; *LDS_AT(u16, pw + 32) = (u16)(p0 >> 16);
      *LDS_AT(u16, pw + 256) = (u16)p1; *LDS_AT(u16, pw + 288) = (u16)(p1 >> 16);
      asm volatile("" ::: "memory");
      const float ls = wave_sum((__uint_as_float(p0 << 16) + __uint_as_float(p0 & 0xffff0000u)) + (__uint_as_float(p1 << 16) + __uint_as_float(p1 & 0xffff0000u)));
      f32x4 o = {0.f, 0.f, 0.f, 0.f};
#pragma unroll
      for (int i = 0; i < 16; ++i) o = MFMA16(*LDS_AT(const s16x8, pr + i * 32), f[i], o);
      int pv = __float_as_int((lane & 16) ? o[1] : o[0]);
      asm volatile("s_nop 0\n\tv_writelane_b32 %0, %1, 32\n\tv_writelane_b32 %0, %2, 33" : "+v"(pv) : "s"(ls), "s"(mx));
      __builtin_amdgcn_raw_buffer_store_b32(f2word(__int_as_float(pv)), rsh, lane < 34 ? (int)((PA - hand) + (head * 8 + tmi) * 64 + lane) * 4 : HAND_N * 4, 0, 16);
    }
    if (wid == 8) STAMP(dk, 8);
    if (tmi != ((idx >> 7) & 7)) return;
    {
      const int src = (int)((PA - hand) + head * 512 + (lane < 34 ? lane : 0)) * 4;
      const unsigned ta = tb + (lane & 31) * 2;
      const float so = wave_sum(lane < 32 ? bf2f(*LDS_AT(const u16, ta)) * bf2f(*LDS_AT(const u16, ta + 128)) : 0.f);
      const float vo = lane < 32 ? bf2f(*LDS_AT(const u16, ta + 256)) : 1.0f;
      unsigned v[8];
      __builtin_amdgcn_s_sleep(PRE5);
#pragma unroll 1
      for (int spin = 0; spin < SPIN_MAX; ++spin) {
        unsigned mx = 0u;
#pragma unroll
        for (int i = 0; i < 8; ++i) v[i] = __builtin_amdgcn_raw_buffer_load_b32(rsh, src + i * 256, 0, 16);
#pragma unroll
        for (int i = 0; i < 8; ++i) mx = umax(mx, v[i]);
        if (none(mx == SENT)) break;
        __builtin_amdgcn_s_sleep(PSLP);
        asm volatile("" ::: "memory");
      }
      float m8[8], mx = so;
#pragma unroll
      for (int i = 0; i < 8; ++i) { m8[i] = __int_as_float(__builtin_amdgcn_readlane((int)v[i], 33)); mx = fmaxf(mx, m8[i]); }
      float acc = __builtin_amdgcn_exp2f(so - mx) * vo;
#pragma unroll
      for (int i = 0; i < 8; ++i) acc += __builtin_amdgcn_exp2f(m8[i] - mx) * __uint_as_float(v[i]);
      const float l = __int_as_float(__builtin_amdgcn_readlane(__float_as_int(acc), 32));
      const float ov = acc / l;
      const float o1 = __int_as_float(__builtin_amdgcn_update_dpp(0, __float_as_int(ov), 0x55, 0xf, 0xf, false));
      const float o2 = __int_as_float(__builtin_amdgcn_update_dpp(0, __float_as_int(ov), 0xaa, 0xf, 0xf, false));
      const float o3 = __int_as_float(__builtin_amdgcn_update_dpp(0, __float_as_int(ov), 0xff, 0xf, 0xf, false));
      const __amdgpu_buffer_rsrc_t rso = __builtin_amdgcn_make_buffer_rsrc(Of, 0, S_ * D_ * 2, 0x00020000);
      if (lane < 32 && (lane & 3) == 0)
        __builtin_amdgcn_raw_buffer_store_b64(u32x2{pack2(ov, o1), pack2(o2, o3)}, rso,
            (((idx >> 5) * 32 + head) * 1024 + (lane >> 4) * 512 + (((lane >> 2) & 1) * 32 + (idx & 31)) * 8 + ((lane >> 3) & 1) * 4) * 2, 0, 16);
      asm volatile("s_waitcnt vmcnt(0)" ::: "memory");
      if (lane < RO) st_ag(gran + GR_O + ((16 + (idx >> 7)) * RO + lane) * 64 + 16 + head, tag);
      STAMP(dk, 9);
    }
    }();
    }();
    __builtin_amdgcn_s_setprio(0);
    asm volatile("s_waitcnt vmcnt(0) lgkmcnt(0)" ::: "memory");
    PIN();
    ARGS_HERE;
    u32x2 w[16];
    auto ld_wd = [&]() __attribute__((always_inline)) {
      const __amdgpu_buffer_rsrc_t rd_ = __builtin_amdgcn_make_buffer_rsrc(const_cast<u16*>(wd), 0, D_ * F_ * 2, 0x00020000);
#pragma unroll
      for (int f = 0; f < 16; ++f) w[f] = __builtin_amdgcn_raw_buffer_load_b64(rd_, t * 8 + (f & 1) * 2048, (fg * 16 + (f & ~1)) * (D_ * 2), 0);
      PIN();
    };
    float fnum = 0.f, fvar = 0.f;
    u32x2 wq = {0u, 0u};
    const int rrow = omi * 128 + (oni >> 3) * 64 + (hw & 1) * 32;
    if (LAY2) {
      ld_wd();
      wq = *reinterpret_cast<const u32x2*>(w_r + hw * 256 + (lane & 3) * 64 + (lane >> 2) * 4);
      if (!p1blk) {
        const int fr = rrow + (lane & 7) * 4 + (lane >> 4);
        const double s1 = ld_ag(stats + fr), s2 = ld_ag(stats + S_ + fr), sw = ld_ag(stats + 2 * S_ + fr);
        lds_wr(lbase + SPM + 132, __float_as_uint(wsum));
        float mean, var, mb, den;
        ln_from_sums(s1, s2, mean, var, mb, den);
        const float g1 = 1.0f + frcp(den);
        fnum = fr == idx ? __uint_as_float(0x7fc00000u) : fmaf(-mean, wsum, (float)sw) * g1;
        fvar = var * g1 * g1;
      }
      asm volatile("" : "+v"(fnum), "+v"(fvar));
      PIN();
    }
    if (hw == 3) {
      const int nfl = (LAY2 && (idx >> 7) == omi) ? 48 : 16;
      auto pfl = (__attribute__((address_space(1))) const unsigned*)(gran + GR_O + (((LAY2 ? 16 : 0) + omi) * RO + oni % RO) * 64 + (lane < nfl ? lane : 0));
      constexpr unsigned vs = 8u * D_ * 2u - 1024u;
      unsigned vw0 = (unsigned)((lane >> 3) * D_ + oni * 64 + ((lane & 7) ^ ((lane >> 3) & 6)) * 8) * 2u, vw1 = vw0 + vs, vw2 = vw0 + 2 * vs, vw3 = vw0 + 3 * vs;
      asm volatile("" : "+v"(pfl), "+v"(vw0), "+v"(vw1), "+v"(vw2), "+v"(vw3));
      auto wait_arr = [&](unsigned n) {
#pragma unroll 1
        for (int i = 0; i < 4 * SPIN_MAX; ++i) {
          if (__builtin_amdgcn_readfirstlane(*LDS_AT(const volatile unsigned, lbase + FLG + 16)) >= n) break;
          __builtin_amdgcn_s_sleep(1);
        }
        asm volatile("" ::: "memory");
        PIN();
      };
      wait_arr(12u);
      unsigned fl;
      asm volatile("global_load_dword %0, %1, off sc1" : "=v"(fl) : "v"(pfl) : "memory");
#pragma unroll 1
      for (int i = 0; i < 4 * SPIN_MAX; ++i) {
        if (__builtin_amdgcn_readfirstlane(*LDS_AT(const volatile unsigned, lbase + FLG + 24)) >= 8u) break;
        __builtin_amdgcn_s_sleep(1);
      }
      asm volatile("" ::: "memory");
      PIN();
#pragma unroll
      for (int j = 24; j < 32; ++j)
        glds16x4<false>(reinterpret_cast<const char*>(wo) + j * (32u * D_ * 2u), vw0, vw1, vw2, vw3, lbase + j * 4096);
      unsigned keep;
      asm volatile("s_mov_b32 %0, m0\n\ts_mov_b32 m0, %3\n\ts_nop 2\n\tglobal_load_lds_dword %1, %2 sc1\n\ts_mov_b32 m0, %0"
                   : "=&s"(keep) : "v"(0u), "s"(reinterpret_cast<const char*>(gran + GR_N + bid * 64 + 39)), "s"(lbase + FLG - 252) : "memory");
      asm volatile("s_waitcnt vmcnt(33)" : "+v"(fl) :: "memory");
      if (__builtin_amdgcn_ballot_w64(fl != tag)) {
        __builtin_amdgcn_s_sleep(CSL1);
#pragma unroll 1
        for (int spin = 0; spin < SPIN_MAX; ++spin) {
          if (!__builtin_amdgcn_ballot_w64(__hip_atomic_load(pfl, __ATOMIC_RELAXED, __HIP_MEMORY_SCOPE_AGENT) != tag)) break;
          __builtin_amdgcn_s_sleep(CSLP);
        }
      }
      asm volatile("" ::: "memory");
      PIN();
    }
    u32x4 pv[4];
    asm volatile("" : "=v"(pv[0]), "=v"(pv[1]), "=v"(pv[2]), "=v"(pv[3]));
    if (LAY2 && !p1blk && hw < 2) {
      const __amdgpu_buffer_rsrc_t rsp = __builtin_amdgcn_make_buffer_rsrc(hand + 2 * HAND_N + PP_O, 0, H_ * S_ * 4, 0x00020000);
      const int src = ((lane >> 3) * S_ + rrow + (lane & 7) * 4) * 4;
#pragma unroll 1
      for (int i = 0; i < 4 * SPIN_MAX; ++i) {
        if (__builtin_amdgcn_readfirstlane(*LDS_AT(const volatile unsigned, lbase + FLG + 24)) >= 8u) break;
        __builtin_amdgcn_s_sleep(2);
      }
#pragma unroll 1
      for (int spin = 0; spin < SPIN_MAX; ++spin) {
        asm volatile("" ::: "memory");
        unsigned mx = 0u;
#pragma unroll
        for (int i = 0; i < 4; ++i) pv[i] = __builtin_amdgcn_raw_buffer_load_b128(rsp, src, i * (8 * S_ * 4), 16);
#pragma unroll
        for (int i = 0; i < 4; ++i) mx = umax(umax(umax(mx, pv[i][0]), pv[i][1]), umax(pv[i][2], pv[i][3]));
        if (none(mx == SENT)) break;
        __builtin_amdgcn_s_sleep(PPSLP);
      }
    }
    asm volatile("s_waitcnt lgkmcnt(0)" ::: "memory");
    __builtin_amdgcn_s_barrier();
    if (!LAY2) {
      PIN();
      asm volatile(AGAIN "\ngate_up_code:\n\t"
                 "v_lshlrev_b32 v84, 16, v100\n\tv_and_b32 v85, 0xffff0000, v100\n\tv_lshlrev_b32 v86, 16, v101\n\tv_and_b32 v87, 0xffff0000, v101\n\t"
                 "v_subrev_f32 v84, s84, v84\n\tv_subrev_f32 v85, s84, v85\n\tv_subrev_f32 v86, s84, v86\n\tv_subrev_f32 v87, s84, v87\n\t"
                 "v_cvt_pk_bf16_f32 v85, v84, v85\n\tv_cvt_pk_bf16_f32 v87, v86, v87\n\ts_mov_b32 s88, 0x01000100\n\ts_mov_b32 s89, 0x03020302\n\t"
                 "v_lshlrev_b32 v84, 16, v85\n\tv_and_b32 v85, 0xffff0000, v85\n\tv_lshlrev_b32 v86, 16, v87\n\tv_and_b32 v87, 0xffff0000, v87\n\t"
                 "v_mul_f32 v88, s86, v84\n\tv_mul_f32 v89, s86, v85\n\tv_mul_f32 v90, s86, v86\n\tv_mul_f32 v91, s86, v87\n\tv_fma_f32 v84, -v88, s85, v84\n\t"
                 "v_fma_f32 v85, -v89, s85, v85\n\tv_fma_f32 v86, -v90, s85, v86\n\tv_fma_f32 v87, -v91, s85, v87\n\tv_fmac_f32 v88, s86, v84\n\t"
                 "v_fmac_f32 v89, s86, v85\n\tv_fmac_f32 v90, s86, v86\n\tv_fmac_f32 v91, s86, v87\n\tv_cvt_pk_bf16_f32 v88, v88, v89\n\t"
                 "v_cvt_pk_bf16_f32 v90, v90, v91\n\tv_perm_b32 v92, v88, v88, s88\n\tv_perm_b32 v93, v88, v88, s89\n\tv_perm_b32 v94, v90, v90, s88\n\t"
                 "v_perm_b32 v95, v90, v90, s89\n\ts_nop 0\n\tv_and_b32_dpp v96, v92, v102 quad_perm:[0,0,0,0] row_mask:0xf bank_mask:0xf\n\t"
                 "v_and_b32_dpp v97, v92, v103 quad_perm:[0,0,0,0] row_mask:0xf bank_mask:0xf\n\t"
                 "v_and_b32_dpp v100, v93, v102 quad_perm:[0,0,0,0] row_mask:0xf bank_mask:0xf\n\t"
                 "v_and_b32_dpp v101, v93, v103 quad_perm:[0,0,0,0] row_mask:0xf bank_mask:0xf\n\tv_mfma_f32_4x4x4_16b_bf16 v[84:87], v[96:97], v[104:105], 0\n\t"
                 "v_mfma_f32_4x4x4_16b_bf16 v[88:91], v[96:97], v[106:107], 0\n\tv_and_b32_dpp v96, v94, v102 quad_perm:[0,0,0,0] row_mask:0xf bank_mask:0xf\n\t"
                 "v_and_b32_dpp v97, v94, v103 quad_perm:[0,0,0,0] row_mask:0xf bank_mask:0xf\n\t"
                 "v_mfma_f32_4x4x4_16b_bf16 v[84:87], v[100:101], v[108:109], v[84:87]\n\tv_mfma_f32_4x4x4_16b_bf16 v[88:91], v[100:101], v[110:111], v[88:91]\n\t"
                 "v_and_b32_dpp v100, v95, v102 quad_perm:[0,0,0,0] row_mask:0xf bank_mask:0xf\n\t"
                 "v_and_b32_dpp v101, v95, v103 quad_perm:[0,0,0,0] row_mask:0xf bank_mask:0xf\n\tv_mfma_f32_4x4x4_16b_bf16 v[84:87], v[96:97], v[112:113], v[84:87]\n\t"
                 "v_mfma_f32_4x4x4_16b_bf16 v[88:91], v[96:97], v[114:115], v[88:91]\n\tv_and_b32_dpp v96, v92, v102 quad_perm:[1,1,1,1] row_mask:0xf bank_mask:0xf\n\t"
                 "v_and_b32_dpp v97, v92, v103 quad_perm:[1,1,1,1] row_mask:0xf bank_mask:0xf\n\t"
                 "v_mfma_f32_4x4x4_16b_bf16 v[84:87], v[100:101], v[116:117], v[84:87]\n\tv_mfma_f32_4x4x4_16b_bf16 v[88:91], v[100:101], v[118:119], v[88:91]\n\t"
                 "v_and_b32_dpp v100, v93, v102 quad_perm:[1,1,1,1] row_mask:0xf bank_mask:0xf\n\t"
                 "v_and_b32_dpp v101, v93, v103 quad_perm:[1,1,1,1] row_mask:0xf bank_mask:0xf\n\tv_mfma_f32_4x4x4_16b_bf16 v[84:87], v[96:97], v[120:121], v[84:87]\n\t"
                 "v_mfma_f32_4x4x4_16b_bf16 v[88:91], v[96:97], v[122:123], v[88:91]\n\tv_and_b32_dpp v96, v94, v102 quad_perm:[1,1,1,1] row_mask:0xf bank_mask:0xf\n\t"
                 "v_and_b32_dpp v97, v94, v103 quad_perm:[1,1,1,1] row_mask:0xf bank_mask:0xf\n\t"
                 "v_mfma_f32_4x4x4_16b_bf16 v[84:87], v[100:101], v[124:125], v[84:87]\n\tv_mfma_f32_4x4x4_16b_bf16 v[88:91], v[100:101], v[126:127], v[88:91]\n\t"
                 "v_and_b32_dpp v100, v95, v102 quad_perm:[1,1,1,1] row_mask:0xf bank_mask:0xf\n\t"
                 "v_and_b32_dpp v101, v95, v103 quad_perm:[1,1,1,1] row_mask:0xf bank_mask:0xf\n\tv_mfma_f32_4x4x4_16b_bf16 v[84:87], v[96:97], v[128:129], v[84:87]\n\t"
                 "v_mfma_f32_4x4x4_16b_bf16 v[88:91], v[96:97], v[130:131], v[88:91]\n\tv_and_b32_dpp v96, v92, v102 quad_perm:[2,2,2,2] row_mask:0xf bank_mask:0xf\n\t"
                 "v_and_b32_dpp v97, v92, v103 quad_perm:[2,2,2,2] row_mask:0xf bank_mask:0xf\n\t"
                 "v_mfma_f32_4x4x4_16b_bf16 v[84:87], v[100:101], v[132:133], v[84:87]\n\tv_mfma_f32_4x4x4_16b_bf16 v[88:91], v[100:101], v[134:135], v[88:91]\n\t"
                 "v_and_b32_dpp v100, v93, v102 quad_perm:[2,2,2,2] row_mask:0xf bank_mask:0xf\n\t"
                 "v_and_b32_dpp v101, v93, v103 quad_perm:[2,2,2,2] row_mask:0xf bank_mask:0xf\n\tv_mfma_f32_4x4x4_16b_bf16 v[84:87], v[96:97], v[136:137], v[84:87]\n\t"
                 "v_mfma_f32_4x4x4_16b_bf16 v[88:91], v[96:97], v[138:139], v[88:91]\n\tv_and_b32_dpp v96, v94, v102 quad_perm:[2,2,2,2] row_mask:0xf bank_mask:0xf\n\t"
                 "v_and_b32_dpp v97, v94, v103 quad_perm:[2,2,2,2] row_mask:0xf bank_mask:0xf\n\t"
                 "v_mfma_f32_4x4x4_16b_bf16 v[84:87], v[100:101], v[140:141], v[84:87]\n\tv_mfma_f32_4x4x4_16b_bf16 v[88:91], v[100:101], v[142:143], v[88:91]\n\t"
                 "v_and_b32_dpp v100, v95, v102 quad_perm:[2,2,2,2] row_mask:0xf bank_mask:0xf\n\t"
                 "v_and_b32_dpp v101, v95, v103 quad_perm:[2,2,2,2] row_mask:0xf bank_mask:0xf\n\tv_mfma_f32_4x4x4_16b_bf16 v[84:87], v[96:97], v[144:145], v[84:87]\n\t"
                 "v_mfma_f32_4x4x4_16b_bf16 v[88:91], v[96:97], v[146:147], v[88:91]\n\tv_and_b32_dpp v96, v92, v102 quad_perm:[3,3,3,3] row_mask:0xf bank_mask:0xf\n\t"
                 "v_and_b32_dpp v97, v92, v103 quad_perm:[3,3,3,3] row_mask:0xf bank_mask:0xf\n\t"
                 "v_mfma_f32_4x4x4_16b_bf16 v[84:87], v[100:101], v[148:149], v[84:87]\n\tv_mfma_f32_4x4x4_16b_bf16 v[88:91], v[100:101], v[150:151], v[88:91]\n\t"
                 "v_and_b32_dpp v100, v93, v102 quad_perm:[3,3,3,3] row_mask:0xf bank_mask:0xf\n\t"
                 "v_and_b32_dpp v101, v93, v103 quad_perm:[3,3,3,3] row_mask:0xf bank_mask:0xf\n\tv_mfma_f32_4x4x4_16b_bf16 v[84:87], v[96:97], v[152:153], v[84:87]\n\t"
                 "v_mfma_f32_4x4x4_16b_bf16 v[88:91], v[96:97], v[154:155], v[88:91]\n\tv_and_b32_dpp v96, v94, v102 quad_perm:[3,3,3,3] row_mask:0xf bank_mask:0xf\n\t"
                 "v_and_b32_dpp v97, v94, v103 quad_perm:[3,3,3,3] row_mask:0xf bank_mask:0xf\n\t"
                 "v_mfma_f32_4x4x4_16b_bf16 v[84:87], v[100:101], v[156:157], v[84:87]\n\tv_mfma_f32_4x4x4_16b_bf16 v[88:91], v[100:101], v[158:159], v[88:91]\n\t"
                 "v_and_b32_dpp v100, v95, v102 quad_perm:[3,3,3,3] row_mask:0xf bank_mask:0xf\n\t"
                 "v_and_b32_dpp v101, v95, v103 quad_perm:[3,3,3,3] row_mask:0xf bank_mask:0xf\n\tv_mfma_f32_4x4x4_16b_bf16 v[84:87], v[96:97], v[160:161], v[84:87]\n\t"
                 "v_mfma_f32_4x4x4_16b_bf16 v[88:91], v[96:97], v[162:163], v[88:91]\n\ts_nop 1\n\t"
                 "v_mfma_f32_4x4x4_16b_bf16 v[84:87], v[100:101], v[164:165], v[84:87]\n\tv_mfma_f32_4x4x4_16b_bf16 v[88:91], v[100:101], v[166:167], v[88:91]\n\t"
                 "s_nop 4\n\tv_permlane32_swap_b32 v84, v88\n\tv_permlane32_swap_b32 v85, v89\n\tv_permlane32_swap_b32 v86, v90\n\tv_permlane32_swap_b32 v87, v91\n\t"
                 "v_add_f32 v84, v84, v88\n\tv_add_f32 v85, v85, v89\n\tv_add_f32 v86, v86, v90\n\tv_add_f32 v87, v87, v91\n\ts_nop 0\n\t"
                 "v_permlane16_swap_b32 v84, v85\n\tv_permlane16_swap_b32 v86, v87\n\ts_nop 0\n\tv_add_f32 v84, v84, v85\n\tv_add_f32 v86, v86, v87\n\ts_nop 1\n\t"
                 "v_add_f32_dpp v84, v84, v84 row_ror:8 row_mask:0xf bank_mask:0xf\n\tv_add_f32_dpp v86, v86, v86 row_ror:8 row_mask:0xf bank_mask:0xf\n\ts_nop 0\n\t"
                 "v_add_f32_dpp v84, v84, v84 row_ror:4 row_mask:0xf bank_mask:0xf\n\tv_add_f32_dpp v86, v86, v86 row_ror:4 row_mask:0xf bank_mask:0xf\n\t"
                 "ds_write2_b32 v99, v84, v86 offset1:8\n\ts_setpc_b64 s[92:93]\n\t"
                   :: "{s[0:1]}"(ap + 1), "{s2}"(bid), "{v0}"(wid * 64), KEPT() : "s90", "s91", "scc", "memory");
      __builtin_unreachable();
    }
    {
    constexpr int dk = 8;
    constexpr int BEST = FLG + 40, MEET = FLG + 48;
    __builtin_amdgcn_s_setprio(HPRIO);
    if (hw == 0) STAMP(dk, 0);
    const unsigned tb1 = (unsigned)__builtin_amdgcn_s_memrealtime();
    unsigned* hand2 = hand + HAND_N;
    const __amdgpu_buffer_rsrc_t rsh = __builtin_amdgcn_make_buffer_rsrc(hand2, 0, (HAND_N + PS_N) * 4, 0x00020000);
    const __amdgpu_buffer_rsrc_t rsx = __builtin_amdgcn_make_buffer_rsrc(xout, 0, 3 * D_ * 2, 0x00020000);
    constexpr int OOR = (HAND_N + PS_N) * 4;
    if (!p1blk && hw < 2) {
      f32x2 a[2], c[2];
#pragma unroll
      for (int e = 0; e < 4; ++e) {
        a[e >> 1][e & 1] = (__uint_as_float(pv[0][e] << 16) + __uint_as_float(pv[1][e] << 16)) + (__uint_as_float(pv[2][e] << 16) + __uint_as_float(pv[3][e] << 16));
        c[e >> 1][e & 1] = (__uint_as_float(pv[0][e] & 0xffff0000u) + __uint_as_float(pv[1][e] & 0xffff0000u)) + (__uint_as_float(pv[2][e] & 0xffff0000u) + __uint_as_float(pv[3][e] & 0xffff0000u));
      }
      const float r = (fnum + fold8(a[0], a[1])) * __builtin_amdgcn_rsqf(fvar + fold8(c[0], c[1]));
      unsigned key = (__float_as_uint(r) ^ ((unsigned)(__float_as_int(r) >> 31) | 0x80000000u)) & 0xfffff800u;
      if (r != r) key = 0u;
      key = wave_umax(key | (unsigned)(2047 - (rrow + (lane & 7) * 4 + (lane >> 4))));
      __builtin_amdgcn_raw_buffer_store_b64(u32x2{key, tb1 & 0x7fffffffu}, rsh, lane < NREP ? (HAND_N + PK_O + lane * 128 + (rrow >> 5) * 2) * 4 : OOR, 0, 16);
      if (hw == 1) STAMP(dk, 2);
    }
    gu_setup();
    bool got = true;
    unsigned fra = lbase;
    int fro = S_ * D_ * 2;
    auto get_fc = [&]() __attribute__((always_inline)) {
      const u32x2 fw = __builtin_amdgcn_raw_buffer_load_b64(rsh, (HAND_N + PK_O + (bid % NREP) * 128 + lane * 2) * 4, 0, 16);
      got = none(umax(fw[0], fw[1]) == SENT);
      if (got) {
        const unsigned c1 = wave_umax(fw[0]), c2 = wave_umax(fw[0] == c1 ? 0u : fw[0]);
#define BADFC 0
        const unsigned r1 = (2047u - (c1 & 0x7ffu)) ^ BADFC, r2 = 2047u - (c2 & 0x7ffu);
        const unsigned r = (lane & 8) ? r2 : r1;
        fra = lbase + QOFF + (r & 127u) * 128 + (((r ^ lane) & 7u) << 4);
        fro = (r >> 7) == (unsigned)omi && lane < 16 ? ((lane >> 3) * D_ + oni * 64 + (lane & 7) * 8) * 2 : S_ * D_ * 2;
        unsigned dt = (unsigned)((int)((fw[1] - tb1) << 1) >> 1) + 512u;
        dt = dt < 1024u ? dt : 512u;
        *LDS_AT(volatile u32x2, lbase + SPM + 144) = u32x2{r1 | (r2 << 16), (unsigned)__builtin_amdgcn_readlane(dt, r1 >> 5) | (wave_umax(dt) << 16)};
      }
    };
    if (hw == 0) {
      __builtin_amdgcn_s_sleep(FCPRE);
#pragma unroll 1
      for (int i = 0; i < SPIN_MAX; ++i) {
        if (__builtin_amdgcn_readfirstlane(*LDS_AT(const volatile unsigned, lbase + FLG + 28)) - best >= 4u) break;
        __builtin_amdgcn_s_sleep(FCSLP);
      }
      asm volatile("" ::: "memory");
      get_fc();
    } else __builtin_amdgcn_s_sleep(TPRE - 10);
    PIN();
    tile_at(2u);
    const unsigned tb2 = (unsigned)__builtin_amdgcn_s_memrealtime() - 512u;
    LOGDEF;
    LOG();
#pragma unroll 1
    for (int spin = 0; spin < SPIN_MAX && !got; ++spin) { get_fc(); asm volatile("" ::: "memory"); }
    if (hw == 0) __builtin_amdgcn_raw_buffer_store_b128(lds_rd4(fra), rsx, fro, 0, XAUX);
    PIN();
    u32x2 fm = {SENT, 0u};
#pragma unroll 1
    for (int i = 0; i < 4 * SPIN_MAX && fm[0] == SENT; ++i) {
      const u32x2 q = *LDS_AT(const volatile u32x2, lbase + SPM + 144);
      fm = u32x2{(unsigned)__builtin_amdgcn_readfirstlane(q[0]), (unsigned)__builtin_amdgcn_readfirstlane(q[1])};
    }
    const int fr1 = fm[0] & 0x7ffu, fr2 = (fm[0] >> 16) & 0x7ffu;
    const unsigned t1 = tb2 + (fm[1] & 1023u), tmx = tb2 + ((fm[1] >> 16) & 1023u);
    auto until = [&](unsigned te) { while ((int)((unsigned)__builtin_amdgcn_s_memrealtime() - te) < 0) __builtin_amdgcn_s_sleep(1); asm volatile("" ::: "memory"); };
    lds_wr(lbase + SPM + 136, tmx);
    PIN();
    if (hw == 0) STAMP(dk, 3);
    const int xoff = (hw * 256 + (lane & 3) * 64 + (lane >> 2) * 4) * 2;
    const int oc = (omi * 2 + (oni >> 3)) * 32, col = oc + (lane & 7) * 4 + (lane >> 4);
    const int xoo = !p1blk && hw == 3 ? (col & ~1) * 2 : S_ * D_ * 2;
    auto get_row = [&](int r, int sl, u32x2& xr, unsigned& xo) __attribute__((always_inline)) {
      if ((r >> 7) == omi) {
#pragma unroll 1
        for (int i = 0; i < SPIN_MAX; ++i) {
          if (__builtin_amdgcn_readfirstlane(*LDS_AT(const volatile unsigned, lbase + FLG + 24)) >= 16u) break;
          __builtin_amdgcn_s_sleep(1);
        }
        asm volatile("" ::: "memory");
      }
#pragma unroll 1
      for (int spin = 0; spin < SPIN_MAX; ++spin) {
        LOG();
        xr = __builtin_amdgcn_raw_buffer_load_b64(rsx, xoff, sl * (D_ * 2), 16);
        xo = __builtin_amdgcn_raw_buffer_load_b32(rsx, xoo, sl * (D_ * 2), 16);
        LOG();
        if (none(umax(umax(xr[0], xr[1]), xo) == SENT)) break;
        __builtin_amdgcn_s_sleep(XRSLP);
        asm volatile("" ::: "memory");
      }
      PIN();
    };
    unsigned rk1;
    auto row_md = [&](const u32x2& xr, int k) __attribute__((always_inline)) {
      auto dot = [&](unsigned x, unsigned y, float z) { return __builtin_amdgcn_fdot2_f32_bf16(__builtin_bit_cast(bf16x2, x), __builtin_bit_cast(bf16x2, y), z, false); };
      const float p1 = dot(xr[1], 0x3f803f80u, dot(xr[0], 0x3f803f80u, 0.f)), p2 = dot(xr[1], xr[1], dot(xr[0], xr[0], 0.f)), p3 = dot(xr[1], wq[1], dot(xr[0], wq[0], 0.f));
      const auto q = __builtin_amdgcn_permlane32_swap(__float_as_uint(p1), __float_as_uint(p2), false, false);
      const auto q3 = __builtin_amdgcn_permlane32_swap(__float_as_uint(p3), __float_as_uint(p3), false, false);
      const auto q2 = __builtin_amdgcn_permlane16_swap(__float_as_uint(__uint_as_float(q[0]) + __uint_as_float(q[1])), __float_as_uint(__uint_as_float(q3[0]) + __uint_as_float(q3[1])), false, false);
      float sv = __uint_as_float(q2[0]) + __uint_as_float(q2[1]);
      sv = dpp_add<0x121, 0xf>(dpp_add<0x122, 0xf>(dpp_add<0x124, 0xf>(dpp_add<0x128, 0xf>(sv))));
      lds_wr(lbase + SPM + k * 64 + (lane >> 4) * 16 + hw * 4, f2word(sv));
      u32x4 a, c, e;
#pragma unroll 1
      for (int i = 0; i < SPIN_MAX; ++i) {
        a = lds_rd4(lbase + SPM + k * 64); c = lds_rd4(lbase + SPM + k * 64 + 32); e = lds_rd4(lbase + SPM + k * 64 + 16);
        if (__builtin_amdgcn_readfirstlane(umax(umax(a[0], a[1]), umax(a[2], a[3]))) != SENT) break;
      }
      auto sum4 = [&](const u32x4& v) { return (__uint_as_float(v[0]) + __uint_as_float(v[1])) + (__uint_as_float(v[2]) + __uint_as_float(v[3])); };
      const float mean = sum4(a) * (1.0f / 1024.0f), msq = sum4(c) * (1.0f / 1024.0f);
      const float den = rbf(__builtin_amdgcn_sqrtf(rbf(rbf(fmaxf(fmaf(-mean, mean, msq), 0.f)) + 1e-5f)));
      if (k == 0) { const float r = fmaf(-rbf(mean), wsum, sum4(e)) * frcp(den); rk1 = __builtin_amdgcn_readfirstlane(pack2(r, r)) >> 16; }
      return (unsigned)__builtin_amdgcn_readfirstlane((pack2(mean, mean) & 0xffffu) | (__float_as_uint(den) & 0xffff0000u));
    };
    auto ffn = [&](const u32x2& xr, unsigned md, unsigned n, const __amdgpu_buffer_rsrc_t rs, int yo, auto&& off) __attribute__((always_inline)) {
      gate_up(xr, md);
      if (hw == 0 && n == 2u) STAMP(dk, 8);
      asm volatile("" ::: "memory");
      PIN();
      lds_wr(lbase + MEET + hw * 4, n + off());
      u32x4 mt;
#pragma unroll 1
      for (int i = 0; i < SPIN_MAX; ++i) {
        mt = lds_rd4(lbase + MEET);
        if (__builtin_amdgcn_readfirstlane(min4(mt)) >= n) break;
      }
      asm volatile("" ::: "memory");
      PIN();
      if (hw == 0 && n == 2u) STAMP(dk, 9);
      if (__builtin_amdgcn_readfirstlane(mt[0]) != n + 1u)
        __builtin_amdgcn_raw_buffer_store_b128(down(act_diag(n == 4u ? 512 : 0), w), rs, (yo + fg * D_) * 4 + t * 16, 0, 16);
      dad += n == 2u ? 512 : -512;
      if (hw == 0 && n == 2u) STAMP(dk, 4);
    };
    auto y_sum4 = [&](const __amdgpu_buffer_rsrc_t rs, int yo, int k) __attribute__((always_inline)) {
      const int src = (yo + (hw * 56 + (lane >> 3)) * D_ + oc + (lane & 7) * 4) * 4;
      u32x4 v[7];
#pragma unroll
      for (int i = 0; i < 7; ++i) asm volatile("" : "=v"(v[i]));
      unsigned todo = 0x7fu;
#pragma unroll 1
      for (int spin = 0; spin < SPIN_MAX; ++spin) {
        LOG();
#pragma unroll
        for (int i = 0; i < 7; ++i) if ((todo >> i) & 1u) v[i] = __builtin_amdgcn_raw_buffer_load_b128(rs, src, i * (8 * D_ * 4), 16);
#pragma unroll
        for (int i = 0; i < 7; ++i) if (none(umax(umax(v[i][0], v[i][1]), umax(v[i][2], v[i][3])) == SENT)) todo &= ~(1u << i);
        if (!todo) break;
        __builtin_amdgcn_s_sleep(YSLP);
        asm volatile("" ::: "memory");
      }
      f32x2 z0 = {0.f, 0.f}, z1 = {0.f, 0.f};
#pragma unroll
      for (int i = 0; i < 7; ++i) {
        z0 += f32x2{__uint_as_float(v[i][0]), __uint_as_float(v[i][1])};
        z1 += f32x2{__uint_as_float(v[i][2]), __uint_as_float(v[i][3])};
      }
      const unsigned ya = lbase + SPM + 256 + k * 512 + ((lane & 7) * 4 + (lane >> 4)) * 16;
      lds_wr((lane & 8) ? lbase + DUM + lane * 4 : ya + hw * 4, f2word(fold8(z0, z1)));
      u32x4 q = {0u, 0u, 0u, 0u};
      if (hw == 3) {
#pragma unroll 1
        for (int i = 0; i < 4 * SPIN_MAX; ++i) {
          q = lds_rd4(ya);
          if (none(umax(umax(q[0], q[1]), umax(q[2], q[3])) == SENT)) break;
        }
      }
      return (__uint_as_float(q[0]) + __uint_as_float(q[1])) + (__uint_as_float(q[2]) + __uint_as_float(q[3]));
    };
    const __amdgpu_buffer_rsrc_t rs1 = __builtin_amdgcn_make_buffer_rsrc(hand, 0, HAND_N * 4, 0x00020000);
    u32x2 xr;
    unsigned xo, xo2;
    until(t1 + XDT);
    get_row(fr1, 0, xr, xo);
    if (hw == 0) STAMP(dk, 7);
#define FAILFC 0
    const unsigned md1 = row_md(xr, 0) ^ (FAILFC & 1);
    if (hw == 0) STAMP(dk, 10);
    LOGAT(20);
    u32x2 xr2;
    unsigned md2 = 0u;
    if (p1blk) {
      xr2 = __builtin_amdgcn_raw_buffer_load_b64(rsx, (fr2 >> 7) == omi ? S_ * D_ * 2 : xoff, D_ * 2, 16);
      PIN();
      ffn(xr, md1, 2u, rsh, GRAN_N, [&]() { return 0u; });
      __builtin_amdgcn_s_setprio(R2PRIO);
      LOG();
      if ((fr2 >> 7) == omi || !none(umax(xr2[0], xr2[1]) == SENT)) get_row(fr2, 1, xr2, xo2);
      md2 = row_md(xr2, 1) ^ (FAILFC & 2);
      ffn(xr2, md2, 4u, rs1, GRAN_N, [&]() {
        const u32x2 v = *LDS_AT(const volatile u32x2, lbase + BEST);
        return v[0] != 0u && (2047 - (int)(v[0] & 0x7ffu) != fr2 || v[1] != md2) ? 1u : 0u; });
    }
    if (hw == 0) STAMP(dk, 5);
    LOG();
    float y = 0.f;
    auto put_row = [&](int r, unsigned md, unsigned rb) __attribute__((always_inline)) {
      const float rden = __uint_as_float(md & 0xffff0000u), rs = rbf(frcp(1.0f + __expf(-__uint_as_float(rb << 16))));
      const float xa = __uint_as_float((col & 1) ? xo & 0xffff0000u : xo << 16);
      float ha = xa, hb = xa;
      ln_apply2(ha, hb, __uint_as_float(md << 16), rden, frcp(rden));
      const float o = xa + rbf(ha + rbf(rbf(y) * rs));
      __builtin_amdgcn_raw_buffer_store_b16((unsigned short)(pack2(o, o) & 0xffffu), __builtin_amdgcn_make_buffer_rsrc(out, 0, hw == 3 ? S_ * D_ * 2 : 0, 0x00020000),
                                            (lane & 8) ? S_ * D_ * 2 : (r * D_ + col) * 2, 0, OAUX);
    };
    if (!p1blk) {
      until(t1 + XDT + YD1); until(tmx + YD2);
      if (hw == 3) STAMP(dk, 13);
      LOGAT(8);
      y = y_sum4(rsh, GRAN_N, 0);
      put_row(fr1, md1, rk1);
      if (hw == 3) STAMP(dk, 14);
      LOGAT(24);
    }
    unsigned bmd = 0u;
    best = 0u;
#pragma unroll 1
    for (int spin = 0; spin < 4 * SPIN_MAX; ++spin) {
      const u32x2 v = *LDS_AT(const volatile u32x2, lbase + BEST);
      best = __builtin_amdgcn_readfirstlane(v[0]); bmd = __builtin_amdgcn_readfirstlane(v[1]);
      if (best) break;
      __builtin_amdgcn_s_sleep(1);
    }
    asm volatile("" ::: "memory");
    const int idx = 2047 - (int)(best & 0x7ffu);
    const unsigned rk = best >> 16, rb = (rk & 0x8000u) ? (rk & 0x7fffu) : (~rk & 0xffffu);
    if (hw == 0) STAMP(dk, 1);
    LOGAT(25);
    LOGOUT(hw);
    if (__builtin_expect(idx != fr1 || bmd != md1 || rb != rk1, 0)) {
      const __amdgpu_buffer_rsrc_t rsf = __builtin_amdgcn_make_buffer_rsrc(gran + GR_F, 0, !p1blk && hw == 3 && idx != fr1 ? 48 * 128 : 0, 0x00020000);
      asm volatile("s_waitcnt vmcnt(0)" ::: "memory");
      __builtin_amdgcn_raw_buffer_store_b32(tag, rsf, lane == 0 ? (16 + (oc >> 5)) * 128 : 48 * 128, 0, 16);
      if (idx != fr1 || bmd != md1) {
        if (!p1blk || idx != fr2 || bmd != md2) get_row(idx, idx == fr1 ? 0 : (idx == fr2 ? 1 : 2), xr, xo);
        if (!p1blk && idx == fr2) md2 = row_md(xr, 1) ^ (FAILFC & 2);
        if (idx == fr2 && bmd == md2) {
          if (!p1blk) y = y_sum4(rs1, GRAN_N, 1);
        } else {
          if (p1blk) ffn(xr, bmd, 6u, rsh, HAND_N + Y3_O, [&]() { return 0u; });
          else y = y_sum4(rsh, HAND_N + Y3_O, 2);
        }
      }
      if (p1blk || hw != 3) return;
#pragma unroll 1
      for (int spin = 0; spin < SPIN_MAX && idx != fr1; ++spin) {
        if (__builtin_amdgcn_readfirstlane(__builtin_amdgcn_raw_buffer_load_b32(rsf, (oc >> 6) * 128, 0, 16)) == tag) break;
        asm volatile("" ::: "memory");
      }
      put_row(idx, bmd, rb);
    }
    if (p1blk || hw != 3) return;
    }
    return;
  }
  if (wid >= 4) {
    const int lane = fresh_lane();
    const int lw = wid - 4;
    const long long lay_ = ap->lay;
    const u16* A = ap->a; const u16* W = ap->W; const u16* wc = ap->wc;
#define LPRIO 3
#define LP0 3
    __builtin_amdgcn_s_setprio(LP0);
    unsigned va[4], vw[6];
    const unsigned ul = lane;
    va[0] = (unsigned)(m0 + lw * 32) * (K * 2) + (ul >> 3) * (K * 2) + (((ul & 7u) ^ (ul >> 4)) << 4);
    va[1] = va[0] + (8 * K * 2 - 1024);
    va[2] = (va[0] ^ 64u) + 2 * (8 * K * 2 - 1024);
    va[3] = va[2] + (8 * K * 2 - 1024);
#pragma unroll
    for (int i = 0; i < 3; ++i) {
      const unsigned n = ul + 64u * i, k = __umul24(n, 171u) >> 12, cp = n - __umul24(k, 24u);
      vw[i] = __umul24(k, ldw * 2) + (cp >> 3) * 2048u + (((cp & 7u) ^ ((k & 2u) << 1)) << 4) + ((unsigned)(lw * 16 * ldw + n0) * 2u - i * 1024u);
      vw[i + 3] = (vw[i] ^ 16u) + 8 * ldw * 2;
    }
    PIN();
    asm volatile("" : "+v"(va[0]), "+v"(va[1]), "+v"(va[2]), "+v"(va[3]), "+v"(vw[0]), "+v"(vw[1]), "+v"(vw[2]), "+v"(vw[3]), "+v"(vw[4]), "+v"(vw[5]));
    const bool LAY2 = lay_ != 0;
    const int krot = tmi & 8;
    if (LAY2 && slow_cols(W, tni)) W = wc;
    auto stage_a = [&](int kt, int slot) {
      const char* pa = reinterpret_cast<const char*>(A) + (size_t)(kt ^ krot) * 128;
      asm volatile("s_mov_b32 m0, %5\n\ts_nop 0\n\tglobal_load_lds_dwordx4 %0, %4 sc1\n\tglobal_load_lds_dwordx4 %1, %4 offset:1024 sc1\n\t"
                   "global_load_lds_dwordx4 %2, %4 offset:2048 sc1\n\tglobal_load_lds_dwordx4 %3, %4 offset:3072 sc1"
                   :: "v"(va[0]), "v"(va[1]), "v"(va[2]), "v"(va[3]), "s"(pa), "s"(lbase + slot * STB + lw * 4096) : "m0", "memory");
    };
    auto stage_w = [&](int kt, int slot) {
      const char* pw = reinterpret_cast<const char*>(W) + (size_t)(kt ^ krot) * 128 * ldw;
      asm volatile("s_mov_b32 m0, %7\n\ts_nop 0\n\tglobal_load_lds_dwordx4 %0, %6 sc1\n\tglobal_load_lds_dwordx4 %1, %6 offset:1024 sc1\n\t"
                   "global_load_lds_dwordx4 %2, %6 offset:2048 sc1\n\ts_addk_i32 m0, 0xc00\n\ts_nop 0\n\tglobal_load_lds_dwordx4 %3, %6 sc1\n\t"
                   "global_load_lds_dwordx4 %4, %6 offset:1024 sc1\n\tglobal_load_lds_dwordx4 %5, %6 offset:2048 sc1"
                   :: "v"(vw[0]), "v"(vw[1]), "v"(vw[2]), "v"(vw[3]), "v"(vw[4]), "v"(vw[5]), "s"(pw), "s"(lbase + slot * STB + ABYTES + lw * 6144) : "m0", "scc", "memory");
    };
    if (LAY2) {
      __builtin_amdgcn_s_setprio(0);
      __builtin_amdgcn_s_sleep(TPRE - 10);
      tile_at(3u);
      __builtin_amdgcn_s_setprio(LPRIO);
#pragma unroll
      for (int st = 0; st < NST; ++st) stage_w(st, st);
      PIN();
      __builtin_amdgcn_s_barrier();
      PIN();
#pragma unroll
      for (int st = 0; st < NST; ++st) stage_a(st, st);
      asm volatile("s_waitcnt vmcnt(8)" ::: "memory");
    } else {
      __builtin_amdgcn_s_setprio(LPRIO);
      stage_a(0, 0); stage_w(0, 0);
      if (PRO > 1) { stage_a(1, 1); stage_w(1, 1); }
      if (PRO > 2) { stage_a(2, 2); stage_w(2, 2); }
      asm volatile("s_waitcnt vmcnt(%0)" :: "n"(10 * PRO - 10) : "memory");
    }
    int slot = 0;
    __builtin_amdgcn_s_barrier();
    if (!LAY2) {
      if (PRO < 2) { stage_a(1, 1); stage_w(1, 1); }
      if (PRO < 3) { stage_a(2, 2); stage_w(2, 2); }
    }
    for (int kt = 0; kt + 1 < NKT; ++kt) {
      if (LAY2 && kt == 0) asm volatile("s_waitcnt vmcnt(4)" ::: "memory");
      else if (kt + 2 >= NKT) asm volatile("s_waitcnt vmcnt(0)" ::: "memory");
      else asm volatile("s_waitcnt vmcnt(10)" ::: "memory");
      __builtin_amdgcn_s_barrier();
      if (kt + NST < NKT) { stage_a(kt + NST, slot); stage_w(kt + NST, slot); }
      slot = slot == NST - 1 ? 0 : slot + 1;
    }
    __builtin_amdgcn_s_setprio(0);
    __builtin_amdgcn_s_barrier();
  } else {
  LAYER_HERE;
  const int dk = LAY2 ? 5 : 1;
  const int lane = fresh_lane(), wm = wid & 1, wn = wid >> 1;
  if (wid == 0) STAMP(dk, 0);
  f32x4 acc[4][6];
#pragma unroll
  for (int i = 0; i < 4; ++i)
#pragma unroll
    for (int j = 0; j < 6; ++j) acc[i][j] = f32x4{0.f, 0.f, 0.f, 0.f};
  const int q16 = lane & 15, g4 = lane >> 4;
  const int e0 = 16 * (q16 >> 3) + (q16 & 7);
  s16x8 af[2][4];
  unsigned aad[2], wad[2];
  {
    const int row = wm * 64 + e0, swz = ((row >> 1) & 3) | (((row >> 4) & 1) << 2);
#pragma unroll
    for (int h = 0; h < 2; ++h) aad[h] = lbase + row * 128 + (((4 * h + g4) ^ swz) << 4);
    const int tq = (lane & 15) >> 2, tp = lane & 3, k = 8 * g4 + tq, ch = wn * 4 + 2 * (tp >> 1);
    wad[0] = lbase + ABYTES + k * 384 + ((ch ^ (4 * ((k >> 1) & 1)) ^ (g4 & 1)) << 4) + 8 * (tp & 1);
    wad[1] = wad[0] ^ 16;
  }
  auto lda = [&](unsigned a, int i) { return *LDS_AT(const s16x8, a + (i >> 1) * 4096 + (i & 1) * 1024); };
  auto ldw_ = [&](int h, int j) {
    const unsigned w = wad[j & 1] + h * 12288 + (j >> 1) * 128;
    s16x4 lo = __builtin_amdgcn_ds_read_tr16_b64_v4i16(LDS_AT(s16x4, w));
    s16x4 h4 = __builtin_amdgcn_ds_read_tr16_b64_v4i16(LDS_AT(s16x4, w + 1536));
    return (s16x8)__builtin_shufflevector(lo, h4, 0, 1, 2, 3, 4, 5, 6, 7);
  };
  ARGS_HERE;
  unsigned lto = lbase + wid * 4096;
  char* kvb = reinterpret_cast<char*>(KV) + ((tni * 2 + wn) * 64 + tmi * 4 + wm * 2) * 2048 - lto;
  {
    const unsigned ato = lbase + ALPHA + wm * 256, dum = lbase + DUM + lane * 4;
    *LDS_AT(u32x4, lto + QOFF + lane * 16) = u32x4{lane >= 16 ? lbase + CRED : lbase + TABA + (wm * 64 + e0) * 16, lbase + TABW + (wn * 32 + e0) * 16,
                                                   ato + e0 * 4, lto + (lane >> 5) * 1024 + ((lane & 28) * 2 + (lane & 3)) * 16};
    *LDS_AT(u32x2, lto + QOFF + 2048 + lane * 16) = u32x2{aad[0], aad[1]};
    *LDS_AT(u32x4, lto + QOFF + 1024 + lane * 16) = u32x4{ato + g4 * 16 + (lane >> 5) * 32, lto + (lane >> 5) * 1024 + ((lane & 24) * 2 + (lane & 7)) * 16,
                                                          lane == 0 ? lbase + FLG + 32 : dum, lane == 0 ? lbase + FLG + 16 : dum};
  }
  asm volatile("" : "+s"(lto), "+s"(kvb) :: "memory");
#pragma unroll
  for (int i = 0; i < 4; ++i)
#pragma unroll
    for (int j = 0; j < 6; ++j) asm volatile("" : "+v"(acc[i][j]));
  asm volatile("" : "+v"(aad[0]), "+v"(aad[1]), "+v"(wad[0]), "+v"(wad[1]));
  if (LAY2) __builtin_amdgcn_s_barrier();
  __builtin_amdgcn_s_barrier();
  PIN();
  if (wid == 0) STAMP(dk, 1);
  s16x8 wq[4][2];
  wq[0][0] = ldw_(0, 0); af[0][0] = lda(aad[0], 0); wq[0][1] = ldw_(1, 0); af[1][0] = lda(aad[1], 0); wq[1][0] = ldw_(0, 1); wq[1][1] = ldw_(1, 1);
  af[0][1] = lda(aad[0], 1); af[1][1] = lda(aad[1], 1); af[0][2] = lda(aad[0], 2); af[1][2] = lda(aad[1], 2);
  PIN();
  s16x4 wlo;
  auto rw = [&](s16x8& d, int h, int j, int hi, int off = 0) __attribute__((always_inline)) {
    const s16x4 v = __builtin_amdgcn_ds_read_tr16_b64_v4i16(LDS_AT(s16x4, wad[j & 1] + h * 12288 + (j >> 1) * 128 + hi * 1536 + off));
    if (hi) d = (s16x8)__builtin_shufflevector(wlo, v, 0, 1, 2, 3, 4, 5, 6, 7);
    else wlo = v;
  };
  auto at = [&](s16x8& x, int r) __attribute__((always_inline)) {
    if (r == 0) asm volatile("" :: "{v[104:107]}"(x));
    if (r == 1) asm volatile("" :: "{v[108:111]}"(x));
    if (r == 2) asm volatile("" :: "{v[112:115]}"(x));
    if (r == 3) asm volatile("" :: "{v[116:119]}"(x));
    if (r == 4) asm volatile("" :: "{v[120:123]}"(x));
    if (r == 5) asm volatile("" :: "{v[124:127]}"(x));
    if (r == 6) asm volatile("" :: "{v[128:131]}"(x));
    if (r == 7) asm volatile("" :: "{v[132:135]}"(x));
    if (r == 8) asm volatile("" :: "{v[136:139]}"(x));
    if (r == 9) asm volatile("" :: "{v[140:143]}"(x));
    if (r == 10) asm volatile("" :: "{v[144:147]}"(x));
    if (r == 11) asm volatile("" :: "{v[148:151]}"(x));
    if (r == 12) asm volatile("" :: "{v[152:155]}"(x));
    if (r == 13) asm volatile("" :: "{v[156:159]}"(x));
    if (r == 14) asm volatile("" :: "{v[160:163]}"(x));
    if (r == 15) asm volatile("" :: "{v[164:167]}"(x));
  };
  auto mm = [&](int i, int j, int h, int sl, int hw_) __attribute__((always_inline)) {
    s16x8 &a = af[h][i], &w = wq[sl][hw_];
    at(a, h * 4 + i); at(w, 8 + sl * 2 + hw_);
    if (j >= 4) asm volatile("v_mfma_f32_16x16x32_bf16 %0, %1, %2, %0" : "+v"(acc[i][j]) : "v"(a), "v"(w));
    else asm volatile("v_mfma_f32_16x16x32_bf16 %0, %1, %2, %0" : "+v"(acc[i][j]) : "v"(w), "v"(a));
    PIN();
  };
  auto next = [&](int g, int J0, int s0, int s1, int off) __attribute__((always_inline)) {
    if (g == 2 || g == 3) rw(wq[s0][0], 0, J0, g & 1, off);
    if (g == 4 || g == 5) af[g & 1][0] = lda(aad[g & 1] + off, 0);
    if (g == 6 || g == 7) rw(wq[s0][1], 1, J0, g & 1, off);
    if (g >= 8 && g < 12) rw(wq[s1][(g - 8) >> 1], (g - 8) >> 1, J0 + 1, g & 1, off);
    if (g >= 12) af[g & 1][(g - 10) >> 1] = lda(aad[g & 1] + off, (g - 10) >> 1);
  };
  auto third = [&](int p, int J0, int delta) __attribute__((always_inline)) {
    auto mv = [&](unsigned& x) __attribute__((always_inline)) { asm volatile("v_add_u32 %0, %1, %0" : "+v"(x) : "s"(delta)); };
#pragma unroll
    for (int g = 0; g < 16; ++g) {
      const int h = g & 1, i = p == 1 ? (g >> 1) & 3 : g >> 2, t = p == 1 ? g >> 3 : (g >> 1) & 1, j = 2 * p + t, sl = p == 1 ? 2 - 2 * t : p + t;
      mm(i, j, h, sl, h);
      if (p == 0) {
        if (g < 2) af[g][3] = lda(aad[g], 3);
        else if (g < 6) rw(wq[2][(g - 2) >> 1], (g - 2) >> 1, 2, g & 1);
        else if (g < 10) rw(wq[3][(g - 6) >> 1], (g - 6) >> 1, 5, g & 1);
        else if (g < 12) mv(aad[g - 10]);
      }
      if (p == 1) {
        if (g < 4) rw(wq[0][g >> 1], g >> 1, 3, g & 1);
        if (g == 4) mv(wad[1]);
        if (g >= 8 && g < 12) rw(wq[2][(g - 8) >> 1], (g - 8) >> 1, 4, g & 1);
        if (g == 12) mv(wad[0]);
      }
      if (p == 2) {
        if (g == BARG) {
          asm volatile("" ::: "memory");
          PIN();
          __builtin_amdgcn_s_barrier();
          asm volatile("" ::: "memory");
        }
        next(g, J0, 0, 1, 0);
      }
      PIN();
      {
        if ((p == 2 && g == 15) || (p == 0 && g == 7)) __builtin_amdgcn_s_waitcnt(0xc67f);
        if (p == 0 && g == 2) __builtin_amdgcn_s_waitcnt(0xc57f);
        if ((p == 0 && g == 14) || (p == 1 && (g & 7) == 7)) __builtin_amdgcn_s_waitcnt(0xc07f);
        PIN();
      }
    }
  };
  third(0, 0, STB); third(1, 0, STB);
  {
    static_assert((NKT - NST - 1) % NST == 0, "");
#pragma unroll 1
    for (int kt = 0; kt < NKT - NST - 1; kt += NST) {
#pragma unroll
      for (int u = 0; u < NST; ++u) {
        const int delta = u == 1 ? -(NST - 1) * STB : STB;
        third(2, 0, 0); third(0, 0, delta); third(1, 0, delta);
      }
    }
  }
  static_assert(NKT % NST == 1, "slots of the last stages");
#define TT(i)
  auto bar = [&]() { PIN(); __builtin_amdgcn_s_barrier(); asm volatile("" ::: "memory"); PIN(); };
  TT(0);
  third(2, 2, 0);
  const __amdgpu_buffer_rsrc_t rsk = __builtin_amdgcn_make_buffer_rsrc(kvb, 0, 0x7fffffff, 0x00020000), rsv = __builtin_amdgcn_make_buffer_rsrc(kvb + VOFF, 0, 0x7fffffff, 0x00020000);
  unsigned pk;
  u32x2 t0, t1, t2, t3;
  auto park = [&](int i) { return *LDS_AT(const volatile u32x2, pk + (i >> 1) * 1024 + (i & 1) * 8); };
  float al[4];
  f32x4 av[4];
  u32x4 kvp[12];
  auto cacc = [&](int p, int x, int t) -> f32x4& { return p < 2 ? acc[x][2 * p + t] : acc[(x >> 1) * 2 + t][4 + (x & 1)]; };
  auto signal = [&](unsigned v) { __hip_atomic_fetch_add(LDS_AT(unsigned, t3[0]), v, __ATOMIC_RELAXED, __HIP_MEMORY_SCOPE_WORKGROUP); PIN(); };
  auto outop = [&](int p, int k) {
    if (k >= 0 && k < 16) {
      const int x = k >> 2, t = (k >> 1) & 1;
      f32x4& c = cacc(p, x, t);
#pragma unroll
      for (int o = 2 * (k & 1); o < 2 * (k & 1) + 2; ++o) {
        float r;
        asm volatile("v_mul_f32 %0, %1, %2" : "=v"(r) : "v"(c[o]), "v"(p < 2 ? al[x] : av[(x >> 1) * 2 + t][o]));
        c[o] = r;
      }
      PIN();
    }
    if (k >= 1 && k <= 16) {
      const int x = (k - 1) >> 2, t = ((k - 1) >> 1) & 1, o = (k - 1) & 1;
      kvp[p * 4 + x][2 * t + o] = pack2(cacc(p, x, t)[2 * o], cacc(p, x, t)[2 * o + 1]);
      PIN();
      if (t && o) {
        if (p) __builtin_amdgcn_raw_buffer_store_b128(kvp[p * 4 + x], p == 1 ? rsk : rsv, (p == 1 ? t1[1] : t2[1]) + (x >> 1) * 2048 + (x & 1) * (p == 1 ? 64 : 128), 0, 16);
        else *LDS_AT(u32x4, t2[1] + (x >> 1) * 2048 + (x & 1) * 128) = kvp[x];
        PIN();
      }
    }
  };
  static_for<0, 168>([&](auto nc) __attribute__((always_inline)) {
    constexpr int n = decltype(nc)::value, sec = n < 80 ? n >> 4 : (n < 104 ? 5 : 6 + ((n - 104) >> 4)), g = n < 104 ? n - 16 * sec : (n - 104) & 15;
    auto taba = [&](int i) { af[0][i] = *LDS_AT(const s16x8, t0[0] + (i >> 1) * 512 + (i & 1) * 128); };
    auto tabw = [&](int j) { wq[(2 + (j >> 1)) & 3][j & 1] = *LDS_AT(const s16x8, t0[1] + ((j >> 1) * 64 + (j & 1) * 8) * 16); };
    auto rwn = [&](int e, int sl, int j, int off) { if (e >= 0 && e < 4) rw(wq[sl][e >> 1], e >> 1, j, e & 1, off); };
    if (g == 0) TT(1 + sec);
    if (sec == 5) {
      const int i = (g / 12) * 2 + (g & 1), j = (g % 12) >> 1;
      mm(i, j, 0, (2 + (j >> 1)) & 3, j & 1);
    } else {
      constexpr int part[10] = {1, 2, 1, 2, 0, 0, 1, 2, 0, 0}, s0[10] = {0, 2, 0, 2, 0, 0, 1, 3, 1, 3};
      const int h = g & 1, i = sec == 7 ? (g >> 3) * 2 + ((g >> 1) & 1) : g >> 2, t = sec == 7 ? (g >> 2) & 1 : (g >> 1) & 1;
      mm(i, 2 * part[sec] + t, h, (s0[sec] + t) & 3, h);
    }
    if (sec == 0 || sec == 2) {
      if (g < 2) af[g][3] = lda(aad[g] + sec / 2 * STB, 3);
      rwn(g - 2, 2, 4, sec / 2 * STB); rwn(g - 6, 3, 5, sec / 2 * STB);
    }
    if (sec == 1) {
      if (g == 1) bar();
      next(g, 2, 0, 1, STB);
    }
    if ((sec == 3 && g == 12) || (sec == 6 && g == 13)) { pk = lto + QOFF + fresh_lane() * 16; asm volatile("" : "+v"(pk)); }
    if (sec == 3) {
      rwn(g, 0, 0, STB); rwn(g - 4, 1, 1, STB);
      if (g >= 8 && g < 12) asm volatile("v_subrev_u32 %0, %1, %0" : "+v"(g < 10 ? aad[g & 1] : wad[g & 1]) : "s"(STB));
      if (g == 13) t1 = park(1);
      if (g == 14) t0 = park(0);
    }
    if (sec == 4) {
      if (g == B15) bar();
      if (g == 4) taba(0);
      if (g == 5 || g == 6) tabw(g - 5);
      if (g == 7) taba(1);
      if (g == 8 || g == 9) tabw(g - 6);
      if (g == 10 || g == 11) af[1][g - 10] = lda(aad[1], g - 10);
      if (g == 12) taba(2);
      if (g == 15) taba(3);
    }
    if (sec == 5) {
      if (g < 2) tabw(4 + g);
      rwn(g - 2, 1, 2, 0);
      if (g == 12 || g == 13) af[0][g - 12] = lda(aad[0], g - 12);
      if (g == 22) { al[0] = *LDS_AT(const float, t1[0]); al[1] = *LDS_AT(const float, t1[0] + 32); }
      rwn(g - 16, 2, 3, 0);
      if (g == 20 || g == 21) af[1][g - 18] = lda(aad[1], g - 18);
    }
    if (sec == 6) {
      if (g < 2) af[0][2 + g] = lda(aad[0], 2 + g);
      rwn(g - 2, 3, 4, 0);
      if (g == 9) { al[2] = *LDS_AT(const float, t1[0] + 128); al[3] = *LDS_AT(const float, t1[0] + 160); }
      if (g == 14) t2 = park(2);
      if ((g & 3) >= 2) rwn((g - 10) / 4 * 2 + (g & 1) - (g < 10) * 8, 0, 5, 0);
    }
    if (sec == 7) {
      if (g == 2 || g == 3 || g == 10 || g == 11) av[(g >> 3) * 2 + (g & 1)] = *LDS_AT(const f32x4, t2[0] + ((g >> 3) * 32 + (g & 1) * 8) * 4);
      rwn(g < 2 ? g : (g < 4 ? -1 : g - 2), 1, 0, 0); rwn(g - 6, 2, 1, 0);
      if (g == 12) t3 = park(3);
      if (g == 13) { const u32x2 a = park(4); aad[0] = a[0]; aad[1] = a[1]; }
      if (g == 15) {
        asm volatile("" ::: "memory");
        __builtin_amdgcn_s_waitcnt(0xc07f);
        bar();
      }
    }
    if (sec == 8) next(g, 0, 3, 0, STB);
    if (sec == 9) {
      if (g < 2) af[g][3] = lda(aad[g] + STB, 3);
      if (g == 2) signal(0x10000u);
    }
    PIN();
    outop(1, n - 104 - KO);
    outop(2, n - 120 - KO);
    {
      constexpr int wg[8] = WGAPS;
#pragma unroll
      for (int e = 0; e < 8; ++e)
        if (n - 136 == wg[e]) {
          *LDS_AT(u32x4, (e < 4 ? t1[1] + (e & 1) * 64 : t2[1] + 2048 + (e & 1) * 128) + ((e >> 1) & 1) * ASB) = kvp[4 + e];
          PIN();
        }
    }
    if (n == 152 + 3 || n == 152 + 4) {
      al[2 * (n & 1)] *= QSCALE; al[2 * (n & 1) + 1] *= QSCALE;
      asm volatile("" : "+v"(al[2 * (n & 1)]), "+v"(al[2 * (n & 1) + 1]));
      if (n & 1) asm volatile("v_add_u32 %0, %1, %0" : "+v"(t2[1]) : "s"(QOFF));
      PIN();
    }
    outop(0, n - 152 - KO);
  });
  TT(13);
  if (wid == 0) STAMP(dk, 18);
#pragma unroll
  for (int k = 16 - KO; k <= 16; ++k) outop(0, k);
  __hip_atomic_fetch_add(LDS_AT(unsigned, t3[1]), 1u, __ATOMIC_RELAXED, __HIP_MEMORY_SCOPE_WORKGROUP);
  TT(14);
  asm volatile("s_waitcnt vmcnt(0)" ::: "memory");
  signal(1u);
  TT(15);
  if (wid == 0) STAMP(dk, 19);
  }
  PIN();
  {
  const int lane = fresh_lane(), tid = wid * 64 + lane;
  LAYER_HERE;
  const int dk = LAY2 ? 6 : 2;
  const int g = wid >> 2, hh = (wid >> 1) & 1, qg = wid & 1;
  const int head = tni * 2 + hh, qt = tmi * 4 + qg * 2;
  unsigned qaa = lbase + QOFF + (hh * 2 + qg) * 4096 + lane * 16 + (g ? 0 : 2048), qab = qaa ^ 2048;
  const unsigned wbase = lbase + (hh * 2 + g) * 4096 + lane * 16;
  auto sptr = [&](int sl) { unsigned a = wbase + sl * ASB; asm volatile("" : "+v"(a)); return LDS_AT(const s16x8, a); };
  auto rd = [&](__attribute__((address_space(3))) const s16x8* sp, int off) { return sp[off >> 4]; };
  auto sl0 = sptr(0);
  const unsigned arr_a = lane == 0 ? lbase + FLG + 16 : lbase + DUM + lane * 4;
  auto arrive = [&]() { __hip_atomic_fetch_add(LDS_AT(unsigned, arr_a), 1u, __ATOMIC_RELAXED, __HIP_MEMORY_SCOPE_WORKGROUP); };
  unsigned arr_w = lane == 0 ? lbase + FLG + 8 + g * 4 : lbase + DUM + lane * 4;
  unsigned arr_i = 1u << (8 * (wid & 3));
  auto arrive_w = [&]() { __hip_atomic_fetch_add(LDS_AT(unsigned, arr_w), arr_i, __ATOMIC_RELAXED, __HIP_MEMORY_SCOPE_WORKGROUP); };
  s16x8 sel;
#pragma unroll
  for (int e = 0; e < 8; ++e) sel[e] = ((lane & 7) == (e & 4)) ? (short)0x3F80 : (short)0;
  int sidx = 0xDDDDDDDD;
  f32x16 oa, ob, nn, sa, sb;
  f32x4 la = {0.f, 0.f, 0.f, 0.f}, lb = {0.f, 0.f, 0.f, 0.f};
#pragma unroll
  for (int e = 0; e < 16; ++e) { oa[e] = 0.f; ob[e] = 0.f; sa[e] = 0.f; sb[e] = 0.f; }
  asm volatile("" : "+v"(sidx), "+v"(sel), "+v"(oa), "+v"(ob), "+v"(la), "+v"(lb), "+v"(qaa), "+v"(qab), "+v"(arr_w), "+v"(arr_i));
  PIN();
  s16x8 qa0, qa1, qb0, qb1, k00, k01, v00, v01, v10, v11, k10, k11;
  u32x4 fw;
  auto first = [&]() {
    fw = lds_rd4(lbase + FLG + 16);
    asm volatile("" ::: "memory");
    k00 = rd(sl0, 0); qa0 = *LDS_AT(const s16x8, qaa); qb0 = *LDS_AT(const s16x8, qab);
    k01 = rd(sl0, 1024); qa1 = *LDS_AT(const s16x8, qaa + 1024); qb1 = *LDS_AT(const s16x8, qab + 1024);
    v00 = rd(sl0, 2048); v01 = rd(sl0, 3072);
    PIN();
  };
  first();
  if (__builtin_amdgcn_readfirstlane(fw[0]) < 4u) {
#pragma unroll 1
    for (int i = 0; i < SPIN_MAX; ++i) {
      if (__builtin_amdgcn_readfirstlane(*LDS_AT(const volatile unsigned, lbase + FLG + 16)) >= 4u) break;
      __builtin_amdgcn_s_sleep(ASLP);
    }
    asm volatile("" ::: "memory");
    first();
  }
  const unsigned tag = __builtin_amdgcn_readfirstlane(fw[1]);
  const int idx = LAY2 ? (2047 - (int)(__builtin_amdgcn_readfirstlane(fw[3]) & 0xffffu)) & 2047 : -64;
  float ma;
  const float zoff = __uint_as_float(__builtin_amdgcn_readfirstlane(LAY2 && g == ((idx >> 6) & 1) ? 0u : 0xff800000u));
  {
    sa = MFMA32(k00, qa0, sa); sb = MFMA32(k00, qb0, sb);
    sa = MFMA32(k01, qa1, sa); sb = MFMA32(k01, qb1, sb);
    float mb = sb[0];
    ma = sa[0];
#pragma unroll
    for (int e = 1; e < 16; ++e) { ma = fmaxf(ma, sa[e]); mb = fmaxf(mb, sb[e]); }
    ma = fmaxf(ma, mb);
    const auto sw = __builtin_amdgcn_permlane32_swap(__float_as_uint(ma), __float_as_uint(ma), false, false);
    ma = fmaxf(__uint_as_float(sw[0]), __uint_as_float(sw[1]));
    f32x2 n2 = {-ma, -ma};
    asm volatile("" : "+v"(n2));
#pragma unroll
    for (int e = 0; e < 16; ++e) nn[e] = n2[e & 1];
    sa = MFMA32(k00, qa0, nn); sa = MFMA32(k01, qa1, sa);
    la[0] = lb[0] = -__uint_as_float(__float_as_uint(__builtin_amdgcn_exp2f(zoff - ma)) & 0xffff0000u);
  }
#define SB() __builtin_amdgcn_sched_barrier(0)
#define EX(i) sc[i] = __builtin_amdgcn_exp2f(sc[i])
  auto unit = [&](f32x16& sc, f32x16& sn, const s16x8& kn0, const s16x8& kn1, const s16x8& qn0, const s16x8& qn1,
                  f32x16& op, f32x4& lp, const s16x8& vp,
                  f32x16& oc, f32x4& lc, const s16x8& vc, auto&& mid) {
    lp = SMF16(sel, PHI(sn), lp); SB();
    op = SMF32(vp, PHI(sn), op); SB();
    EX(0); EX(1); EX(2); EX(3); SB();
    sn = MFMA32(kn0, qn0, nn); SB();
    EX(4); EX(5); EX(6); EX(7); SB();
    sn = MFMA32(kn1, qn1, sn); SB();
    mid();
    EX(8); EX(9); EX(10); EX(11); SB();
    lc = SMF16(sel, PLO(sc), lc); SB();
    oc = SMF32(vc, PLO(sc), oc); SB();
    EX(12); EX(13); EX(14); EX(15); SB();
    asm volatile("" : "+v"(sc));
  };
  sl0 = sptr(1);
  unsigned f0 = 0u;
  auto check = [&](unsigned need) {
    if (__builtin_expect(__builtin_amdgcn_readfirstlane(f0) < need, 0)) {
#pragma unroll 1
      for (int i = 0; i < SPIN_MAX; ++i) {
        if (__builtin_amdgcn_readfirstlane(*LDS_AT(const volatile unsigned, lbase + FLG)) >= need) break;
        __builtin_amdgcn_s_sleep(1);
      }
    }
    asm volatile("" ::: "memory");
  };
#define PAT 0x22221110
#define SAW8(i) do { if (((PAT >> (28 - 4 * (i))) & 3) != ((PAT >> ((32 - 4 * (i)) & 31)) & 3)) { __builtin_amdgcn_s_setprio((PAT >> (28 - 4 * (i))) & 3); SB(); } } while (0)
#define SAW(i) do { if (odd) SAW8(4 + (i)); else SAW8(i); } while (0)
  auto three = [&](int st, bool odd) {
    SAW(0);
    unit(sa, sb, k00, k01, qb0, qb1, ob, lb, v11, oa, la, v00, [&]() { k10 = rd(sl0, 0); k11 = rd(sl0, 1024); SB(); });
    SAW(1);
    unit(sb, sa, k10, k11, qa0, qa1, oa, la, v01, ob, lb, v00, [&]() { v10 = rd(sl0, 2048); v11 = rd(sl0, 3072); SB(); });
    sl0 = sptr((2 * st + 2) & 7);
    SB();
    SAW(2);
    unit(sa, sb, k10, k11, qb0, qb1, ob, lb, v01, oa, la, v10, [&]() { k00 = rd(sl0, 0); k01 = rd(sl0, 1024); SB(); });
  };
  auto fourth = [&](int st, bool odd) {
    SAW(3);
    unit(sb, sa, k00, k01, qa0, qa1, oa, la, v11, ob, lb, v10, [&]() {
      v00 = rd(sl0, 2048); v01 = rd(sl0, 3072);
      f0 = *LDS_AT(const volatile unsigned, lbase + FLG);
      SB();
      arrive_w();
      SB(); });
    check(st >= 14 ? 32u : 2u * st + 5u);
    sl0 = sptr((2 * st + 3) & 7);
    SB();
  };
  SB();
  SAW8(0);
  {
    f32x16& sc = sa;
    sb = MFMA32(k00, qb0, nn); SB();
    k10 = rd(sl0, 0); k11 = rd(sl0, 1024); SB();
    sb = MFMA32(k01, qb1, sb); SB();
    EX(0); EX(1); EX(2); EX(3); EX(4); EX(5); EX(6); EX(7); EX(8); SB();
    oa = SMF32(v00, PLO(sa), oa); SB();
    EX(9); EX(10); EX(11); SB();
    la = SMF16(sel, PLO(sa), la); SB();
    EX(12); EX(13); EX(14); EX(15); SB();
    asm volatile("" : "+v"(sa));
  }
  SAW8(1);
  unit(sb, sa, k10, k11, qa0, qa1, oa, la, v01, ob, lb, v00, [&]() { v10 = rd(sl0, 2048); v11 = rd(sl0, 3072); SB(); });
  sl0 = sptr(2);
  SB();
  SAW8(2);
  unit(sa, sb, k10, k11, qb0, qb1, ob, lb, v01, oa, la, v10, [&]() {
    f0 = *LDS_AT(const volatile unsigned, lbase + FLG);
    asm volatile("" ::: "memory");
    k00 = rd(sl0, 0); k01 = rd(sl0, 1024); SB(); });
  if (__builtin_amdgcn_readfirstlane(f0) < 3u) { check(3u); k00 = rd(sl0, 0); k01 = rd(sl0, 1024); }
  SB();
  SAW8(3);
  unit(sb, sa, k00, k01, qa0, qa1, oa, la, v11, ob, lb, v10, [&]() {
    v00 = rd(sl0, 2048); v01 = rd(sl0, 3072); f0 = *LDS_AT(const volatile unsigned, lbase + FLG); SB(); arrive_w(); SB(); });
  check(5u);
  sl0 = sptr(3);
  SB();
  three(1, true);
#pragma unroll 1
  for (int st = 2; st < 16; st += 2) { fourth(st - 1, true); three(st, false); fourth(st, false); three(st + 1, true); }
  SAW8(7);
  unsigned vr, tk, tv;
  u32x4 f4;
  auto table = [&]() {
    f4 = lds_rd4(lbase + FLG);
    k00 = *LDS_AT(const volatile s16x8, tk); k01 = *LDS_AT(const volatile s16x8, tk + 32);
    vr = *LDS_AT(const volatile u16, tv);
  };
  unsigned xw, xm;
  u32x2 ml;
  u32x4 o2[4];
  auto take = [&]() {
    ml = *LDS_AT(const volatile u32x2, xm);
#pragma unroll
    for (int e = 0; e < 4; ++e) o2[e] = lds_rd4(xw + e * 1024);
  };
  float pzt;
  int dso;
  f32x16& sc = sb;
#define EXP() do { asm volatile("" : "+v"(sc)); SB(); } while (0)
  oa = SMF32(v11, PHI(sa), oa); SB();
  tk = lbase + TAB + 128 + hh * 64 + (lane >> 5) * 16; tv = lbase + TAB + 256 + hh * 64 + (lane & 31) * 2;
  table();
  SB();
  EX(0); EX(1); EXP();
  la = SMF16(sel, PHI(sa), la); SB();
  EX(2); EX(3); EX(4); EXP();
  xw = wbase + (qg ? QOFF : 6 * ASB); xm = lbase + XML + wid * 512 + lane * 8;
  pzt = __builtin_amdgcn_exp2f(nn[0] + (zoff - 8.f));
  asm volatile("" : "+v"(xw), "+v"(xm), "+v"(pzt), "+v"(f4));
  SB();
  {
    const unsigned ph = LAY2 ? 0x79797979u : 0x80808080u, pg = qg ? 0u : 0x80u << (16 * hh + 8);
    auto ready = [&]() {
      return (((__builtin_amdgcn_readfirstlane(f4[1]) + ph) & 0x80808080u) == 0x80808080u) & (((__builtin_amdgcn_readfirstlane(g ? f4[3] : f4[2]) + 0x71717171u) & pg) == pg);
    };
    if (__builtin_expect(!ready(), 0)) {
#pragma unroll 1
      for (int i = 0; i < SPIN_MAX; ++i) {
        __builtin_amdgcn_s_sleep(1);
        table();
        if (ready()) break;
      }
    }
    asm volatile("" ::: "memory");
  }
  SB();
  sa = MFMA32(k00, qb0, nn); asm volatile("" : "+v"(sa)); SB();
#pragma unroll
  for (int e = 0; e < 4; ++e) *LDS_AT(volatile f32x4, xw + e * 1024) = f32x4{oa[4 * e], oa[4 * e + 1], oa[4 * e + 2], oa[4 * e + 3]};
  *LDS_AT(volatile u32x2, xm) = u32x2{__float_as_uint(ma), f2word(pzt <= la[0] ? la[0] : 0.f)};
  SB();
  EX(5); EX(6); EX(7); EXP();
  sa = MFMA32(k01, qb1, sa); asm volatile("" : "+v"(sa), "+v"(vr)); SB();
  ARGS_HERE;
  EX(8); EX(9); EX(10); EXP();
  ob = SMF32(v10, PLO(sb), ob); SB();
  EX(11); EX(12); EX(13); EXP();
  lb = SMF16(sel, PLO(sb), lb); SB();
  const bool used = !LAY2 || (qt + g) * 32 + (lane & 31) != idx;
  dso = used ? (((qt + g) * 32 + head) * 1024 + lane * 8) * 2 : S_ * D_ * 2;
  xw += g ? -4096 : 4096; xm += g ? -2048 : 2048;
  asm volatile("" : "+v"(dso), "+v"(xw), "+v"(xm));
  SB();
  EX(14); EX(15); EXP();
  take();
  SB();
  lb = SMF16(sel, PHI(sb), lb); SB();
  ob = SMF32(v11, PHI(sb), ob); SB();
  __builtin_amdgcn_s_setprio(EPRIO);
  TE(0, 0); TE(1, 0); TE(2, 0); TE(3, 0); TE(7, 0);
  float lsb = lb[0];
  if (LAY2) {
    sa[0] = __builtin_amdgcn_exp2f(sa[0]);
    ob = SMF32((u32x4{lane < 32 ? vr : 0u, 0u, 0u, 0u}), PLO(sa), ob);
    lsb += __uint_as_float(__float_as_uint(sa[0]) & 0xffff0000u);
    lsb = pzt <= lsb ? lsb : 0.f;
  }
  const __amdgpu_buffer_rsrc_t rso = __builtin_amdgcn_make_buffer_rsrc(Of, 0, S_ * D_ * 2, 0x00020000);
  TE(0, 1);
  if (__builtin_amdgcn_ballot_w64(ml[1] == SENT) != 0ull) {
#pragma unroll 1
    for (int i = 0; i < SPIN_MAX; ++i) {
      __builtin_amdgcn_s_sleep(XSLP);
      if (!__builtin_amdgcn_ballot_w64(*LDS_AT(const volatile unsigned, xm + 4) == SENT)) break;
    }
    asm volatile("s_waitcnt lgkmcnt(0)" ::: "memory");
    take();
  }
  arrive();
  TE(0, 2);
  TE(0, 3); TE(1, 1);
  {
    const float m2 = __uint_as_float(ml[0]), l2 = __uint_as_float(ml[1]);
    const float mx = fmaxf(ma, m2);
    const float f1 = __builtin_amdgcn_exp2f(ma - mx), f2 = __builtin_amdgcn_exp2f(m2 - mx);
    const float inv = frcp(lsb * f1 + l2 * f2);
    const float c1 = f1 * inv, c2 = f2 * inv;
    f32x2 r[8];
#pragma unroll
    for (int e = 0; e < 8; ++e) {
      const unsigned x0 = o2[e >> 1][(e & 1) * 2], x1 = o2[e >> 1][(e & 1) * 2 + 1];
      r[e] = f32x2{__uint_as_float(x0) * c2, __uint_as_float(x1) * c2};
    }
#pragma unroll
    for (int e = 0; e < 8; ++e) r[e] = f32x2{ob[2 * e], ob[2 * e + 1]} * f32x2{c1, c1} + r[e];
    auto put = [&]() {
#pragma unroll
      for (int sx = 0; sx < 2; ++sx)
        __builtin_amdgcn_raw_buffer_store_b128(u32x4{pack2(r[4 * sx][0], r[4 * sx][1]), pack2(r[4 * sx + 1][0], r[4 * sx + 1][1]),
            pack2(r[4 * sx + 2][0], r[4 * sx + 2][1]), pack2(r[4 * sx + 3][0], r[4 * sx + 3][1])}, rso, dso + sx * 1024, 0, 16);
    };
    put();
    PIN();
    __builtin_amdgcn_s_setprio(0);
    PIN();
    f32x2 chk = {lsb - __builtin_amdgcn_fmed3f(lsb, 8.6736174e-19f, 1.2676506e30f), l2 - __builtin_amdgcn_fmed3f(l2, 8.6736174e-19f, 1.2676506e30f)};
#pragma unroll
    for (int e = 0; e < 8; ++e) chk = r[e] * f32x2{0.f, 0.f} + chk;
    if (__builtin_expect(__builtin_amdgcn_ballot_w64(used && !(chk[0] + chk[1] == 0.f)) != 0ull, 0)) {
      float l;
      attn_safe(KV, head * 64, qb0, qb1, ob, l, LAY2 ? head * 64 + (idx >> 5) : -1, idx & 31, lbase + TAB + 128 + hh * 64);
      const float inv2 = 1.0f / l;
#pragma unroll
      for (int e = 0; e < 8; ++e) r[e] = f32x2{ob[2 * e] * inv2, ob[2 * e + 1] * inv2};
      asm volatile("s_waitcnt vmcnt(0)" ::: "memory");
      put();
    }
    unsigned pp = 0u;
    if (LAY2) {
      const unsigned ua = lbase + UGL + hh * 128 + (lane >> 5) * 16;
      f32x2 du = {0.f, 0.f}, dn = {0.f, 0.f};
#pragma unroll
      for (int q = 0; q < 4; ++q) {
        const f32x4 uu = *LDS_AT(const f32x4, ua + q * 32), gg = *LDS_AT(const f32x4, ua + 256 + q * 32);
        du = r[2 * q] * f32x2{uu[0], uu[1]} + du; du = r[2 * q + 1] * f32x2{uu[2], uu[3]} + du;
        const f32x2 t0 = r[2 * q] * f32x2{gg[0], gg[1]}, t1 = r[2 * q + 1] * f32x2{gg[2], gg[3]};
        dn = t0 * t0 + dn; dn = t1 * t1 + dn;
      }
      const auto a = __builtin_amdgcn_permlane32_swap(__float_as_uint(du[0] + du[1]), __float_as_uint(du[0] + du[1]), false, false);
      const auto c = __builtin_amdgcn_permlane32_swap(__float_as_uint(dn[0] + dn[1]), __float_as_uint(dn[0] + dn[1]), false, false);
      pp = f2word(__uint_as_float(pack2(__uint_as_float(a[0]) + __uint_as_float(a[1]), __uint_as_float(c[0]) + __uint_as_float(c[1]))));
    }
    __builtin_amdgcn_raw_buffer_store_b32(pp, __builtin_amdgcn_make_buffer_rsrc(hand + 2 * HAND_N + PP_O, 0, LAY2 ? H_ * S_ * 4 : 0, 0x00020000),
                                          lane < 32 ? (head * S_ + (qt + g) * 32 + lane) * 4 : H_ * S_ * 4, 0, 16);
    PIN();
  }
  TE(1, 2);
  u32x4 xres[2];
  oproj_tail(LAY2, tid, lbase, lbase + FLG, omi, oni, idx, gran + GR_O + ((LAY2 ? 16 : 0) + tmi) * (64 * RO) + tni,
             gran + GR_X + omi * (128 * RX) + oni * 4, tag, Of, xout, A, w_r, stats_out, stats, hand, xres TEPASS);
  if (LAY2) {
    const int row = omi * 128 + (wid & 3) * 32 + (wid >> 2) * 16 + (lane >> 3), col = oni * 64 + (lane & 7) * 8;
    unsigned md[2], best = 0u;
    const __amdgpu_buffer_rsrc_t rsk = __builtin_amdgcn_make_buffer_rsrc(hand + HAND_N, 0, (HAND_N + PS_N) * 4, 0x00020000);
    f32x2 xf[2][4];
#pragma unroll
    for (int p = 0; p < 2; ++p)
#pragma unroll
      for (int e = 0; e < 4; ++e) {
        xf[p][e] = f32x2{__uint_as_float(xres[p][e] << 16), __uint_as_float(xres[p][e] & 0xffff0000u)};
        asm volatile("" : "+v"(xf[p][e]));
      }
    LOGDEF;
    LOG();
    const bool rtr = (oni & 7) == 7;
    {
      const int rl = (wid & 3) * 32 + (wid >> 2) * 16 + (lane & 15), rr = omi * 128 + rl;
      int src = (HAND_N + ((omi * 16 + (lane >> 4) * 4) * 128 + rl) * 4) * 4;
      int ko = rtr && (wid >> 2) == (oni >> 3) && lane < NREP ? lane * 1024 + (rr >> 4) * 8 : (HAND_N + PS_N) * 4;
      int bp = (lane >> 3) * 4;
      const float wsum = *LDS_AT(const volatile float, lbase + SPM + 132);
      asm volatile("" : "+v"(src), "+v"(ko), "+v"(bp));
      u32x4 v[4];
      __builtin_amdgcn_s_sleep(MPRE);
#pragma unroll 1
      for (int spin = 0; spin < SPIN_MAX; ++spin) {
        LOG();
#pragma unroll
        for (int i = 0; i < 4; ++i) v[i] = __builtin_amdgcn_raw_buffer_load_b128(rsk, src + (i & 1) * 2048, (i >> 1) * 4096, 16);
        unsigned mx = 0u;
#pragma unroll
        for (int i = 0; i < 4; ++i) mx = umax(umax(umax(mx, v[i][0]), v[i][1]), v[i][2]);
        if (none(mx == SENT)) break;
        __builtin_amdgcn_s_sleep(MSLP);
        asm volatile("" ::: "memory");
      }
      LOGAT(12);
      if (rtr) __builtin_amdgcn_s_setprio(3); else __builtin_amdgcn_s_setprio(LNPRIO);
      double sm[3];
#pragma unroll
      for (int c = 0; c < 3; ++c) {
        const double a = ((double)__uint_as_float(v[0][c]) + (double)__uint_as_float(v[1][c])) + ((double)__uint_as_float(v[2][c]) + (double)__uint_as_float(v[3][c]));
        u32x2 q = __builtin_bit_cast(u32x2, a);
        const auto lo = __builtin_amdgcn_permlane32_swap(q[0], q[0], false, false), hi = __builtin_amdgcn_permlane32_swap(q[1], q[1], false, false);
        q = __builtin_bit_cast(u32x2, __builtin_bit_cast(double, u32x2{lo[0], hi[0]}) + __builtin_bit_cast(double, u32x2{lo[1], hi[1]}));
        const auto l2 = __builtin_amdgcn_permlane16_swap(q[0], q[0], false, false), h2 = __builtin_amdgcn_permlane16_swap(q[1], q[1], false, false);
        sm[c] = __builtin_bit_cast(double, u32x2{l2[0], h2[0]}) + __builtin_bit_cast(double, u32x2{l2[1], h2[1]});
      }
      float mean, var, mb, den;
      ln_from_sums(sm[0], sm[1], mean, var, mb, den);
      const unsigned mdr = f2word(__uint_as_float((__float_as_uint(mb) >> 16) | (__float_as_uint(den) & 0xffff0000u)));
      md[0] = __builtin_amdgcn_ds_bpermute(bp, mdr); md[1] = __builtin_amdgcn_ds_bpermute(bp + 32, mdr);
      if (rtr) {
        const float r = (float)fma(-(double)mb, (double)wsum, sm[2]) * frcp(den);
        const unsigned rb = pack2(r, r);
        unsigned rkey = (rb ^ ((unsigned)((int)rb >> 31) | 0x80000000u)) & 0xffff0000u;
        if (r != r) rkey = 0u;
        const unsigned top = wave_umax(rkey | (unsigned)(2047 - rr));
        const unsigned mdt = __builtin_amdgcn_readlane(mdr, (2047 - (int)(top & 0xffffu)) & 15);
        __builtin_amdgcn_raw_buffer_store_b64(u32x2{top, mdt}, rsk, ko, 0, 16);
        PIN();
        __builtin_amdgcn_s_setprio(LNPRIO);
        __hip_atomic_fetch_add(LDS_AT(unsigned, lane == 0 ? lbase + SPM + 128 : lbase + DUM + lane * 4), 1u, __ATOMIC_RELAXED, __HIP_MEMORY_SCOPE_WORKGROUP);
        if (wid == 2 || wid == 5) STAMP(8, 6);
      }
      LOGAT(13);
    }
    auto keys = [&](const u32x4& v) __attribute__((always_inline)) {
      if (none(umax(umax(v[0], v[1]), umax(v[2], v[3])) == SENT)) {
        best = wave_umax(umax(v[0], v[2])) | 0x8000u;
        *LDS_AT(volatile u32x2, lbase + FLG + 40) = u32x2{best, (unsigned)__builtin_amdgcn_readlane(v[0] > v[2] ? v[1] : v[3], ((2047 - (int)(best & 0x7ffu)) >> 5) & 63)};
        STAMP(8, 11);
      }
    };
    auto ln_rows = [&]() __attribute__((always_inline)) {
#pragma unroll
      for (int p = 0; p < 2; ++p) {
        const float mb_ = __uint_as_float(md[p] << 16), rin = frcp(__uint_as_float(md[p] & 0xffff0000u));
#pragma unroll
        for (int e = 0; e < 4; ++e) {
          const f32x2 h = (xf[p][e] - f32x2{mb_, mb_}) * f32x2{rin, rin} + xf[p][e];
          xres[p][e] = pack2(h[0], h[1]);
        }
      }
      asm volatile("" : "+v"(xres[0]), "+v"(xres[1]));
    };
    if (wid == 0) STAMP(8, 12);
    ln_rows();
    LOGAT(14);
    const __amdgpu_buffer_rsrc_t rsu = __builtin_amdgcn_make_buffer_rsrc(out, 0, S_ * D_ * 2, 0x00020000);
    unsigned fr1 = SENT;
#pragma unroll 1
    for (int i = 0; i < 4 * SPIN_MAX && fr1 == SENT; ++i) fr1 = __builtin_amdgcn_readfirstlane(*LDS_AT(const volatile unsigned, lbase + SPM + 144));
    fr1 &= 0x7ffu;
    unsigned tmx = __builtin_amdgcn_readfirstlane(*LDS_AT(const volatile unsigned, lbase + SPM + 136)) + KDT;
    if (tmx - (unsigned)__builtin_amdgcn_s_memrealtime() > 1024u) tmx = 0u;
    auto due = [&]() { return tmx == 0u || (int)((unsigned)__builtin_amdgcn_s_memrealtime() - tmx) >= 0; };
    lk_set(15);
    if (wid == 6) {
      if (rtr) {
#pragma unroll 1
        for (int i = 0; i < SPIN_MAX; ++i) {
          if (__builtin_amdgcn_readfirstlane(*LDS_AT(const volatile unsigned, lbase + SPM + 128)) == 7u) break;
          __builtin_amdgcn_s_sleep(1);
        }
        asm volatile("" ::: "memory");
      }
    }
#pragma unroll 1
    for (int i = 0; i < OSLP && !best; ++i) {
      if (wid == 6 && due()) {
        LOG();
        keys(__builtin_amdgcn_raw_buffer_load_b128(rsk, (bid % NREP) * 1024 + lane * 16, 0, 16));
        i += 2;
      } else {
        if (__builtin_amdgcn_readfirstlane(*LDS_AT(const volatile unsigned, lbase + FLG + 40))) break;
        __builtin_amdgcn_s_sleep(2);
      }
      asm volatile("" ::: "memory");
    }
    u32x4 kv = {SENT, SENT, SENT, SENT};
    if (wid == 6 && !best && due()) kv = __builtin_amdgcn_raw_buffer_load_b128(rsk, (bid % NREP) * 1024 + lane * 16, 0, 16);
    PIN();
#pragma unroll
    for (int p = 0; p < 2; ++p) __builtin_amdgcn_raw_buffer_store_b128(xres[p], rsu, row + 8 * p != (int)fr1 ? ((row + 8 * p) * D_ + col) * 2 : S_ * D_ * 2, 0, OAUX);
    PIN();
    if (wid == 6 && !best) { keys(kv); while (!due()) __builtin_amdgcn_s_sleep(1); }
#pragma unroll 1
    for (int i = 0; i < 4 * SPIN_MAX && !best; ++i) {
      if (wid == 6) {
        LOG();
        keys(__builtin_amdgcn_raw_buffer_load_b128(rsk, (bid % NREP) * 1024 + lane * 16, 0, 16));
        if (!best) __builtin_amdgcn_s_sleep(KSLP);
      } else {
        best = __builtin_amdgcn_readfirstlane(*LDS_AT(const volatile unsigned, lbase + FLG + 40));
        if (!best) __builtin_amdgcn_s_sleep(1);
      }
      asm volatile("" ::: "memory");
    }
    const int idx2 = 2047 - (int)(best & 0x7ffu);
    if (__builtin_expect(idx2 != (int)fr1, 0)) {
      const __amdgpu_buffer_rsrc_t rsf = __builtin_amdgcn_make_buffer_rsrc(gran + GR_F, 0, 48 * 128, 0x00020000);
      if (!none((row | 8) == (idx2 | 8))) {
#pragma unroll
        for (int p = 0; p < 2; ++p)
          __builtin_amdgcn_raw_buffer_store_b128(u32x4{pack2(xf[p][0][0], xf[p][0][1]), pack2(xf[p][1][0], xf[p][1][1]), pack2(xf[p][2][0], xf[p][2][1]), pack2(xf[p][3][0], xf[p][3][1])},
              __builtin_amdgcn_make_buffer_rsrc(xout, 0, 3 * D_ * 2, 0x00020000), row + 8 * p == idx2 ? (2 * D_ + col) * 2 : S_ * D_ * 2, 0, 16);
        asm volatile("s_waitcnt vmcnt(0)" ::: "memory");
        __builtin_amdgcn_raw_buffer_store_b32(tag, rsf, lane == 0 ? oni * 128 : 48 * 128, 0, 16);
      }
      if (!none((row | 8) == ((int)fr1 | 8))) {
#pragma unroll 1
        for (int spin = 0; spin < SPIN_MAX; ++spin) {
          if (none(__builtin_amdgcn_raw_buffer_load_b32(rsf, (16 + oni * 2 + (lane & 1)) * 128, 0, 16) != tag)) break;
          asm volatile("" ::: "memory");
        }
#pragma unroll
        for (int p = 0; p < 2; ++p) __builtin_amdgcn_raw_buffer_store_b128(xres[p], rsu, row + 8 * p == (int)fr1 ? ((row + 8 * p) * D_ + col) * 2 : S_ * D_ * 2, 0, OAUX);
      }
    }
  }
  }
}
struct Args2 { Args l[2]; };
extern "C" __global__ __launch_bounds__(768, 3) void layers_kernel(const Args2 args) {
  __shared__ __attribute__((aligned(128))) char smem[163840];
  const unsigned long long t_entry = 0;
  const int tid = threadIdx.x, bid = blockIdx.x;
  int wid = __builtin_amdgcn_readfirstlane(tid >> 6);
  asm volatile("" : "+s"(wid));
  const ArgP ap = (ArgP)__builtin_amdgcn_kernarg_segment_ptr();
  layer_body(wid, bid, (unsigned)(uintptr_t)smem, ap, t_entry);
  LAYER_HERE;
  if (!LAY2) {
    asm volatile(AGAIN :: "{s[0:1]}"(ap + 1), "{s2}"(bid), "{v0}"(wid * 64) : "s90", "s91", "scc", "memory");
    __builtin_unreachable();
  }
}

extern "C" __global__ void end_marker_kernel() {}

extern "C" void kernel_launch(void* const* d_in, const int* in_sizes, int n_in, void* d_out, int out_size,
                              void* d_ws, size_t ws_size, hipStream_t stream) {
  const u16* x0 = (const u16*)d_in[0];
  const u16* wqkv = (const u16*)d_in[1];
  const u16* wo = (const u16*)d_in[2];
  const u16* w_r = (const u16*)d_in[4];
  const u16* wg = (const u16*)d_in[5];
  const u16* wu = (const u16*)d_in[6];
  const u16* wd = (const u16*)d_in[7];
  u16* out = (u16*)d_out;

  char* ws = (char*)d_ws;
  const size_t MB4 = (size_t)S_ * D_ * 2;
#define ALIAS 0
  u16* kv1 = (u16*)(ws);
  u16* o1 = (u16*)(ws + 2 * MB4);
  u16* x1 = (u16*)(ws + 3 * MB4);
  unsigned* gran = (unsigned*)(ws + 4 * MB4);
  float* csf = (float*)(gran + GR_F + 48 * 32);
  double* stats = (double*)(csf + 8192);
  unsigned* hand = (unsigned*)(stats + 2 * STAT_N);
  static_assert((GR_F + 48 * 32) % 32 == 0 && (size_t)(GR_F + 48 * 32 + 8192 + 4 * STAT_N + 2 * HAND_N + PS_N) * 4 <= 6u << 20, "");
  u16* x3 = (u16*)(hand + HAND_N + GRAN_N + Y_N);
  u16* wc = (u16*)((((uintptr_t)ws + 4 * MB4 + (6u << 20) + 2047) & ~(uintptr_t)2047) + (((uintptr_t)wqkv + 128) & 2047));
  u16* kv2 = (ALIAS & 1) ? kv1 : (u16*)(ws + 8 * MB4);
  u16* o2 = (ALIAS & 2) ? o1 : (u16*)(ws + 10 * MB4);
  Args2 a;
  for (int l = 0; l < 2; ++l) {
    Args& r = a.l[l];
    r.lay = l; r.a = l ? x1 : x0; r.kv = l ? kv2 : kv1; r.o = l ? o2 : o1; r.xo = l ? x3 : x1; r.so = l ? stats + STAT_N : stats;
    r.W = wqkv; r.wc = wc; r.wg = wg; r.wu = wu; r.wd = wd; r.wo = wo; r.w_r = w_r; r.gran = gran; r.csf = csf; r.stats = stats; r.hand = hand; r.out = out;
  }
  layers_kernel<<<256, 768, 0, stream>>>(a);
}
```

```cpp
#include <hip/hip_runtime.h>
#include <hip/hip_bf16.h>
#include <stdint.h>
#include <type_traits>
#include <utility>

typedef unsigned short u16;
typedef short s16x4 __attribute__((ext_vector_type(4)));
typedef short s16x8 __attribute__((ext_vector_type(8)));
typedef float f32x16 __attribute__((ext_vector_type(16)));
typedef float f32x4 __attribute__((ext_vector_type(4)));
typedef unsigned int u32x4 __attribute__((ext_vector_type(4)));
typedef unsigned int u32x2 __attribute__((ext_vector_type(2)));
typedef __bf16 bf16x8 __attribute__((ext_vector_type(8)));
typedef __bf16 bf16x16 __attribute__((ext_vector_type(16)));

__device__ __forceinline__ int fresh_lane() {
  int l;
  asm volatile("v_mbcnt_lo_u32_b32 %0, -1, 0\n\tv_mbcnt_hi_u32_b32 %0, -1, %0" : "=v"(l));
  return l;
}
#define STAMP(k, i)
#define STAMPV(k, i, v)
#define LOGDEF
#define LOG()
#define LOGAT(k)
#define lk_set(k)
#define LOGOUT(slot)
#define TE(grp, k)
#define TEARG
#define TEPASS
constexpr int S_ = 2048, D_ = 1024, H_ = 32, F_ = 3584;

__device__ __forceinline__ int kpos(int r) { return (r & ~12) | ((r & 4) << 1) | ((r & 8) >> 1); }
__device__ __forceinline__ float bf2f(u16 v) { return __uint_as_float(((unsigned)v) << 16); }
__device__ __forceinline__ u16 f2bf(float f) {
  __hip_bfloat16 b = __float2bfloat16(f);
  return *reinterpret_cast<u16*>(&b);
}
typedef float f32x2 __attribute__((ext_vector_type(2)));
typedef __bf16 bf16x2 __attribute__((ext_vector_type(2)));
__device__ __forceinline__ unsigned pack2(float lo, float hi) {
  f32x2 v = {lo, hi};
  return __builtin_bit_cast(unsigned, __builtin_convertvector(v, bf16x2));
}
__device__ __forceinline__ float rbf(float f) { return __uint_as_float(pack2(f, f) << 16); }
__device__ __forceinline__ void rbf2(float& a, float& b) {
  const unsigned p = pack2(a, b);
  a = __uint_as_float(p << 16); b = __uint_as_float(p & 0xffff0000u);
}

__device__ __forceinline__ float fadd(float a, float b) { float r = a + b; asm("" : "+v"(r)); return r; }
template <int CTRL, int RMASK>
__device__ __forceinline__ float dpp_add(float v) {
  const int t = __builtin_amdgcn_update_dpp(0, __float_as_int(v), CTRL, RMASK, 0xf, false);
  return v + __int_as_float(t);
}
__device__ __forceinline__ float wave_sum(float v) {
  v = dpp_add<0x111, 0xf>(v);
  v = dpp_add<0x112, 0xf>(v);
  v = dpp_add<0x114, 0xf>(v);
  v = dpp_add<0x118, 0xf>(v);
  v = dpp_add<0x142, 0xa>(v);
  v = dpp_add<0x143, 0xc>(v);
  return __int_as_float(__builtin_amdgcn_readlane(__float_as_int(v), 63));
}

template <int CTRL, int RMASK>
__device__ __forceinline__ unsigned dpp_umax(unsigned v) {
  const unsigned t = (unsigned)__builtin_amdgcn_update_dpp(0, (int)v, CTRL, RMASK, 0xf, false);
  return v > t ? v : t;
}
__device__ __forceinline__ unsigned wave_umax(unsigned v) {
  v = dpp_umax<0x111, 0xf>(v); v = dpp_umax<0x112, 0xf>(v); v = dpp_umax<0x114, 0xf>(v); v = dpp_umax<0x118, 0xf>(v);
  v = dpp_umax<0x142, 0xa>(v); v = dpp_umax<0x143, 0xc>(v);
  return (unsigned)__builtin_amdgcn_readlane((int)v, 63);
}

#define NREP 32
constexpr unsigned SENT = 0xFFFFFFFFu;
constexpr int GRAN_N = 256 * NREP;
constexpr int Y_N = F_ / 16 * D_;
constexpr int X2_N = D_ / 2;
constexpr int XS_N = 128;
constexpr int P4_N = 3 * D_ * 16;
constexpr int PA_N = H_ * 8 * 64;
constexpr int HAND_N = GRAN_N + Y_N + X2_N + XS_N + P4_N + PA_N;
constexpr int MD_O = 16 * 16 * 128 * 4;
constexpr int PK_O = MD_O;
constexpr int PP_O = PK_O + 128 * NREP;
constexpr int Y3_O = PP_O + H_ * S_;
constexpr int PS_N = Y3_O + Y_N;
constexpr int GR_ROW = 64, GR_COL = GR_ROW + 4 * S_, GR_KV = GR_COL + 16 * 16 * 192;
#define RO 16
#define RK 16
#define RX 1
constexpr int GR_O = GR_KV + 2 * 16 * 32 * RK, GR_X = GR_O + 2 * 16 * 64 * RO, GR_N = GR_X + 16 * 128 * RX;
constexpr int GR_F = GR_N + 256 * 64;
constexpr int STAT_N = 3 * S_;

__device__ __forceinline__ void st_ag(unsigned* p, unsigned v) { __hip_atomic_store(p, v, __ATOMIC_RELAXED, __HIP_MEMORY_SCOPE_AGENT); }
__device__ __forceinline__ unsigned ld_ag(const unsigned* p) { return __hip_atomic_load(p, __ATOMIC_RELAXED, __HIP_MEMORY_SCOPE_AGENT); }
__device__ __forceinline__ double ld_ag(const double* p) { return __hip_atomic_load(p, __ATOMIC_RELAXED, __HIP_MEMORY_SCOPE_AGENT); }
__device__ __forceinline__ unsigned f2word(float f) { const unsigned u = __float_as_uint(f); return u < 0xFFFFFFFEu ? u : 0xFFFFFFFEu; }
constexpr int SPIN_MAX = 1 << 18;
__device__ __forceinline__ unsigned umax(unsigned a, unsigned b) { return a > b ? a : b; }
__device__ __forceinline__ bool none(bool p) { return __builtin_amdgcn_ballot_w64(p) == 0ull; }

__device__ __forceinline__ void ln_from_sums(double s1, double s2, float& mean, float& var, float& mb, float& den) {
  const double md = s1 * (1.0 / 1024.0);
  const double vd = fma(-md, md, s2 * (1.0 / 1024.0));
  mean = (float)md; var = fmaxf((float)vd, 0.f);
  mb = rbf(mean);
  den = rbf(__builtin_amdgcn_sqrtf(rbf(rbf(var) + 1e-5f)));
}
__device__ __forceinline__ float frcp(float x) { return __builtin_amdgcn_rcpf(x); }
__device__ __forceinline__ void ln_apply2(float& a, float& b, float mb, float den, float rin) {
  a -= mb; b -= mb;
  rbf2(a, b);
  float qa = a * rin, qb = b * rin;
  qa = fmaf(fmaf(-qa, den, a), rin, qa);
  qb = fmaf(fmaf(-qb, den, b), rin, qb);
  rbf2(qa, qb);
  a = qa; b = qb;
}

#define CODE_ADDR(SYM, out64)                                                                                          \
  do {                                                                                                                 \
    unsigned lo_, hi_;                                                                                                 \
    asm volatile("s_getpc_b64 s[90:91]\n\ts_add_u32 s90, s90, " SYM "@rel32@lo+4\n\ts_addc_u32 s91, s91, " SYM          \
                 "@rel32@hi+12\n\ts_mov_b32 %0, s90\n\ts_mov_b32 %1, s91"                                             \
                 : "=s"(lo_), "=s"(hi_) :: "s90", "s91", "scc");                                                        \
    out64 = ((unsigned long long)hi_ << 32) | lo_;                                                                     \
  } while (0)

#define QNX 4
constexpr int QMX = 32 / QNX, QNG = 16 / QNX, QMG = 8 / QNG;

__device__ __forceinline__ unsigned touch(const void* p) { return *reinterpret_cast<const unsigned*>(p); }

__device__ __forceinline__ unsigned touch_code(unsigned long long lo, unsigned long long hi, int pc, int lane) {
  const unsigned long long p = lo + (unsigned)(pc * 8192 + lane * 128);
  return p + 4 <= hi ? *reinterpret_cast<__attribute__((address_space(1))) const unsigned*>(p) : 0u;
}

template <bool SC = false>
__device__ __forceinline__ void glds16s(const void* sbase, unsigned voff, unsigned lds_dst) {
  unsigned keep;
  if (SC)
    asm volatile("s_mov_b32 %0, m0\n\ts_mov_b32 m0, %3\n\ts_nop 2\n\tglobal_load_lds_dwordx4 %1, %2 sc1\n\ts_mov_b32 m0, %0"
                 : "=&s"(keep) : "v"(voff), "s"(sbase), "s"(lds_dst) : "memory");
  else
  asm volatile("s_mov_b32 %0, m0\n\ts_mov_b32 m0, %3\n\ts_nop 2\n\tglobal_load_lds_dwordx4 %1, %2\n\ts_mov_b32 m0, %0"
               : "=&s"(keep) : "v"(voff), "s"(sbase), "s"(lds_dst) : "memory");
}
template <bool SC>
__device__ __forceinline__ void glds16x4(const void* sbase, unsigned v0, unsigned v1, unsigned v2, unsigned v3, unsigned lds_dst) {
  unsigned keep;
  if (SC)
    asm volatile("s_mov_b32 %0, m0\n\ts_mov_b32 m0, %6\n\ts_nop 2\n\tglobal_load_lds_dwordx4 %1, %5 sc1\n\tglobal_load_lds_dwordx4 %2, %5 offset:1024 sc1\n\t"
                 "global_load_lds_dwordx4 %3, %5 offset:2048 sc1\n\tglobal_load_lds_dwordx4 %4, %5 offset:3072 sc1\n\ts_mov_b32 m0, %0"
                 : "=&s"(keep) : "v"(v0), "v"(v1), "v"(v2), "v"(v3), "s"(sbase), "s"(__builtin_amdgcn_readfirstlane(lds_dst)) : "memory");
  else
    asm volatile("s_mov_b32 %0, m0\n\ts_mov_b32 m0, %6\n\ts_nop 2\n\tglobal_load_lds_dwordx4 %1, %5\n\tglobal_load_lds_dwordx4 %2, %5 offset:1024\n\t"
                 "global_load_lds_dwordx4 %3, %5 offset:2048\n\tglobal_load_lds_dwordx4 %4, %5 offset:3072\n\ts_mov_b32 m0, %0"
                 : "=&s"(keep) : "v"(v0), "v"(v1), "v"(v2), "v"(v3), "s"(sbase), "s"(__builtin_amdgcn_readfirstlane(lds_dst)) : "memory");
}
constexpr float QSCALE = 0.17677669529663687f * 1.4426950408889634f;

__device__ __forceinline__ unsigned min4(u32x4 v) { const unsigned a = v[0] < v[1] ? v[0] : v[1], b = v[2] < v[3] ? v[2] : v[3]; return a < b ? a : b; }
#define LDS_AT(T, a) ((__attribute__((address_space(3))) T*)(uintptr_t)(a))
__device__ __forceinline__ u32x4 lds_rd4(unsigned a) { return *LDS_AT(const volatile u32x4, a); }
__device__ __forceinline__ void lds_wr(unsigned a, unsigned v) { *LDS_AT(volatile unsigned, a) = v; }
__device__ __forceinline__ void split3(float v, unsigned& p0, unsigned& p1, unsigned& p2) {
  p0 = pack2(v, v) & 0xffffu; v -= __uint_as_float(p0 << 16);
  p1 = pack2(v, v) & 0xffffu; v -= __uint_as_float(p1 << 16);
  p2 = pack2(v, v) & 0xffffu;
}

#define PIN() __builtin_amdgcn_sched_barrier(0)
template <int B, class F, int... I>
__device__ __forceinline__ void static_for_(F&& f, std::integer_sequence<int, I...>) { (f(std::integral_constant<int, B + I>{}), ...); }
template <int B, int E, class F>
__device__ __forceinline__ void static_for(F&& f) { static_for_<B>(f, std::make_integer_sequence<int, E - B>{}); }

#define TPRE 60
#define TPSL 0
#define FCPRE 44
#define FCSLP 2
#define SIGAT 24
#define XPRIO 1
#define OPD 6
#define LAUX 16
#define CSLP 2
#define EPRIO 3
#define OPRIO 1
#define XSLP 1
#define CSL1 8
#define XAUX 16
__device__ __forceinline__ void oproj_tail(const bool RES2, const int tid, const unsigned lbase, const unsigned flg, const int tmi, const int tni, const int idx,
                                           unsigned* osig, unsigned* xsig, const unsigned tag,
                                           const u16* A, u16* out,
                                           const u16* res, const u16* __restrict__ w_r, double* stats,
                                           const double* stats_in, unsigned* hand, u32x4 (&xres)[2] TEARG) {
  constexpr int ldw = D_, PD = OPD;
  const int lane = tid & 63, wid = __builtin_amdgcn_readfirstlane(tid >> 6);
  const int m0 = tmi * 128, n0 = tni * 64;
  const int dk = RES2 ? 7 : 3;
  if (wid == 0) STAMP(dk, 0);
  TE(1, 3);
  asm volatile("" : "+s"(A) :: "memory");
  PIN();
  const int row = m0 + (wid & 3) * 32 + (wid >> 2) * 16 + (lane >> 3), col = n0 + (lane & 7) * 8;
  const unsigned tb = lbase + 147456;
  typedef __attribute__((address_space(1))) double* gdp;
  typedef __attribute__((address_space(1))) unsigned* gup;
  unsigned tr, oad;
  gdp pst;
  gup pps;
  int xo;
  auto setup = [&]() __attribute__((always_inline)) {
    tr = tb + (row - m0) * 128 + (((lane ^ (lane >> 3)) & 7) << 4);
    pst = (gdp)(stats + ((lane & 3) == 3 ? 0 : (lane & 3)) * S_ + row + 8 * ((lane >> 2) & 1));
    pps = (gup)(hand + 2 * HAND_N + ((tmi * 16 + tni) * 128 + row - m0 + 8 * ((lane >> 2) & 1)) * 4 + (lane & 3));
    xo = (row * ldw + col) * 2;
    asm volatile("" : "+v"(tr), "+v"(pst), "+v"(pps), "+v"(xo));
    PIN();
  };
  {
    oad = lane == 0 ? flg + 24 : flg + 64 + lane * 4;
    gup ps = (gup)(osig + lane * 64);
    asm volatile("" : "+v"(oad), "+v"(ps));
    asm volatile("s_waitcnt vmcnt(1)" ::: "memory");
    TE(2, 1);
    const unsigned old = __hip_atomic_fetch_add(LDS_AT(unsigned, oad), 1u, __ATOMIC_RELAXED, __HIP_MEMORY_SCOPE_WORKGROUP);
    if (__builtin_amdgcn_readfirstlane(old) == 7u && lane < RO) __hip_atomic_store(ps, tag, __ATOMIC_RELAXED, __HIP_MEMORY_SCOPE_AGENT);
    PIN();
    TE(2, 2);
    if (wid == 0) STAMP(dk, 12);
  }
  auto announce = [&]() __attribute__((always_inline)) {
    asm volatile("" ::: "memory");
    PIN();
    __builtin_amdgcn_s_barrier();
    PIN();
    if (wid == 0) STAMP(dk, 1);
    TE(2, 3); TE(4, 0); TE(7, 1);
  };
  u32x4 rwr;
  auto sums = [&](const u32x4& x0, const u32x4& x1) __attribute__((always_inline)) {
    float f[2][3];
#pragma unroll
    for (int p = 0; p < 2; ++p) {
      f[p][0] = f[p][1] = f[p][2] = 0.f;
#pragma unroll
      for (int e = 0; e < 4; ++e) {
        const unsigned xu = p ? x1[e] : x0[e], wu = rwr[e];
        const bf16x2 x = __builtin_bit_cast(bf16x2, xu);
        f[p][0] = __builtin_amdgcn_fdot2_f32_bf16(x, __builtin_bit_cast(bf16x2, 0x3f803f80u), f[p][0], false);
        f[p][1] = __builtin_amdgcn_fdot2_f32_bf16(x, x, f[p][1], false);
        f[p][2] = __builtin_amdgcn_fdot2_f32_bf16(x, __builtin_bit_cast(bf16x2, wu), f[p][2], false);
      }
    }
    auto addx = [&](float keep, float give, auto ctl) {
      return keep + __int_as_float(__builtin_amdgcn_update_dpp(0, __float_as_int(give), decltype(ctl)::value, 0xf, 0xf, false));
    };
    const bool b2 = lane & 4, b1 = lane & 2, z0 = (lane & 3) == 0, z1 = (lane & 3) == 1;
    float g[3];
#pragma unroll
    for (int c = 0; c < 3; ++c) g[c] = addx(b2 ? f[1][c] : f[0][c], b2 ? f[0][c] : f[1][c], std::integral_constant<int, 0x141>{});
    const float u1 = addx(b1 ? g[2] : g[0], b1 ? g[0] : g[2], std::integral_constant<int, 0x4e>{});
    const float u2 = addx(g[1], g[1], std::integral_constant<int, 0x4e>{});
    return addx(z1 ? u2 : u1, z0 ? u2 : u1, std::integral_constant<int, 0xb1>{});
  };
  if (wid < 4) {
    const __amdgpu_buffer_rsrc_t rsa = __builtin_amdgcn_make_buffer_rsrc(const_cast<u16*>(A), 0, S_ * D_ * 2, 0x00020000);
    int sa0 = (tmi * 4 + wid) * 65536, va0 = (lane >> 4) * 512 + (lane & 15) * 16;
    asm volatile("" : "+v"(va0));
    asm volatile("" : "+s"(sa0));
    auto lda = [&](int i, int h) { return __builtin_amdgcn_raw_buffer_load_b128(rsa, va0 + (i & 1) * 2048 + h * 256, sa0 + (i >> 1) * 4096, LAUX); };
    unsigned wad[4], tw[4];
    f32x4 acc[2][4];
    const int k = (lane >> 5) * 16 + ((lane >> 2) & 7);
#pragma unroll
    for (int j = 0; j < 4; ++j) {
      wad[j] = lbase + k * 128 + (((2 * j) ^ (k & 6)) << 4) + 8 * (lane & 3);
      tw[j] = tb + wid * 4096 + (lane & 15) * 128 + (((2 * j + (lane >> 5)) ^ (lane & 7)) << 4) + ((lane >> 4) & 1) * 8;
      asm volatile("" : "+v"(wad[j]), "+v"(tw[j]));
    }
    auto ldw_ = [&](int i, int j) {
      const unsigned a = wad[j] + (i & 3) * 4096;
      s16x4 lo = __builtin_amdgcn_ds_read_tr16_b64_v4i16(LDS_AT(s16x4, a));
      s16x4 h4 = __builtin_amdgcn_ds_read_tr16_b64_v4i16(LDS_AT(s16x4, a + 1024));
      return (s16x8)__builtin_shufflevector(lo, h4, 0, 1, 2, 3, 4, 5, 6, 7);
    };
    u32x4 id[2];
#pragma unroll
    for (int j = 0; j < 2; ++j) {
#pragma unroll
      for (int e = 0; e < 4; ++e) id[j][e] = (lane >> 4) == 2 * j + ((lane >> 3) & 1) && ((lane & 7) >> 1) == e ? ((lane & 1) ? 0x3f800000u : 0x3f80u) : 0u;
      asm volatile("" : "+v"(id[j]));
    }
    unsigned rad = tb + wid * 4096 + (lane & 15) * 128 + (((lane >> 4) ^ (lane & 7)) << 4), rfa = flg + 36 + wid;
    asm volatile("" : "+v"(rad), "+v"(rfa));
    u32x4 rb[2][2];
    rwr = *reinterpret_cast<const u32x4*>(w_r + col);
    setup();
    asm volatile("" : "+v"(rwr));
    announce();
    u32x4 af[PD][2];
    s16x8 wf[2][4];
#pragma unroll
    for (int i = 0; i < PD; ++i) { af[i][0] = lda(i, 0); af[i][1] = lda(i, 1); }
    PIN();
    __builtin_amdgcn_s_setprio(OPRIO);
#pragma unroll
    for (int q = 0; q < 4; ++q) rb[q >> 1][q & 1] = lds_rd4((rad ^ ((q & 1) * 64)) + (q >> 1) * 2048);
    PIN();
#pragma unroll
    for (int j = 0; j < 4; ++j)
#pragma unroll
      for (int h = 0; h < 2; ++h) { asm volatile("v_mfma_f32_16x16x32_bf16 %0, %1, %2, 0" : "=&v"(acc[h][j]) : "v"(id[j & 1]), "v"(rb[h][j >> 1])); PIN(); }
#pragma unroll 1
    for (int spin = 0; spin < SPIN_MAX; ++spin)
      if (__builtin_amdgcn_readfirstlane(*LDS_AT(const volatile unsigned, flg)) >= 40u) break;
    asm volatile("" : "+v"(va0) :: "memory");
    PIN();
#pragma unroll
    for (int j = 0; j < 4; ++j) wf[0][j] = ldw_(0, j);
    s16x4 wlo;
    static_for<0, 32>([&](auto ic) __attribute__((always_inline)) {
      constexpr int i = decltype(ic)::value;
      if (i == SIGAT) { __hip_atomic_fetch_add(LDS_AT(unsigned, oad + 4), 1u, __ATOMIC_RELAXED, __HIP_MEMORY_SCOPE_WORKGROUP); PIN(); }
#pragma unroll
      for (int g = 0; g < 8; ++g) {
        const int j = g >> 1, h = g & 1;
        asm volatile("v_mfma_f32_16x16x32_bf16 %0, %1, %2, %0" : "+v"(acc[h][j]) : "v"(wf[i & 1][j]), "v"(af[i % PD][h]));
        PIN();
        if (i == 15 && !(g & 1)) { wad[g >> 1] += 65536; asm volatile("" : "+v"(wad[g >> 1])); }
        if (i + 1 < 32) {
          const s16x4 v = __builtin_amdgcn_ds_read_tr16_b64_v4i16(LDS_AT(s16x4, wad[g >> 1] + ((i + 1) & 15) * 4096 + (g & 1) * 1024));
          if (g & 1) wf[(i + 1) & 1][g >> 1] = (s16x8)__builtin_shufflevector(wlo, v, 0, 1, 2, 3, 4, 5, 6, 7);
          else wlo = v;
          PIN();
        }
        if (g >= 6 && i + PD < 32) { af[i % PD][h] = lda(i + PD, h); PIN(); }
        if (i + 1 < 32 && i != SIGAT && i + 1 != SIGAT) {
          if (g == 3) { __builtin_amdgcn_s_waitcnt(0xc47f); PIN(); }
          if (g == 7 && i + PD < 32) { __builtin_amdgcn_s_waitcnt(0x0470 | (2 * PD - 2)); PIN(); }
          if (g == 7 && i + PD >= 32) { __builtin_amdgcn_s_waitcnt(0xc47f); PIN(); }
        }
      }
    });
    asm volatile("s_nop 3" : "+v"(acc[0][0]), "+v"(acc[1][0]));
    TE(4, 1); TE(7, 2);
    if (wid == 0) STAMP(dk, 17);
    if (wid == 1 && !RES2) STAMP(4, 0);
#pragma unroll
    for (int j = 0; j < 4; ++j)
#pragma unroll
      for (int h = 0; h < 2; ++h) {
        *LDS_AT(u32x2, tw[j] + h * 2048) = u32x2{pack2(acc[h][j][0], acc[h][j][1]), pack2(acc[h][j][2], acc[h][j][3])};
        PIN();
      }
    asm volatile("" ::: "memory");
    *LDS_AT(volatile unsigned char, rfa) = (unsigned char)2;
#pragma unroll
    for (int p = 0; p < 2; ++p) xres[p] = lds_rd4(tr + p * 1024);
    PIN();
    if (!RES2) {
      const __amdgpu_buffer_rsrc_t rso = __builtin_amdgcn_make_buffer_rsrc(out, 0, S_ * D_ * 2, 0x00020000);
      const __amdgpu_buffer_rsrc_t rsf = __builtin_amdgcn_make_buffer_rsrc(xsig + wid, 0, RX * 512, 0x00020000);
      const u32x4 x2 = lds_rd4(tr + 2048), x3 = lds_rd4(tr + 3072);
      PIN();
#pragma unroll
      for (int p = 0; p < 4; ++p) { __builtin_amdgcn_raw_buffer_store_b128(p < 2 ? xres[p] : (p < 3 ? x2 : x3), rso, xo + p * (8 * ldw * 2), 0, XAUX); PIN(); }
      TE(4, 2); TE(5, 0);
      const float s0 = sums(xres[0], xres[1]), s1 = sums(x2, x3);
      if ((lane & 3) != 3) { __builtin_amdgcn_global_atomic_fadd_f64(pst, (double)s0); __builtin_amdgcn_global_atomic_fadd_f64(pst + 16, (double)s1); }
      PIN();
      TE(4, 3); TE(5, 1);
      asm volatile("s_waitcnt vmcnt(2)" ::: "memory");
      __builtin_amdgcn_raw_buffer_store_b32(tag, rsf, lane * 512 + 256, 0, 16);
      *LDS_AT(volatile unsigned char, rfa) = (unsigned char)3;
      PIN();
      TE(5, 2);
      asm volatile("s_waitcnt vmcnt(1)" ::: "memory");
      __builtin_amdgcn_raw_buffer_store_b32(tag, rsf, lane * 512, 0, 16);
      PIN();
      TE(5, 3);
      __builtin_amdgcn_s_setprio(0);
      return;
    }
    TE(4, 2); TE(5, 0);
  } else {
    u32x4 rres[4];
    double is1[4], is2[4];
#pragma unroll
    for (int p = 0; p < 4; ++p)
      rres[p] = __builtin_amdgcn_raw_buffer_load_b128(__builtin_amdgcn_make_buffer_rsrc(const_cast<u16*>(res), 0, S_ * D_ * 2, 0x00020000), ((row - 16 + 8 * p) * ldw + col) * 2, 0, 16);
    rwr = *reinterpret_cast<const u32x4*>(w_r + col);
#pragma unroll
    for (int p = 0; p < 4; ++p) { is1[p] = ld_ag(stats_in + row - 16 + 8 * p); is2[p] = ld_ag(stats_in + S_ + row - 16 + 8 * p); }
    PIN();
    setup();
    if (RES2) {
#pragma unroll
      for (int p = 0; p < 4; ++p) {
        float mean, var, mb, den;
        ln_from_sums(is1[p], is2[p], mean, var, mb, den);
        const float rin = frcp(den);
#pragma unroll
        for (int e = 0; e < 4; ++e) {
          const f32x2 x = {__uint_as_float(rres[p][e] << 16), __uint_as_float(rres[p][e] & 0xffff0000u)};
          const f32x2 h = (x - f32x2{mb, mb}) * f32x2{rin, rin} + x;
          rres[p][e] = pack2(h[0], h[1]);
        }
        asm volatile("" : "+v"(rres[p]));
      }
      PIN();
    }
    if ((idx >> 5) == tmi * 4 + wid - 4) {
      const int dr = idx - (row - 16);
      u32x4 qq;
#pragma unroll 1
      for (int i = 0; i < SPIN_MAX; ++i) {
        qq = __builtin_amdgcn_raw_buffer_load_b128(__builtin_amdgcn_make_buffer_rsrc(hand, 0, HAND_N * 4, 0x00020000), (GRAN_N + Y_N) * 4 + col * 2, 0, 16);
        if (none(umax(umax(qq[0], qq[1]), umax(qq[2], qq[3])) == SENT)) break;
        asm volatile("" ::: "memory");
      }
#pragma unroll
      for (int p = 0; p < 4; ++p)
#pragma unroll
        for (int e = 0; e < 4; ++e) rres[p][e] = dr == 8 * p ? qq[e] : rres[p][e];
    }
#pragma unroll 1
    for (int i = 0; i < 4 * SPIN_MAX; ++i)
      if (__builtin_amdgcn_readfirstlane(*LDS_AT(const volatile unsigned, flg + 16)) >= 12u) break;
#pragma unroll
    for (int p = 0; p < 4; ++p) *LDS_AT(volatile u32x4, tr - 2048 + p * 1024) = rres[p];
    asm volatile("s_waitcnt lgkmcnt(0)" ::: "memory");
    announce();
    TE(4, 1); TE(7, 2);
    asm volatile("s_waitcnt vmcnt(0)" : "+v"(rwr) :: "memory");
    if (!RES2) return;
    __builtin_amdgcn_s_sleep(TPRE);
#pragma unroll 1
    for (int i = 0; i < 4 * SPIN_MAX; ++i) {
      if (__builtin_amdgcn_readfirstlane(*LDS_AT(const volatile unsigned char, flg + 32 + wid)) == 2u) break;
      __builtin_amdgcn_s_sleep(TPSL);
    }
    asm volatile("" ::: "memory");
    __builtin_amdgcn_s_setprio(XPRIO);
    PIN();
    TE(4, 2); TE(5, 0);
#pragma unroll
    for (int p = 0; p < 2; ++p) xres[p] = lds_rd4(tr + p * 1024);
  }
  TE(4, 3); TE(7, 3);
  const float sv = sums(xres[0], xres[1]);
  if (wid == 0) STAMP(dk, 14);
  if ((lane & 3) != 3) __hip_atomic_store(pps, f2word(sv), __ATOMIC_RELAXED, __HIP_MEMORY_SCOPE_AGENT);
  PIN();
  __builtin_amdgcn_s_setprio(0);
  __hip_atomic_fetch_add(LDS_AT(unsigned, oad), 1u, __ATOMIC_RELAXED, __HIP_MEMORY_SCOPE_WORKGROUP);
  TE(5, 1); TE(5, 2);
  PIN();
}

#define MFMA32(a, b, c) __builtin_amdgcn_mfma_f32_32x32x16_bf16(a, b, c, 0, 0, 0)
#define MFMA16(a, b, c) __builtin_amdgcn_mfma_f32_16x16x32_bf16(a, b, c, 0, 0, 0)

#define PLO(x) __builtin_bit_cast(bf16x16, __builtin_shufflevector(x, x, 0, 1, 2, 3, 4, 5, 6, 7))
#define PHI(x) __builtin_bit_cast(bf16x16, __builtin_shufflevector(x, x, 8, 9, 10, 11, 12, 13, 14, 15))
#define SMF32(a, b, c) __builtin_amdgcn_smfmac_f32_32x32x32_bf16(__builtin_bit_cast(bf16x8, a), b, c, sidx, 0, 0)
#define SMF16(a, b, c) __builtin_amdgcn_smfmac_f32_16x16x64_bf16(__builtin_bit_cast(bf16x8, a), b, c, sidx, 0, 0)

__device__ __forceinline__ void patch_kv(unsigned tab, int r, s16x8& k0, s16x8& k1, s16x8& v0, s16x8& v1, bool on = true) {
  const int lane = fresh_lane(), hi = lane >> 5;
  if (on && (lane & 31) == kpos(r)) {
    k0 = *LDS_AT(const s16x8, tab + hi * 16); k1 = *LDS_AT(const s16x8, tab + 32 + hi * 16);
  }
  const short vv = *LDS_AT(const short, tab + 128 + (lane & 31) * 2);
  const int js = (on && hi == ((r >> 2) & 1)) ? (r >> 4) * 8 + ((r >> 3) & 1) * 4 + (r & 3) : -1;
#pragma unroll
  for (int e = 0; e < 8; ++e) { v0[e] = js == e ? vv : v0[e]; v1[e] = js == 8 + e ? vv : v1[e]; }
}

__device__ __forceinline__ void attn_safe(const u16* KV, int tile0, s16x8 q0, s16x8 q1,
                                       f32x16& o, float& l_out, int ptile, int prow, unsigned ptab) {
  const int lane = fresh_lane();
  float m = -INFINITY, l = 0.f;
  const int sidx = 0xDDDDDDDD;
  const __amdgpu_buffer_rsrc_t rs = __builtin_amdgcn_make_buffer_rsrc(const_cast<u16*>(KV), 0, 2 * S_ * D_ * 2, 0x00020000);
#pragma unroll
  for (int e = 0; e < 16; ++e) o[e] = 0.f;
  for (int tt = 0; tt < 64; ++tt) {
    const int t = tile0 + tt, off = t * 2048 + lane * 16;
    s16x8 k0 = __builtin_bit_cast(s16x8, __builtin_amdgcn_raw_buffer_load_b128(rs, off, 0, 16));
    s16x8 k1 = __builtin_bit_cast(s16x8, __builtin_amdgcn_raw_buffer_load_b128(rs, off + 1024, 0, 16));
    s16x8 v0 = __builtin_bit_cast(s16x8, __builtin_amdgcn_raw_buffer_load_b128(rs, off + S_ * D_ * 2, 0, 16));
    s16x8 v1 = __builtin_bit_cast(s16x8, __builtin_amdgcn_raw_buffer_load_b128(rs, off + S_ * D_ * 2 + 1024, 0, 16));
    if (t == ptile) patch_kv(ptab, prow, k0, k1, v0, v1);
    f32x16 sc;
#pragma unroll
    for (int e = 0; e < 16; ++e) sc[e] = 0.f;
    sc = MFMA32(k0, q0, sc); sc = MFMA32(k1, q1, sc);
    float mt = sc[0];
#pragma unroll
    for (int e = 1; e < 16; ++e) mt = fmaxf(mt, sc[e]);
    mt = fmaxf(mt, __shfl_xor(mt, 32, 64));
    const float mn = fmaxf(m, mt);
    const float alpha = __builtin_amdgcn_exp2f(m - mn);
    m = mn;
#pragma unroll
    for (int e = 0; e < 16; ++e) sc[e] = __builtin_amdgcn_exp2f(sc[e] - mn);
    float ls = 0.f;
#pragma unroll
    for (int e = 0; e < 16; ++e) ls += __uint_as_float(__float_as_uint(sc[e]) & 0xffff0000u);
    l = l * alpha + ls;
#pragma unroll
    for (int e = 0; e < 16; ++e) o[e] *= alpha;
    o = SMF32(v0, PLO(sc), o); o = SMF32(v1, PHI(sc), o);
  }
  l_out = l + __shfl_xor(l, 32, 64);
}

__device__ __forceinline__ float wave_fmax(float v) {
  asm volatile("s_nop 1\n\tv_max_f32_dpp %0, %0, %0 row_shr:1 row_mask:0xf bank_mask:0xf\n\ts_nop 1\n\tv_max_f32_dpp %0, %0, %0 row_shr:2 row_mask:0xf bank_mask:0xf\n\t"
               "s_nop 1\n\tv_max_f32_dpp %0, %0, %0 row_shr:4 row_mask:0xf bank_mask:0xf\n\ts_nop 1\n\tv_max_f32_dpp %0, %0, %0 row_shr:8 row_mask:0xf bank_mask:0xf\n\t"
               "s_nop 1\n\tv_max_f32_dpp %0, %0, %0 row_bcast:15 row_mask:0xa bank_mask:0xf\n\ts_nop 1\n\tv_max_f32_dpp %0, %0, %0 row_bcast:31 row_mask:0xc bank_mask:0xf\n\ts_nop 0"
               : "+v"(v));
  return __int_as_float(__builtin_amdgcn_readlane(__float_as_int(v), 63));
}

#define PSLP 8
#define WSLP 3
#define PRE1 8
#define PRE5 8
#define W3SLP 4
#define FSTART 10
#define OAUX 16
#define OMX 4
#define T0 2u
#define TW 10
#define HW 2
#define HP1 1
#define POLLAT 11
#define X2AT 2
#define HPRIO 3
#define SSLP 1
#define SPRE 6
#define WGAPS {0, 1, 7, 8, 9, 10, 11, 12}
#define KVS 4
#define PNS 12
#define G0 5u
#define GS 2u
#define IAT 1u
#define YSLP 2
#define ISLP 8
#define KSLP 2
#define UAT 13u
#define UHW 0
#define PPSLP 2
#define XRSLP 2
#define MPRE 20
#define YD1 150
#define YD2 180
#define KDT 200
#define XDT 45
#define R2PRIO 0
#define LNPRIO 1
#define MSLP 4
#define BARG 0
struct Args {
  long long lay;
  const u16* a; u16* kv; u16* o; u16* xo; double* so;
  const u16* W; u16* wc;
  const u16 *wg, *wu, *wd, *wo, *w_r;
  unsigned* gran; float* csf; double* stats; unsigned* hand; u16* out;
};
typedef __attribute__((address_space(4))) const Args* ArgP;
#define WCAT 3
#define CAP0 1
#define PRE4 50
#define PRE7 20
#define UARR 12u
#define GF0 10
#define XGATE 8
#define ASLP 1
#define B15 3
#define KO 5
#define PRO 2
#define HS 2
#define XD2 90
#define WMAP 0x32100333
#define SMAP 0
#define STCH 6
#define KPOLL 10
#define TPRIO 2
#define TSLP 9
__device__ __forceinline__ bool slow_cols(const u16* W, int tni) { return ((((unsigned)(uintptr_t)W >> 7) + tni) & 7) == 3; }
#define ARGS_HERE                                                                                                      \
  ArgP ap_ = ap; asm volatile("" : "+s"(ap_));                                                                          \
  const u16* A = ap_->a; const u16* __restrict__ W = ap_->W; u16* KV = ap_->kv; u16* Of = ap_->o;        \
  const u16* __restrict__ wg = ap_->wg; const u16* __restrict__ wu = ap_->wu; const u16* __restrict__ wd = ap_->wd;       \
  const u16* __restrict__ wo = ap_->wo; const u16* __restrict__ w_r = ap_->w_r; unsigned* gran = ap_->gran;              \
  float* csf = ap_->csf; const double* stats = ap_->stats; double* stats_out = ap_->so; unsigned* hand = ap_->hand; \
  u16* xout = ap_->xo; u16* __restrict__ out = ap_->out
#define AGAIN "s_getpc_b64 s[90:91]\n\ts_add_u32 s90, s90, layers_kernel@rel32@lo+4\n\ts_addc_u32 s91, s91, layers_kernel@rel32@hi+12\n\ts_setpc_b64 s[90:91]"
#define KEPT(c)                                                                                                        \
  c "{v[104:107]}"(gu[0]), c "{v[108:111]}"(gu[1]), c "{v[112:115]}"(gu[2]), c "{v[116:119]}"(gu[3]), c "{v[120:123]}"(gu[4]),      \
  c "{v[124:127]}"(gu[5]), c "{v[128:131]}"(gu[6]), c "{v[132:135]}"(gu[7]), c "{v[136:139]}"(gu[8]), c "{v[140:143]}"(gu[9]),      \
  c "{v[144:147]}"(gu[10]), c "{v[148:151]}"(gu[11]), c "{v[152:155]}"(gu[12]), c "{v[156:159]}"(gu[13]), c "{v[160:163]}"(gu[14]), \
  c "{v[164:167]}"(gu[15])
#define LAYER_HERE ArgP apl_ = ap; asm volatile("" : "+s"(apl_)); const bool LAY2 = apl_->lay != 0
__device__ __forceinline__ void layer_body(const int wid, const int bid, const unsigned lbase, const ArgP ap, const unsigned long long t_entry) {
  constexpr int K = 1024, NKT = 16, NST = 3, ABYTES = 16384, WBYTES = 24576, STB = ABYTES + WBYTES, ldw = 3 * D_;
  constexpr int TABA = NST * STB, ALPHA = TABA + 2048, CRED = ALPHA + 512;
  constexpr int ASB = 16384, FLG = 8 * ASB + 256;
  constexpr int DUM = FLG + 64, TAB = DUM + 256, RED = TAB + 512, SACT = RED + 4096, XML = SACT + 3584, UGL = XML + 4096, SPM = UGL + 1024, QOFF = 9 * ASB;
  constexpr int TABW = RED;
  static_assert(SPM + 2048 <= QOFF && CRED <= 8 * ASB && RED + 8192 <= QOFF && RED % 16 == 0, "lds");
  const int xcd = (bid & 1) * 4 + ((bid >> 1) & 3), loc = bid >> 3;
  const int tmi = (xcd / QNG) * QMX + loc / QNX, tni = (xcd % QNG) * QNX + loc % QNX;
  const int m0 = tmi * 128, n0 = tni * 64;
  const int omi = (xcd / (OMX / 2)) * OMX + loc / (32 / OMX), oni = (xcd % (OMX / 2)) * (32 / OMX) + loc % (32 / OMX);
  constexpr unsigned VOFF = (unsigned)S_ * D_ * 2;
  auto tile_at = [&](unsigned n) __attribute__((always_inline)) {
#pragma unroll 1
    for (int i = 0; i < 4 * SPIN_MAX; ++i) {
      if (__builtin_amdgcn_readfirstlane(*LDS_AT(const volatile unsigned, lbase + FLG + 36)) == n * 0x01010101u) break;
      __builtin_amdgcn_s_sleep(1);
    }
    asm volatile("" ::: "memory");
    PIN();
  };
  auto all_ge = [&](unsigned w, unsigned n) { return ((w + (128u - n) * 0x01010101u) & 0x80808080u) == 0x80808080u; };
  if (wid >= 8) {
    u32x4 gu[16];
    LAYER_HERE;
    constexpr int dk = 6;
    const int hw = wid - 8, b = bid;
    int nb = 0;
    constexpr int lim = NKT - 1;
    auto tick_nw = [&]() {
      PIN();
      if (nb < lim) { __builtin_amdgcn_s_barrier(); ++nb; } else __builtin_amdgcn_s_sleep(2);
      asm volatile("" ::: "memory");
      PIN();
    };
    auto tick = [&]() {
      asm volatile("s_waitcnt lgkmcnt(0)" ::: "memory");
      tick_nw();
    };
    auto fold16 = [&](f32x2 y0, f32x2 y1) __attribute__((always_inline)) {
      const auto q0 = __builtin_amdgcn_permlane32_swap(__float_as_uint(y0[0]), __float_as_uint(y1[0]), false, false);
      const auto q1 = __builtin_amdgcn_permlane32_swap(__float_as_uint(y0[1]), __float_as_uint(y1[1]), false, false);
      const auto q = __builtin_amdgcn_permlane16_swap(__float_as_uint(__uint_as_float(q0[0]) + __uint_as_float(q0[1])),
                                                      __float_as_uint(__uint_as_float(q1[0]) + __uint_as_float(q1[1])), false, false);
      return __uint_as_float(q[0]) + __uint_as_float(q[1]);
    };
    unsigned best, tag, rmd;
    float alpha, m, wsum;
    u32x4 tw;
    u32x2 xq;
    s16x8 sl[8];
    const int kcap = CAP0 + (tmi & 7);
    if (!LAY2) {
      ARGS_HERE;
      const int lane = fresh_lane(), t = hw * 64 + lane;
      const unsigned ep = ld_ag(gran);
      const int srow = m0 + tni * 8 + hw * 2;
      u32x4 xa[2][2], wv[8];
      const int wr_ = (3 - ((unsigned)(uintptr_t)W >> 7) - (tni & ~3)) & 7;
      const bool cpy = hw == 3 && wr_ < 4 && wr_ != (tni & 3), wld = hw < 3 ? wr_ != (tni & 3) : cpy;
      const int wcn = cpy ? (tni & ~3) + wr_ : tni, wcp = cpy ? (tni - wr_ - 1) & 3 : hw;
      const int wof = ((tmi * 64 + (lane >> 3)) * ldw + wcp * D_ + wcn * 64 + (lane & 7) * 8) * 2;
      auto ld_w = [&]() {
        const __amdgpu_buffer_rsrc_t rw = __builtin_amdgcn_make_buffer_rsrc(const_cast<u16*>(W), 0, wld ? D_ * ldw * 2 : 0, 0x00020000);
#pragma unroll
        for (int r = 0; r < 8; ++r) wv[r] = __builtin_amdgcn_raw_buffer_load_b128(rw, wof, r * 8 * ldw * 2, 0);
        PIN();
      };
      auto wcopy = [&]() __attribute__((always_inline)) {
        if (cpy) {
          const __amdgpu_buffer_rsrc_t rw = __builtin_amdgcn_make_buffer_rsrc(ap_->wc, 0, D_ * ldw * 2, 0x00020000);
#pragma unroll
          for (int r = 0; r < 8; ++r) __builtin_amdgcn_raw_buffer_store_b128(wv[r], rw, wof + r * 8 * ldw * 2, 0, 16);
        }
        PIN();
      };
      const int wat = ((hw == 3 ? SMAP : WMAP) >> (4 * (tmi & 7))) & 3;
      unsigned ps = 0u;
      auto stouch = [&](int at) __attribute__((always_inline)) {
        if (at == STCH && cpy) ps = __builtin_amdgcn_raw_buffer_load_b32(__builtin_amdgcn_make_buffer_rsrc(const_cast<u16*>(W), 0, D_ * ldw * 2, 0x00020000),
                                                                        (((tmi ^ 8) * 64 + lane) * ldw + wcp * D_ + wcn * 64) * 2, 0, 0);
        PIN();
      };
      auto ld_x = [&](int h) {
#pragma unroll
        for (int r = 0; r < 2; ++r) xa[r][h] = *reinterpret_cast<const u32x4*>(A + (size_t)(srow + r) * D_ + (h ^ (tmi >> 3)) * 512 + lane * 8);
        PIN();
      };
      ld_x(0);
      if (X2AT == 0) ld_x(1);
      if (wat == 0) ld_w();
      unsigned pc = 0u;
      if (hw == 3) {
        unsigned long long a0, a1;
        CODE_ADDR("layers_kernel", a0);
        CODE_ADDR("end_marker_kernel", a1);
        pc = touch_code(a0, a1, loc, lane);
      }
      if (hw == 2) st_ag(gran + GR_N + bid * 64 + lane, lane + 1u);
      {
        const int gi = bid * 256 + t;
        if (gi < GRAN_N / 4) __builtin_amdgcn_raw_buffer_store_b128(u32x4{SENT, SENT, SENT, SENT}, __builtin_amdgcn_make_buffer_rsrc(hand, 0, GRAN_N * 4, 0x00020000), gi * 16, 0, 16);
        if (t < 24) __hip_atomic_store(stats_out + (t >> 3) * S_ + m0 + tni * 8 + (t & 7), 0.0, __ATOMIC_RELAXED, __HIP_MEMORY_SCOPE_AGENT);
      }
      tick();
      if (wat == 1) ld_w();
      if (X2AT == 1) ld_x(1);
      tick();
      if (wat == 2) ld_w();
      if (WCAT == 2) wcopy();
      if (X2AT == 2) ld_x(1);
      asm volatile("" :: "v"(ep));
      tick();
      if (wat == 3) ld_w();
      if (WCAT == 3) wcopy();
      if (X2AT == 3) ld_x(1);
      stouch(3);
#pragma unroll
      for (int i = 0; i < HW; ++i) { tick(); stouch(4 + i); }
      __builtin_amdgcn_s_setprio(HP1);
      PIN();
      tag = ep * 1664525u + 1013904223u;
      const __amdgpu_buffer_rsrc_t rsc = __builtin_amdgcn_make_buffer_rsrc(gran, 0, GR_N * 4, 0x00020000);
      {
        u32x4 od;
#pragma unroll
        for (int q = 0; q < 4; ++q) od[q] = ((lane & 7) >> 1) == q ? ((lane & 1) ? 0x3f800000u : 0x00003f80u) : 0u;
        f32x4 d[2] = {{0.f, 0.f, 0.f, 0.f}, {0.f, 0.f, 0.f, 0.f}};
#pragma unroll
        for (int r = 0; r < 8; ++r) d[r & 1] = __builtin_amdgcn_mfma_f32_16x16x32_bf16(__builtin_bit_cast(s16x8, od), __builtin_bit_cast(s16x8, wv[r]), d[r & 1], 0, 0, 0);
        u32x4 gr;
#pragma unroll
        for (int q = 0; q < 4; ++q) gr[q] = ((__float_as_uint(dpp_add<0x128, 0xf>(fadd(d[0][q], d[1][q]))) + 0x80u) & ~0xffu) | ((tag >> (8 * q)) & 0xffu);
        asm volatile("s_waitcnt vmcnt(0)" ::: "memory");
        __builtin_amdgcn_raw_buffer_store_b128(gr, rsc, (lane & 40) || !wld ? GR_N * 4 : (GR_COL + (wcn * 16 + tmi) * 192 + wcp * 64 + (lane & 7) * 8 + (lane >> 4) * 4) * 4, 0, 16);
      }
      asm volatile("" :: "v"(pc));
      tick_nw();
      stouch(6);
      {
        float xv[2][16], mean[2];
#pragma unroll
        for (int r = 0; r < 2; ++r) {
          float sm = 0.f;
#pragma unroll
          for (int i = 0; i < 4; ++i) {
            xv[r][2 * i] = __uint_as_float(xa[r][0][i] << 16); xv[r][2 * i + 1] = __uint_as_float(xa[r][0][i] & 0xffff0000u);
            xv[r][8 + 2 * i] = __uint_as_float(xa[r][1][i] << 16); xv[r][8 + 2 * i + 1] = __uint_as_float(xa[r][1][i] & 0xffff0000u);
          }
#pragma unroll
          for (int i = 0; i < 16; ++i) sm += xv[r][i];
          mean[r] = wave_sum(sm) * (1.0f / 1024.0f);
          tick();
          stouch(7 + r);
        }
        float vr[2];
#pragma unroll
        for (int r = 0; r < 2; ++r) {
          float v = 0.f;
#pragma unroll
          for (int i = 0; i < 16; ++i) { const float d = xv[r][i] - mean[r]; v += d * d; }
          vr[r] = wave_sum(v) * (1.0f / 1024.0f);
          if (r == 0) tick();
        }
        const float vv = lane & 2 ? vr[1] : vr[0], mm = lane & 2 ? mean[1] : mean[0];
        const float rin = 1.0f / rbf(sqrtf(rbf(rbf(vv) + 1e-5f)));
        if (lane < 4) __builtin_amdgcn_raw_buffer_store_b64(u32x2{__float_as_uint((lane & 1) ? rbf(mm) : rin), tag}, rsc, (GR_ROW + srow * 4 + lane * 2) * 4, 0, 16);
      }
      tick();
      float cs;
      while (nb < POLLAT) tick();
      asm volatile("" :: "v"(ps));
#pragma unroll 1
      for (int spin = 0;; ++spin) {
        u32x4 csv[4];
        const __amdgpu_buffer_rsrc_t rsw = __builtin_amdgcn_make_buffer_rsrc(gran, 0, hw < 3 ? GR_N * 4 : 0, 0x00020000);
#pragma unroll
        for (int i = 0; i < 4; ++i) csv[i] = __builtin_amdgcn_raw_buffer_load_b128(rsw, (GR_COL + (tni * 16 + (lane >> 4)) * 192 + hw * 64 + (lane & 15) * 4) * 4, i * (4 * 192 * 4), 16);
        const u32x4 rp = __builtin_amdgcn_raw_buffer_load_b128(rsc, (GR_ROW + (m0 + (t & 127)) * 4) * 4, 0, 16);
        PIN();
        tick(); tick();
        unsigned bad = 0u;
        float z[4];
#pragma unroll
        for (int q = 0; q < 4; ++q) {
          const unsigned tq = tag >> (8 * q);
          bad |= ((csv[0][q] ^ tq) | (csv[1][q] ^ tq)) | ((csv[2][q] ^ tq) | (csv[3][q] ^ tq));
          z[q] = fadd(fadd(__uint_as_float(csv[0][q] & ~0xffu), __uint_as_float(csv[1][q] & ~0xffu)), fadd(__uint_as_float(csv[2][q] & ~0xffu), __uint_as_float(csv[3][q] & ~0xffu)));
        }
        cs = fold16(f32x2{z[0], z[1]}, f32x2{z[2], z[3]});
        bad = (hw < 3 ? bad & 0xffu : 0u) | (rp[1] ^ tag) | (rp[3] ^ tag);
        alpha = __uint_as_float(rp[0]); m = __uint_as_float(rp[2]);
        if (none(bad != 0u) || spin >= SPIN_MAX) break;
      }
      if (tmi == 0 && hw < 3) st_ag(reinterpret_cast<unsigned*>(csf) + hw * 1024 + n0 + (lane & 15) * 4 + (lane >> 4), __float_as_uint(cs));
      unsigned c0, c1, c2;
      split3(cs, c0, c1, c2);
      tw = u32x4{c0 | (c1 << 16), c0 | (c2 << 16), c1 | (c0 << 16), 0u};
      best = rmd = 0u;
      wsum = 0.f;
      asm volatile("" : "=v"(xq), KEPT("="));
#pragma unroll
      for (int i = 0; i < 8; ++i) asm volatile("" : "=v"(sl[i]));
    } else {
      asm volatile("; kept registers" : KEPT("="));
      PIN();
      ARGS_HERE;
      const int lane = fresh_lane(), t = hw * 64 + lane;
      best = rmd = 0u;
      tag = __builtin_amdgcn_readfirstlane(*LDS_AT(const volatile unsigned, lbase + FLG + 20));
#pragma unroll
      for (int i = 0; i < 8; ++i) asm volatile("" : "=v"(sl[i]));
      unsigned sad;
      auto tickc = [&]() __attribute__((always_inline)) {
        tick();
        if (nb == kcap + 1) {
#pragma unroll
          for (int i = 0; i < 8; ++i) {
            const unsigned a = (sad ^ ((i >> 2) * 64) ^ (((i >> 1) & 1) * 16)) + (i & 1) * 12288;
            sl[i] = __builtin_shufflevector(__builtin_amdgcn_ds_read_tr16_b64_v4i16(LDS_AT(s16x4, a)), __builtin_amdgcn_ds_read_tr16_b64_v4i16(LDS_AT(s16x4, a + 1536)), 0, 1, 2, 3, 4, 5, 6, 7);
          }
        }
        PIN();
      };
      static_assert(CAP0 + 8 <= KPOLL, "the stage is taken before that");
      if (hw == 1) STAMP(4, 4);
      if (hw == 0) {
        __builtin_amdgcn_s_sleep(TPRE);
        tile_at(3u);
        __builtin_amdgcn_s_sleep(XGATE);
        STAMP(4, 3);
#pragma unroll 1
        for (int spin = 0; spin < SPIN_MAX; ++spin) {
          if (none(ld_ag(gran + GR_X + (tmi * RX + tni % RX) * 128 + 64 + lane) != tag)) break;
          __builtin_amdgcn_s_sleep(1);
        }
      }
      if (hw == 0) STAMP(4, 5);
      asm volatile("" ::: "memory");
      PIN();
      __builtin_amdgcn_s_barrier();
      PIN();
      if (hw == 1) STAMP(4, 6);
      {
        const int ln = fresh_lane(), g4 = ln >> 4, tp = ln & 3, k = 8 * g4 + ((ln & 15) >> 2);
        sad = lbase + (kcap % NST) * STB + ABYTES + k * 384 + (((2 * (tp >> 1)) ^ (4 * ((k >> 1) & 1)) ^ (g4 & 1)) << 4) + 8 * (tp & 1) + (hw % 3) * 128;
      }
      wsum = __uint_as_float(__builtin_amdgcn_readfirstlane(*LDS_AT(const volatile unsigned, lbase + FLG + 60)));
      asm volatile("" : "+v"(sad), "=v"(tw), "=v"(alpha), "=v"(m));
      PIN();
      const int row = m0 + (t & 127);
      const __amdgpu_buffer_rsrc_t rsg = __builtin_amdgcn_make_buffer_rsrc(hand, 0, GRAN_N * 4, 0x00020000);
      if (hw < 2 || tmi == tni) {
      while (nb < HS) tickc();
#pragma unroll 1
      for (int spin = 0; spin < SPIN_MAX; ++spin) {
        const unsigned v = ld_ag(gran + GR_X + (tmi * RX + tni % RX) * 128 + lane);
        tickc();
        if (none(v != tag)) break;
      }
      asm volatile("" ::: "memory");
      PIN();
      const double s1 = ld_ag(stats + row), s2 = ld_ag(stats + S_ + row), sw = ld_ag(stats + 2 * S_ + row);
      tickc();
      float mean, var, mb, den;
      ln_from_sums(s1, s2, mean, var, mb, den);
      const float rin = frcp(den), g1 = 1.0f + rin;
      const float mb2 = rbf(mean + (mean - mb) * rin);
      const float den2 = rbf(__builtin_amdgcn_sqrtf(rbf(rbf(var * g1 * g1) + 1e-5f)));
      alpha = g1 * frcp(den2); m = (mb * rin + mb2) * frcp(g1);
      if (tmi == tni && hw >= 2) {
        const float r = (float)fma(-(double)mb, (double)wsum, sw) * rin;
        const unsigned rb = pack2(r, r);
        unsigned rkey = (rb ^ ((unsigned)((int)rb >> 31) | 0x80000000u)) & 0xffff0000u;
        if (r != r) rkey = 0u;
        rkey = wave_umax(rkey | (unsigned)(2047 - row));
        const unsigned md = __builtin_amdgcn_readlane(f2word(__uint_as_float((__float_as_uint(mb) >> 16) | (__float_as_uint(den) & 0xffff0000u))), (2047 - (int)(rkey & 0xffffu)) & 63);
        __builtin_amdgcn_raw_buffer_store_b64(u32x2{rkey, md}, rsg, lane < NREP ? lane * 256 + (tmi * 2 + hw - 2) * 8 : GRAN_N * 4, 0, 16);
      }
      }
      while (nb < KPOLL) tickc();
#pragma unroll 1
      for (int spin = 0; spin < SPIN_MAX; ++spin) {
        const u32x2 v = __builtin_amdgcn_raw_buffer_load_b64(rsg, (bid % NREP) * 256 + (lane & 31) * 8, 0, 16);
        tick(); tick();
        if (none(umax(v[0], v[1]) == SENT)) {
          best = wave_umax(v[0]);
          rmd = __builtin_amdgcn_readlane(v[1], ((2047 - (int)(best & 0xffffu)) >> 6) & 31);
          break;
        }
      }
      const int ix = (2047 - (int)(best & 0xffffu)) & 2047;
      if (row == ix) alpha = 0.f;
      xq = __builtin_amdgcn_raw_buffer_load_b64(__builtin_amdgcn_make_buffer_rsrc(const_cast<u16*>(A), 0, S_ * D_ * 2, 0x00020000), (ix * D_ + hw * 256 + (lane & 3) * 64 + (lane >> 2) * 4) * 2, 0, 16);
      PIN();
    }
    tag = __builtin_amdgcn_readfirstlane(tag);
    wsum = __uint_as_float(__builtin_amdgcn_readfirstlane(__float_as_uint(wsum)));
    const int lane = fresh_lane(), t = hw * 64 + lane;
    {
      unsigned a0, a1, a2;
      split3(-m, a0, a1, a2);
      u32x4 ta = {a0 | (a0 << 16), a1 | (a0 << 16), a1 | (a2 << 16), 0u};
      asm volatile("" : "+v"(tw), "+v"(ta), "+v"(alpha));
      __builtin_amdgcn_s_setprio(TPRIO);
      if (nb < lim) {
        while (nb < lim) tick();
        __builtin_amdgcn_s_sleep(TSLP);
      }
      if (!LAY2 && hw < 3) *LDS_AT(u32x4, lbase + TABW + (hw * 64 + (lane & 15) * 4 + (lane >> 4)) * 16) = tw;
      if (t < 128) {
        *LDS_AT(u32x4, lbase + TABA + t * 16) = ta;
        *LDS_AT(float, lbase + ALPHA + t * 4) = alpha;
      }
    }
    if (t >= 192 && t < 196) *LDS_AT(u32x4, lbase + CRED + ((t >> 1) & 1) * 512 + (t & 1) * 128) = u32x4{0u, 0u, 0u, 0u};
    asm volatile("s_waitcnt lgkmcnt(0)" ::: "memory");
    PIN();
    __builtin_amdgcn_s_barrier();
    PIN();
    *LDS_AT(u32x4, lbase + XML + t * 16) = u32x4{SENT, SENT, SENT, SENT};
    *LDS_AT(u32x4, lbase + SPM + (t & 127) * 16) = u32x4{SENT, SENT, SENT, SENT};
    if (hw == 0) {
      unsigned v;
      asm volatile("v_mov_b32 %0, 0\n\tv_writelane_b32 %0, 2, 0\n\tv_writelane_b32 %0, %1, 5\n\tv_writelane_b32 %0, %2, 7"
                   : "=&v"(v) : "s"(tag), "s"(best));
      lds_wr(lbase + FLG + lane * 4, v);
    }
    const int idx = LAY2 ? (2047 - (int)(best & 0xffffu)) & 2047 : -64;
    const bool p1blk = (oni & 7) != 7, fast = tmi < 8, own = fast && (loc & 1);
    const int fg = xcd * 28 + (p1blk ? loc - (loc >> 3) : 0), ow = (xcd & 3) * 16 + (loc >> 1);
    static_assert(OMX == 4, "oni % 8 = loc % 8");
#define GLD(a, b) "global_load_dwordx4 v[" #a ":" #b "], %0, off\n\tv_lshl_add_u64 %0, %0, 0, %1\n\t"
#define GLD4(...) asm volatile(__VA_ARGS__ : "+v"(p) : "s"((unsigned long long)(F_ * 2)), KEPT() : "memory")
    auto gptr = [&](const u16* wg, const u16* wu) {
      const int ln = fresh_lane();
      return ((ln & 2) ? wu : wg) + (size_t)(hw * 256 + (ln >> 2) * 4) * F_ + fg * 16 + (ln & 1) * 8;
    };
    auto gather = [&](const u16*& p, int e) __attribute__((always_inline)) {
      if (e == 0) GLD4(GLD(104, 107) GLD(108, 111) GLD(112, 115) GLD(116, 119));
      else if (e == 1) GLD4(GLD(120, 123) GLD(124, 127) GLD(128, 131) GLD(132, 135));
      else if (e == 2) GLD4(GLD(136, 139) GLD(140, 143) GLD(144, 147) GLD(148, 151));
      else GLD4(GLD(152, 155) GLD(156, 159) GLD(160, 163) GLD(164, 167));
      p += 60 * F_;
    };
    auto dmask = [&](int q) { const int ln = fresh_lane(); return ((ln >> 1) & 1) == q ? ((ln & 1) ? 0xffff0000u : 0x0000ffffu) : 0u; };
    unsigned dm0, dm1, dad;
    auto gu_setup = [&]() __attribute__((always_inline)) {
      const int ln = fresh_lane();
      dm0 = dmask(0); dm1 = dmask(1);
      dad = lbase + SACT + ((ln & 3) * 8 + (ln >> 5) * 4 + ((ln >> 4) & 1)) * 16 + hw * 4;
      asm volatile("" : "+v"(dm0), "+v"(dm1), "+v"(dad));
    };
    auto gate_up = [&](u32x2 x, unsigned md) __attribute__((always_inline)) {
      const unsigned rin = __builtin_amdgcn_readfirstlane(__float_as_uint(frcp(__uint_as_float(md & 0xffff0000u))));
      asm volatile("s_getpc_b64 s[90:91]\n\ts_add_u32 s90, s90, gate_up_code@rel32@lo+4\n\ts_addc_u32 s91, s91, gate_up_code@rel32@hi+12\n\ts_swappc_b64 s[92:93], s[90:91]"
                   : "+{v[100:101]}"(x) : "{s84}"(md << 16), "{s85}"(md & 0xffff0000u), "{s86}"(rin), "{v99}"(dad), "{v102}"(dm0), "{v103}"(dm1), KEPT()
                   : "v84", "v85", "v86", "v87", "v88", "v89", "v90", "v91", "v92", "v93", "v94", "v95", "v96", "v97", "s88", "s89", "s90", "s91", "s92", "s93", "scc", "memory");
    };
    auto act_diag = [&](int so = 0) __attribute__((always_inline)) {
      const int ln = fresh_lane();
      const f32x4 g4 = *LDS_AT(const volatile f32x4, lbase + SACT + so + (ln >> 2) * 16), u4 = *LDS_AT(const volatile f32x4, lbase + SACT + so + 256 + (ln >> 2) * 16);
      float gv = fadd(fadd(g4[0], g4[1]), fadd(g4[2], g4[3])), uv = fadd(fadd(u4[0], u4[1]), fadd(u4[2], u4[3]));
      rbf2(gv, uv);
      const float av = rbf(gv * frcp(1.0f + __expf(-gv))) * uv;
      const unsigned ap2 = pack2(av, av);
      return __builtin_bit_cast(s16x4, u32x2{ap2 & dm0, ap2 & dm1});
    };
    auto down = [&](const s16x4& a, const u32x2 (&w)[16]) __attribute__((always_inline)) {
      f32x4 d[2] = {{0.f, 0.f, 0.f, 0.f}, {0.f, 0.f, 0.f, 0.f}};
      static_for<0, 16>([&](auto fc) __attribute__((always_inline)) {
        constexpr int f = decltype(fc)::value;
        d[f & 1] = __builtin_amdgcn_mfma_f32_4x4x4bf16_1k(a, __builtin_bit_cast(s16x4, w[f]), d[f & 1], 4, f, 0);
      });
      f32x4 z;
#pragma unroll
      for (int e = 0; e < 4; ++e) { z[e] = d[0][e] + d[1][e]; asm volatile("" : "+v"(z[e])); }
      return u32x4{f2word(z[0]), f2word(z[1]), f2word(z[2]), f2word(z[3])};
    };
    auto fold8 = [&](f32x2 y0, f32x2 y1) __attribute__((always_inline)) { return dpp_add<0x128, 0xf>(fold16(y0, y1)); };
    auto fold4 = [&](f32x2 y0, f32x2 y1) __attribute__((always_inline)) { return dpp_add<0x124, 0xf>(fold8(y0, y1)); };
    auto y_sum = [&](const __amdgpu_buffer_rsrc_t rs, auto nbc) __attribute__((always_inline)) {
      constexpr int NB = decltype(nbc)::value, N = 14 / NB;
      const int ln = fresh_lane(), src = (GRAN_N + (ln >> 2) * D_ + ow * 16 + (ln & 3) * 4) * 4;
      f32x2 y0 = {0.f, 0.f}, y1 = {0.f, 0.f};
#pragma unroll 1
      for (int bt = 0; bt < NB; ++bt) {
        f32x2 z0, z1;
#pragma unroll 1
        for (int spin = 0; spin < SPIN_MAX; ++spin) {
          u32x4 v[N];
#pragma unroll
          for (int i = 0; i < N; ++i) v[i] = __builtin_amdgcn_raw_buffer_load_b128(rs, src, (bt * N + i) * (16 * D_ * 4), 16);
          z0 = y0; z1 = y1;
#pragma unroll
          for (int i = 0; i < N; ++i) {
            z0 = f32x2{fadd(z0[0], __uint_as_float(v[i][0])), fadd(z0[1], __uint_as_float(v[i][1]))};
            z1 = f32x2{fadd(z1[0], __uint_as_float(v[i][2])), fadd(z1[1], __uint_as_float(v[i][3]))};
          }
          const float chk = fadd(fadd(z0[0], z0[1]), fadd(z1[0], z1[1]));
          if (none(chk != chk)) break;
          unsigned mx = 0u;
#pragma unroll
          for (int i = 0; i < N; ++i) mx = umax(umax(umax(mx, v[i][0]), v[i][1]), umax(v[i][2], v[i][3]));
          if (none(mx == SENT)) break;
          __builtin_amdgcn_s_sleep(YSLP);
          asm volatile("" ::: "memory");
        }
        y0 = z0; y1 = z1;
      }
      return fold4(y0, y1);
    };
    [&]() __attribute__((always_inline)) {
    ARGS_HERE;
    const unsigned l16 = lane * 16;
    unsigned* kvflag = gran + GR_KV + ((LAY2 ? 16 : 0) + tni) * (32 * RK);
    auto wait_sig = [&](int sh) {
#pragma unroll 1
      for (int i = 0; i < 4 * SPIN_MAX; ++i) {
        if (((__builtin_amdgcn_readfirstlane(*LDS_AT(const volatile unsigned, lbase + FLG + 32)) >> sh) & 7u) >= 4u) break;
        __builtin_amdgcn_s_sleep(SSLP);
      }
      asm volatile("" ::: "memory");
      PIN();
    };
    PIN();
    asm volatile("s_waitcnt lgkmcnt(0)" ::: "memory");
    if (!LAY2) asm volatile("s_waitcnt vmcnt(0)" ::: "memory");
    __builtin_amdgcn_s_barrier();
    PIN();
    __builtin_amdgcn_s_setprio(HPRIO);
    if (hw < 3) {
      __builtin_amdgcn_s_sleep(SPRE);
      wait_sig(0);
      __builtin_amdgcn_raw_buffer_store_b32(tag, __builtin_amdgcn_make_buffer_rsrc(kvflag, 0, hw == 0 ? RK * 128 : 0, 0x00020000), lane * 128 + tmi * 4, 0, 16);
      if (hw == 0) STAMP(LAY2 ? 5 : 1, 16);
    }
    if (hw == 3) {
      __builtin_amdgcn_s_sleep(KVS);
      const unsigned kv0 = ld_ag(kvflag + (tmi % RK) * 32 + (lane & 15));
      PIN();
      const char* cnt = reinterpret_cast<const char*>(gran + GR_N + bid * 64 + 2);
      unsigned vk[8], vp = lbase + FLG + 8 + (lane & 7), pw = 0u;
#pragma unroll
      for (int j = 0; j < 8; ++j) { vk[j] = l16 + (j >> 2) * 131072u + ((j >> 1) & 1) * 4096u + (j & 1) * (VOFF - 2048u); asm volatile("" : "+v"(vk[j])); }
#define G4(a, b, c, d, sc) "s_nop 0\n\tglobal_load_lds_dwordx4 %" #a ", %8" sc "\n\tglobal_load_lds_dwordx4 %" #b ", %8 offset:1024" sc "\n\t"             \
                       "global_load_lds_dwordx4 %" #c ", %8 offset:2048" sc "\n\tglobal_load_lds_dwordx4 %" #d ", %8 offset:3072" sc "\n\t"
#define M4 "s_addk_i32 m0, 0x1000\n\t"
      auto stage = [&](const char* sp, unsigned dst) __attribute__((always_inline)) {
        asm volatile("s_mov_b32 m0, %9\n\t" G4(0, 0, 1, 1, " sc1") M4 G4(2, 2, 3, 3, " sc1") M4 G4(4, 4, 5, 5, " sc1") M4 G4(6, 6, 7, 7, " sc1")
                     :: "v"(vk[0]), "v"(vk[1]), "v"(vk[2]), "v"(vk[3]), "v"(vk[4]), "v"(vk[5]), "v"(vk[6]), "v"(vk[7]), "s"(sp), "s"(dst) : "m0", "scc", "memory");
      };
      auto count = [&]() __attribute__((always_inline)) {
        asm volatile("s_mov_b32 m0, %2\n\ts_nop 0\n\tglobal_load_lds_dword %0, %1 sc1" :: "v"(0u), "s"(cnt), "s"(lbase + FLG - 252) : "m0", "memory");
        cnt += 8;
      };
      auto freed = [&](unsigned n) __attribute__((always_inline)) {
        if (!none(pw < n)) {
#pragma unroll 1
          for (int i = 0; i < 4 * SPIN_MAX; ++i) {
            pw = *LDS_AT(const volatile unsigned char, vp);
            if (none(pw < n)) break;
            __builtin_amdgcn_s_sleep(W3SLP);
          }
        }
        asm volatile("" ::: "memory");
      };
      unsigned long long ord = (0xfedcba9876543210ull >> 4 & ~((1ull << 4 * (tmi & 7)) - 1ull)) | (0xfedcba9876543210ull & ((1ull << 4 * (tmi & 7)) - 1ull));
      ord ^= (tmi & 8) * 0x1111111111111111ull;
      unsigned seen;
      const char* kvb = reinterpret_cast<const char*>(KV) + (size_t)tni * (128 * 2048);
      auto panel = [&]() __attribute__((always_inline)) {
        const unsigned pn = (unsigned)ord & 15u;
        ord >>= 4;
        if (__builtin_expect(!((seen >> pn) & 1u), 0)) {
#pragma unroll 1
          for (int i = 0; i < SPIN_MAX && !((seen >> pn) & 1u); ++i) seen = (unsigned)__ballot(ld_ag(kvflag + (tmi % RK) * 32 + (lane & 15)) == tag);
        }
        asm volatile("" ::: "memory");
        return kvb + pn * 8192u;
      };
      asm volatile("" : "+s"(ord), "+s"(kvb), "+s"(cnt));
      asm volatile("" : "+v"(vp), "+v"(pw));
      PIN();
      seen = (unsigned)__ballot(kv0 == tag);
      const char* sp = panel();
      wait_sig(16);
      stage(sp, lbase + 2 * ASB);
      count();
      const int ksp = LAY2 ? PNS / 2 : -1, ksg = LAY2 ? 64 : GF0;
      const u16* gp = gptr(wg, wu);
#pragma unroll 1
      for (int k = 1; k < 15; ++k) {
        if (__builtin_expect(k == ksp, 0)) {
          gu_setup();
          gate_up(xq, rmd);
          PIN();
          *LDS_AT(volatile unsigned char, lbase + FLG + 7) = (unsigned char)0x7f;
        }
        freed((unsigned)(0xba9876543211000ull >> 4 * k) & 15u);
        stage(sp + 2048, lbase + ((2 * k + 1) & 7) * ASB);
        sp = panel();
        stage(sp, lbase + ((2 * k + 2) & 7) * ASB);
        count();
        if (__builtin_expect((unsigned)(k - ksg) < 4u, 0)) gather(gp, k - ksg);
      }
      freed(12u);
      glds16s<true>(csf + 4096 + tni * 128, l16, lbase + UGL);
      stage(sp + 2048, lbase + 7 * ASB);
      count();
      const unsigned vwo = (unsigned)((lane >> 3) * D_ + oni * 64 + ((lane & 7) ^ ((lane >> 3) & 6)) * 8) * 2u;
      constexpr unsigned vs = 8u * D_ * 2u - 1024u;
#pragma unroll 1
      for (int ns = 0; ns < 6; ++ns) {
        freed((unsigned)(ns + 25) >> 1);
        const char* wp = reinterpret_cast<const char*>(wo) + (size_t)ns * (128 * D_ * 2);
#pragma unroll
        for (int j = 0; j < 4; ++j)
          glds16x4<false>(wp + j * (32u * D_ * 2u), vwo, vwo + vs, vwo + 2 * vs, vwo + 3 * vs, lbase + ns * ASB + j * 4096);
      }
      asm volatile("s_waitcnt vmcnt(0)" ::: "memory");
      return;
    }
    if (!LAY2) {
      auto until = [&](unsigned n) {
        while ((__builtin_amdgcn_readfirstlane(*LDS_AT(const volatile unsigned, lbase + FLG + 8)) & 255u) < n) __builtin_amdgcn_s_sleep(16);
        asm volatile("" ::: "memory");
        PIN();
      };
      if (hw != 1) {
        constexpr int N2 = HAND_N + Y_N + 3 * D_ / 2;
        const __amdgpu_buffer_rsrc_t rsi = __builtin_amdgcn_make_buffer_rsrc(hand + (hw ? GRAN_N : 2 * HAND_N), 0, (hw ? N2 : PS_N) * 4, 0x00020000);
        until(IAT);
#pragma unroll 1
        for (int i = 0; i < ((hw ? N2 : PS_N) / 4 + 16383) / 16384; ++i) {
          __builtin_amdgcn_raw_buffer_store_b128(u32x4{SENT, SENT, SENT, SENT}, rsi, ((i * 256 + bid) * 64 + lane) * 16, 0, 16);
          __builtin_amdgcn_s_sleep(ISLP);
        }
      }
      const int ng = loc & 15, fc = xcd * 2 + (loc >> 4);
      const size_t og = (size_t)((fg - xcd * 28) / 7 * 256 + lane) * F_ + (xcd * 7 + fg % 7) * 64;
      const u16* pd = wd + (size_t)(fc * 224 + (lane < 56 ? lane : 0)) * D_ + ng * 64;
      const u16* p = gptr(wg, wu);
#pragma unroll 1
      for (unsigned n = T0; n < T0 + 13u; ++n) {
        until(n);
        if (hw == 1) {
          const unsigned i = n - T0;
          const u16* q = i < 8u ? ((i & 1u) ? wu : wg) + og + (size_t)(i >> 1) * (64 * F_) : pd + (size_t)((i - 8u - (i > TW)) & 3u) * (56 * D_);
          if (i == TW) {
            q = wo + (size_t)(768 + (loc / (32 / OMX)) * (256 / OMX) + (lane & (256 / OMX - 1) & 63)) * D_ + oni * 64;
            unsigned long long a0, a1;
            CODE_ADDR("layers_kernel", a0); CODE_ADDR("end_marker_kernel", a1);
            if (loc < 8) { const unsigned p1 = touch_code(a0, a1, 3 + loc, lane); asm volatile("" :: "v"(p1)); }
          }
          const unsigned p0 = touch(q);
          asm volatile("" :: "v"(p0));
        }
        if (n >= G0 && (n - G0) % GS == 0u && (n - G0) / GS < 4u) gather(p, (n - G0) / GS);
        if (n == UAT && hw == UHW) {
          u32x4 a[4][2], c[2];
#pragma unroll
          for (int i = 0; i < 4; ++i)
#pragma unroll
            for (int h = 0; h < 2; ++h) a[i][h] = *reinterpret_cast<const u32x4*>(wo + (size_t)(tni * 64 + tmi * 4 + i) * D_ + h * 512 + lane * 8);
#pragma unroll
          for (int h = 0; h < 2; ++h) c[h] = *reinterpret_cast<const u32x4*>(w_r + h * 512 + lane * 8);
          auto dot = [&](unsigned x, unsigned y, float z) { return __builtin_amdgcn_fdot2_f32_bf16(__builtin_bit_cast(bf16x2, x), __builtin_bit_cast(bf16x2, y), z, false); };
          PIN();
          __builtin_amdgcn_s_setprio(0);
#pragma unroll 1
          for (int i = 0; i < SPIN_MAX; ++i) {
            if (__builtin_amdgcn_readfirstlane(*LDS_AT(const volatile unsigned, lbase + FLG + 16)) >= UARR) break;
            __builtin_amdgcn_s_sleep(4);
          }
          PIN();
          float wm = 0.f;
#pragma unroll
          for (int e = 0; e < 8; ++e) wm = dot(c[e >> 2][e & 3], 0x3f803f80u, wm);
          wm = wave_sum(wm);
          lds_wr(lbase + FLG + 60, __float_as_uint(wm));
          wm *= -1.0f / 1024.0f;
          f32x2 u[2], g[2];
#pragma unroll
          for (int i = 0; i < 4; ++i) {
            float p1 = 0.f, pw = 0.f, p2 = 0.f;
#pragma unroll
            for (int e = 0; e < 8; ++e) {
              const unsigned x = a[i][e >> 2][e & 3];
              p1 = dot(x, 0x3f803f80u, p1); pw = dot(x, c[e >> 2][e & 3], pw); p2 = dot(x, x, p2);
            }
            u[i >> 1][i & 1] = fmaf(p1, wm, pw); g[i >> 1][i & 1] = p2;
          }
          float us = fold4(u[0], u[1]), gs = fold4(g[0], g[1]);
          us = dpp_add<0x121, 0xf>(dpp_add<0x122, 0xf>(us)); gs = dpp_add<0x121, 0xf>(dpp_add<0x122, 0xf>(gs));
          __builtin_amdgcn_raw_buffer_store_b32(__float_as_uint((lane & 1) ? __builtin_amdgcn_sqrtf(gs) * (1.0f / 32.0f) : us),
              __builtin_amdgcn_make_buffer_rsrc(csf + 4096 + tni * 128, 0, 512, 0x00020000), (lane & 14) ? 512 : ((lane & 1) * 64 + tmi * 4 + (lane >> 4)) * 4, 0, 16);
        }
      }
      if (hw == 2 && bid == 0) {
        const __amdgpu_buffer_rsrc_t rsc = __builtin_amdgcn_make_buffer_rsrc(gran, 0, GR_N * 4, 0x00020000);
#pragma unroll 1
        for (int spin = 0; spin < SPIN_MAX; ++spin) {
          bool ok = true;
#pragma unroll
          for (int i = 0; i < 4; ++i) ok &= __builtin_amdgcn_raw_buffer_load_b32(rsc, (GR_ROW + (i * 64 + lane) * 32 + 1) * 4, 0, 16) == tag;
          if (__all(ok)) break;
          __builtin_amdgcn_s_sleep(8);
          asm volatile("" ::: "memory");
        }
        if (lane == 0) st_ag(gran, tag);
      }
      return;
    }
    unsigned* X2 = hand + GRAN_N + Y_N;
    unsigned* XS = X2 + X2_N;
    unsigned* P4 = XS + XS_N;
    unsigned* PA = P4 + P4_N;
    const __amdgpu_buffer_rsrc_t rsh = __builtin_amdgcn_make_buffer_rsrc(hand, 0, HAND_N * 4, 0x00020000);
    auto signal = [&](unsigned ph) {
      asm volatile("" ::: "memory");
      PIN();
      *LDS_AT(volatile unsigned char, lbase + FLG + 4 + hw) = (unsigned char)ph;
    };
    auto wait_all = [&](unsigned ph) {
#pragma unroll 1
      for (int i = 0; i < SPIN_MAX; ++i) {
        if (all_ge(__builtin_amdgcn_readfirstlane(*LDS_AT(const volatile unsigned, lbase + FLG + 4)), ph)) break;
        __builtin_amdgcn_s_sleep(WSLP);
      }
      asm volatile("" ::: "memory");
      PIN();
    };
    gu_setup();
    gate_up(xq, rmd);
    signal(hw == 2 ? 2u : 0x7fu);
    if (wid == 8) STAMP(dk, 2);
    if (hw < 2) {
#pragma unroll 1
      for (int i = 0; i < SPIN_MAX; ++i) {
        if (__builtin_amdgcn_readfirstlane(*LDS_AT(const volatile unsigned, lbase + FLG)) >= FSTART) break;
        __builtin_amdgcn_s_sleep(WSLP);
      }
      asm volatile("" ::: "memory");
      PIN();
      if (wid == 8) STAMP(dk, 10);
      u32x2 w[16];
      const __amdgpu_buffer_rsrc_t rd_ = __builtin_amdgcn_make_buffer_rsrc(const_cast<u16*>(wd), 0, D_ * F_ * 2, 0x00020000);
      auto ldw = [&](int c) __attribute__((always_inline)) {
#pragma unroll
        for (int f = 0; f < 16; ++f) w[f] = __builtin_amdgcn_raw_buffer_load_b64(rd_, lane * 8 + c * 512 + (f & 1) * 2048, (fg * 16 + (f & ~1)) * (D_ * 2) + hw * 1024, 0);
        PIN();
      };
      ldw(0);
      const unsigned pt = __builtin_amdgcn_raw_buffer_load_b32(rd_, (lane >> 2) * (D_ * 2) + (lane & 3) * 128 + 512, fg * 16 * (D_ * 2) + hw * 1024, 0);
      int yo = p1blk ? (GRAN_N + fg * D_ + hw * 512) * 4 + lane * 16 : HAND_N * 4;
      asm volatile("" : "+v"(yo));
      wait_all(2u);
      if (wid == 8) STAMP(dk, 3);
      const s16x4 a = act_diag();
      const u32x4 y0 = down(a, w);
      PIN();
      ldw(1);
      __builtin_amdgcn_raw_buffer_store_b128(y0, rsh, yo, 0, 16);
      PIN();
      __builtin_amdgcn_raw_buffer_store_b128(down(a, w), rsh, yo + 1024, 0, 16);
      asm volatile("" :: "v"(pt));
      if (wid == 8) STAMP(dk, 4);
    }
    PIN();
    [&]() __attribute__((always_inline)) {
    const int lane = fresh_lane();
    const float rmb = __uint_as_float(rmd << 16);
    auto first_words = [&](int src, auto nc) __attribute__((always_inline)) {
#pragma unroll 1
      for (int spin = 0; spin < SPIN_MAX; ++spin) {
        unsigned mx = 0u;
#pragma unroll
        for (int i = 0; i < decltype(nc)::value; ++i) mx = umax(mx, __builtin_amdgcn_raw_buffer_load_b32(rsh, src, i * (56 * D_ * 4), 16));
        if (none(mx == SENT)) break;
        __builtin_amdgcn_s_sleep(PSLP);
        asm volatile("" ::: "memory");
      }
    };
    if (hw == 2) {
      wait_all(2u);
      if (own) {
        const float rden = __uint_as_float(rmd & 0xffff0000u), rrin = frcp(rden);
        const int col = ow * 16 + (lane & 3) * 4 + (lane >> 4);
        const unsigned xo2 = ld_ag(reinterpret_cast<const unsigned*>(A + (size_t)idx * D_ + (col & ~1)));
        __builtin_amdgcn_s_sleep(PRE1);
        first_words((GRAN_N + (lane < 56 ? lane : 0) * D_ + ow * 16) * 4, std::integral_constant<int, 4>{});
        const float y = y_sum(rsh, std::integral_constant<int, 2>{});
        const unsigned rk = best >> 16;
        const float rv = __uint_as_float(((rk & 0x8000u) ? (rk & 0x7fffu) : (~rk & 0xffffu)) << 16);
        const float rs = rbf(frcp(1.0f + __expf(-rv)));
        const float x1v = __uint_as_float((col & 1) ? xo2 & 0xffff0000u : xo2 << 16);
        float h0 = x1v, h1 = x1v;
        ln_apply2(h0, h1, rmb, rden, rrin);
        const float x2v = rbf(x1v + rbf(h0 + rbf(rbf(y) * rs)));
        const auto pr = __builtin_amdgcn_permlane16_swap(__float_as_uint(x2v), __float_as_uint(x2v), false, false);
        __builtin_amdgcn_raw_buffer_store_b32(f2word(__uint_as_float((pr[0] >> 16) | pr[1])), rsh, (lane & 28) ? HAND_N * 4 : (int)(X2 - hand) * 4 + col * 2, 0, 16);
        const float d = x2v - rmb;
        const float sd = wave_sum(d), sq = wave_sum(d * d);
        if (lane < 2) st_ag(XS + ow * 2 + lane, f2word(lane ? sq : sd));
        STAMP(dk, 5);
      }
    }
    {
      const int ks = kcap ^ (tmi & 8), g4 = lane >> 4;
      const int src = (int)(X2 - hand) * 4 + ks * 128 + g4 * 16;
      int dst = (lane & 12) ? HAND_N * 4 : (int)((P4 - hand) + (tni * 16 + ks) * 192 + hw * 64 + (lane & 2) * 16 + (lane & 1) * 8 + (g4 >> 1) * 16 + (g4 & 1) * 4) * 4;
      asm volatile("" : "+v"(dst));
      u32x4 x0, x1;
      if (!(hw == 2 && own)) __builtin_amdgcn_s_sleep(PRE4);
#pragma unroll 1
      for (int spin = 0; spin < SPIN_MAX; ++spin) {
        x0 = __builtin_amdgcn_raw_buffer_load_b128(rsh, src, 0, 16); x1 = __builtin_amdgcn_raw_buffer_load_b128(rsh, src + 64, 0, 16);
        if (none(umax(umax(umax(x0[0], x0[1]), umax(x0[2], x0[3])), umax(umax(x1[0], x1[1]), umax(x1[2], x1[3]))) == SENT)) break;
        __builtin_amdgcn_s_sleep(PSLP);
        asm volatile("" ::: "memory");
      }
      f32x4 d[4];
#pragma unroll
      for (int i = 0; i < 4; ++i) d[i] = MFMA16(sl[2 * i], __builtin_bit_cast(s16x8, x0), (f32x4{0.f, 0.f, 0.f, 0.f}));
#pragma unroll
      for (int i = 0; i < 4; ++i) d[i] = MFMA16(sl[2 * i + 1], __builtin_bit_cast(s16x8, x1), d[i]);
      u32x4 o;
#pragma unroll
      for (int e = 0; e < 4; ++e) o[e] = f2word((lane & 2) ? ((lane & 1) ? d[3][e] : d[2][e]) : ((lane & 1) ? d[1][e] : d[0][e]));
      __builtin_amdgcn_raw_buffer_store_b128(o, rsh, dst, 0, 16);
      PIN();
      if (wid == 8) STAMP(dk, 6);
    }
    if (hw == 2) {
      const int col = (lane & 15) * 4 + (lane >> 4), src = (int)((P4 - hand) + (tni * 16 + (lane >> 4)) * 192 + (lane & 15) * 4) * 4;
      const __amdgpu_buffer_rsrc_t rsq = __builtin_amdgcn_make_buffer_rsrc(hand, 0, fast ? HAND_N * 4 : 0, 0x00020000);
      float cs[3], val[3];
#pragma unroll
      for (int p = 0; p < 3; ++p) cs[p] = __uint_as_float(ld_ag(reinterpret_cast<const unsigned*>(csf) + p * 1024 + n0 + col));
      u32x2 xs;
      __builtin_amdgcn_s_sleep(PRE7);
      first_words((int)((P4 - hand) + (tni * 16 + (lane & 15)) * 192 + ((lane >> 4) % 3) * 64) * 4, std::integral_constant<int, 1>{});
#pragma unroll 1
      for (int spin = 0; spin < SPIN_MAX; ++spin) {
        u32x4 v[3][4];
        xs = __builtin_amdgcn_raw_buffer_load_b64(rsh, (int)((XS - hand) + lane * 2) * 4, 0, 16);
#pragma unroll
        for (int p = 2; p >= 0; --p)
#pragma unroll
          for (int i = 0; i < 4; ++i) v[p][i] = __builtin_amdgcn_raw_buffer_load_b128(p ? rsh : rsq, src + p * 256, i * (4 * 192 * 4), 16);
        auto part = [&](int p) __attribute__((always_inline)) {
          float z[4];
#pragma unroll
          for (int e = 0; e < 4; ++e) {
            z[e] = fadd(fadd(__uint_as_float(v[p][0][e]), __uint_as_float(v[p][1][e])), fadd(__uint_as_float(v[p][2][e]), __uint_as_float(v[p][3][e])));
          }
          val[p] = fold16(f32x2{z[0], z[1]}, f32x2{z[2], z[3]});
        };
        part(2); part(1);
        val[0] = 0.f;
        if (fast) part(0);
        const float chk = fadd(fadd(fadd(val[0], val[1]), fadd(val[2], __uint_as_float(xs[0]))), __uint_as_float(xs[1]));
        if (none(chk != chk)) break;
        unsigned mx = umax(xs[0], xs[1]);
#pragma unroll
        for (int i = 0; i < 12; ++i) mx = umax(umax(umax(mx, v[i >> 2][i & 3][0]), v[i >> 2][i & 3][1]), umax(v[i >> 2][i & 3][2], v[i >> 2][i & 3][3]));
        if (none(mx == SENT)) break;
        __builtin_amdgcn_s_sleep(PSLP);
        asm volatile("" ::: "memory");
      }
      const auto sw = __builtin_amdgcn_permlane32_swap(xs[0], xs[1], false, false);
      float sm = __uint_as_float(sw[0]) + __uint_as_float(sw[1]);
      sm = dpp_add<0x142, 0xa>(dpp_add<0x118, 0xf>(dpp_add<0x114, 0xf>(dpp_add<0x112, 0xf>(dpp_add<0x111, 0xf>(sm)))));
      const float dm = __int_as_float(__builtin_amdgcn_readlane(__float_as_int(sm), 31)) * (0.25f / 1024.0f);
      const float var = fmaxf(__int_as_float(__builtin_amdgcn_readlane(__float_as_int(sm), 63)) * (0.25f / 1024.0f) - dm * dm, 0.f);
      const float mb = rbf(rmb + dm);
      const float rin = frcp(rbf(__builtin_amdgcn_sqrtf(rbf(rbf(var) + 1e-5f))));
      const unsigned kv = pack2((val[1] - mb * cs[1]) * rin, (val[2] - mb * cs[2]) * rin);
      *LDS_AT(u16, lbase + TAB + 128 + kpos(col) * 2) = (u16)kv;
      *LDS_AT(u16, lbase + TAB + 256 + col * 2) = (u16)(kv >> 16);
      if (fast) *LDS_AT(u16, lbase + TAB + kpos(col) * 2) = f2bf((val[0] - mb * cs[0]) * rin * QSCALE);
      signal(7u);
      STAMP(dk, 7);
      if (own) {
#pragma unroll
        for (int i = 0; i < 14; ++i)
          __builtin_amdgcn_raw_buffer_store_b128(u32x4{SENT, SENT, SENT, SENT}, rsh, (GRAN_N + (i * 16 + (lane >> 2)) * D_ + ow * 16 + (lane & 3) * 4) * 4, 0, 16);
      }
      return;
    }
    if (!fast) return;
    const int head = tni * 2 + hw;
    const unsigned tb = lbase + TAB + hw * 64;
    {
      const __amdgpu_buffer_rsrc_t rs = __builtin_amdgcn_make_buffer_rsrc(KV, 0, 2 * S_ * D_ * 2, 0x00020000);
      const int src = (head * 64 + tmi * 4) * 2048 + lane * 16;
      auto ld = [&](int i, unsigned off) { return __builtin_bit_cast(s16x8, __builtin_amdgcn_raw_buffer_load_b128(rs, src + (i & 3) * 1024, (i >> 3) * 65536 + ((i >> 2) & 1) * 4096 + off, 16)); };
#pragma unroll 1
      for (int i = 0; i < SPIN_MAX; ++i)
        if (__builtin_amdgcn_readfirstlane(ld_ag(kvflag + (tmi % RK) * 32 + tmi + 8)) == tag) break;
      asm volatile("" ::: "memory");
      s16x8 f[16];
#pragma unroll
      for (int i = 0; i < 16; ++i) f[i] = ld(i, 0u);
      PIN();
      const bool on = ((lane ^ (lane >> 4)) & 1) == 0;
      const unsigned pl = lbase + RED + hw * 1024;
      unsigned pw = pl + (lane >> 4) * 64 + (lane & 15) * 2, pr = pl + (on ? (lane >> 5) * 16 : 512), qr = on ? tb + (lane >> 5) * 16 : pl + 512;
      const int gone = (((idx >> 7) & 7) == tmi) ? ((idx >> 10) * 2 + (kpos(idx & 31) >> 4)) * 64 + ((idx >> 5) & 3) * 16 + (kpos(idx & 31) & 15) : -1;
      asm volatile("" : "+v"(pw), "+v"(pr), "+v"(qr));
      *LDS_AT(u32x2, pl + 512 + lane * 8) = u32x2{0u, 0u};
      wait_all(7u);
      u32x4 q[2];
#pragma unroll
      for (int sx = 0; sx < 2; ++sx) {
        q[sx] = *LDS_AT(const u32x4, qr + sx * 32);
      }
      float sc[4];
#pragma unroll
      for (int i = 0; i < 8; ++i) {
        f32x4 d = MFMA16(__builtin_bit_cast(s16x8, q[0]), f[2 * i], (f32x4{0.f, 0.f, 0.f, 0.f}));
        d = MFMA16(__builtin_bit_cast(s16x8, q[1]), f[2 * i + 1], d);
        PIN();
        f[2 * i] = ld(2 * i, VOFF); f[2 * i + 1] = ld(2 * i + 1, VOFF);
        PIN();
#pragma unroll
        for (int r = 0; r < 2; ++r) sc[(i >> 2) * 2 + r] = (i & 3) == 0 || (lane >> 4) == (i & 3) ? d[r] : sc[(i >> 2) * 2 + r];
      }
      if (gone >= 0) {
#pragma unroll
        for (int r = 0; r < 4; ++r) sc[r] = r * 64 + lane == gone ? -INFINITY : sc[r];
      }
      const float mx = wave_fmax(fmaxf(fmaxf(sc[0], sc[1]), fmaxf(sc[2], sc[3])));
      const unsigned p0 = pack2(__builtin_amdgcn_exp2f(sc[0] - mx), __builtin_amdgcn_exp2f(sc[1] - mx)), p1 = pack2(__builtin_amdgcn_exp2f(sc[2] - mx), __builtin_amdgcn_exp2f(sc[3] - mx));
      *LDS_AT(u16, pw) = (u16)p0; *LDS_AT(u16, pw + 32) = (u16)(p0 >> 16);
      *LDS_AT(u16, pw + 256) = (u16)p1; *LDS_AT(u16, pw + 288) = (u16)(p1 >> 16);
      asm volatile("" ::: "memory");
      const float ls = wave_sum((__uint_as_float(p0 << 16) + __uint_as_float(p0 & 0xffff0000u)) + (__uint_as_float(p1 << 16) + __uint_as_float(p1 & 0xffff0000u)));
      f32x4 o = {0.f, 0.f, 0.f, 0.f};
#pragma unroll
      for (int i = 0; i < 16; ++i) o = MFMA16(*LDS_AT(const s16x8, pr + i * 32), f[i], o);
      int pv = __float_as_int((lane & 16) ? o[1] : o[0]);
      asm volatile("s_nop 0\n\tv_writelane_b32 %0, %1, 32\n\tv_writelane_b32 %0, %2, 33" : "+v"(pv) : "s"(ls), "s"(mx));
      __builtin_amdgcn_raw_buffer_store_b32(f2word(__int_as_float(pv)), rsh, lane < 34 ? (int)((PA - hand) + (head * 8 + tmi) * 64 + lane) * 4 : HAND_N * 4, 0, 16);
    }
    if (wid == 8) STAMP(dk, 8);
    if (tmi != ((idx >> 7) & 7)) return;
    {
      const int src = (int)((PA - hand) + head * 512 + (lane < 34 ? lane : 0)) * 4;
      const unsigned ta = tb + (lane & 31) * 2;
      const float so = wave_sum(lane < 32 ? bf2f(*LDS_AT(const u16, ta)) * bf2f(*LDS_AT(const u16, ta + 128)) : 0.f);
      const float vo = lane < 32 ? bf2f(*LDS_AT(const u16, ta + 256)) : 1.0f;
      unsigned v[8];
      __builtin_amdgcn_s_sleep(PRE5);
#pragma unroll 1
      for (int spin = 0; spin < SPIN_MAX; ++spin) {
        unsigned mx = 0u;
#pragma unroll
        for (int i = 0; i < 8; ++i) v[i] = __builtin_amdgcn_raw_buffer_load_b32(rsh, src + i * 256, 0, 16);
#pragma unroll
        for (int i = 0; i < 8; ++i) mx = umax(mx, v[i]);
        if (none(mx == SENT)) break;
        __builtin_amdgcn_s_sleep(PSLP);
        asm volatile("" ::: "memory");
      }
      float m8[8], mx = so;
#pragma unroll
      for (int i = 0; i < 8; ++i) { m8[i] = __int_as_float(__builtin_amdgcn_readlane((int)v[i], 33)); mx = fmaxf(mx, m8[i]); }
      float acc = __builtin_amdgcn_exp2f(so - mx) * vo;
#pragma unroll
      for (int i = 0; i < 8; ++i) acc += __builtin_amdgcn_exp2f(m8[i] - mx) * __uint_as_float(v[i]);
      const float l = __int_as_float(__builtin_amdgcn_readlane(__float_as_int(acc), 32));
      const float ov = acc / l;
      const float o1 = __int_as_float(__builtin_amdgcn_update_dpp(0, __float_as_int(ov), 0x55, 0xf, 0xf, false));
      const float o2 = __int_as_float(__builtin_amdgcn_update_dpp(0, __float_as_int(ov), 0xaa, 0xf, 0xf, false));
      const float o3 = __int_as_float(__builtin_amdgcn_update_dpp(0, __float_as_int(ov), 0xff, 0xf, 0xf, false));
      const __amdgpu_buffer_rsrc_t rso = __builtin_amdgcn_make_buffer_rsrc(Of, 0, S_ * D_ * 2, 0x00020000);
      if (lane < 32 && (lane & 3) == 0)
        __builtin_amdgcn_raw_buffer_store_b64(u32x2{pack2(ov, o1), pack2(o2, o3)}, rso,
            (((idx >> 5) * 32 + head) * 1024 + (lane >> 4) * 512 + (((lane >> 2) & 1) * 32 + (idx & 31)) * 8 + ((lane >> 3) & 1) * 4) * 2, 0, 16);
      asm volatile("s_waitcnt vmcnt(0)" ::: "memory");
      if (lane < RO) st_ag(gran + GR_O + ((16 + (idx >> 7)) * RO + lane) * 64 + 16 + head, tag);
      STAMP(dk, 9);
    }
    }();
    }();
    __builtin_amdgcn_s_setprio(0);
    asm volatile("s_waitcnt vmcnt(0) lgkmcnt(0)" ::: "memory");
    PIN();
    ARGS_HERE;
    u32x2 w[16];
    auto ld_wd = [&]() __attribute__((always_inline)) {
      const __amdgpu_buffer_rsrc_t rd_ = __builtin_amdgcn_make_buffer_rsrc(const_cast<u16*>(wd), 0, D_ * F_ * 2, 0x00020000);
#pragma unroll
      for (int f = 0; f < 16; ++f) w[f] = __builtin_amdgcn_raw_buffer_load_b64(rd_, t * 8 + (f & 1) * 2048, (fg * 16 + (f & ~1)) * (D_ * 2), 0);
      PIN();
    };
    float fnum = 0.f, fvar = 0.f;
    const int rrow = omi * 128 + (oni >> 3) * 64 + (hw & 1) * 32;
    if (LAY2) {
      ld_wd();
      if (!p1blk) {
        const int fr = rrow + (lane & 7) * 4 + (lane >> 4);
        const double s1 = ld_ag(stats + fr), s2 = ld_ag(stats + S_ + fr), sw = ld_ag(stats + 2 * S_ + fr);
        lds_wr(lbase + SPM + 132, __float_as_uint(wsum));
        float mean, var, mb, den;
        ln_from_sums(s1, s2, mean, var, mb, den);
        const float g1 = 1.0f + frcp(den);
        fnum = fr == idx ? __uint_as_float(0x7fc00000u) : fmaf(-mean, wsum, (float)sw) * g1;
        fvar = var * g1 * g1;
      }
      asm volatile("" : "+v"(fnum), "+v"(fvar));
      PIN();
    }
    if (hw == 3) {
      const int nfl = (LAY2 && (idx >> 7) == omi) ? 48 : 16;
      auto pfl = (__attribute__((address_space(1))) const unsigned*)(gran + GR_O + (((LAY2 ? 16 : 0) + omi) * RO + oni % RO) * 64 + (lane < nfl ? lane : 0));
      constexpr unsigned vs = 8u * D_ * 2u - 1024u;
      unsigned vw0 = (unsigned)((lane >> 3) * D_ + oni * 64 + ((lane & 7) ^ ((lane >> 3) & 6)) * 8) * 2u, vw1 = vw0 + vs, vw2 = vw0 + 2 * vs, vw3 = vw0 + 3 * vs;
      asm volatile("" : "+v"(pfl), "+v"(vw0), "+v"(vw1), "+v"(vw2), "+v"(vw3));
      auto wait_arr = [&](unsigned n) {
#pragma unroll 1
        for (int i = 0; i < 4 * SPIN_MAX; ++i) {
          if (__builtin_amdgcn_readfirstlane(*LDS_AT(const volatile unsigned, lbase + FLG + 16)) >= n) break;
          __builtin_amdgcn_s_sleep(1);
        }
        asm volatile("" ::: "memory");
        PIN();
      };
      wait_arr(12u);
      unsigned fl;
      asm volatile("global_load_dword %0, %1, off sc1" : "=v"(fl) : "v"(pfl) : "memory");
#pragma unroll 1
      for (int i = 0; i < 4 * SPIN_MAX; ++i) {
        if (__builtin_amdgcn_readfirstlane(*LDS_AT(const volatile unsigned, lbase + FLG + 24)) >= 8u) break;
        __builtin_amdgcn_s_sleep(1);
      }
      asm volatile("" ::: "memory");
      PIN();
#pragma unroll
      for (int j = 24; j < 32; ++j)
        glds16x4<false>(reinterpret_cast<const char*>(wo) + j * (32u * D_ * 2u), vw0, vw1, vw2, vw3, lbase + j * 4096);
      unsigned keep;
      asm volatile("s_mov_b32 %0, m0\n\ts_mov_b32 m0, %3\n\ts_nop 2\n\tglobal_load_lds_dword %1, %2 sc1\n\ts_mov_b32 m0, %0"
                   : "=&s"(keep) : "v"(0u), "s"(reinterpret_cast<const char*>(gran + GR_N + bid * 64 + 39)), "s"(lbase + FLG - 252) : "memory");
      asm volatile("s_waitcnt vmcnt(33)" : "+v"(fl) :: "memory");
      if (__builtin_amdgcn_ballot_w64(fl != tag)) {
        __builtin_amdgcn_s_sleep(CSL1);
#pragma unroll 1
        for (int spin = 0; spin < SPIN_MAX; ++spin) {
          if (!__builtin_amdgcn_ballot_w64(__hip_atomic_load(pfl, __ATOMIC_RELAXED, __HIP_MEMORY_SCOPE_AGENT) != tag)) break;
          __builtin_amdgcn_s_sleep(CSLP);
        }
      }
      asm volatile("" ::: "memory");
      PIN();
    }
    u32x4 pv[4];
    asm volatile("" : "=v"(pv[0]), "=v"(pv[1]), "=v"(pv[2]), "=v"(pv[3]));
    if (LAY2 && !p1blk && hw < 2) {
      const __amdgpu_buffer_rsrc_t rsp = __builtin_amdgcn_make_buffer_rsrc(hand + 2 * HAND_N + PP_O, 0, H_ * S_ * 4, 0x00020000);
      const int src = ((lane >> 3) * S_ + rrow + (lane & 7) * 4) * 4;
#pragma unroll 1
      for (int i = 0; i < 4 * SPIN_MAX; ++i) {
        if (__builtin_amdgcn_readfirstlane(*LDS_AT(const volatile unsigned, lbase + FLG + 24)) >= 8u) break;
        __builtin_amdgcn_s_sleep(2);
      }
#pragma unroll 1
      for (int spin = 0; spin < SPIN_MAX; ++spin) {
        asm volatile("" ::: "memory");
        unsigned mx = 0u;
#pragma unroll
        for (int i = 0; i < 4; ++i) pv[i] = __builtin_amdgcn_raw_buffer_load_b128(rsp, src, i * (8 * S_ * 4), 16);
#pragma unroll
        for (int i = 0; i < 4; ++i) mx = umax(umax(umax(mx, pv[i][0]), pv[i][1]), umax(pv[i][2], pv[i][3]));
        if (none(mx == SENT)) break;
        __builtin_amdgcn_s_sleep(PPSLP);
      }
    }
    asm volatile("s_waitcnt lgkmcnt(0)" ::: "memory");
    __builtin_amdgcn_s_barrier();
    if (!LAY2) {
      PIN();
      asm volatile(AGAIN "\ngate_up_code:\n\t"
                 "v_lshlrev_b32 v84, 16, v100\n\tv_and_b32 v85, 0xffff0000, v100\n\tv_lshlrev_b32 v86, 16, v101\n\tv_and_b32 v87, 0xffff0000, v101\n\t"
                 "v_subrev_f32 v84, s84, v84\n\tv_subrev_f32 v85, s84, v85\n\tv_subrev_f32 v86, s84, v86\n\tv_subrev_f32 v87, s84, v87\n\t"
                 "v_cvt_pk_bf16_f32 v85, v84, v85\n\tv_cvt_pk_bf16_f32 v87, v86, v87\n\ts_mov_b32 s88, 0x01000100\n\ts_mov_b32 s89, 0x03020302\n\t"
                 "v_lshlrev_b32 v84, 16, v85\n\tv_and_b32 v85, 0xffff0000, v85\n\tv_lshlrev_b32 v86, 16, v87\n\tv_and_b32 v87, 0xffff0000, v87\n\t"
                 "v_mul_f32 v88, s86, v84\n\tv_mul_f32 v89, s86, v85\n\tv_mul_f32 v90, s86, v86\n\tv_mul_f32 v91, s86, v87\n\tv_fma_f32 v84, -v88, s85, v84\n\t"
                 "v_fma_f32 v85, -v89, s85, v85\n\tv_fma_f32 v86, -v90, s85, v86\n\tv_fma_f32 v87, -v91, s85, v87\n\tv_fmac_f32 v88, s86, v84\n\t"
                 "v_fmac_f32 v89, s86, v85\n\tv_fmac_f32 v90, s86, v86\n\tv_fmac_f32 v91, s86, v87\n\tv_cvt_pk_bf16_f32 v88, v88, v89\n\t"
                 "v_cvt_pk_bf16_f32 v90, v90, v91\n\tv_perm_b32 v92, v88, v88, s88\n\tv_perm_b32 v93, v88, v88, s89\n\tv_perm_b32 v94, v90, v90, s88\n\t"
                 "v_perm_b32 v95, v90, v90, s89\n\ts_nop 0\n\tv_and_b32_dpp v96, v92, v102 quad_perm:[0,0,0,0] row_mask:0xf bank_mask:0xf\n\t"
                 "v_and_b32_dpp v97, v92, v103 quad_perm:[0,0,0,0] row_mask:0xf bank_mask:0xf\n\t"
                 "v_and_b32_dpp v100, v93, v102 quad_perm:[0,0,0,0] row_mask:0xf bank_mask:0xf\n\t"
                 "v_and_b32_dpp v101, v93, v103 quad_perm:[0,0,0,0] row_mask:0xf bank_mask:0xf\n\tv_mfma_f32_4x4x4_16b_bf16 v[84:87], v[96:97], v[104:105], 0\n\t"
                 "v_mfma_f32_4x4x4_16b_bf16 v[88:91], v[96:97], v[106:107], 0\n\tv_and_b32_dpp v96, v94, v102 quad_perm:[0,0,0,0] row_mask:0xf bank_mask:0xf\n\t"
                 "v_and_b32_dpp v97, v94, v103 quad_perm:[0,0,0,0] row_mask:0xf bank_mask:0xf\n\t"
                 "v_mfma_f32_4x4x4_16b_bf16 v[84:87], v[100:101], v[108:109], v[84:87]\n\tv_mfma_f32_4x4x4_16b_bf16 v[88:91], v[100:101], v[110:111], v[88:91]\n\t"
                 "v_and_b32_dpp v100, v95, v102 quad_perm:[0,0,0,0] row_mask:0xf bank_mask:0xf\n\t"
                 "v_and_b32_dpp v101, v95, v103 quad_perm:[0,0,0,0] row_mask:0xf bank_mask:0xf\n\tv_mfma_f32_4x4x4_16b_bf16 v[84:87], v[96:97], v[112:113], v[84:87]\n\t"
                 "v_mfma_f32_4x4x4_16b_bf16 v[88:91], v[96:97], v[114:115], v[88:91]\n\tv_and_b32_dpp v96, v92, v102 quad_perm:[1,1,1,1] row_mask:0xf bank_mask:0xf\n\t"
                 "v_and_b32_dpp v97, v92, v103 quad_perm:[1,1,1,1] row_mask:0xf bank_mask:0xf\n\t"
                 "v_mfma_f32_4x4x4_16b_bf16 v[84:87], v[100:101], v[116:117], v[84:87]\n\tv_mfma_f32_4x4x4_16b_bf16 v[88:91], v[100:101], v[118:119], v[88:91]\n\t"
                 "v_and_b32_dpp v100, v93, v102 quad_perm:[1,1,1,1] row_mask:0xf bank_mask:0xf\n\t"
                 "v_and_b32_dpp v101, v93, v103 quad_perm:[1,1,1,1] row_mask:0xf bank_mask:0xf\n\tv_mfma_f32_4x4x4_16b_bf16 v[84:87], v[96:97], v[120:121], v[84:87]\n\t"
                 "v_mfma_f32_4x4x4_16b_bf16 v[88:91], v[96:97], v[122:123], v[88:91]\n\tv_and_b32_dpp v96, v94, v102 quad_perm:[1,1,1,1] row_mask:0xf bank_mask:0xf\n\t"
                 "v_and_b32_dpp v97, v94, v103 quad_perm:[1,1,1,1] row_mask:0xf bank_mask:0xf\n\t"
                 "v_mfma_f32_4x4x4_16b_bf16 v[84:87], v[100:101], v[124:125], v[84:87]\n\tv_mfma_f32_4x4x4_16b_bf16 v[88:91], v[100:101], v[126:127], v[88:91]\n\t"
                 "v_and_b32_dpp v100, v95, v102 quad_perm:[1,1,1,1] row_mask:0xf bank_mask:0xf\n\t"
                 "v_and_b32_dpp v101, v95, v103 quad_perm:[1,1,1,1] row_mask:0xf bank_mask:0xf\n\tv_mfma_f32_4x4x4_16b_bf16 v[84:87], v[96:97], v[128:129], v[84:87]\n\t"
                 "v_mfma_f32_4x4x4_16b_bf16 v[88:91], v[96:97], v[130:131], v[88:91]\n\tv_and_b32_dpp v96, v92, v102 quad_perm:[2,2,2,2] row_mask:0xf bank_mask:0xf\n\t"
                 "v_and_b32_dpp v97, v92, v103 quad_perm:[2,2,2,2] row_mask:0xf bank_mask:0xf\n\t"
                 "v_mfma_f32_4x4x4_16b_bf16 v[84:87], v[100:101], v[132:133], v[84:87]\n\tv_mfma_f32_4x4x4_16b_bf16 v[88:91], v[100:101], v[134:135], v[88:91]\n\t"
                 "v_and_b32_dpp v100, v93, v102 quad_perm:[2,2,2,2] row_mask:0xf bank_mask:0xf\n\t"
                 "v_and_b32_dpp v101, v93, v103 quad_perm:[2,2,2,2] row_mask:0xf bank_mask:0xf\n\tv_mfma_f32_4x4x4_16b_bf16 v[84:87], v[96:97], v[136:137], v[84:87]\n\t"
                 "v_mfma_f32_4x4x4_16b_bf16 v[88:91], v[96:97], v[138:139], v[88:91]\n\tv_and_b32_dpp v96, v94, v102 quad_perm:[2,2,2,2] row_mask:0xf bank_mask:0xf\n\t"
                 "v_and_b32_dpp v97, v94, v103 quad_perm:[2,2,2,2] row_mask:0xf bank_mask:0xf\n\t"
                 "v_mfma_f32_4x4x4_16b_bf16 v[84:87], v[100:101], v[140:141], v[84:87]\n\tv_mfma_f32_4x4x4_16b_bf16 v[88:91], v[100:101], v[142:143], v[88:91]\n\t"
                 "v_and_b32_dpp v100, v95, v102 quad_perm:[2,2,2,2] row_mask:0xf bank_mask:0xf\n\t"
                 "v_and_b32_dpp v101, v95, v103 quad_perm:[2,2,2,2] row_mask:0xf bank_mask:0xf\n\tv_mfma_f32_4x4x4_16b_bf16 v[84:87], v[96:97], v[144:145], v[84:87]\n\t"
                 "v_mfma_f32_4x4x4_16b_bf16 v[88:91], v[96:97], v[146:147], v[88:91]\n\tv_and_b32_dpp v96, v92, v102 quad_perm:[3,3,3,3] row_mask:0xf bank_mask:0xf\n\t"
                 "v_and_b32_dpp v97, v92, v103 quad_perm:[3,3,3,3] row_mask:0xf bank_mask:0xf\n\t"
                 "v_mfma_f32_4x4x4_16b_bf16 v[84:87], v[100:101], v[148:149], v[84:87]\n\tv_mfma_f32_4x4x4_16b_bf16 v[88:91], v[100:101], v[150:151], v[88:91]\n\t"
                 "v_and_b32_dpp v100, v93, v102 quad_perm:[3,3,3,3] row_mask:0xf bank_mask:0xf\n\t"
                 "v_and_b32_dpp v101, v93, v103 quad_perm:[3,3,3,3] row_mask:0xf bank_mask:0xf\n\tv_mfma_f32_4x4x4_16b_bf16 v[84:87], v[96:97], v[152:153], v[84:87]\n\t"
                 "v_mfma_f32_4x4x4_16b_bf16 v[88:91], v[96:97], v[154:155], v[88:91]\n\tv_and_b32_dpp v96, v94, v102 quad_perm:[3,3,3,3] row_mask:0xf bank_mask:0xf\n\t"
                 "v_and_b32_dpp v97, v94, v103 quad_perm:[3,3,3,3] row_mask:0xf bank_mask:0xf\n\t"
                 "v_mfma_f32_4x4x4_16b_bf16 v[84:87], v[100:101], v[156:157], v[84:87]\n\tv_mfma_f32_4x4x4_16b_bf16 v[88:91], v[100:101], v[158:159], v[88:91]\n\t"
                 "v_and_b32_dpp v100, v95, v102 quad_perm:[3,3,3,3] row_mask:0xf bank_mask:0xf\n\t"
                 "v_and_b32_dpp v101, v95, v103 quad_perm:[3,3,3,3] row_mask:0xf bank_mask:0xf\n\tv_mfma_f32_4x4x4_16b_bf16 v[84:87], v[96:97], v[160:161], v[84:87]\n\t"
                 "v_mfma_f32_4x4x4_16b_bf16 v[88:91], v[96:97], v[162:163], v[88:91]\n\ts_nop 1\n\t"
                 "v_mfma_f32_4x4x4_16b_bf16 v[84:87], v[100:101], v[164:165], v[84:87]\n\tv_mfma_f32_4x4x4_16b_bf16 v[88:91], v[100:101], v[166:167], v[88:91]\n\t"
                 "s_nop 4\n\tv_permlane32_swap_b32 v84, v88\n\tv_permlane32_swap_b32 v85, v89\n\tv_permlane32_swap_b32 v86, v90\n\tv_permlane32_swap_b32 v87, v91\n\t"
                 "v_add_f32 v84, v84, v88\n\tv_add_f32 v85, v85, v89\n\tv_add_f32 v86, v86, v90\n\tv_add_f32 v87, v87, v91\n\ts_nop 0\n\t"
                 "v_permlane16_swap_b32 v84, v85\n\tv_permlane16_swap_b32 v86, v87\n\ts_nop 0\n\tv_add_f32 v84, v84, v85\n\tv_add_f32 v86, v86, v87\n\ts_nop 1\n\t"
                 "v_add_f32_dpp v84, v84, v84 row_ror:8 row_mask:0xf bank_mask:0xf\n\tv_add_f32_dpp v86, v86, v86 row_ror:8 row_mask:0xf bank_mask:0xf\n\ts_nop 0\n\t"
                 "v_add_f32_dpp v84, v84, v84 row_ror:4 row_mask:0xf bank_mask:0xf\n\tv_add_f32_dpp v86, v86, v86 row_ror:4 row_mask:0xf bank_mask:0xf\n\t"
                 "ds_write2_b32 v99, v84, v86 offset1:8\n\ts_setpc_b64 s[92:93]\n\t"
                   :: "{s[0:1]}"(ap + 1), "{s2}"(bid), "{v0}"(wid * 64), KEPT() : "s90", "s91", "scc", "memory");
      __builtin_unreachable();
    }
    {
    constexpr int dk = 8;
    constexpr int BEST = FLG + 40, MEET = FLG + 48;
    __builtin_amdgcn_s_setprio(HPRIO);
    if (hw == 0) STAMP(dk, 0);
    const unsigned tb1 = (unsigned)__builtin_amdgcn_s_memrealtime();
    unsigned* hand2 = hand + HAND_N;
    const __amdgpu_buffer_rsrc_t rsh = __builtin_amdgcn_make_buffer_rsrc(hand2, 0, (HAND_N + PS_N) * 4, 0x00020000);
    const __amdgpu_buffer_rsrc_t rsx = __builtin_amdgcn_make_buffer_rsrc(xout, 0, 3 * D_ * 2, 0x00020000);
    constexpr int OOR = (HAND_N + PS_N) * 4;
    if (!p1blk && hw < 2) {
      f32x2 a[2], c[2];
#pragma unroll
      for (int e = 0; e < 4; ++e) {
        a[e >> 1][e & 1] = (__uint_as_float(pv[0][e] << 16) + __uint_as_float(pv[1][e] << 16)) + (__uint_as_float(pv[2][e] << 16) + __uint_as_float(pv[3][e] << 16));
        c[e >> 1][e & 1] = (__uint_as_float(pv[0][e] & 0xffff0000u) + __uint_as_float(pv[1][e] & 0xffff0000u)) + (__uint_as_float(pv[2][e] & 0xffff0000u) + __uint_as_float(pv[3][e] & 0xffff0000u));
      }
      const float r = (fnum + fold8(a[0], a[1])) * __builtin_amdgcn_rsqf(fvar + fold8(c[0], c[1]));
      unsigned key = (__float_as_uint(r) ^ ((unsigned)(__float_as_int(r) >> 31) | 0x80000000u)) & 0xfffff800u;
      if (r != r) key = 0u;
      key = wave_umax(key | (unsigned)(2047 - (rrow + (lane & 7) * 4 + (lane >> 4))));
      __builtin_amdgcn_raw_buffer_store_b64(u32x2{key, tb1 & 0x7fffffffu}, rsh, lane < NREP ? (HAND_N + PK_O + lane * 128 + (rrow >> 5) * 2) * 4 : OOR, 0, 16);
      if (hw == 1) STAMP(dk, 2);
    }
    gu_setup();
    bool got = true;
    unsigned fra = lbase;
    int fro = S_ * D_ * 2;
    auto get_fc = [&]() __attribute__((always_inline)) {
      const u32x2 fw = __builtin_amdgcn_raw_buffer_load_b64(rsh, (HAND_N + PK_O + (bid % NREP) * 128 + lane * 2) * 4, 0, 16);
      got = none(umax(fw[0], fw[1]) == SENT);
      if (got) {
        const unsigned c1 = wave_umax(fw[0]), c2 = wave_umax(fw[0] == c1 ? 0u : fw[0]);
#define BADFC 0
        const unsigned r1 = (2047u - (c1 & 0x7ffu)) ^ BADFC, r2 = 2047u - (c2 & 0x7ffu);
        const unsigned r = (lane & 8) ? r2 : r1;
        fra = lbase + QOFF + (r & 127u) * 128 + (((r ^ lane) & 7u) << 4);
        fro = (r >> 7) == (unsigned)omi && lane < 16 ? ((lane >> 3) * D_ + oni * 64 + (lane & 7) * 8) * 2 : S_ * D_ * 2;
        unsigned dt = (unsigned)((int)((fw[1] - tb1) << 1) >> 1) + 512u;
        dt = dt < 1024u ? dt : 512u;
        *LDS_AT(volatile u32x2, lbase + SPM + 144) = u32x2{r1 | (r2 << 16), (unsigned)__builtin_amdgcn_readlane(dt, r1 >> 5) | (wave_umax(dt) << 16)};
      }
    };
    if (hw == 0) {
      __builtin_amdgcn_s_sleep(FCPRE);
#pragma unroll 1
      for (int i = 0; i < SPIN_MAX; ++i) {
        if (__builtin_amdgcn_readfirstlane(*LDS_AT(const volatile unsigned, lbase + FLG + 28)) - best >= 4u) break;
        __builtin_amdgcn_s_sleep(FCSLP);
      }
      asm volatile("" ::: "memory");
      get_fc();
    } else __builtin_amdgcn_s_sleep(TPRE - 10);
    PIN();
    tile_at(2u);
    const unsigned tb2 = (unsigned)__builtin_amdgcn_s_memrealtime() - 512u;
    LOGDEF;
    LOG();
#pragma unroll 1
    for (int spin = 0; spin < SPIN_MAX && !got; ++spin) { get_fc(); asm volatile("" ::: "memory"); }
    if (hw == 0) __builtin_amdgcn_raw_buffer_store_b128(lds_rd4(fra), rsx, fro, 0, XAUX);
    PIN();
    u32x2 fm = {SENT, 0u};
#pragma unroll 1
    for (int i = 0; i < 4 * SPIN_MAX && fm[0] == SENT; ++i) {
      const u32x2 q = *LDS_AT(const volatile u32x2, lbase + SPM + 144);
      fm = u32x2{(unsigned)__builtin_amdgcn_readfirstlane(q[0]), (unsigned)__builtin_amdgcn_readfirstlane(q[1])};
    }
    const int fr1 = fm[0] & 0x7ffu, fr2 = (fm[0] >> 16) & 0x7ffu;
    const unsigned t1 = tb2 + (fm[1] & 1023u), tmx = tb2 + ((fm[1] >> 16) & 1023u);
    auto until = [&](unsigned te) { while ((int)((unsigned)__builtin_amdgcn_s_memrealtime() - te) < 0) __builtin_amdgcn_s_sleep(1); asm volatile("" ::: "memory"); };
    lds_wr(lbase + SPM + 136, tmx);
    PIN();
    if (hw == 0) STAMP(dk, 3);
    const int xoff = (hw * 256 + (lane & 3) * 64 + (lane >> 2) * 4) * 2;
    const int oc = (omi * 2 + (oni >> 3)) * 32, col = oc + (lane & 7) * 4 + (lane >> 4);
    const int xoo = !p1blk && hw == 3 ? (col & ~1) * 2 : S_ * D_ * 2;
    auto get_row = [&](int r, int sl, u32x2& xr, unsigned& xo) __attribute__((always_inline)) {
      if ((r >> 7) == omi) {
#pragma unroll 1
        for (int i = 0; i < SPIN_MAX; ++i) {
          if (__builtin_amdgcn_readfirstlane(*LDS_AT(const volatile unsigned, lbase + FLG + 24)) >= 16u) break;
          __builtin_amdgcn_s_sleep(1);
        }
        asm volatile("" ::: "memory");
      }
#pragma unroll 1
      for (int spin = 0; spin < SPIN_MAX; ++spin) {
        LOG();
        xr = __builtin_amdgcn_raw_buffer_load_b64(rsx, xoff, sl * (D_ * 2), 16);
        xo = __builtin_amdgcn_raw_buffer_load_b32(rsx, xoo, sl * (D_ * 2), 16);
        LOG();
        if (none(umax(umax(xr[0], xr[1]), xo) == SENT)) break;
        __builtin_amdgcn_s_sleep(XRSLP);
        asm volatile("" ::: "memory");
      }
      PIN();
    };
    auto row_md = [&](const u32x2& xr, int k) __attribute__((always_inline)) {
      auto dot = [&](unsigned x, unsigned y, float z) { return __builtin_amdgcn_fdot2_f32_bf16(__builtin_bit_cast(bf16x2, x), __builtin_bit_cast(bf16x2, y), z, false); };
      const float p1 = dot(xr[1], 0x3f803f80u, dot(xr[0], 0x3f803f80u, 0.f)), p2 = dot(xr[1], xr[1], dot(xr[0], xr[0], 0.f));
      const auto q = __builtin_amdgcn_permlane32_swap(__float_as_uint(p1), __float_as_uint(p2), false, false);
      const float qs = __uint_as_float(q[0]) + __uint_as_float(q[1]);
      const auto q2 = __builtin_amdgcn_permlane16_swap(__float_as_uint(qs), __float_as_uint(qs), false, false);
      float sv = __uint_as_float(q2[0]) + __uint_as_float(q2[1]);
      sv = dpp_add<0x121, 0xf>(dpp_add<0x122, 0xf>(dpp_add<0x124, 0xf>(dpp_add<0x128, 0xf>(sv))));
      lds_wr(lbase + SPM + k * 64 + (lane >> 4) * 16 + hw * 4, f2word(sv));
      u32x4 a, c;
#pragma unroll 1
      for (int i = 0; i < SPIN_MAX; ++i) {
        a = lds_rd4(lbase + SPM + k * 64); c = lds_rd4(lbase + SPM + k * 64 + 32);
        if (__builtin_amdgcn_readfirstlane(umax(umax(a[0], a[1]), umax(a[2], a[3]))) != SENT) break;
      }
      auto sum4 = [&](const u32x4& v) { return (__uint_as_float(v[0]) + __uint_as_float(v[1])) + (__uint_as_float(v[2]) + __uint_as_float(v[3])); };
      const float mean = sum4(a) * (1.0f / 1024.0f), msq = sum4(c) * (1.0f / 1024.0f);
      const float den = rbf(__builtin_amdgcn_sqrtf(rbf(rbf(fmaxf(fmaf(-mean, mean, msq), 0.f)) + 1e-5f)));
      return (unsigned)__builtin_amdgcn_readfirstlane((pack2(mean, mean) & 0xffffu) | (__float_as_uint(den) & 0xffff0000u));
    };
    auto ffn = [&](const u32x2& xr, unsigned md, unsigned n, const __amdgpu_buffer_rsrc_t rs, int yo, auto&& off) __attribute__((always_inline)) {
      gate_up(xr, md);
      if (hw == 0 && n == 2u) STAMP(dk, 8);
      asm volatile("" ::: "memory");
      PIN();
      lds_wr(lbase + MEET + hw * 4, n + off());
      u32x4 mt;
#pragma unroll 1
      for (int i = 0; i < SPIN_MAX; ++i) {
        mt = lds_rd4(lbase + MEET);
        if (__builtin_amdgcn_readfirstlane(min4(mt)) >= n) break;
      }
      asm volatile("" ::: "memory");
      PIN();
      if (hw == 0 && n == 2u) STAMP(dk, 9);
      if (__builtin_amdgcn_readfirstlane(mt[0]) != n + 1u)
        __builtin_amdgcn_raw_buffer_store_b128(down(act_diag(n == 4u ? 512 : 0), w), rs, (yo + fg * D_) * 4 + t * 16, 0, 16);
      dad += n == 2u ? 512 : -512;
      if (hw == 0 && n == 2u) STAMP(dk, 4);
    };
    auto y_sum4 = [&](const __amdgpu_buffer_rsrc_t rs, int yo, int k) __attribute__((always_inline)) {
      const int src = (yo + (hw * 56 + (lane >> 3)) * D_ + oc + (lane & 7) * 4) * 4;
      u32x4 v[7];
#pragma unroll
      for (int i = 0; i < 7; ++i) asm volatile("" : "=v"(v[i]));
      unsigned todo = 0x7fu;
#pragma unroll 1
      for (int spin = 0; spin < SPIN_MAX; ++spin) {
        LOG();
#pragma unroll
        for (int i = 0; i < 7; ++i) if ((todo >> i) & 1u) v[i] = __builtin_amdgcn_raw_buffer_load_b128(rs, src, i * (8 * D_ * 4), 16);
#pragma unroll
        for (int i = 0; i < 7; ++i) if (none(umax(umax(v[i][0], v[i][1]), umax(v[i][2], v[i][3])) == SENT)) todo &= ~(1u << i);
        if (!todo) break;
        __builtin_amdgcn_s_sleep(YSLP);
        asm volatile("" ::: "memory");
      }
      f32x2 z0 = {0.f, 0.f}, z1 = {0.f, 0.f};
#pragma unroll
      for (int i = 0; i < 7; ++i) {
        z0 += f32x2{__uint_as_float(v[i][0]), __uint_as_float(v[i][1])};
        z1 += f32x2{__uint_as_float(v[i][2]), __uint_as_float(v[i][3])};
      }
      const unsigned ya = lbase + SPM + 256 + k * 512 + ((lane & 7) * 4 + (lane >> 4)) * 16;
      lds_wr((lane & 8) ? lbase + DUM + lane * 4 : ya + hw * 4, f2word(fold8(z0, z1)));
      u32x4 q = {0u, 0u, 0u, 0u};
      if (hw == 3) {
#pragma unroll 1
        for (int i = 0; i < 4 * SPIN_MAX; ++i) {
          q = lds_rd4(ya);
          if (none(umax(umax(q[0], q[1]), umax(q[2], q[3])) == SENT)) break;
        }
      }
      return (__uint_as_float(q[0]) + __uint_as_float(q[1])) + (__uint_as_float(q[2]) + __uint_as_float(q[3]));
    };
    const __amdgpu_buffer_rsrc_t rs1 = __builtin_amdgcn_make_buffer_rsrc(hand, 0, HAND_N * 4, 0x00020000);
    u32x2 xr;
    unsigned xo, xo2;
    until(t1 + XDT);
    get_row(fr1, 0, xr, xo);
    if (hw == 0) STAMP(dk, 7);
#define FAILFC 0
    const unsigned md1 = row_md(xr, 0) ^ (FAILFC & 1);
    if (hw == 0) STAMP(dk, 10);
    LOGAT(20);
    u32x2 xr2;
    unsigned md2 = 0u;
    if (p1blk) {
      xr2 = __builtin_amdgcn_raw_buffer_load_b64(rsx, (fr2 >> 7) == omi ? S_ * D_ * 2 : xoff, D_ * 2, 16);
      PIN();
      ffn(xr, md1, 2u, rsh, GRAN_N, [&]() { return 0u; });
      __builtin_amdgcn_s_setprio(R2PRIO);
      LOG();
      if ((fr2 >> 7) == omi || !none(umax(xr2[0], xr2[1]) == SENT)) get_row(fr2, 1, xr2, xo2);
      md2 = row_md(xr2, 1) ^ (FAILFC & 2);
      ffn(xr2, md2, 4u, rs1, GRAN_N, [&]() {
        const u32x2 v = *LDS_AT(const volatile u32x2, lbase + BEST);
        return v[0] != 0u && (2047 - (int)(v[0] & 0x7ffu) != fr2 || v[1] != md2) ? 1u : 0u; });
    }
    if (hw == 0) STAMP(dk, 5);
    LOG();
    float y = 0.f;
    auto put_row = [&](int r, unsigned md, unsigned rb) __attribute__((always_inline)) {
      const float rden = __uint_as_float(md & 0xffff0000u), rs = rbf(frcp(1.0f + __expf(-__uint_as_float(rb << 16))));
      const float xa = __uint_as_float((col & 1) ? xo & 0xffff0000u : xo << 16);
      float ha = xa, hb = xa;
      ln_apply2(ha, hb, __uint_as_float(md << 16), rden, frcp(rden));
      const float o = xa + rbf(ha + rbf(rbf(y) * rs));
      __builtin_amdgcn_raw_buffer_store_b16((unsigned short)(pack2(o, o) & 0xffffu), __builtin_amdgcn_make_buffer_rsrc(out, 0, S_ * D_ * 2, 0x00020000),
                                            (lane & 8) ? S_ * D_ * 2 : (r * D_ + col) * 2, 0, OAUX);
    };
    if (!p1blk) {
      until(tmx + XD2);
      get_row(fr2, 1, xr2, xo2);
      md2 = row_md(xr2, 1) ^ (FAILFC & 2);
      until(t1 + XDT + YD1); until(tmx + YD2);
      if (hw == 3) STAMP(dk, 13);
      LOGAT(8);
      y = y_sum4(rsh, GRAN_N, 0);
      if (hw == 3) STAMP(dk, 14);
      LOGAT(24);
    }
    unsigned bmd = 0u;
    best = 0u;
#pragma unroll 1
    for (int spin = 0; spin < 4 * SPIN_MAX; ++spin) {
      const u32x2 v = *LDS_AT(const volatile u32x2, lbase + BEST);
      best = __builtin_amdgcn_readfirstlane(v[0]); bmd = __builtin_amdgcn_readfirstlane(v[1]);
      if (best) break;
      __builtin_amdgcn_s_sleep(1);
    }
    asm volatile("" ::: "memory");
    const int idx = 2047 - (int)(best & 0x7ffu);
    const unsigned rk = best >> 16, rb = (rk & 0x8000u) ? (rk & 0x7fffu) : (~rk & 0xffffu);
    if (hw == 0) STAMP(dk, 1);
    LOGAT(25);
    LOGOUT(hw);
    if (__builtin_expect(idx != fr1 || bmd != md1, 0)) {
      if (idx == fr2 && bmd == md2) {
        if (p1blk) return;
        y = y_sum4(rs1, GRAN_N, 1);
        xo = xo2;
      } else {
        get_row(idx, idx == fr1 ? 0 : (idx == fr2 ? 1 : 2), xr, xo);
        if (p1blk) ffn(xr, bmd, 6u, rsh, HAND_N + Y3_O, [&]() { return 0u; });
        else y = y_sum4(rsh, HAND_N + Y3_O, 2);
        if (p1blk || hw != 3) return;
#pragma unroll 1
        for (int spin = 0; spin < SPIN_MAX && idx != fr1 && idx != fr2; ++spin) {
          if (__builtin_amdgcn_readfirstlane(ld_ag(gran + GR_F + (oc >> 6) * 32)) == tag) break;
          asm volatile("" ::: "memory");
        }
      }
    }
    if (p1blk || hw != 3) return;
    put_row(idx, bmd, rb);
    }
    return;
  }
  if (wid >= 4) {
    const int lane = fresh_lane();
    const int lw = wid - 4;
    const long long lay_ = ap->lay;
    const u16* A = ap->a; const u16* W = ap->W; const u16* wc = ap->wc;
#define LPRIO 3
#define LP0 3
    __builtin_amdgcn_s_setprio(LP0);
    unsigned va[4], vw[6];
    const unsigned ul = lane;
    va[0] = (unsigned)(m0 + lw * 32) * (K * 2) + (ul >> 3) * (K * 2) + (((ul & 7u) ^ (ul >> 4)) << 4);
    va[1] = va[0] + (8 * K * 2 - 1024);
    va[2] = (va[0] ^ 64u) + 2 * (8 * K * 2 - 1024);
    va[3] = va[2] + (8 * K * 2 - 1024);
#pragma unroll
    for (int i = 0; i < 3; ++i) {
      const unsigned n = ul + 64u * i, k = __umul24(n, 171u) >> 12, cp = n - __umul24(k, 24u);
      vw[i] = __umul24(k, ldw * 2) + (cp >> 3) * 2048u + (((cp & 7u) ^ ((k & 2u) << 1)) << 4) + ((unsigned)(lw * 16 * ldw + n0) * 2u - i * 1024u);
      vw[i + 3] = (vw[i] ^ 16u) + 8 * ldw * 2;
    }
    PIN();
    asm volatile("" : "+v"(va[0]), "+v"(va[1]), "+v"(va[2]), "+v"(va[3]), "+v"(vw[0]), "+v"(vw[1]), "+v"(vw[2]), "+v"(vw[3]), "+v"(vw[4]), "+v"(vw[5]));
    const bool LAY2 = lay_ != 0;
    const int krot = tmi & 8;
    if (LAY2 && slow_cols(W, tni)) W = wc;
    auto stage_a = [&](int kt, int slot) {
      const char* pa = reinterpret_cast<const char*>(A) + (size_t)(kt ^ krot) * 128;
      asm volatile("s_mov_b32 m0, %5\n\ts_nop 0\n\tglobal_load_lds_dwordx4 %0, %4 sc1\n\tglobal_load_lds_dwordx4 %1, %4 offset:1024 sc1\n\t"
                   "global_load_lds_dwordx4 %2, %4 offset:2048 sc1\n\tglobal_load_lds_dwordx4 %3, %4 offset:3072 sc1"
                   :: "v"(va[0]), "v"(va[1]), "v"(va[2]), "v"(va[3]), "s"(pa), "s"(lbase + slot * STB + lw * 4096) : "m0", "memory");
    };
    auto stage_w = [&](int kt, int slot) {
      const char* pw = reinterpret_cast<const char*>(W) + (size_t)(kt ^ krot) * 128 * ldw;
      asm volatile("s_mov_b32 m0, %7\n\ts_nop 0\n\tglobal_load_lds_dwordx4 %0, %6 sc1\n\tglobal_load_lds_dwordx4 %1, %6 offset:1024 sc1\n\t"
                   "global_load_lds_dwordx4 %2, %6 offset:2048 sc1\n\ts_addk_i32 m0, 0xc00\n\ts_nop 0\n\tglobal_load_lds_dwordx4 %3, %6 sc1\n\t"
                   "global_load_lds_dwordx4 %4, %6 offset:1024 sc1\n\tglobal_load_lds_dwordx4 %5, %6 offset:2048 sc1"
                   :: "v"(vw[0]), "v"(vw[1]), "v"(vw[2]), "v"(vw[3]), "v"(vw[4]), "v"(vw[5]), "s"(pw), "s"(lbase + slot * STB + ABYTES + lw * 6144) : "m0", "scc", "memory");
    };
    if (LAY2) {
      __builtin_amdgcn_s_setprio(0);
      __builtin_amdgcn_s_sleep(TPRE - 10);
      tile_at(3u);
      __builtin_amdgcn_s_setprio(LPRIO);
#pragma unroll
      for (int st = 0; st < NST; ++st) stage_w(st, st);
      PIN();
      __builtin_amdgcn_s_barrier();
      PIN();
#pragma unroll
      for (int st = 0; st < NST; ++st) stage_a(st, st);
      asm volatile("s_waitcnt vmcnt(8)" ::: "memory");
    } else {
      __builtin_amdgcn_s_setprio(LPRIO);
      stage_a(0, 0); stage_w(0, 0);
      if (PRO > 1) { stage_a(1, 1); stage_w(1, 1); }
      if (PRO > 2) { stage_a(2, 2); stage_w(2, 2); }
      asm volatile("s_waitcnt vmcnt(%0)" :: "n"(10 * PRO - 10) : "memory");
    }
    int slot = 0;
    __builtin_amdgcn_s_barrier();
    if (!LAY2) {
      if (PRO < 2) { stage_a(1, 1); stage_w(1, 1); }
      if (PRO < 3) { stage_a(2, 2); stage_w(2, 2); }
    }
    for (int kt = 0; kt + 1 < NKT; ++kt) {
      if (LAY2 && kt == 0) asm volatile("s_waitcnt vmcnt(4)" ::: "memory");
      else if (kt + 2 >= NKT) asm volatile("s_waitcnt vmcnt(0)" ::: "memory");
      else asm volatile("s_waitcnt vmcnt(10)" ::: "memory");
      __builtin_amdgcn_s_barrier();
      if (kt + NST < NKT) { stage_a(kt + NST, slot); stage_w(kt + NST, slot); }
      slot = slot == NST - 1 ? 0 : slot + 1;
    }
    __builtin_amdgcn_s_setprio(0);
    __builtin_amdgcn_s_barrier();
  } else {
  LAYER_HERE;
  const int dk = LAY2 ? 5 : 1;
  const int lane = fresh_lane(), wm = wid & 1, wn = wid >> 1;
  if (wid == 0) STAMP(dk, 0);
  f32x4 acc[4][6];
#pragma unroll
  for (int i = 0; i < 4; ++i)
#pragma unroll
    for (int j = 0; j < 6; ++j) acc[i][j] = f32x4{0.f, 0.f, 0.f, 0.f};
  const int q16 = lane & 15, g4 = lane >> 4;
  const int e0 = 16 * (q16 >> 3) + (q16 & 7);
  s16x8 af[2][4];
  unsigned aad[2], wad[2];
  {
    const int row = wm * 64 + e0, swz = ((row >> 1) & 3) | (((row >> 4) & 1) << 2);
#pragma unroll
    for (int h = 0; h < 2; ++h) aad[h] = lbase + row * 128 + (((4 * h + g4) ^ swz) << 4);
    const int tq = (lane & 15) >> 2, tp = lane & 3, k = 8 * g4 + tq, ch = wn * 4 + 2 * (tp >> 1);
    wad[0] = lbase + ABYTES + k * 384 + ((ch ^ (4 * ((k >> 1) & 1)) ^ (g4 & 1)) << 4) + 8 * (tp & 1);
    wad[1] = wad[0] ^ 16;
  }
  auto lda = [&](unsigned a, int i) { return *LDS_AT(const s16x8, a + (i >> 1) * 4096 + (i & 1) * 1024); };
  auto ldw_ = [&](int h, int j) {
    const unsigned w = wad[j & 1] + h * 12288 + (j >> 1) * 128;
    s16x4 lo = __builtin_amdgcn_ds_read_tr16_b64_v4i16(LDS_AT(s16x4, w));
    s16x4 h4 = __builtin_amdgcn_ds_read_tr16_b64_v4i16(LDS_AT(s16x4, w + 1536));
    return (s16x8)__builtin_shufflevector(lo, h4, 0, 1, 2, 3, 4, 5, 6, 7);
  };
  ARGS_HERE;
  unsigned lto = lbase + wid * 4096;
  char* kvb = reinterpret_cast<char*>(KV) + ((tni * 2 + wn) * 64 + tmi * 4 + wm * 2) * 2048 - lto;
  {
    const unsigned ato = lbase + ALPHA + wm * 256, dum = lbase + DUM + lane * 4;
    *LDS_AT(u32x4, lto + QOFF + lane * 16) = u32x4{lane >= 16 ? lbase + CRED : lbase + TABA + (wm * 64 + e0) * 16, lbase + TABW + (wn * 32 + e0) * 16,
                                                   ato + e0 * 4, lto + (lane >> 5) * 1024 + ((lane & 28) * 2 + (lane & 3)) * 16};
    *LDS_AT(u32x2, lto + QOFF + 2048 + lane * 16) = u32x2{aad[0], aad[1]};
    *LDS_AT(u32x4, lto + QOFF + 1024 + lane * 16) = u32x4{ato + g4 * 16 + (lane >> 5) * 32, lto + (lane >> 5) * 1024 + ((lane & 24) * 2 + (lane & 7)) * 16,
                                                          lane == 0 ? lbase + FLG + 32 : dum, lane == 0 ? lbase + FLG + 16 : dum};
  }
  asm volatile("" : "+s"(lto), "+s"(kvb) :: "memory");
#pragma unroll
  for (int i = 0; i < 4; ++i)
#pragma unroll
    for (int j = 0; j < 6; ++j) asm volatile("" : "+v"(acc[i][j]));
  asm volatile("" : "+v"(aad[0]), "+v"(aad[1]), "+v"(wad[0]), "+v"(wad[1]));
  if (LAY2) __builtin_amdgcn_s_barrier();
  __builtin_amdgcn_s_barrier();
  PIN();
  if (wid == 0) STAMP(dk, 1);
  s16x8 wq[4][2];
  wq[0][0] = ldw_(0, 0); af[0][0] = lda(aad[0], 0); wq[0][1] = ldw_(1, 0); af[1][0] = lda(aad[1], 0); wq[1][0] = ldw_(0, 1); wq[1][1] = ldw_(1, 1);
  af[0][1] = lda(aad[0], 1); af[1][1] = lda(aad[1], 1); af[0][2] = lda(aad[0], 2); af[1][2] = lda(aad[1], 2);
  PIN();
  s16x4 wlo;
  auto rw = [&](s16x8& d, int h, int j, int hi, int off = 0) __attribute__((always_inline)) {
    const s16x4 v = __builtin_amdgcn_ds_read_tr16_b64_v4i16(LDS_AT(s16x4, wad[j & 1] + h * 12288 + (j >> 1) * 128 + hi * 1536 + off));
    if (hi) d = (s16x8)__builtin_shufflevector(wlo, v, 0, 1, 2, 3, 4, 5, 6, 7);
    else wlo = v;
  };
  auto at = [&](s16x8& x, int r) __attribute__((always_inline)) {
    if (r == 0) asm volatile("" :: "{v[104:107]}"(x));
    if (r == 1) asm volatile("" :: "{v[108:111]}"(x));
    if (r == 2) asm volatile("" :: "{v[112:115]}"(x));
    if (r == 3) asm volatile("" :: "{v[116:119]}"(x));
    if (r == 4) asm volatile("" :: "{v[120:123]}"(x));
    if (r == 5) asm volatile("" :: "{v[124:127]}"(x));
    if (r == 6) asm volatile("" :: "{v[128:131]}"(x));
    if (r == 7) asm volatile("" :: "{v[132:135]}"(x));
    if (r == 8) asm volatile("" :: "{v[136:139]}"(x));
    if (r == 9) asm volatile("" :: "{v[140:143]}"(x));
    if (r == 10) asm volatile("" :: "{v[144:147]}"(x));
    if (r == 11) asm volatile("" :: "{v[148:151]}"(x));
    if (r == 12) asm volatile("" :: "{v[152:155]}"(x));
    if (r == 13) asm volatile("" :: "{v[156:159]}"(x));
    if (r == 14) asm volatile("" :: "{v[160:163]}"(x));
    if (r == 15) asm volatile("" :: "{v[164:167]}"(x));
  };
  auto mm = [&](int i, int j, int h, int sl, int hw_) __attribute__((always_inline)) {
    s16x8 &a = af[h][i], &w = wq[sl][hw_];
    at(a, h * 4 + i); at(w, 8 + sl * 2 + hw_);
    if (j >= 4) asm volatile("v_mfma_f32_16x16x32_bf16 %0, %1, %2, %0" : "+v"(acc[i][j]) : "v"(a), "v"(w));
    else asm volatile("v_mfma_f32_16x16x32_bf16 %0, %1, %2, %0" : "+v"(acc[i][j]) : "v"(w), "v"(a));
    PIN();
  };
  auto next = [&](int g, int J0, int s0, int s1, int off) __attribute__((always_inline)) {
    if (g == 2 || g == 3) rw(wq[s0][0], 0, J0, g & 1, off);
    if (g == 4 || g == 5) af[g & 1][0] = lda(aad[g & 1] + off, 0);
    if (g == 6 || g == 7) rw(wq[s0][1], 1, J0, g & 1, off);
    if (g >= 8 && g < 12) rw(wq[s1][(g - 8) >> 1], (g - 8) >> 1, J0 + 1, g & 1, off);
    if (g >= 12) af[g & 1][(g - 10) >> 1] = lda(aad[g & 1] + off, (g - 10) >> 1);
  };
  auto third = [&](int p, int J0, int delta) __attribute__((always_inline)) {
    auto mv = [&](unsigned& x) __attribute__((always_inline)) { asm volatile("v_add_u32 %0, %1, %0" : "+v"(x) : "s"(delta)); };
#pragma unroll
    for (int g = 0; g < 16; ++g) {
      const int h = g & 1, i = p == 1 ? (g >> 1) & 3 : g >> 2, t = p == 1 ? g >> 3 : (g >> 1) & 1, j = 2 * p + t, sl = p == 1 ? 2 - 2 * t : p + t;
      mm(i, j, h, sl, h);
      if (p == 0) {
        if (g < 2) af[g][3] = lda(aad[g], 3);
        else if (g < 6) rw(wq[2][(g - 2) >> 1], (g - 2) >> 1, 2, g & 1);
        else if (g < 10) rw(wq[3][(g - 6) >> 1], (g - 6) >> 1, 5, g & 1);
        else if (g < 12) mv(aad[g - 10]);
      }
      if (p == 1) {
        if (g < 4) rw(wq[0][g >> 1], g >> 1, 3, g & 1);
        if (g == 4) mv(wad[1]);
        if (g >= 8 && g < 12) rw(wq[2][(g - 8) >> 1], (g - 8) >> 1, 4, g & 1);
        if (g == 12) mv(wad[0]);
      }
      if (p == 2) {
        if (g == BARG) {
          asm volatile("" ::: "memory");
          PIN();
          __builtin_amdgcn_s_barrier();
          asm volatile("" ::: "memory");
        }
        next(g, J0, 0, 1, 0);
      }
      PIN();
      {
        if ((p == 2 && g == 15) || (p == 0 && g == 7)) __builtin_amdgcn_s_waitcnt(0xc67f);
        if (p == 0 && g == 2) __builtin_amdgcn_s_waitcnt(0xc57f);
        if ((p == 0 && g == 14) || (p == 1 && (g & 7) == 7)) __builtin_amdgcn_s_waitcnt(0xc07f);
        PIN();
      }
    }
  };
  third(0, 0, STB); third(1, 0, STB);
  {
    static_assert((NKT - NST - 1) % NST == 0, "");
#pragma unroll 1
    for (int kt = 0; kt < NKT - NST - 1; kt += NST) {
#pragma unroll
      for (int u = 0; u < NST; ++u) {
        const int delta = u == 1 ? -(NST - 1) * STB : STB;
        third(2, 0, 0); third(0, 0, delta); third(1, 0, delta);
      }
    }
  }
  static_assert(NKT % NST == 1, "slots of the last stages");
#define TT(i)
  auto bar = [&]() { PIN(); __builtin_amdgcn_s_barrier(); asm volatile("" ::: "memory"); PIN(); };
  TT(0);
  third(2, 2, 0);
  const __amdgpu_buffer_rsrc_t rsk = __builtin_amdgcn_make_buffer_rsrc(kvb, 0, 0x7fffffff, 0x00020000), rsv = __builtin_amdgcn_make_buffer_rsrc(kvb + VOFF, 0, 0x7fffffff, 0x00020000);
  unsigned pk;
  u32x2 t0, t1, t2, t3;
  auto park = [&](int i) { return *LDS_AT(const volatile u32x2, pk + (i >> 1) * 1024 + (i & 1) * 8); };
  float al[4];
  f32x4 av[4];
  u32x4 kvp[12];
  auto cacc = [&](int p, int x, int t) -> f32x4& { return p < 2 ? acc[x][2 * p + t] : acc[(x >> 1) * 2 + t][4 + (x & 1)]; };
  auto signal = [&](unsigned v) { __hip_atomic_fetch_add(LDS_AT(unsigned, t3[0]), v, __ATOMIC_RELAXED, __HIP_MEMORY_SCOPE_WORKGROUP); PIN(); };
  auto outop = [&](int p, int k) {
    if (k >= 0 && k < 16) {
      const int x = k >> 2, t = (k >> 1) & 1;
      f32x4& c = cacc(p, x, t);
#pragma unroll
      for (int o = 2 * (k & 1); o < 2 * (k & 1) + 2; ++o) {
        float r;
        asm volatile("v_mul_f32 %0, %1, %2" : "=v"(r) : "v"(c[o]), "v"(p < 2 ? al[x] : av[(x >> 1) * 2 + t][o]));
        c[o] = r;
      }
      PIN();
    }
    if (k >= 1 && k <= 16) {
      const int x = (k - 1) >> 2, t = ((k - 1) >> 1) & 1, o = (k - 1) & 1;
      kvp[p * 4 + x][2 * t + o] = pack2(cacc(p, x, t)[2 * o], cacc(p, x, t)[2 * o + 1]);
      PIN();
      if (t && o) {
        if (p) __builtin_amdgcn_raw_buffer_store_b128(kvp[p * 4 + x], p == 1 ? rsk : rsv, (p == 1 ? t1[1] : t2[1]) + (x >> 1) * 2048 + (x & 1) * (p == 1 ? 64 : 128), 0, 16);
        else *LDS_AT(u32x4, t2[1] + (x >> 1) * 2048 + (x & 1) * 128) = kvp[x];
        PIN();
      }
    }
  };
  static_for<0, 168>([&](auto nc) __attribute__((always_inline)) {
    constexpr int n = decltype(nc)::value, sec = n < 80 ? n >> 4 : (n < 104 ? 5 : 6 + ((n - 104) >> 4)), g = n < 104 ? n - 16 * sec : (n - 104) & 15;
    auto taba = [&](int i) { af[0][i] = *LDS_AT(const s16x8, t0[0] + (i >> 1) * 512 + (i & 1) * 128); };
    auto tabw = [&](int j) { wq[(2 + (j >> 1)) & 3][j & 1] = *LDS_AT(const s16x8, t0[1] + ((j >> 1) * 64 + (j & 1) * 8) * 16); };
    auto rwn = [&](int e, int sl, int j, int off) { if (e >= 0 && e < 4) rw(wq[sl][e >> 1], e >> 1, j, e & 1, off); };
    if (g == 0) TT(1 + sec);
    if (sec == 5) {
      const int i = (g / 12) * 2 + (g & 1), j = (g % 12) >> 1;
      mm(i, j, 0, (2 + (j >> 1)) & 3, j & 1);
    } else {
      constexpr int part[10] = {1, 2, 1, 2, 0, 0, 1, 2, 0, 0}, s0[10] = {0, 2, 0, 2, 0, 0, 1, 3, 1, 3};
      const int h = g & 1, i = sec == 7 ? (g >> 3) * 2 + ((g >> 1) & 1) : g >> 2, t = sec == 7 ? (g >> 2) & 1 : (g >> 1) & 1;
      mm(i, 2 * part[sec] + t, h, (s0[sec] + t) & 3, h);
    }
    if (sec == 0 || sec == 2) {
      if (g < 2) af[g][3] = lda(aad[g] + sec / 2 * STB, 3);
      rwn(g - 2, 2, 4, sec / 2 * STB); rwn(g - 6, 3, 5, sec / 2 * STB);
    }
    if (sec == 1) {
      if (g == 1) bar();
      next(g, 2, 0, 1, STB);
    }
    if ((sec == 3 && g == 12) || (sec == 6 && g == 13)) { pk = lto + QOFF + fresh_lane() * 16; asm volatile("" : "+v"(pk)); }
    if (sec == 3) {
      rwn(g, 0, 0, STB); rwn(g - 4, 1, 1, STB);
      if (g >= 8 && g < 12) asm volatile("v_subrev_u32 %0, %1, %0" : "+v"(g < 10 ? aad[g & 1] : wad[g & 1]) : "s"(STB));
      if (g == 13) t1 = park(1);
      if (g == 14) t0 = park(0);
    }
    if (sec == 4) {
      if (g == B15) bar();
      if (g == 4) taba(0);
      if (g == 5 || g == 6) tabw(g - 5);
      if (g == 7) taba(1);
      if (g == 8 || g == 9) tabw(g - 6);
      if (g == 10 || g == 11) af[1][g - 10] = lda(aad[1], g - 10);
      if (g == 12) taba(2);
      if (g == 15) taba(3);
    }
    if (sec == 5) {
      if (g < 2) tabw(4 + g);
      rwn(g - 2, 1, 2, 0);
      if (g == 12 || g == 13) af[0][g - 12] = lda(aad[0], g - 12);
      if (g == 22) { al[0] = *LDS_AT(const float, t1[0]); al[1] = *LDS_AT(const float, t1[0] + 32); }
      rwn(g - 16, 2, 3, 0);
      if (g == 20 || g == 21) af[1][g - 18] = lda(aad[1], g - 18);
    }
    if (sec == 6) {
      if (g < 2) af[0][2 + g] = lda(aad[0], 2 + g);
      rwn(g - 2, 3, 4, 0);
      if (g == 9) { al[2] = *LDS_AT(const float, t1[0] + 128); al[3] = *LDS_AT(const float, t1[0] + 160); }
      if (g == 14) t2 = park(2);
      if ((g & 3) >= 2) rwn((g - 10) / 4 * 2 + (g & 1) - (g < 10) * 8, 0, 5, 0);
    }
    if (sec == 7) {
      if (g == 2 || g == 3 || g == 10 || g == 11) av[(g >> 3) * 2 + (g & 1)] = *LDS_AT(const f32x4, t2[0] + ((g >> 3) * 32 + (g & 1) * 8) * 4);
      rwn(g < 2 ? g : (g < 4 ? -1 : g - 2), 1, 0, 0); rwn(g - 6, 2, 1, 0);
      if (g == 12) t3 = park(3);
      if (g == 13) { const u32x2 a = park(4); aad[0] = a[0]; aad[1] = a[1]; }
      if (g == 15) {
        asm volatile("" ::: "memory");
        __builtin_amdgcn_s_waitcnt(0xc07f);
        bar();
      }
    }
    if (sec == 8) next(g, 0, 3, 0, STB);
    if (sec == 9) {
      if (g < 2) af[g][3] = lda(aad[g] + STB, 3);
      if (g == 2) signal(0x10000u);
    }
    PIN();
    outop(1, n - 104 - KO);
    outop(2, n - 120 - KO);
    {
      constexpr int wg[8] = WGAPS;
#pragma unroll
      for (int e = 0; e < 8; ++e)
        if (n - 136 == wg[e]) {
          *LDS_AT(u32x4, (e < 4 ? t1[1] + (e & 1) * 64 : t2[1] + 2048 + (e & 1) * 128) + ((e >> 1) & 1) * ASB) = kvp[4 + e];
          PIN();
        }
    }
    if (n == 152 + 3 || n == 152 + 4) {
      al[2 * (n & 1)] *= QSCALE; al[2 * (n & 1) + 1] *= QSCALE;
      asm volatile("" : "+v"(al[2 * (n & 1)]), "+v"(al[2 * (n & 1) + 1]));
      if (n & 1) asm volatile("v_add_u32 %0, %1, %0" : "+v"(t2[1]) : "s"(QOFF));
      PIN();
    }
    outop(0, n - 152 - KO);
  });
  TT(13);
  if (wid == 0) STAMP(dk, 18);
#pragma unroll
  for (int k = 16 - KO; k <= 16; ++k) outop(0, k);
  __hip_atomic_fetch_add(LDS_AT(unsigned, t3[1]), 1u, __ATOMIC_RELAXED, __HIP_MEMORY_SCOPE_WORKGROUP);
  TT(14);
  asm volatile("s_waitcnt vmcnt(0)" ::: "memory");
  signal(1u);
  TT(15);
  if (wid == 0) STAMP(dk, 19);
  }
  PIN();
  {
  const int lane = fresh_lane(), tid = wid * 64 + lane;
  LAYER_HERE;
  const int dk = LAY2 ? 6 : 2;
  const int g = wid >> 2, hh = (wid >> 1) & 1, qg = wid & 1;
  const int head = tni * 2 + hh, qt = tmi * 4 + qg * 2;
  unsigned qaa = lbase + QOFF + (hh * 2 + qg) * 4096 + lane * 16 + (g ? 0 : 2048), qab = qaa ^ 2048;
  const unsigned wbase = lbase + (hh * 2 + g) * 4096 + lane * 16;
  auto sptr = [&](int sl) { unsigned a = wbase + sl * ASB; asm volatile("" : "+v"(a)); return LDS_AT(const s16x8, a); };
  auto rd = [&](__attribute__((address_space(3))) const s16x8* sp, int off) { return sp[off >> 4]; };
  auto sl0 = sptr(0);
  const unsigned arr_a = lane == 0 ? lbase + FLG + 16 : lbase + DUM + lane * 4;
  auto arrive = [&]() { __hip_atomic_fetch_add(LDS_AT(unsigned, arr_a), 1u, __ATOMIC_RELAXED, __HIP_MEMORY_SCOPE_WORKGROUP); };
  unsigned arr_w = lane == 0 ? lbase + FLG + 8 + g * 4 : lbase + DUM + lane * 4;
  unsigned arr_i = 1u << (8 * (wid & 3));
  auto arrive_w = [&]() { __hip_atomic_fetch_add(LDS_AT(unsigned, arr_w), arr_i, __ATOMIC_RELAXED, __HIP_MEMORY_SCOPE_WORKGROUP); };
  s16x8 sel;
#pragma unroll
  for (int e = 0; e < 8; ++e) sel[e] = ((lane & 7) == (e & 4)) ? (short)0x3F80 : (short)0;
  int sidx = 0xDDDDDDDD;
  f32x16 oa, ob, nn, sa, sb;
  f32x4 la = {0.f, 0.f, 0.f, 0.f}, lb = {0.f, 0.f, 0.f, 0.f};
#pragma unroll
  for (int e = 0; e < 16; ++e) { oa[e] = 0.f; ob[e] = 0.f; sa[e] = 0.f; sb[e] = 0.f; }
  asm volatile("" : "+v"(sidx), "+v"(sel), "+v"(oa), "+v"(ob), "+v"(la), "+v"(lb), "+v"(qaa), "+v"(qab), "+v"(arr_w), "+v"(arr_i));
  PIN();
  s16x8 qa0, qa1, qb0, qb1, k00, k01, v00, v01, v10, v11, k10, k11;
  u32x4 fw;
  auto first = [&]() {
    fw = lds_rd4(lbase + FLG + 16);
    asm volatile("" ::: "memory");
    k00 = rd(sl0, 0); qa0 = *LDS_AT(const s16x8, qaa); qb0 = *LDS_AT(const s16x8, qab);
    k01 = rd(sl0, 1024); qa1 = *LDS_AT(const s16x8, qaa + 1024); qb1 = *LDS_AT(const s16x8, qab + 1024);
    v00 = rd(sl0, 2048); v01 = rd(sl0, 3072);
    PIN();
  };
  first();
  if (__builtin_amdgcn_readfirstlane(fw[0]) < 4u) {
#pragma unroll 1
    for (int i = 0; i < SPIN_MAX; ++i) {
      if (__builtin_amdgcn_readfirstlane(*LDS_AT(const volatile unsigned, lbase + FLG + 16)) >= 4u) break;
      __builtin_amdgcn_s_sleep(ASLP);
    }
    asm volatile("" ::: "memory");
    first();
  }
  const unsigned tag = __builtin_amdgcn_readfirstlane(fw[1]);
  const int idx = LAY2 ? (2047 - (int)(__builtin_amdgcn_readfirstlane(fw[3]) & 0xffffu)) & 2047 : -64;
  float ma;
  const float zoff = __uint_as_float(__builtin_amdgcn_readfirstlane(LAY2 && g == ((idx >> 6) & 1) ? 0u : 0xff800000u));
  {
    sa = MFMA32(k00, qa0, sa); sb = MFMA32(k00, qb0, sb);
    sa = MFMA32(k01, qa1, sa); sb = MFMA32(k01, qb1, sb);
    float mb = sb[0];
    ma = sa[0];
#pragma unroll
    for (int e = 1; e < 16; ++e) { ma = fmaxf(ma, sa[e]); mb = fmaxf(mb, sb[e]); }
    ma = fmaxf(ma, mb);
    const auto sw = __builtin_amdgcn_permlane32_swap(__float_as_uint(ma), __float_as_uint(ma), false, false);
    ma = fmaxf(__uint_as_float(sw[0]), __uint_as_float(sw[1]));
    f32x2 n2 = {-ma, -ma};
    asm volatile("" : "+v"(n2));
#pragma unroll
    for (int e = 0; e < 16; ++e) nn[e] = n2[e & 1];
    sa = MFMA32(k00, qa0, nn); sa = MFMA32(k01, qa1, sa);
    la[0] = lb[0] = -__uint_as_float(__float_as_uint(__builtin_amdgcn_exp2f(zoff - ma)) & 0xffff0000u);
  }
#define SB() __builtin_amdgcn_sched_barrier(0)
#define EX(i) sc[i] = __builtin_amdgcn_exp2f(sc[i])
  auto unit = [&](f32x16& sc, f32x16& sn, const s16x8& kn0, const s16x8& kn1, const s16x8& qn0, const s16x8& qn1,
                  f32x16& op, f32x4& lp, const s16x8& vp,
                  f32x16& oc, f32x4& lc, const s16x8& vc, auto&& mid) {
    lp = SMF16(sel, PHI(sn), lp); SB();
    op = SMF32(vp, PHI(sn), op); SB();
    EX(0); EX(1); EX(2); EX(3); SB();
    sn = MFMA32(kn0, qn0, nn); SB();
    EX(4); EX(5); EX(6); EX(7); SB();
    sn = MFMA32(kn1, qn1, sn); SB();
    mid();
    EX(8); EX(9); EX(10); EX(11); SB();
    lc = SMF16(sel, PLO(sc), lc); SB();
    oc = SMF32(vc, PLO(sc), oc); SB();
    EX(12); EX(13); EX(14); EX(15); SB();
    asm volatile("" : "+v"(sc));
  };
  sl0 = sptr(1);
  unsigned f0 = 0u;
  auto check = [&](unsigned need) {
    if (__builtin_expect(__builtin_amdgcn_readfirstlane(f0) < need, 0)) {
#pragma unroll 1
      for (int i = 0; i < SPIN_MAX; ++i) {
        if (__builtin_amdgcn_readfirstlane(*LDS_AT(const volatile unsigned, lbase + FLG)) >= need) break;
        __builtin_amdgcn_s_sleep(1);
      }
    }
    asm volatile("" ::: "memory");
  };
#define PAT 0x22221110
#define SAW8(i) do { if (((PAT >> (28 - 4 * (i))) & 3) != ((PAT >> ((32 - 4 * (i)) & 31)) & 3)) { __builtin_amdgcn_s_setprio((PAT >> (28 - 4 * (i))) & 3); SB(); } } while (0)
#define SAW(i) do { if (odd) SAW8(4 + (i)); else SAW8(i); } while (0)
  auto three = [&](int st, bool odd) {
    SAW(0);
    unit(sa, sb, k00, k01, qb0, qb1, ob, lb, v11, oa, la, v00, [&]() { k10 = rd(sl0, 0); k11 = rd(sl0, 1024); SB(); });
    SAW(1);
    unit(sb, sa, k10, k11, qa0, qa1, oa, la, v01, ob, lb, v00, [&]() { v10 = rd(sl0, 2048); v11 = rd(sl0, 3072); SB(); });
    sl0 = sptr((2 * st + 2) & 7);
    SB();
    SAW(2);
    unit(sa, sb, k10, k11, qb0, qb1, ob, lb, v01, oa, la, v10, [&]() { k00 = rd(sl0, 0); k01 = rd(sl0, 1024); SB(); });
  };
  auto fourth = [&](int st, bool odd) {
    SAW(3);
    unit(sb, sa, k00, k01, qa0, qa1, oa, la, v11, ob, lb, v10, [&]() {
      v00 = rd(sl0, 2048); v01 = rd(sl0, 3072);
      f0 = *LDS_AT(const volatile unsigned, lbase + FLG);
      SB();
      arrive_w();
      SB(); });
    check(st >= 14 ? 32u : 2u * st + 5u);
    sl0 = sptr((2 * st + 3) & 7);
    SB();
  };
  SB();
  SAW8(0);
  {
    f32x16& sc = sa;
    sb = MFMA32(k00, qb0, nn); SB();
    k10 = rd(sl0, 0); k11 = rd(sl0, 1024); SB();
    sb = MFMA32(k01, qb1, sb); SB();
    EX(0); EX(1); EX(2); EX(3); EX(4); EX(5); EX(6); EX(7); EX(8); SB();
    oa = SMF32(v00, PLO(sa), oa); SB();
    EX(9); EX(10); EX(11); SB();
    la = SMF16(sel, PLO(sa), la); SB();
    EX(12); EX(13); EX(14); EX(15); SB();
    asm volatile("" : "+v"(sa));
  }
  SAW8(1);
  unit(sb, sa, k10, k11, qa0, qa1, oa, la, v01, ob, lb, v00, [&]() { v10 = rd(sl0, 2048); v11 = rd(sl0, 3072); SB(); });
  sl0 = sptr(2);
  SB();
  SAW8(2);
  unit(sa, sb, k10, k11, qb0, qb1, ob, lb, v01, oa, la, v10, [&]() {
    f0 = *LDS_AT(const volatile unsigned, lbase + FLG);
    asm volatile("" ::: "memory");
    k00 = rd(sl0, 0); k01 = rd(sl0, 1024); SB(); });
  if (__builtin_amdgcn_readfirstlane(f0) < 3u) { check(3u); k00 = rd(sl0, 0); k01 = rd(sl0, 1024); }
  SB();
  SAW8(3);
  unit(sb, sa, k00, k01, qa0, qa1, oa, la, v11, ob, lb, v10, [&]() {
    v00 = rd(sl0, 2048); v01 = rd(sl0, 3072); f0 = *LDS_AT(const volatile unsigned, lbase + FLG); SB(); arrive_w(); SB(); });
  check(5u);
  sl0 = sptr(3);
  SB();
  three(1, true);
#pragma unroll 1
  for (int st = 2; st < 16; st += 2) { fourth(st - 1, true); three(st, false); fourth(st, false); three(st + 1, true); }
  SAW8(7);
  unsigned vr, tk, tv;
  u32x4 f4;
  auto table = [&]() {
    f4 = lds_rd4(lbase + FLG);
    k00 = *LDS_AT(const volatile s16x8, tk); k01 = *LDS_AT(const volatile s16x8, tk + 32);
    vr = *LDS_AT(const volatile u16, tv);
  };
  unsigned xw, xm;
  u32x2 ml;
  u32x4 o2[4];
  auto take = [&]() {
    ml = *LDS_AT(const volatile u32x2, xm);
#pragma unroll
    for (int e = 0; e < 4; ++e) o2[e] = lds_rd4(xw + e * 1024);
  };
  float pzt;
  int dso;
  f32x16& sc = sb;
#define EXP() do { asm volatile("" : "+v"(sc)); SB(); } while (0)
  oa = SMF32(v11, PHI(sa), oa); SB();
  tk = lbase + TAB + 128 + hh * 64 + (lane >> 5) * 16; tv = lbase + TAB + 256 + hh * 64 + (lane & 31) * 2;
  table();
  SB();
  EX(0); EX(1); EXP();
  la = SMF16(sel, PHI(sa), la); SB();
  EX(2); EX(3); EX(4); EXP();
  xw = wbase + (qg ? QOFF : 6 * ASB); xm = lbase + XML + wid * 512 + lane * 8;
  pzt = __builtin_amdgcn_exp2f(nn[0] + (zoff - 8.f));
  asm volatile("" : "+v"(xw), "+v"(xm), "+v"(pzt), "+v"(f4));
  SB();
  {
    const unsigned ph = LAY2 ? 0x79797979u : 0x80808080u, pg = qg ? 0u : 0x80u << (16 * hh + 8);
    auto ready = [&]() {
      return (((__builtin_amdgcn_readfirstlane(f4[1]) + ph) & 0x80808080u) == 0x80808080u) & (((__builtin_amdgcn_readfirstlane(g ? f4[3] : f4[2]) + 0x71717171u) & pg) == pg);
    };
    if (__builtin_expect(!ready(), 0)) {
#pragma unroll 1
      for (int i = 0; i < SPIN_MAX; ++i) {
        __builtin_amdgcn_s_sleep(1);
        table();
        if (ready()) break;
      }
    }
    asm volatile("" ::: "memory");
  }
  SB();
  sa = MFMA32(k00, qb0, nn); asm volatile("" : "+v"(sa)); SB();
#pragma unroll
  for (int e = 0; e < 4; ++e) *LDS_AT(volatile f32x4, xw + e * 1024) = f32x4{oa[4 * e], oa[4 * e + 1], oa[4 * e + 2], oa[4 * e + 3]};
  *LDS_AT(volatile u32x2, xm) = u32x2{__float_as_uint(ma), f2word(pzt <= la[0] ? la[0] : 0.f)};
  SB();
  EX(5); EX(6); EX(7); EXP();
  sa = MFMA32(k01, qb1, sa); asm volatile("" : "+v"(sa), "+v"(vr)); SB();
  ARGS_HERE;
  EX(8); EX(9); EX(10); EXP();
  ob = SMF32(v10, PLO(sb), ob); SB();
  EX(11); EX(12); EX(13); EXP();
  lb = SMF16(sel, PLO(sb), lb); SB();
  const bool used = !LAY2 || (qt + g) * 32 + (lane & 31) != idx;
  dso = used ? (((qt + g) * 32 + head) * 1024 + lane * 8) * 2 : S_ * D_ * 2;
  xw += g ? -4096 : 4096; xm += g ? -2048 : 2048;
  asm volatile("" : "+v"(dso), "+v"(xw), "+v"(xm));
  SB();
  EX(14); EX(15); EXP();
  take();
  SB();
  lb = SMF16(sel, PHI(sb), lb); SB();
  ob = SMF32(v11, PHI(sb), ob); SB();
  __builtin_amdgcn_s_setprio(EPRIO);
  TE(0, 0); TE(1, 0); TE(2, 0); TE(3, 0); TE(7, 0);
  float lsb = lb[0];
  if (LAY2) {
    sa[0] = __builtin_amdgcn_exp2f(sa[0]);
    ob = SMF32((u32x4{lane < 32 ? vr : 0u, 0u, 0u, 0u}), PLO(sa), ob);
    lsb += __uint_as_float(__float_as_uint(sa[0]) & 0xffff0000u);
    lsb = pzt <= lsb ? lsb : 0.f;
  }
  const __amdgpu_buffer_rsrc_t rso = __builtin_amdgcn_make_buffer_rsrc(Of, 0, S_ * D_ * 2, 0x00020000);
  TE(0, 1);
  if (__builtin_amdgcn_ballot_w64(ml[1] == SENT) != 0ull) {
#pragma unroll 1
    for (int i = 0; i < SPIN_MAX; ++i) {
      __builtin_amdgcn_s_sleep(XSLP);
      if (!__builtin_amdgcn_ballot_w64(*LDS_AT(const volatile unsigned, xm + 4) == SENT)) break;
    }
    asm volatile("s_waitcnt lgkmcnt(0)" ::: "memory");
    take();
  }
  arrive();
  TE(0, 2);
  TE(0, 3); TE(1, 1);
  {
    const float m2 = __uint_as_float(ml[0]), l2 = __uint_as_float(ml[1]);
    const float mx = fmaxf(ma, m2);
    const float f1 = __builtin_amdgcn_exp2f(ma - mx), f2 = __builtin_amdgcn_exp2f(m2 - mx);
    const float inv = frcp(lsb * f1 + l2 * f2);
    const float c1 = f1 * inv, c2 = f2 * inv;
    f32x2 r[8];
#pragma unroll
    for (int e = 0; e < 8; ++e) {
      const unsigned x0 = o2[e >> 1][(e & 1) * 2], x1 = o2[e >> 1][(e & 1) * 2 + 1];
      r[e] = f32x2{__uint_as_float(x0) * c2, __uint_as_float(x1) * c2};
    }
#pragma unroll
    for (int e = 0; e < 8; ++e) r[e] = f32x2{ob[2 * e], ob[2 * e + 1]} * f32x2{c1, c1} + r[e];
    auto put = [&]() {
#pragma unroll
      for (int sx = 0; sx < 2; ++sx)
        __builtin_amdgcn_raw_buffer_store_b128(u32x4{pack2(r[4 * sx][0], r[4 * sx][1]), pack2(r[4 * sx + 1][0], r[4 * sx + 1][1]),
            pack2(r[4 * sx + 2][0], r[4 * sx + 2][1]), pack2(r[4 * sx + 3][0], r[4 * sx + 3][1])}, rso, dso + sx * 1024, 0, 16);
    };
    put();
    PIN();
    __builtin_amdgcn_s_setprio(0);
    PIN();
    f32x2 chk = {lsb - __builtin_amdgcn_fmed3f(lsb, 8.6736174e-19f, 1.2676506e30f), l2 - __builtin_amdgcn_fmed3f(l2, 8.6736174e-19f, 1.2676506e30f)};
#pragma unroll
    for (int e = 0; e < 8; ++e) chk = r[e] * f32x2{0.f, 0.f} + chk;
    if (__builtin_expect(__builtin_amdgcn_ballot_w64(used && !(chk[0] + chk[1] == 0.f)) != 0ull, 0)) {
      float l;
      attn_safe(KV, head * 64, qb0, qb1, ob, l, LAY2 ? head * 64 + (idx >> 5) : -1, idx & 31, lbase + TAB + 128 + hh * 64);
      const float inv2 = 1.0f / l;
#pragma unroll
      for (int e = 0; e < 8; ++e) r[e] = f32x2{ob[2 * e] * inv2, ob[2 * e + 1] * inv2};
      asm volatile("s_waitcnt vmcnt(0)" ::: "memory");
      put();
    }
    unsigned pp = 0u;
    if (LAY2) {
      const unsigned ua = lbase + UGL + hh * 128 + (lane >> 5) * 16;
      f32x2 du = {0.f, 0.f}, dn = {0.f, 0.f};
#pragma unroll
      for (int q = 0; q < 4; ++q) {
        const f32x4 uu = *LDS_AT(const f32x4, ua + q * 32), gg = *LDS_AT(const f32x4, ua + 256 + q * 32);
        du = r[2 * q] * f32x2{uu[0], uu[1]} + du; du = r[2 * q + 1] * f32x2{uu[2], uu[3]} + du;
        const f32x2 t0 = r[2 * q] * f32x2{gg[0], gg[1]}, t1 = r[2 * q + 1] * f32x2{gg[2], gg[3]};
        dn = t0 * t0 + dn; dn = t1 * t1 + dn;
      }
      const auto a = __builtin_amdgcn_permlane32_swap(__float_as_uint(du[0] + du[1]), __float_as_uint(du[0] + du[1]), false, false);
      const auto c = __builtin_amdgcn_permlane32_swap(__float_as_uint(dn[0] + dn[1]), __float_as_uint(dn[0] + dn[1]), false, false);
      pp = f2word(__uint_as_float(pack2(__uint_as_float(a[0]) + __uint_as_float(a[1]), __uint_as_float(c[0]) + __uint_as_float(c[1]))));
    }
    __builtin_amdgcn_raw_buffer_store_b32(pp, __builtin_amdgcn_make_buffer_rsrc(hand + 2 * HAND_N + PP_O, 0, LAY2 ? H_ * S_ * 4 : 0, 0x00020000),
                                          lane < 32 ? (head * S_ + (qt + g) * 32 + lane) * 4 : H_ * S_ * 4, 0, 16);
    PIN();
  }
  TE(1, 2);
  u32x4 xres[2];
  oproj_tail(LAY2, tid, lbase, lbase + FLG, omi, oni, idx, gran + GR_O + ((LAY2 ? 16 : 0) + tmi) * (64 * RO) + tni,
             gran + GR_X + omi * (128 * RX) + oni * 4, tag, Of, xout, A, w_r, stats_out, stats, hand, xres TEPASS);
  if (LAY2) {
    const int row = omi * 128 + (wid & 3) * 32 + (wid >> 2) * 16 + (lane >> 3), col = oni * 64 + (lane & 7) * 8;
    unsigned md[2], best = 0u;
    const __amdgpu_buffer_rsrc_t rsk = __builtin_amdgcn_make_buffer_rsrc(hand + HAND_N, 0, (HAND_N + PS_N) * 4, 0x00020000);
    f32x2 xf[2][4];
#pragma unroll
    for (int p = 0; p < 2; ++p)
#pragma unroll
      for (int e = 0; e < 4; ++e) {
        xf[p][e] = f32x2{__uint_as_float(xres[p][e] << 16), __uint_as_float(xres[p][e] & 0xffff0000u)};
        asm volatile("" : "+v"(xf[p][e]));
      }
    LOGDEF;
    LOG();
    const bool rtr = (oni & 7) == 7;
    {
      const int rl = (wid & 3) * 32 + (wid >> 2) * 16 + (lane & 15), rr = omi * 128 + rl;
      int src = (HAND_N + ((omi * 16 + (lane >> 4) * 4) * 128 + rl) * 4) * 4;
      int ko = rtr && (wid >> 2) == (oni >> 3) && lane < NREP ? lane * 1024 + (rr >> 4) * 8 : (HAND_N + PS_N) * 4;
      int bp = (lane >> 3) * 4;
      const float wsum = *LDS_AT(const volatile float, lbase + SPM + 132);
      asm volatile("" : "+v"(src), "+v"(ko), "+v"(bp));
      u32x4 v[4];
      __builtin_amdgcn_s_sleep(MPRE);
#pragma unroll 1
      for (int spin = 0; spin < SPIN_MAX; ++spin) {
        LOG();
#pragma unroll
        for (int i = 0; i < 4; ++i) v[i] = __builtin_amdgcn_raw_buffer_load_b128(rsk, src + (i & 1) * 2048, (i >> 1) * 4096, 16);
        unsigned mx = 0u;
#pragma unroll
        for (int i = 0; i < 4; ++i) mx = umax(umax(umax(mx, v[i][0]), v[i][1]), v[i][2]);
        if (none(mx == SENT)) break;
        __builtin_amdgcn_s_sleep(MSLP);
        asm volatile("" ::: "memory");
      }
      LOGAT(12);
      if (rtr) __builtin_amdgcn_s_setprio(3); else __builtin_amdgcn_s_setprio(LNPRIO);
      double sm[3];
#pragma unroll
      for (int c = 0; c < 3; ++c) {
        const double a = ((double)__uint_as_float(v[0][c]) + (double)__uint_as_float(v[1][c])) + ((double)__uint_as_float(v[2][c]) + (double)__uint_as_float(v[3][c]));
        u32x2 q = __builtin_bit_cast(u32x2, a);
        const auto lo = __builtin_amdgcn_permlane32_swap(q[0], q[0], false, false), hi = __builtin_amdgcn_permlane32_swap(q[1], q[1], false, false);
        q = __builtin_bit_cast(u32x2, __builtin_bit_cast(double, u32x2{lo[0], hi[0]}) + __builtin_bit_cast(double, u32x2{lo[1], hi[1]}));
        const auto l2 = __builtin_amdgcn_permlane16_swap(q[0], q[0], false, false), h2 = __builtin_amdgcn_permlane16_swap(q[1], q[1], false, false);
        sm[c] = __builtin_bit_cast(double, u32x2{l2[0], h2[0]}) + __builtin_bit_cast(double, u32x2{l2[1], h2[1]});
      }
      float mean, var, mb, den;
      ln_from_sums(sm[0], sm[1], mean, var, mb, den);
      const unsigned mdr = f2word(__uint_as_float((__float_as_uint(mb) >> 16) | (__float_as_uint(den) & 0xffff0000u)));
      md[0] = __builtin_amdgcn_ds_bpermute(bp, mdr); md[1] = __builtin_amdgcn_ds_bpermute(bp + 32, mdr);
      if (rtr) {
        const float r = (float)fma(-(double)mb, (double)wsum, sm[2]) * frcp(den);
        const unsigned rb = pack2(r, r);
        unsigned rkey = (rb ^ ((unsigned)((int)rb >> 31) | 0x80000000u)) & 0xffff0000u;
        if (r != r) rkey = 0u;
        const unsigned top = wave_umax(rkey | (unsigned)(2047 - rr));
        const unsigned mdt = __builtin_amdgcn_readlane(mdr, (2047 - (int)(top & 0xffffu)) & 15);
        __builtin_amdgcn_raw_buffer_store_b64(u32x2{top, mdt}, rsk, ko, 0, 16);
        PIN();
        __builtin_amdgcn_s_setprio(LNPRIO);
        __hip_atomic_fetch_add(LDS_AT(unsigned, lane == 0 ? lbase + SPM + 128 : lbase + DUM + lane * 4), 1u, __ATOMIC_RELAXED, __HIP_MEMORY_SCOPE_WORKGROUP);
        if (wid == 2 || wid == 5) STAMP(8, 6);
      }
      LOGAT(13);
    }
    auto keys = [&](const u32x4& v) __attribute__((always_inline)) {
      if (none(umax(umax(v[0], v[1]), umax(v[2], v[3])) == SENT)) {
        best = wave_umax(umax(v[0], v[2])) | 0x8000u;
        *LDS_AT(volatile u32x2, lbase + FLG + 40) = u32x2{best, (unsigned)__builtin_amdgcn_readlane(v[0] > v[2] ? v[1] : v[3], ((2047 - (int)(best & 0x7ffu)) >> 5) & 63)};
        STAMP(8, 11);
      }
    };
    auto ln_rows = [&]() __attribute__((always_inline)) {
#pragma unroll
      for (int p = 0; p < 2; ++p) {
        const float mb_ = __uint_as_float(md[p] << 16), rin = frcp(__uint_as_float(md[p] & 0xffff0000u));
#pragma unroll
        for (int e = 0; e < 4; ++e) {
          const f32x2 h = (xf[p][e] - f32x2{mb_, mb_}) * f32x2{rin, rin} + xf[p][e];
          xres[p][e] = pack2(h[0], h[1]);
        }
      }
      asm volatile("" : "+v"(xres[0]), "+v"(xres[1]));
    };
    if (wid == 0) STAMP(8, 12);
    ln_rows();
    LOGAT(14);
    const __amdgpu_buffer_rsrc_t rsu = __builtin_amdgcn_make_buffer_rsrc(out, 0, S_ * D_ * 2, 0x00020000);
    unsigned fr1 = SENT;
#pragma unroll 1
    for (int i = 0; i < 4 * SPIN_MAX && fr1 == SENT; ++i) fr1 = __builtin_amdgcn_readfirstlane(*LDS_AT(const volatile unsigned, lbase + SPM + 144));
    const int fr2 = (fr1 >> 16) & 0x7ffu;
    fr1 &= 0x7ffu;
    unsigned tmx = __builtin_amdgcn_readfirstlane(*LDS_AT(const volatile unsigned, lbase + SPM + 136)) + KDT;
    if (tmx - (unsigned)__builtin_amdgcn_s_memrealtime() > 1024u) tmx = 0u;
    auto due = [&]() { return tmx == 0u || (int)((unsigned)__builtin_amdgcn_s_memrealtime() - tmx) >= 0; };
    lk_set(15);
    const bool held = !none((row | 8) == ((int)fr1 | 8) || (row | 8) == (fr2 | 8));
#pragma unroll
    for (int p = 0; p < 2; ++p) __builtin_amdgcn_raw_buffer_store_b128(xres[p], rsu, row + 8 * p != (int)fr1 && row + 8 * p != fr2 ? ((row + 8 * p) * D_ + col) * 2 : S_ * D_ * 2, 0, OAUX);
    PIN();
    if (wid == 6) {
      if (rtr) {
#pragma unroll 1
        for (int i = 0; i < SPIN_MAX; ++i) {
          if (__builtin_amdgcn_readfirstlane(*LDS_AT(const volatile unsigned, lbase + SPM + 128)) == 7u) break;
          __builtin_amdgcn_s_sleep(1);
        }
        asm volatile("" ::: "memory");
      }
      while (!due()) __builtin_amdgcn_s_sleep(1);
    }
#pragma unroll 1
    for (int i = 0; i < 4 * SPIN_MAX && !best; ++i) {
      if (wid == 6) {
        LOG();
        keys(__builtin_amdgcn_raw_buffer_load_b128(rsk, (bid % NREP) * 1024 + lane * 16, 0, 16));
        if (!best) __builtin_amdgcn_s_sleep(KSLP);
      } else {
        best = __builtin_amdgcn_readfirstlane(*LDS_AT(const volatile unsigned, lbase + FLG + 40));
        if (!best) __builtin_amdgcn_s_sleep(1);
      }
      asm volatile("" ::: "memory");
    }
    const int idx2 = 2047 - (int)(best & 0x7ffu);
    if (held) {
#pragma unroll
      for (int p = 0; p < 2; ++p) {
        const int r = row + 8 * p;
        __builtin_amdgcn_raw_buffer_store_b128(xres[p], rsu, (r == (int)fr1 || r == fr2) && r != idx2 ? (r * D_ + col) * 2 : S_ * D_ * 2, 0, OAUX);
      }
    }
    if (__builtin_expect(idx2 != (int)fr1 && idx2 != fr2, 0) && !none((row | 8) == (idx2 | 8))) {
#pragma unroll
      for (int p = 0; p < 2; ++p)
        __builtin_amdgcn_raw_buffer_store_b128(u32x4{pack2(xf[p][0][0], xf[p][0][1]), pack2(xf[p][1][0], xf[p][1][1]), pack2(xf[p][2][0], xf[p][2][1]), pack2(xf[p][3][0], xf[p][3][1])},
            __builtin_amdgcn_make_buffer_rsrc(xout, 0, 3 * D_ * 2, 0x00020000), row + 8 * p == idx2 ? (2 * D_ + col) * 2 : S_ * D_ * 2, 0, 16);
      asm volatile("s_waitcnt vmcnt(0)" ::: "memory");
      if (lane == 0) st_ag(gran + GR_F + oni * 32, tag);
    }
  }
  }
}
struct Args2 { Args l[2]; };
extern "C" __global__ __launch_bounds__(768, 3) void layers_kernel(const Args2 args) {
  __shared__ __attribute__((aligned(128))) char smem[163840];
  const unsigned long long t_entry = 0;
  const int tid = threadIdx.x, bid = blockIdx.x;
  int wid = __builtin_amdgcn_readfirstlane(tid >> 6);
  asm volatile("" : "+s"(wid));
  const ArgP ap = (ArgP)__builtin_amdgcn_kernarg_segment_ptr();
  layer_body(wid, bid, (unsigned)(uintptr_t)smem, ap, t_entry);
  LAYER_HERE;
  if (!LAY2) {
    asm volatile(AGAIN :: "{s[0:1]}"(ap + 1), "{s2}"(bid), "{v0}"(wid * 64) : "s90", "s91", "scc", "memory");
    __builtin_unreachable();
  }
}

extern "C" __global__ void end_marker_kernel() {}

extern "C" void kernel_launch(void* const* d_in, const int* in_sizes, int n_in, void* d_out, int out_size,
                              void* d_ws, size_t ws_size, hipStream_t stream) {
  const u16* x0 = (const u16*)d_in[0];
  const u16* wqkv = (const u16*)d_in[1];
  const u16* wo = (const u16*)d_in[2];
  const u16* w_r = (const u16*)d_in[4];
  const u16* wg = (const u16*)d_in[5];
  const u16* wu = (const u16*)d_in[6];
  const u16* wd = (const u16*)d_in[7];
  u16* out = (u16*)d_out;

  char* ws = (char*)d_ws;
  const size_t MB4 = (size_t)S_ * D_ * 2;
  u16* kv1 = (u16*)(ws);
  u16* o1 = (u16*)(ws + 2 * MB4);
  u16* x1 = (u16*)(ws + 3 * MB4);
  unsigned* gran = (unsigned*)(ws + 4 * MB4);
  float* csf = (float*)(gran + GR_F + 16 * 32);
  double* stats = (double*)(csf + 8192);
  unsigned* hand = (unsigned*)(stats + 2 * STAT_N);
  static_assert((GR_F + 16 * 32) % 32 == 0 && (size_t)(GR_F + 16 * 32 + 8192 + 4 * STAT_N + 2 * HAND_N + PS_N) * 4 <= 6u << 20, "");
  u16* x3 = (u16*)(hand + HAND_N + GRAN_N + Y_N);
  u16* wc = (u16*)((((uintptr_t)ws + 4 * MB4 + (6u << 20) + 2047) & ~(uintptr_t)2047) + (((uintptr_t)wqkv + 128) & 2047));
  u16* kv2 = (u16*)(ws + 8 * MB4);
  u16* o2 = (u16*)(ws + 10 * MB4);
  Args2 a;
  for (int l = 0; l < 2; ++l) {
    Args& r = a.l[l];
    r.lay = l; r.a = l ? x1 : x0; r.kv = l ? kv2 : kv1; r.o = l ? o2 : o1; r.xo = l ? x3 : x1; r.so = l ? stats + STAT_N : stats;
    r.W = wqkv; r.wc = wc; r.wg = wg; r.wu = wu; r.wd = wd; r.wo = wo; r.w_r = w_r; r.gran = gran; r.csf = csf; r.stats = stats; r.hand = hand; r.out = out;
  }
  layers_kernel<<<256, 768, 0, stream>>>(a);
}
```

```cpp
#include <hip/hip_runtime.h>
#include <hip/hip_bf16.h>
#include <stdint.h>
#include <type_traits>
#include <utility>

typedef unsigned short u16;
typedef short s16x4 __attribute__((ext_vector_type(4)));
typedef short s16x8 __attribute__((ext_vector_type(8)));
typedef float f32x16 __attribute__((ext_vector_type(16)));
typedef float f32x4 __attribute__((ext_vector_type(4)));
typedef unsigned int u32x4 __attribute__((ext_vector_type(4)));
typedef unsigned int u32x2 __attribute__((ext_vector_type(2)));
typedef __bf16 bf16x8 __attribute__((ext_vector_type(8)));
typedef __bf16 bf16x16 __attribute__((ext_vector_type(16)));

__device__ __forceinline__ int fresh_lane() {
  int l;
  asm volatile("v_mbcnt_lo_u32_b32 %0, -1, 0\n\tv_mbcnt_hi_u32_b32 %0, -1, %0" : "=v"(l));
  return l;
}
#define STAMP(k, i)
#define STAMPV(k, i, v)
#define LOGDEF
#define LOG()
#define LOGAT(k)
#define lk_set(k)
#define LOGOUT(slot)
#define TE(grp, k)
#define TEARG
#define TEPASS
constexpr int S_ = 2048, D_ = 1024, H_ = 32, F_ = 3584;

__device__ __forceinline__ int kpos(int r) { return (r & ~12) | ((r & 4) << 1) | ((r & 8) >> 1); }
__device__ __forceinline__ float bf2f(u16 v) { return __uint_as_float(((unsigned)v) << 16); }
__device__ __forceinline__ u16 f2bf(float f) {
  __hip_bfloat16 b = __float2bfloat16(f);
  return *reinterpret_cast<u16*>(&b);
}
typedef float f32x2 __attribute__((ext_vector_type(2)));
typedef __bf16 bf16x2 __attribute__((ext_vector_type(2)));
__device__ __forceinline__ unsigned pack2(float lo, float hi) {
  f32x2 v = {lo, hi};
  return __builtin_bit_cast(unsigned, __builtin_convertvector(v, bf16x2));
}
__device__ __forceinline__ float rbf(float f) { return __uint_as_float(pack2(f, f) << 16); }
__device__ __forceinline__ void rbf2(float& a, float& b) {
  const unsigned p = pack2(a, b);
  a = __uint_as_float(p << 16); b = __uint_as_float(p & 0xffff0000u);
}

__device__ __forceinline__ float fadd(float a, float b) { float r = a + b; asm("" : "+v"(r)); return r; }
template <int CTRL, int RMASK>
__device__ __forceinline__ float dpp_add(float v) {
  const int t = __builtin_amdgcn_update_dpp(0, __float_as_int(v), CTRL, RMASK, 0xf, false);
  return v + __int_as_float(t);
}
__device__ __forceinline__ float wave_sum(float v) {
  v = dpp_add<0x111, 0xf>(v);
  v = dpp_add<0x112, 0xf>(v);
  v = dpp_add<0x114, 0xf>(v);
  v = dpp_add<0x118, 0xf>(v);
  v = dpp_add<0x142, 0xa>(v);
  v = dpp_add<0x143, 0xc>(v);
  return __int_as_float(__builtin_amdgcn_readlane(__float_as_int(v), 63));
}

template <int CTRL, int RMASK>
__device__ __forceinline__ unsigned dpp_umax(unsigned v) {
  const unsigned t = (unsigned)__builtin_amdgcn_update_dpp(0, (int)v, CTRL, RMASK, 0xf, false);
  return v > t ? v : t;
}
__device__ __forceinline__ unsigned wave_umax(unsigned v) {
  v = dpp_umax<0x111, 0xf>(v); v = dpp_umax<0x112, 0xf>(v); v = dpp_umax<0x114, 0xf>(v); v = dpp_umax<0x118, 0xf>(v);
  v = dpp_umax<0x142, 0xa>(v); v = dpp_umax<0x143, 0xc>(v);
  return (unsigned)__builtin_amdgcn_readlane((int)v, 63);
}

#define NREP 32
constexpr unsigned SENT = 0xFFFFFFFFu;
constexpr int GRAN_N = 256 * NREP;
constexpr int Y_N = F_ / 16 * D_;
constexpr int X2_N = D_ / 2;
constexpr int XS_N = 128;
constexpr int P4_N = 3 * D_ * 16;
constexpr int PA_N = H_ * 8 * 64;
constexpr int HAND_N = GRAN_N + Y_N + X2_N + XS_N + P4_N + PA_N;
constexpr int MD_O = 16 * 16 * 128 * 4;
constexpr int PK_O = MD_O;
constexpr int PP_O = PK_O + 128 * NREP;
constexpr int Y3_O = PP_O + H_ * S_;
constexpr int PS_N = Y3_O + Y_N;
constexpr int GR_ROW = 64, GR_COL = GR_ROW + 4 * S_, GR_KV = GR_COL + 16 * 16 * 192;
#define RO 16
#define RK 16
#define RX 1
constexpr int GR_O = GR_KV + 2 * 16 * 32 * RK, GR_X = GR_O + 2 * 16 * 64 * RO, GR_N = GR_X + 16 * 128 * RX;
constexpr int GR_F = GR_N + 256 * 64;
constexpr int STAT_N = 3 * S_;

__device__ __forceinline__ void st_ag(unsigned* p, unsigned v) { __hip_atomic_store(p, v, __ATOMIC_RELAXED, __HIP_MEMORY_SCOPE_AGENT); }
__device__ __forceinline__ unsigned ld_ag(const unsigned* p) { return __hip_atomic_load(p, __ATOMIC_RELAXED, __HIP_MEMORY_SCOPE_AGENT); }
__device__ __forceinline__ double ld_ag(const double* p) { return __hip_atomic_load(p, __ATOMIC_RELAXED, __HIP_MEMORY_SCOPE_AGENT); }
__device__ __forceinline__ unsigned f2word(float f) { const unsigned u = __float_as_uint(f); return u < 0xFFFFFFFEu ? u : 0xFFFFFFFEu; }
constexpr int SPIN_MAX = 1 << 18;
__device__ __forceinline__ unsigned umax(unsigned a, unsigned b) { return a > b ? a : b; }
__device__ __forceinline__ bool none(bool p) { return __builtin_amdgcn_ballot_w64(p) == 0ull; }

__device__ __forceinline__ void ln_from_sums(double s1, double s2, float& mean, float& var, float& mb, float& den) {
  const double md = s1 * (1.0 / 1024.0);
  const double vd = fma(-md, md, s2 * (1.0 / 1024.0));
  mean = (float)md; var = fmaxf((float)vd, 0.f);
  mb = rbf(mean);
  den = rbf(__builtin_amdgcn_sqrtf(rbf(rbf(var) + 1e-5f)));
}
__device__ __forceinline__ float frcp(float x) { return __builtin_amdgcn_rcpf(x); }
__device__ __forceinline__ void ln_apply2(float& a, float& b, float mb, float den, float rin) {
  a -= mb; b -= mb;
  rbf2(a, b);
  float qa = a * rin, qb = b * rin;
  qa = fmaf(fmaf(-qa, den, a), rin, qa);
  qb = fmaf(fmaf(-qb, den, b), rin, qb);
  rbf2(qa, qb);
  a = qa; b = qb;
}

#define CODE_ADDR(SYM, out64)                                                                                          \
  do {                                                                                                                 \
    unsigned lo_, hi_;                                                                                                 \
    asm volatile("s_getpc_b64 s[90:91]\n\ts_add_u32 s90, s90, " SYM "@rel32@lo+4\n\ts_addc_u32 s91, s91, " SYM          \
                 "@rel32@hi+12\n\ts_mov_b32 %0, s90\n\ts_mov_b32 %1, s91"                                             \
                 : "=s"(lo_), "=s"(hi_) :: "s90", "s91", "scc");                                                        \
    out64 = ((unsigned long long)hi_ << 32) | lo_;                                                                     \
  } while (0)

#define QNX 4
constexpr int QMX = 32 / QNX, QNG = 16 / QNX, QMG = 8 / QNG;

__device__ __forceinline__ unsigned touch(const void* p) { return *reinterpret_cast<const unsigned*>(p); }

__device__ __forceinline__ unsigned touch_code(unsigned long long lo, unsigned long long hi, int pc, int lane) {
  const unsigned long long p = lo + (unsigned)(pc * 8192 + lane * 128);
  return p + 4 <= hi ? *reinterpret_cast<__attribute__((address_space(1))) const unsigned*>(p) : 0u;
}

template <bool SC = false>
__device__ __forceinline__ void glds16s(const void* sbase, unsigned voff, unsigned lds_dst) {
  unsigned keep;
  if (SC)
    asm volatile("s_mov_b32 %0, m0\n\ts_mov_b32 m0, %3\n\ts_nop 2\n\tglobal_load_lds_dwordx4 %1, %2 sc1\n\ts_mov_b32 m0, %0"
                 : "=&s"(keep) : "v"(voff), "s"(sbase), "s"(lds_dst) : "memory");
  else
  asm volatile("s_mov_b32 %0, m0\n\ts_mov_b32 m0, %3\n\ts_nop 2\n\tglobal_load_lds_dwordx4 %1, %2\n\ts_mov_b32 m0, %0"
               : "=&s"(keep) : "v"(voff), "s"(sbase), "s"(lds_dst) : "memory");
}
template <bool SC>
__device__ __forceinline__ void glds16x4(const void* sbase, unsigned v0, unsigned v1, unsigned v2, unsigned v3, unsigned lds_dst) {
  unsigned keep;
  if (SC)
    asm volatile("s_mov_b32 %0, m0\n\ts_mov_b32 m0, %6\n\ts_nop 2\n\tglobal_load_lds_dwordx4 %1, %5 sc1\n\tglobal_load_lds_dwordx4 %2, %5 offset:1024 sc1\n\t"
                 "global_load_lds_dwordx4 %3, %5 offset:2048 sc1\n\tglobal_load_lds_dwordx4 %4, %5 offset:3072 sc1\n\ts_mov_b32 m0, %0"
                 : "=&s"(keep) : "v"(v0), "v"(v1), "v"(v2), "v"(v3), "s"(sbase), "s"(__builtin_amdgcn_readfirstlane(lds_dst)) : "memory");
  else
    asm volatile("s_mov_b32 %0, m0\n\ts_mov_b32 m0, %6\n\ts_nop 2\n\tglobal_load_lds_dwordx4 %1, %5\n\tglobal_load_lds_dwordx4 %2, %5 offset:1024\n\t"
                 "global_load_lds_dwordx4 %3, %5 offset:2048\n\tglobal_load_lds_dwordx4 %4, %5 offset:3072\n\ts_mov_b32 m0, %0"
                 : "=&s"(keep) : "v"(v0), "v"(v1), "v"(v2), "v"(v3), "s"(sbase), "s"(__builtin_amdgcn_readfirstlane(lds_dst)) : "memory");
}
constexpr float QSCALE = 0.17677669529663687f * 1.4426950408889634f;

__device__ __forceinline__ unsigned min4(u32x4 v) { const unsigned a = v[0] < v[1] ? v[0] : v[1], b = v[2] < v[3] ? v[2] : v[3]; return a < b ? a : b; }
#define LDS_AT(T, a) ((__attribute__((address_space(3))) T*)(uintptr_t)(a))
__device__ __forceinline__ u32x4 lds_rd4(unsigned a) { return *LDS_AT(const volatile u32x4, a); }
__device__ __forceinline__ void lds_wr(unsigned a, unsigned v) { *LDS_AT(volatile unsigned, a) = v; }
__device__ __forceinline__ void split3(float v, unsigned& p0, unsigned& p1, unsigned& p2) {
  p0 = pack2(v, v) & 0xffffu; v -= __uint_as_float(p0 << 16);
  p1 = pack2(v, v) & 0xffffu; v -= __uint_as_float(p1 << 16);
  p2 = pack2(v, v) & 0xffffu;
}

#define PIN() __builtin_amdgcn_sched_barrier(0)
template <int B, class F, int... I>
__device__ __forceinline__ void static_for_(F&& f, std::integer_sequence<int, I...>) { (f(std::integral_constant<int, B + I>{}), ...); }
template <int B, int E, class F>
__device__ __forceinline__ void static_for(F&& f) { static_for_<B>(f, std::make_integer_sequence<int, E - B>{}); }

#define TPRE 60
#define TPSL 0
#define FCPRE 44
#define FCSLP 2
#define SIGAT 24
#define XPRIO 1
#define OPD 6
#define LAUX 16
#define CSLP 2
#define EPRIO 3
#define OPRIO 1
#define XSLP 1
#define CSL1 8
#define XAUX 16
__device__ __forceinline__ void oproj_tail(const bool RES2, const int tid, const unsigned lbase, const unsigned flg, const int tmi, const int tni, const int idx,
                                           unsigned* osig, unsigned* xsig, const unsigned tag,
                                           const u16* A, u16* out,
                                           const u16* res, const u16* __restrict__ w_r, double* stats,
                                           const double* stats_in, unsigned* hand, u32x4 (&xres)[2] TEARG) {
  constexpr int ldw = D_, PD = OPD;
  const int lane = tid & 63, wid = __builtin_amdgcn_readfirstlane(tid >> 6);
  const int m0 = tmi * 128, n0 = tni * 64;
  const int dk = RES2 ? 7 : 3;
  if (wid == 0) STAMP(dk, 0);
  TE(1, 3);
  asm volatile("" : "+s"(A) :: "memory");
  PIN();
  const int row = m0 + (wid & 3) * 32 + (wid >> 2) * 16 + (lane >> 3), col = n0 + (lane & 7) * 8;
  const unsigned tb = lbase + 147456;
  typedef __attribute__((address_space(1))) double* gdp;
  typedef __attribute__((address_space(1))) unsigned* gup;
  unsigned tr, oad;
  gdp pst;
  gup pps;
  int xo;
  auto setup = [&]() __attribute__((always_inline)) {
    tr = tb + (row - m0) * 128 + (((lane ^ (lane >> 3)) & 7) << 4);
    pst = (gdp)(stats + ((lane & 3) == 3 ? 0 : (lane & 3)) * S_ + row + 8 * ((lane >> 2) & 1));
    pps = (gup)(hand + 2 * HAND_N + ((tmi * 16 + tni) * 128 + row - m0 + 8 * ((lane >> 2) & 1)) * 4 + (lane & 3));
    xo = (row * ldw + col) * 2;
    asm volatile("" : "+v"(tr), "+v"(pst), "+v"(pps), "+v"(xo));
    PIN();
  };
  {
    oad = lane == 0 ? flg + 24 : flg + 64 + lane * 4;
    gup ps = (gup)(osig + lane * 64);
    asm volatile("" : "+v"(oad), "+v"(ps));
    asm volatile("s_waitcnt vmcnt(1)" ::: "memory");
    TE(2, 1);
    const unsigned old = __hip_atomic_fetch_add(LDS_AT(unsigned, oad), 1u, __ATOMIC_RELAXED, __HIP_MEMORY_SCOPE_WORKGROUP);
    if (__builtin_amdgcn_readfirstlane(old) == 7u && lane < RO) __hip_atomic_store(ps, tag, __ATOMIC_RELAXED, __HIP_MEMORY_SCOPE_AGENT);
    PIN();
    TE(2, 2);
    if (wid == 0) STAMP(dk, 12);
  }
  auto announce = [&]() __attribute__((always_inline)) {
    asm volatile("" ::: "memory");
    PIN();
    __builtin_amdgcn_s_barrier();
    PIN();
    if (wid == 0) STAMP(dk, 1);
    TE(2, 3); TE(4, 0); TE(7, 1);
  };
  u32x4 rwr;
  auto sums = [&](const u32x4& x0, const u32x4& x1) __attribute__((always_inline)) {
    float f[2][3];
#pragma unroll
    for (int p = 0; p < 2; ++p) {
      f[p][0] = f[p][1] = f[p][2] = 0.f;
#pragma unroll
      for (int e = 0; e < 4; ++e) {
        const unsigned xu = p ? x1[e] : x0[e], wu = rwr[e];
        const bf16x2 x = __builtin_bit_cast(bf16x2, xu);
        f[p][0] = __builtin_amdgcn_fdot2_f32_bf16(x, __builtin_bit_cast(bf16x2, 0x3f803f80u), f[p][0], false);
        f[p][1] = __builtin_amdgcn_fdot2_f32_bf16(x, x, f[p][1], false);
        f[p][2] = __builtin_amdgcn_fdot2_f32_bf16(x, __builtin_bit_cast(bf16x2, wu), f[p][2], false);
      }
    }
    auto addx = [&](float keep, float give, auto ctl) {
      return keep + __int_as_float(__builtin_amdgcn_update_dpp(0, __float_as_int(give), decltype(ctl)::value, 0xf, 0xf, false));
    };
    const bool b2 = lane & 4, b1 = lane & 2, z0 = (lane & 3) == 0, z1 = (lane & 3) == 1;
    float g[3];
#pragma unroll
    for (int c = 0; c < 3; ++c) g[c] = addx(b2 ? f[1][c] : f[0][c], b2 ? f[0][c] : f[1][c], std::integral_constant<int, 0x141>{});
    const float u1 = addx(b1 ? g[2] : g[0], b1 ? g[0] : g[2], std::integral_constant<int, 0x4e>{});
    const float u2 = addx(g[1], g[1], std::integral_constant<int, 0x4e>{});
    return addx(z1 ? u2 : u1, z0 ? u2 : u1, std::integral_constant<int, 0xb1>{});
  };
  if (wid < 4) {
    const __amdgpu_buffer_rsrc_t rsa = __builtin_amdgcn_make_buffer_rsrc(const_cast<u16*>(A), 0, S_ * D_ * 2, 0x00020000);
    int sa0 = (tmi * 4 + wid) * 65536, va0 = (lane >> 4) * 512 + (lane & 15) * 16;
    asm volatile("" : "+v"(va0));
    asm volatile("" : "+s"(sa0));
    auto lda = [&](int i, int h) { return __builtin_amdgcn_raw_buffer_load_b128(rsa, va0 + (i & 1) * 2048 + h * 256, sa0 + (i >> 1) * 4096, LAUX); };
    unsigned wad[4], tw[4];
    f32x4 acc[2][4];
    const int k = (lane >> 5) * 16 + ((lane >> 2) & 7);
#pragma unroll
    for (int j = 0; j < 4; ++j) {
      wad[j] = lbase + k * 128 + (((2 * j) ^ (k & 6)) << 4) + 8 * (lane & 3);
      tw[j] = tb + wid * 4096 + (lane & 15) * 128 + (((2 * j + (lane >> 5)) ^ (lane & 7)) << 4) + ((lane >> 4) & 1) * 8;
      asm volatile("" : "+v"(wad[j]), "+v"(tw[j]));
    }
    auto ldw_ = [&](int i, int j) {
      const unsigned a = wad[j] + (i & 3) * 4096;
      s16x4 lo = __builtin_amdgcn_ds_read_tr16_b64_v4i16(LDS_AT(s16x4, a));
      s16x4 h4 = __builtin_amdgcn_ds_read_tr16_b64_v4i16(LDS_AT(s16x4, a + 1024));
      return (s16x8)__builtin_shufflevector(lo, h4, 0, 1, 2, 3, 4, 5, 6, 7);
    };
    u32x4 id[2];
#pragma unroll
    for (int j = 0; j < 2; ++j) {
#pragma unroll
      for (int e = 0; e < 4; ++e) id[j][e] = (lane >> 4) == 2 * j + ((lane >> 3) & 1) && ((lane & 7) >> 1) == e ? ((lane & 1) ? 0x3f800000u : 0x3f80u) : 0u;
      asm volatile("" : "+v"(id[j]));
    }
    unsigned rad = tb + wid * 4096 + (lane & 15) * 128 + (((lane >> 4) ^ (lane & 7)) << 4), rfa = flg + 36 + wid;
    asm volatile("" : "+v"(rad), "+v"(rfa));
    u32x4 rb[2][2];
    rwr = *reinterpret_cast<const u32x4*>(w_r + col);
    setup();
    asm volatile("" : "+v"(rwr));
    announce();
    u32x4 af[PD][2];
    s16x8 wf[2][4];
#pragma unroll
    for (int i = 0; i < PD; ++i) { af[i][0] = lda(i, 0); af[i][1] = lda(i, 1); }
    PIN();
    __builtin_amdgcn_s_setprio(OPRIO);
#pragma unroll
    for (int q = 0; q < 4; ++q) rb[q >> 1][q & 1] = lds_rd4((rad ^ ((q & 1) * 64)) + (q >> 1) * 2048);
    PIN();
#pragma unroll
    for (int j = 0; j < 4; ++j)
#pragma unroll
      for (int h = 0; h < 2; ++h) { asm volatile("v_mfma_f32_16x16x32_bf16 %0, %1, %2, 0" : "=&v"(acc[h][j]) : "v"(id[j & 1]), "v"(rb[h][j >> 1])); PIN(); }
#pragma unroll 1
    for (int spin = 0; spin < SPIN_MAX; ++spin)
      if (__builtin_amdgcn_readfirstlane(*LDS_AT(const volatile unsigned, flg)) >= 40u) break;
    asm volatile("" : "+v"(va0) :: "memory");
    PIN();
#pragma unroll
    for (int j = 0; j < 4; ++j) wf[0][j] = ldw_(0, j);
    s16x4 wlo;
    static_for<0, 32>([&](auto ic) __attribute__((always_inline)) {
      constexpr int i = decltype(ic)::value;
      if (i == SIGAT) { __hip_atomic_fetch_add(LDS_AT(unsigned, oad + 4), 1u, __ATOMIC_RELAXED, __HIP_MEMORY_SCOPE_WORKGROUP); PIN(); }
#pragma unroll
      for (int g = 0; g < 8; ++g) {
        const int j = g >> 1, h = g & 1;
        asm volatile("v_mfma_f32_16x16x32_bf16 %0, %1, %2, %0" : "+v"(acc[h][j]) : "v"(wf[i & 1][j]), "v"(af[i % PD][h]));
        PIN();
        if (i == 15 && !(g & 1)) { wad[g >> 1] += 65536; asm volatile("" : "+v"(wad[g >> 1])); }
        if (i + 1 < 32) {
          const s16x4 v = __builtin_amdgcn_ds_read_tr16_b64_v4i16(LDS_AT(s16x4, wad[g >> 1] + ((i + 1) & 15) * 4096 + (g & 1) * 1024));
          if (g & 1) wf[(i + 1) & 1][g >> 1] = (s16x8)__builtin_shufflevector(wlo, v, 0, 1, 2, 3, 4, 5, 6, 7);
          else wlo = v;
          PIN();
        }
        if (g >= 6 && i + PD < 32) { af[i % PD][h] = lda(i + PD, h); PIN(); }
        if (i + 1 < 32 && i != SIGAT && i + 1 != SIGAT) {
          if (g == 3) { __builtin_amdgcn_s_waitcnt(0xc47f); PIN(); }
          if (g == 7 && i + PD < 32) { __builtin_amdgcn_s_waitcnt(0x0470 | (2 * PD - 2)); PIN(); }
          if (g == 7 && i + PD >= 32) { __builtin_amdgcn_s_waitcnt(0xc47f); PIN(); }
        }
      }
    });
    asm volatile("s_nop 3" : "+v"(acc[0][0]), "+v"(acc[1][0]));
    TE(4, 1); TE(7, 2);
    if (wid == 0) STAMP(dk, 17);
    if (wid == 1 && !RES2) STAMP(4, 0);
#pragma unroll
    for (int j = 0; j < 4; ++j)
#pragma unroll
      for (int h = 0; h < 2; ++h) {
        *LDS_AT(u32x2, tw[j] + h * 2048) = u32x2{pack2(acc[h][j][0], acc[h][j][1]), pack2(acc[h][j][2], acc[h][j][3])};
        PIN();
      }
    asm volatile("" ::: "memory");
    *LDS_AT(volatile unsigned char, rfa) = (unsigned char)2;
#pragma unroll
    for (int p = 0; p < 2; ++p) xres[p] = lds_rd4(tr + p * 1024);
    PIN();
    if (!RES2) {
      const __amdgpu_buffer_rsrc_t rso = __builtin_amdgcn_make_buffer_rsrc(out, 0, S_ * D_ * 2, 0x00020000);
      const __amdgpu_buffer_rsrc_t rsf = __builtin_amdgcn_make_buffer_rsrc(xsig + wid, 0, RX * 512, 0x00020000);
      const u32x4 x2 = lds_rd4(tr + 2048), x3 = lds_rd4(tr + 3072);
      PIN();
#pragma unroll
      for (int p = 0; p < 4; ++p) { __builtin_amdgcn_raw_buffer_store_b128(p < 2 ? xres[p] : (p < 3 ? x2 : x3), rso, xo + p * (8 * ldw * 2), 0, XAUX); PIN(); }
      TE(4, 2); TE(5, 0);
      const float s0 = sums(xres[0], xres[1]), s1 = sums(x2, x3);
      if ((lane & 3) != 3) { __builtin_amdgcn_global_atomic_fadd_f64(pst, (double)s0); __builtin_amdgcn_global_atomic_fadd_f64(pst + 16, (double)s1); }
      PIN();
      TE(4, 3); TE(5, 1);
      asm volatile("s_waitcnt vmcnt(2)" ::: "memory");
      __builtin_amdgcn_raw_buffer_store_b32(tag, rsf, lane * 512 + 256, 0, 16);
      *LDS_AT(volatile unsigned char, rfa) = (unsigned char)3;
      PIN();
      TE(5, 2);
      asm volatile("s_waitcnt vmcnt(1)" ::: "memory");
      __builtin_amdgcn_raw_buffer_store_b32(tag, rsf, lane * 512, 0, 16);
      PIN();
      TE(5, 3);
      __builtin_amdgcn_s_setprio(0);
      return;
    }
    TE(4, 2); TE(5, 0);
  } else {
    u32x4 rres[4];
    double is1[4], is2[4];
#pragma unroll
    for (int p = 0; p < 4; ++p)
      rres[p] = __builtin_amdgcn_raw_buffer_load_b128(__builtin_amdgcn_make_buffer_rsrc(const_cast<u16*>(res), 0, S_ * D_ * 2, 0x00020000), ((row - 16 + 8 * p) * ldw + col) * 2, 0, 16);
    rwr = *reinterpret_cast<const u32x4*>(w_r + col);
#pragma unroll
    for (int p = 0; p < 4; ++p) { is1[p] = ld_ag(stats_in + row - 16 + 8 * p); is2[p] = ld_ag(stats_in + S_ + row - 16 + 8 * p); }
    PIN();
    setup();
    if (RES2) {
#pragma unroll
      for (int p = 0; p < 4; ++p) {
        float mean, var, mb, den;
        ln_from_sums(is1[p], is2[p], mean, var, mb, den);
        const float rin = frcp(den);
#pragma unroll
        for (int e = 0; e < 4; ++e) {
          const f32x2 x = {__uint_as_float(rres[p][e] << 16), __uint_as_float(rres[p][e] & 0xffff0000u)};
          const f32x2 h = (x - f32x2{mb, mb}) * f32x2{rin, rin} + x;
          rres[p][e] = pack2(h[0], h[1]);
        }
        asm volatile("" : "+v"(rres[p]));
      }
      PIN();
    }
    if ((idx >> 5) == tmi * 4 + wid - 4) {
      const int dr = idx - (row - 16);
      u32x4 qq;
#pragma unroll 1
      for (int i = 0; i < SPIN_MAX; ++i) {
        qq = __builtin_amdgcn_raw_buffer_load_b128(__builtin_amdgcn_make_buffer_rsrc(hand, 0, HAND_N * 4, 0x00020000), (GRAN_N + Y_N) * 4 + col * 2, 0, 16);
        if (none(umax(umax(qq[0], qq[1]), umax(qq[2], qq[3])) == SENT)) break;
        asm volatile("" ::: "memory");
      }
#pragma unroll
      for (int p = 0; p < 4; ++p)
#pragma unroll
        for (int e = 0; e < 4; ++e) rres[p][e] = dr == 8 * p ? qq[e] : rres[p][e];
    }
#pragma unroll 1
    for (int i = 0; i < 4 * SPIN_MAX; ++i)
      if (__builtin_amdgcn_readfirstlane(*LDS_AT(const volatile unsigned, flg + 16)) >= 12u) break;
#pragma unroll
    for (int p = 0; p < 4; ++p) *LDS_AT(volatile u32x4, tr - 2048 + p * 1024) = rres[p];
    asm volatile("s_waitcnt lgkmcnt(0)" ::: "memory");
    announce();
    TE(4, 1); TE(7, 2);
    asm volatile("s_waitcnt vmcnt(0)" : "+v"(rwr) :: "memory");
    if (!RES2) return;
    __builtin_amdgcn_s_sleep(TPRE);
#pragma unroll 1
    for (int i = 0; i < 4 * SPIN_MAX; ++i) {
      if (__builtin_amdgcn_readfirstlane(*LDS_AT(const volatile unsigned char, flg + 32 + wid)) == 2u) break;
      __builtin_amdgcn_s_sleep(TPSL);
    }
    asm volatile("" ::: "memory");
    __builtin_amdgcn_s_setprio(XPRIO);
    PIN();
    TE(4, 2); TE(5, 0);
#pragma unroll
    for (int p = 0; p < 2; ++p) xres[p] = lds_rd4(tr + p * 1024);
  }
  TE(4, 3); TE(7, 3);
  const float sv = sums(xres[0], xres[1]);
  if (wid == 0) STAMP(dk, 14);
  if ((lane & 3) != 3) __hip_atomic_store(pps, f2word(sv), __ATOMIC_RELAXED, __HIP_MEMORY_SCOPE_AGENT);
  PIN();
  __builtin_amdgcn_s_setprio(0);
  __hip_atomic_fetch_add(LDS_AT(unsigned, oad), 1u, __ATOMIC_RELAXED, __HIP_MEMORY_SCOPE_WORKGROUP);
  TE(5, 1); TE(5, 2);
  PIN();
}

#define MFMA32(a, b, c) __builtin_amdgcn_mfma_f32_32x32x16_bf16(a, b, c, 0, 0, 0)
#define MFMA16(a, b, c) __builtin_amdgcn_mfma_f32_16x16x32_bf16(a, b, c, 0, 0, 0)

#define PLO(x) __builtin_bit_cast(bf16x16, __builtin_shufflevector(x, x, 0, 1, 2, 3, 4, 5, 6, 7))
#define PHI(x) __builtin_bit_cast(bf16x16, __builtin_shufflevector(x, x, 8, 9, 10, 11, 12, 13, 14, 15))
#define SMF32(a, b, c) __builtin_amdgcn_smfmac_f32_32x32x32_bf16(__builtin_bit_cast(bf16x8, a), b, c, sidx, 0, 0)
#define SMF16(a, b, c) __builtin_amdgcn_smfmac_f32_16x16x64_bf16(__builtin_bit_cast(bf16x8, a), b, c, sidx, 0, 0)

__device__ __forceinline__ void patch_kv(unsigned tab, int r, s16x8& k0, s16x8& k1, s16x8& v0, s16x8& v1, bool on = true) {
  const int lane = fresh_lane(), hi = lane >> 5;
  if (on && (lane & 31) == kpos(r)) {
    k0 = *LDS_AT(const s16x8, tab + hi * 16); k1 = *LDS_AT(const s16x8, tab + 32 + hi * 16);
  }
  const short vv = *LDS_AT(const short, tab + 128 + (lane & 31) * 2);
  const int js = (on && hi == ((r >> 2) & 1)) ? (r >> 4) * 8 + ((r >> 3) & 1) * 4 + (r & 3) : -1;
#pragma unroll
  for (int e = 0; e < 8; ++e) { v0[e] = js == e ? vv : v0[e]; v1[e] = js == 8 + e ? vv : v1[e]; }
}

__device__ __forceinline__ void attn_safe(const u16* KV, int tile0, s16x8 q0, s16x8 q1,
                                       f32x16& o, float& l_out, int ptile, int prow, unsigned ptab) {
  const int lane = fresh_lane();
  float m = -INFINITY, l = 0.f;
  const int sidx = 0xDDDDDDDD;
  const __amdgpu_buffer_rsrc_t rs = __builtin_amdgcn_make_buffer_rsrc(const_cast<u16*>(KV), 0, 2 * S_ * D_ * 2, 0x00020000);
#pragma unroll
  for (int e = 0; e < 16; ++e) o[e] = 0.f;
  for (int tt = 0; tt < 64; ++tt) {
    const int t = tile0 + tt, off = t * 2048 + lane * 16;
    s16x8 k0 = __builtin_bit_cast(s16x8, __builtin_amdgcn_raw_buffer_load_b128(rs, off, 0, 16));
    s16x8 k1 = __builtin_bit_cast(s16x8, __builtin_amdgcn_raw_buffer_load_b128(rs, off + 1024, 0, 16));
    s16x8 v0 = __builtin_bit_cast(s16x8, __builtin_amdgcn_raw_buffer_load_b128(rs, off + S_ * D_ * 2, 0, 16));
    s16x8 v1 = __builtin_bit_cast(s16x8, __builtin_amdgcn_raw_buffer_load_b128(rs, off + S_ * D_ * 2 + 1024, 0, 16));
    if (t == ptile) patch_kv(ptab, prow, k0, k1, v0, v1);
    f32x16 sc;
#pragma unroll
    for (int e = 0; e < 16; ++e) sc[e] = 0.f;
    sc = MFMA32(k0, q0, sc); sc = MFMA32(k1, q1, sc);
    float mt = sc[0];
#pragma unroll
    for (int e = 1; e < 16; ++e) mt = fmaxf(mt, sc[e]);
    mt = fmaxf(mt, __shfl_xor(mt, 32, 64));
    const float mn = fmaxf(m, mt);
    const float alpha = __builtin_amdgcn_exp2f(m - mn);
    m = mn;
#pragma unroll
    for (int e = 0; e < 16; ++e) sc[e] = __builtin_amdgcn_exp2f(sc[e] - mn);
    float ls = 0.f;
#pragma unroll
    for (int e = 0; e < 16; ++e) ls += __uint_as_float(__float_as_uint(sc[e]) & 0xffff0000u);
    l = l * alpha + ls;
#pragma unroll
    for (int e = 0; e < 16; ++e) o[e] *= alpha;
    o = SMF32(v0, PLO(sc), o); o = SMF32(v1, PHI(sc), o);
  }
  l_out = l + __shfl_xor(l, 32, 64);
}

__device__ __forceinline__ float wave_fmax(float v) {
  asm volatile("s_nop 1\n\tv_max_f32_dpp %0, %0, %0 row_shr:1 row_mask:0xf bank_mask:0xf\n\ts_nop 1\n\tv_max_f32_dpp %0, %0, %0 row_shr:2 row_mask:0xf bank_mask:0xf\n\t"
               "s_nop 1\n\tv_max_f32_dpp %0, %0, %0 row_shr:4 row_mask:0xf bank_mask:0xf\n\ts_nop 1\n\tv_max_f32_dpp %0, %0, %0 row_shr:8 row_mask:0xf bank_mask:0xf\n\t"
               "s_nop 1\n\tv_max_f32_dpp %0, %0, %0 row_bcast:15 row_mask:0xa bank_mask:0xf\n\ts_nop 1\n\tv_max_f32_dpp %0, %0, %0 row_bcast:31 row_mask:0xc bank_mask:0xf\n\ts_nop 0"
               : "+v"(v));
  return __int_as_float(__builtin_amdgcn_readlane(__float_as_int(v), 63));
}

#define PSLP 8
#define WSLP 3
#define PRE1 8
#define PRE5 8
#define W3SLP 4
#define FSTART 10
#define OAUX 16
#define OMX 4
#define T0 2u
#define TW 10
#define HW 2
#define HP1 1
#define POLLAT 11
#define X2AT 2
#define HPRIO 3
#define SSLP 1
#define SPRE 6
#define WGAPS {0, 1, 7, 8, 9, 10, 11, 12}
#define KVS 4
#define PNS 12
#define G0 5u
#define GS 2u
#define IAT 1u
#define YSLP 2
#define ISLP 8
#define KSLP 2
#define UAT 13u
#define UHW 0
#define PPSLP 2
#define XRSLP 2
#define MPRE 20
#define YD1 150
#define YD2 180
#define KDT 200
#define XDT 45
#define R2PRIO 0
#define LNPRIO 1
#define MSLP 4
#define BARG 0
struct Args {
  long long lay;
  const u16* a; u16* kv; u16* o; u16* xo; double* so;
  const u16* W; u16* wc;
  const u16 *wg, *wu, *wd, *wo, *w_r;
  unsigned* gran; float* csf; double* stats; unsigned* hand; u16* out;
};
typedef __attribute__((address_space(4))) const Args* ArgP;
#define WCAT 3
#define CAP0 1
#define PRE4 50
#define PRE7 20
#define UARR 12u
#define GF0 10
#define XGATE 8
#define ASLP 1
#define B15 3
#define KO 5
#define PRO 2
#define HS 2
#define XD2 90
#define WEND 0
#define WOT 0
#define FLT 0
#define FL1 1
#define FL2 4u
#define ALIAS 0
#define DT 0
#define T2 8u
#define WMAP 0x32100333
#define SMAP 0
#define STCH 6
#define KPOLL 10
#define TPRIO 2
#define TSLP 9
__device__ __forceinline__ bool slow_cols(const u16* W, int tni) { return ((((unsigned)(uintptr_t)W >> 7) + tni) & 7) == 3; }
#define ARGS_HERE                                                                                                      \
  ArgP ap_ = ap; asm volatile("" : "+s"(ap_));                                                                          \
  const u16* A = ap_->a; const u16* __restrict__ W = ap_->W; u16* KV = ap_->kv; u16* Of = ap_->o;        \
  const u16* __restrict__ wg = ap_->wg; const u16* __restrict__ wu = ap_->wu; const u16* __restrict__ wd = ap_->wd;       \
  const u16* __restrict__ wo = ap_->wo; const u16* __restrict__ w_r = ap_->w_r; unsigned* gran = ap_->gran;              \
  float* csf = ap_->csf; const double* stats = ap_->stats; double* stats_out = ap_->so; unsigned* hand = ap_->hand; \
  u16* xout = ap_->xo; u16* __restrict__ out = ap_->out
#define AGAIN "s_getpc_b64 s[90:91]\n\ts_add_u32 s90, s90, layers_kernel@rel32@lo+4\n\ts_addc_u32 s91, s91, layers_kernel@rel32@hi+12\n\ts_setpc_b64 s[90:91]"
#define KEPT(c)                                                                                                        \
  c "{v[104:107]}"(gu[0]), c "{v[108:111]}"(gu[1]), c "{v[112:115]}"(gu[2]), c "{v[116:119]}"(gu[3]), c "{v[120:123]}"(gu[4]),      \
  c "{v[124:127]}"(gu[5]), c "{v[128:131]}"(gu[6]), c "{v[132:135]}"(gu[7]), c "{v[136:139]}"(gu[8]), c "{v[140:143]}"(gu[9]),      \
  c "{v[144:147]}"(gu[10]), c "{v[148:151]}"(gu[11]), c "{v[152:155]}"(gu[12]), c "{v[156:159]}"(gu[13]), c "{v[160:163]}"(gu[14]), \
  c "{v[164:167]}"(gu[15])
#define LAYER_HERE ArgP apl_ = ap; asm volatile("" : "+s"(apl_)); const bool LAY2 = apl_->lay != 0
__device__ __forceinline__ void layer_body(const int wid, const int bid, const unsigned lbase, const ArgP ap, const unsigned long long t_entry) {
  constexpr int K = 1024, NKT = 16, NST = 3, ABYTES = 16384, WBYTES = 24576, STB = ABYTES + WBYTES, ldw = 3 * D_;
  constexpr int TABA = NST * STB, ALPHA = TABA + 2048, CRED = ALPHA + 512;
  constexpr int ASB = 16384, FLG = 8 * ASB + 256;
  constexpr int DUM = FLG + 64, TAB = DUM + 256, RED = TAB + 512, SACT = RED + 4096, XML = SACT + 3584, UGL = XML + 4096, SPM = UGL + 1024, QOFF = 9 * ASB;
  constexpr int TABW = RED;
  static_assert(SPM + 2048 <= QOFF && CRED <= 8 * ASB && RED + 8192 <= QOFF && RED % 16 == 0, "lds");
  const int xcd = (bid & 1) * 4 + ((bid >> 1) & 3), loc = bid >> 3;
  const int tmi = (xcd / QNG) * QMX + loc / QNX, tni = (xcd % QNG) * QNX + loc % QNX;
  const int m0 = tmi * 128, n0 = tni * 64;
  const int omi = (xcd / (OMX / 2)) * OMX + loc / (32 / OMX), oni = (xcd % (OMX / 2)) * (32 / OMX) + loc % (32 / OMX);
  constexpr unsigned VOFF = (unsigned)S_ * D_ * 2;
  auto tile_at = [&](unsigned n) __attribute__((always_inline)) {
#pragma unroll 1
    for (int i = 0; i < 4 * SPIN_MAX; ++i) {
      if (__builtin_amdgcn_readfirstlane(*LDS_AT(const volatile unsigned, lbase + FLG + 36)) == n * 0x01010101u) break;
      __builtin_amdgcn_s_sleep(1);
    }
    asm volatile("" ::: "memory");
    PIN();
  };
  auto all_ge = [&](unsigned w, unsigned n) { return ((w + (128u - n) * 0x01010101u) & 0x80808080u) == 0x80808080u; };
  if (wid >= 8) {
    u32x4 gu[16];
    LAYER_HERE;
    constexpr int dk = 6;
    const int hw = wid - 8, b = bid;
    int nb = 0;
    constexpr int lim = NKT - 1;
    auto tick_nw = [&]() {
      PIN();
      if (nb < lim) { __builtin_amdgcn_s_barrier(); ++nb; } else __builtin_amdgcn_s_sleep(2);
      asm volatile("" ::: "memory");
      PIN();
    };
    auto tick = [&]() {
      asm volatile("s_waitcnt lgkmcnt(0)" ::: "memory");
      tick_nw();
    };
    auto fold16 = [&](f32x2 y0, f32x2 y1) __attribute__((always_inline)) {
      const auto q0 = __builtin_amdgcn_permlane32_swap(__float_as_uint(y0[0]), __float_as_uint(y1[0]), false, false);
      const auto q1 = __builtin_amdgcn_permlane32_swap(__float_as_uint(y0[1]), __float_as_uint(y1[1]), false, false);
      const auto q = __builtin_amdgcn_permlane16_swap(__float_as_uint(__uint_as_float(q0[0]) + __uint_as_float(q0[1])),
                                                      __float_as_uint(__uint_as_float(q1[0]) + __uint_as_float(q1[1])), false, false);
      return __uint_as_float(q[0]) + __uint_as_float(q[1]);
    };
    unsigned best, tag, rmd;
    float alpha, m, wsum;
    u32x4 tw;
    u32x2 xq;
    s16x8 sl[8];
    const int kcap = CAP0 + (tmi & 7);
    if (!LAY2) {
      ARGS_HERE;
      const int lane = fresh_lane(), t = hw * 64 + lane;
      const unsigned ep = ld_ag(gran);
      const int srow = m0 + tni * 8 + hw * 2;
      u32x4 xa[2][2], wv[8];
      const int wr_ = (3 - ((unsigned)(uintptr_t)W >> 7) - (tni & ~3)) & 7;
      const bool cpy = hw == 3 && wr_ < 4 && wr_ != (tni & 3), wld = hw < 3 ? wr_ != (tni & 3) : cpy;
      const int wcn = cpy ? (tni & ~3) + wr_ : tni, wcp = cpy ? (tni - wr_ - 1) & 3 : hw;
      const int wof = ((tmi * 64 + (lane >> 3)) * ldw + wcp * D_ + wcn * 64 + (lane & 7) * 8) * 2;
      auto ld_w = [&]() {
        const __amdgpu_buffer_rsrc_t rw = __builtin_amdgcn_make_buffer_rsrc(const_cast<u16*>(W), 0, wld ? D_ * ldw * 2 : 0, 0x00020000);
#pragma unroll
        for (int r = 0; r < 8; ++r) wv[r] = __builtin_amdgcn_raw_buffer_load_b128(rw, wof, r * 8 * ldw * 2, 0);
        PIN();
      };
      auto wcopy = [&]() __attribute__((always_inline)) {
        if (cpy) {
          const __amdgpu_buffer_rsrc_t rw = __builtin_amdgcn_make_buffer_rsrc(ap_->wc, 0, D_ * ldw * 2, 0x00020000);
#pragma unroll
          for (int r = 0; r < 8; ++r) __builtin_amdgcn_raw_buffer_store_b128(wv[r], rw, wof + r * 8 * ldw * 2, 0, 16);
        }
        PIN();
      };
      const int wat = ((hw == 3 ? SMAP : WMAP) >> (4 * (tmi & 7))) & 3;
      unsigned ps = 0u;
      auto stouch = [&](int at) __attribute__((always_inline)) {
        if (at == STCH && cpy) ps = __builtin_amdgcn_raw_buffer_load_b32(__builtin_amdgcn_make_buffer_rsrc(const_cast<u16*>(W), 0, D_ * ldw * 2, 0x00020000),
                                                                        (((tmi ^ 8) * 64 + lane) * ldw + wcp * D_ + wcn * 64) * 2, 0, 0);
        PIN();
      };
      auto ld_x = [&](int h) {
#pragma unroll
        for (int r = 0; r < 2; ++r) xa[r][h] = *reinterpret_cast<const u32x4*>(A + (size_t)(srow + r) * D_ + (h ^ (tmi >> 3)) * 512 + lane * 8);
        PIN();
      };
      ld_x(0);
      if (X2AT == 0) ld_x(1);
      if (wat == 0) ld_w();
      unsigned pc = 0u;
      if (hw == 3) {
        unsigned long long a0, a1;
        CODE_ADDR("layers_kernel", a0);
        CODE_ADDR("end_marker_kernel", a1);
        pc = touch_code(a0, a1, loc, lane);
      }
      if (FL1 && hw == 1) pc = ld_ag(gran + GR_KV + (tni * RK + tmi % RK) * 32);
      if (hw == 2) st_ag(gran + GR_N + bid * 64 + lane, lane + 1u);
      {
        const int gi = bid * 256 + t;
        if (gi < GRAN_N / 4) __builtin_amdgcn_raw_buffer_store_b128(u32x4{SENT, SENT, SENT, SENT}, __builtin_amdgcn_make_buffer_rsrc(hand, 0, GRAN_N * 4, 0x00020000), gi * 16, 0, 16);
        if (t < 24) __hip_atomic_store(stats_out + (t >> 3) * S_ + m0 + tni * 8 + (t & 7), 0.0, __ATOMIC_RELAXED, __HIP_MEMORY_SCOPE_AGENT);
      }
      tick();
      if (wat == 1) ld_w();
      if (X2AT == 1) ld_x(1);
      tick();
      if (wat == 2) ld_w();
      if (WCAT == 2) wcopy();
      if (X2AT == 2) ld_x(1);
      asm volatile("" :: "v"(ep));
      tick();
      if (wat == 3) ld_w();
      if (WCAT == 3) wcopy();
      if (X2AT == 3) ld_x(1);
      stouch(3);
#pragma unroll
      for (int i = 0; i < HW; ++i) { tick(); stouch(4 + i); }
      __builtin_amdgcn_s_setprio(HP1);
      PIN();
      tag = ep * 1664525u + 1013904223u;
      const __amdgpu_buffer_rsrc_t rsc = __builtin_amdgcn_make_buffer_rsrc(gran, 0, GR_N * 4, 0x00020000);
      {
        u32x4 od;
#pragma unroll
        for (int q = 0; q < 4; ++q) od[q] = ((lane & 7) >> 1) == q ? ((lane & 1) ? 0x3f800000u : 0x00003f80u) : 0u;
        f32x4 d[2] = {{0.f, 0.f, 0.f, 0.f}, {0.f, 0.f, 0.f, 0.f}};
#pragma unroll
        for (int r = 0; r < 8; ++r) d[r & 1] = __builtin_amdgcn_mfma_f32_16x16x32_bf16(__builtin_bit_cast(s16x8, od), __builtin_bit_cast(s16x8, wv[r]), d[r & 1], 0, 0, 0);
        u32x4 gr;
#pragma unroll
        for (int q = 0; q < 4; ++q) gr[q] = ((__float_as_uint(dpp_add<0x128, 0xf>(fadd(d[0][q], d[1][q]))) + 0x80u) & ~0xffu) | ((tag >> (8 * q)) & 0xffu);
        asm volatile("s_waitcnt vmcnt(0)" ::: "memory");
        __builtin_amdgcn_raw_buffer_store_b128(gr, rsc, (lane & 40) || !wld ? GR_N * 4 : (GR_COL + (wcn * 16 + tmi) * 192 + wcp * 64 + (lane & 7) * 8 + (lane >> 4) * 4) * 4, 0, 16);
      }
      asm volatile("" :: "v"(pc));
      tick_nw();
      stouch(6);
      {
        float xv[2][16], mean[2];
#pragma unroll
        for (int r = 0; r < 2; ++r) {
          float sm = 0.f;
#pragma unroll
          for (int i = 0; i < 4; ++i) {
            xv[r][2 * i] = __uint_as_float(xa[r][0][i] << 16); xv[r][2 * i + 1] = __uint_as_float(xa[r][0][i] & 0xffff0000u);
            xv[r][8 + 2 * i] = __uint_as_float(xa[r][1][i] << 16); xv[r][8 + 2 * i + 1] = __uint_as_float(xa[r][1][i] & 0xffff0000u);
          }
#pragma unroll
          for (int i = 0; i < 16; ++i) sm += xv[r][i];
          mean[r] = wave_sum(sm) * (1.0f / 1024.0f);
          tick();
          stouch(7 + r);
        }
        float vr[2];
#pragma unroll
        for (int r = 0; r < 2; ++r) {
          float v = 0.f;
#pragma unroll
          for (int i = 0; i < 16; ++i) { const float d = xv[r][i] - mean[r]; v += d * d; }
          vr[r] = wave_sum(v) * (1.0f / 1024.0f);
          if (r == 0) tick();
        }
        const float vv = lane & 2 ? vr[1] : vr[0], mm = lane & 2 ? mean[1] : mean[0];
        const float rin = 1.0f / rbf(sqrtf(rbf(rbf(vv) + 1e-5f)));
        if (lane < 4) __builtin_amdgcn_raw_buffer_store_b64(u32x2{__float_as_uint((lane & 1) ? rbf(mm) : rin), tag}, rsc, (GR_ROW + srow * 4 + lane * 2) * 4, 0, 16);
      }
      tick();
      float cs;
      while (nb < POLLAT) tick();
      asm volatile("" :: "v"(ps));
#pragma unroll 1
      for (int spin = 0;; ++spin) {
        u32x4 csv[4];
        const __amdgpu_buffer_rsrc_t rsw = __builtin_amdgcn_make_buffer_rsrc(gran, 0, hw < 3 ? GR_N * 4 : 0, 0x00020000);
#pragma unroll
        for (int i = 0; i < 4; ++i) csv[i] = __builtin_amdgcn_raw_buffer_load_b128(rsw, (GR_COL + (tni * 16 + (lane >> 4)) * 192 + hw * 64 + (lane & 15) * 4) * 4, i * (4 * 192 * 4), 16);
        const u32x4 rp = __builtin_amdgcn_raw_buffer_load_b128(rsc, (GR_ROW + (m0 + (t & 127)) * 4) * 4, 0, 16);
        PIN();
        tick(); tick();
        unsigned bad = 0u;
        float z[4];
#pragma unroll
        for (int q = 0; q < 4; ++q) {
          const unsigned tq = tag >> (8 * q);
          bad |= ((csv[0][q] ^ tq) | (csv[1][q] ^ tq)) | ((csv[2][q] ^ tq) | (csv[3][q] ^ tq));
          z[q] = fadd(fadd(__uint_as_float(csv[0][q] & ~0xffu), __uint_as_float(csv[1][q] & ~0xffu)), fadd(__uint_as_float(csv[2][q] & ~0xffu), __uint_as_float(csv[3][q] & ~0xffu)));
        }
        cs = fold16(f32x2{z[0], z[1]}, f32x2{z[2], z[3]});
        bad = (hw < 3 ? bad & 0xffu : 0u) | (rp[1] ^ tag) | (rp[3] ^ tag);
        alpha = __uint_as_float(rp[0]); m = __uint_as_float(rp[2]);
        if (none(bad != 0u) || spin >= SPIN_MAX) break;
      }
      if (tmi == 0 && hw < 3) st_ag(reinterpret_cast<unsigned*>(csf) + hw * 1024 + n0 + (lane & 15) * 4 + (lane >> 4), __float_as_uint(cs));
      unsigned c0, c1, c2;
      split3(cs, c0, c1, c2);
      tw = u32x4{c0 | (c1 << 16), c0 | (c2 << 16), c1 | (c0 << 16), 0u};
      best = rmd = 0u;
      wsum = 0.f;
      asm volatile("" : "=v"(xq), KEPT("="));
#pragma unroll
      for (int i = 0; i < 8; ++i) asm volatile("" : "=v"(sl[i]));
    } else {
      asm volatile("; kept registers" : KEPT("="));
      PIN();
      ARGS_HERE;
      const int lane = fresh_lane(), t = hw * 64 + lane;
      best = rmd = 0u;
      tag = __builtin_amdgcn_readfirstlane(*LDS_AT(const volatile unsigned, lbase + FLG + 20));
#pragma unroll
      for (int i = 0; i < 8; ++i) asm volatile("" : "=v"(sl[i]));
      unsigned sad;
      auto tickc = [&]() __attribute__((always_inline)) {
        tick();
        if (nb == kcap + 1) {
#pragma unroll
          for (int i = 0; i < 8; ++i) {
            const unsigned a = (sad ^ ((i >> 2) * 64) ^ (((i >> 1) & 1) * 16)) + (i & 1) * 12288;
            sl[i] = __builtin_shufflevector(__builtin_amdgcn_ds_read_tr16_b64_v4i16(LDS_AT(s16x4, a)), __builtin_amdgcn_ds_read_tr16_b64_v4i16(LDS_AT(s16x4, a + 1536)), 0, 1, 2, 3, 4, 5, 6, 7);
          }
        }
        PIN();
      };
      static_assert(CAP0 + 8 <= KPOLL, "the stage is taken before that");
      if (hw == 1) STAMP(4, 4);
      if (hw == 0) {
        __builtin_amdgcn_s_sleep(TPRE);
        tile_at(3u);
        __builtin_amdgcn_s_sleep(XGATE);
        STAMP(4, 3);
#pragma unroll 1
        for (int spin = 0; spin < SPIN_MAX; ++spin) {
          if (none(ld_ag(gran + GR_X + (tmi * RX + tni % RX) * 128 + 64 + lane) != tag)) break;
          __builtin_amdgcn_s_sleep(1);
        }
      }
      static_assert(UHW == 0, "");
      if (hw == 0) asm volatile("s_waitcnt vmcnt(0)" ::: "memory");
      if (hw == 0) STAMP(4, 5);
      asm volatile("" ::: "memory");
      PIN();
      __builtin_amdgcn_s_barrier();
      PIN();
      if (hw == 1) STAMP(4, 6);
      {
        const int ln = fresh_lane(), g4 = ln >> 4, tp = ln & 3, k = 8 * g4 + ((ln & 15) >> 2);
        sad = lbase + (kcap % NST) * STB + ABYTES + k * 384 + (((2 * (tp >> 1)) ^ (4 * ((k >> 1) & 1)) ^ (g4 & 1)) << 4) + 8 * (tp & 1) + (hw % 3) * 128;
      }
      wsum = __uint_as_float(__builtin_amdgcn_readfirstlane(*LDS_AT(const volatile unsigned, lbase + FLG + 60)));
      asm volatile("" : "+v"(sad), "=v"(tw), "=v"(alpha), "=v"(m));
      PIN();
      const int row = m0 + (t & 127);
      const __amdgpu_buffer_rsrc_t rsg = __builtin_amdgcn_make_buffer_rsrc(hand, 0, GRAN_N * 4, 0x00020000);
      if (hw < 2 || tmi == tni) {
      while (nb < HS) tickc();
#pragma unroll 1
      for (int spin = 0; spin < SPIN_MAX; ++spin) {
        const unsigned v = ld_ag(gran + GR_X + (tmi * RX + tni % RX) * 128 + lane);
        tickc();
        if (none(v != tag)) break;
      }
      asm volatile("" ::: "memory");
      PIN();
      const double s1 = ld_ag(stats + row), s2 = ld_ag(stats + S_ + row), sw = ld_ag(stats + 2 * S_ + row);
      tickc();
      float mean, var, mb, den;
      ln_from_sums(s1, s2, mean, var, mb, den);
      const float rin = frcp(den), g1 = 1.0f + rin;
      const float mb2 = rbf(mean + (mean - mb) * rin);
      const float den2 = rbf(__builtin_amdgcn_sqrtf(rbf(rbf(var * g1 * g1) + 1e-5f)));
      alpha = g1 * frcp(den2); m = (mb * rin + mb2) * frcp(g1);
      if (tmi == tni && hw >= 2) {
        const float r = (float)fma(-(double)mb, (double)wsum, sw) * rin;
        const unsigned rb = pack2(r, r);
        unsigned rkey = (rb ^ ((unsigned)((int)rb >> 31) | 0x80000000u)) & 0xffff0000u;
        if (r != r) rkey = 0u;
        rkey = wave_umax(rkey | (unsigned)(2047 - row));
        const unsigned md = __builtin_amdgcn_readlane(f2word(__uint_as_float((__float_as_uint(mb) >> 16) | (__float_as_uint(den) & 0xffff0000u))), (2047 - (int)(rkey & 0xffffu)) & 63);
        __builtin_amdgcn_raw_buffer_store_b64(u32x2{rkey, md}, rsg, lane < NREP ? lane * 256 + (tmi * 2 + hw - 2) * 8 : GRAN_N * 4, 0, 16);
      }
      }
      while (nb < KPOLL) tickc();
#pragma unroll 1
      for (int spin = 0; spin < SPIN_MAX; ++spin) {
        const u32x2 v = __builtin_amdgcn_raw_buffer_load_b64(rsg, (bid % NREP) * 256 + (lane & 31) * 8, 0, 16);
        tick(); tick();
        if (none(umax(v[0], v[1]) == SENT)) {
          best = wave_umax(v[0]);
          rmd = __builtin_amdgcn_readlane(v[1], ((2047 - (int)(best & 0xffffu)) >> 6) & 31);
          break;
        }
      }
      const int ix = (2047 - (int)(best & 0xffffu)) & 2047;
      if (row == ix) alpha = 0.f;
      xq = __builtin_amdgcn_raw_buffer_load_b64(__builtin_amdgcn_make_buffer_rsrc(const_cast<u16*>(A), 0, S_ * D_ * 2, 0x00020000), (ix * D_ + hw * 256 + (lane & 3) * 64 + (lane >> 2) * 4) * 2, 0, 16);
      PIN();
    }
    tag = __builtin_amdgcn_readfirstlane(tag);
    wsum = __uint_as_float(__builtin_amdgcn_readfirstlane(__float_as_uint(wsum)));
    const int lane = fresh_lane(), t = hw * 64 + lane;
    {
      unsigned a0, a1, a2;
      split3(-m, a0, a1, a2);
      u32x4 ta = {a0 | (a0 << 16), a1 | (a0 << 16), a1 | (a2 << 16), 0u};
      asm volatile("" : "+v"(tw), "+v"(ta), "+v"(alpha));
      __builtin_amdgcn_s_setprio(TPRIO);
      if (nb < lim) {
        while (nb < lim) tick();
        __builtin_amdgcn_s_sleep(TSLP);
      }
      if (!LAY2 && hw < 3) *LDS_AT(u32x4, lbase + TABW + (hw * 64 + (lane & 15) * 4 + (lane >> 4)) * 16) = tw;
      if (t < 128) {
        *LDS_AT(u32x4, lbase + TABA + t * 16) = ta;
        *LDS_AT(float, lbase + ALPHA + t * 4) = alpha;
      }
    }
    if (t >= 192 && t < 196) *LDS_AT(u32x4, lbase + CRED + ((t >> 1) & 1) * 512 + (t & 1) * 128) = u32x4{0u, 0u, 0u, 0u};
    asm volatile("s_waitcnt lgkmcnt(0)" ::: "memory");
    PIN();
    __builtin_amdgcn_s_barrier();
    PIN();
    *LDS_AT(u32x4, lbase + XML + t * 16) = u32x4{SENT, SENT, SENT, SENT};
    *LDS_AT(u32x4, lbase + SPM + (t & 127) * 16) = u32x4{SENT, SENT, SENT, SENT};
    if (hw == 0) {
      unsigned v;
      asm volatile("v_mov_b32 %0, 0\n\tv_writelane_b32 %0, 2, 0\n\tv_writelane_b32 %0, %1, 5\n\tv_writelane_b32 %0, %2, 7"
                   : "=&v"(v) : "s"(tag), "s"(best));
      lds_wr(lbase + FLG + lane * 4, v);
    }
    const int idx = LAY2 ? (2047 - (int)(best & 0xffffu)) & 2047 : -64;
    const bool p1blk = (oni & 7) != 7, fast = tmi < 8, own = fast && (loc & 1);
    const int fg = xcd * 28 + (p1blk ? loc - (loc >> 3) : 0), ow = (xcd & 3) * 16 + (loc >> 1);
    static_assert(OMX == 4, "oni % 8 = loc % 8");
#define GLD(a, b) "global_load_dwordx4 v[" #a ":" #b "], %0, off\n\tv_lshl_add_u64 %0, %0, 0, %1\n\t"
#define GLD4(...) asm volatile(__VA_ARGS__ : "+v"(p) : "s"((unsigned long long)(F_ * 2)), KEPT() : "memory")
    auto gptr = [&](const u16* wg, const u16* wu) {
      const int ln = fresh_lane();
      return ((ln & 2) ? wu : wg) + (size_t)(hw * 256 + (ln >> 2) * 4) * F_ + fg * 16 + (ln & 1) * 8;
    };
    auto gather = [&](const u16*& p, int e) __attribute__((always_inline)) {
      if (e == 0) GLD4(GLD(104, 107) GLD(108, 111) GLD(112, 115) GLD(116, 119));
      else if (e == 1) GLD4(GLD(120, 123) GLD(124, 127) GLD(128, 131) GLD(132, 135));
      else if (e == 2) GLD4(GLD(136, 139) GLD(140, 143) GLD(144, 147) GLD(148, 151));
      else GLD4(GLD(152, 155) GLD(156, 159) GLD(160, 163) GLD(164, 167));
      p += 60 * F_;
    };
    auto dmask = [&](int q) { const int ln = fresh_lane(); return ((ln >> 1) & 1) == q ? ((ln & 1) ? 0xffff0000u : 0x0000ffffu) : 0u; };
    unsigned dm0, dm1, dad;
    auto gu_setup = [&]() __attribute__((always_inline)) {
      const int ln = fresh_lane();
      dm0 = dmask(0); dm1 = dmask(1);
      dad = lbase + SACT + ((ln & 3) * 8 + (ln >> 5) * 4 + ((ln >> 4) & 1)) * 16 + hw * 4;
      asm volatile("" : "+v"(dm0), "+v"(dm1), "+v"(dad));
    };
    auto gate_up = [&](u32x2 x, unsigned md) __attribute__((always_inline)) {
      const unsigned rin = __builtin_amdgcn_readfirstlane(__float_as_uint(frcp(__uint_as_float(md & 0xffff0000u))));
      asm volatile("s_getpc_b64 s[90:91]\n\ts_add_u32 s90, s90, gate_up_code@rel32@lo+4\n\ts_addc_u32 s91, s91, gate_up_code@rel32@hi+12\n\ts_swappc_b64 s[92:93], s[90:91]"
                   : "+{v[100:101]}"(x) : "{s84}"(md << 16), "{s85}"(md & 0xffff0000u), "{s86}"(rin), "{v99}"(dad), "{v102}"(dm0), "{v103}"(dm1), KEPT()
                   : "v84", "v85", "v86", "v87", "v88", "v89", "v90", "v91", "v92", "v93", "v94", "v95", "v96", "v97", "s88", "s89", "s90", "s91", "s92", "s93", "scc", "memory");
    };
    auto act_diag = [&](int so = 0) __attribute__((always_inline)) {
      const int ln = fresh_lane();
      const f32x4 g4 = *LDS_AT(const volatile f32x4, lbase + SACT + so + (ln >> 2) * 16), u4 = *LDS_AT(const volatile f32x4, lbase + SACT + so + 256 + (ln >> 2) * 16);
      float gv = fadd(fadd(g4[0], g4[1]), fadd(g4[2], g4[3])), uv = fadd(fadd(u4[0], u4[1]), fadd(u4[2], u4[3]));
      rbf2(gv, uv);
      const float av = rbf(gv * frcp(1.0f + __expf(-gv))) * uv;
      const unsigned ap2 = pack2(av, av);
      return __builtin_bit_cast(s16x4, u32x2{ap2 & dm0, ap2 & dm1});
    };
    auto down = [&](const s16x4& a, const u32x2 (&w)[16]) __attribute__((always_inline)) {
      f32x4 d[2] = {{0.f, 0.f, 0.f, 0.f}, {0.f, 0.f, 0.f, 0.f}};
      static_for<0, 16>([&](auto fc) __attribute__((always_inline)) {
        constexpr int f = decltype(fc)::value;
        d[f & 1] = __builtin_amdgcn_mfma_f32_4x4x4bf16_1k(a, __builtin_bit_cast(s16x4, w[f]), d[f & 1], 4, f, 0);
      });
      f32x4 z;
#pragma unroll
      for (int e = 0; e < 4; ++e) { z[e] = d[0][e] + d[1][e]; asm volatile("" : "+v"(z[e])); }
      return u32x4{f2word(z[0]), f2word(z[1]), f2word(z[2]), f2word(z[3])};
    };
    auto fold8 = [&](f32x2 y0, f32x2 y1) __attribute__((always_inline)) { return dpp_add<0x128, 0xf>(fold16(y0, y1)); };
    auto fold4 = [&](f32x2 y0, f32x2 y1) __attribute__((always_inline)) { return dpp_add<0x124, 0xf>(fold8(y0, y1)); };
    auto y_sum = [&](const __amdgpu_buffer_rsrc_t rs, auto nbc) __attribute__((always_inline)) {
      constexpr int NB = decltype(nbc)::value, N = 14 / NB;
      const int ln = fresh_lane(), src = (GRAN_N + (ln >> 2) * D_ + ow * 16 + (ln & 3) * 4) * 4;
      f32x2 y0 = {0.f, 0.f}, y1 = {0.f, 0.f};
#pragma unroll 1
      for (int bt = 0; bt < NB; ++bt) {
        f32x2 z0, z1;
#pragma unroll 1
        for (int spin = 0; spin < SPIN_MAX; ++spin) {
          u32x4 v[N];
#pragma unroll
          for (int i = 0; i < N; ++i) v[i] = __builtin_amdgcn_raw_buffer_load_b128(rs, src, (bt * N + i) * (16 * D_ * 4), 16);
          z0 = y0; z1 = y1;
#pragma unroll
          for (int i = 0; i < N; ++i) {
            z0 = f32x2{fadd(z0[0], __uint_as_float(v[i][0])), fadd(z0[1], __uint_as_float(v[i][1]))};
            z1 = f32x2{fadd(z1[0], __uint_as_float(v[i][2])), fadd(z1[1], __uint_as_float(v[i][3]))};
          }
          const float chk = fadd(fadd(z0[0], z0[1]), fadd(z1[0], z1[1]));
          if (none(chk != chk)) break;
          unsigned mx = 0u;
#pragma unroll
          for (int i = 0; i < N; ++i) mx = umax(umax(umax(mx, v[i][0]), v[i][1]), umax(v[i][2], v[i][3]));
          if (none(mx == SENT)) break;
          __builtin_amdgcn_s_sleep(YSLP);
          asm volatile("" ::: "memory");
        }
        y0 = z0; y1 = z1;
      }
      return fold4(y0, y1);
    };
    [&]() __attribute__((always_inline)) {
    ARGS_HERE;
    const unsigned l16 = lane * 16;
    unsigned* kvflag = gran + GR_KV + ((LAY2 ? 16 : 0) + tni) * (32 * RK);
    auto wait_sig = [&](int sh) {
#pragma unroll 1
      for (int i = 0; i < 4 * SPIN_MAX; ++i) {
        if (((__builtin_amdgcn_readfirstlane(*LDS_AT(const volatile unsigned, lbase + FLG + 32)) >> sh) & 7u) >= 4u) break;
        __builtin_amdgcn_s_sleep(SSLP);
      }
      asm volatile("" ::: "memory");
      PIN();
    };
    PIN();
    asm volatile("s_waitcnt lgkmcnt(0)" ::: "memory");
    if (!LAY2) asm volatile("s_waitcnt vmcnt(0)" ::: "memory");
    __builtin_amdgcn_s_barrier();
    PIN();
    __builtin_amdgcn_s_setprio(HPRIO);
    if (hw < 3) {
      __builtin_amdgcn_s_sleep(SPRE);
      wait_sig(0);
      __builtin_amdgcn_raw_buffer_store_b32(tag, __builtin_amdgcn_make_buffer_rsrc(kvflag, 0, hw == 0 ? RK * 128 : 0, 0x00020000), lane * 128 + tmi * 4, 0, 16);
      if (hw == 0) STAMP(LAY2 ? 5 : 1, 16);
    }
    if (hw == 3) {
      __builtin_amdgcn_s_sleep(KVS);
      const unsigned kv0 = ld_ag(kvflag + (tmi % RK) * 32 + (lane & 15));
      PIN();
      const char* cnt = reinterpret_cast<const char*>(gran + GR_N + bid * 64 + 2);
      unsigned vk[8], vp = lbase + FLG + 8 + (lane & 7), pw = 0u;
#pragma unroll
      for (int j = 0; j < 8; ++j) { vk[j] = l16 + (j >> 2) * 131072u + ((j >> 1) & 1) * 4096u + (j & 1) * (VOFF - 2048u); asm volatile("" : "+v"(vk[j])); }
#define G4(a, b, c, d, sc) "s_nop 0\n\tglobal_load_lds_dwordx4 %" #a ", %8" sc "\n\tglobal_load_lds_dwordx4 %" #b ", %8 offset:1024" sc "\n\t"             \
                       "global_load_lds_dwordx4 %" #c ", %8 offset:2048" sc "\n\tglobal_load_lds_dwordx4 %" #d ", %8 offset:3072" sc "\n\t"
#define M4 "s_addk_i32 m0, 0x1000\n\t"
      auto stage = [&](const char* sp, unsigned dst) __attribute__((always_inline)) {
        asm volatile("s_mov_b32 m0, %9\n\t" G4(0, 0, 1, 1, " sc1") M4 G4(2, 2, 3, 3, " sc1") M4 G4(4, 4, 5, 5, " sc1") M4 G4(6, 6, 7, 7, " sc1")
                     :: "v"(vk[0]), "v"(vk[1]), "v"(vk[2]), "v"(vk[3]), "v"(vk[4]), "v"(vk[5]), "v"(vk[6]), "v"(vk[7]), "s"(sp), "s"(dst) : "m0", "scc", "memory");
      };
      auto count = [&]() __attribute__((always_inline)) {
        asm volatile("s_mov_b32 m0, %2\n\ts_nop 0\n\tglobal_load_lds_dword %0, %1 sc1" :: "v"(0u), "s"(cnt), "s"(lbase + FLG - 252) : "m0", "memory");
        cnt += 8;
      };
      auto freed = [&](unsigned n) __attribute__((always_inline)) {
        if (!none(pw < n)) {
#pragma unroll 1
          for (int i = 0; i < 4 * SPIN_MAX; ++i) {
            pw = *LDS_AT(const volatile unsigned char, vp);
            if (none(pw < n)) break;
            __builtin_amdgcn_s_sleep(W3SLP);
          }
        }
        asm volatile("" ::: "memory");
      };
      unsigned long long ord = (0xfedcba9876543210ull >> 4 & ~((1ull << 4 * (tmi & 7)) - 1ull)) | (0xfedcba9876543210ull & ((1ull << 4 * (tmi & 7)) - 1ull));
      ord ^= (tmi & 8) * 0x1111111111111111ull;
      unsigned seen;
      const char* kvb = reinterpret_cast<const char*>(KV) + (size_t)tni * (128 * 2048);
      auto panel = [&]() __attribute__((always_inline)) {
        const unsigned pn = (unsigned)ord & 15u;
        ord >>= 4;
        if (__builtin_expect(!((seen >> pn) & 1u), 0)) {
#pragma unroll 1
          for (int i = 0; i < SPIN_MAX && !((seen >> pn) & 1u); ++i) seen = (unsigned)__ballot(ld_ag(kvflag + (tmi % RK) * 32 + (lane & 15)) == tag);
        }
        asm volatile("" ::: "memory");
        return kvb + pn * 8192u;
      };
      asm volatile("" : "+s"(ord), "+s"(kvb), "+s"(cnt));
      asm volatile("" : "+v"(vp), "+v"(pw));
      PIN();
      seen = (unsigned)__ballot(kv0 == tag);
      const char* sp = panel();
      wait_sig(16);
      stage(sp, lbase + 2 * ASB);
      count();
      const int ksp = LAY2 ? PNS / 2 : -1, ksg = LAY2 ? 64 : GF0;
      const u16* gp = gptr(wg, wu);
#pragma unroll 1
      for (int k = 1; k < 15; ++k) {
        if (__builtin_expect(k == ksp, 0)) {
          gu_setup();
          gate_up(xq, rmd);
          PIN();
          *LDS_AT(volatile unsigned char, lbase + FLG + 7) = (unsigned char)0x7f;
        }
        freed((unsigned)(0xba9876543211000ull >> 4 * k) & 15u);
        stage(sp + 2048, lbase + ((2 * k + 1) & 7) * ASB);
        sp = panel();
        stage(sp, lbase + ((2 * k + 2) & 7) * ASB);
        count();
        if (__builtin_expect((unsigned)(k - ksg) < 4u, 0)) gather(gp, k - ksg);
      }
      freed(12u);
      glds16s<true>(csf + 4096 + tni * 128, l16, lbase + UGL);
      stage(sp + 2048, lbase + 7 * ASB);
      count();
      const unsigned vwo = (unsigned)((lane >> 3) * D_ + oni * 64 + ((lane & 7) ^ ((lane >> 3) & 6)) * 8) * 2u;
      constexpr unsigned vs = 8u * D_ * 2u - 1024u;
#pragma unroll 1
      for (int ns = 0; ns < 6; ++ns) {
        freed((unsigned)(ns + 25) >> 1);
        const char* wp = reinterpret_cast<const char*>(wo) + (size_t)ns * (128 * D_ * 2);
#pragma unroll
        for (int j = 0; j < 4; ++j)
          glds16x4<false>(wp + j * (32u * D_ * 2u), vwo, vwo + vs, vwo + 2 * vs, vwo + 3 * vs, lbase + ns * ASB + j * 4096);
      }
      asm volatile("s_waitcnt vmcnt(0)" ::: "memory");
      return;
    }
    if (!LAY2) {
      auto until = [&](unsigned n) {
        while ((__builtin_amdgcn_readfirstlane(*LDS_AT(const volatile unsigned, lbase + FLG + 8)) & 255u) < n) __builtin_amdgcn_s_sleep(16);
        asm volatile("" ::: "memory");
        PIN();
      };
      if (hw != 1) {
        constexpr int N2 = HAND_N + Y_N + 3 * D_ / 2;
        const __amdgpu_buffer_rsrc_t rsi = __builtin_amdgcn_make_buffer_rsrc(hand + (hw ? GRAN_N : 2 * HAND_N), 0, (hw ? N2 : PS_N) * 4, 0x00020000);
        until(IAT);
#pragma unroll 1
        for (int i = 0; i < ((hw ? N2 : PS_N) / 4 + 16383) / 16384; ++i) {
          __builtin_amdgcn_raw_buffer_store_b128(u32x4{SENT, SENT, SENT, SENT}, rsi, ((i * 256 + bid) * 64 + lane) * 16, 0, 16);
          __builtin_amdgcn_s_sleep(ISLP);
        }
      }
      const int ng = loc & 15, fc = xcd * 2 + (loc >> 4);
      const size_t og = (size_t)((fg - xcd * 28) / 7 * 256 + lane) * F_ + (xcd * 7 + fg % 7) * 64;
      const u16* pd = wd + (size_t)(fc * 224 + (lane < 56 ? lane : 0)) * D_ + ng * 64;
      const u16* p = gptr(wg, wu);
#pragma unroll 1
      for (unsigned n = T0; n < T0 + 13u; ++n) {
        until(n);
        const unsigned i = hw == 1 ? n - T0 : n + (8u - T2);
        if (hw == 1 ? i < 8u : (hw == 2 && i - 8u < 5u)) {
          const u16* q = i < 8u ? ((i & 1u) ? wu : wg) + og + (size_t)(i >> 1) * (64 * F_) : pd + (size_t)((i - 8u - (i > TW)) & 3u) * (56 * D_);
          if (i == TW) {
            q = wo + (size_t)(768 + (loc / (32 / OMX)) * (256 / OMX) + (lane & (256 / OMX - 1) & 63)) * D_ + oni * 64;
            unsigned long long a0, a1;
            CODE_ADDR("layers_kernel", a0); CODE_ADDR("end_marker_kernel", a1);
            if (loc < 8) { const unsigned p1 = touch_code(a0, a1, 3 + loc, lane); asm volatile("" :: "v"(p1)); }
          }
          const unsigned p0 = touch(q);
          asm volatile("" :: "v"(p0));
        }
        if (WOT && n == WOT && hw == 2) { const unsigned p1 = touch(wo + (size_t)bid * 4096 + fresh_lane() * 64); asm volatile("" :: "v"(p1)); }
        if (FL2 && n == FL2 && hw == 2) {
          const int ln = fresh_lane() & 15;
          const unsigned p1 = ld_ag(gran + (ln < 4 ? GR_O + (((ln >> 1) * 16 + omi) * RO + oni % RO) * 64 + (ln & 1) * 32 :
                                            (ln < 8 ? GR_X + (tmi * RX + tni % RX) * 128 + (ln & 3) * 32 : GR_KV + ((16 + tni) * RK + tmi % RK) * 32)));
          asm volatile("" :: "v"(p1));
        }
        if (n >= G0 && (n - G0) % GS == 0u && (n - G0) / GS < 4u) gather(p, (n - G0) / GS);
        if (n == UAT && hw == UHW) {
          u32x4 a[4][2], c[2];
#pragma unroll
          for (int i = 0; i < 4; ++i)
#pragma unroll
            for (int h = 0; h < 2; ++h) a[i][h] = *reinterpret_cast<const u32x4*>(wo + (size_t)(tni * 64 + tmi * 4 + i) * D_ + h * 512 + lane * 8);
#pragma unroll
          for (int h = 0; h < 2; ++h) c[h] = *reinterpret_cast<const u32x4*>(w_r + h * 512 + lane * 8);
          auto dot = [&](unsigned x, unsigned y, float z) { return __builtin_amdgcn_fdot2_f32_bf16(__builtin_bit_cast(bf16x2, x), __builtin_bit_cast(bf16x2, y), z, false); };
          PIN();
          __builtin_amdgcn_s_setprio(0);
#pragma unroll 1
          for (int i = 0; i < SPIN_MAX; ++i) {
            if (__builtin_amdgcn_readfirstlane(*LDS_AT(const volatile unsigned, lbase + FLG + 16)) >= UARR) break;
            __builtin_amdgcn_s_sleep(4);
          }
          asm volatile("s_waitcnt vmcnt(0)" ::: "memory");
          PIN();
          float wm = 0.f;
#pragma unroll
          for (int e = 0; e < 8; ++e) wm = dot(c[e >> 2][e & 3], 0x3f803f80u, wm);
          wm = wave_sum(wm);
          lds_wr(lbase + FLG + 60, __float_as_uint(wm));
          wm *= -1.0f / 1024.0f;
          f32x2 u[2], g[2];
#pragma unroll
          for (int i = 0; i < 4; ++i) {
            float p1 = 0.f, pw = 0.f, p2 = 0.f;
#pragma unroll
            for (int e = 0; e < 8; ++e) {
              const unsigned x = a[i][e >> 2][e & 3];
              p1 = dot(x, 0x3f803f80u, p1); pw = dot(x, c[e >> 2][e & 3], pw); p2 = dot(x, x, p2);
            }
            u[i >> 1][i & 1] = fmaf(p1, wm, pw); g[i >> 1][i & 1] = p2;
          }
          float us = fold4(u[0], u[1]), gs = fold4(g[0], g[1]);
          us = dpp_add<0x121, 0xf>(dpp_add<0x122, 0xf>(us)); gs = dpp_add<0x121, 0xf>(dpp_add<0x122, 0xf>(gs));
          __builtin_amdgcn_raw_buffer_store_b32(__float_as_uint((lane & 1) ? __builtin_amdgcn_sqrtf(gs) * (1.0f / 32.0f) : us),
              __builtin_amdgcn_make_buffer_rsrc(csf + 4096 + tni * 128, 0, 512, 0x00020000), (lane & 14) ? 512 : ((lane & 1) * 64 + tmi * 4 + (lane >> 4)) * 4, 0, 16);
        }
      }
      if (hw == 2 && bid == 0) {
        const __amdgpu_buffer_rsrc_t rsc = __builtin_amdgcn_make_buffer_rsrc(gran, 0, GR_N * 4, 0x00020000);
#pragma unroll 1
        for (int spin = 0; spin < SPIN_MAX; ++spin) {
          bool ok = true;
#pragma unroll
          for (int i = 0; i < 4; ++i) ok &= __builtin_amdgcn_raw_buffer_load_b32(rsc, (GR_ROW + (i * 64 + lane) * 32 + 1) * 4, 0, 16) == tag;
          if (__all(ok)) break;
          __builtin_amdgcn_s_sleep(8);
          asm volatile("" ::: "memory");
        }
        if (lane == 0) st_ag(gran, tag);
      }
      return;
    }
    unsigned* X2 = hand + GRAN_N + Y_N;
    unsigned* XS = X2 + X2_N;
    unsigned* P4 = XS + XS_N;
    unsigned* PA = P4 + P4_N;
    const __amdgpu_buffer_rsrc_t rsh = __builtin_amdgcn_make_buffer_rsrc(hand, 0, HAND_N * 4, 0x00020000);
    auto signal = [&](unsigned ph) {
      asm volatile("" ::: "memory");
      PIN();
      *LDS_AT(volatile unsigned char, lbase + FLG + 4 + hw) = (unsigned char)ph;
    };
    auto wait_all = [&](unsigned ph) {
#pragma unroll 1
      for (int i = 0; i < SPIN_MAX; ++i) {
        if (all_ge(__builtin_amdgcn_readfirstlane(*LDS_AT(const volatile unsigned, lbase + FLG + 4)), ph)) break;
        __builtin_amdgcn_s_sleep(WSLP);
      }
      asm volatile("" ::: "memory");
      PIN();
    };
    gu_setup();
    gate_up(xq, rmd);
    signal(hw == 2 ? 2u : 0x7fu);
    if (wid == 8) STAMP(dk, 2);
    if (hw < 2) {
#pragma unroll 1
      for (int i = 0; i < SPIN_MAX; ++i) {
        if (__builtin_amdgcn_readfirstlane(*LDS_AT(const volatile unsigned, lbase + FLG)) >= FSTART) break;
        __builtin_amdgcn_s_sleep(WSLP);
      }
      asm volatile("" ::: "memory");
      PIN();
      if (wid == 8) STAMP(dk, 10);
      u32x2 w[16];
      const __amdgpu_buffer_rsrc_t rd_ = __builtin_amdgcn_make_buffer_rsrc(const_cast<u16*>(wd), 0, D_ * F_ * 2, 0x00020000);
      auto ldw = [&](int c) __attribute__((always_inline)) {
#pragma unroll
        for (int f = 0; f < 16; ++f) w[f] = __builtin_amdgcn_raw_buffer_load_b64(rd_, lane * 8 + c * 512 + (f & 1) * 2048, (fg * 16 + (f & ~1)) * (D_ * 2) + hw * 1024, 0);
        PIN();
      };
      ldw(0);
      const unsigned pt = __builtin_amdgcn_raw_buffer_load_b32(rd_, (lane >> 2) * (D_ * 2) + (lane & 3) * 128 + 512, fg * 16 * (D_ * 2) + hw * 1024, 0);
      int yo = p1blk ? (GRAN_N + fg * D_ + hw * 512) * 4 + lane * 16 : HAND_N * 4;
      asm volatile("" : "+v"(yo));
      wait_all(2u);
      if (wid == 8) STAMP(dk, 3);
      const s16x4 a = act_diag();
      const u32x4 y0 = down(a, w);
      PIN();
      ldw(1);
      __builtin_amdgcn_raw_buffer_store_b128(y0, rsh, yo, 0, 16);
      PIN();
      __builtin_amdgcn_raw_buffer_store_b128(down(a, w), rsh, yo + 1024, 0, 16);
      asm volatile("" :: "v"(pt));
      if (wid == 8) STAMP(dk, 4);
    }
    PIN();
    [&]() __attribute__((always_inline)) {
    const int lane = fresh_lane();
    const float rmb = __uint_as_float(rmd << 16);
    auto first_words = [&](int src, auto nc) __attribute__((always_inline)) {
#pragma unroll 1
      for (int spin = 0; spin < SPIN_MAX; ++spin) {
        unsigned mx = 0u;
#pragma unroll
        for (int i = 0; i < decltype(nc)::value; ++i) mx = umax(mx, __builtin_amdgcn_raw_buffer_load_b32(rsh, src, i * (56 * D_ * 4), 16));
        if (none(mx == SENT)) break;
        __builtin_amdgcn_s_sleep(PSLP);
        asm volatile("" ::: "memory");
      }
    };
    if (hw == 2) {
      wait_all(2u);
      if (own) {
        const float rden = __uint_as_float(rmd & 0xffff0000u), rrin = frcp(rden);
        const int col = ow * 16 + (lane & 3) * 4 + (lane >> 4);
        const unsigned xo2 = ld_ag(reinterpret_cast<const unsigned*>(A + (size_t)idx * D_ + (col & ~1)));
        __builtin_amdgcn_s_sleep(PRE1);
        first_words((GRAN_N + (lane < 56 ? lane : 0) * D_ + ow * 16) * 4, std::integral_constant<int, 4>{});
        const float y = y_sum(rsh, std::integral_constant<int, 2>{});
        const unsigned rk = best >> 16;
        const float rv = __uint_as_float(((rk & 0x8000u) ? (rk & 0x7fffu) : (~rk & 0xffffu)) << 16);
        const float rs = rbf(frcp(1.0f + __expf(-rv)));
        const float x1v = __uint_as_float((col & 1) ? xo2 & 0xffff0000u : xo2 << 16);
        float h0 = x1v, h1 = x1v;
        ln_apply2(h0, h1, rmb, rden, rrin);
        const float x2v = rbf(x1v + rbf(h0 + rbf(rbf(y) * rs)));
        const auto pr = __builtin_amdgcn_permlane16_swap(__float_as_uint(x2v), __float_as_uint(x2v), false, false);
        __builtin_amdgcn_raw_buffer_store_b32(f2word(__uint_as_float((pr[0] >> 16) | pr[1])), rsh, (lane & 28) ? HAND_N * 4 : (int)(X2 - hand) * 4 + col * 2, 0, 16);
        const float d = x2v - rmb;
        const float sd = wave_sum(d), sq = wave_sum(d * d);
        if (lane < 2) st_ag(XS + ow * 2 + lane, f2word(lane ? sq : sd));
        STAMP(dk, 5);
      }
    }
    {
      const int ks = kcap ^ (tmi & 8), g4 = lane >> 4;
      const int src = (int)(X2 - hand) * 4 + ks * 128 + g4 * 16;
      int dst = (lane & 12) ? HAND_N * 4 : (int)((P4 - hand) + (tni * 16 + ks) * 192 + hw * 64 + (lane & 2) * 16 + (lane & 1) * 8 + (g4 >> 1) * 16 + (g4 & 1) * 4) * 4;
      asm volatile("" : "+v"(dst));
      u32x4 x0, x1;
      if (!(hw == 2 && own)) __builtin_amdgcn_s_sleep(PRE4);
#pragma unroll 1
      for (int spin = 0; spin < SPIN_MAX; ++spin) {
        x0 = __builtin_amdgcn_raw_buffer_load_b128(rsh, src, 0, 16); x1 = __builtin_amdgcn_raw_buffer_load_b128(rsh, src + 64, 0, 16);
        if (none(umax(umax(umax(x0[0], x0[1]), umax(x0[2], x0[3])), umax(umax(x1[0], x1[1]), umax(x1[2], x1[3]))) == SENT)) break;
        __builtin_amdgcn_s_sleep(PSLP);
        asm volatile("" ::: "memory");
      }
      f32x4 d[4];
#pragma unroll
      for (int i = 0; i < 4; ++i) d[i] = MFMA16(sl[2 * i], __builtin_bit_cast(s16x8, x0), (f32x4{0.f, 0.f, 0.f, 0.f}));
#pragma unroll
      for (int i = 0; i < 4; ++i) d[i] = MFMA16(sl[2 * i + 1], __builtin_bit_cast(s16x8, x1), d[i]);
      u32x4 o;
#pragma unroll
      for (int e = 0; e < 4; ++e) o[e] = f2word((lane & 2) ? ((lane & 1) ? d[3][e] : d[2][e]) : ((lane & 1) ? d[1][e] : d[0][e]));
      __builtin_amdgcn_raw_buffer_store_b128(o, rsh, dst, 0, 16);
      PIN();
      if (wid == 8) STAMP(dk, 6);
    }
    if (hw == 2) {
      const int col = (lane & 15) * 4 + (lane >> 4), src = (int)((P4 - hand) + (tni * 16 + (lane >> 4)) * 192 + (lane & 15) * 4) * 4;
      const __amdgpu_buffer_rsrc_t rsq = __builtin_amdgcn_make_buffer_rsrc(hand, 0, fast ? HAND_N * 4 : 0, 0x00020000);
      float cs[3], val[3];
#pragma unroll
      for (int p = 0; p < 3; ++p) cs[p] = __uint_as_float(ld_ag(reinterpret_cast<const unsigned*>(csf) + p * 1024 + n0 + col));
      u32x2 xs;
      __builtin_amdgcn_s_sleep(PRE7);
      first_words((int)((P4 - hand) + (tni * 16 + (lane & 15)) * 192 + ((lane >> 4) % 3) * 64) * 4, std::integral_constant<int, 1>{});
#pragma unroll 1
      for (int spin = 0; spin < SPIN_MAX; ++spin) {
        u32x4 v[3][4];
        xs = __builtin_amdgcn_raw_buffer_load_b64(rsh, (int)((XS - hand) + lane * 2) * 4, 0, 16);
#pragma unroll
        for (int p = 2; p >= 0; --p)
#pragma unroll
          for (int i = 0; i < 4; ++i) v[p][i] = __builtin_amdgcn_raw_buffer_load_b128(p ? rsh : rsq, src + p * 256, i * (4 * 192 * 4), 16);
        auto part = [&](int p) __attribute__((always_inline)) {
          float z[4];
#pragma unroll
          for (int e = 0; e < 4; ++e) {
            z[e] = fadd(fadd(__uint_as_float(v[p][0][e]), __uint_as_float(v[p][1][e])), fadd(__uint_as_float(v[p][2][e]), __uint_as_float(v[p][3][e])));
          }
          val[p] = fold16(f32x2{z[0], z[1]}, f32x2{z[2], z[3]});
        };
        part(2); part(1);
        val[0] = 0.f;
        if (fast) part(0);
        const float chk = fadd(fadd(fadd(val[0], val[1]), fadd(val[2], __uint_as_float(xs[0]))), __uint_as_float(xs[1]));
        if (none(chk != chk)) break;
        unsigned mx = umax(xs[0], xs[1]);
#pragma unroll
        for (int i = 0; i < 12; ++i) mx = umax(umax(umax(mx, v[i >> 2][i & 3][0]), v[i >> 2][i & 3][1]), umax(v[i >> 2][i & 3][2], v[i >> 2][i & 3][3]));
        if (none(mx == SENT)) break;
        __builtin_amdgcn_s_sleep(PSLP);
        asm volatile("" ::: "memory");
      }
      const auto sw = __builtin_amdgcn_permlane32_swap(xs[0], xs[1], false, false);
      float sm = __uint_as_float(sw[0]) + __uint_as_float(sw[1]);
      sm = dpp_add<0x142, 0xa>(dpp_add<0x118, 0xf>(dpp_add<0x114, 0xf>(dpp_add<0x112, 0xf>(dpp_add<0x111, 0xf>(sm)))));
      const float dm = __int_as_float(__builtin_amdgcn_readlane(__float_as_int(sm), 31)) * (0.25f / 1024.0f);
      const float var = fmaxf(__int_as_float(__builtin_amdgcn_readlane(__float_as_int(sm), 63)) * (0.25f / 1024.0f) - dm * dm, 0.f);
      const float mb = rbf(rmb + dm);
      const float rin = frcp(rbf(__builtin_amdgcn_sqrtf(rbf(rbf(var) + 1e-5f))));
      const unsigned kv = pack2((val[1] - mb * cs[1]) * rin, (val[2] - mb * cs[2]) * rin);
      *LDS_AT(u16, lbase + TAB + 128 + kpos(col) * 2) = (u16)kv;
      *LDS_AT(u16, lbase + TAB + 256 + col * 2) = (u16)(kv >> 16);
      if (fast) *LDS_AT(u16, lbase + TAB + kpos(col) * 2) = f2bf((val[0] - mb * cs[0]) * rin * QSCALE);
      signal(7u);
      STAMP(dk, 7);
      if (own) {
#pragma unroll
        for (int i = 0; i < 14; ++i)
          __builtin_amdgcn_raw_buffer_store_b128(u32x4{SENT, SENT, SENT, SENT}, rsh, (GRAN_N + (i * 16 + (lane >> 2)) * D_ + ow * 16 + (lane & 3) * 4) * 4, 0, 16);
      }
      return;
    }
    if (!fast) return;
    const int head = tni * 2 + hw;
    const unsigned tb = lbase + TAB + hw * 64;
    {
      const __amdgpu_buffer_rsrc_t rs = __builtin_amdgcn_make_buffer_rsrc(KV, 0, 2 * S_ * D_ * 2, 0x00020000);
      const int src = (head * 64 + tmi * 4) * 2048 + lane * 16;
      auto ld = [&](int i, unsigned off) { return __builtin_bit_cast(s16x8, __builtin_amdgcn_raw_buffer_load_b128(rs, src + (i & 3) * 1024, (i >> 3) * 65536 + ((i >> 2) & 1) * 4096 + off, 16)); };
#pragma unroll 1
      for (int i = 0; i < SPIN_MAX; ++i)
        if (__builtin_amdgcn_readfirstlane(ld_ag(kvflag + (tmi % RK) * 32 + tmi + 8)) == tag) break;
      asm volatile("" ::: "memory");
      s16x8 f[16];
#pragma unroll
      for (int i = 0; i < 16; ++i) f[i] = ld(i, 0u);
      PIN();
      const bool on = ((lane ^ (lane >> 4)) & 1) == 0;
      const unsigned pl = lbase + RED + hw * 1024;
      unsigned pw = pl + (lane >> 4) * 64 + (lane & 15) * 2, pr = pl + (on ? (lane >> 5) * 16 : 512), qr = on ? tb + (lane >> 5) * 16 : pl + 512;
      const int gone = (((idx >> 7) & 7) == tmi) ? ((idx >> 10) * 2 + (kpos(idx & 31) >> 4)) * 64 + ((idx >> 5) & 3) * 16 + (kpos(idx & 31) & 15) : -1;
      asm volatile("" : "+v"(pw), "+v"(pr), "+v"(qr));
      *LDS_AT(u32x2, pl + 512 + lane * 8) = u32x2{0u, 0u};
      wait_all(7u);
      u32x4 q[2];
#pragma unroll
      for (int sx = 0; sx < 2; ++sx) {
        q[sx] = *LDS_AT(const u32x4, qr + sx * 32);
      }
      float sc[4];
#pragma unroll
      for (int i = 0; i < 8; ++i) {
        f32x4 d = MFMA16(__builtin_bit_cast(s16x8, q[0]), f[2 * i], (f32x4{0.f, 0.f, 0.f, 0.f}));
        d = MFMA16(__builtin_bit_cast(s16x8, q[1]), f[2 * i + 1], d);
        PIN();
        f[2 * i] = ld(2 * i, VOFF); f[2 * i + 1] = ld(2 * i + 1, VOFF);
        PIN();
#pragma unroll
        for (int r = 0; r < 2; ++r) sc[(i >> 2) * 2 + r] = (i & 3) == 0 || (lane >> 4) == (i & 3) ? d[r] : sc[(i >> 2) * 2 + r];
      }
      if (gone >= 0) {
#pragma unroll
        for (int r = 0; r < 4; ++r) sc[r] = r * 64 + lane == gone ? -INFINITY : sc[r];
      }
      const float mx = wave_fmax(fmaxf(fmaxf(sc[0], sc[1]), fmaxf(sc[2], sc[3])));
      const unsigned p0 = pack2(__builtin_amdgcn_exp2f(sc[0] - mx), __builtin_amdgcn_exp2f(sc[1] - mx)), p1 = pack2(__builtin_amdgcn_exp2f(sc[2] - mx), __builtin_amdgcn_exp2f(sc[3] - mx));
      *LDS_AT(u16, pw) = (u16)p0; *LDS_AT(u16, pw + 32) = (u16)(p0 >> 16);
      *LDS_AT(u16, pw + 256) = (u16)p1; *LDS_AT(u16, pw + 288) = (u16)(p1 >> 16);
      asm volatile("" ::: "memory");
      const float ls = wave_sum((__uint_as_float(p0 << 16) + __uint_as_float(p0 & 0xffff0000u)) + (__uint_as_float(p1 << 16) + __uint_as_float(p1 & 0xffff0000u)));
      f32x4 o = {0.f, 0.f, 0.f, 0.f};
#pragma unroll
      for (int i = 0; i < 16; ++i) o = MFMA16(*LDS_AT(const s16x8, pr + i * 32), f[i], o);
      int pv = __float_as_int((lane & 16) ? o[1] : o[0]);
      asm volatile("s_nop 0\n\tv_writelane_b32 %0, %1, 32\n\tv_writelane_b32 %0, %2, 33" : "+v"(pv) : "s"(ls), "s"(mx));
      __builtin_amdgcn_raw_buffer_store_b32(f2word(__int_as_float(pv)), rsh, lane < 34 ? (int)((PA - hand) + (head * 8 + tmi) * 64 + lane) * 4 : HAND_N * 4, 0, 16);
    }
    if (wid == 8) STAMP(dk, 8);
    if (tmi != ((idx >> 7) & 7)) return;
    {
      const int src = (int)((PA - hand) + head * 512 + (lane < 34 ? lane : 0)) * 4;
      const unsigned ta = tb + (lane & 31) * 2;
      const float so = wave_sum(lane < 32 ? bf2f(*LDS_AT(const u16, ta)) * bf2f(*LDS_AT(const u16, ta + 128)) : 0.f);
      const float vo = lane < 32 ? bf2f(*LDS_AT(const u16, ta + 256)) : 1.0f;
      unsigned v[8];
      __builtin_amdgcn_s_sleep(PRE5);
#pragma unroll 1
      for (int spin = 0; spin < SPIN_MAX; ++spin) {
        unsigned mx = 0u;
#pragma unroll
        for (int i = 0; i < 8; ++i) v[i] = __builtin_amdgcn_raw_buffer_load_b32(rsh, src + i * 256, 0, 16);
#pragma unroll
        for (int i = 0; i < 8; ++i) mx = umax(mx, v[i]);
        if (none(mx == SENT)) break;
        __builtin_amdgcn_s_sleep(PSLP);
        asm volatile("" ::: "memory");
      }
      float m8[8], mx = so;
#pragma unroll
      for (int i = 0; i < 8; ++i) { m8[i] = __int_as_float(__builtin_amdgcn_readlane((int)v[i], 33)); mx = fmaxf(mx, m8[i]); }
      float acc = __builtin_amdgcn_exp2f(so - mx) * vo;
#pragma unroll
      for (int i = 0; i < 8; ++i) acc += __builtin_amdgcn_exp2f(m8[i] - mx) * __uint_as_float(v[i]);
      const float l = __int_as_float(__builtin_amdgcn_readlane(__float_as_int(acc), 32));
      const float ov = acc / l;
      const float o1 = __int_as_float(__builtin_amdgcn_update_dpp(0, __float_as_int(ov), 0x55, 0xf, 0xf, false));
      const float o2 = __int_as_float(__builtin_amdgcn_update_dpp(0, __float_as_int(ov), 0xaa, 0xf, 0xf, false));
      const float o3 = __int_as_float(__builtin_amdgcn_update_dpp(0, __float_as_int(ov), 0xff, 0xf, 0xf, false));
      const __amdgpu_buffer_rsrc_t rso = __builtin_amdgcn_make_buffer_rsrc(Of, 0, S_ * D_ * 2, 0x00020000);
      if (lane < 32 && (lane & 3) == 0)
        __builtin_amdgcn_raw_buffer_store_b64(u32x2{pack2(ov, o1), pack2(o2, o3)}, rso,
            (((idx >> 5) * 32 + head) * 1024 + (lane >> 4) * 512 + (((lane >> 2) & 1) * 32 + (idx & 31)) * 8 + ((lane >> 3) & 1) * 4) * 2, 0, 16);
      asm volatile("s_waitcnt vmcnt(0)" ::: "memory");
      if (lane < RO) st_ag(gran + GR_O + ((16 + (idx >> 7)) * RO + lane) * 64 + 16 + head, tag);
      STAMP(dk, 9);
    }
    }();
    }();
    __builtin_amdgcn_s_setprio(0);
    if (LAY2 || hw != UHW) asm volatile("s_waitcnt vmcnt(0)" ::: "memory");
    PIN();
    ARGS_HERE;
    u32x2 w[16];
    auto ld_wd = [&]() __attribute__((always_inline)) {
      const __amdgpu_buffer_rsrc_t rd_ = __builtin_amdgcn_make_buffer_rsrc(const_cast<u16*>(wd), 0, D_ * F_ * 2, 0x00020000);
#pragma unroll
      for (int f = 0; f < 16; ++f) w[f] = __builtin_amdgcn_raw_buffer_load_b64(rd_, t * 8 + (f & 1) * 2048, (fg * 16 + (f & ~1)) * (D_ * 2), 0);
      PIN();
    };
    float fnum = 0.f, fvar = 0.f;
    const int rrow = omi * 128 + (oni >> 3) * 64 + (hw & 1) * 32;
    if (LAY2) {
      ld_wd();
      if (!p1blk) {
        const int fr = rrow + (lane & 7) * 4 + (lane >> 4);
        const double s1 = ld_ag(stats + fr), s2 = ld_ag(stats + S_ + fr), sw = ld_ag(stats + 2 * S_ + fr);
        lds_wr(lbase + SPM + 132, __float_as_uint(wsum));
        float mean, var, mb, den;
        ln_from_sums(s1, s2, mean, var, mb, den);
        const float g1 = 1.0f + frcp(den);
        fnum = fr == idx ? __uint_as_float(0x7fc00000u) : fmaf(-mean, wsum, (float)sw) * g1;
        fvar = var * g1 * g1;
      }
      asm volatile("" : "+v"(fnum), "+v"(fvar));
      PIN();
    }
    if (hw == 3) {
      const int nfl = (LAY2 && (idx >> 7) == omi) ? 48 : 16;
      auto pfl = (__attribute__((address_space(1))) const unsigned*)(gran + GR_O + (((LAY2 ? 16 : 0) + omi) * RO + oni % RO) * 64 + (lane < nfl ? lane : 0));
      constexpr unsigned vs = 8u * D_ * 2u - 1024u;
      unsigned vw0 = (unsigned)((lane >> 3) * D_ + oni * 64 + ((lane & 7) ^ ((lane >> 3) & 6)) * 8) * 2u, vw1 = vw0 + vs, vw2 = vw0 + 2 * vs, vw3 = vw0 + 3 * vs;
      asm volatile("" : "+v"(pfl), "+v"(vw0), "+v"(vw1), "+v"(vw2), "+v"(vw3));
      auto wait_arr = [&](unsigned n) {
#pragma unroll 1
        for (int i = 0; i < 4 * SPIN_MAX; ++i) {
          if (__builtin_amdgcn_readfirstlane(*LDS_AT(const volatile unsigned, lbase + FLG + 16)) >= n) break;
          __builtin_amdgcn_s_sleep(1);
        }
        asm volatile("" ::: "memory");
        PIN();
      };
      wait_arr(12u);
      unsigned fl;
      asm volatile("global_load_dword %0, %1, off sc1" : "=v"(fl) : "v"(pfl) : "memory");
#pragma unroll 1
      for (int i = 0; i < 4 * SPIN_MAX; ++i) {
        if (__builtin_amdgcn_readfirstlane(*LDS_AT(const volatile unsigned, lbase + FLG + 24)) >= 8u) break;
        __builtin_amdgcn_s_sleep(1);
      }
      asm volatile("" ::: "memory");
      PIN();
#pragma unroll
      for (int j = 24; j < 32; ++j)
        glds16x4<false>(reinterpret_cast<const char*>(wo) + j * (32u * D_ * 2u), vw0, vw1, vw2, vw3, lbase + j * 4096);
      unsigned keep;
      asm volatile("s_mov_b32 %0, m0\n\ts_mov_b32 m0, %3\n\ts_nop 2\n\tglobal_load_lds_dword %1, %2 sc1\n\ts_mov_b32 m0, %0"
                   : "=&s"(keep) : "v"(0u), "s"(reinterpret_cast<const char*>(gran + GR_N + bid * 64 + 39)), "s"(lbase + FLG - 252) : "memory");
      asm volatile("s_waitcnt vmcnt(33)" : "+v"(fl) :: "memory");
      if (__builtin_amdgcn_ballot_w64(fl != tag)) {
        __builtin_amdgcn_s_sleep(CSL1);
#pragma unroll 1
        for (int spin = 0; spin < SPIN_MAX; ++spin) {
          if (!__builtin_amdgcn_ballot_w64(__hip_atomic_load(pfl, __ATOMIC_RELAXED, __HIP_MEMORY_SCOPE_AGENT) != tag)) break;
          __builtin_amdgcn_s_sleep(CSLP);
        }
      }
      asm volatile("" ::: "memory");
      PIN();
    }
    u32x4 pv[4];
    asm volatile("" : "=v"(pv[0]), "=v"(pv[1]), "=v"(pv[2]), "=v"(pv[3]));
    if (LAY2 && !p1blk && hw < 2) {
      const __amdgpu_buffer_rsrc_t rsp = __builtin_amdgcn_make_buffer_rsrc(hand + 2 * HAND_N + PP_O, 0, H_ * S_ * 4, 0x00020000);
      const int src = ((lane >> 3) * S_ + rrow + (lane & 7) * 4) * 4;
#pragma unroll 1
      for (int i = 0; i < 4 * SPIN_MAX; ++i) {
        if (__builtin_amdgcn_readfirstlane(*LDS_AT(const volatile unsigned, lbase + FLG + 24)) >= 8u) break;
        __builtin_amdgcn_s_sleep(2);
      }
#pragma unroll 1
      for (int spin = 0; spin < SPIN_MAX; ++spin) {
        asm volatile("" ::: "memory");
        unsigned mx = 0u;
#pragma unroll
        for (int i = 0; i < 4; ++i) pv[i] = __builtin_amdgcn_raw_buffer_load_b128(rsp, src, i * (8 * S_ * 4), 16);
#pragma unroll
        for (int i = 0; i < 4; ++i) mx = umax(umax(umax(mx, pv[i][0]), pv[i][1]), umax(pv[i][2], pv[i][3]));
        if (none(mx == SENT)) break;
        __builtin_amdgcn_s_sleep(PPSLP);
      }
    }
    asm volatile("s_waitcnt lgkmcnt(0)" ::: "memory");
    __builtin_amdgcn_s_barrier();
    if (!LAY2) {
      PIN();
      asm volatile(AGAIN "\ngate_up_code:\n\t"
                 "v_lshlrev_b32 v84, 16, v100\n\tv_and_b32 v85, 0xffff0000, v100\n\tv_lshlrev_b32 v86, 16, v101\n\tv_and_b32 v87, 0xffff0000, v101\n\t"
                 "v_subrev_f32 v84, s84, v84\n\tv_subrev_f32 v85, s84, v85\n\tv_subrev_f32 v86, s84, v86\n\tv_subrev_f32 v87, s84, v87\n\t"
                 "v_cvt_pk_bf16_f32 v85, v84, v85\n\tv_cvt_pk_bf16_f32 v87, v86, v87\n\ts_mov_b32 s88, 0x01000100\n\ts_mov_b32 s89, 0x03020302\n\t"
                 "v_lshlrev_b32 v84, 16, v85\n\tv_and_b32 v85, 0xffff0000, v85\n\tv_lshlrev_b32 v86, 16, v87\n\tv_and_b32 v87, 0xffff0000, v87\n\t"
                 "v_mul_f32 v88, s86, v84\n\tv_mul_f32 v89, s86, v85\n\tv_mul_f32 v90, s86, v86\n\tv_mul_f32 v91, s86, v87\n\tv_fma_f32 v84, -v88, s85, v84\n\t"
                 "v_fma_f32 v85, -v89, s85, v85\n\tv_fma_f32 v86, -v90, s85, v86\n\tv_fma_f32 v87, -v91, s85, v87\n\tv_fmac_f32 v88, s86, v84\n\t"
                 "v_fmac_f32 v89, s86, v85\n\tv_fmac_f32 v90, s86, v86\n\tv_fmac_f32 v91, s86, v87\n\tv_cvt_pk_bf16_f32 v88, v88, v89\n\t"
                 "v_cvt_pk_bf16_f32 v90, v90, v91\n\tv_perm_b32 v92, v88, v88, s88\n\tv_perm_b32 v93, v88, v88, s89\n\tv_perm_b32 v94, v90, v90, s88\n\t"
                 "v_perm_b32 v95, v90, v90, s89\n\ts_nop 0\n\tv_and_b32_dpp v96, v92, v102 quad_perm:[0,0,0,0] row_mask:0xf bank_mask:0xf\n\t"
                 "v_and_b32_dpp v97, v92, v103 quad_perm:[0,0,0,0] row_mask:0xf bank_mask:0xf\n\t"
                 "v_and_b32_dpp v100, v93, v102 quad_perm:[0,0,0,0] row_mask:0xf bank_mask:0xf\n\t"
                 "v_and_b32_dpp v101, v93, v103 quad_perm:[0,0,0,0] row_mask:0xf bank_mask:0xf\n\tv_mfma_f32_4x4x4_16b_bf16 v[84:87], v[96:97], v[104:105], 0\n\t"
                 "v_mfma_f32_4x4x4_16b_bf16 v[88:91], v[96:97], v[106:107], 0\n\tv_and_b32_dpp v96, v94, v102 quad_perm:[0,0,0,0] row_mask:0xf bank_mask:0xf\n\t"
                 "v_and_b32_dpp v97, v94, v103 quad_perm:[0,0,0,0] row_mask:0xf bank_mask:0xf\n\t"
                 "v_mfma_f32_4x4x4_16b_bf16 v[84:87], v[100:101], v[108:109], v[84:87]\n\tv_mfma_f32_4x4x4_16b_bf16 v[88:91], v[100:101], v[110:111], v[88:91]\n\t"
                 "v_and_b32_dpp v100, v95, v102 quad_perm:[0,0,0,0] row_mask:0xf bank_mask:0xf\n\t"
                 "v_and_b32_dpp v101, v95, v103 quad_perm:[0,0,0,0] row_mask:0xf bank_mask:0xf\n\tv_mfma_f32_4x4x4_16b_bf16 v[84:87], v[96:97], v[112:113], v[84:87]\n\t"
                 "v_mfma_f32_4x4x4_16b_bf16 v[88:91], v[96:97], v[114:115], v[88:91]\n\tv_and_b32_dpp v96, v92, v102 quad_perm:[1,1,1,1] row_mask:0xf bank_mask:0xf\n\t"
                 "v_and_b32_dpp v97, v92, v103 quad_perm:[1,1,1,1] row_mask:0xf bank_mask:0xf\n\t"
                 "v_mfma_f32_4x4x4_16b_bf16 v[84:87], v[100:101], v[116:117], v[84:87]\n\tv_mfma_f32_4x4x4_16b_bf16 v[88:91], v[100:101], v[118:119], v[88:91]\n\t"
                 "v_and_b32_dpp v100, v93, v102 quad_perm:[1,1,1,1] row_mask:0xf bank_mask:0xf\n\t"
                 "v_and_b32_dpp v101, v93, v103 quad_perm:[1,1,1,1] row_mask:0xf bank_mask:0xf\n\tv_mfma_f32_4x4x4_16b_bf16 v[84:87], v[96:97], v[120:121], v[84:87]\n\t"
                 "v_mfma_f32_4x4x4_16b_bf16 v[88:91], v[96:97], v[122:123], v[88:91]\n\tv_and_b32_dpp v96, v94, v102 quad_perm:[1,1,1,1] row_mask:0xf bank_mask:0xf\n\t"
                 "v_and_b32_dpp v97, v94, v103 quad_perm:[1,1,1,1] row_mask:0xf bank_mask:0xf\n\t"
                 "v_mfma_f32_4x4x4_16b_bf16 v[84:87], v[100:101], v[124:125], v[84:87]\n\tv_mfma_f32_4x4x4_16b_bf16 v[88:91], v[100:101], v[126:127], v[88:91]\n\t"
                 "v_and_b32_dpp v100, v95, v102 quad_perm:[1,1,1,1] row_mask:0xf bank_mask:0xf\n\t"
                 "v_and_b32_dpp v101, v95, v103 quad_perm:[1,1,1,1] row_mask:0xf bank_mask:0xf\n\tv_mfma_f32_4x4x4_16b_bf16 v[84:87], v[96:97], v[128:129], v[84:87]\n\t"
                 "v_mfma_f32_4x4x4_16b_bf16 v[88:91], v[96:97], v[130:131], v[88:91]\n\tv_and_b32_dpp v96, v92, v102 quad_perm:[2,2,2,2] row_mask:0xf bank_mask:0xf\n\t"
                 "v_and_b32_dpp v97, v92, v103 quad_perm:[2,2,2,2] row_mask:0xf bank_mask:0xf\n\t"
                 "v_mfma_f32_4x4x4_16b_bf16 v[84:87], v[100:101], v[132:133], v[84:87]\n\tv_mfma_f32_4x4x4_16b_bf16 v[88:91], v[100:101], v[134:135], v[88:91]\n\t"
                 "v_and_b32_dpp v100, v93, v102 quad_perm:[2,2,2,2] row_mask:0xf bank_mask:0xf\n\t"
                 "v_and_b32_dpp v101, v93, v103 quad_perm:[2,2,2,2] row_mask:0xf bank_mask:0xf\n\tv_mfma_f32_4x4x4_16b_bf16 v[84:87], v[96:97], v[136:137], v[84:87]\n\t"
                 "v_mfma_f32_4x4x4_16b_bf16 v[88:91], v[96:97], v[138:139], v[88:91]\n\tv_and_b32_dpp v96, v94, v102 quad_perm:[2,2,2,2] row_mask:0xf bank_mask:0xf\n\t"
                 "v_and_b32_dpp v97, v94, v103 quad_perm:[2,2,2,2] row_mask:0xf bank_mask:0xf\n\t"
                 "v_mfma_f32_4x4x4_16b_bf16 v[84:87], v[100:101], v[140:141], v[84:87]\n\tv_mfma_f32_4x4x4_16b_bf16 v[88:91], v[100:101], v[142:143], v[88:91]\n\t"
                 "v_and_b32_dpp v100, v95, v102 quad_perm:[2,2,2,2] row_mask:0xf bank_mask:0xf\n\t"
                 "v_and_b32_dpp v101, v95, v103 quad_perm:[2,2,2,2] row_mask:0xf bank_mask:0xf\n\tv_mfma_f32_4x4x4_16b_bf16 v[84:87], v[96:97], v[144:145], v[84:87]\n\t"
                 "v_mfma_f32_4x4x4_16b_bf16 v[88:91], v[96:97], v[146:147], v[88:91]\n\tv_and_b32_dpp v96, v92, v102 quad_perm:[3,3,3,3] row_mask:0xf bank_mask:0xf\n\t"
                 "v_and_b32_dpp v97, v92, v103 quad_perm:[3,3,3,3] row_mask:0xf bank_mask:0xf\n\t"
                 "v_mfma_f32_4x4x4_16b_bf16 v[84:87], v[100:101], v[148:149], v[84:87]\n\tv_mfma_f32_4x4x4_16b_bf16 v[88:91], v[100:101], v[150:151], v[88:91]\n\t"
                 "v_and_b32_dpp v100, v93, v102 quad_perm:[3,3,3,3] row_mask:0xf bank_mask:0xf\n\t"
                 "v_and_b32_dpp v101, v93, v103 quad_perm:[3,3,3,3] row_mask:0xf bank_mask:0xf\n\tv_mfma_f32_4x4x4_16b_bf16 v[84:87], v[96:97], v[152:153], v[84:87]\n\t"
                 "v_mfma_f32_4x4x4_16b_bf16 v[88:91], v[96:97], v[154:155], v[88:91]\n\tv_and_b32_dpp v96, v94, v102 quad_perm:[3,3,3,3] row_mask:0xf bank_mask:0xf\n\t"
                 "v_and_b32_dpp v97, v94, v103 quad_perm:[3,3,3,3] row_mask:0xf bank_mask:0xf\n\t"
                 "v_mfma_f32_4x4x4_16b_bf16 v[84:87], v[100:101], v[156:157], v[84:87]\n\tv_mfma_f32_4x4x4_16b_bf16 v[88:91], v[100:101], v[158:159], v[88:91]\n\t"
                 "v_and_b32_dpp v100, v95, v102 quad_perm:[3,3,3,3] row_mask:0xf bank_mask:0xf\n\t"
                 "v_and_b32_dpp v101, v95, v103 quad_perm:[3,3,3,3] row_mask:0xf bank_mask:0xf\n\tv_mfma_f32_4x4x4_16b_bf16 v[84:87], v[96:97], v[160:161], v[84:87]\n\t"
                 "v_mfma_f32_4x4x4_16b_bf16 v[88:91], v[96:97], v[162:163], v[88:91]\n\ts_nop 1\n\t"
                 "v_mfma_f32_4x4x4_16b_bf16 v[84:87], v[100:101], v[164:165], v[84:87]\n\tv_mfma_f32_4x4x4_16b_bf16 v[88:91], v[100:101], v[166:167], v[88:91]\n\t"
                 "s_nop 4\n\tv_permlane32_swap_b32 v84, v88\n\tv_permlane32_swap_b32 v85, v89\n\tv_permlane32_swap_b32 v86, v90\n\tv_permlane32_swap_b32 v87, v91\n\t"
                 "v_add_f32 v84, v84, v88\n\tv_add_f32 v85, v85, v89\n\tv_add_f32 v86, v86, v90\n\tv_add_f32 v87, v87, v91\n\ts_nop 0\n\t"
                 "v_permlane16_swap_b32 v84, v85\n\tv_permlane16_swap_b32 v86, v87\n\ts_nop 0\n\tv_add_f32 v84, v84, v85\n\tv_add_f32 v86, v86, v87\n\ts_nop 1\n\t"
                 "v_add_f32_dpp v84, v84, v84 row_ror:8 row_mask:0xf bank_mask:0xf\n\tv_add_f32_dpp v86, v86, v86 row_ror:8 row_mask:0xf bank_mask:0xf\n\ts_nop 0\n\t"
                 "v_add_f32_dpp v84, v84, v84 row_ror:4 row_mask:0xf bank_mask:0xf\n\tv_add_f32_dpp v86, v86, v86 row_ror:4 row_mask:0xf bank_mask:0xf\n\t"
                 "ds_write2_b32 v99, v84, v86 offset1:8\n\ts_setpc_b64 s[92:93]\n\t"
                   :: "{s[0:1]}"(ap + 1), "{s2}"(bid), "{v0}"(wid * 64), KEPT() : "s90", "s91", "scc", "memory");
      __builtin_unreachable();
    }
    {
    constexpr int dk = 8;
    constexpr int BEST = FLG + 40, MEET = FLG + 48;
    __builtin_amdgcn_s_setprio(HPRIO);
    if (hw == 0) STAMP(dk, 0);
    const unsigned tb1 = (unsigned)__builtin_amdgcn_s_memrealtime();
    unsigned* hand2 = hand + HAND_N;
    const __amdgpu_buffer_rsrc_t rsh = __builtin_amdgcn_make_buffer_rsrc(hand2, 0, (HAND_N + PS_N) * 4, 0x00020000);
    const __amdgpu_buffer_rsrc_t rsx = __builtin_amdgcn_make_buffer_rsrc(xout, 0, 3 * D_ * 2, 0x00020000);
    constexpr int OOR = (HAND_N + PS_N) * 4;
    if (!p1blk && hw < 2) {
      f32x2 a[2], c[2];
#pragma unroll
      for (int e = 0; e < 4; ++e) {
        a[e >> 1][e & 1] = (__uint_as_float(pv[0][e] << 16) + __uint_as_float(pv[1][e] << 16)) + (__uint_as_float(pv[2][e] << 16) + __uint_as_float(pv[3][e] << 16));
        c[e >> 1][e & 1] = (__uint_as_float(pv[0][e] & 0xffff0000u) + __uint_as_float(pv[1][e] & 0xffff0000u)) + (__uint_as_float(pv[2][e] & 0xffff0000u) + __uint_as_float(pv[3][e] & 0xffff0000u));
      }
      const float r = (fnum + fold8(a[0], a[1])) * __builtin_amdgcn_rsqf(fvar + fold8(c[0], c[1]));
      unsigned key = (__float_as_uint(r) ^ ((unsigned)(__float_as_int(r) >> 31) | 0x80000000u)) & 0xfffff800u;
      if (r != r) key = 0u;
      key = wave_umax(key | (unsigned)(2047 - (rrow + (lane & 7) * 4 + (lane >> 4))));
      __builtin_amdgcn_raw_buffer_store_b64(u32x2{key, tb1 & 0x7fffffffu}, rsh, lane < NREP ? (HAND_N + PK_O + lane * 128 + (rrow >> 5) * 2) * 4 : OOR, 0, 16);
      if (hw == 1) STAMP(dk, 2);
    }
    gu_setup();
    bool got = true;
    unsigned fra = lbase;
    int fro = S_ * D_ * 2;
    auto get_fc = [&]() __attribute__((always_inline)) {
      const u32x2 fw = __builtin_amdgcn_raw_buffer_load_b64(rsh, (HAND_N + PK_O + (bid % NREP) * 128 + lane * 2) * 4, 0, 16);
      got = none(umax(fw[0], fw[1]) == SENT);
      if (got) {
        const unsigned c1 = wave_umax(fw[0]), c2 = wave_umax(fw[0] == c1 ? 0u : fw[0]);
#define BADFC 0
        const unsigned r1 = (2047u - (c1 & 0x7ffu)) ^ BADFC, r2 = 2047u - (c2 & 0x7ffu);
        const unsigned r = (lane & 8) ? r2 : r1;
        fra = lbase + QOFF + (r & 127u) * 128 + (((r ^ lane) & 7u) << 4);
        fro = (r >> 7) == (unsigned)omi && lane < 16 ? ((lane >> 3) * D_ + oni * 64 + (lane & 7) * 8) * 2 : S_ * D_ * 2;
        unsigned dt = (unsigned)((int)((fw[1] - tb1) << 1) >> 1) + 512u;
        dt = dt < 1024u ? dt : 512u;
        *LDS_AT(volatile u32x2, lbase + SPM + 144) = u32x2{r1 | (r2 << 16), (unsigned)__builtin_amdgcn_readlane(dt, r1 >> 5) | (wave_umax(dt) << 16)};
      }
    };
    if (hw == 0) {
      __builtin_amdgcn_s_sleep(FCPRE);
#pragma unroll 1
      for (int i = 0; i < SPIN_MAX; ++i) {
        if (__builtin_amdgcn_readfirstlane(*LDS_AT(const volatile unsigned, lbase + FLG + 28)) - best >= 4u) break;
        __builtin_amdgcn_s_sleep(FCSLP);
      }
      asm volatile("" ::: "memory");
      get_fc();
    } else __builtin_amdgcn_s_sleep(TPRE - 10);
    PIN();
    tile_at(2u);
    const unsigned tb2 = (unsigned)__builtin_amdgcn_s_memrealtime() - 512u;
    LOGDEF;
    LOG();
#pragma unroll 1
    for (int spin = 0; spin < SPIN_MAX && !got; ++spin) { get_fc(); asm volatile("" ::: "memory"); }
    if (hw == 0) __builtin_amdgcn_raw_buffer_store_b128(lds_rd4(fra), rsx, fro, 0, XAUX);
    PIN();
    u32x2 fm = {SENT, 0u};
#pragma unroll 1
    for (int i = 0; i < 4 * SPIN_MAX && fm[0] == SENT; ++i) {
      const u32x2 q = *LDS_AT(const volatile u32x2, lbase + SPM + 144);
      fm = u32x2{(unsigned)__builtin_amdgcn_readfirstlane(q[0]), (unsigned)__builtin_amdgcn_readfirstlane(q[1])};
    }
    const int fr1 = fm[0] & 0x7ffu, fr2 = (fm[0] >> 16) & 0x7ffu;
    const unsigned t1 = tb2 + (fm[1] & 1023u), tmx = tb2 + ((fm[1] >> 16) & 1023u);
    auto until = [&](unsigned te) { while ((int)((unsigned)__builtin_amdgcn_s_memrealtime() - te) < 0) __builtin_amdgcn_s_sleep(1); asm volatile("" ::: "memory"); };
    lds_wr(lbase + SPM + 136, tmx);
    PIN();
    if (hw == 0) STAMP(dk, 3);
    const int xoff = (hw * 256 + (lane & 3) * 64 + (lane >> 2) * 4) * 2;
    const int oc = (omi * 2 + (oni >> 3)) * 32, col = oc + (lane & 7) * 4 + (lane >> 4);
    const int xoo = !p1blk && hw == 3 ? (col & ~1) * 2 : S_ * D_ * 2;
    auto get_row = [&](int r, int sl, u32x2& xr, unsigned& xo) __attribute__((always_inline)) {
      if ((r >> 7) == omi) {
#pragma unroll 1
        for (int i = 0; i < SPIN_MAX; ++i) {
          if (__builtin_amdgcn_readfirstlane(*LDS_AT(const volatile unsigned, lbase + FLG + 24)) >= 16u) break;
          __builtin_amdgcn_s_sleep(1);
        }
        asm volatile("" ::: "memory");
      }
#pragma unroll 1
      for (int spin = 0; spin < SPIN_MAX; ++spin) {
        LOG();
        xr = __builtin_amdgcn_raw_buffer_load_b64(rsx, xoff, sl * (D_ * 2), 16);
        xo = __builtin_amdgcn_raw_buffer_load_b32(rsx, xoo, sl * (D_ * 2), 16);
        LOG();
        if (none(umax(umax(xr[0], xr[1]), xo) == SENT)) break;
        __builtin_amdgcn_s_sleep(XRSLP);
        asm volatile("" ::: "memory");
      }
      PIN();
    };
    auto row_md = [&](const u32x2& xr, int k) __attribute__((always_inline)) {
      auto dot = [&](unsigned x, unsigned y, float z) { return __builtin_amdgcn_fdot2_f32_bf16(__builtin_bit_cast(bf16x2, x), __builtin_bit_cast(bf16x2, y), z, false); };
      const float p1 = dot(xr[1], 0x3f803f80u, dot(xr[0], 0x3f803f80u, 0.f)), p2 = dot(xr[1], xr[1], dot(xr[0], xr[0], 0.f));
      const auto q = __builtin_amdgcn_permlane32_swap(__float_as_uint(p1), __float_as_uint(p2), false, false);
      const float qs = __uint_as_float(q[0]) + __uint_as_float(q[1]);
      const auto q2 = __builtin_amdgcn_permlane16_swap(__float_as_uint(qs), __float_as_uint(qs), false, false);
      float sv = __uint_as_float(q2[0]) + __uint_as_float(q2[1]);
      sv = dpp_add<0x121, 0xf>(dpp_add<0x122, 0xf>(dpp_add<0x124, 0xf>(dpp_add<0x128, 0xf>(sv))));
      lds_wr(lbase + SPM + k * 64 + (lane >> 4) * 16 + hw * 4, f2word(sv));
      u32x4 a, c;
#pragma unroll 1
      for (int i = 0; i < SPIN_MAX; ++i) {
        a = lds_rd4(lbase + SPM + k * 64); c = lds_rd4(lbase + SPM + k * 64 + 32);
        if (__builtin_amdgcn_readfirstlane(umax(umax(a[0], a[1]), umax(a[2], a[3]))) != SENT) break;
      }
      auto sum4 = [&](const u32x4& v) { return (__uint_as_float(v[0]) + __uint_as_float(v[1])) + (__uint_as_float(v[2]) + __uint_as_float(v[3])); };
      const float mean = sum4(a) * (1.0f / 1024.0f), msq = sum4(c) * (1.0f / 1024.0f);
      const float den = rbf(__builtin_amdgcn_sqrtf(rbf(rbf(fmaxf(fmaf(-mean, mean, msq), 0.f)) + 1e-5f)));
      return (unsigned)__builtin_amdgcn_readfirstlane((pack2(mean, mean) & 0xffffu) | (__float_as_uint(den) & 0xffff0000u));
    };
    auto ffn = [&](const u32x2& xr, unsigned md, unsigned n, const __amdgpu_buffer_rsrc_t rs, int yo, auto&& off) __attribute__((always_inline)) {
      gate_up(xr, md);
      if (hw == 0 && n == 2u) STAMP(dk, 8);
      asm volatile("" ::: "memory");
      PIN();
      lds_wr(lbase + MEET + hw * 4, n + off());
      u32x4 mt;
#pragma unroll 1
      for (int i = 0; i < SPIN_MAX; ++i) {
        mt = lds_rd4(lbase + MEET);
        if (__builtin_amdgcn_readfirstlane(min4(mt)) >= n) break;
      }
      asm volatile("" ::: "memory");
      PIN();
      if (hw == 0 && n == 2u) STAMP(dk, 9);
      if (__builtin_amdgcn_readfirstlane(mt[0]) != n + 1u)
        __builtin_amdgcn_raw_buffer_store_b128(down(act_diag(n == 4u ? 512 : 0), w), rs, (yo + fg * D_) * 4 + t * 16, 0, 16);
      dad += n == 2u ? 512 : -512;
      if (hw == 0 && n == 2u) STAMP(dk, 4);
    };
    auto y_sum4 = [&](const __amdgpu_buffer_rsrc_t rs, int yo, int k) __attribute__((always_inline)) {
      const int src = (yo + (hw * 56 + (lane >> 3)) * D_ + oc + (lane & 7) * 4) * 4;
      u32x4 v[7];
#pragma unroll
      for (int i = 0; i < 7; ++i) asm volatile("" : "=v"(v[i]));
      unsigned todo = 0x7fu;
#pragma unroll 1
      for (int spin = 0; spin < SPIN_MAX; ++spin) {
        LOG();
#pragma unroll
        for (int i = 0; i < 7; ++i) if ((todo >> i) & 1u) v[i] = __builtin_amdgcn_raw_buffer_load_b128(rs, src, i * (8 * D_ * 4), 16);
#pragma unroll
        for (int i = 0; i < 7; ++i) if (none(umax(umax(v[i][0], v[i][1]), umax(v[i][2], v[i][3])) == SENT)) todo &= ~(1u << i);
        if (!todo) break;
        __builtin_amdgcn_s_sleep(YSLP);
        asm volatile("" ::: "memory");
      }
      f32x2 z0 = {0.f, 0.f}, z1 = {0.f, 0.f};
#pragma unroll
      for (int i = 0; i < 7; ++i) {
        z0 += f32x2{__uint_as_float(v[i][0]), __uint_as_float(v[i][1])};
        z1 += f32x2{__uint_as_float(v[i][2]), __uint_as_float(v[i][3])};
      }
      const unsigned ya = lbase + SPM + 256 + k * 512 + ((lane & 7) * 4 + (lane >> 4)) * 16;
      lds_wr((lane & 8) ? lbase + DUM + lane * 4 : ya + hw * 4, f2word(fold8(z0, z1)));
      u32x4 q = {0u, 0u, 0u, 0u};
      if (hw == 3) {
#pragma unroll 1
        for (int i = 0; i < 4 * SPIN_MAX; ++i) {
          q = lds_rd4(ya);
          if (none(umax(umax(q[0], q[1]), umax(q[2], q[3])) == SENT)) break;
        }
      }
      return (__uint_as_float(q[0]) + __uint_as_float(q[1])) + (__uint_as_float(q[2]) + __uint_as_float(q[3]));
    };
    const __amdgpu_buffer_rsrc_t rs1 = __builtin_amdgcn_make_buffer_rsrc(hand, 0, HAND_N * 4, 0x00020000);
    u32x2 xr;
    unsigned xo, xo2;
    until(t1 + XDT);
    get_row(fr1, 0, xr, xo);
    if (hw == 0) STAMP(dk, 7);
#define FAILFC 0
    const unsigned md1 = row_md(xr, 0) ^ (FAILFC & 1);
    if (hw == 0) STAMP(dk, 10);
    LOGAT(20);
    u32x2 xr2;
    unsigned md2 = 0u;
    if (p1blk) {
      xr2 = __builtin_amdgcn_raw_buffer_load_b64(rsx, (fr2 >> 7) == omi ? S_ * D_ * 2 : xoff, D_ * 2, 16);
      PIN();
      ffn(xr, md1, 2u, rsh, GRAN_N, [&]() { return 0u; });
      __builtin_amdgcn_s_setprio(R2PRIO);
      LOG();
      if ((fr2 >> 7) == omi || !none(umax(xr2[0], xr2[1]) == SENT)) get_row(fr2, 1, xr2, xo2);
      md2 = row_md(xr2, 1) ^ (FAILFC & 2);
      ffn(xr2, md2, 4u, rs1, GRAN_N, [&]() {
        const u32x2 v = *LDS_AT(const volatile u32x2, lbase + BEST);
        return v[0] != 0u && (2047 - (int)(v[0] & 0x7ffu) != fr2 || v[1] != md2) ? 1u : 0u; });
    }
    if (hw == 0) STAMP(dk, 5);
    LOG();
    float y = 0.f;
    auto put_row = [&](int r, unsigned md, unsigned rb) __attribute__((always_inline)) {
      const float rden = __uint_as_float(md & 0xffff0000u), rs = rbf(frcp(1.0f + __expf(-__uint_as_float(rb << 16))));
      const float xa = __uint_as_float((col & 1) ? xo & 0xffff0000u : xo << 16);
      float ha = xa, hb = xa;
      ln_apply2(ha, hb, __uint_as_float(md << 16), rden, frcp(rden));
      const float o = xa + rbf(ha + rbf(rbf(y) * rs));
      __builtin_amdgcn_raw_buffer_store_b16((unsigned short)(pack2(o, o) & 0xffffu), __builtin_amdgcn_make_buffer_rsrc(out, 0, S_ * D_ * 2, 0x00020000),
                                            (lane & 8) ? S_ * D_ * 2 : (r * D_ + col) * 2, 0, OAUX);
    };
    if (!p1blk) {
      until(tmx + XD2);
      get_row(fr2, 1, xr2, xo2);
      md2 = row_md(xr2, 1) ^ (FAILFC & 2);
      until(t1 + XDT + YD1); until(tmx + YD2);
      if (hw == 3) STAMP(dk, 13);
      LOGAT(8);
      y = y_sum4(rsh, GRAN_N, 0);
      if (hw == 3) STAMP(dk, 14);
      LOGAT(24);
    }
    unsigned bmd = 0u;
    best = 0u;
#pragma unroll 1
    for (int spin = 0; spin < 4 * SPIN_MAX; ++spin) {
      const u32x2 v = *LDS_AT(const volatile u32x2, lbase + BEST);
      best = __builtin_amdgcn_readfirstlane(v[0]); bmd = __builtin_amdgcn_readfirstlane(v[1]);
      if (best) break;
      __builtin_amdgcn_s_sleep(1);
    }
    asm volatile("" ::: "memory");
    const int idx = 2047 - (int)(best & 0x7ffu);
    const unsigned rk = best >> 16, rb = (rk & 0x8000u) ? (rk & 0x7fffu) : (~rk & 0xffffu);
    if (hw == 0) STAMP(dk, 1);
    LOGAT(25);
    LOGOUT(hw);
    if (__builtin_expect(idx != fr1 || bmd != md1, 0)) {
      if (idx == fr2 && bmd == md2) {
        if (p1blk) return;
        y = y_sum4(rs1, GRAN_N, 1);
        xo = xo2;
      } else {
        get_row(idx, idx == fr1 ? 0 : (idx == fr2 ? 1 : 2), xr, xo);
        if (p1blk) ffn(xr, bmd, 6u, rsh, HAND_N + Y3_O, [&]() { return 0u; });
        else y = y_sum4(rsh, HAND_N + Y3_O, 2);
        if (p1blk || hw != 3) return;
#pragma unroll 1
        for (int spin = 0; spin < SPIN_MAX && idx != fr1 && idx != fr2; ++spin) {
          if (__builtin_amdgcn_readfirstlane(ld_ag(gran + GR_F + (oc >> 6) * 32)) == tag) break;
          asm volatile("" ::: "memory");
        }
      }
    }
    if (p1blk || hw != 3) return;
    put_row(idx, bmd, rb);
    }
    return;
  }
  if (wid >= 4) {
    const int lane = fresh_lane();
    const int lw = wid - 4;
    const long long lay_ = ap->lay;
    const u16* A = ap->a; const u16* W = ap->W; const u16* wc = ap->wc;
#define LPRIO 3
#define LP0 3
    __builtin_amdgcn_s_setprio(LP0);
    unsigned va[4], vw[6];
    const unsigned ul = lane;
    va[0] = (unsigned)(m0 + lw * 32) * (K * 2) + (ul >> 3) * (K * 2) + (((ul & 7u) ^ (ul >> 4)) << 4);
    va[1] = va[0] + (8 * K * 2 - 1024);
    va[2] = (va[0] ^ 64u) + 2 * (8 * K * 2 - 1024);
    va[3] = va[2] + (8 * K * 2 - 1024);
#pragma unroll
    for (int i = 0; i < 3; ++i) {
      const unsigned n = ul + 64u * i, k = __umul24(n, 171u) >> 12, cp = n - __umul24(k, 24u);
      vw[i] = __umul24(k, ldw * 2) + (cp >> 3) * 2048u + (((cp & 7u) ^ ((k & 2u) << 1)) << 4) + ((unsigned)(lw * 16 * ldw + n0) * 2u - i * 1024u);
      vw[i + 3] = (vw[i] ^ 16u) + 8 * ldw * 2;
    }
    PIN();
    asm volatile("" : "+v"(va[0]), "+v"(va[1]), "+v"(va[2]), "+v"(va[3]), "+v"(vw[0]), "+v"(vw[1]), "+v"(vw[2]), "+v"(vw[3]), "+v"(vw[4]), "+v"(vw[5]));
    const bool LAY2 = lay_ != 0;
    const int krot = tmi & 8;
    if (LAY2 && slow_cols(W, tni)) W = wc;
    auto stage_a = [&](int kt, int slot) {
      const char* pa = reinterpret_cast<const char*>(A) + (size_t)(kt ^ krot) * 128;
      asm volatile("s_mov_b32 m0, %5\n\ts_nop 0\n\tglobal_load_lds_dwordx4 %0, %4 sc1\n\tglobal_load_lds_dwordx4 %1, %4 offset:1024 sc1\n\t"
                   "global_load_lds_dwordx4 %2, %4 offset:2048 sc1\n\tglobal_load_lds_dwordx4 %3, %4 offset:3072 sc1"
                   :: "v"(va[0]), "v"(va[1]), "v"(va[2]), "v"(va[3]), "s"(pa), "s"(lbase + slot * STB + lw * 4096) : "m0", "memory");
    };
    auto stage_w = [&](int kt, int slot) {
      const char* pw = reinterpret_cast<const char*>(W) + (size_t)(kt ^ krot) * 128 * ldw;
      asm volatile("s_mov_b32 m0, %7\n\ts_nop 0\n\tglobal_load_lds_dwordx4 %0, %6 sc1\n\tglobal_load_lds_dwordx4 %1, %6 offset:1024 sc1\n\t"
                   "global_load_lds_dwordx4 %2, %6 offset:2048 sc1\n\ts_addk_i32 m0, 0xc00\n\ts_nop 0\n\tglobal_load_lds_dwordx4 %3, %6 sc1\n\t"
                   "global_load_lds_dwordx4 %4, %6 offset:1024 sc1\n\tglobal_load_lds_dwordx4 %5, %6 offset:2048 sc1"
                   :: "v"(vw[0]), "v"(vw[1]), "v"(vw[2]), "v"(vw[3]), "v"(vw[4]), "v"(vw[5]), "s"(pw), "s"(lbase + slot * STB + ABYTES + lw * 6144) : "m0", "scc", "memory");
    };
    if (LAY2) {
      __builtin_amdgcn_s_setprio(0);
      __builtin_amdgcn_s_sleep(TPRE - 10);
      tile_at(3u);
      __builtin_amdgcn_s_setprio(LPRIO);
#pragma unroll
      for (int st = 0; st < NST; ++st) stage_w(st, st);
      PIN();
      __builtin_amdgcn_s_barrier();
      PIN();
#pragma unroll
      for (int st = 0; st < NST; ++st) stage_a(st, st);
      asm volatile("s_waitcnt vmcnt(8)" ::: "memory");
    } else {
      __builtin_amdgcn_s_setprio(LPRIO);
      stage_a(0, 0); stage_w(0, 0);
      if (PRO > 1) { stage_a(1, 1); stage_w(1, 1); }
      if (PRO > 2) { stage_a(2, 2); stage_w(2, 2); }
      asm volatile("s_waitcnt vmcnt(%0)" :: "n"(10 * PRO - 10) : "memory");
    }
    int slot = 0;
    __builtin_amdgcn_s_barrier();
    if (!LAY2) {
      if (PRO < 2) { stage_a(1, 1); stage_w(1, 1); }
      if (PRO < 3) { stage_a(2, 2); stage_w(2, 2); }
    }
    for (int kt = 0; kt + 1 < NKT; ++kt) {
      if (LAY2 && kt == 0) asm volatile("s_waitcnt vmcnt(4)" ::: "memory");
      else if (kt + 2 >= NKT) asm volatile("s_waitcnt vmcnt(0)" ::: "memory");
      else asm volatile("s_waitcnt vmcnt(10)" ::: "memory");
      __builtin_amdgcn_s_barrier();
      if (kt + NST < NKT) { stage_a(kt + NST, slot); stage_w(kt + NST, slot); }
      slot = slot == NST - 1 ? 0 : slot + 1;
    }
    __builtin_amdgcn_s_setprio(0);
    __builtin_amdgcn_s_barrier();
  } else {
  LAYER_HERE;
  const int dk = LAY2 ? 5 : 1;
  const int lane = fresh_lane(), wm = wid & 1, wn = wid >> 1;
  if (wid == 0) STAMP(dk, 0);
  f32x4 acc[4][6];
#pragma unroll
  for (int i = 0; i < 4; ++i)
#pragma unroll
    for (int j = 0; j < 6; ++j) acc[i][j] = f32x4{0.f, 0.f, 0.f, 0.f};
  const int q16 = lane & 15, g4 = lane >> 4;
  const int e0 = 16 * (q16 >> 3) + (q16 & 7);
  s16x8 af[2][4];
  unsigned aad[2], wad[2];
  {
    const int row = wm * 64 + e0, swz = ((row >> 1) & 3) | (((row >> 4) & 1) << 2);
#pragma unroll
    for (int h = 0; h < 2; ++h) aad[h] = lbase + row * 128 + (((4 * h + g4) ^ swz) << 4);
    const int tq = (lane & 15) >> 2, tp = lane & 3, k = 8 * g4 + tq, ch = wn * 4 + 2 * (tp >> 1);
    wad[0] = lbase + ABYTES + k * 384 + ((ch ^ (4 * ((k >> 1) & 1)) ^ (g4 & 1)) << 4) + 8 * (tp & 1);
    wad[1] = wad[0] ^ 16;
  }
  auto lda = [&](unsigned a, int i) { return *LDS_AT(const s16x8, a + (i >> 1) * 4096 + (i & 1) * 1024); };
  auto ldw_ = [&](int h, int j) {
    const unsigned w = wad[j & 1] + h * 12288 + (j >> 1) * 128;
    s16x4 lo = __builtin_amdgcn_ds_read_tr16_b64_v4i16(LDS_AT(s16x4, w));
    s16x4 h4 = __builtin_amdgcn_ds_read_tr16_b64_v4i16(LDS_AT(s16x4, w + 1536));
    return (s16x8)__builtin_shufflevector(lo, h4, 0, 1, 2, 3, 4, 5, 6, 7);
  };
  ARGS_HERE;
  unsigned lto = lbase + wid * 4096;
  char* kvb = reinterpret_cast<char*>(KV) + ((tni * 2 + wn) * 64 + tmi * 4 + wm * 2) * 2048 - lto;
  {
    const unsigned ato = lbase + ALPHA + wm * 256, dum = lbase + DUM + lane * 4;
    *LDS_AT(u32x4, lto + QOFF + lane * 16) = u32x4{lane >= 16 ? lbase + CRED : lbase + TABA + (wm * 64 + e0) * 16, lbase + TABW + (wn * 32 + e0) * 16,
                                                   ato + e0 * 4, lto + (lane >> 5) * 1024 + ((lane & 28) * 2 + (lane & 3)) * 16};
    *LDS_AT(u32x2, lto + QOFF + 2048 + lane * 16) = u32x2{aad[0], aad[1]};
    *LDS_AT(u32x4, lto + QOFF + 1024 + lane * 16) = u32x4{ato + g4 * 16 + (lane >> 5) * 32, lto + (lane >> 5) * 1024 + ((lane & 24) * 2 + (lane & 7)) * 16,
                                                          lane == 0 ? lbase + FLG + 32 : dum, lane == 0 ? lbase + FLG + 16 : dum};
  }
  asm volatile("" : "+s"(lto), "+s"(kvb) :: "memory");
#pragma unroll
  for (int i = 0; i < 4; ++i)
#pragma unroll
    for (int j = 0; j < 6; ++j) asm volatile("" : "+v"(acc[i][j]));
  asm volatile("" : "+v"(aad[0]), "+v"(aad[1]), "+v"(wad[0]), "+v"(wad[1]));
  if (LAY2) __builtin_amdgcn_s_barrier();
  __builtin_amdgcn_s_barrier();
  PIN();
  if (wid == 0) STAMP(dk, 1);
  s16x8 wq[4][2];
  wq[0][0] = ldw_(0, 0); af[0][0] = lda(aad[0], 0); wq[0][1] = ldw_(1, 0); af[1][0] = lda(aad[1], 0); wq[1][0] = ldw_(0, 1); wq[1][1] = ldw_(1, 1);
  af[0][1] = lda(aad[0], 1); af[1][1] = lda(aad[1], 1); af[0][2] = lda(aad[0], 2); af[1][2] = lda(aad[1], 2);
  PIN();
  s16x4 wlo;
  auto rw = [&](s16x8& d, int h, int j, int hi, int off = 0) __attribute__((always_inline)) {
    const s16x4 v = __builtin_amdgcn_ds_read_tr16_b64_v4i16(LDS_AT(s16x4, wad[j & 1] + h * 12288 + (j >> 1) * 128 + hi * 1536 + off));
    if (hi) d = (s16x8)__builtin_shufflevector(wlo, v, 0, 1, 2, 3, 4, 5, 6, 7);
    else wlo = v;
  };
  auto at = [&](s16x8& x, int r) __attribute__((always_inline)) {
    if (r == 0) asm volatile("" :: "{v[104:107]}"(x));
    if (r == 1) asm volatile("" :: "{v[108:111]}"(x));
    if (r == 2) asm volatile("" :: "{v[112:115]}"(x));
    if (r == 3) asm volatile("" :: "{v[116:119]}"(x));
    if (r == 4) asm volatile("" :: "{v[120:123]}"(x));
    if (r == 5) asm volatile("" :: "{v[124:127]}"(x));
    if (r == 6) asm volatile("" :: "{v[128:131]}"(x));
    if (r == 7) asm volatile("" :: "{v[132:135]}"(x));
    if (r == 8) asm volatile("" :: "{v[136:139]}"(x));
    if (r == 9) asm volatile("" :: "{v[140:143]}"(x));
    if (r == 10) asm volatile("" :: "{v[144:147]}"(x));
    if (r == 11) asm volatile("" :: "{v[148:151]}"(x));
    if (r == 12) asm volatile("" :: "{v[152:155]}"(x));
    if (r == 13) asm volatile("" :: "{v[156:159]}"(x));
    if (r == 14) asm volatile("" :: "{v[160:163]}"(x));
    if (r == 15) asm volatile("" :: "{v[164:167]}"(x));
  };
  auto mm = [&](int i, int j, int h, int sl, int hw_) __attribute__((always_inline)) {
    s16x8 &a = af[h][i], &w = wq[sl][hw_];
    at(a, h * 4 + i); at(w, 8 + sl * 2 + hw_);
    if (j >= 4) asm volatile("v_mfma_f32_16x16x32_bf16 %0, %1, %2, %0" : "+v"(acc[i][j]) : "v"(a), "v"(w));
    else asm volatile("v_mfma_f32_16x16x32_bf16 %0, %1, %2, %0" : "+v"(acc[i][j]) : "v"(w), "v"(a));
    PIN();
  };
  auto next = [&](int g, int J0, int s0, int s1, int off) __attribute__((always_inline)) {
    if (g == 2 || g == 3) rw(wq[s0][0], 0, J0, g & 1, off);
    if (g == 4 || g == 5) af[g & 1][0] = lda(aad[g & 1] + off, 0);
    if (g == 6 || g == 7) rw(wq[s0][1], 1, J0, g & 1, off);
    if (g >= 8 && g < 12) rw(wq[s1][(g - 8) >> 1], (g - 8) >> 1, J0 + 1, g & 1, off);
    if (g >= 12) af[g & 1][(g - 10) >> 1] = lda(aad[g & 1] + off, (g - 10) >> 1);
  };
  auto third = [&](int p, int J0, int delta) __attribute__((always_inline)) {
    auto mv = [&](unsigned& x) __attribute__((always_inline)) { asm volatile("v_add_u32 %0, %1, %0" : "+v"(x) : "s"(delta)); };
#pragma unroll
    for (int g = 0; g < 16; ++g) {
      const int h = g & 1, i = p == 1 ? (g >> 1) & 3 : g >> 2, t = p == 1 ? g >> 3 : (g >> 1) & 1, j = 2 * p + t, sl = p == 1 ? 2 - 2 * t : p + t;
      mm(i, j, h, sl, h);
      if (p == 0) {
        if (g < 2) af[g][3] = lda(aad[g], 3);
        else if (g < 6) rw(wq[2][(g - 2) >> 1], (g - 2) >> 1, 2, g & 1);
        else if (g < 10) rw(wq[3][(g - 6) >> 1], (g - 6) >> 1, 5, g & 1);
        else if (g < 12) mv(aad[g - 10]);
      }
      if (p == 1) {
        if (g < 4) rw(wq[0][g >> 1], g >> 1, 3, g & 1);
        if (g == 4) mv(wad[1]);
        if (g >= 8 && g < 12) rw(wq[2][(g - 8) >> 1], (g - 8) >> 1, 4, g & 1);
        if (g == 12) mv(wad[0]);
      }
      if (p == 2) {
        if (g == BARG) {
          asm volatile("" ::: "memory");
          PIN();
          __builtin_amdgcn_s_barrier();
          asm volatile("" ::: "memory");
        }
        next(g, J0, 0, 1, 0);
      }
      PIN();
      {
        if ((p == 2 && g == 15) || (p == 0 && g == 7)) __builtin_amdgcn_s_waitcnt(0xc67f);
        if (p == 0 && g == 2) __builtin_amdgcn_s_waitcnt(0xc57f);
        if ((p == 0 && g == 14) || (p == 1 && (g & 7) == 7)) __builtin_amdgcn_s_waitcnt(0xc07f);
        PIN();
      }
    }
  };
  third(0, 0, STB); third(1, 0, STB);
  {
    static_assert((NKT - NST - 1) % NST == 0, "");
#pragma unroll 1
    for (int kt = 0; kt < NKT - NST - 1; kt += NST) {
#pragma unroll
      for (int u = 0; u < NST; ++u) {
        const int delta = u == 1 ? -(NST - 1) * STB : STB;
        third(2, 0, 0); third(0, 0, delta); third(1, 0, delta);
      }
    }
  }
  static_assert(NKT % NST == 1, "slots of the last stages");
#define TT(i)
  auto bar = [&]() { PIN(); __builtin_amdgcn_s_barrier(); asm volatile("" ::: "memory"); PIN(); };
  TT(0);
  third(2, 2, 0);
  const __amdgpu_buffer_rsrc_t rsk = __builtin_amdgcn_make_buffer_rsrc(kvb, 0, 0x7fffffff, 0x00020000), rsv = __builtin_amdgcn_make_buffer_rsrc(kvb + VOFF, 0, 0x7fffffff, 0x00020000);
  unsigned pk;
  u32x2 t0, t1, t2, t3;
  auto park = [&](int i) { return *LDS_AT(const volatile u32x2, pk + (i >> 1) * 1024 + (i & 1) * 8); };
  float al[4];
  f32x4 av[4];
  u32x4 kvp[12];
  auto cacc = [&](int p, int x, int t) -> f32x4& { return p < 2 ? acc[x][2 * p + t] : acc[(x >> 1) * 2 + t][4 + (x & 1)]; };
  auto signal = [&](unsigned v) { __hip_atomic_fetch_add(LDS_AT(unsigned, t3[0]), v, __ATOMIC_RELAXED, __HIP_MEMORY_SCOPE_WORKGROUP); PIN(); };
  auto outop = [&](int p, int k) {
    if (k >= 0 && k < 16) {
      const int x = k >> 2, t = (k >> 1) & 1;
      f32x4& c = cacc(p, x, t);
#pragma unroll
      for (int o = 2 * (k & 1); o < 2 * (k & 1) + 2; ++o) {
        float r;
        asm volatile("v_mul_f32 %0, %1, %2" : "=v"(r) : "v"(c[o]), "v"(p < 2 ? al[x] : av[(x >> 1) * 2 + t][o]));
        c[o] = r;
      }
      PIN();
    }
    if (k >= 1 && k <= 16) {
      const int x = (k - 1) >> 2, t = ((k - 1) >> 1) & 1, o = (k - 1) & 1;
      kvp[p * 4 + x][2 * t + o] = pack2(cacc(p, x, t)[2 * o], cacc(p, x, t)[2 * o + 1]);
      PIN();
      if (t && o) {
        if (p) __builtin_amdgcn_raw_buffer_store_b128(kvp[p * 4 + x], p == 1 ? rsk : rsv, (p == 1 ? t1[1] : t2[1]) + (x >> 1) * 2048 + (x & 1) * (p == 1 ? 64 : 128), 0, 16);
        else *LDS_AT(u32x4, t2[1] + (x >> 1) * 2048 + (x & 1) * 128) = kvp[x];
        PIN();
      }
    }
  };
  static_for<0, 168>([&](auto nc) __attribute__((always_inline)) {
    constexpr int n = decltype(nc)::value, sec = n < 80 ? n >> 4 : (n < 104 ? 5 : 6 + ((n - 104) >> 4)), g = n < 104 ? n - 16 * sec : (n - 104) & 15;
    auto taba = [&](int i) { af[0][i] = *LDS_AT(const s16x8, t0[0] + (i >> 1) * 512 + (i & 1) * 128); };
    auto tabw = [&](int j) { wq[(2 + (j >> 1)) & 3][j & 1] = *LDS_AT(const s16x8, t0[1] + ((j >> 1) * 64 + (j & 1) * 8) * 16); };
    auto rwn = [&](int e, int sl, int j, int off) { if (e >= 0 && e < 4) rw(wq[sl][e >> 1], e >> 1, j, e & 1, off); };
    if (g == 0) TT(1 + sec);
    if (sec == 5) {
      const int i = (g / 12) * 2 + (g & 1), j = (g % 12) >> 1;
      mm(i, j, 0, (2 + (j >> 1)) & 3, j & 1);
    } else {
      constexpr int part[10] = {1, 2, 1, 2, 0, 0, 1, 2, 0, 0}, s0[10] = {0, 2, 0, 2, 0, 0, 1, 3, 1, 3};
      const int h = g & 1, i = sec == 7 ? (g >> 3) * 2 + ((g >> 1) & 1) : g >> 2, t = sec == 7 ? (g >> 2) & 1 : (g >> 1) & 1;
      mm(i, 2 * part[sec] + t, h, (s0[sec] + t) & 3, h);
    }
    if (sec == 0 || sec == 2) {
      if (g < 2) af[g][3] = lda(aad[g] + sec / 2 * STB, 3);
      rwn(g - 2, 2, 4, sec / 2 * STB); rwn(g - 6, 3, 5, sec / 2 * STB);
    }
    if (sec == 1) {
      if (g == 1) bar();
      next(g, 2, 0, 1, STB);
    }
    if ((sec == 3 && g == 12) || (sec == 6 && g == 13)) { pk = lto + QOFF + fresh_lane() * 16; asm volatile("" : "+v"(pk)); }
    if (sec == 3) {
      rwn(g, 0, 0, STB); rwn(g - 4, 1, 1, STB);
      if (g >= 8 && g < 12) asm volatile("v_subrev_u32 %0, %1, %0" : "+v"(g < 10 ? aad[g & 1] : wad[g & 1]) : "s"(STB));
      if (g == 13) t1 = park(1);
      if (g == 14) t0 = park(0);
    }
    if (sec == 4) {
      if (g == B15) bar();
      if (g == 4) taba(0);
      if (g == 5 || g == 6) tabw(g - 5);
      if (g == 7) taba(1);
      if (g == 8 || g == 9) tabw(g - 6);
      if (g == 10 || g == 11) af[1][g - 10] = lda(aad[1], g - 10);
      if (g == 12) taba(2);
      if (g == 15) taba(3);
    }
    if (sec == 5) {
      if (g < 2) tabw(4 + g);
      rwn(g - 2, 1, 2, 0);
      if (g == 12 || g == 13) af[0][g - 12] = lda(aad[0], g - 12);
      if (g == 22) { al[0] = *LDS_AT(const float, t1[0]); al[1] = *LDS_AT(const float, t1[0] + 32); }
      rwn(g - 16, 2, 3, 0);
      if (g == 20 || g == 21) af[1][g - 18] = lda(aad[1], g - 18);
    }
    if (sec == 6) {
      if (g < 2) af[0][2 + g] = lda(aad[0], 2 + g);
      rwn(g - 2, 3, 4, 0);
      if (g == 9) { al[2] = *LDS_AT(const float, t1[0] + 128); al[3] = *LDS_AT(const float, t1[0] + 160); }
      if (g == 14) t2 = park(2);
      if ((g & 3) >= 2) rwn((g - 10) / 4 * 2 + (g & 1) - (g < 10) * 8, 0, 5, 0);
    }
    if (sec == 7) {
      if (g == 2 || g == 3 || g == 10 || g == 11) av[(g >> 3) * 2 + (g & 1)] = *LDS_AT(const f32x4, t2[0] + ((g >> 3) * 32 + (g & 1) * 8) * 4);
      rwn(g < 2 ? g : (g < 4 ? -1 : g - 2), 1, 0, 0); rwn(g - 6, 2, 1, 0);
      if (g == 12) t3 = park(3);
      if (g == 13) { const u32x2 a = park(4); aad[0] = a[0]; aad[1] = a[1]; }
      if (g == 15) {
        asm volatile("" ::: "memory");
        __builtin_amdgcn_s_waitcnt(0xc07f);
        bar();
      }
    }
    if (sec == 8) next(g, 0, 3, 0, STB);
    if (sec == 9) {
      if (g < 2) af[g][3] = lda(aad[g] + STB, 3);
      if (g == 2) signal(0x10000u);
    }
    PIN();
    outop(1, n - 104 - KO);
    outop(2, n - 120 - KO);
    {
      constexpr int wg[8] = WGAPS;
#pragma unroll
      for (int e = 0; e < 8; ++e)
        if (n - 136 == wg[e]) {
          *LDS_AT(u32x4, (e < 4 ? t1[1] + (e & 1) * 64 : t2[1] + 2048 + (e & 1) * 128) + ((e >> 1) & 1) * ASB) = kvp[4 + e];
          PIN();
        }
    }
    if (n == 152 + 3 || n == 152 + 4) {
      al[2 * (n & 1)] *= QSCALE; al[2 * (n & 1) + 1] *= QSCALE;
      asm volatile("" : "+v"(al[2 * (n & 1)]), "+v"(al[2 * (n & 1) + 1]));
      if (n & 1) asm volatile("v_add_u32 %0, %1, %0" : "+v"(t2[1]) : "s"(QOFF));
      PIN();
    }
    outop(0, n - 152 - KO);
  });
  TT(13);
  if (wid == 0) STAMP(dk, 18);
#pragma unroll
  for (int k = 16 - KO; k <= 16; ++k) outop(0, k);
  __hip_atomic_fetch_add(LDS_AT(unsigned, t3[1]), 1u, __ATOMIC_RELAXED, __HIP_MEMORY_SCOPE_WORKGROUP);
  TT(14);
  asm volatile("s_waitcnt vmcnt(0)" ::: "memory");
  signal(1u);
  TT(15);
  if (wid == 0) STAMP(dk, 19);
  }
  PIN();
  {
  const int lane = fresh_lane(), tid = wid * 64 + lane;
  LAYER_HERE;
  const int dk = LAY2 ? 6 : 2;
  const int g = wid >> 2, hh = (wid >> 1) & 1, qg = wid & 1;
  const int head = tni * 2 + hh, qt = tmi * 4 + qg * 2;
  unsigned qaa = lbase + QOFF + (hh * 2 + qg) * 4096 + lane * 16 + (g ? 0 : 2048), qab = qaa ^ 2048;
  const unsigned wbase = lbase + (hh * 2 + g) * 4096 + lane * 16;
  auto sptr = [&](int sl) { unsigned a = wbase + sl * ASB; asm volatile("" : "+v"(a)); return LDS_AT(const s16x8, a); };
  auto rd = [&](__attribute__((address_space(3))) const s16x8* sp, int off) { return sp[off >> 4]; };
  auto sl0 = sptr(0);
  const unsigned arr_a = lane == 0 ? lbase + FLG + 16 : lbase + DUM + lane * 4;
  auto arrive = [&]() { __hip_atomic_fetch_add(LDS_AT(unsigned, arr_a), 1u, __ATOMIC_RELAXED, __HIP_MEMORY_SCOPE_WORKGROUP); };
  unsigned arr_w = lane == 0 ? lbase + FLG + 8 + g * 4 : lbase + DUM + lane * 4;
  unsigned arr_i = 1u << (8 * (wid & 3));
  auto arrive_w = [&]() { __hip_atomic_fetch_add(LDS_AT(unsigned, arr_w), arr_i, __ATOMIC_RELAXED, __HIP_MEMORY_SCOPE_WORKGROUP); };
  s16x8 sel;
#pragma unroll
  for (int e = 0; e < 8; ++e) sel[e] = ((lane & 7) == (e & 4)) ? (short)0x3F80 : (short)0;
  int sidx = 0xDDDDDDDD;
  f32x16 oa, ob, nn, sa, sb;
  f32x4 la = {0.f, 0.f, 0.f, 0.f}, lb = {0.f, 0.f, 0.f, 0.f};
#pragma unroll
  for (int e = 0; e < 16; ++e) { oa[e] = 0.f; ob[e] = 0.f; sa[e] = 0.f; sb[e] = 0.f; }
  asm volatile("" : "+v"(sidx), "+v"(sel), "+v"(oa), "+v"(ob), "+v"(la), "+v"(lb), "+v"(qaa), "+v"(qab), "+v"(arr_w), "+v"(arr_i));
  PIN();
  s16x8 qa0, qa1, qb0, qb1, k00, k01, v00, v01, v10, v11, k10, k11;
  u32x4 fw;
  auto first = [&]() {
    fw = lds_rd4(lbase + FLG + 16);
    asm volatile("" ::: "memory");
    k00 = rd(sl0, 0); qa0 = *LDS_AT(const s16x8, qaa); qb0 = *LDS_AT(const s16x8, qab);
    k01 = rd(sl0, 1024); qa1 = *LDS_AT(const s16x8, qaa + 1024); qb1 = *LDS_AT(const s16x8, qab + 1024);
    v00 = rd(sl0, 2048); v01 = rd(sl0, 3072);
    PIN();
  };
  first();
  if (__builtin_amdgcn_readfirstlane(fw[0]) < 4u) {
#pragma unroll 1
    for (int i = 0; i < SPIN_MAX; ++i) {
      if (__builtin_amdgcn_readfirstlane(*LDS_AT(const volatile unsigned, lbase + FLG + 16)) >= 4u) break;
      __builtin_amdgcn_s_sleep(ASLP);
    }
    asm volatile("" ::: "memory");
    first();
  }
  const unsigned tag = __builtin_amdgcn_readfirstlane(fw[1]);
  const int idx = LAY2 ? (2047 - (int)(__builtin_amdgcn_readfirstlane(fw[3]) & 0xffffu)) & 2047 : -64;
  float ma;
  const float zoff = __uint_as_float(__builtin_amdgcn_readfirstlane(LAY2 && g == ((idx >> 6) & 1) ? 0u : 0xff800000u));
  {
    sa = MFMA32(k00, qa0, sa); sb = MFMA32(k00, qb0, sb);
    sa = MFMA32(k01, qa1, sa); sb = MFMA32(k01, qb1, sb);
    float mb = sb[0];
    ma = sa[0];
#pragma unroll
    for (int e = 1; e < 16; ++e) { ma = fmaxf(ma, sa[e]); mb = fmaxf(mb, sb[e]); }
    ma = fmaxf(ma, mb);
    const auto sw = __builtin_amdgcn_permlane32_swap(__float_as_uint(ma), __float_as_uint(ma), false, false);
    ma = fmaxf(__uint_as_float(sw[0]), __uint_as_float(sw[1]));
    f32x2 n2 = {-ma, -ma};
    asm volatile("" : "+v"(n2));
#pragma unroll
    for (int e = 0; e < 16; ++e) nn[e] = n2[e & 1];
    sa = MFMA32(k00, qa0, nn); sa = MFMA32(k01, qa1, sa);
    la[0] = lb[0] = -__uint_as_float(__float_as_uint(__builtin_amdgcn_exp2f(zoff - ma)) & 0xffff0000u);
  }
#define SB() __builtin_amdgcn_sched_barrier(0)
#define EX(i) sc[i] = __builtin_amdgcn_exp2f(sc[i])
  auto unit = [&](f32x16& sc, f32x16& sn, const s16x8& kn0, const s16x8& kn1, const s16x8& qn0, const s16x8& qn1,
                  f32x16& op, f32x4& lp, const s16x8& vp,
                  f32x16& oc, f32x4& lc, const s16x8& vc, auto&& mid) {
    lp = SMF16(sel, PHI(sn), lp); SB();
    op = SMF32(vp, PHI(sn), op); SB();
    EX(0); EX(1); EX(2); EX(3); SB();
    sn = MFMA32(kn0, qn0, nn); SB();
    EX(4); EX(5); EX(6); EX(7); SB();
    sn = MFMA32(kn1, qn1, sn); SB();
    mid();
    EX(8); EX(9); EX(10); EX(11); SB();
    lc = SMF16(sel, PLO(sc), lc); SB();
    oc = SMF32(vc, PLO(sc), oc); SB();
    EX(12); EX(13); EX(14); EX(15); SB();
    asm volatile("" : "+v"(sc));
  };
  sl0 = sptr(1);
  unsigned f0 = 0u;
  auto check = [&](unsigned need) {
    if (__builtin_expect(__builtin_amdgcn_readfirstlane(f0) < need, 0)) {
#pragma unroll 1
      for (int i = 0; i < SPIN_MAX; ++i) {
        if (__builtin_amdgcn_readfirstlane(*LDS_AT(const volatile unsigned, lbase + FLG)) >= need) break;
        __builtin_amdgcn_s_sleep(1);
      }
    }
    asm volatile("" ::: "memory");
  };
#define PAT 0x22221110
#define SAW8(i) do { if (((PAT >> (28 - 4 * (i))) & 3) != ((PAT >> ((32 - 4 * (i)) & 31)) & 3)) { __builtin_amdgcn_s_setprio((PAT >> (28 - 4 * (i))) & 3); SB(); } } while (0)
#define SAW(i) do { if (odd) SAW8(4 + (i)); else SAW8(i); } while (0)
  auto three = [&](int st, bool odd) {
    SAW(0);
    unit(sa, sb, k00, k01, qb0, qb1, ob, lb, v11, oa, la, v00, [&]() { k10 = rd(sl0, 0); k11 = rd(sl0, 1024); SB(); });
    SAW(1);
    unit(sb, sa, k10, k11, qa0, qa1, oa, la, v01, ob, lb, v00, [&]() { v10 = rd(sl0, 2048); v11 = rd(sl0, 3072); SB(); });
    sl0 = sptr((2 * st + 2) & 7);
    SB();
    SAW(2);
    unit(sa, sb, k10, k11, qb0, qb1, ob, lb, v01, oa, la, v10, [&]() { k00 = rd(sl0, 0); k01 = rd(sl0, 1024); SB(); });
  };
  auto fourth = [&](int st, bool odd) {
    SAW(3);
    unit(sb, sa, k00, k01, qa0, qa1, oa, la, v11, ob, lb, v10, [&]() {
      v00 = rd(sl0, 2048); v01 = rd(sl0, 3072);
      f0 = *LDS_AT(const volatile unsigned, lbase + FLG);
      SB();
      arrive_w();
      SB(); });
    check(st >= 14 ? 32u : 2u * st + 5u);
    sl0 = sptr((2 * st + 3) & 7);
    SB();
  };
  SB();
  SAW8(0);
  {
    f32x16& sc = sa;
    sb = MFMA32(k00, qb0, nn); SB();
    k10 = rd(sl0, 0); k11 = rd(sl0, 1024); SB();
    sb = MFMA32(k01, qb1, sb); SB();
    EX(0); EX(1); EX(2); EX(3); EX(4); EX(5); EX(6); EX(7); EX(8); SB();
    oa = SMF32(v00, PLO(sa), oa); SB();
    EX(9); EX(10); EX(11); SB();
    la = SMF16(sel, PLO(sa), la); SB();
    EX(12); EX(13); EX(14); EX(15); SB();
    asm volatile("" : "+v"(sa));
  }
  SAW8(1);
  unit(sb, sa, k10, k11, qa0, qa1, oa, la, v01, ob, lb, v00, [&]() { v10 = rd(sl0, 2048); v11 = rd(sl0, 3072); SB(); });
  sl0 = sptr(2);
  SB();
  SAW8(2);
  unit(sa, sb, k10, k11, qb0, qb1, ob, lb, v01, oa, la, v10, [&]() {
    f0 = *LDS_AT(const volatile unsigned, lbase + FLG);
    asm volatile("" ::: "memory");
    k00 = rd(sl0, 0); k01 = rd(sl0, 1024); SB(); });
  if (__builtin_amdgcn_readfirstlane(f0) < 3u) { check(3u); k00 = rd(sl0, 0); k01 = rd(sl0, 1024); }
  SB();
  SAW8(3);
  unit(sb, sa, k00, k01, qa0, qa1, oa, la, v11, ob, lb, v10, [&]() {
    v00 = rd(sl0, 2048); v01 = rd(sl0, 3072); f0 = *LDS_AT(const volatile unsigned, lbase + FLG); SB(); arrive_w(); SB(); });
  check(5u);
  sl0 = sptr(3);
  SB();
  three(1, true);
#pragma unroll 1
  for (int st = 2; st < 16; st += 2) { fourth(st - 1, true); three(st, false); fourth(st, false); three(st + 1, true); }
  SAW8(7);
  unsigned vr, tk, tv;
  u32x4 f4;
  auto table = [&]() {
    f4 = lds_rd4(lbase + FLG);
    k00 = *LDS_AT(const volatile s16x8, tk); k01 = *LDS_AT(const volatile s16x8, tk + 32);
    vr = *LDS_AT(const volatile u16, tv);
  };
  unsigned xw, xm;
  u32x2 ml;
  u32x4 o2[4];
  auto take = [&]() {
    ml = *LDS_AT(const volatile u32x2, xm);
#pragma unroll
    for (int e = 0; e < 4; ++e) o2[e] = lds_rd4(xw + e * 1024);
  };
  float pzt;
  int dso;
  f32x16& sc = sb;
#define EXP() do { asm volatile("" : "+v"(sc)); SB(); } while (0)
  oa = SMF32(v11, PHI(sa), oa); SB();
  tk = lbase + TAB + 128 + hh * 64 + (lane >> 5) * 16; tv = lbase + TAB + 256 + hh * 64 + (lane & 31) * 2;
  table();
  SB();
  EX(0); EX(1); EXP();
  la = SMF16(sel, PHI(sa), la); SB();
  EX(2); EX(3); EX(4); EXP();
  xw = wbase + (qg ? QOFF : 6 * ASB); xm = lbase + XML + wid * 512 + lane * 8;
  pzt = __builtin_amdgcn_exp2f(nn[0] + (zoff - 8.f));
  asm volatile("" : "+v"(xw), "+v"(xm), "+v"(pzt), "+v"(f4));
  SB();
  {
    const unsigned ph = LAY2 ? 0x79797979u : 0x80808080u, pg = qg ? 0u : 0x80u << (16 * hh + 8);
    auto ready = [&]() {
      return (((__builtin_amdgcn_readfirstlane(f4[1]) + ph) & 0x80808080u) == 0x80808080u) & (((__builtin_amdgcn_readfirstlane(g ? f4[3] : f4[2]) + 0x71717171u) & pg) == pg);
    };
    if (__builtin_expect(!ready(), 0)) {
#pragma unroll 1
      for (int i = 0; i < SPIN_MAX; ++i) {
        __builtin_amdgcn_s_sleep(1);
        table();
        if (ready()) break;
      }
    }
    asm volatile("" ::: "memory");
  }
  SB();
  sa = MFMA32(k00, qb0, nn); asm volatile("" : "+v"(sa)); SB();
#pragma unroll
  for (int e = 0; e < 4; ++e) *LDS_AT(volatile f32x4, xw + e * 1024) = f32x4{oa[4 * e], oa[4 * e + 1], oa[4 * e + 2], oa[4 * e + 3]};
  *LDS_AT(volatile u32x2, xm) = u32x2{__float_as_uint(ma), f2word(pzt <= la[0] ? la[0] : 0.f)};
  SB();
  EX(5); EX(6); EX(7); EXP();
  sa = MFMA32(k01, qb1, sa); asm volatile("" : "+v"(sa), "+v"(vr)); SB();
  ARGS_HERE;
  EX(8); EX(9); EX(10); EXP();
  ob = SMF32(v10, PLO(sb), ob); SB();
  EX(11); EX(12); EX(13); EXP();
  lb = SMF16(sel, PLO(sb), lb); SB();
  const bool used = !LAY2 || (qt + g) * 32 + (lane & 31) != idx;
  dso = used ? (((qt + g) * 32 + head) * 1024 + lane * 8) * 2 : S_ * D_ * 2;
  xw += g ? -4096 : 4096; xm += g ? -2048 : 2048;
  asm volatile("" : "+v"(dso), "+v"(xw), "+v"(xm));
  SB();
  EX(14); EX(15); EXP();
  take();
  SB();
  lb = SMF16(sel, PHI(sb), lb); SB();
  ob = SMF32(v11, PHI(sb), ob); SB();
  __builtin_amdgcn_s_setprio(EPRIO);
  TE(0, 0); TE(1, 0); TE(2, 0); TE(3, 0); TE(7, 0);
  float lsb = lb[0];
  if (LAY2) {
    sa[0] = __builtin_amdgcn_exp2f(sa[0]);
    ob = SMF32((u32x4{lane < 32 ? vr : 0u, 0u, 0u, 0u}), PLO(sa), ob);
    lsb += __uint_as_float(__float_as_uint(sa[0]) & 0xffff0000u);
    lsb = pzt <= lsb ? lsb : 0.f;
  }
  const __amdgpu_buffer_rsrc_t rso = __builtin_amdgcn_make_buffer_rsrc(Of, 0, S_ * D_ * 2, 0x00020000);
  TE(0, 1);
  if (__builtin_amdgcn_ballot_w64(ml[1] == SENT) != 0ull) {
#pragma unroll 1
    for (int i = 0; i < SPIN_MAX; ++i) {
      __builtin_amdgcn_s_sleep(XSLP);
      if (!__builtin_amdgcn_ballot_w64(*LDS_AT(const volatile unsigned, xm + 4) == SENT)) break;
    }
    asm volatile("s_waitcnt lgkmcnt(0)" ::: "memory");
    take();
  }
  arrive();
  TE(0, 2);
  TE(0, 3); TE(1, 1);
  {
    const float m2 = __uint_as_float(ml[0]), l2 = __uint_as_float(ml[1]);
    const float mx = fmaxf(ma, m2);
    const float f1 = __builtin_amdgcn_exp2f(ma - mx), f2 = __builtin_amdgcn_exp2f(m2 - mx);
    const float inv = frcp(lsb * f1 + l2 * f2);
    const float c1 = f1 * inv, c2 = f2 * inv;
    f32x2 r[8];
#pragma unroll
    for (int e = 0; e < 8; ++e) {
      const unsigned x0 = o2[e >> 1][(e & 1) * 2], x1 = o2[e >> 1][(e & 1) * 2 + 1];
      r[e] = f32x2{__uint_as_float(x0) * c2, __uint_as_float(x1) * c2};
    }
#pragma unroll
    for (int e = 0; e < 8; ++e) r[e] = f32x2{ob[2 * e], ob[2 * e + 1]} * f32x2{c1, c1} + r[e];
    auto put = [&]() {
#pragma unroll
      for (int sx = 0; sx < 2; ++sx)
        __builtin_amdgcn_raw_buffer_store_b128(u32x4{pack2(r[4 * sx][0], r[4 * sx][1]), pack2(r[4 * sx + 1][0], r[4 * sx + 1][1]),
            pack2(r[4 * sx + 2][0], r[4 * sx + 2][1]), pack2(r[4 * sx + 3][0], r[4 * sx + 3][1])}, rso, dso + sx * 1024, 0, 16);
    };
    put();
    PIN();
    __builtin_amdgcn_s_setprio(0);
    PIN();
    f32x2 chk = {lsb - __builtin_amdgcn_fmed3f(lsb, 8.6736174e-19f, 1.2676506e30f), l2 - __builtin_amdgcn_fmed3f(l2, 8.6736174e-19f, 1.2676506e30f)};
#pragma unroll
    for (int e = 0; e < 8; ++e) chk = r[e] * f32x2{0.f, 0.f} + chk;
    if (__builtin_expect(__builtin_amdgcn_ballot_w64(used && !(chk[0] + chk[1] == 0.f)) != 0ull, 0)) {
      float l;
      attn_safe(KV, head * 64, qb0, qb1, ob, l, LAY2 ? head * 64 + (idx >> 5) : -1, idx & 31, lbase + TAB + 128 + hh * 64);
      const float inv2 = 1.0f / l;
#pragma unroll
      for (int e = 0; e < 8; ++e) r[e] = f32x2{ob[2 * e] * inv2, ob[2 * e + 1] * inv2};
      asm volatile("s_waitcnt vmcnt(0)" ::: "memory");
      put();
    }
    unsigned pp = 0u;
    if (LAY2) {
      const unsigned ua = lbase + UGL + hh * 128 + (lane >> 5) * 16;
      f32x2 du = {0.f, 0.f}, dn = {0.f, 0.f};
#pragma unroll
      for (int q = 0; q < 4; ++q) {
        const f32x4 uu = *LDS_AT(const f32x4, ua + q * 32), gg = *LDS_AT(const f32x4, ua + 256 + q * 32);
        du = r[2 * q] * f32x2{uu[0], uu[1]} + du; du = r[2 * q + 1] * f32x2{uu[2], uu[3]} + du;
        const f32x2 t0 = r[2 * q] * f32x2{gg[0], gg[1]}, t1 = r[2 * q + 1] * f32x2{gg[2], gg[3]};
        dn = t0 * t0 + dn; dn = t1 * t1 + dn;
      }
      const auto a = __builtin_amdgcn_permlane32_swap(__float_as_uint(du[0] + du[1]), __float_as_uint(du[0] + du[1]), false, false);
      const auto c = __builtin_amdgcn_permlane32_swap(__float_as_uint(dn[0] + dn[1]), __float_as_uint(dn[0] + dn[1]), false, false);
      pp = f2word(__uint_as_float(pack2(__uint_as_float(a[0]) + __uint_as_float(a[1]), __uint_as_float(c[0]) + __uint_as_float(c[1]))));
    }
    __builtin_amdgcn_raw_buffer_store_b32(pp, __builtin_amdgcn_make_buffer_rsrc(hand + 2 * HAND_N + PP_O, 0, LAY2 ? H_ * S_ * 4 : 0, 0x00020000),
                                          lane < 32 ? (head * S_ + (qt + g) * 32 + lane) * 4 : H_ * S_ * 4, 0, 16);
    PIN();
  }
  TE(1, 2);
  u32x4 xres[2];
  oproj_tail(LAY2, tid, lbase, lbase + FLG, omi, oni, idx, gran + GR_O + ((LAY2 ? 16 : 0) + tmi) * (64 * RO) + tni,
             gran + GR_X + omi * (128 * RX) + oni * 4, tag, Of, xout, A, w_r, stats_out, stats, hand, xres TEPASS);
  if (LAY2) {
    const int row = omi * 128 + (wid & 3) * 32 + (wid >> 2) * 16 + (lane >> 3), col = oni * 64 + (lane & 7) * 8;
    unsigned md[2], best = 0u;
    const __amdgpu_buffer_rsrc_t rsk = __builtin_amdgcn_make_buffer_rsrc(hand + HAND_N, 0, (HAND_N + PS_N) * 4, 0x00020000);
    f32x2 xf[2][4];
#pragma unroll
    for (int p = 0; p < 2; ++p)
#pragma unroll
      for (int e = 0; e < 4; ++e) {
        xf[p][e] = f32x2{__uint_as_float(xres[p][e] << 16), __uint_as_float(xres[p][e] & 0xffff0000u)};
        asm volatile("" : "+v"(xf[p][e]));
      }
    LOGDEF;
    LOG();
    const bool rtr = (oni & 7) == 7;
    {
      const int rl = (wid & 3) * 32 + (wid >> 2) * 16 + (lane & 15), rr = omi * 128 + rl;
      int src = (HAND_N + ((omi * 16 + (lane >> 4) * 4) * 128 + rl) * 4) * 4;
      int ko = rtr && (wid >> 2) == (oni >> 3) && lane < NREP ? lane * 1024 + (rr >> 4) * 8 : (HAND_N + PS_N) * 4;
      int bp = (lane >> 3) * 4;
      const float wsum = *LDS_AT(const volatile float, lbase + SPM + 132);
      asm volatile("" : "+v"(src), "+v"(ko), "+v"(bp));
      u32x4 v[4];
      __builtin_amdgcn_s_sleep(MPRE);
#pragma unroll 1
      for (int spin = 0; spin < SPIN_MAX; ++spin) {
        LOG();
#pragma unroll
        for (int i = 0; i < 4; ++i) v[i] = __builtin_amdgcn_raw_buffer_load_b128(rsk, src + (i & 1) * 2048, (i >> 1) * 4096, 16);
        unsigned mx = 0u;
#pragma unroll
        for (int i = 0; i < 4; ++i) mx = umax(umax(umax(mx, v[i][0]), v[i][1]), v[i][2]);
        if (none(mx == SENT)) break;
        __builtin_amdgcn_s_sleep(MSLP);
        asm volatile("" ::: "memory");
      }
      LOGAT(12);
      if (rtr) __builtin_amdgcn_s_setprio(3); else __builtin_amdgcn_s_setprio(LNPRIO);
      double sm[3];
#pragma unroll
      for (int c = 0; c < 3; ++c) {
        const double a = ((double)__uint_as_float(v[0][c]) + (double)__uint_as_float(v[1][c])) + ((double)__uint_as_float(v[2][c]) + (double)__uint_as_float(v[3][c]));
        u32x2 q = __builtin_bit_cast(u32x2, a);
        const auto lo = __builtin_amdgcn_permlane32_swap(q[0], q[0], false, false), hi = __builtin_amdgcn_permlane32_swap(q[1], q[1], false, false);
        q = __builtin_bit_cast(u32x2, __builtin_bit_cast(double, u32x2{lo[0], hi[0]}) + __builtin_bit_cast(double, u32x2{lo[1], hi[1]}));
        const auto l2 = __builtin_amdgcn_permlane16_swap(q[0], q[0], false, false), h2 = __builtin_amdgcn_permlane16_swap(q[1], q[1], false, false);
        sm[c] = __builtin_bit_cast(double, u32x2{l2[0], h2[0]}) + __builtin_bit_cast(double, u32x2{l2[1], h2[1]});
      }
      float mean, var, mb, den;
      ln_from_sums(sm[0], sm[1], mean, var, mb, den);
      const unsigned mdr = f2word(__uint_as_float((__float_as_uint(mb) >> 16) | (__float_as_uint(den) & 0xffff0000u)));
      md[0] = __builtin_amdgcn_ds_bpermute(bp, mdr); md[1] = __builtin_amdgcn_ds_bpermute(bp + 32, mdr);
      if (rtr) {
        const float r = (float)fma(-(double)mb, (double)wsum, sm[2]) * frcp(den);
        const unsigned rb = pack2(r, r);
        unsigned rkey = (rb ^ ((unsigned)((int)rb >> 31) | 0x80000000u)) & 0xffff0000u;
        if (r != r) rkey = 0u;
        const unsigned top = wave_umax(rkey | (unsigned)(2047 - rr));
        const unsigned mdt = __builtin_amdgcn_readlane(mdr, (2047 - (int)(top & 0xffffu)) & 15);
        __builtin_amdgcn_raw_buffer_store_b64(u32x2{top, mdt}, rsk, ko, 0, 16);
        PIN();
        __builtin_amdgcn_s_setprio(LNPRIO);
        __hip_atomic_fetch_add(LDS_AT(unsigned, lane == 0 ? lbase + SPM + 128 : lbase + DUM + lane * 4), 1u, __ATOMIC_RELAXED, __HIP_MEMORY_SCOPE_WORKGROUP);
        if (wid == 2 || wid == 5) STAMP(8, 6);
      }
      LOGAT(13);
    }
    auto keys = [&](const u32x4& v) __attribute__((always_inline)) {
      if (none(umax(umax(v[0], v[1]), umax(v[2], v[3])) == SENT)) {
        best = wave_umax(umax(v[0], v[2])) | 0x8000u;
        *LDS_AT(volatile u32x2, lbase + FLG + 40) = u32x2{best, (unsigned)__builtin_amdgcn_readlane(v[0] > v[2] ? v[1] : v[3], ((2047 - (int)(best & 0x7ffu)) >> 5) & 63)};
        STAMP(8, 11);
      }
    };
    auto ln_rows = [&]() __attribute__((always_inline)) {
#pragma unroll
      for (int p = 0; p < 2; ++p) {
        const float mb_ = __uint_as_float(md[p] << 16), rin = frcp(__uint_as_float(md[p] & 0xffff0000u));
#pragma unroll
        for (int e = 0; e < 4; ++e) {
          const f32x2 h = (xf[p][e] - f32x2{mb_, mb_}) * f32x2{rin, rin} + xf[p][e];
          xres[p][e] = pack2(h[0], h[1]);
        }
      }
      asm volatile("" : "+v"(xres[0]), "+v"(xres[1]));
    };
    if (wid == 0) STAMP(8, 12);
    ln_rows();
    LOGAT(14);
    const __amdgpu_buffer_rsrc_t rsu = __builtin_amdgcn_make_buffer_rsrc(out, 0, S_ * D_ * 2, 0x00020000);
    unsigned fr1 = SENT;
#pragma unroll 1
    for (int i = 0; i < 4 * SPIN_MAX && fr1 == SENT; ++i) fr1 = __builtin_amdgcn_readfirstlane(*LDS_AT(const volatile unsigned, lbase + SPM + 144));
    const int fr2 = (fr1 >> 16) & 0x7ffu;
    fr1 &= 0x7ffu;
    unsigned tmx = __builtin_amdgcn_readfirstlane(*LDS_AT(const volatile unsigned, lbase + SPM + 136)) + KDT;
    if (tmx - (unsigned)__builtin_amdgcn_s_memrealtime() > 1024u) tmx = 0u;
    auto due = [&]() { return tmx == 0u || (int)((unsigned)__builtin_amdgcn_s_memrealtime() - tmx) >= 0; };
    lk_set(15);
    const bool held = !none((row | 8) == ((int)fr1 | 8) || (row | 8) == (fr2 | 8));
#pragma unroll
    for (int p = 0; p < 2; ++p) __builtin_amdgcn_raw_buffer_store_b128(xres[p], rsu, row + 8 * p != (int)fr1 && row + 8 * p != fr2 ? ((row + 8 * p) * D_ + col) * 2 : S_ * D_ * 2, 0, OAUX);
    PIN();
    if (wid == 6) {
      if (rtr) {
#pragma unroll 1
        for (int i = 0; i < SPIN_MAX; ++i) {
          if (__builtin_amdgcn_readfirstlane(*LDS_AT(const volatile unsigned, lbase + SPM + 128)) == 7u) break;
          __builtin_amdgcn_s_sleep(1);
        }
        asm volatile("" ::: "memory");
      }
      while (!due()) __builtin_amdgcn_s_sleep(1);
    }
#pragma unroll 1
    for (int i = 0; i < 4 * SPIN_MAX && !best; ++i) {
      if (wid == 6) {
        LOG();
        keys(__builtin_amdgcn_raw_buffer_load_b128(rsk, (bid % NREP) * 1024 + lane * 16, 0, 16));
        if (!best) __builtin_amdgcn_s_sleep(KSLP);
      } else {
        best = __builtin_amdgcn_readfirstlane(*LDS_AT(const volatile unsigned, lbase + FLG + 40));
        if (!best) __builtin_amdgcn_s_sleep(1);
      }
      asm volatile("" ::: "memory");
    }
    const int idx2 = 2047 - (int)(best & 0x7ffu);
    if (held) {
#pragma unroll
      for (int p = 0; p < 2; ++p) {
        const int r = row + 8 * p;
        __builtin_amdgcn_raw_buffer_store_b128(xres[p], rsu, (r == (int)fr1 || r == fr2) && r != idx2 ? (r * D_ + col) * 2 : S_ * D_ * 2, 0, OAUX);
      }
    }
    if (__builtin_expect(idx2 != (int)fr1 && idx2 != fr2, 0) && !none((row | 8) == (idx2 | 8))) {
#pragma unroll
      for (int p = 0; p < 2; ++p)
        __builtin_amdgcn_raw_buffer_store_b128(u32x4{pack2(xf[p][0][0], xf[p][0][1]), pack2(xf[p][1][0], xf[p][1][1]), pack2(xf[p][2][0], xf[p][2][1]), pack2(xf[p][3][0], xf[p][3][1])},
            __builtin_amdgcn_make_buffer_rsrc(xout, 0, 3 * D_ * 2, 0x00020000), row + 8 * p == idx2 ? (2 * D_ + col) * 2 : S_ * D_ * 2, 0, 16);
      asm volatile("s_waitcnt vmcnt(0)" ::: "memory");
      if (lane == 0) st_ag(gran + GR_F + oni * 32, tag);
    }
  }
  }
}
struct Args2 { Args l[2]; };
extern "C" __global__ __launch_bounds__(768, 3) void layers_kernel(const Args2 args) {
  __shared__ __attribute__((aligned(128))) char smem[163840];
  const unsigned long long t_entry = 0;
  const int tid = threadIdx.x, bid = blockIdx.x;
  int wid = __builtin_amdgcn_readfirstlane(tid >> 6);
  asm volatile("" : "+s"(wid));
  const ArgP ap = (ArgP)__builtin_amdgcn_kernarg_segment_ptr();
  layer_body(wid, bid, (unsigned)(uintptr_t)smem, ap, t_entry);
  LAYER_HERE;
  if (!LAY2) {
    asm volatile(AGAIN :: "{s[0:1]}"(ap + 1), "{s2}"(bid), "{v0}"(wid * 64) : "s90", "s91", "scc", "memory");
    __builtin_unreachable();
  }
}

extern "C" __global__ void end_marker_kernel() {}

extern "C" void kernel_launch(void* const* d_in, const int* in_sizes, int n_in, void* d_out, int out_size,
                              void* d_ws, size_t ws_size, hipStream_t stream) {
  const u16* x0 = (const u16*)d_in[0];
  const u16* wqkv = (const u16*)d_in[1];
  const u16* wo = (const u16*)d_in[2];
  const u16* w_r = (const u16*)d_in[4];
  const u16* wg = (const u16*)d_in[5];
  const u16* wu = (const u16*)d_in[6];
  const u16* wd = (const u16*)d_in[7];
  u16* out = (u16*)d_out;

  char* ws = (char*)d_ws + (WEND ? (ws_size - (48u << 20)) & ~(size_t)0x1fffff : 0);
  const size_t MB4 = (size_t)S_ * D_ * 2;
  u16* kv1 = (u16*)(ws);
  u16* o1 = (u16*)(ws + 2 * MB4);
  u16* x1 = (u16*)(ws + 3 * MB4);
  unsigned* gran = (unsigned*)(ws + 4 * MB4);
  float* csf = (float*)(gran + GR_F + 16 * 32);
  double* stats = (double*)(csf + 8192);
  unsigned* hand = (unsigned*)(stats + 2 * STAT_N);
  static_assert((GR_F + 16 * 32) % 32 == 0 && (size_t)(GR_F + 16 * 32 + 8192 + 4 * STAT_N + 2 * HAND_N + PS_N) * 4 <= 6u << 20, "");
  u16* x3 = (u16*)(hand + HAND_N + GRAN_N + Y_N);
  u16* wc = (u16*)((((uintptr_t)ws + 4 * MB4 + (6u << 20) + 2047) & ~(uintptr_t)2047) + (((uintptr_t)wqkv + 128) & 2047));
  u16* kv2 = (ALIAS & 1) ? kv1 : (u16*)(ws + 8 * MB4);
  u16* o2 = (ALIAS & 2) ? o1 : (u16*)(ws + 10 * MB4);
  Args2 a;
  for (int l = 0; l < 2; ++l) {
    Args& r = a.l[l];
    r.lay = l; r.a = l ? x1 : x0; r.kv = l ? kv2 : kv1; r.o = l ? o2 : o1; r.xo = l ? x3 : x1; r.so = l ? stats + STAT_N : stats;
    r.W = wqkv; r.wc = wc; r.wg = wg; r.wu = wu; r.wd = wd; r.wo = wo; r.w_r = w_r; r.gran = gran; r.csf = csf; r.stats = stats; r.hand = hand; r.out = out;
  }
  layers_kernel<<<256, 768, 0, stream>>>(a);
}
```

```cpp
#include <hip/hip_runtime.h>
#include <hip/hip_bf16.h>
#include <stdint.h>
#include <type_traits>
#include <utility>

typedef unsigned short u16;
typedef short s16x4 __attribute__((ext_vector_type(4)));
typedef short s16x8 __attribute__((ext_vector_type(8)));
typedef float f32x16 __attribute__((ext_vector_type(16)));
typedef float f32x4 __attribute__((ext_vector_type(4)));
typedef unsigned int u32x4 __attribute__((ext_vector_type(4)));
typedef unsigned int u32x2 __attribute__((ext_vector_type(2)));
typedef __bf16 bf16x8 __attribute__((ext_vector_type(8)));
typedef __bf16 bf16x16 __attribute__((ext_vector_type(16)));

__device__ __forceinline__ int fresh_lane() {
  int l;
  asm volatile("v_mbcnt_lo_u32_b32 %0, -1, 0\n\tv_mbcnt_hi_u32_b32 %0, -1, %0" : "=v"(l));
  return l;
}
#define STAMP(k, i)
#define STAMPV(k, i, v)
#define LOGDEF
#define LOG()
#define LOGAT(k)
#define lk_set(k)
#define LOGOUT(slot)
#define TE(grp, k)
#define TEARG
#define TEPASS
constexpr int S_ = 2048, D_ = 1024, H_ = 32, F_ = 3584;

__device__ __forceinline__ int kpos(int r) { return (r & ~12) | ((r & 4) << 1) | ((r & 8) >> 1); }
__device__ __forceinline__ float bf2f(u16 v) { return __uint_as_float(((unsigned)v) << 16); }
__device__ __forceinline__ u16 f2bf(float f) {
  __hip_bfloat16 b = __float2bfloat16(f);
  return *reinterpret_cast<u16*>(&b);
}
typedef float f32x2 __attribute__((ext_vector_type(2)));
typedef __bf16 bf16x2 __attribute__((ext_vector_type(2)));
__device__ __forceinline__ unsigned pack2(float lo, float hi) {
  f32x2 v = {lo, hi};
  return __builtin_bit_cast(unsigned, __builtin_convertvector(v, bf16x2));
}
__device__ __forceinline__ float rbf(float f) { return __uint_as_float(pack2(f, f) << 16); }
__device__ __forceinline__ void rbf2(float& a, float& b) {
  const unsigned p = pack2(a, b);
  a = __uint_as_float(p << 16); b = __uint_as_float(p & 0xffff0000u);
}

__device__ __forceinline__ float fadd(float a, float b) { float r = a + b; asm("" : "+v"(r)); return r; }
template <int CTRL, int RMASK>
__device__ __forceinline__ float dpp_add(float v) {
  const int t = __builtin_amdgcn_update_dpp(0, __float_as_int(v), CTRL, RMASK, 0xf, false);
  return v + __int_as_float(t);
}
__device__ __forceinline__ float wave_sum(float v) {
  v = dpp_add<0x111, 0xf>(v);
  v = dpp_add<0x112, 0xf>(v);
  v = dpp_add<0x114, 0xf>(v);
  v = dpp_add<0x118, 0xf>(v);
  v = dpp_add<0x142, 0xa>(v);
  v = dpp_add<0x143, 0xc>(v);
  return __int_as_float(__builtin_amdgcn_readlane(__float_as_int(v), 63));
}

template <int CTRL, int RMASK>
__device__ __forceinline__ unsigned dpp_umax(unsigned v) {
  const unsigned t = (unsigned)__builtin_amdgcn_update_dpp(0, (int)v, CTRL, RMASK, 0xf, false);
  return v > t ? v : t;
}
__device__ __forceinline__ unsigned wave_umax(unsigned v) {
  v = dpp_umax<0x111, 0xf>(v); v = dpp_umax<0x112, 0xf>(v); v = dpp_umax<0x114, 0xf>(v); v = dpp_umax<0x118, 0xf>(v);
  v = dpp_umax<0x142, 0xa>(v); v = dpp_umax<0x143, 0xc>(v);
  return (unsigned)__builtin_amdgcn_readlane((int)v, 63);
}

#define NREP 32
constexpr unsigned SENT = 0xFFFFFFFFu;
constexpr int GRAN_N = 256 * NREP;
constexpr int Y_N = F_ / 16 * D_;
constexpr int X2_N = D_ / 2;
constexpr int XS_N = 128;
constexpr int P4_N = 3 * D_ * 16;
constexpr int PA_N = H_ * 8 * 64;
constexpr int HAND_N = GRAN_N + Y_N + X2_N + XS_N + P4_N + PA_N;
constexpr int MD_O = 16 * 16 * 128 * 4;
constexpr int PK_O = MD_O;
constexpr int PP_O = PK_O + 128 * NREP;
constexpr int Y3_O = PP_O + H_ * S_;
constexpr int PS_N = Y3_O + Y_N;
constexpr int GR_ROW = 64, GR_COL = GR_ROW + 4 * S_, GR_KV = GR_COL + 16 * 16 * 192;
#define RO 16
#define RK 16
#define RX 1
constexpr int GR_O = GR_KV + 2 * 16 * 32 * RK, GR_X = GR_O + 2 * 16 * 64 * RO, GR_N = GR_X + 16 * 128 * RX;
constexpr int GR_F = GR_N + 256 * 64;
constexpr int STAT_N = 3 * S_;

__device__ __forceinline__ void st_ag(unsigned* p, unsigned v) { __hip_atomic_store(p, v, __ATOMIC_RELAXED, __HIP_MEMORY_SCOPE_AGENT); }
__device__ __forceinline__ unsigned ld_ag(const unsigned* p) { return __hip_atomic_load(p, __ATOMIC_RELAXED, __HIP_MEMORY_SCOPE_AGENT); }
__device__ __forceinline__ double ld_ag(const double* p) { return __hip_atomic_load(p, __ATOMIC_RELAXED, __HIP_MEMORY_SCOPE_AGENT); }
__device__ __forceinline__ unsigned f2word(float f) { const unsigned u = __float_as_uint(f); return u < 0xFFFFFFFEu ? u : 0xFFFFFFFEu; }
constexpr int SPIN_MAX = 1 << 18;
__device__ __forceinline__ unsigned umax(unsigned a, unsigned b) { return a > b ? a : b; }
__device__ __forceinline__ bool none(bool p) { return __builtin_amdgcn_ballot_w64(p) == 0ull; }

__device__ __forceinline__ void ln_from_sums(double s1, double s2, float& mean, float& var, float& mb, float& den) {
  const double md = s1 * (1.0 / 1024.0);
  const double vd = fma(-md, md, s2 * (1.0 / 1024.0));
  mean = (float)md; var = fmaxf((float)vd, 0.f);
  mb = rbf(mean);
  den = rbf(__builtin_amdgcn_sqrtf(rbf(rbf(var) + 1e-5f)));
}
__device__ __forceinline__ float frcp(float x) { return __builtin_amdgcn_rcpf(x); }
__device__ __forceinline__ void ln_apply2(float& a, float& b, float mb, float den, float rin) {
  a -= mb; b -= mb;
  rbf2(a, b);
  float qa = a * rin, qb = b * rin;
  qa = fmaf(fmaf(-qa, den, a), rin, qa);
  qb = fmaf(fmaf(-qb, den, b), rin, qb);
  rbf2(qa, qb);
  a = qa; b = qb;
}

#define CODE_ADDR(SYM, out64)                                                                                          \
  do {                                                                                                                 \
    unsigned lo_, hi_;                                                                                                 \
    asm volatile("s_getpc_b64 s[90:91]\n\ts_add_u32 s90, s90, " SYM "@rel32@lo+4\n\ts_addc_u32 s91, s91, " SYM          \
                 "@rel32@hi+12\n\ts_mov_b32 %0, s90\n\ts_mov_b32 %1, s91"                                             \
                 : "=s"(lo_), "=s"(hi_) :: "s90", "s91", "scc");                                                        \
    out64 = ((unsigned long long)hi_ << 32) | lo_;                                                                     \
  } while (0)

#define QNX 4
constexpr int QMX = 32 / QNX, QNG = 16 / QNX, QMG = 8 / QNG;

__device__ __forceinline__ unsigned touch(const void* p) { return *reinterpret_cast<const unsigned*>(p); }

__device__ __forceinline__ unsigned touch_code(unsigned long long lo, unsigned long long hi, int pc, int lane) {
  const unsigned long long p = lo + (unsigned)(pc * 8192 + lane * 128);
  return p + 4 <= hi ? *reinterpret_cast<__attribute__((address_space(1))) const unsigned*>(p) : 0u;
}

template <bool SC = false>
__device__ __forceinline__ void glds16s(const void* sbase, unsigned voff, unsigned lds_dst) {
  unsigned keep;
  if (SC)
    asm volatile("s_mov_b32 %0, m0\n\ts_mov_b32 m0, %3\n\ts_nop 2\n\tglobal_load_lds_dwordx4 %1, %2 sc1\n\ts_mov_b32 m0, %0"
                 : "=&s"(keep) : "v"(voff), "s"(sbase), "s"(lds_dst) : "memory");
  else
  asm volatile("s_mov_b32 %0, m0\n\ts_mov_b32 m0, %3\n\ts_nop 2\n\tglobal_load_lds_dwordx4 %1, %2\n\ts_mov_b32 m0, %0"
               : "=&s"(keep) : "v"(voff), "s"(sbase), "s"(lds_dst) : "memory");
}
template <bool SC>
__device__ __forceinline__ void glds16x4(const void* sbase, unsigned v0, unsigned v1, unsigned v2, unsigned v3, unsigned lds_dst) {
  unsigned keep;
  if (SC)
    asm volatile("s_mov_b32 %0, m0\n\ts_mov_b32 m0, %6\n\ts_nop 2\n\tglobal_load_lds_dwordx4 %1, %5 sc1\n\tglobal_load_lds_dwordx4 %2, %5 offset:1024 sc1\n\t"
                 "global_load_lds_dwordx4 %3, %5 offset:2048 sc1\n\tglobal_load_lds_dwordx4 %4, %5 offset:3072 sc1\n\ts_mov_b32 m0, %0"
                 : "=&s"(keep) : "v"(v0), "v"(v1), "v"(v2), "v"(v3), "s"(sbase), "s"(__builtin_amdgcn_readfirstlane(lds_dst)) : "memory");
  else
    asm volatile("s_mov_b32 %0, m0\n\ts_mov_b32 m0, %6\n\ts_nop 2\n\tglobal_load_lds_dwordx4 %1, %5\n\tglobal_load_lds_dwordx4 %2, %5 offset:1024\n\t"
                 "global_load_lds_dwordx4 %3, %5 offset:2048\n\tglobal_load_lds_dwordx4 %4, %5 offset:3072\n\ts_mov_b32 m0, %0"
                 : "=&s"(keep) : "v"(v0), "v"(v1), "v"(v2), "v"(v3), "s"(sbase), "s"(__builtin_amdgcn_readfirstlane(lds_dst)) : "memory");
}
constexpr float QSCALE = 0.17677669529663687f * 1.4426950408889634f;

__device__ __forceinline__ unsigned min4(u32x4 v) { const unsigned a = v[0] < v[1] ? v[0] : v[1], b = v[2] < v[3] ? v[2] : v[3]; return a < b ? a : b; }
#define LDS_AT(T, a) ((__attribute__((address_space(3))) T*)(uintptr_t)(a))
__device__ __forceinline__ u32x4 lds_rd4(unsigned a) { return *LDS_AT(const volatile u32x4, a); }
__device__ __forceinline__ void lds_wr(unsigned a, unsigned v) { *LDS_AT(volatile unsigned, a) = v; }
__device__ __forceinline__ void split3(float v, unsigned& p0, unsigned& p1, unsigned& p2) {
  p0 = pack2(v, v) & 0xffffu; v -= __uint_as_float(p0 << 16);
  p1 = pack2(v, v) & 0xffffu; v -= __uint_as_float(p1 << 16);
  p2 = pack2(v, v) & 0xffffu;
}

#define PIN() __builtin_amdgcn_sched_barrier(0)
template <int B, class F, int... I>
__device__ __forceinline__ void static_for_(F&& f, std::integer_sequence<int, I...>) { (f(std::integral_constant<int, B + I>{}), ...); }
template <int B, int E, class F>
__device__ __forceinline__ void static_for(F&& f) { static_for_<B>(f, std::make_integer_sequence<int, E - B>{}); }

#define TPRE 60
#define TPSL 0
#define FCPRE 44
#define FCSLP 2
#define SIGAT 24
#define XPRIO 1
#define OPD 6
#define LAUX 16
#define CSLP 2
#define EPRIO 3
#define OPRIO 1
#define XSLP 1
#define CSL1 8
#define XAUX 16
__device__ __forceinline__ void oproj_tail(const bool RES2, const int tid, const unsigned lbase, const unsigned flg, const int tmi, const int tni, const int idx,
                                           unsigned* osig, unsigned* xsig, const unsigned tag,
                                           const u16* A, u16* out,
                                           const u16* res, const u16* __restrict__ w_r, double* stats,
                                           const double* stats_in, unsigned* hand, u32x4 (&xres)[2] TEARG) {
  constexpr int ldw = D_, PD = OPD;
  const int lane = tid & 63, wid = __builtin_amdgcn_readfirstlane(tid >> 6);
  const int m0 = tmi * 128, n0 = tni * 64;
  const int dk = RES2 ? 7 : 3;
  if (wid == 0) STAMP(dk, 0);
  TE(1, 3);
  asm volatile("" : "+s"(A) :: "memory");
  PIN();
  const int row = m0 + (wid & 3) * 32 + (wid >> 2) * 16 + (lane >> 3), col = n0 + (lane & 7) * 8;
  const unsigned tb = lbase + 147456;
  typedef __attribute__((address_space(1))) double* gdp;
  typedef __attribute__((address_space(1))) unsigned* gup;
  unsigned tr, oad;
  gdp pst;
  gup pps;
  int xo;
  auto setup = [&]() __attribute__((always_inline)) {
    tr = tb + (row - m0) * 128 + (((lane ^ (lane >> 3)) & 7) << 4);
    pst = (gdp)(stats + ((lane & 3) == 3 ? 0 : (lane & 3)) * S_ + row + 8 * ((lane >> 2) & 1));
    pps = (gup)(hand + 2 * HAND_N + ((tmi * 16 + tni) * 128 + row - m0 + 8 * ((lane >> 2) & 1)) * 4 + (lane & 3));
    xo = (row * ldw + col) * 2;
    asm volatile("" : "+v"(tr), "+v"(pst), "+v"(pps), "+v"(xo));
    PIN();
  };
  {
    oad = lane == 0 ? flg + 24 : flg + 64 + lane * 4;
    gup ps = (gup)(osig + lane * 64);
    asm volatile("" : "+v"(oad), "+v"(ps));
    asm volatile("s_waitcnt vmcnt(1)" ::: "memory");
    TE(2, 1);
    const unsigned old = __hip_atomic_fetch_add(LDS_AT(unsigned, oad), 1u, __ATOMIC_RELAXED, __HIP_MEMORY_SCOPE_WORKGROUP);
    if (__builtin_amdgcn_readfirstlane(old) == 7u && lane < RO) __hip_atomic_store(ps, tag, __ATOMIC_RELAXED, __HIP_MEMORY_SCOPE_AGENT);
    PIN();
    TE(2, 2);
    if (wid == 0) STAMP(dk, 12);
  }
  auto announce = [&]() __attribute__((always_inline)) {
    asm volatile("" ::: "memory");
    PIN();
    __builtin_amdgcn_s_barrier();
    PIN();
    if (wid == 0) STAMP(dk, 1);
    TE(2, 3); TE(4, 0); TE(7, 1);
  };
  u32x4 rwr;
  auto sums = [&](const u32x4& x0, const u32x4& x1) __attribute__((always_inline)) {
    float f[2][3];
#pragma unroll
    for (int p = 0; p < 2; ++p) {
      f[p][0] = f[p][1] = f[p][2] = 0.f;
#pragma unroll
      for (int e = 0; e < 4; ++e) {
        const unsigned xu = p ? x1[e] : x0[e], wu = rwr[e];
        const bf16x2 x = __builtin_bit_cast(bf16x2, xu);
        f[p][0] = __builtin_amdgcn_fdot2_f32_bf16(x, __builtin_bit_cast(bf16x2, 0x3f803f80u), f[p][0], false);
        f[p][1] = __builtin_amdgcn_fdot2_f32_bf16(x, x, f[p][1], false);
        f[p][2] = __builtin_amdgcn_fdot2_f32_bf16(x, __builtin_bit_cast(bf16x2, wu), f[p][2], false);
      }
    }
    auto addx = [&](float keep, float give, auto ctl) {
      return keep + __int_as_float(__builtin_amdgcn_update_dpp(0, __float_as_int(give), decltype(ctl)::value, 0xf, 0xf, false));
    };
    const bool b2 = lane & 4, b1 = lane & 2, z0 = (lane & 3) == 0, z1 = (lane & 3) == 1;
    float g[3];
#pragma unroll
    for (int c = 0; c < 3; ++c) g[c] = addx(b2 ? f[1][c] : f[0][c], b2 ? f[0][c] : f[1][c], std::integral_constant<int, 0x141>{});
    const float u1 = addx(b1 ? g[2] : g[0], b1 ? g[0] : g[2], std::integral_constant<int, 0x4e>{});
    const float u2 = addx(g[1], g[1], std::integral_constant<int, 0x4e>{});
    return addx(z1 ? u2 : u1, z0 ? u2 : u1, std::integral_constant<int, 0xb1>{});
  };
  if (wid < 4) {
    const __amdgpu_buffer_rsrc_t rsa = __builtin_amdgcn_make_buffer_rsrc(const_cast<u16*>(A), 0, S_ * D_ * 2, 0x00020000);
    int sa0 = (tmi * 4 + wid) * 65536, va0 = (lane >> 4) * 512 + (lane & 15) * 16;
    asm volatile("" : "+v"(va0));
    asm volatile("" : "+s"(sa0));
    auto lda = [&](int i, int h) { return __builtin_amdgcn_raw_buffer_load_b128(rsa, va0 + (i & 1) * 2048 + h * 256, sa0 + (i >> 1) * 4096, LAUX); };
    unsigned wad[4], tw[4];
    f32x4 acc[2][4];
    const int k = (lane >> 5) * 16 + ((lane >> 2) & 7);
#pragma unroll
    for (int j = 0; j < 4; ++j) {
      wad[j] = lbase + k * 128 + (((2 * j) ^ (k & 6)) << 4) + 8 * (lane & 3);
      tw[j] = tb + wid * 4096 + (lane & 15) * 128 + (((2 * j + (lane >> 5)) ^ (lane & 7)) << 4) + ((lane >> 4) & 1) * 8;
      asm volatile("" : "+v"(wad[j]), "+v"(tw[j]));
    }
    auto ldw_ = [&](int i, int j) {
      const unsigned a = wad[j] + (i & 3) * 4096;
      s16x4 lo = __builtin_amdgcn_ds_read_tr16_b64_v4i16(LDS_AT(s16x4, a));
      s16x4 h4 = __builtin_amdgcn_ds_read_tr16_b64_v4i16(LDS_AT(s16x4, a + 1024));
      return (s16x8)__builtin_shufflevector(lo, h4, 0, 1, 2, 3, 4, 5, 6, 7);
    };
    u32x4 id[2];
#pragma unroll
    for (int j = 0; j < 2; ++j) {
#pragma unroll
      for (int e = 0; e < 4; ++e) id[j][e] = (lane >> 4) == 2 * j + ((lane >> 3) & 1) && ((lane & 7) >> 1) == e ? ((lane & 1) ? 0x3f800000u : 0x3f80u) : 0u;
      asm volatile("" : "+v"(id[j]));
    }
    unsigned rad = tb + wid * 4096 + (lane & 15) * 128 + (((lane >> 4) ^ (lane & 7)) << 4), rfa = flg + 36 + wid;
    asm volatile("" : "+v"(rad), "+v"(rfa));
    u32x4 rb[2][2];
    rwr = *reinterpret_cast<const u32x4*>(w_r + col);
    setup();
    asm volatile("" : "+v"(rwr));
    announce();
    u32x4 af[PD][2];
    s16x8 wf[2][4];
#pragma unroll
    for (int i = 0; i < PD; ++i) { af[i][0] = lda(i, 0); af[i][1] = lda(i, 1); }
    PIN();
    __builtin_amdgcn_s_setprio(OPRIO);
#pragma unroll
    for (int q = 0; q < 4; ++q) rb[q >> 1][q & 1] = lds_rd4((rad ^ ((q & 1) * 64)) + (q >> 1) * 2048);
    PIN();
#pragma unroll
    for (int j = 0; j < 4; ++j)
#pragma unroll
      for (int h = 0; h < 2; ++h) { asm volatile("v_mfma_f32_16x16x32_bf16 %0, %1, %2, 0" : "=&v"(acc[h][j]) : "v"(id[j & 1]), "v"(rb[h][j >> 1])); PIN(); }
#pragma unroll 1
    for (int spin = 0; spin < SPIN_MAX; ++spin)
      if (__builtin_amdgcn_readfirstlane(*LDS_AT(const volatile unsigned, flg)) >= 40u) break;
    asm volatile("" : "+v"(va0) :: "memory");
    PIN();
#pragma unroll
    for (int j = 0; j < 4; ++j) wf[0][j] = ldw_(0, j);
    s16x4 wlo;
    static_for<0, 32>([&](auto ic) __attribute__((always_inline)) {
      constexpr int i = decltype(ic)::value;
      if (i == SIGAT) { __hip_atomic_fetch_add(LDS_AT(unsigned, oad + 4), 1u, __ATOMIC_RELAXED, __HIP_MEMORY_SCOPE_WORKGROUP); PIN(); }
#pragma unroll
      for (int g = 0; g < 8; ++g) {
        const int j = g >> 1, h = g & 1;
        asm volatile("v_mfma_f32_16x16x32_bf16 %0, %1, %2, %0" : "+v"(acc[h][j]) : "v"(wf[i & 1][j]), "v"(af[i % PD][h]));
        PIN();
        if (i == 15 && !(g & 1)) { wad[g >> 1] += 65536; asm volatile("" : "+v"(wad[g >> 1])); }
        if (i + 1 < 32) {
          const s16x4 v = __builtin_amdgcn_ds_read_tr16_b64_v4i16(LDS_AT(s16x4, wad[g >> 1] + ((i + 1) & 15) * 4096 + (g & 1) * 1024));
          if (g & 1) wf[(i + 1) & 1][g >> 1] = (s16x8)__builtin_shufflevector(wlo, v, 0, 1, 2, 3, 4, 5, 6, 7);
          else wlo = v;
          PIN();
        }
        if (g >= 6 && i + PD < 32) { af[i % PD][h] = lda(i + PD, h); PIN(); }
        if (i + 1 < 32 && i != SIGAT && i + 1 != SIGAT) {
          if (g == 3) { __builtin_amdgcn_s_waitcnt(0xc47f); PIN(); }
          if (g == 7 && i + PD < 32) { __builtin_amdgcn_s_waitcnt(0x0470 | (2 * PD - 2)); PIN(); }
          if (g == 7 && i + PD >= 32) { __builtin_amdgcn_s_waitcnt(0xc47f); PIN(); }
        }
      }
    });
    asm volatile("s_nop 3" : "+v"(acc[0][0]), "+v"(acc[1][0]));
    TE(4, 1); TE(7, 2);
    if (wid == 0) STAMP(dk, 17);
    if (wid == 1 && !RES2) STAMP(4, 0);
#pragma unroll
    for (int j = 0; j < 4; ++j)
#pragma unroll
      for (int h = 0; h < 2; ++h) {
        *LDS_AT(u32x2, tw[j] + h * 2048) = u32x2{pack2(acc[h][j][0], acc[h][j][1]), pack2(acc[h][j][2], acc[h][j][3])};
        PIN();
      }
    asm volatile("" ::: "memory");
    *LDS_AT(volatile unsigned char, rfa) = (unsigned char)2;
#pragma unroll
    for (int p = 0; p < 2; ++p) xres[p] = lds_rd4(tr + p * 1024);
    PIN();
    if (!RES2) {
      const __amdgpu_buffer_rsrc_t rso = __builtin_amdgcn_make_buffer_rsrc(out, 0, S_ * D_ * 2, 0x00020000);
      const __amdgpu_buffer_rsrc_t rsf = __builtin_amdgcn_make_buffer_rsrc(xsig + wid, 0, RX * 512, 0x00020000);
      const u32x4 x2 = lds_rd4(tr + 2048), x3 = lds_rd4(tr + 3072);
      PIN();
#pragma unroll
      for (int p = 0; p < 4; ++p) { __builtin_amdgcn_raw_buffer_store_b128(p < 2 ? xres[p] : (p < 3 ? x2 : x3), rso, xo + p * (8 * ldw * 2), 0, XAUX); PIN(); }
      TE(4, 2); TE(5, 0);
      const float s0 = sums(xres[0], xres[1]), s1 = sums(x2, x3);
      if ((lane & 3) != 3) { __builtin_amdgcn_global_atomic_fadd_f64(pst, (double)s0); __builtin_amdgcn_global_atomic_fadd_f64(pst + 16, (double)s1); }
      PIN();
      TE(4, 3); TE(5, 1);
      asm volatile("s_waitcnt vmcnt(2)" ::: "memory");
      __builtin_amdgcn_raw_buffer_store_b32(tag, rsf, lane * 512 + 256, 0, 16);
      *LDS_AT(volatile unsigned char, rfa) = (unsigned char)3;
      PIN();
      TE(5, 2);
      asm volatile("s_waitcnt vmcnt(1)" ::: "memory");
      __builtin_amdgcn_raw_buffer_store_b32(tag, rsf, lane * 512, 0, 16);
      PIN();
      TE(5, 3);
      __builtin_amdgcn_s_setprio(0);
      return;
    }
    TE(4, 2); TE(5, 0);
  } else {
    u32x4 rres[4];
    double is1[4], is2[4];
#pragma unroll
    for (int p = 0; p < 4; ++p)
      rres[p] = __builtin_amdgcn_raw_buffer_load_b128(__builtin_amdgcn_make_buffer_rsrc(const_cast<u16*>(res), 0, S_ * D_ * 2, 0x00020000), ((row - 16 + 8 * p) * ldw + col) * 2, 0, 16);
    rwr = *reinterpret_cast<const u32x4*>(w_r + col);
#pragma unroll
    for (int p = 0; p < 4; ++p) { is1[p] = ld_ag(stats_in + row - 16 + 8 * p); is2[p] = ld_ag(stats_in + S_ + row - 16 + 8 * p); }
    PIN();
    setup();
    if (RES2) {
#pragma unroll
      for (int p = 0; p < 4; ++p) {
        float mean, var, mb, den;
        ln_from_sums(is1[p], is2[p], mean, var, mb, den);
        const float rin = frcp(den);
#pragma unroll
        for (int e = 0; e < 4; ++e) {
          const f32x2 x = {__uint_as_float(rres[p][e] << 16), __uint_as_float(rres[p][e] & 0xffff0000u)};
          const f32x2 h = (x - f32x2{mb, mb}) * f32x2{rin, rin} + x;
          rres[p][e] = pack2(h[0], h[1]);
        }
        asm volatile("" : "+v"(rres[p]));
      }
      PIN();
    }
    if ((idx >> 5) == tmi * 4 + wid - 4) {
      const int dr = idx - (row - 16);
      u32x4 qq;
#pragma unroll 1
      for (int i = 0; i < SPIN_MAX; ++i) {
        qq = __builtin_amdgcn_raw_buffer_load_b128(__builtin_amdgcn_make_buffer_rsrc(hand, 0, HAND_N * 4, 0x00020000), (GRAN_N + Y_N) * 4 + col * 2, 0, 16);
        if (none(umax(umax(qq[0], qq[1]), umax(qq[2], qq[3])) == SENT)) break;
        asm volatile("" ::: "memory");
      }
#pragma unroll
      for (int p = 0; p < 4; ++p)
#pragma unroll
        for (int e = 0; e < 4; ++e) rres[p][e] = dr == 8 * p ? qq[e] : rres[p][e];
    }
#pragma unroll 1
    for (int i = 0; i < 4 * SPIN_MAX; ++i)
      if (__builtin_amdgcn_readfirstlane(*LDS_AT(const volatile unsigned, flg + 16)) >= 12u) break;
#pragma unroll
    for (int p = 0; p < 4; ++p) *LDS_AT(volatile u32x4, tr - 2048 + p * 1024) = rres[p];
    asm volatile("s_waitcnt lgkmcnt(0)" ::: "memory");
    announce();
    TE(4, 1); TE(7, 2);
    asm volatile("s_waitcnt vmcnt(0)" : "+v"(rwr) :: "memory");
    if (!RES2) return;
    __builtin_amdgcn_s_sleep(TPRE);
#pragma unroll 1
    for (int i = 0; i < 4 * SPIN_MAX; ++i) {
      if (__builtin_amdgcn_readfirstlane(*LDS_AT(const volatile unsigned char, flg + 32 + wid)) == 2u) break;
      __builtin_amdgcn_s_sleep(TPSL);
    }
    asm volatile("" ::: "memory");
    __builtin_amdgcn_s_setprio(XPRIO);
    PIN();
    TE(4, 2); TE(5, 0);
#pragma unroll
    for (int p = 0; p < 2; ++p) xres[p] = lds_rd4(tr + p * 1024);
  }
  TE(4, 3); TE(7, 3);
  const float sv = sums(xres[0], xres[1]);
  if (wid == 0) STAMP(dk, 14);
  if ((lane & 3) != 3) __hip_atomic_store(pps, f2word(sv), __ATOMIC_RELAXED, __HIP_MEMORY_SCOPE_AGENT);
  PIN();
  __builtin_amdgcn_s_setprio(0);
  __hip_atomic_fetch_add(LDS_AT(unsigned, oad), 1u, __ATOMIC_RELAXED, __HIP_MEMORY_SCOPE_WORKGROUP);
  TE(5, 1); TE(5, 2);
  PIN();
}

#define MFMA32(a, b, c) __builtin_amdgcn_mfma_f32_32x32x16_bf16(a, b, c, 0, 0, 0)
#define MFMA16(a, b, c) __builtin_amdgcn_mfma_f32_16x16x32_bf16(a, b, c, 0, 0, 0)

#define PLO(x) __builtin_bit_cast(bf16x16, __builtin_shufflevector(x, x, 0, 1, 2, 3, 4, 5, 6, 7))
#define PHI(x) __builtin_bit_cast(bf16x16, __builtin_shufflevector(x, x, 8, 9, 10, 11, 12, 13, 14, 15))
#define SMF32(a, b, c) __builtin_amdgcn_smfmac_f32_32x32x32_bf16(__builtin_bit_cast(bf16x8, a), b, c, sidx, 0, 0)
#define SMF16(a, b, c) __builtin_amdgcn_smfmac_f32_16x16x64_bf16(__builtin_bit_cast(bf16x8, a), b, c, sidx, 0, 0)

__device__ __forceinline__ void patch_kv(unsigned tab, int r, s16x8& k0, s16x8& k1, s16x8& v0, s16x8& v1, bool on = true) {
  const int lane = fresh_lane(), hi = lane >> 5;
  if (on && (lane & 31) == kpos(r)) {
    k0 = *LDS_AT(const s16x8, tab + hi * 16); k1 = *LDS_AT(const s16x8, tab + 32 + hi * 16);
  }
  const short vv = *LDS_AT(const short, tab + 128 + (lane & 31) * 2);
  const int js = (on && hi == ((r >> 2) & 1)) ? (r >> 4) * 8 + ((r >> 3) & 1) * 4 + (r & 3) : -1;
#pragma unroll
  for (int e = 0; e < 8; ++e) { v0[e] = js == e ? vv : v0[e]; v1[e] = js == 8 + e ? vv : v1[e]; }
}

__device__ __forceinline__ void attn_safe(const u16* KV, int tile0, s16x8 q0, s16x8 q1,
                                       f32x16& o, float& l_out, int ptile, int prow, unsigned ptab) {
  const int lane = fresh_lane();
  float m = -INFINITY, l = 0.f;
  const int sidx = 0xDDDDDDDD;
  const __amdgpu_buffer_rsrc_t rs = __builtin_amdgcn_make_buffer_rsrc(const_cast<u16*>(KV), 0, 2 * S_ * D_ * 2, 0x00020000);
#pragma unroll
  for (int e = 0; e < 16; ++e) o[e] = 0.f;
  for (int tt = 0; tt < 64; ++tt) {
    const int t = tile0 + tt, off = t * 2048 + lane * 16;
    s16x8 k0 = __builtin_bit_cast(s16x8, __builtin_amdgcn_raw_buffer_load_b128(rs, off, 0, 16));
    s16x8 k1 = __builtin_bit_cast(s16x8, __builtin_amdgcn_raw_buffer_load_b128(rs, off + 1024, 0, 16));
    s16x8 v0 = __builtin_bit_cast(s16x8, __builtin_amdgcn_raw_buffer_load_b128(rs, off + S_ * D_ * 2, 0, 16));
    s16x8 v1 = __builtin_bit_cast(s16x8, __builtin_amdgcn_raw_buffer_load_b128(rs, off + S_ * D_ * 2 + 1024, 0, 16));
    if (t == ptile) patch_kv(ptab, prow, k0, k1, v0, v1);
    f32x16 sc;
#pragma unroll
    for (int e = 0; e < 16; ++e) sc[e] = 0.f;
    sc = MFMA32(k0, q0, sc); sc = MFMA32(k1, q1, sc);
    float mt = sc[0];
#pragma unroll
    for (int e = 1; e < 16; ++e) mt = fmaxf(mt, sc[e]);
    mt = fmaxf(mt, __shfl_xor(mt, 32, 64));
    const float mn = fmaxf(m, mt);
    const float alpha = __builtin_amdgcn_exp2f(m - mn);
    m = mn;
#pragma unroll
    for (int e = 0; e < 16; ++e) sc[e] = __builtin_amdgcn_exp2f(sc[e] - mn);
    float ls = 0.f;
#pragma unroll
    for (int e = 0; e < 16; ++e) ls += __uint_as_float(__float_as_uint(sc[e]) & 0xffff0000u);
    l = l * alpha + ls;
#pragma unroll
    for (int e = 0; e < 16; ++e) o[e] *= alpha;
    o = SMF32(v0, PLO(sc), o); o = SMF32(v1, PHI(sc), o);
  }
  l_out = l + __shfl_xor(l, 32, 64);
}

__device__ __forceinline__ float wave_fmax(float v) {
  asm volatile("s_nop 1\n\tv_max_f32_dpp %0, %0, %0 row_shr:1 row_mask:0xf bank_mask:0xf\n\ts_nop 1\n\tv_max_f32_dpp %0, %0, %0 row_shr:2 row_mask:0xf bank_mask:0xf\n\t"
               "s_nop 1\n\tv_max_f32_dpp %0, %0, %0 row_shr:4 row_mask:0xf bank_mask:0xf\n\ts_nop 1\n\tv_max_f32_dpp %0, %0, %0 row_shr:8 row_mask:0xf bank_mask:0xf\n\t"
               "s_nop 1\n\tv_max_f32_dpp %0, %0, %0 row_bcast:15 row_mask:0xa bank_mask:0xf\n\ts_nop 1\n\tv_max_f32_dpp %0, %0, %0 row_bcast:31 row_mask:0xc bank_mask:0xf\n\ts_nop 0"
               : "+v"(v));
  return __int_as_float(__builtin_amdgcn_readlane(__float_as_int(v), 63));
}

#define PSLP 8
#define WSLP 3
#define PRE1 8
#define PRE5 8
#define W3SLP 4
#define FSTART 10
#define OAUX 16
#define OMX 4
#define T0 2u
#define TW 10u
#define HW 2
#define HP1 1
#define POLLAT 11
#define X2AT 2
#define HPRIO 3
#define SSLP 1
#define SPRE 6
#define WGAPS {0, 1, 7, 8, 9, 10, 11, 12}
#define KVS 4
#define PNS 12
#define G0 5u
#define GS 2u
#define IAT 1u
#define YSLP 2
#define ISLP 8
#define KSLP 2
#define UAT 13u
#define UHW 0
#define PPSLP 2
#define XRSLP 2
#define MPRE 20
#define YD1 150
#define YD2 180
#define KDT 200
#define XDT 45
#define R2PRIO 0
#define LNPRIO 1
#define MSLP 4
#define BARG 0
struct Args {
  long long lay;
  const u16* a; u16* kv; u16* o; u16* xo; double* so;
  const u16* W; u16* wc;
  const u16 *wg, *wu, *wd, *wo, *w_r;
  unsigned* gran; float* csf; double* stats; unsigned* hand; u16* out;
};
typedef __attribute__((address_space(4))) const Args* ArgP;
#define WCAT 3
#define CAP0 1
#define PRE4 50
#define PRE7 20
#define UARR 12u
#define GF0 10
#define XGATE 8
#define ASLP 1
#define B15 3
#define KO 5
#define PRO 2
#define HS 2
#define XD2 90
#define TFL 4u
#define WMAP 0x32100333
#define SMAP 0
#define STCH 6
#define KPOLL 10
#define TPRIO 2
#define TSLP 9
__device__ __forceinline__ bool slow_cols(const u16* W, int tni) { return ((((unsigned)(uintptr_t)W >> 7) + tni) & 7) == 3; }
#define ARGS_HERE                                                                                                      \
  ArgP ap_ = ap; asm volatile("" : "+s"(ap_));                                                                          \
  const u16* A = ap_->a; const u16* __restrict__ W = ap_->W; u16* KV = ap_->kv; u16* Of = ap_->o;        \
  const u16* __restrict__ wg = ap_->wg; const u16* __restrict__ wu = ap_->wu; const u16* __restrict__ wd = ap_->wd;       \
  const u16* __restrict__ wo = ap_->wo; const u16* __restrict__ w_r = ap_->w_r; unsigned* gran = ap_->gran;              \
  float* csf = ap_->csf; const double* stats = ap_->stats; double* stats_out = ap_->so; unsigned* hand = ap_->hand; \
  u16* xout = ap_->xo; u16* __restrict__ out = ap_->out
#define AGAIN "s_getpc_b64 s[90:91]\n\ts_add_u32 s90, s90, layers_kernel@rel32@lo+4\n\ts_addc_u32 s91, s91, layers_kernel@rel32@hi+12\n\ts_setpc_b64 s[90:91]"
#define KEPT(c)                                                                                                        \
  c "{v[104:107]}"(gu[0]), c "{v[108:111]}"(gu[1]), c "{v[112:115]}"(gu[2]), c "{v[116:119]}"(gu[3]), c "{v[120:123]}"(gu[4]),      \
  c "{v[124:127]}"(gu[5]), c "{v[128:131]}"(gu[6]), c "{v[132:135]}"(gu[7]), c "{v[136:139]}"(gu[8]), c "{v[140:143]}"(gu[9]),      \
  c "{v[144:147]}"(gu[10]), c "{v[148:151]}"(gu[11]), c "{v[152:155]}"(gu[12]), c "{v[156:159]}"(gu[13]), c "{v[160:163]}"(gu[14]), \
  c "{v[164:167]}"(gu[15])
#define LAYER_HERE ArgP apl_ = ap; asm volatile("" : "+s"(apl_)); const bool LAY2 = apl_->lay != 0
__device__ __forceinline__ void layer_body(const int wid, const int bid, const unsigned lbase, const ArgP ap, const unsigned long long t_entry) {
  constexpr int K = 1024, NKT = 16, NST = 3, ABYTES = 16384, WBYTES = 24576, STB = ABYTES + WBYTES, ldw = 3 * D_;
  constexpr int TABA = NST * STB, ALPHA = TABA + 2048, CRED = ALPHA + 512;
  constexpr int ASB = 16384, FLG = 8 * ASB + 256;
  constexpr int DUM = FLG + 64, TAB = DUM + 256, RED = TAB + 512, SACT = RED + 4096, XML = SACT + 3584, UGL = XML + 4096, SPM = UGL + 1024, QOFF = 9 * ASB;
  constexpr int TABW = RED;
  static_assert(SPM + 2048 <= QOFF && CRED <= 8 * ASB && RED + 8192 <= QOFF && RED % 16 == 0, "lds");
  const int xcd = (bid & 1) * 4 + ((bid >> 1) & 3), loc = bid >> 3;
  const int tmi = (xcd / QNG) * QMX + loc / QNX, tni = (xcd % QNG) * QNX + loc % QNX;
  const int m0 = tmi * 128, n0 = tni * 64;
  const int omi = (xcd / (OMX / 2)) * OMX + loc / (32 / OMX), oni = (xcd % (OMX / 2)) * (32 / OMX) + loc % (32 / OMX);
  constexpr unsigned VOFF = (unsigned)S_ * D_ * 2;
  auto tile_at = [&](unsigned n) __attribute__((always_inline)) {
#pragma unroll 1
    for (int i = 0; i < 4 * SPIN_MAX; ++i) {
      if (__builtin_amdgcn_readfirstlane(*LDS_AT(const volatile unsigned, lbase + FLG + 36)) == n * 0x01010101u) break;
      __builtin_amdgcn_s_sleep(1);
    }
    asm volatile("" ::: "memory");
    PIN();
  };
  auto all_ge = [&](unsigned w, unsigned n) { return ((w + (128u - n) * 0x01010101u) & 0x80808080u) == 0x80808080u; };
  if (wid >= 8) {
    u32x4 gu[16];
    LAYER_HERE;
    constexpr int dk = 6;
    const int hw = wid - 8, b = bid;
    int nb = 0;
    constexpr int lim = NKT - 1;
    auto tick_nw = [&]() {
      PIN();
      if (nb < lim) { __builtin_amdgcn_s_barrier(); ++nb; } else __builtin_amdgcn_s_sleep(2);
      asm volatile("" ::: "memory");
      PIN();
    };
    auto tick = [&]() {
      asm volatile("s_waitcnt lgkmcnt(0)" ::: "memory");
      tick_nw();
    };
    auto fold16 = [&](f32x2 y0, f32x2 y1) __attribute__((always_inline)) {
      const auto q0 = __builtin_amdgcn_permlane32_swap(__float_as_uint(y0[0]), __float_as_uint(y1[0]), false, false);
      const auto q1 = __builtin_amdgcn_permlane32_swap(__float_as_uint(y0[1]), __float_as_uint(y1[1]), false, false);
      const auto q = __builtin_amdgcn_permlane16_swap(__float_as_uint(__uint_as_float(q0[0]) + __uint_as_float(q0[1])),
                                                      __float_as_uint(__uint_as_float(q1[0]) + __uint_as_float(q1[1])), false, false);
      return __uint_as_float(q[0]) + __uint_as_float(q[1]);
    };
    unsigned best, tag, rmd;
    float alpha, m, wsum;
    u32x4 tw;
    u32x2 xq;
    s16x8 sl[8];
    const int kcap = CAP0 + (tmi & 7);
    if (!LAY2) {
      ARGS_HERE;
      const int lane = fresh_lane(), t = hw * 64 + lane;
      const unsigned ep = ld_ag(gran);
      const int srow = m0 + tni * 8 + hw * 2;
      u32x4 xa[2][2], wv[8];
      const int wr_ = (3 - ((unsigned)(uintptr_t)W >> 7) - (tni & ~3)) & 7;
      const bool cpy = hw == 3 && wr_ < 4 && wr_ != (tni & 3), wld = hw < 3 ? wr_ != (tni & 3) : cpy;
      const int wcn = cpy ? (tni & ~3) + wr_ : tni, wcp = cpy ? (tni - wr_ - 1) & 3 : hw;
      const int wof = ((tmi * 64 + (lane >> 3)) * ldw + wcp * D_ + wcn * 64 + (lane & 7) * 8) * 2;
      auto ld_w = [&]() {
        const __amdgpu_buffer_rsrc_t rw = __builtin_amdgcn_make_buffer_rsrc(const_cast<u16*>(W), 0, wld ? D_ * ldw * 2 : 0, 0x00020000);
#pragma unroll
        for (int r = 0; r < 8; ++r) wv[r] = __builtin_amdgcn_raw_buffer_load_b128(rw, wof, r * 8 * ldw * 2, 0);
        PIN();
      };
      auto wcopy = [&]() __attribute__((always_inline)) {
        if (cpy) {
          const __amdgpu_buffer_rsrc_t rw = __builtin_amdgcn_make_buffer_rsrc(ap_->wc, 0, D_ * ldw * 2, 0x00020000);
#pragma unroll
          for (int r = 0; r < 8; ++r) __builtin_amdgcn_raw_buffer_store_b128(wv[r], rw, wof + r * 8 * ldw * 2, 0, 16);
        }
        PIN();
      };
      const int wat = ((hw == 3 ? SMAP : WMAP) >> (4 * (tmi & 7))) & 3;
      unsigned ps = 0u;
      auto stouch = [&](int at) __attribute__((always_inline)) {
        if (at == STCH && cpy) ps = __builtin_amdgcn_raw_buffer_load_b32(__builtin_amdgcn_make_buffer_rsrc(const_cast<u16*>(W), 0, D_ * ldw * 2, 0x00020000),
                                                                        (((tmi ^ 8) * 64 + lane) * ldw + wcp * D_ + wcn * 64) * 2, 0, 0);
        PIN();
      };
      auto ld_x = [&](int h) {
#pragma unroll
        for (int r = 0; r < 2; ++r) xa[r][h] = *reinterpret_cast<const u32x4*>(A + (size_t)(srow + r) * D_ + (h ^ (tmi >> 3)) * 512 + lane * 8);
        PIN();
      };
      ld_x(0);
      if (X2AT == 0) ld_x(1);
      if (wat == 0) ld_w();
      unsigned pc = 0u;
      if (hw == 3) {
        unsigned long long a0, a1;
        CODE_ADDR("layers_kernel", a0);
        CODE_ADDR("end_marker_kernel", a1);
        pc = touch_code(a0, a1, loc, lane);
      }
      if (hw == 1) pc = ld_ag(gran + ((lane & 1) ? GR_ROW + (m0 + tni * 8) * 4 : GR_KV + (tni * RK + tmi % RK) * 32));
      if (hw == 2) st_ag(gran + GR_N + bid * 64 + lane, lane + 1u);
      {
        const int gi = bid * 256 + t;
        if (gi < GRAN_N / 4) __builtin_amdgcn_raw_buffer_store_b128(u32x4{SENT, SENT, SENT, SENT}, __builtin_amdgcn_make_buffer_rsrc(hand, 0, GRAN_N * 4, 0x00020000), gi * 16, 0, 16);
        if (t < 24) __hip_atomic_store(stats_out + (t >> 3) * S_ + m0 + tni * 8 + (t & 7), 0.0, __ATOMIC_RELAXED, __HIP_MEMORY_SCOPE_AGENT);
      }
      tick();
      if (wat == 1) ld_w();
      if (X2AT == 1) ld_x(1);
      tick();
      if (wat == 2) ld_w();
      if (WCAT == 2) wcopy();
      if (X2AT == 2) ld_x(1);
      asm volatile("" :: "v"(ep));
      tick();
      if (wat == 3) ld_w();
      if (WCAT == 3) wcopy();
      if (X2AT == 3) ld_x(1);
      stouch(3);
#pragma unroll
      for (int i = 0; i < HW; ++i) { tick(); stouch(4 + i); }
      __builtin_amdgcn_s_setprio(HP1);
      PIN();
      tag = ep * 1664525u + 1013904223u;
      const __amdgpu_buffer_rsrc_t rsc = __builtin_amdgcn_make_buffer_rsrc(gran, 0, GR_N * 4, 0x00020000);
      {
        u32x4 od;
#pragma unroll
        for (int q = 0; q < 4; ++q) od[q] = ((lane & 7) >> 1) == q ? ((lane & 1) ? 0x3f800000u : 0x00003f80u) : 0u;
        f32x4 d[2] = {{0.f, 0.f, 0.f, 0.f}, {0.f, 0.f, 0.f, 0.f}};
#pragma unroll
        for (int r = 0; r < 8; ++r) d[r & 1] = __builtin_amdgcn_mfma_f32_16x16x32_bf16(__builtin_bit_cast(s16x8, od), __builtin_bit_cast(s16x8, wv[r]), d[r & 1], 0, 0, 0);
        u32x4 gr;
#pragma unroll
        for (int q = 0; q < 4; ++q) gr[q] = ((__float_as_uint(dpp_add<0x128, 0xf>(fadd(d[0][q], d[1][q]))) + 0x80u) & ~0xffu) | ((tag >> (8 * q)) & 0xffu);
        asm volatile("s_waitcnt vmcnt(0)" ::: "memory");
        __builtin_amdgcn_raw_buffer_store_b128(gr, rsc, (lane & 40) || !wld ? GR_N * 4 : (GR_COL + (wcn * 16 + tmi) * 192 + wcp * 64 + (lane & 7) * 8 + (lane >> 4) * 4) * 4, 0, 16);
      }
      asm volatile("" :: "v"(pc));
      tick_nw();
      stouch(6);
      {
        float xv[2][16], mean[2];
#pragma unroll
        for (int r = 0; r < 2; ++r) {
          float sm = 0.f;
#pragma unroll
          for (int i = 0; i < 4; ++i) {
            xv[r][2 * i] = __uint_as_float(xa[r][0][i] << 16); xv[r][2 * i + 1] = __uint_as_float(xa[r][0][i] & 0xffff0000u);
            xv[r][8 + 2 * i] = __uint_as_float(xa[r][1][i] << 16); xv[r][8 + 2 * i + 1] = __uint_as_float(xa[r][1][i] & 0xffff0000u);
          }
#pragma unroll
          for (int i = 0; i < 16; ++i) sm += xv[r][i];
          mean[r] = wave_sum(sm) * (1.0f / 1024.0f);
          tick();
          stouch(7 + r);
        }
        float vr[2];
#pragma unroll
        for (int r = 0; r < 2; ++r) {
          float v = 0.f;
#pragma unroll
          for (int i = 0; i < 16; ++i) { const float d = xv[r][i] - mean[r]; v += d * d; }
          vr[r] = wave_sum(v) * (1.0f / 1024.0f);
          if (r == 0) tick();
        }
        const float vv = lane & 2 ? vr[1] : vr[0], mm = lane & 2 ? mean[1] : mean[0];
        const float rin = 1.0f / rbf(sqrtf(rbf(rbf(vv) + 1e-5f)));
        if (lane < 4) __builtin_amdgcn_raw_buffer_store_b64(u32x2{__float_as_uint((lane & 1) ? rbf(mm) : rin), tag}, rsc, (GR_ROW + srow * 4 + lane * 2) * 4, 0, 16);
      }
      tick();
      float cs;
      while (nb < POLLAT) tick();
      asm volatile("" :: "v"(ps));
#pragma unroll 1
      for (int spin = 0;; ++spin) {
        u32x4 csv[4];
        const __amdgpu_buffer_rsrc_t rsw = __builtin_amdgcn_make_buffer_rsrc(gran, 0, hw < 3 ? GR_N * 4 : 0, 0x00020000);
#pragma unroll
        for (int i = 0; i < 4; ++i) csv[i] = __builtin_amdgcn_raw_buffer_load_b128(rsw, (GR_COL + (tni * 16 + (lane >> 4)) * 192 + hw * 64 + (lane & 15) * 4) * 4, i * (4 * 192 * 4), 16);
        const u32x4 rp = __builtin_amdgcn_raw_buffer_load_b128(rsc, (GR_ROW + (m0 + (t & 127)) * 4) * 4, 0, 16);
        PIN();
        tick(); tick();
        unsigned bad = 0u;
        float z[4];
#pragma unroll
        for (int q = 0; q < 4; ++q) {
          const unsigned tq = tag >> (8 * q);
          bad |= ((csv[0][q] ^ tq) | (csv[1][q] ^ tq)) | ((csv[2][q] ^ tq) | (csv[3][q] ^ tq));
          z[q] = fadd(fadd(__uint_as_float(csv[0][q] & ~0xffu), __uint_as_float(csv[1][q] & ~0xffu)), fadd(__uint_as_float(csv[2][q] & ~0xffu), __uint_as_float(csv[3][q] & ~0xffu)));
        }
        cs = fold16(f32x2{z[0], z[1]}, f32x2{z[2], z[3]});
        bad = (hw < 3 ? bad & 0xffu : 0u) | (rp[1] ^ tag) | (rp[3] ^ tag);
        alpha = __uint_as_float(rp[0]); m = __uint_as_float(rp[2]);
        if (none(bad != 0u) || spin >= SPIN_MAX) break;
      }
      if (tmi == 0 && hw < 3) st_ag(reinterpret_cast<unsigned*>(csf) + hw * 1024 + n0 + (lane & 15) * 4 + (lane >> 4), __float_as_uint(cs));
      unsigned c0, c1, c2;
      split3(cs, c0, c1, c2);
      tw = u32x4{c0 | (c1 << 16), c0 | (c2 << 16), c1 | (c0 << 16), 0u};
      best = rmd = 0u;
      wsum = 0.f;
      asm volatile("" : "=v"(xq), KEPT("="));
#pragma unroll
      for (int i = 0; i < 8; ++i) asm volatile("" : "=v"(sl[i]));
    } else {
      asm volatile("; kept registers" : KEPT("="));
      PIN();
      ARGS_HERE;
      const int lane = fresh_lane(), t = hw * 64 + lane;
      best = rmd = 0u;
      tag = __builtin_amdgcn_readfirstlane(*LDS_AT(const volatile unsigned, lbase + FLG + 20));
#pragma unroll
      for (int i = 0; i < 8; ++i) asm volatile("" : "=v"(sl[i]));
      unsigned sad;
      auto tickc = [&]() __attribute__((always_inline)) {
        tick();
        if (nb == kcap + 1) {
#pragma unroll
          for (int i = 0; i < 8; ++i) {
            const unsigned a = (sad ^ ((i >> 2) * 64) ^ (((i >> 1) & 1) * 16)) + (i & 1) * 12288;
            sl[i] = __builtin_shufflevector(__builtin_amdgcn_ds_read_tr16_b64_v4i16(LDS_AT(s16x4, a)), __builtin_amdgcn_ds_read_tr16_b64_v4i16(LDS_AT(s16x4, a + 1536)), 0, 1, 2, 3, 4, 5, 6, 7);
          }
        }
        PIN();
      };
      static_assert(CAP0 + 8 <= KPOLL, "the stage is taken before that");
      if (hw == 1) STAMP(4, 4);
      if (hw == 0) {
        __builtin_amdgcn_s_sleep(TPRE);
        tile_at(3u);
        __builtin_amdgcn_s_sleep(XGATE);
        STAMP(4, 3);
#pragma unroll 1
        for (int spin = 0; spin < SPIN_MAX; ++spin) {
          if (none(ld_ag(gran + GR_X + (tmi * RX + tni % RX) * 128 + 64 + lane) != tag)) break;
          __builtin_amdgcn_s_sleep(1);
        }
      }
      static_assert(UHW == 0, "");
      if (hw == 0) asm volatile("s_waitcnt vmcnt(0)" ::: "memory");
      if (hw == 0) STAMP(4, 5);
      asm volatile("" ::: "memory");
      PIN();
      __builtin_amdgcn_s_barrier();
      PIN();
      if (hw == 1) STAMP(4, 6);
      {
        const int ln = fresh_lane(), g4 = ln >> 4, tp = ln & 3, k = 8 * g4 + ((ln & 15) >> 2);
        sad = lbase + (kcap % NST) * STB + ABYTES + k * 384 + (((2 * (tp >> 1)) ^ (4 * ((k >> 1) & 1)) ^ (g4 & 1)) << 4) + 8 * (tp & 1) + (hw % 3) * 128;
      }
      wsum = __uint_as_float(__builtin_amdgcn_readfirstlane(*LDS_AT(const volatile unsigned, lbase + FLG + 60)));
      asm volatile("" : "+v"(sad), "=v"(tw), "=v"(alpha), "=v"(m));
      PIN();
      const int row = m0 + (t & 127);
      const __amdgpu_buffer_rsrc_t rsg = __builtin_amdgcn_make_buffer_rsrc(hand, 0, GRAN_N * 4, 0x00020000);
      if (hw < 2 || tmi == tni) {
      while (nb < HS) tickc();
#pragma unroll 1
      for (int spin = 0; spin < SPIN_MAX; ++spin) {
        const unsigned v = ld_ag(gran + GR_X + (tmi * RX + tni % RX) * 128 + lane);
        tickc();
        if (none(v != tag)) break;
      }
      asm volatile("" ::: "memory");
      PIN();
      const double s1 = ld_ag(stats + row), s2 = ld_ag(stats + S_ + row), sw = ld_ag(stats + 2 * S_ + row);
      tickc();
      float mean, var, mb, den;
      ln_from_sums(s1, s2, mean, var, mb, den);
      const float rin = frcp(den), g1 = 1.0f + rin;
      const float mb2 = rbf(mean + (mean - mb) * rin);
      const float den2 = rbf(__builtin_amdgcn_sqrtf(rbf(rbf(var * g1 * g1) + 1e-5f)));
      alpha = g1 * frcp(den2); m = (mb * rin + mb2) * frcp(g1);
      if (tmi == tni && hw >= 2) {
        const float r = (float)fma(-(double)mb, (double)wsum, sw) * rin;
        const unsigned rb = pack2(r, r);
        unsigned rkey = (rb ^ ((unsigned)((int)rb >> 31) | 0x80000000u)) & 0xffff0000u;
        if (r != r) rkey = 0u;
        rkey = wave_umax(rkey | (unsigned)(2047 - row));
        const unsigned md = __builtin_amdgcn_readlane(f2word(__uint_as_float((__float_as_uint(mb) >> 16) | (__float_as_uint(den) & 0xffff0000u))), (2047 - (int)(rkey & 0xffffu)) & 63);
        __builtin_amdgcn_raw_buffer_store_b64(u32x2{rkey, md}, rsg, lane < NREP ? lane * 256 + (tmi * 2 + hw - 2) * 8 : GRAN_N * 4, 0, 16);
      }
      }
      while (nb < KPOLL) tickc();
#pragma unroll 1
      for (int spin = 0; spin < SPIN_MAX; ++spin) {
        const u32x2 v = __builtin_amdgcn_raw_buffer_load_b64(rsg, (bid % NREP) * 256 + (lane & 31) * 8, 0, 16);
        tick(); tick();
        if (none(umax(v[0], v[1]) == SENT)) {
          best = wave_umax(v[0]);
          rmd = __builtin_amdgcn_readlane(v[1], ((2047 - (int)(best & 0xffffu)) >> 6) & 31);
          break;
        }
      }
      const int ix = (2047 - (int)(best & 0xffffu)) & 2047;
      if (row == ix) alpha = 0.f;
      xq = __builtin_amdgcn_raw_buffer_load_b64(__builtin_amdgcn_make_buffer_rsrc(const_cast<u16*>(A), 0, S_ * D_ * 2, 0x00020000), (ix * D_ + hw * 256 + (lane & 3) * 64 + (lane >> 2) * 4) * 2, 0, 16);
      PIN();
    }
    tag = __builtin_amdgcn_readfirstlane(tag);
    wsum = __uint_as_float(__builtin_amdgcn_readfirstlane(__float_as_uint(wsum)));
    const int lane = fresh_lane(), t = hw * 64 + lane;
    {
      unsigned a0, a1, a2;
      split3(-m, a0, a1, a2);
      u32x4 ta = {a0 | (a0 << 16), a1 | (a0 << 16), a1 | (a2 << 16), 0u};
      asm volatile("" : "+v"(tw), "+v"(ta), "+v"(alpha));
      __builtin_amdgcn_s_setprio(TPRIO);
      if (nb < lim) {
        while (nb < lim) tick();
        __builtin_amdgcn_s_sleep(TSLP);
      }
      if (!LAY2 && hw < 3) *LDS_AT(u32x4, lbase + TABW + (hw * 64 + (lane & 15) * 4 + (lane >> 4)) * 16) = tw;
      if (t < 128) {
        *LDS_AT(u32x4, lbase + TABA + t * 16) = ta;
        *LDS_AT(float, lbase + ALPHA + t * 4) = alpha;
      }
    }
    if (t >= 192 && t < 196) *LDS_AT(u32x4, lbase + CRED + ((t >> 1) & 1) * 512 + (t & 1) * 128) = u32x4{0u, 0u, 0u, 0u};
    asm volatile("s_waitcnt lgkmcnt(0)" ::: "memory");
    PIN();
    __builtin_amdgcn_s_barrier();
    PIN();
    *LDS_AT(u32x4, lbase + XML + t * 16) = u32x4{SENT, SENT, SENT, SENT};
    *LDS_AT(u32x4, lbase + SPM + (t & 127) * 16) = u32x4{SENT, SENT, SENT, SENT};
    if (hw == 0) {
      unsigned v;
      asm volatile("v_mov_b32 %0, 0\n\tv_writelane_b32 %0, 2, 0\n\tv_writelane_b32 %0, %1, 5\n\tv_writelane_b32 %0, %2, 7"
                   : "=&v"(v) : "s"(tag), "s"(best));
      lds_wr(lbase + FLG + lane * 4, v);
    }
    const int idx = LAY2 ? (2047 - (int)(best & 0xffffu)) & 2047 : -64;
    const bool p1blk = (oni & 7) != 7, fast = tmi < 8, own = fast && (loc & 1);
    const int fg = xcd * 28 + (p1blk ? loc - (loc >> 3) : 0), ow = (xcd & 3) * 16 + (loc >> 1);
    static_assert(OMX == 4, "oni % 8 = loc % 8");
#define GLD(a, b) "global_load_dwordx4 v[" #a ":" #b "], %0, off\n\tv_lshl_add_u64 %0, %0, 0, %1\n\t"
#define GLD4(...) asm volatile(__VA_ARGS__ : "+v"(p) : "s"((unsigned long long)(F_ * 2)), KEPT() : "memory")
    auto gptr = [&](const u16* wg, const u16* wu) {
      const int ln = fresh_lane();
      return ((ln & 2) ? wu : wg) + (size_t)(hw * 256 + (ln >> 2) * 4) * F_ + fg * 16 + (ln & 1) * 8;
    };
    auto gather = [&](const u16*& p, int e) __attribute__((always_inline)) {
      if (e == 0) GLD4(GLD(104, 107) GLD(108, 111) GLD(112, 115) GLD(116, 119));
      else if (e == 1) GLD4(GLD(120, 123) GLD(124, 127) GLD(128, 131) GLD(132, 135));
      else if (e == 2) GLD4(GLD(136, 139) GLD(140, 143) GLD(144, 147) GLD(148, 151));
      else GLD4(GLD(152, 155) GLD(156, 159) GLD(160, 163) GLD(164, 167));
      p += 60 * F_;
    };
    auto dmask = [&](int q) { const int ln = fresh_lane(); return ((ln >> 1) & 1) == q ? ((ln & 1) ? 0xffff0000u : 0x0000ffffu) : 0u; };
    unsigned dm0, dm1, dad;
    auto gu_setup = [&]() __attribute__((always_inline)) {
      const int ln = fresh_lane();
      dm0 = dmask(0); dm1 = dmask(1);
      dad = lbase + SACT + ((ln & 3) * 8 + (ln >> 5) * 4 + ((ln >> 4) & 1)) * 16 + hw * 4;
      asm volatile("" : "+v"(dm0), "+v"(dm1), "+v"(dad));
    };
    auto gate_up = [&](u32x2 x, unsigned md) __attribute__((always_inline)) {
      const unsigned rin = __builtin_amdgcn_readfirstlane(__float_as_uint(frcp(__uint_as_float(md & 0xffff0000u))));
      asm volatile("s_getpc_b64 s[90:91]\n\ts_add_u32 s90, s90, gate_up_code@rel32@lo+4\n\ts_addc_u32 s91, s91, gate_up_code@rel32@hi+12\n\ts_swappc_b64 s[92:93], s[90:91]"
                   : "+{v[100:101]}"(x) : "{s84}"(md << 16), "{s85}"(md & 0xffff0000u), "{s86}"(rin), "{v99}"(dad), "{v102}"(dm0), "{v103}"(dm1), KEPT()
                   : "v84", "v85", "v86", "v87", "v88", "v89", "v90", "v91", "v92", "v93", "v94", "v95", "v96", "v97", "s88", "s89", "s90", "s91", "s92", "s93", "scc", "memory");
    };
    auto act_diag = [&](int so = 0) __attribute__((always_inline)) {
      const int ln = fresh_lane();
      const f32x4 g4 = *LDS_AT(const volatile f32x4, lbase + SACT + so + (ln >> 2) * 16), u4 = *LDS_AT(const volatile f32x4, lbase + SACT + so + 256 + (ln >> 2) * 16);
      float gv = fadd(fadd(g4[0], g4[1]), fadd(g4[2], g4[3])), uv = fadd(fadd(u4[0], u4[1]), fadd(u4[2], u4[3]));
      rbf2(gv, uv);
      const float av = rbf(gv * frcp(1.0f + __expf(-gv))) * uv;
      const unsigned ap2 = pack2(av, av);
      return __builtin_bit_cast(s16x4, u32x2{ap2 & dm0, ap2 & dm1});
    };
    auto down = [&](const s16x4& a, const u32x2 (&w)[16]) __attribute__((always_inline)) {
      f32x4 d[2] = {{0.f, 0.f, 0.f, 0.f}, {0.f, 0.f, 0.f, 0.f}};
      static_for<0, 16>([&](auto fc) __attribute__((always_inline)) {
        constexpr int f = decltype(fc)::value;
        d[f & 1] = __builtin_amdgcn_mfma_f32_4x4x4bf16_1k(a, __builtin_bit_cast(s16x4, w[f]), d[f & 1], 4, f, 0);
      });
      f32x4 z;
#pragma unroll
      for (int e = 0; e < 4; ++e) { z[e] = d[0][e] + d[1][e]; asm volatile("" : "+v"(z[e])); }
      return u32x4{f2word(z[0]), f2word(z[1]), f2word(z[2]), f2word(z[3])};
    };
    auto fold8 = [&](f32x2 y0, f32x2 y1) __attribute__((always_inline)) { return dpp_add<0x128, 0xf>(fold16(y0, y1)); };
    auto fold4 = [&](f32x2 y0, f32x2 y1) __attribute__((always_inline)) { return dpp_add<0x124, 0xf>(fold8(y0, y1)); };
    auto y_sum = [&](const __amdgpu_buffer_rsrc_t rs, auto nbc) __attribute__((always_inline)) {
      constexpr int NB = decltype(nbc)::value, N = 14 / NB;
      const int ln = fresh_lane(), src = (GRAN_N + (ln >> 2) * D_ + ow * 16 + (ln & 3) * 4) * 4;
      f32x2 y0 = {0.f, 0.f}, y1 = {0.f, 0.f};
#pragma unroll 1
      for (int bt = 0; bt < NB; ++bt) {
        f32x2 z0, z1;
#pragma unroll 1
        for (int spin = 0; spin < SPIN_MAX; ++spin) {
          u32x4 v[N];
#pragma unroll
          for (int i = 0; i < N; ++i) v[i] = __builtin_amdgcn_raw_buffer_load_b128(rs, src, (bt * N + i) * (16 * D_ * 4), 16);
          z0 = y0; z1 = y1;
#pragma unroll
          for (int i = 0; i < N; ++i) {
            z0 = f32x2{fadd(z0[0], __uint_as_float(v[i][0])), fadd(z0[1], __uint_as_float(v[i][1]))};
            z1 = f32x2{fadd(z1[0], __uint_as_float(v[i][2])), fadd(z1[1], __uint_as_float(v[i][3]))};
          }
          const float chk = fadd(fadd(z0[0], z0[1]), fadd(z1[0], z1[1]));
          if (none(chk != chk)) break;
          unsigned mx = 0u;
#pragma unroll
          for (int i = 0; i < N; ++i) mx = umax(umax(umax(mx, v[i][0]), v[i][1]), umax(v[i][2], v[i][3]));
          if (none(mx == SENT)) break;
          __builtin_amdgcn_s_sleep(YSLP);
          asm volatile("" ::: "memory");
        }
        y0 = z0; y1 = z1;
      }
      return fold4(y0, y1);
    };
    [&]() __attribute__((always_inline)) {
    ARGS_HERE;
    const unsigned l16 = lane * 16;
    unsigned* kvflag = gran + GR_KV + ((LAY2 ? 16 : 0) + tni) * (32 * RK);
    auto wait_sig = [&](int sh) {
#pragma unroll 1
      for (int i = 0; i < 4 * SPIN_MAX; ++i) {
        if (((__builtin_amdgcn_readfirstlane(*LDS_AT(const volatile unsigned, lbase + FLG + 32)) >> sh) & 7u) >= 4u) break;
        __builtin_amdgcn_s_sleep(SSLP);
      }
      asm volatile("" ::: "memory");
      PIN();
    };
    PIN();
    asm volatile("s_waitcnt lgkmcnt(0)" ::: "memory");
    if (!LAY2) asm volatile("s_waitcnt vmcnt(0)" ::: "memory");
    __builtin_amdgcn_s_barrier();
    PIN();
    __builtin_amdgcn_s_setprio(HPRIO);
    if (hw < 3) {
      __builtin_amdgcn_s_sleep(SPRE);
      wait_sig(0);
      __builtin_amdgcn_raw_buffer_store_b32(tag, __builtin_amdgcn_make_buffer_rsrc(kvflag, 0, hw == 0 ? RK * 128 : 0, 0x00020000), lane * 128 + tmi * 4, 0, 16);
      if (hw == 0) STAMP(LAY2 ? 5 : 1, 16);
    }
    if (hw == 3) {
      __builtin_amdgcn_s_sleep(KVS);
      const unsigned kv0 = ld_ag(kvflag + (tmi % RK) * 32 + (lane & 15));
      PIN();
      const char* cnt = reinterpret_cast<const char*>(gran + GR_N + bid * 64 + 2);
      unsigned vk[8], vp = lbase + FLG + 8 + (lane & 7), pw = 0u;
#pragma unroll
      for (int j = 0; j < 8; ++j) { vk[j] = l16 + (j >> 2) * 131072u + ((j >> 1) & 1) * 4096u + (j & 1) * (VOFF - 2048u); asm volatile("" : "+v"(vk[j])); }
#define G4(a, b, c, d, sc) "s_nop 0\n\tglobal_load_lds_dwordx4 %" #a ", %8" sc "\n\tglobal_load_lds_dwordx4 %" #b ", %8 offset:1024" sc "\n\t"             \
                       "global_load_lds_dwordx4 %" #c ", %8 offset:2048" sc "\n\tglobal_load_lds_dwordx4 %" #d ", %8 offset:3072" sc "\n\t"
#define M4 "s_addk_i32 m0, 0x1000\n\t"
      auto stage = [&](const char* sp, unsigned dst) __attribute__((always_inline)) {
        asm volatile("s_mov_b32 m0, %9\n\t" G4(0, 0, 1, 1, " sc1") M4 G4(2, 2, 3, 3, " sc1") M4 G4(4, 4, 5, 5, " sc1") M4 G4(6, 6, 7, 7, " sc1")
                     :: "v"(vk[0]), "v"(vk[1]), "v"(vk[2]), "v"(vk[3]), "v"(vk[4]), "v"(vk[5]), "v"(vk[6]), "v"(vk[7]), "s"(sp), "s"(dst) : "m0", "scc", "memory");
      };
      auto count = [&]() __attribute__((always_inline)) {
        asm volatile("s_mov_b32 m0, %2\n\ts_nop 0\n\tglobal_load_lds_dword %0, %1 sc1" :: "v"(0u), "s"(cnt), "s"(lbase + FLG - 252) : "m0", "memory");
        cnt += 8;
      };
      auto freed = [&](unsigned n) __attribute__((always_inline)) {
        if (!none(pw < n)) {
#pragma unroll 1
          for (int i = 0; i < 4 * SPIN_MAX; ++i) {
            pw = *LDS_AT(const volatile unsigned char, vp);
            if (none(pw < n)) break;
            __builtin_amdgcn_s_sleep(W3SLP);
          }
        }
        asm volatile("" ::: "memory");
      };
      unsigned long long ord = (0xfedcba9876543210ull >> 4 & ~((1ull << 4 * (tmi & 7)) - 1ull)) | (0xfedcba9876543210ull & ((1ull << 4 * (tmi & 7)) - 1ull));
      ord ^= (tmi & 8) * 0x1111111111111111ull;
      unsigned seen;
      const char* kvb = reinterpret_cast<const char*>(KV) + (size_t)tni * (128 * 2048);
      auto panel = [&]() __attribute__((always_inline)) {
        const unsigned pn = (unsigned)ord & 15u;
        ord >>= 4;
        if (__builtin_expect(!((seen >> pn) & 1u), 0)) {
#pragma unroll 1
          for (int i = 0; i < SPIN_MAX && !((seen >> pn) & 1u); ++i) seen = (unsigned)__ballot(ld_ag(kvflag + (tmi % RK) * 32 + (lane & 15)) == tag);
        }
        asm volatile("" ::: "memory");
        return kvb + pn * 8192u;
      };
      asm volatile("" : "+s"(ord), "+s"(kvb), "+s"(cnt));
      asm volatile("" : "+v"(vp), "+v"(pw));
      PIN();
      seen = (unsigned)__ballot(kv0 == tag);
      const char* sp = panel();
      wait_sig(16);
      stage(sp, lbase + 2 * ASB);
      count();
      const int ksp = LAY2 ? PNS / 2 : -1, ksg = LAY2 ? 64 : GF0;
      const u16* gp = gptr(wg, wu);
#pragma unroll 1
      for (int k = 1; k < 15; ++k) {
        if (__builtin_expect(k == ksp, 0)) {
          gu_setup();
          gate_up(xq, rmd);
          PIN();
          *LDS_AT(volatile unsigned char, lbase + FLG + 7) = (unsigned char)0x7f;
        }
        freed((unsigned)(0xba9876543211000ull >> 4 * k) & 15u);
        stage(sp + 2048, lbase + ((2 * k + 1) & 7) * ASB);
        sp = panel();
        stage(sp, lbase + ((2 * k + 2) & 7) * ASB);
        count();
        if (__builtin_expect((unsigned)(k - ksg) < 4u, 0)) gather(gp, k - ksg);
      }
      freed(12u);
      glds16s<true>(csf + 4096 + tni * 128, l16, lbase + UGL);
      stage(sp + 2048, lbase + 7 * ASB);
      count();
      const unsigned vwo = (unsigned)((lane >> 3) * D_ + oni * 64 + ((lane & 7) ^ ((lane >> 3) & 6)) * 8) * 2u;
      constexpr unsigned vs = 8u * D_ * 2u - 1024u;
#pragma unroll 1
      for (int ns = 0; ns < 6; ++ns) {
        freed((unsigned)(ns + 25) >> 1);
        const char* wp = reinterpret_cast<const char*>(wo) + (size_t)ns * (128 * D_ * 2);
#pragma unroll
        for (int j = 0; j < 4; ++j)
          glds16x4<false>(wp + j * (32u * D_ * 2u), vwo, vwo + vs, vwo + 2 * vs, vwo + 3 * vs, lbase + ns * ASB + j * 4096);
      }
      asm volatile("s_waitcnt vmcnt(0)" ::: "memory");
      return;
    }
    if (!LAY2) {
      auto until = [&](unsigned n) {
        while ((__builtin_amdgcn_readfirstlane(*LDS_AT(const volatile unsigned, lbase + FLG + 8)) & 255u) < n) __builtin_amdgcn_s_sleep(16);
        asm volatile("" ::: "memory");
        PIN();
      };
      if (hw != 1) {
        constexpr int N2 = HAND_N + Y_N, X3N = 3 * D_ / 2, N0 = X3N + PS_N;
        const __amdgpu_buffer_rsrc_t rsi = __builtin_amdgcn_make_buffer_rsrc(hand + (hw ? GRAN_N : 2 * HAND_N - X3N), 0, (hw ? N2 : N0) * 4, 0x00020000);
        until(IAT);
#pragma unroll 1
        for (int i = 0; i < ((hw ? N2 : N0) / 4 + 16383) / 16384; ++i) {
          __builtin_amdgcn_raw_buffer_store_b128(u32x4{SENT, SENT, SENT, SENT}, rsi, ((i * 256 + bid) * 64 + lane) * 16, 0, 16);
          __builtin_amdgcn_s_sleep(ISLP);
        }
      }
      const size_t og = (size_t)((fg - xcd * 28) / 7 * 256 + lane) * F_ + (xcd * 7 + fg % 7) * 64;
      const u16* p = gptr(wg, wu);
#pragma unroll 1
      for (unsigned n = T0; n < T0 + 13u; ++n) {
        until(n);
        if (hw == 1 && n < T0 + 8u) {
          const unsigned p0 = touch((((n - T0) & 1u) ? wu : wg) + og + (size_t)((n - T0) >> 1) * (64 * F_));
          asm volatile("" :: "v"(p0));
        }
        if (hw == 2 && n == TFL) {
          const int ln = fresh_lane() & 15;
          const unsigned p1 = ld_ag(gran + (ln < 4 ? GR_O + (((ln >> 1) * 16 + omi) * RO + oni % RO) * 64 + (ln & 1) * 32 :
                                            (ln < 8 ? GR_X + (tmi * RX + tni % RX) * 128 + (ln & 3) * 32 : GR_KV + ((16 + tni) * RK + tmi % RK) * 32)));
          asm volatile("" :: "v"(p1));
        }
        if (hw == 2 && n == TW) {
          const int ln = fresh_lane();
          unsigned long long a0, a1;
          CODE_ADDR("layers_kernel", a0); CODE_ADDR("end_marker_kernel", a1);
          const unsigned p1 = touch_code(a0, a1, 3 + loc, ln), p0 = touch(wo + (size_t)(768 + (loc / (32 / OMX)) * (256 / OMX) + ln) * D_ + oni * 64);
          asm volatile("" :: "v"(p0), "v"(p1));
        }
        if (n >= G0 && (n - G0) % GS == 0u && (n - G0) / GS < 4u) gather(p, (n - G0) / GS);
        if (n == UAT && hw == UHW) {
          u32x4 a[4][2], c[2];
#pragma unroll
          for (int i = 0; i < 4; ++i)
#pragma unroll
            for (int h = 0; h < 2; ++h) a[i][h] = *reinterpret_cast<const u32x4*>(wo + (size_t)(tni * 64 + tmi * 4 + i) * D_ + h * 512 + lane * 8);
#pragma unroll
          for (int h = 0; h < 2; ++h) c[h] = *reinterpret_cast<const u32x4*>(w_r + h * 512 + lane * 8);
          auto dot = [&](unsigned x, unsigned y, float z) { return __builtin_amdgcn_fdot2_f32_bf16(__builtin_bit_cast(bf16x2, x), __builtin_bit_cast(bf16x2, y), z, false); };
          PIN();
          __builtin_amdgcn_s_setprio(0);
#pragma unroll 1
          for (int i = 0; i < SPIN_MAX; ++i) {
            if (__builtin_amdgcn_readfirstlane(*LDS_AT(const volatile unsigned, lbase + FLG + 16)) >= UARR) break;
            __builtin_amdgcn_s_sleep(4);
          }
          asm volatile("s_waitcnt vmcnt(0)" ::: "memory");
          PIN();
          float wm = 0.f;
#pragma unroll
          for (int e = 0; e < 8; ++e) wm = dot(c[e >> 2][e & 3], 0x3f803f80u, wm);
          wm = wave_sum(wm);
          lds_wr(lbase + FLG + 60, __float_as_uint(wm));
          wm *= -1.0f / 1024.0f;
          f32x2 u[2], g[2];
#pragma unroll
          for (int i = 0; i < 4; ++i) {
            float p1 = 0.f, pw = 0.f, p2 = 0.f;
#pragma unroll
            for (int e = 0; e < 8; ++e) {
              const unsigned x = a[i][e >> 2][e & 3];
              p1 = dot(x, 0x3f803f80u, p1); pw = dot(x, c[e >> 2][e & 3], pw); p2 = dot(x, x, p2);
            }
            u[i >> 1][i & 1] = fmaf(p1, wm, pw); g[i >> 1][i & 1] = p2;
          }
          float us = fold4(u[0], u[1]), gs = fold4(g[0], g[1]);
          us = dpp_add<0x121, 0xf>(dpp_add<0x122, 0xf>(us)); gs = dpp_add<0x121, 0xf>(dpp_add<0x122, 0xf>(gs));
          __builtin_amdgcn_raw_buffer_store_b32(__float_as_uint((lane & 1) ? __builtin_amdgcn_sqrtf(gs) * (1.0f / 32.0f) : us),
              __builtin_amdgcn_make_buffer_rsrc(csf + 4096 + tni * 128, 0, 512, 0x00020000), (lane & 14) ? 512 : ((lane & 1) * 64 + tmi * 4 + (lane >> 4)) * 4, 0, 16);
        }
      }
      if (hw == 2 && bid == 0) {
        const __amdgpu_buffer_rsrc_t rsc = __builtin_amdgcn_make_buffer_rsrc(gran, 0, GR_N * 4, 0x00020000);
#pragma unroll 1
        for (int spin = 0; spin < SPIN_MAX; ++spin) {
          bool ok = true;
#pragma unroll
          for (int i = 0; i < 4; ++i) ok &= __builtin_amdgcn_raw_buffer_load_b32(rsc, (GR_ROW + (i * 64 + lane) * 32 + 1) * 4, 0, 16) == tag;
          if (__all(ok)) break;
          __builtin_amdgcn_s_sleep(8);
          asm volatile("" ::: "memory");
        }
        if (lane == 0) st_ag(gran, tag);
      }
      return;
    }
    unsigned* X2 = hand + GRAN_N + Y_N;
    unsigned* XS = X2 + X2_N;
    unsigned* P4 = XS + XS_N;
    unsigned* PA = P4 + P4_N;
    const __amdgpu_buffer_rsrc_t rsh = __builtin_amdgcn_make_buffer_rsrc(hand, 0, HAND_N * 4, 0x00020000);
    auto signal = [&](unsigned ph) {
      asm volatile("" ::: "memory");
      PIN();
      *LDS_AT(volatile unsigned char, lbase + FLG + 4 + hw) = (unsigned char)ph;
    };
    auto wait_all = [&](unsigned ph) {
#pragma unroll 1
      for (int i = 0; i < SPIN_MAX; ++i) {
        if (all_ge(__builtin_amdgcn_readfirstlane(*LDS_AT(const volatile unsigned, lbase + FLG + 4)), ph)) break;
        __builtin_amdgcn_s_sleep(WSLP);
      }
      asm volatile("" ::: "memory");
      PIN();
    };
    gu_setup();
    gate_up(xq, rmd);
    signal(hw == 2 ? 2u : 0x7fu);
    if (wid == 8) STAMP(dk, 2);
    if (hw < 2) {
#pragma unroll 1
      for (int i = 0; i < SPIN_MAX; ++i) {
        if (__builtin_amdgcn_readfirstlane(*LDS_AT(const volatile unsigned, lbase + FLG)) >= FSTART) break;
        __builtin_amdgcn_s_sleep(WSLP);
      }
      asm volatile("" ::: "memory");
      PIN();
      if (wid == 8) STAMP(dk, 10);
      u32x2 w[16];
      const __amdgpu_buffer_rsrc_t rd_ = __builtin_amdgcn_make_buffer_rsrc(const_cast<u16*>(wd), 0, D_ * F_ * 2, 0x00020000);
      auto ldw = [&](int c) __attribute__((always_inline)) {
#pragma unroll
        for (int f = 0; f < 16; ++f) w[f] = __builtin_amdgcn_raw_buffer_load_b64(rd_, lane * 8 + c * 512 + (f & 1) * 2048, (fg * 16 + (f & ~1)) * (D_ * 2) + hw * 1024, 0);
        PIN();
      };
      ldw(0);
      const unsigned pt = __builtin_amdgcn_raw_buffer_load_b32(rd_, (lane >> 2) * (D_ * 2) + (lane & 3) * 128 + 512, fg * 16 * (D_ * 2) + hw * 1024, 0);
      int yo = p1blk ? (GRAN_N + fg * D_ + hw * 512) * 4 + lane * 16 : HAND_N * 4;
      asm volatile("" : "+v"(yo));
      wait_all(2u);
      if (wid == 8) STAMP(dk, 3);
      const s16x4 a = act_diag();
      const u32x4 y0 = down(a, w);
      PIN();
      ldw(1);
      __builtin_amdgcn_raw_buffer_store_b128(y0, rsh, yo, 0, 16);
      PIN();
      __builtin_amdgcn_raw_buffer_store_b128(down(a, w), rsh, yo + 1024, 0, 16);
      asm volatile("" :: "v"(pt));
      if (wid == 8) STAMP(dk, 4);
    }
    PIN();
    [&]() __attribute__((always_inline)) {
    const int lane = fresh_lane();
    const float rmb = __uint_as_float(rmd << 16);
    auto first_words = [&](int src, auto nc) __attribute__((always_inline)) {
#pragma unroll 1
      for (int spin = 0; spin < SPIN_MAX; ++spin) {
        unsigned mx = 0u;
#pragma unroll
        for (int i = 0; i < decltype(nc)::value; ++i) mx = umax(mx, __builtin_amdgcn_raw_buffer_load_b32(rsh, src, i * (56 * D_ * 4), 16));
        if (none(mx == SENT)) break;
        __builtin_amdgcn_s_sleep(PSLP);
        asm volatile("" ::: "memory");
      }
    };
    if (hw == 2) {
      wait_all(2u);
      if (own) {
        const float rden = __uint_as_float(rmd & 0xffff0000u), rrin = frcp(rden);
        const int col = ow * 16 + (lane & 3) * 4 + (lane >> 4);
        const unsigned xo2 = ld_ag(reinterpret_cast<const unsigned*>(A + (size_t)idx * D_ + (col & ~1)));
        __builtin_amdgcn_s_sleep(PRE1);
        first_words((GRAN_N + (lane < 56 ? lane : 0) * D_ + ow * 16) * 4, std::integral_constant<int, 4>{});
        const float y = y_sum(rsh, std::integral_constant<int, 2>{});
        const unsigned rk = best >> 16;
        const float rv = __uint_as_float(((rk & 0x8000u) ? (rk & 0x7fffu) : (~rk & 0xffffu)) << 16);
        const float rs = rbf(frcp(1.0f + __expf(-rv)));
        const float x1v = __uint_as_float((col & 1) ? xo2 & 0xffff0000u : xo2 << 16);
        float h0 = x1v, h1 = x1v;
        ln_apply2(h0, h1, rmb, rden, rrin);
        const float x2v = rbf(x1v + rbf(h0 + rbf(rbf(y) * rs)));
        const auto pr = __builtin_amdgcn_permlane16_swap(__float_as_uint(x2v), __float_as_uint(x2v), false, false);
        __builtin_amdgcn_raw_buffer_store_b32(f2word(__uint_as_float((pr[0] >> 16) | pr[1])), rsh, (lane & 28) ? HAND_N * 4 : (int)(X2 - hand) * 4 + col * 2, 0, 16);
        const float d = x2v - rmb;
        const float sd = wave_sum(d), sq = wave_sum(d * d);
        if (lane < 2) st_ag(XS + ow * 2 + lane, f2word(lane ? sq : sd));
        STAMP(dk, 5);
      }
    }
    {
      const int ks = kcap ^ (tmi & 8), g4 = lane >> 4;
      const int src = (int)(X2 - hand) * 4 + ks * 128 + g4 * 16;
      int dst = (lane & 12) ? HAND_N * 4 : (int)((P4 - hand) + (tni * 16 + ks) * 192 + hw * 64 + (lane & 2) * 16 + (lane & 1) * 8 + (g4 >> 1) * 16 + (g4 & 1) * 4) * 4;
      asm volatile("" : "+v"(dst));
      u32x4 x0, x1;
      if (!(hw == 2 && own)) __builtin_amdgcn_s_sleep(PRE4);
#pragma unroll 1
      for (int spin = 0; spin < SPIN_MAX; ++spin) {
        x0 = __builtin_amdgcn_raw_buffer_load_b128(rsh, src, 0, 16); x1 = __builtin_amdgcn_raw_buffer_load_b128(rsh, src + 64, 0, 16);
        if (none(umax(umax(umax(x0[0], x0[1]), umax(x0[2], x0[3])), umax(umax(x1[0], x1[1]), umax(x1[2], x1[3]))) == SENT)) break;
        __builtin_amdgcn_s_sleep(PSLP);
        asm volatile("" ::: "memory");
      }
      f32x4 d[4];
#pragma unroll
      for (int i = 0; i < 4; ++i) d[i] = MFMA16(sl[2 * i], __builtin_bit_cast(s16x8, x0), (f32x4{0.f, 0.f, 0.f, 0.f}));
#pragma unroll
      for (int i = 0; i < 4; ++i) d[i] = MFMA16(sl[2 * i + 1], __builtin_bit_cast(s16x8, x1), d[i]);
      u32x4 o;
#pragma unroll
      for (int e = 0; e < 4; ++e) o[e] = f2word((lane & 2) ? ((lane & 1) ? d[3][e] : d[2][e]) : ((lane & 1) ? d[1][e] : d[0][e]));
      __builtin_amdgcn_raw_buffer_store_b128(o, rsh, dst, 0, 16);
      PIN();
      if (wid == 8) STAMP(dk, 6);
    }
    if (hw == 2) {
      const int col = (lane & 15) * 4 + (lane >> 4), src = (int)((P4 - hand) + (tni * 16 + (lane >> 4)) * 192 + (lane & 15) * 4) * 4;
      const __amdgpu_buffer_rsrc_t rsq = __builtin_amdgcn_make_buffer_rsrc(hand, 0, fast ? HAND_N * 4 : 0, 0x00020000);
      if (own) {
#pragma unroll
        for (int i = 0; i < 14; ++i)
          __builtin_amdgcn_raw_buffer_store_b128(u32x4{SENT, SENT, SENT, SENT}, rsh, (GRAN_N + (i * 16 + (lane >> 2)) * D_ + ow * 16 + (lane & 3) * 4) * 4, 0, 16);
      }
      float cs[3], val[3];
#pragma unroll
      for (int p = 0; p < 3; ++p) cs[p] = __uint_as_float(ld_ag(reinterpret_cast<const unsigned*>(csf) + p * 1024 + n0 + col));
      u32x2 xs;
      __builtin_amdgcn_s_sleep(PRE7);
      first_words((int)((P4 - hand) + (tni * 16 + (lane & 15)) * 192 + ((lane >> 4) % 3) * 64) * 4, std::integral_constant<int, 1>{});
#pragma unroll 1
      for (int spin = 0; spin < SPIN_MAX; ++spin) {
        u32x4 v[3][4];
        xs = __builtin_amdgcn_raw_buffer_load_b64(rsh, (int)((XS - hand) + lane * 2) * 4, 0, 16);
#pragma unroll
        for (int p = 2; p >= 0; --p)
#pragma unroll
          for (int i = 0; i < 4; ++i) v[p][i] = __builtin_amdgcn_raw_buffer_load_b128(p ? rsh : rsq, src + p * 256, i * (4 * 192 * 4), 16);
        auto part = [&](int p) __attribute__((always_inline)) {
          float z[4];
#pragma unroll
          for (int e = 0; e < 4; ++e) {
            z[e] = fadd(fadd(__uint_as_float(v[p][0][e]), __uint_as_float(v[p][1][e])), fadd(__uint_as_float(v[p][2][e]), __uint_as_float(v[p][3][e])));
          }
          val[p] = fold16(f32x2{z[0], z[1]}, f32x2{z[2], z[3]});
        };
        part(2); part(1);
        val[0] = 0.f;
        if (fast) part(0);
        const float chk = fadd(fadd(fadd(val[0], val[1]), fadd(val[2], __uint_as_float(xs[0]))), __uint_as_float(xs[1]));
        if (none(chk != chk)) break;
        unsigned mx = umax(xs[0], xs[1]);
#pragma unroll
        for (int i = 0; i < 12; ++i) mx = umax(umax(umax(mx, v[i >> 2][i & 3][0]), v[i >> 2][i & 3][1]), umax(v[i >> 2][i & 3][2], v[i >> 2][i & 3][3]));
        if (none(mx == SENT)) break;
        __builtin_amdgcn_s_sleep(PSLP);
        asm volatile("" ::: "memory");
      }
      const auto sw = __builtin_amdgcn_permlane32_swap(xs[0], xs[1], false, false);
      float sm = __uint_as_float(sw[0]) + __uint_as_float(sw[1]);
      sm = dpp_add<0x142, 0xa>(dpp_add<0x118, 0xf>(dpp_add<0x114, 0xf>(dpp_add<0x112, 0xf>(dpp_add<0x111, 0xf>(sm)))));
      const float dm = __int_as_float(__builtin_amdgcn_readlane(__float_as_int(sm), 31)) * (0.25f / 1024.0f);
      const float var = fmaxf(__int_as_float(__builtin_amdgcn_readlane(__float_as_int(sm), 63)) * (0.25f / 1024.0f) - dm * dm, 0.f);
      const float mb = rbf(rmb + dm);
      const float rin = frcp(rbf(__builtin_amdgcn_sqrtf(rbf(rbf(var) + 1e-5f))));
      const unsigned kv = pack2((val[1] - mb * cs[1]) * rin, (val[2] - mb * cs[2]) * rin);
      *LDS_AT(u16, lbase + TAB + 128 + kpos(col) * 2) = (u16)kv;
      *LDS_AT(u16, lbase + TAB + 256 + col * 2) = (u16)(kv >> 16);
      if (fast) *LDS_AT(u16, lbase + TAB + kpos(col) * 2) = f2bf((val[0] - mb * cs[0]) * rin * QSCALE);
      signal(7u);
      STAMP(dk, 7);
      return;
    }
    if (!fast) return;
    const int head = tni * 2 + hw;
    const unsigned tb = lbase + TAB + hw * 64;
    {
      const __amdgpu_buffer_rsrc_t rs = __builtin_amdgcn_make_buffer_rsrc(KV, 0, 2 * S_ * D_ * 2, 0x00020000);
      const int src = (head * 64 + tmi * 4) * 2048 + lane * 16;
      auto ld = [&](int i, unsigned off) { return __builtin_bit_cast(s16x8, __builtin_amdgcn_raw_buffer_load_b128(rs, src + (i & 3) * 1024, (i >> 3) * 65536 + ((i >> 2) & 1) * 4096 + off, 16)); };
#pragma unroll 1
      for (int i = 0; i < SPIN_MAX; ++i)
        if (__builtin_amdgcn_readfirstlane(ld_ag(kvflag + (tmi % RK) * 32 + tmi + 8)) == tag) break;
      asm volatile("" ::: "memory");
      s16x8 f[16];
#pragma unroll
      for (int i = 0; i < 16; ++i) f[i] = ld(i, 0u);
      PIN();
      const bool on = ((lane ^ (lane >> 4)) & 1) == 0;
      const unsigned pl = lbase + RED + hw * 1024;
      unsigned pw = pl + (lane >> 4) * 64 + (lane & 15) * 2, pr = pl + (on ? (lane >> 5) * 16 : 512), qr = on ? tb + (lane >> 5) * 16 : pl + 512;
      const int gone = (((idx >> 7) & 7) == tmi) ? ((idx >> 10) * 2 + (kpos(idx & 31) >> 4)) * 64 + ((idx >> 5) & 3) * 16 + (kpos(idx & 31) & 15) : -1;
      asm volatile("" : "+v"(pw), "+v"(pr), "+v"(qr));
      *LDS_AT(u32x2, pl + 512 + lane * 8) = u32x2{0u, 0u};
      wait_all(7u);
      u32x4 q[2];
#pragma unroll
      for (int sx = 0; sx < 2; ++sx) {
        q[sx] = *LDS_AT(const u32x4, qr + sx * 32);
      }
      float sc[4];
#pragma unroll
      for (int i = 0; i < 8; ++i) {
        f32x4 d = MFMA16(__builtin_bit_cast(s16x8, q[0]), f[2 * i], (f32x4{0.f, 0.f, 0.f, 0.f}));
        d = MFMA16(__builtin_bit_cast(s16x8, q[1]), f[2 * i + 1], d);
        PIN();
        f[2 * i] = ld(2 * i, VOFF); f[2 * i + 1] = ld(2 * i + 1, VOFF);
        PIN();
#pragma unroll
        for (int r = 0; r < 2; ++r) sc[(i >> 2) * 2 + r] = (i & 3) == 0 || (lane >> 4) == (i & 3) ? d[r] : sc[(i >> 2) * 2 + r];
      }
      if (gone >= 0) {
#pragma unroll
        for (int r = 0; r < 4; ++r) sc[r] = r * 64 + lane == gone ? -INFINITY : sc[r];
      }
      const float mx = wave_fmax(fmaxf(fmaxf(sc[0], sc[1]), fmaxf(sc[2], sc[3])));
      const unsigned p0 = pack2(__builtin_amdgcn_exp2f(sc[0] - mx), __builtin_amdgcn_exp2f(sc[1] - mx)), p1 = pack2(__builtin_amdgcn_exp2f(sc[2] - mx), __builtin_amdgcn_exp2f(sc[3] - mx));
      *LDS_AT(u16, pw) = (u16)p0; *LDS_AT(u16, pw + 32) = (u16)(p0 >> 16);
      *LDS_AT(u16, pw + 256) = (u16)p1; *LDS_AT(u16, pw + 288) = (u16)(p1 >> 16);
      asm volatile("" ::: "memory");
      const float ls = wave_sum((__uint_as_float(p0 << 16) + __uint_as_float(p0 & 0xffff0000u)) + (__uint_as_float(p1 << 16) + __uint_as_float(p1 & 0xffff0000u)));
      f32x4 o = {0.f, 0.f, 0.f, 0.f};
#pragma unroll
      for (int i = 0; i < 16; ++i) o = MFMA16(*LDS_AT(const s16x8, pr + i * 32), f[i], o);
      int pv = __float_as_int((lane & 16) ? o[1] : o[0]);
      asm volatile("s_nop 0\n\tv_writelane_b32 %0, %1, 32\n\tv_writelane_b32 %0, %2, 33" : "+v"(pv) : "s"(ls), "s"(mx));
      __builtin_amdgcn_raw_buffer_store_b32(f2word(__int_as_float(pv)), rsh, lane < 34 ? (int)((PA - hand) + (head * 8 + tmi) * 64 + lane) * 4 : HAND_N * 4, 0, 16);
    }
    if (wid == 8) STAMP(dk, 8);
    if (tmi != ((idx >> 7) & 7)) return;
    {
      const int src = (int)((PA - hand) + head * 512 + (lane < 34 ? lane : 0)) * 4;
      const unsigned ta = tb + (lane & 31) * 2;
      const float so = wave_sum(lane < 32 ? bf2f(*LDS_AT(const u16, ta)) * bf2f(*LDS_AT(const u16, ta + 128)) : 0.f);
      const float vo = lane < 32 ? bf2f(*LDS_AT(const u16, ta + 256)) : 1.0f;
      unsigned v[8];
      __builtin_amdgcn_s_sleep(PRE5);
#pragma unroll 1
      for (int spin = 0; spin < SPIN_MAX; ++spin) {
        unsigned mx = 0u;
#pragma unroll
        for (int i = 0; i < 8; ++i) v[i] = __builtin_amdgcn_raw_buffer_load_b32(rsh, src + i * 256, 0, 16);
#pragma unroll
        for (int i = 0; i < 8; ++i) mx = umax(mx, v[i]);
        if (none(mx == SENT)) break;
        __builtin_amdgcn_s_sleep(PSLP);
        asm volatile("" ::: "memory");
      }
      float m8[8], mx = so;
#pragma unroll
      for (int i = 0; i < 8; ++i) { m8[i] = __int_as_float(__builtin_amdgcn_readlane((int)v[i], 33)); mx = fmaxf(mx, m8[i]); }
      float acc = __builtin_amdgcn_exp2f(so - mx) * vo;
#pragma unroll
      for (int i = 0; i < 8; ++i) acc += __builtin_amdgcn_exp2f(m8[i] - mx) * __uint_as_float(v[i]);
      const float l = __int_as_float(__builtin_amdgcn_readlane(__float_as_int(acc), 32));
      const float ov = acc / l;
      const float o1 = __int_as_float(__builtin_amdgcn_update_dpp(0, __float_as_int(ov), 0x55, 0xf, 0xf, false));
      const float o2 = __int_as_float(__builtin_amdgcn_update_dpp(0, __float_as_int(ov), 0xaa, 0xf, 0xf, false));
      const float o3 = __int_as_float(__builtin_amdgcn_update_dpp(0, __float_as_int(ov), 0xff, 0xf, 0xf, false));
      const __amdgpu_buffer_rsrc_t rso = __builtin_amdgcn_make_buffer_rsrc(Of, 0, S_ * D_ * 2, 0x00020000);
      if (lane < 32 && (lane & 3) == 0)
        __builtin_amdgcn_raw_buffer_store_b64(u32x2{pack2(ov, o1), pack2(o2, o3)}, rso,
            (((idx >> 5) * 32 + head) * 1024 + (lane >> 4) * 512 + (((lane >> 2) & 1) * 32 + (idx & 31)) * 8 + ((lane >> 3) & 1) * 4) * 2, 0, 16);
      asm volatile("s_waitcnt vmcnt(0)" ::: "memory");
      if (lane < RO) st_ag(gran + GR_O + ((16 + (idx >> 7)) * RO + lane) * 64 + 16 + head, tag);
      STAMP(dk, 9);
    }
    }();
    }();
    __builtin_amdgcn_s_setprio(0);
    if (LAY2 || hw != UHW) asm volatile("s_waitcnt vmcnt(0)" ::: "memory");
    PIN();
    ARGS_HERE;
    u32x2 w[16];
    auto ld_wd = [&]() __attribute__((always_inline)) {
      const __amdgpu_buffer_rsrc_t rd_ = __builtin_amdgcn_make_buffer_rsrc(const_cast<u16*>(wd), 0, D_ * F_ * 2, 0x00020000);
#pragma unroll
      for (int f = 0; f < 16; ++f) w[f] = __builtin_amdgcn_raw_buffer_load_b64(rd_, t * 8 + (f & 1) * 2048, (fg * 16 + (f & ~1)) * (D_ * 2), 0);
      PIN();
    };
    float fnum = 0.f, fvar = 0.f;
    const int rrow = omi * 128 + (oni >> 3) * 64 + (hw & 1) * 32;
    if (LAY2) {
      ld_wd();
      if (!p1blk) {
        const int fr = rrow + (lane & 7) * 4 + (lane >> 4);
        const double s1 = ld_ag(stats + fr), s2 = ld_ag(stats + S_ + fr), sw = ld_ag(stats + 2 * S_ + fr);
        lds_wr(lbase + SPM + 132, __float_as_uint(wsum));
        float mean, var, mb, den;
        ln_from_sums(s1, s2, mean, var, mb, den);
        const float g1 = 1.0f + frcp(den);
        fnum = fr == idx ? __uint_as_float(0x7fc00000u) : fmaf(-mean, wsum, (float)sw) * g1;
        fvar = var * g1 * g1;
      }
      asm volatile("" : "+v"(fnum), "+v"(fvar));
      PIN();
    }
    if (hw == 3) {
      const int nfl = (LAY2 && (idx >> 7) == omi) ? 48 : 16;
      auto pfl = (__attribute__((address_space(1))) const unsigned*)(gran + GR_O + (((LAY2 ? 16 : 0) + omi) * RO + oni % RO) * 64 + (lane < nfl ? lane : 0));
      constexpr unsigned vs = 8u * D_ * 2u - 1024u;
      unsigned vw0 = (unsigned)((lane >> 3) * D_ + oni * 64 + ((lane & 7) ^ ((lane >> 3) & 6)) * 8) * 2u, vw1 = vw0 + vs, vw2 = vw0 + 2 * vs, vw3 = vw0 + 3 * vs;
      asm volatile("" : "+v"(pfl), "+v"(vw0), "+v"(vw1), "+v"(vw2), "+v"(vw3));
      auto wait_arr = [&](unsigned n) {
#pragma unroll 1
        for (int i = 0; i < 4 * SPIN_MAX; ++i) {
          if (__builtin_amdgcn_readfirstlane(*LDS_AT(const volatile unsigned, lbase + FLG + 16)) >= n) break;
          __builtin_amdgcn_s_sleep(1);
        }
        asm volatile("" ::: "memory");
        PIN();
      };
      wait_arr(12u);
      unsigned fl;
      asm volatile("global_load_dword %0, %1, off sc1" : "=v"(fl) : "v"(pfl) : "memory");
#pragma unroll 1
      for (int i = 0; i < 4 * SPIN_MAX; ++i) {
        if (__builtin_amdgcn_readfirstlane(*LDS_AT(const volatile unsigned, lbase + FLG + 24)) >= 8u) break;
        __builtin_amdgcn_s_sleep(1);
      }
      asm volatile("" ::: "memory");
      PIN();
#pragma unroll
      for (int j = 24; j < 32; ++j)
        glds16x4<false>(reinterpret_cast<const char*>(wo) + j * (32u * D_ * 2u), vw0, vw1, vw2, vw3, lbase + j * 4096);
      unsigned keep;
      asm volatile("s_mov_b32 %0, m0\n\ts_mov_b32 m0, %3\n\ts_nop 2\n\tglobal_load_lds_dword %1, %2 sc1\n\ts_mov_b32 m0, %0"
                   : "=&s"(keep) : "v"(0u), "s"(reinterpret_cast<const char*>(gran + GR_N + bid * 64 + 39)), "s"(lbase + FLG - 252) : "memory");
      asm volatile("s_waitcnt vmcnt(33)" : "+v"(fl) :: "memory");
      if (__builtin_amdgcn_ballot_w64(fl != tag)) {
        __builtin_amdgcn_s_sleep(CSL1);
#pragma unroll 1
        for (int spin = 0; spin < SPIN_MAX; ++spin) {
          if (!__builtin_amdgcn_ballot_w64(__hip_atomic_load(pfl, __ATOMIC_RELAXED, __HIP_MEMORY_SCOPE_AGENT) != tag)) break;
          __builtin_amdgcn_s_sleep(CSLP);
        }
      }
      asm volatile("" ::: "memory");
      PIN();
    }
    u32x4 pv[4];
    asm volatile("" : "=v"(pv[0]), "=v"(pv[1]), "=v"(pv[2]), "=v"(pv[3]));
    if (LAY2 && !p1blk && hw < 2) {
      const __amdgpu_buffer_rsrc_t rsp = __builtin_amdgcn_make_buffer_rsrc(hand + 2 * HAND_N + PP_O, 0, H_ * S_ * 4, 0x00020000);
      const int src = ((lane >> 3) * S_ + rrow + (lane & 7) * 4) * 4;
#pragma unroll 1
      for (int i = 0; i < 4 * SPIN_MAX; ++i) {
        if (__builtin_amdgcn_readfirstlane(*LDS_AT(const volatile unsigned, lbase + FLG + 24)) >= 8u) break;
        __builtin_amdgcn_s_sleep(2);
      }
#pragma unroll 1
      for (int spin = 0; spin < SPIN_MAX; ++spin) {
        asm volatile("" ::: "memory");
        unsigned mx = 0u;
#pragma unroll
        for (int i = 0; i < 4; ++i) pv[i] = __builtin_amdgcn_raw_buffer_load_b128(rsp, src, i * (8 * S_ * 4), 16);
#pragma unroll
        for (int i = 0; i < 4; ++i) mx = umax(umax(umax(mx, pv[i][0]), pv[i][1]), umax(pv[i][2], pv[i][3]));
        if (none(mx == SENT)) break;
        __builtin_amdgcn_s_sleep(PPSLP);
      }
    }
    asm volatile("s_waitcnt lgkmcnt(0)" ::: "memory");
    __builtin_amdgcn_s_barrier();
    if (!LAY2) {
      PIN();
      asm volatile(AGAIN "\ngate_up_code:\n\t"
                 "v_lshlrev_b32 v84, 16, v100\n\tv_and_b32 v85, 0xffff0000, v100\n\tv_lshlrev_b32 v86, 16, v101\n\tv_and_b32 v87, 0xffff0000, v101\n\t"
                 "v_subrev_f32 v84, s84, v84\n\tv_subrev_f32 v85, s84, v85\n\tv_subrev_f32 v86, s84, v86\n\tv_subrev_f32 v87, s84, v87\n\t"
                 "v_cvt_pk_bf16_f32 v85, v84, v85\n\tv_cvt_pk_bf16_f32 v87, v86, v87\n\ts_mov_b32 s88, 0x01000100\n\ts_mov_b32 s89, 0x03020302\n\t"
                 "v_lshlrev_b32 v84, 16, v85\n\tv_and_b32 v85, 0xffff0000, v85\n\tv_lshlrev_b32 v86, 16, v87\n\tv_and_b32 v87, 0xffff0000, v87\n\t"
                 "v_mul_f32 v88, s86, v84\n\tv_mul_f32 v89, s86, v85\n\tv_mul_f32 v90, s86, v86\n\tv_mul_f32 v91, s86, v87\n\tv_fma_f32 v84, -v88, s85, v84\n\t"
                 "v_fma_f32 v85, -v89, s85, v85\n\tv_fma_f32 v86, -v90, s85, v86\n\tv_fma_f32 v87, -v91, s85, v87\n\tv_fmac_f32 v88, s86, v84\n\t"
                 "v_fmac_f32 v89, s86, v85\n\tv_fmac_f32 v90, s86, v86\n\tv_fmac_f32 v91, s86, v87\n\tv_cvt_pk_bf16_f32 v88, v88, v89\n\t"
                 "v_cvt_pk_bf16_f32 v90, v90, v91\n\tv_perm_b32 v92, v88, v88, s88\n\tv_perm_b32 v93, v88, v88, s89\n\tv_perm_b32 v94, v90, v90, s88\n\t"
                 "v_perm_b32 v95, v90, v90, s89\n\ts_nop 0\n\tv_and_b32_dpp v96, v92, v102 quad_perm:[0,0,0,0] row_mask:0xf bank_mask:0xf\n\t"
                 "v_and_b32_dpp v97, v92, v103 quad_perm:[0,0,0,0] row_mask:0xf bank_mask:0xf\n\t"
                 "v_and_b32_dpp v100, v93, v102 quad_perm:[0,0,0,0] row_mask:0xf bank_mask:0xf\n\t"
                 "v_and_b32_dpp v101, v93, v103 quad_perm:[0,0,0,0] row_mask:0xf bank_mask:0xf\n\tv_mfma_f32_4x4x4_16b_bf16 v[84:87], v[96:97], v[104:105], 0\n\t"
                 "v_mfma_f32_4x4x4_16b_bf16 v[88:91], v[96:97], v[106:107], 0\n\tv_and_b32_dpp v96, v94, v102 quad_perm:[0,0,0,0] row_mask:0xf bank_mask:0xf\n\t"
                 "v_and_b32_dpp v97, v94, v103 quad_perm:[0,0,0,0] row_mask:0xf bank_mask:0xf\n\t"
                 "v_mfma_f32_4x4x4_16b_bf16 v[84:87], v[100:101], v[108:109], v[84:87]\n\tv_mfma_f32_4x4x4_16b_bf16 v[88:91], v[100:101], v[110:111], v[88:91]\n\t"
                 "v_and_b32_dpp v100, v95, v102 quad_perm:[0,0,0,0] row_mask:0xf bank_mask:0xf\n\t"
                 "v_and_b32_dpp v101, v95, v103 quad_perm:[0,0,0,0] row_mask:0xf bank_mask:0xf\n\tv_mfma_f32_4x4x4_16b_bf16 v[84:87], v[96:97], v[112:113], v[84:87]\n\t"
                 "v_mfma_f32_4x4x4_16b_bf16 v[88:91], v[96:97], v[114:115], v[88:91]\n\tv_and_b32_dpp v96, v92, v102 quad_perm:[1,1,1,1] row_mask:0xf bank_mask:0xf\n\t"
                 "v_and_b32_dpp v97, v92, v103 quad_perm:[1,1,1,1] row_mask:0xf bank_mask:0xf\n\t"
                 "v_mfma_f32_4x4x4_16b_bf16 v[84:87], v[100:101], v[116:117], v[84:87]\n\tv_mfma_f32_4x4x4_16b_bf16 v[88:91], v[100:101], v[118:119], v[88:91]\n\t"
                 "v_and_b32_dpp v100, v93, v102 quad_perm:[1,1,1,1] row_mask:0xf bank_mask:0xf\n\t"
                 "v_and_b32_dpp v101, v93, v103 quad_perm:[1,1,1,1] row_mask:0xf bank_mask:0xf\n\tv_mfma_f32_4x4x4_16b_bf16 v[84:87], v[96:97], v[120:121], v[84:87]\n\t"
                 "v_mfma_f32_4x4x4_16b_bf16 v[88:91], v[96:97], v[122:123], v[88:91]\n\tv_and_b32_dpp v96, v94, v102 quad_perm:[1,1,1,1] row_mask:0xf bank_mask:0xf\n\t"
                 "v_and_b32_dpp v97, v94, v103 quad_perm:[1,1,1,1] row_mask:0xf bank_mask:0xf\n\t"
                 "v_mfma_f32_4x4x4_16b_bf16 v[84:87], v[100:101], v[124:125], v[84:87]\n\tv_mfma_f32_4x4x4_16b_bf16 v[88:91], v[100:101], v[126:127], v[88:91]\n\t"
                 "v_and_b32_dpp v100, v95, v102 quad_perm:[1,1,1,1] row_mask:0xf bank_mask:0xf\n\t"
                 "v_and_b32_dpp v101, v95, v103 quad_perm:[1,1,1,1] row_mask:0xf bank_mask:0xf\n\tv_mfma_f32_4x4x4_16b_bf16 v[84:87], v[96:97], v[128:129], v[84:87]\n\t"
                 "v_mfma_f32_4x4x4_16b_bf16 v[88:91], v[96:97], v[130:131], v[88:91]\n\tv_and_b32_dpp v96, v92, v102 quad_perm:[2,2,2,2] row_mask:0xf bank_mask:0xf\n\t"
                 "v_and_b32_dpp v97, v92, v103 quad_perm:[2,2,2,2] row_mask:0xf bank_mask:0xf\n\t"
                 "v_mfma_f32_4x4x4_16b_bf16 v[84:87], v[100:101], v[132:133], v[84:87]\n\tv_mfma_f32_4x4x4_16b_bf16 v[88:91], v[100:101], v[134:135], v[88:91]\n\t"
                 "v_and_b32_dpp v100, v93, v102 quad_perm:[2,2,2,2] row_mask:0xf bank_mask:0xf\n\t"
                 "v_and_b32_dpp v101, v93, v103 quad_perm:[2,2,2,2] row_mask:0xf bank_mask:0xf\n\tv_mfma_f32_4x4x4_16b_bf16 v[84:87], v[96:97], v[136:137], v[84:87]\n\t"
                 "v_mfma_f32_4x4x4_16b_bf16 v[88:91], v[96:97], v[138:139], v[88:91]\n\tv_and_b32_dpp v96, v94, v102 quad_perm:[2,2,2,2] row_mask:0xf bank_mask:0xf\n\t"
                 "v_and_b32_dpp v97, v94, v103 quad_perm:[2,2,2,2] row_mask:0xf bank_mask:0xf\n\t"
                 "v_mfma_f32_4x4x4_16b_bf16 v[84:87], v[100:101], v[140:141], v[84:87]\n\tv_mfma_f32_4x4x4_16b_bf16 v[88:91], v[100:101], v[142:143], v[88:91]\n\t"
                 "v_and_b32_dpp v100, v95, v102 quad_perm:[2,2,2,2] row_mask:0xf bank_mask:0xf\n\t"
                 "v_and_b32_dpp v101, v95, v103 quad_perm:[2,2,2,2] row_mask:0xf bank_mask:0xf\n\tv_mfma_f32_4x4x4_16b_bf16 v[84:87], v[96:97], v[144:145], v[84:87]\n\t"
                 "v_mfma_f32_4x4x4_16b_bf16 v[88:91], v[96:97], v[146:147], v[88:91]\n\tv_and_b32_dpp v96, v92, v102 quad_perm:[3,3,3,3] row_mask:0xf bank_mask:0xf\n\t"
                 "v_and_b32_dpp v97, v92, v103 quad_perm:[3,3,3,3] row_mask:0xf bank_mask:0xf\n\t"
                 "v_mfma_f32_4x4x4_16b_bf16 v[84:87], v[100:101], v[148:149], v[84:87]\n\tv_mfma_f32_4x4x4_16b_bf16 v[88:91], v[100:101], v[150:151], v[88:91]\n\t"
                 "v_and_b32_dpp v100, v93, v102 quad_perm:[3,3,3,3] row_mask:0xf bank_mask:0xf\n\t"
                 "v_and_b32_dpp v101, v93, v103 quad_perm:[3,3,3,3] row_mask:0xf bank_mask:0xf\n\tv_mfma_f32_4x4x4_16b_bf16 v[84:87], v[96:97], v[152:153], v[84:87]\n\t"
                 "v_mfma_f32_4x4x4_16b_bf16 v[88:91], v[96:97], v[154:155], v[88:91]\n\tv_and_b32_dpp v96, v94, v102 quad_perm:[3,3,3,3] row_mask:0xf bank_mask:0xf\n\t"
                 "v_and_b32_dpp v97, v94, v103 quad_perm:[3,3,3,3] row_mask:0xf bank_mask:0xf\n\t"
                 "v_mfma_f32_4x4x4_16b_bf16 v[84:87], v[100:101], v[156:157], v[84:87]\n\tv_mfma_f32_4x4x4_16b_bf16 v[88:91], v[100:101], v[158:159], v[88:91]\n\t"
                 "v_and_b32_dpp v100, v95, v102 quad_perm:[3,3,3,3] row_mask:0xf bank_mask:0xf\n\t"
                 "v_and_b32_dpp v101, v95, v103 quad_perm:[3,3,3,3] row_mask:0xf bank_mask:0xf\n\tv_mfma_f32_4x4x4_16b_bf16 v[84:87], v[96:97], v[160:161], v[84:87]\n\t"
                 "v_mfma_f32_4x4x4_16b_bf16 v[88:91], v[96:97], v[162:163], v[88:91]\n\ts_nop 1\n\t"
                 "v_mfma_f32_4x4x4_16b_bf16 v[84:87], v[100:101], v[164:165], v[84:87]\n\tv_mfma_f32_4x4x4_16b_bf16 v[88:91], v[100:101], v[166:167], v[88:91]\n\t"
                 "s_nop 4\n\tv_permlane32_swap_b32 v84, v88\n\tv_permlane32_swap_b32 v85, v89\n\tv_permlane32_swap_b32 v86, v90\n\tv_permlane32_swap_b32 v87, v91\n\t"
                 "v_add_f32 v84, v84, v88\n\tv_add_f32 v85, v85, v89\n\tv_add_f32 v86, v86, v90\n\tv_add_f32 v87, v87, v91\n\ts_nop 0\n\t"
                 "v_permlane16_swap_b32 v84, v85\n\tv_permlane16_swap_b32 v86, v87\n\ts_nop 0\n\tv_add_f32 v84, v84, v85\n\tv_add_f32 v86, v86, v87\n\ts_nop 1\n\t"
                 "v_add_f32_dpp v84, v84, v84 row_ror:8 row_mask:0xf bank_mask:0xf\n\tv_add_f32_dpp v86, v86, v86 row_ror:8 row_mask:0xf bank_mask:0xf\n\ts_nop 0\n\t"
                 "v_add_f32_dpp v84, v84, v84 row_ror:4 row_mask:0xf bank_mask:0xf\n\tv_add_f32_dpp v86, v86, v86 row_ror:4 row_mask:0xf bank_mask:0xf\n\t"
                 "ds_write2_b32 v99, v84, v86 offset1:8\n\ts_setpc_b64 s[92:93]\n\t"
                   :: "{s[0:1]}"(ap + 1), "{s2}"(bid), "{v0}"(wid * 64), KEPT() : "s90", "s91", "scc", "memory");
      __builtin_unreachable();
    }
    {
    constexpr int dk = 8;
    constexpr int BEST = FLG + 40, MEET = FLG + 48;
    __builtin_amdgcn_s_setprio(HPRIO);
    if (hw == 0) STAMP(dk, 0);
    const unsigned tb1 = (unsigned)__builtin_amdgcn_s_memrealtime();
    unsigned* hand2 = hand + HAND_N;
    const __amdgpu_buffer_rsrc_t rsh = __builtin_amdgcn_make_buffer_rsrc(hand2, 0, (HAND_N + PS_N) * 4, 0x00020000);
    const __amdgpu_buffer_rsrc_t rsx = __builtin_amdgcn_make_buffer_rsrc(xout, 0, 3 * D_ * 2, 0x00020000);
    constexpr int OOR = (HAND_N + PS_N) * 4;
    if (!p1blk && hw < 2) {
      f32x2 a[2], c[2];
#pragma unroll
      for (int e = 0; e < 4; ++e) {
        a[e >> 1][e & 1] = (__uint_as_float(pv[0][e] << 16) + __uint_as_float(pv[1][e] << 16)) + (__uint_as_float(pv[2][e] << 16) + __uint_as_float(pv[3][e] << 16));
        c[e >> 1][e & 1] = (__uint_as_float(pv[0][e] & 0xffff0000u) + __uint_as_float(pv[1][e] & 0xffff0000u)) + (__uint_as_float(pv[2][e] & 0xffff0000u) + __uint_as_float(pv[3][e] & 0xffff0000u));
      }
      const float r = (fnum + fold8(a[0], a[1])) * __builtin_amdgcn_rsqf(fvar + fold8(c[0], c[1]));
      unsigned key = (__float_as_uint(r) ^ ((unsigned)(__float_as_int(r) >> 31) | 0x80000000u)) & 0xfffff800u;
      if (r != r) key = 0u;
      key = wave_umax(key | (unsigned)(2047 - (rrow + (lane & 7) * 4 + (lane >> 4))));
      __builtin_amdgcn_raw_buffer_store_b64(u32x2{key, tb1 & 0x7fffffffu}, rsh, lane < NREP ? (HAND_N + PK_O + lane * 128 + (rrow >> 5) * 2) * 4 : OOR, 0, 16);
      if (hw == 1) STAMP(dk, 2);
    }
    gu_setup();
    bool got = true;
    unsigned fra = lbase;
    int fro = S_ * D_ * 2;
    auto get_fc = [&]() __attribute__((always_inline)) {
      const u32x2 fw = __builtin_amdgcn_raw_buffer_load_b64(rsh, (HAND_N + PK_O + (bid % NREP) * 128 + lane * 2) * 4, 0, 16);
      got = none(umax(fw[0], fw[1]) == SENT);
      if (got) {
        const unsigned c1 = wave_umax(fw[0]), c2 = wave_umax(fw[0] == c1 ? 0u : fw[0]);
#define BADFC 0
        const unsigned r1 = (2047u - (c1 & 0x7ffu)) ^ BADFC, r2 = 2047u - (c2 & 0x7ffu);
        const unsigned r = (lane & 8) ? r2 : r1;
        fra = lbase + QOFF + (r & 127u) * 128 + (((r ^ lane) & 7u) << 4);
        fro = (r >> 7) == (unsigned)omi && lane < 16 ? ((lane >> 3) * D_ + oni * 64 + (lane & 7) * 8) * 2 : S_ * D_ * 2;
        unsigned dt = (unsigned)((int)((fw[1] - tb1) << 1) >> 1) + 512u;
        dt = dt < 1024u ? dt : 512u;
        *LDS_AT(volatile u32x2, lbase + SPM + 144) = u32x2{r1 | (r2 << 16), (unsigned)__builtin_amdgcn_readlane(dt, r1 >> 5) | (wave_umax(dt) << 16)};
      }
    };
    if (hw == 0) {
      __builtin_amdgcn_s_sleep(FCPRE);
#pragma unroll 1
      for (int i = 0; i < SPIN_MAX; ++i) {
        if (__builtin_amdgcn_readfirstlane(*LDS_AT(const volatile unsigned, lbase + FLG + 28)) - best >= 4u) break;
        __builtin_amdgcn_s_sleep(FCSLP);
      }
      asm volatile("" ::: "memory");
      get_fc();
    } else __builtin_amdgcn_s_sleep(TPRE - 10);
    PIN();
    tile_at(2u);
    const unsigned tb2 = (unsigned)__builtin_amdgcn_s_memrealtime() - 512u;
    LOGDEF;
    LOG();
#pragma unroll 1
    for (int spin = 0; spin < SPIN_MAX && !got; ++spin) { get_fc(); asm volatile("" ::: "memory"); }
    if (hw == 0) __builtin_amdgcn_raw_buffer_store_b128(lds_rd4(fra), rsx, fro, 0, XAUX);
    PIN();
    u32x2 fm = {SENT, 0u};
#pragma unroll 1
    for (int i = 0; i < 4 * SPIN_MAX && fm[0] == SENT; ++i) {
      const u32x2 q = *LDS_AT(const volatile u32x2, lbase + SPM + 144);
      fm = u32x2{(unsigned)__builtin_amdgcn_readfirstlane(q[0]), (unsigned)__builtin_amdgcn_readfirstlane(q[1])};
    }
    const int fr1 = fm[0] & 0x7ffu, fr2 = (fm[0] >> 16) & 0x7ffu;
    const unsigned t1 = tb2 + (fm[1] & 1023u), tmx = tb2 + ((fm[1] >> 16) & 1023u);
    auto until = [&](unsigned te) { while ((int)((unsigned)__builtin_amdgcn_s_memrealtime() - te) < 0) __builtin_amdgcn_s_sleep(1); asm volatile("" ::: "memory"); };
    lds_wr(lbase + SPM + 136, tmx);
    PIN();
    if (hw == 0) STAMP(dk, 3);
    const int xoff = (hw * 256 + (lane & 3) * 64 + (lane >> 2) * 4) * 2;
    const int oc = (omi * 2 + (oni >> 3)) * 32, col = oc + (lane & 7) * 4 + (lane >> 4);
    const int xoo = !p1blk && hw == 3 ? (col & ~1) * 2 : S_ * D_ * 2;
    auto get_row = [&](int r, int sl, u32x2& xr, unsigned& xo) __attribute__((always_inline)) {
      if ((r >> 7) == omi) {
#pragma unroll 1
        for (int i = 0; i < SPIN_MAX; ++i) {
          if (__builtin_amdgcn_readfirstlane(*LDS_AT(const volatile unsigned, lbase + FLG + 24)) >= 16u) break;
          __builtin_amdgcn_s_sleep(1);
        }
        asm volatile("" ::: "memory");
      }
#pragma unroll 1
      for (int spin = 0; spin < SPIN_MAX; ++spin) {
        LOG();
        xr = __builtin_amdgcn_raw_buffer_load_b64(rsx, xoff, sl * (D_ * 2), 16);
        xo = __builtin_amdgcn_raw_buffer_load_b32(rsx, xoo, sl * (D_ * 2), 16);
        LOG();
        if (none(umax(umax(xr[0], xr[1]), xo) == SENT)) break;
        __builtin_amdgcn_s_sleep(XRSLP);
        asm volatile("" ::: "memory");
      }
      PIN();
    };
    auto row_md = [&](const u32x2& xr, int k) __attribute__((always_inline)) {
      auto dot = [&](unsigned x, unsigned y, float z) { return __builtin_amdgcn_fdot2_f32_bf16(__builtin_bit_cast(bf16x2, x), __builtin_bit_cast(bf16x2, y), z, false); };
      const float p1 = dot(xr[1], 0x3f803f80u, dot(xr[0], 0x3f803f80u, 0.f)), p2 = dot(xr[1], xr[1], dot(xr[0], xr[0], 0.f));
      const auto q = __builtin_amdgcn_permlane32_swap(__float_as_uint(p1), __float_as_uint(p2), false, false);
      const float qs = __uint_as_float(q[0]) + __uint_as_float(q[1]);
      const auto q2 = __builtin_amdgcn_permlane16_swap(__float_as_uint(qs), __float_as_uint(qs), false, false);
      float sv = __uint_as_float(q2[0]) + __uint_as_float(q2[1]);
      sv = dpp_add<0x121, 0xf>(dpp_add<0x122, 0xf>(dpp_add<0x124, 0xf>(dpp_add<0x128, 0xf>(sv))));
      lds_wr(lbase + SPM + k * 64 + (lane >> 4) * 16 + hw * 4, f2word(sv));
      u32x4 a, c;
#pragma unroll 1
      for (int i = 0; i < SPIN_MAX; ++i) {
        a = lds_rd4(lbase + SPM + k * 64); c = lds_rd4(lbase + SPM + k * 64 + 32);
        if (__builtin_amdgcn_readfirstlane(umax(umax(a[0], a[1]), umax(a[2], a[3]))) != SENT) break;
      }
      auto sum4 = [&](const u32x4& v) { return (__uint_as_float(v[0]) + __uint_as_float(v[1])) + (__uint_as_float(v[2]) + __uint_as_float(v[3])); };
      const float mean = sum4(a) * (1.0f / 1024.0f), msq = sum4(c) * (1.0f / 1024.0f);
      const float den = rbf(__builtin_amdgcn_sqrtf(rbf(rbf(fmaxf(fmaf(-mean, mean, msq), 0.f)) + 1e-5f)));
      return (unsigned)__builtin_amdgcn_readfirstlane((pack2(mean, mean) & 0xffffu) | (__float_as_uint(den) & 0xffff0000u));
    };
    auto ffn = [&](const u32x2& xr, unsigned md, unsigned n, const __amdgpu_buffer_rsrc_t rs, int yo, auto&& off) __attribute__((always_inline)) {
      gate_up(xr, md);
      if (hw == 0 && n == 2u) STAMP(dk, 8);
      asm volatile("" ::: "memory");
      PIN();
      lds_wr(lbase + MEET + hw * 4, n + off());
      u32x4 mt;
#pragma unroll 1
      for (int i = 0; i < SPIN_MAX; ++i) {
        mt = lds_rd4(lbase + MEET);
        if (__builtin_amdgcn_readfirstlane(min4(mt)) >= n) break;
      }
      asm volatile("" ::: "memory");
      PIN();
      if (hw == 0 && n == 2u) STAMP(dk, 9);
      if (__builtin_amdgcn_readfirstlane(mt[0]) != n + 1u)
        __builtin_amdgcn_raw_buffer_store_b128(down(act_diag(n == 4u ? 512 : 0), w), rs, (yo + fg * D_) * 4 + t * 16, 0, 16);
      dad += n == 2u ? 512 : -512;
      if (hw == 0 && n == 2u) STAMP(dk, 4);
    };
    auto y_sum4 = [&](const __amdgpu_buffer_rsrc_t rs, int yo, int k) __attribute__((always_inline)) {
      const int src = (yo + (hw * 56 + (lane >> 3)) * D_ + oc + (lane & 7) * 4) * 4;
      u32x4 v[7];
#pragma unroll
      for (int i = 0; i < 7; ++i) asm volatile("" : "=v"(v[i]));
      unsigned todo = 0x7fu;
#pragma unroll 1
      for (int spin = 0; spin < SPIN_MAX; ++spin) {
        LOG();
#pragma unroll
        for (int i = 0; i < 7; ++i) if ((todo >> i) & 1u) v[i] = __builtin_amdgcn_raw_buffer_load_b128(rs, src, i * (8 * D_ * 4), 16);
#pragma unroll
        for (int i = 0; i < 7; ++i) if (none(umax(umax(v[i][0], v[i][1]), umax(v[i][2], v[i][3])) == SENT)) todo &= ~(1u << i);
        if (!todo) break;
        __builtin_amdgcn_s_sleep(YSLP);
        asm volatile("" ::: "memory");
      }
      f32x2 z0 = {0.f, 0.f}, z1 = {0.f, 0.f};
#pragma unroll
      for (int i = 0; i < 7; ++i) {
        z0 += f32x2{__uint_as_float(v[i][0]), __uint_as_float(v[i][1])};
        z1 += f32x2{__uint_as_float(v[i][2]), __uint_as_float(v[i][3])};
      }
      const unsigned ya = lbase + SPM + 256 + k * 512 + ((lane & 7) * 4 + (lane >> 4)) * 16;
      lds_wr((lane & 8) ? lbase + DUM + lane * 4 : ya + hw * 4, f2word(fold8(z0, z1)));
      u32x4 q = {0u, 0u, 0u, 0u};
      if (hw == 3) {
#pragma unroll 1
        for (int i = 0; i < 4 * SPIN_MAX; ++i) {
          q = lds_rd4(ya);
          if (none(umax(umax(q[0], q[1]), umax(q[2], q[3])) == SENT)) break;
        }
      }
      return (__uint_as_float(q[0]) + __uint_as_float(q[1])) + (__uint_as_float(q[2]) + __uint_as_float(q[3]));
    };
    const __amdgpu_buffer_rsrc_t rs1 = __builtin_amdgcn_make_buffer_rsrc(hand, 0, HAND_N * 4, 0x00020000);
    u32x2 xr;
    unsigned xo, xo2;
    until(t1 + XDT);
    get_row(fr1, 0, xr, xo);
    if (hw == 0) STAMP(dk, 7);
#define FAILFC 0
    const unsigned md1 = row_md(xr, 0) ^ (FAILFC & 1);
    if (hw == 0) STAMP(dk, 10);
    LOGAT(20);
    u32x2 xr2;
    unsigned md2 = 0u;
    if (p1blk) {
      xr2 = __builtin_amdgcn_raw_buffer_load_b64(rsx, (fr2 >> 7) == omi ? S_ * D_ * 2 : xoff, D_ * 2, 16);
      PIN();
      ffn(xr, md1, 2u, rsh, GRAN_N, [&]() { return 0u; });
      __builtin_amdgcn_s_setprio(R2PRIO);
      LOG();
      if ((fr2 >> 7) == omi || !none(umax(xr2[0], xr2[1]) == SENT)) get_row(fr2, 1, xr2, xo2);
      md2 = row_md(xr2, 1) ^ (FAILFC & 2);
      ffn(xr2, md2, 4u, rs1, GRAN_N, [&]() {
        const u32x2 v = *LDS_AT(const volatile u32x2, lbase + BEST);
        return v[0] != 0u && (2047 - (int)(v[0] & 0x7ffu) != fr2 || v[1] != md2) ? 1u : 0u; });
    }
    if (hw == 0) STAMP(dk, 5);
    LOG();
    float y = 0.f;
    auto put_row = [&](int r, unsigned md, unsigned rb) __attribute__((always_inline)) {
      const float rden = __uint_as_float(md & 0xffff0000u), rs = rbf(frcp(1.0f + __expf(-__uint_as_float(rb << 16))));
      const float xa = __uint_as_float((col & 1) ? xo & 0xffff0000u : xo << 16);
      float ha = xa, hb = xa;
      ln_apply2(ha, hb, __uint_as_float(md << 16), rden, frcp(rden));
      const float o = xa + rbf(ha + rbf(rbf(y) * rs));
      __builtin_amdgcn_raw_buffer_store_b16((unsigned short)(pack2(o, o) & 0xffffu), __builtin_amdgcn_make_buffer_rsrc(out, 0, S_ * D_ * 2, 0x00020000),
                                            (lane & 8) ? S_ * D_ * 2 : (r * D_ + col) * 2, 0, OAUX);
    };
    if (!p1blk) {
      until(tmx + XD2);
      get_row(fr2, 1, xr2, xo2);
      md2 = row_md(xr2, 1) ^ (FAILFC & 2);
      until(t1 + XDT + YD1); until(tmx + YD2);
      if (hw == 3) STAMP(dk, 13);
      LOGAT(8);
      y = y_sum4(rsh, GRAN_N, 0);
      if (hw == 3) STAMP(dk, 14);
      LOGAT(24);
    }
    unsigned bmd = 0u;
    best = 0u;
#pragma unroll 1
    for (int spin = 0; spin < 4 * SPIN_MAX; ++spin) {
      const u32x2 v = *LDS_AT(const volatile u32x2, lbase + BEST);
      best = __builtin_amdgcn_readfirstlane(v[0]); bmd = __builtin_amdgcn_readfirstlane(v[1]);
      if (best) break;
      __builtin_amdgcn_s_sleep(1);
    }
    asm volatile("" ::: "memory");
    const int idx = 2047 - (int)(best & 0x7ffu);
    const unsigned rk = best >> 16, rb = (rk & 0x8000u) ? (rk & 0x7fffu) : (~rk & 0xffffu);
    if (hw == 0) STAMP(dk, 1);
    LOGAT(25);
    LOGOUT(hw);
    if (__builtin_expect(idx != fr1 || bmd != md1, 0)) {
      if (idx == fr2 && bmd == md2) {
        if (p1blk) return;
        y = y_sum4(rs1, GRAN_N, 1);
        xo = xo2;
      } else {
        get_row(idx, idx == fr1 ? 0 : (idx == fr2 ? 1 : 2), xr, xo);
        if (p1blk) ffn(xr, bmd, 6u, rsh, HAND_N + Y3_O, [&]() { return 0u; });
        else y = y_sum4(rsh, HAND_N + Y3_O, 2);
        if (p1blk || hw != 3) return;
#pragma unroll 1
        for (int spin = 0; spin < SPIN_MAX && idx != fr1 && idx != fr2; ++spin) {
          if (__builtin_amdgcn_readfirstlane(ld_ag(gran + GR_F + (oc >> 6) * 32)) == tag) break;
          asm volatile("" ::: "memory");
        }
      }
    }
    if (p1blk || hw != 3) return;
    put_row(idx, bmd, rb);
    }
    return;
  }
  if (wid >= 4) {
    const int lane = fresh_lane();
    const int lw = wid - 4;
    const long long lay_ = ap->lay;
    const u16* A = ap->a; const u16* W = ap->W; const u16* wc = ap->wc;
#define LPRIO 3
#define LP0 3
    __builtin_amdgcn_s_setprio(LP0);
    unsigned va[4], vw[6];
    const unsigned ul = lane;
    va[0] = (unsigned)(m0 + lw * 32) * (K * 2) + (ul >> 3) * (K * 2) + (((ul & 7u) ^ (ul >> 4)) << 4);
    va[1] = va[0] + (8 * K * 2 - 1024);
    va[2] = (va[0] ^ 64u) + 2 * (8 * K * 2 - 1024);
    va[3] = va[2] + (8 * K * 2 - 1024);
#pragma unroll
    for (int i = 0; i < 3; ++i) {
      const unsigned n = ul + 64u * i, k = __umul24(n, 171u) >> 12, cp = n - __umul24(k, 24u);
      vw[i] = __umul24(k, ldw * 2) + (cp >> 3) * 2048u + (((cp & 7u) ^ ((k & 2u) << 1)) << 4) + ((unsigned)(lw * 16 * ldw + n0) * 2u - i * 1024u);
      vw[i + 3] = (vw[i] ^ 16u) + 8 * ldw * 2;
    }
    PIN();
    asm volatile("" : "+v"(va[0]), "+v"(va[1]), "+v"(va[2]), "+v"(va[3]), "+v"(vw[0]), "+v"(vw[1]), "+v"(vw[2]), "+v"(vw[3]), "+v"(vw[4]), "+v"(vw[5]));
    const bool LAY2 = lay_ != 0;
    const int krot = tmi & 8;
    if (LAY2 && slow_cols(W, tni)) W = wc;
    auto stage_a = [&](int kt, int slot) {
      const char* pa = reinterpret_cast<const char*>(A) + (size_t)(kt ^ krot) * 128;
      asm volatile("s_mov_b32 m0, %5\n\ts_nop 0\n\tglobal_load_lds_dwordx4 %0, %4 sc1\n\tglobal_load_lds_dwordx4 %1, %4 offset:1024 sc1\n\t"
                   "global_load_lds_dwordx4 %2, %4 offset:2048 sc1\n\tglobal_load_lds_dwordx4 %3, %4 offset:3072 sc1"
                   :: "v"(va[0]), "v"(va[1]), "v"(va[2]), "v"(va[3]), "s"(pa), "s"(lbase + slot * STB + lw * 4096) : "m0", "memory");
    };
    auto stage_w = [&](int kt, int slot) {
      const char* pw = reinterpret_cast<const char*>(W) + (size_t)(kt ^ krot) * 128 * ldw;
      asm volatile("s_mov_b32 m0, %7\n\ts_nop 0\n\tglobal_load_lds_dwordx4 %0, %6 sc1\n\tglobal_load_lds_dwordx4 %1, %6 offset:1024 sc1\n\t"
                   "global_load_lds_dwordx4 %2, %6 offset:2048 sc1\n\ts_addk_i32 m0, 0xc00\n\ts_nop 0\n\tglobal_load_lds_dwordx4 %3, %6 sc1\n\t"
                   "global_load_lds_dwordx4 %4, %6 offset:1024 sc1\n\tglobal_load_lds_dwordx4 %5, %6 offset:2048 sc1"
                   :: "v"(vw[0]), "v"(vw[1]), "v"(vw[2]), "v"(vw[3]), "v"(vw[4]), "v"(vw[5]), "s"(pw), "s"(lbase + slot * STB + ABYTES + lw * 6144) : "m0", "scc", "memory");
    };
    if (LAY2) {
      __builtin_amdgcn_s_setprio(0);
      __builtin_amdgcn_s_sleep(TPRE - 10);
      tile_at(3u);
      __builtin_amdgcn_s_setprio(LPRIO);
#pragma unroll
      for (int st = 0; st < NST; ++st) stage_w(st, st);
      PIN();
      __builtin_amdgcn_s_barrier();
      PIN();
#pragma unroll
      for (int st = 0; st < NST; ++st) stage_a(st, st);
      asm volatile("s_waitcnt vmcnt(8)" ::: "memory");
    } else {
      __builtin_amdgcn_s_setprio(LPRIO);
      stage_a(0, 0); stage_w(0, 0);
      if (PRO > 1) { stage_a(1, 1); stage_w(1, 1); }
      if (PRO > 2) { stage_a(2, 2); stage_w(2, 2); }
      asm volatile("s_waitcnt vmcnt(%0)" :: "n"(10 * PRO - 10) : "memory");
    }
    int slot = 0;
    __builtin_amdgcn_s_barrier();
    if (!LAY2) {
      if (PRO < 2) { stage_a(1, 1); stage_w(1, 1); }
      if (PRO < 3) { stage_a(2, 2); stage_w(2, 2); }
    }
    for (int kt = 0; kt + 1 < NKT; ++kt) {
      if (LAY2 && kt == 0) asm volatile("s_waitcnt vmcnt(4)" ::: "memory");
      else if (kt + 2 >= NKT) asm volatile("s_waitcnt vmcnt(0)" ::: "memory");
      else asm volatile("s_waitcnt vmcnt(10)" ::: "memory");
      __builtin_amdgcn_s_barrier();
      if (kt + NST < NKT) { stage_a(kt + NST, slot); stage_w(kt + NST, slot); }
      slot = slot == NST - 1 ? 0 : slot + 1;
    }
    __builtin_amdgcn_s_setprio(0);
    __builtin_amdgcn_s_barrier();
  } else {
  LAYER_HERE;
  const int dk = LAY2 ? 5 : 1;
  const int lane = fresh_lane(), wm = wid & 1, wn = wid >> 1;
  if (wid == 0) STAMP(dk, 0);
  f32x4 acc[4][6];
#pragma unroll
  for (int i = 0; i < 4; ++i)
#pragma unroll
    for (int j = 0; j < 6; ++j) acc[i][j] = f32x4{0.f, 0.f, 0.f, 0.f};
  const int q16 = lane & 15, g4 = lane >> 4;
  const int e0 = 16 * (q16 >> 3) + (q16 & 7);
  s16x8 af[2][4];
  unsigned aad[2], wad[2];
  {
    const int row = wm * 64 + e0, swz = ((row >> 1) & 3) | (((row >> 4) & 1) << 2);
#pragma unroll
    for (int h = 0; h < 2; ++h) aad[h] = lbase + row * 128 + (((4 * h + g4) ^ swz) << 4);
    const int tq = (lane & 15) >> 2, tp = lane & 3, k = 8 * g4 + tq, ch = wn * 4 + 2 * (tp >> 1);
    wad[0] = lbase + ABYTES + k * 384 + ((ch ^ (4 * ((k >> 1) & 1)) ^ (g4 & 1)) << 4) + 8 * (tp & 1);
    wad[1] = wad[0] ^ 16;
  }
  auto lda = [&](unsigned a, int i) { return *LDS_AT(const s16x8, a + (i >> 1) * 4096 + (i & 1) * 1024); };
  auto ldw_ = [&](int h, int j) {
    const unsigned w = wad[j & 1] + h * 12288 + (j >> 1) * 128;
    s16x4 lo = __builtin_amdgcn_ds_read_tr16_b64_v4i16(LDS_AT(s16x4, w));
    s16x4 h4 = __builtin_amdgcn_ds_read_tr16_b64_v4i16(LDS_AT(s16x4, w + 1536));
    return (s16x8)__builtin_shufflevector(lo, h4, 0, 1, 2, 3, 4, 5, 6, 7);
  };
  ARGS_HERE;
  unsigned lto = lbase + wid * 4096;
  char* kvb = reinterpret_cast<char*>(KV) + ((tni * 2 + wn) * 64 + tmi * 4 + wm * 2) * 2048 - lto;
  {
    const unsigned ato = lbase + ALPHA + wm * 256, dum = lbase + DUM + lane * 4;
    *LDS_AT(u32x4, lto + QOFF + lane * 16) = u32x4{lane >= 16 ? lbase + CRED : lbase + TABA + (wm * 64 + e0) * 16, lbase + TABW + (wn * 32 + e0) * 16,
                                                   ato + e0 * 4, lto + (lane >> 5) * 1024 + ((lane & 28) * 2 + (lane & 3)) * 16};
    *LDS_AT(u32x2, lto + QOFF + 2048 + lane * 16) = u32x2{aad[0], aad[1]};
    *LDS_AT(u32x4, lto + QOFF + 1024 + lane * 16) = u32x4{ato + g4 * 16 + (lane >> 5) * 32, lto + (lane >> 5) * 1024 + ((lane & 24) * 2 + (lane & 7)) * 16,
                                                          lane == 0 ? lbase + FLG + 32 : dum, lane == 0 ? lbase + FLG + 16 : dum};
  }
  asm volatile("" : "+s"(lto), "+s"(kvb) :: "memory");
#pragma unroll
  for (int i = 0; i < 4; ++i)
#pragma unroll
    for (int j = 0; j < 6; ++j) asm volatile("" : "+v"(acc[i][j]));
  asm volatile("" : "+v"(aad[0]), "+v"(aad[1]), "+v"(wad[0]), "+v"(wad[1]));
  if (LAY2) __builtin_amdgcn_s_barrier();
  __builtin_amdgcn_s_barrier();
  PIN();
  if (wid == 0) STAMP(dk, 1);
  s16x8 wq[4][2];
  wq[0][0] = ldw_(0, 0); af[0][0] = lda(aad[0], 0); wq[0][1] = ldw_(1, 0); af[1][0] = lda(aad[1], 0); wq[1][0] = ldw_(0, 1); wq[1][1] = ldw_(1, 1);
  af[0][1] = lda(aad[0], 1); af[1][1] = lda(aad[1], 1); af[0][2] = lda(aad[0], 2); af[1][2] = lda(aad[1], 2);
  PIN();
  s16x4 wlo;
  auto rw = [&](s16x8& d, int h, int j, int hi, int off = 0) __attribute__((always_inline)) {
    const s16x4 v = __builtin_amdgcn_ds_read_tr16_b64_v4i16(LDS_AT(s16x4, wad[j & 1] + h * 12288 + (j >> 1) * 128 + hi * 1536 + off));
    if (hi) d = (s16x8)__builtin_shufflevector(wlo, v, 0, 1, 2, 3, 4, 5, 6, 7);
    else wlo = v;
  };
  auto at = [&](s16x8& x, int r) __attribute__((always_inline)) {
    if (r == 0) asm volatile("" :: "{v[104:107]}"(x));
    if (r == 1) asm volatile("" :: "{v[108:111]}"(x));
    if (r == 2) asm volatile("" :: "{v[112:115]}"(x));
    if (r == 3) asm volatile("" :: "{v[116:119]}"(x));
    if (r == 4) asm volatile("" :: "{v[120:123]}"(x));
    if (r == 5) asm volatile("" :: "{v[124:127]}"(x));
    if (r == 6) asm volatile("" :: "{v[128:131]}"(x));
    if (r == 7) asm volatile("" :: "{v[132:135]}"(x));
    if (r == 8) asm volatile("" :: "{v[136:139]}"(x));
    if (r == 9) asm volatile("" :: "{v[140:143]}"(x));
    if (r == 10) asm volatile("" :: "{v[144:147]}"(x));
    if (r == 11) asm volatile("" :: "{v[148:151]}"(x));
    if (r == 12) asm volatile("" :: "{v[152:155]}"(x));
    if (r == 13) asm volatile("" :: "{v[156:159]}"(x));
    if (r == 14) asm volatile("" :: "{v[160:163]}"(x));
    if (r == 15) asm volatile("" :: "{v[164:167]}"(x));
  };
  auto mm = [&](int i, int j, int h, int sl, int hw_) __attribute__((always_inline)) {
    s16x8 &a = af[h][i], &w = wq[sl][hw_];
    at(a, h * 4 + i); at(w, 8 + sl * 2 + hw_);
    if (j >= 4) asm volatile("v_mfma_f32_16x16x32_bf16 %0, %1, %2, %0" : "+v"(acc[i][j]) : "v"(a), "v"(w));
    else asm volatile("v_mfma_f32_16x16x32_bf16 %0, %1, %2, %0" : "+v"(acc[i][j]) : "v"(w), "v"(a));
    PIN();
  };
  auto next = [&](int g, int J0, int s0, int s1, int off) __attribute__((always_inline)) {
    if (g == 2 || g == 3) rw(wq[s0][0], 0, J0, g & 1, off);
    if (g == 4 || g == 5) af[g & 1][0] = lda(aad[g & 1] + off, 0);
    if (g == 6 || g == 7) rw(wq[s0][1], 1, J0, g & 1, off);
    if (g >= 8 && g < 12) rw(wq[s1][(g - 8) >> 1], (g - 8) >> 1, J0 + 1, g & 1, off);
    if (g >= 12) af[g & 1][(g - 10) >> 1] = lda(aad[g & 1] + off, (g - 10) >> 1);
  };
  auto third = [&](int p, int J0, int delta) __attribute__((always_inline)) {
    auto mv = [&](unsigned& x) __attribute__((always_inline)) { asm volatile("v_add_u32 %0, %1, %0" : "+v"(x) : "s"(delta)); };
#pragma unroll
    for (int g = 0; g < 16; ++g) {
      const int h = g & 1, i = p == 1 ? (g >> 1) & 3 : g >> 2, t = p == 1 ? g >> 3 : (g >> 1) & 1, j = 2 * p + t, sl = p == 1 ? 2 - 2 * t : p + t;
      mm(i, j, h, sl, h);
      if (p == 0) {
        if (g < 2) af[g][3] = lda(aad[g], 3);
        else if (g < 6) rw(wq[2][(g - 2) >> 1], (g - 2) >> 1, 2, g & 1);
        else if (g < 10) rw(wq[3][(g - 6) >> 1], (g - 6) >> 1, 5, g & 1);
        else if (g < 12) mv(aad[g - 10]);
      }
      if (p == 1) {
        if (g < 4) rw(wq[0][g >> 1], g >> 1, 3, g & 1);
        if (g == 4) mv(wad[1]);
        if (g >= 8 && g < 12) rw(wq[2][(g - 8) >> 1], (g - 8) >> 1, 4, g & 1);
        if (g == 12) mv(wad[0]);
      }
      if (p == 2) {
        if (g == BARG) {
          asm volatile("" ::: "memory");
          PIN();
          __builtin_amdgcn_s_barrier();
          asm volatile("" ::: "memory");
        }
        next(g, J0, 0, 1, 0);
      }
      PIN();
      {
        if ((p == 2 && g == 15) || (p == 0 && g == 7)) __builtin_amdgcn_s_waitcnt(0xc67f);
        if (p == 0 && g == 2) __builtin_amdgcn_s_waitcnt(0xc57f);
        if ((p == 0 && g == 14) || (p == 1 && (g & 7) == 7)) __builtin_amdgcn_s_waitcnt(0xc07f);
        PIN();
      }
    }
  };
  third(0, 0, STB); third(1, 0, STB);
  {
    static_assert((NKT - NST - 1) % NST == 0, "");
#pragma unroll 1
    for (int kt = 0; kt < NKT - NST - 1; kt += NST) {
#pragma unroll
      for (int u = 0; u < NST; ++u) {
        const int delta = u == 1 ? -(NST - 1) * STB : STB;
        third(2, 0, 0); third(0, 0, delta); third(1, 0, delta);
      }
    }
  }
  static_assert(NKT % NST == 1, "slots of the last stages");
#define TT(i)
  auto bar = [&]() { PIN(); __builtin_amdgcn_s_barrier(); asm volatile("" ::: "memory"); PIN(); };
  TT(0);
  third(2, 2, 0);
  const __amdgpu_buffer_rsrc_t rsk = __builtin_amdgcn_make_buffer_rsrc(kvb, 0, 0x7fffffff, 0x00020000), rsv = __builtin_amdgcn_make_buffer_rsrc(kvb + VOFF, 0, 0x7fffffff, 0x00020000);
  unsigned pk;
  u32x2 t0, t1, t2, t3;
  auto park = [&](int i) { return *LDS_AT(const volatile u32x2, pk + (i >> 1) * 1024 + (i & 1) * 8); };
  float al[4];
  f32x4 av[4];
  u32x4 kvp[12];
  auto cacc = [&](int p, int x, int t) -> f32x4& { return p < 2 ? acc[x][2 * p + t] : acc[(x >> 1) * 2 + t][4 + (x & 1)]; };
  auto signal = [&](unsigned v) { __hip_atomic_fetch_add(LDS_AT(unsigned, t3[0]), v, __ATOMIC_RELAXED, __HIP_MEMORY_SCOPE_WORKGROUP); PIN(); };
  auto outop = [&](int p, int k) {
    if (k >= 0 && k < 16) {
      const int x = k >> 2, t = (k >> 1) & 1;
      f32x4& c = cacc(p, x, t);
#pragma unroll
      for (int o = 2 * (k & 1); o < 2 * (k & 1) + 2; ++o) {
        float r;
        asm volatile("v_mul_f32 %0, %1, %2" : "=v"(r) : "v"(c[o]), "v"(p < 2 ? al[x] : av[(x >> 1) * 2 + t][o]));
        c[o] = r;
      }
      PIN();
    }
    if (k >= 1 && k <= 16) {
      const int x = (k - 1) >> 2, t = ((k - 1) >> 1) & 1, o = (k - 1) & 1;
      kvp[p * 4 + x][2 * t + o] = pack2(cacc(p, x, t)[2 * o], cacc(p, x, t)[2 * o + 1]);
      PIN();
      if (t && o) {
        if (p) __builtin_amdgcn_raw_buffer_store_b128(kvp[p * 4 + x], p == 1 ? rsk : rsv, (p == 1 ? t1[1] : t2[1]) + (x >> 1) * 2048 + (x & 1) * (p == 1 ? 64 : 128), 0, 16);
        else *LDS_AT(u32x4, t2[1] + (x >> 1) * 2048 + (x & 1) * 128) = kvp[x];
        PIN();
      }
    }
  };
  static_for<0, 168>([&](auto nc) __attribute__((always_inline)) {
    constexpr int n = decltype(nc)::value, sec = n < 80 ? n >> 4 : (n < 104 ? 5 : 6 + ((n - 104) >> 4)), g = n < 104 ? n - 16 * sec : (n - 104) & 15;
    auto taba = [&](int i) { af[0][i] = *LDS_AT(const s16x8, t0[0] + (i >> 1) * 512 + (i & 1) * 128); };
    auto tabw = [&](int j) { wq[(2 + (j >> 1)) & 3][j & 1] = *LDS_AT(const s16x8, t0[1] + ((j >> 1) * 64 + (j & 1) * 8) * 16); };
    auto rwn = [&](int e, int sl, int j, int off) { if (e >= 0 && e < 4) rw(wq[sl][e >> 1], e >> 1, j, e & 1, off); };
    if (g == 0) TT(1 + sec);
    if (sec == 5) {
      const int i = (g / 12) * 2 + (g & 1), j = (g % 12) >> 1;
      mm(i, j, 0, (2 + (j >> 1)) & 3, j & 1);
    } else {
      constexpr int part[10] = {1, 2, 1, 2, 0, 0, 1, 2, 0, 0}, s0[10] = {0, 2, 0, 2, 0, 0, 1, 3, 1, 3};
      const int h = g & 1, i = sec == 7 ? (g >> 3) * 2 + ((g >> 1) & 1) : g >> 2, t = sec == 7 ? (g >> 2) & 1 : (g >> 1) & 1;
      mm(i, 2 * part[sec] + t, h, (s0[sec] + t) & 3, h);
    }
    if (sec == 0 || sec == 2) {
      if (g < 2) af[g][3] = lda(aad[g] + sec / 2 * STB, 3);
      rwn(g - 2, 2, 4, sec / 2 * STB); rwn(g - 6, 3, 5, sec / 2 * STB);
    }
    if (sec == 1) {
      if (g == 1) bar();
      next(g, 2, 0, 1, STB);
    }
    if ((sec == 3 && g == 12) || (sec == 6 && g == 13)) { pk = lto + QOFF + fresh_lane() * 16; asm volatile("" : "+v"(pk)); }
    if (sec == 3) {
      rwn(g, 0, 0, STB); rwn(g - 4, 1, 1, STB);
      if (g >= 8 && g < 12) asm volatile("v_subrev_u32 %0, %1, %0" : "+v"(g < 10 ? aad[g & 1] : wad[g & 1]) : "s"(STB));
      if (g == 13) t1 = park(1);
      if (g == 14) t0 = park(0);
    }
    if (sec == 4) {
      if (g == B15) bar();
      if (g == 4) taba(0);
      if (g == 5 || g == 6) tabw(g - 5);
      if (g == 7) taba(1);
      if (g == 8 || g == 9) tabw(g - 6);
      if (g == 10 || g == 11) af[1][g - 10] = lda(aad[1], g - 10);
      if (g == 12) taba(2);
      if (g == 15) taba(3);
    }
    if (sec == 5) {
      if (g < 2) tabw(4 + g);
      rwn(g - 2, 1, 2, 0);
      if (g == 12 || g == 13) af[0][g - 12] = lda(aad[0], g - 12);
      if (g == 22) { al[0] = *LDS_AT(const float, t1[0]); al[1] = *LDS_AT(const float, t1[0] + 32); }
      rwn(g - 16, 2, 3, 0);
      if (g == 20 || g == 21) af[1][g - 18] = lda(aad[1], g - 18);
    }
    if (sec == 6) {
      if (g < 2) af[0][2 + g] = lda(aad[0], 2 + g);
      rwn(g - 2, 3, 4, 0);
      if (g == 9) { al[2] = *LDS_AT(const float, t1[0] + 128); al[3] = *LDS_AT(const float, t1[0] + 160); }
      if (g == 14) t2 = park(2);
      if ((g & 3) >= 2) rwn((g - 10) / 4 * 2 + (g & 1) - (g < 10) * 8, 0, 5, 0);
    }
    if (sec == 7) {
      if (g == 2 || g == 3 || g == 10 || g == 11) av[(g >> 3) * 2 + (g & 1)] = *LDS_AT(const f32x4, t2[0] + ((g >> 3) * 32 + (g & 1) * 8) * 4);
      rwn(g < 2 ? g : (g < 4 ? -1 : g - 2), 1, 0, 0); rwn(g - 6, 2, 1, 0);
      if (g == 12) t3 = park(3);
      if (g == 13) { const u32x2 a = park(4); aad[0] = a[0]; aad[1] = a[1]; }
      if (g == 15) {
        asm volatile("" ::: "memory");
        __builtin_amdgcn_s_waitcnt(0xc07f);
        bar();
      }
    }
    if (sec == 8) next(g, 0, 3, 0, STB);
    if (sec == 9) {
      if (g < 2) af[g][3] = lda(aad[g] + STB, 3);
      if (g == 2) signal(0x10000u);
    }
    PIN();
    outop(1, n - 104 - KO);
    outop(2, n - 120 - KO);
    {
      constexpr int wg[8] = WGAPS;
#pragma unroll
      for (int e = 0; e < 8; ++e)
        if (n - 136 == wg[e]) {
          *LDS_AT(u32x4, (e < 4 ? t1[1] + (e & 1) * 64 : t2[1] + 2048 + (e & 1) * 128) + ((e >> 1) & 1) * ASB) = kvp[4 + e];
          PIN();
        }
    }
    if (n == 152 + 3 || n == 152 + 4) {
      al[2 * (n & 1)] *= QSCALE; al[2 * (n & 1) + 1] *= QSCALE;
      asm volatile("" : "+v"(al[2 * (n & 1)]), "+v"(al[2 * (n & 1) + 1]));
      if (n & 1) asm volatile("v_add_u32 %0, %1, %0" : "+v"(t2[1]) : "s"(QOFF));
      PIN();
    }
    outop(0, n - 152 - KO);
  });
  TT(13);
  if (wid == 0) STAMP(dk, 18);
#pragma unroll
  for (int k = 16 - KO; k <= 16; ++k) outop(0, k);
  __hip_atomic_fetch_add(LDS_AT(unsigned, t3[1]), 1u, __ATOMIC_RELAXED, __HIP_MEMORY_SCOPE_WORKGROUP);
  TT(14);
  asm volatile("s_waitcnt vmcnt(0)" ::: "memory");
  signal(1u);
  TT(15);
  if (wid == 0) STAMP(dk, 19);
  }
  PIN();
  {
  const int lane = fresh_lane(), tid = wid * 64 + lane;
  LAYER_HERE;
  const int dk = LAY2 ? 6 : 2;
  const int g = wid >> 2, hh = (wid >> 1) & 1, qg = wid & 1;
  const int head = tni * 2 + hh, qt = tmi * 4 + qg * 2;
  unsigned qaa = lbase + QOFF + (hh * 2 + qg) * 4096 + lane * 16 + (g ? 0 : 2048), qab = qaa ^ 2048;
  const unsigned wbase = lbase + (hh * 2 + g) * 4096 + lane * 16;
  auto sptr = [&](int sl) { unsigned a = wbase + sl * ASB; asm volatile("" : "+v"(a)); return LDS_AT(const s16x8, a); };
  auto rd = [&](__attribute__((address_space(3))) const s16x8* sp, int off) { return sp[off >> 4]; };
  auto sl0 = sptr(0);
  const unsigned arr_a = lane == 0 ? lbase + FLG + 16 : lbase + DUM + lane * 4;
  auto arrive = [&]() { __hip_atomic_fetch_add(LDS_AT(unsigned, arr_a), 1u, __ATOMIC_RELAXED, __HIP_MEMORY_SCOPE_WORKGROUP); };
  unsigned arr_w = lane == 0 ? lbase + FLG + 8 + g * 4 : lbase + DUM + lane * 4;
  unsigned arr_i = 1u << (8 * (wid & 3));
  auto arrive_w = [&]() { __hip_atomic_fetch_add(LDS_AT(unsigned, arr_w), arr_i, __ATOMIC_RELAXED, __HIP_MEMORY_SCOPE_WORKGROUP); };
  s16x8 sel;
#pragma unroll
  for (int e = 0; e < 8; ++e) sel[e] = ((lane & 7) == (e & 4)) ? (short)0x3F80 : (short)0;
  int sidx = 0xDDDDDDDD;
  f32x16 oa, ob, nn, sa, sb;
  f32x4 la = {0.f, 0.f, 0.f, 0.f}, lb = {0.f, 0.f, 0.f, 0.f};
#pragma unroll
  for (int e = 0; e < 16; ++e) { oa[e] = 0.f; ob[e] = 0.f; sa[e] = 0.f; sb[e] = 0.f; }
  asm volatile("" : "+v"(sidx), "+v"(sel), "+v"(oa), "+v"(ob), "+v"(la), "+v"(lb), "+v"(qaa), "+v"(qab), "+v"(arr_w), "+v"(arr_i));
  PIN();
  s16x8 qa0, qa1, qb0, qb1, k00, k01, v00, v01, v10, v11, k10, k11;
  u32x4 fw;
  auto first = [&]() {
    fw = lds_rd4(lbase + FLG + 16);
    asm volatile("" ::: "memory");
    k00 = rd(sl0, 0); qa0 = *LDS_AT(const s16x8, qaa); qb0 = *LDS_AT(const s16x8, qab);
    k01 = rd(sl0, 1024); qa1 = *LDS_AT(const s16x8, qaa + 1024); qb1 = *LDS_AT(const s16x8, qab + 1024);
    v00 = rd(sl0, 2048); v01 = rd(sl0, 3072);
    PIN();
  };
  first();
  if (__builtin_amdgcn_readfirstlane(fw[0]) < 4u) {
#pragma unroll 1
    for (int i = 0; i < SPIN_MAX; ++i) {
      if (__builtin_amdgcn_readfirstlane(*LDS_AT(const volatile unsigned, lbase + FLG + 16)) >= 4u) break;
      __builtin_amdgcn_s_sleep(ASLP);
    }
    asm volatile("" ::: "memory");
    first();
  }
  const unsigned tag = __builtin_amdgcn_readfirstlane(fw[1]);
  const int idx = LAY2 ? (2047 - (int)(__builtin_amdgcn_readfirstlane(fw[3]) & 0xffffu)) & 2047 : -64;
  float ma;
  const float zoff = __uint_as_float(__builtin_amdgcn_readfirstlane(LAY2 && g == ((idx >> 6) & 1) ? 0u : 0xff800000u));
  {
    sa = MFMA32(k00, qa0, sa); sb = MFMA32(k00, qb0, sb);
    sa = MFMA32(k01, qa1, sa); sb = MFMA32(k01, qb1, sb);
    float mb = sb[0];
    ma = sa[0];
#pragma unroll
    for (int e = 1; e < 16; ++e) { ma = fmaxf(ma, sa[e]); mb = fmaxf(mb, sb[e]); }
    ma = fmaxf(ma, mb);
    const auto sw = __builtin_amdgcn_permlane32_swap(__float_as_uint(ma), __float_as_uint(ma), false, false);
    ma = fmaxf(__uint_as_float(sw[0]), __uint_as_float(sw[1]));
    f32x2 n2 = {-ma, -ma};
    asm volatile("" : "+v"(n2));
#pragma unroll
    for (int e = 0; e < 16; ++e) nn[e] = n2[e & 1];
    sa = MFMA32(k00, qa0, nn); sa = MFMA32(k01, qa1, sa);
    la[0] = lb[0] = -__uint_as_float(__float_as_uint(__builtin_amdgcn_exp2f(zoff - ma)) & 0xffff0000u);
  }
#define SB() __builtin_amdgcn_sched_barrier(0)
#define EX(i) sc[i] = __builtin_amdgcn_exp2f(sc[i])
  auto unit = [&](f32x16& sc, f32x16& sn, const s16x8& kn0, const s16x8& kn1, const s16x8& qn0, const s16x8& qn1,
                  f32x16& op, f32x4& lp, const s16x8& vp,
                  f32x16& oc, f32x4& lc, const s16x8& vc, auto&& mid) {
    lp = SMF16(sel, PHI(sn), lp); SB();
    op = SMF32(vp, PHI(sn), op); SB();
    EX(0); EX(1); EX(2); EX(3); SB();
    sn = MFMA32(kn0, qn0, nn); SB();
    EX(4); EX(5); EX(6); EX(7); SB();
    sn = MFMA32(kn1, qn1, sn); SB();
    mid();
    EX(8); EX(9); EX(10); EX(11); SB();
    lc = SMF16(sel, PLO(sc), lc); SB();
    oc = SMF32(vc, PLO(sc), oc); SB();
    EX(12); EX(13); EX(14); EX(15); SB();
    asm volatile("" : "+v"(sc));
  };
  sl0 = sptr(1);
  unsigned f0 = 0u;
  auto check = [&](unsigned need) {
    if (__builtin_expect(__builtin_amdgcn_readfirstlane(f0) < need, 0)) {
#pragma unroll 1
      for (int i = 0; i < SPIN_MAX; ++i) {
        if (__builtin_amdgcn_readfirstlane(*LDS_AT(const volatile unsigned, lbase + FLG)) >= need) break;
        __builtin_amdgcn_s_sleep(1);
      }
    }
    asm volatile("" ::: "memory");
  };
#define PAT 0x22221110
#define SAW8(i) do { if (((PAT >> (28 - 4 * (i))) & 3) != ((PAT >> ((32 - 4 * (i)) & 31)) & 3)) { __builtin_amdgcn_s_setprio((PAT >> (28 - 4 * (i))) & 3); SB(); } } while (0)
#define SAW(i) do { if (odd) SAW8(4 + (i)); else SAW8(i); } while (0)
  auto three = [&](int st, bool odd) {
    SAW(0);
    unit(sa, sb, k00, k01, qb0, qb1, ob, lb, v11, oa, la, v00, [&]() { k10 = rd(sl0, 0); k11 = rd(sl0, 1024); SB(); });
    SAW(1);
    unit(sb, sa, k10, k11, qa0, qa1, oa, la, v01, ob, lb, v00, [&]() { v10 = rd(sl0, 2048); v11 = rd(sl0, 3072); SB(); });
    sl0 = sptr((2 * st + 2) & 7);
    SB();
    SAW(2);
    unit(sa, sb, k10, k11, qb0, qb1, ob, lb, v01, oa, la, v10, [&]() { k00 = rd(sl0, 0); k01 = rd(sl0, 1024); SB(); });
  };
  auto fourth = [&](int st, bool odd) {
    SAW(3);
    unit(sb, sa, k00, k01, qa0, qa1, oa, la, v11, ob, lb, v10, [&]() {
      v00 = rd(sl0, 2048); v01 = rd(sl0, 3072);
      f0 = *LDS_AT(const volatile unsigned, lbase + FLG);
      SB();
      arrive_w();
      SB(); });
    check(st >= 14 ? 32u : 2u * st + 5u);
    sl0 = sptr((2 * st + 3) & 7);
    SB();
  };
  SB();
  SAW8(0);
  {
    f32x16& sc = sa;
    sb = MFMA32(k00, qb0, nn); SB();
    k10 = rd(sl0, 0); k11 = rd(sl0, 1024); SB();
    sb = MFMA32(k01, qb1, sb); SB();
    EX(0); EX(1); EX(2); EX(3); EX(4); EX(5); EX(6); EX(7); EX(8); SB();
    oa = SMF32(v00, PLO(sa), oa); SB();
    EX(9); EX(10); EX(11); SB();
    la = SMF16(sel, PLO(sa), la); SB();
    EX(12); EX(13); EX(14); EX(15); SB();
    asm volatile("" : "+v"(sa));
  }
  SAW8(1);
  unit(sb, sa, k10, k11, qa0, qa1, oa, la, v01, ob, lb, v00, [&]() { v10 = rd(sl0, 2048); v11 = rd(sl0, 3072); SB(); });
  sl0 = sptr(2);
  SB();
  SAW8(2);
  unit(sa, sb, k10, k11, qb0, qb1, ob, lb, v01, oa, la, v10, [&]() {
    f0 = *LDS_AT(const volatile unsigned, lbase + FLG);
    asm volatile("" ::: "memory");
    k00 = rd(sl0, 0); k01 = rd(sl0, 1024); SB(); });
  if (__builtin_amdgcn_readfirstlane(f0) < 3u) { check(3u); k00 = rd(sl0, 0); k01 = rd(sl0, 1024); }
  SB();
  SAW8(3);
  unit(sb, sa, k00, k01, qa0, qa1, oa, la, v11, ob, lb, v10, [&]() {
    v00 = rd(sl0, 2048); v01 = rd(sl0, 3072); f0 = *LDS_AT(const volatile unsigned, lbase + FLG); SB(); arrive_w(); SB(); });
  check(5u);
  sl0 = sptr(3);
  SB();
  three(1, true);
#pragma unroll 1
  for (int st = 2; st < 16; st += 2) { fourth(st - 1, true); three(st, false); fourth(st, false); three(st + 1, true); }
  SAW8(7);
  unsigned vr, tk, tv;
  u32x4 f4;
  auto table = [&]() {
    f4 = lds_rd4(lbase + FLG);
    k00 = *LDS_AT(const volatile s16x8, tk); k01 = *LDS_AT(const volatile s16x8, tk + 32);
    vr = *LDS_AT(const volatile u16, tv);
  };
  unsigned xw, xm;
  u32x2 ml;
  u32x4 o2[4];
  auto take = [&]() {
    ml = *LDS_AT(const volatile u32x2, xm);
#pragma unroll
    for (int e = 0; e < 4; ++e) o2[e] = lds_rd4(xw + e * 1024);
  };
  float pzt;
  int dso;
  f32x16& sc = sb;
#define EXP() do { asm volatile("" : "+v"(sc)); SB(); } while (0)
  oa = SMF32(v11, PHI(sa), oa); SB();
  tk = lbase + TAB + 128 + hh * 64 + (lane >> 5) * 16; tv = lbase + TAB + 256 + hh * 64 + (lane & 31) * 2;
  table();
  SB();
  EX(0); EX(1); EXP();
  la = SMF16(sel, PHI(sa), la); SB();
  EX(2); EX(3); EX(4); EXP();
  xw = wbase + (qg ? QOFF : 6 * ASB); xm = lbase + XML + wid * 512 + lane * 8;
  pzt = __builtin_amdgcn_exp2f(nn[0] + (zoff - 8.f));
  asm volatile("" : "+v"(xw), "+v"(xm), "+v"(pzt), "+v"(f4));
  SB();
  {
    const unsigned ph = LAY2 ? 0x79797979u : 0x80808080u, pg = qg ? 0u : 0x80u << (16 * hh + 8);
    auto ready = [&]() {
      return (((__builtin_amdgcn_readfirstlane(f4[1]) + ph) & 0x80808080u) == 0x80808080u) & (((__builtin_amdgcn_readfirstlane(g ? f4[3] : f4[2]) + 0x71717171u) & pg) == pg);
    };
    if (__builtin_expect(!ready(), 0)) {
#pragma unroll 1
      for (int i = 0; i < SPIN_MAX; ++i) {
        __builtin_amdgcn_s_sleep(1);
        table();
        if (ready()) break;
      }
    }
    asm volatile("" ::: "memory");
  }
  SB();
  sa = MFMA32(k00, qb0, nn); asm volatile("" : "+v"(sa)); SB();
#pragma unroll
  for (int e = 0; e < 4; ++e) *LDS_AT(volatile f32x4, xw + e * 1024) = f32x4{oa[4 * e], oa[4 * e + 1], oa[4 * e + 2], oa[4 * e + 3]};
  *LDS_AT(volatile u32x2, xm) = u32x2{__float_as_uint(ma), f2word(pzt <= la[0] ? la[0] : 0.f)};
  SB();
  EX(5); EX(6); EX(7); EXP();
  sa = MFMA32(k01, qb1, sa); asm volatile("" : "+v"(sa), "+v"(vr)); SB();
  ARGS_HERE;
  EX(8); EX(9); EX(10); EXP();
  ob = SMF32(v10, PLO(sb), ob); SB();
  EX(11); EX(12); EX(13); EXP();
  lb = SMF16(sel, PLO(sb), lb); SB();
  const bool used = !LAY2 || (qt + g) * 32 + (lane & 31) != idx;
  dso = used ? (((qt + g) * 32 + head) * 1024 + lane * 8) * 2 : S_ * D_ * 2;
  xw += g ? -4096 : 4096; xm += g ? -2048 : 2048;
  asm volatile("" : "+v"(dso), "+v"(xw), "+v"(xm));
  SB();
  EX(14); EX(15); EXP();
  take();
  SB();
  lb = SMF16(sel, PHI(sb), lb); SB();
  ob = SMF32(v11, PHI(sb), ob); SB();
  __builtin_amdgcn_s_setprio(EPRIO);
  TE(0, 0); TE(1, 0); TE(2, 0); TE(3, 0); TE(7, 0);
  float lsb = lb[0];
  if (LAY2) {
    sa[0] = __builtin_amdgcn_exp2f(sa[0]);
    ob = SMF32((u32x4{lane < 32 ? vr : 0u, 0u, 0u, 0u}), PLO(sa), ob);
    lsb += __uint_as_float(__float_as_uint(sa[0]) & 0xffff0000u);
    lsb = pzt <= lsb ? lsb : 0.f;
  }
  const __amdgpu_buffer_rsrc_t rso = __builtin_amdgcn_make_buffer_rsrc(Of, 0, S_ * D_ * 2, 0x00020000);
  TE(0, 1);
  if (__builtin_amdgcn_ballot_w64(ml[1] == SENT) != 0ull) {
#pragma unroll 1
    for (int i = 0; i < SPIN_MAX; ++i) {
      __builtin_amdgcn_s_sleep(XSLP);
      if (!__builtin_amdgcn_ballot_w64(*LDS_AT(const volatile unsigned, xm + 4) == SENT)) break;
    }
    asm volatile("s_waitcnt lgkmcnt(0)" ::: "memory");
    take();
  }
  arrive();
  TE(0, 2);
  TE(0, 3); TE(1, 1);
  {
    const float m2 = __uint_as_float(ml[0]), l2 = __uint_as_float(ml[1]);
    const float mx = fmaxf(ma, m2);
    const float f1 = __builtin_amdgcn_exp2f(ma - mx), f2 = __builtin_amdgcn_exp2f(m2 - mx);
    const float inv = frcp(lsb * f1 + l2 * f2);
    const float c1 = f1 * inv, c2 = f2 * inv;
    f32x2 r[8];
#pragma unroll
    for (int e = 0; e < 8; ++e) {
      const unsigned x0 = o2[e >> 1][(e & 1) * 2], x1 = o2[e >> 1][(e & 1) * 2 + 1];
      r[e] = f32x2{__uint_as_float(x0) * c2, __uint_as_float(x1) * c2};
    }
#pragma unroll
    for (int e = 0; e < 8; ++e) r[e] = f32x2{ob[2 * e], ob[2 * e + 1]} * f32x2{c1, c1} + r[e];
    auto put = [&]() {
#pragma unroll
      for (int sx = 0; sx < 2; ++sx)
        __builtin_amdgcn_raw_buffer_store_b128(u32x4{pack2(r[4 * sx][0], r[4 * sx][1]), pack2(r[4 * sx + 1][0], r[4 * sx + 1][1]),
            pack2(r[4 * sx + 2][0], r[4 * sx + 2][1]), pack2(r[4 * sx + 3][0], r[4 * sx + 3][1])}, rso, dso + sx * 1024, 0, 16);
    };
    put();
    PIN();
    __builtin_amdgcn_s_setprio(0);
    PIN();
    f32x2 chk = {lsb - __builtin_amdgcn_fmed3f(lsb, 8.6736174e-19f, 1.2676506e30f), l2 - __builtin_amdgcn_fmed3f(l2, 8.6736174e-19f, 1.2676506e30f)};
#pragma unroll
    for (int e = 0; e < 8; ++e) chk = r[e] * f32x2{0.f, 0.f} + chk;
    if (__builtin_expect(__builtin_amdgcn_ballot_w64(used && !(chk[0] + chk[1] == 0.f)) != 0ull, 0)) {
      float l;
      attn_safe(KV, head * 64, qb0, qb1, ob, l, LAY2 ? head * 64 + (idx >> 5) : -1, idx & 31, lbase + TAB + 128 + hh * 64);
      const float inv2 = 1.0f / l;
#pragma unroll
      for (int e = 0; e < 8; ++e) r[e] = f32x2{ob[2 * e] * inv2, ob[2 * e + 1] * inv2};
      asm volatile("s_waitcnt vmcnt(0)" ::: "memory");
      put();
    }
    unsigned pp = 0u;
    if (LAY2) {
      const unsigned ua = lbase + UGL + hh * 128 + (lane >> 5) * 16;
      f32x2 du = {0.f, 0.f}, dn = {0.f, 0.f};
#pragma unroll
      for (int q = 0; q < 4; ++q) {
        const f32x4 uu = *LDS_AT(const f32x4, ua + q * 32), gg = *LDS_AT(const f32x4, ua + 256 + q * 32);
        du = r[2 * q] * f32x2{uu[0], uu[1]} + du; du = r[2 * q + 1] * f32x2{uu[2], uu[3]} + du;
        const f32x2 t0 = r[2 * q] * f32x2{gg[0], gg[1]}, t1 = r[2 * q + 1] * f32x2{gg[2], gg[3]};
        dn = t0 * t0 + dn; dn = t1 * t1 + dn;
      }
      const auto a = __builtin_amdgcn_permlane32_swap(__float_as_uint(du[0] + du[1]), __float_as_uint(du[0] + du[1]), false, false);
      const auto c = __builtin_amdgcn_permlane32_swap(__float_as_uint(dn[0] + dn[1]), __float_as_uint(dn[0] + dn[1]), false, false);
      pp = f2word(__uint_as_float(pack2(__uint_as_float(a[0]) + __uint_as_float(a[1]), __uint_as_float(c[0]) + __uint_as_float(c[1]))));
    }
    __builtin_amdgcn_raw_buffer_store_b32(pp, __builtin_amdgcn_make_buffer_rsrc(hand + 2 * HAND_N + PP_O, 0, LAY2 ? H_ * S_ * 4 : 0, 0x00020000),
                                          lane < 32 ? (head * S_ + (qt + g) * 32 + lane) * 4 : H_ * S_ * 4, 0, 16);
    PIN();
  }
  TE(1, 2);
  u32x4 xres[2];
  oproj_tail(LAY2, tid, lbase, lbase + FLG, omi, oni, idx, gran + GR_O + ((LAY2 ? 16 : 0) + tmi) * (64 * RO) + tni,
             gran + GR_X + omi * (128 * RX) + oni * 4, tag, Of, xout, A, w_r, stats_out, stats, hand, xres TEPASS);
  if (LAY2) {
    const int row = omi * 128 + (wid & 3) * 32 + (wid >> 2) * 16 + (lane >> 3), col = oni * 64 + (lane & 7) * 8;
    unsigned md[2], best = 0u;
    const __amdgpu_buffer_rsrc_t rsk = __builtin_amdgcn_make_buffer_rsrc(hand + HAND_N, 0, (HAND_N + PS_N) * 4, 0x00020000);
    f32x2 xf[2][4];
#pragma unroll
    for (int p = 0; p < 2; ++p)
#pragma unroll
      for (int e = 0; e < 4; ++e) {
        xf[p][e] = f32x2{__uint_as_float(xres[p][e] << 16), __uint_as_float(xres[p][e] & 0xffff0000u)};
        asm volatile("" : "+v"(xf[p][e]));
      }
    LOGDEF;
    LOG();
    const bool rtr = (oni & 7) == 7;
    {
      const int rl = (wid & 3) * 32 + (wid >> 2) * 16 + (lane & 15), rr = omi * 128 + rl;
      int src = (HAND_N + ((omi * 16 + (lane >> 4) * 4) * 128 + rl) * 4) * 4;
      int ko = rtr && (wid >> 2) == (oni >> 3) && lane < NREP ? lane * 1024 + (rr >> 4) * 8 : (HAND_N + PS_N) * 4;
      int bp = (lane >> 3) * 4;
      const float wsum = *LDS_AT(const volatile float, lbase + SPM + 132);
      asm volatile("" : "+v"(src), "+v"(ko), "+v"(bp));
      u32x4 v[4];
      __builtin_amdgcn_s_sleep(MPRE);
#pragma unroll 1
      for (int spin = 0; spin < SPIN_MAX; ++spin) {
        LOG();
#pragma unroll
        for (int i = 0; i < 4; ++i) v[i] = __builtin_amdgcn_raw_buffer_load_b128(rsk, src + (i & 1) * 2048, (i >> 1) * 4096, 16);
        unsigned mx = 0u;
#pragma unroll
        for (int i = 0; i < 4; ++i) mx = umax(umax(umax(mx, v[i][0]), v[i][1]), v[i][2]);
        if (none(mx == SENT)) break;
        __builtin_amdgcn_s_sleep(MSLP);
        asm volatile("" ::: "memory");
      }
      LOGAT(12);
      if (rtr) __builtin_amdgcn_s_setprio(3); else __builtin_amdgcn_s_setprio(LNPRIO);
      double sm[3];
#pragma unroll
      for (int c = 0; c < 3; ++c) {
        const double a = ((double)__uint_as_float(v[0][c]) + (double)__uint_as_float(v[1][c])) + ((double)__uint_as_float(v[2][c]) + (double)__uint_as_float(v[3][c]));
        u32x2 q = __builtin_bit_cast(u32x2, a);
        const auto lo = __builtin_amdgcn_permlane32_swap(q[0], q[0], false, false), hi = __builtin_amdgcn_permlane32_swap(q[1], q[1], false, false);
        q = __builtin_bit_cast(u32x2, __builtin_bit_cast(double, u32x2{lo[0], hi[0]}) + __builtin_bit_cast(double, u32x2{lo[1], hi[1]}));
        const auto l2 = __builtin_amdgcn_permlane16_swap(q[0], q[0], false, false), h2 = __builtin_amdgcn_permlane16_swap(q[1], q[1], false, false);
        sm[c] = __builtin_bit_cast(double, u32x2{l2[0], h2[0]}) + __builtin_bit_cast(double, u32x2{l2[1], h2[1]});
      }
      float mean, var, mb, den;
      ln_from_sums(sm[0], sm[1], mean, var, mb, den);
      const unsigned mdr = f2word(__uint_as_float((__float_as_uint(mb) >> 16) | (__float_as_uint(den) & 0xffff0000u)));
      md[0] = __builtin_amdgcn_ds_bpermute(bp, mdr); md[1] = __builtin_amdgcn_ds_bpermute(bp + 32, mdr);
      if (rtr) {
        const float r = (float)fma(-(double)mb, (double)wsum, sm[2]) * frcp(den);
        const unsigned rb = pack2(r, r);
        unsigned rkey = (rb ^ ((unsigned)((int)rb >> 31) | 0x80000000u)) & 0xffff0000u;
        if (r != r) rkey = 0u;
        const unsigned top = wave_umax(rkey | (unsigned)(2047 - rr));
        const unsigned mdt = __builtin_amdgcn_readlane(mdr, (2047 - (int)(top & 0xffffu)) & 15);
        __builtin_amdgcn_raw_buffer_store_b64(u32x2{top, mdt}, rsk, ko, 0, 16);
        PIN();
        __builtin_amdgcn_s_setprio(LNPRIO);
        __hip_atomic_fetch_add(LDS_AT(unsigned, lane == 0 ? lbase + SPM + 128 : lbase + DUM + lane * 4), 1u, __ATOMIC_RELAXED, __HIP_MEMORY_SCOPE_WORKGROUP);
        if (wid == 2 || wid == 5) STAMP(8, 6);
      }
      LOGAT(13);
    }
    auto keys = [&](const u32x4& v) __attribute__((always_inline)) {
      if (none(umax(umax(v[0], v[1]), umax(v[2], v[3])) == SENT)) {
        best = wave_umax(umax(v[0], v[2])) | 0x8000u;
        *LDS_AT(volatile u32x2, lbase + FLG + 40) = u32x2{best, (unsigned)__builtin_amdgcn_readlane(v[0] > v[2] ? v[1] : v[3], ((2047 - (int)(best & 0x7ffu)) >> 5) & 63)};
        STAMP(8, 11);
      }
    };
    auto ln_rows = [&]() __attribute__((always_inline)) {
#pragma unroll
      for (int p = 0; p < 2; ++p) {
        const float mb_ = __uint_as_float(md[p] << 16), rin = frcp(__uint_as_float(md[p] & 0xffff0000u));
#pragma unroll
        for (int e = 0; e < 4; ++e) {
          const f32x2 h = (xf[p][e] - f32x2{mb_, mb_}) * f32x2{rin, rin} + xf[p][e];
          xres[p][e] = pack2(h[0], h[1]);
        }
      }
      asm volatile("" : "+v"(xres[0]), "+v"(xres[1]));
    };
    if (wid == 0) STAMP(8, 12);
    ln_rows();
    LOGAT(14);
    const __amdgpu_buffer_rsrc_t rsu = __builtin_amdgcn_make_buffer_rsrc(out, 0, S_ * D_ * 2, 0x00020000);
    unsigned fr1 = SENT;
#pragma unroll 1
    for (int i = 0; i < 4 * SPIN_MAX && fr1 == SENT; ++i) fr1 = __builtin_amdgcn_readfirstlane(*LDS_AT(const volatile unsigned, lbase + SPM + 144));
    const int fr2 = (fr1 >> 16) & 0x7ffu;
    fr1 &= 0x7ffu;
    unsigned tmx = __builtin_amdgcn_readfirstlane(*LDS_AT(const volatile unsigned, lbase + SPM + 136)) + KDT;
    if (tmx - (unsigned)__builtin_amdgcn_s_memrealtime() > 1024u) tmx = 0u;
    auto due = [&]() { return tmx == 0u || (int)((unsigned)__builtin_amdgcn_s_memrealtime() - tmx) >= 0; };
    lk_set(15);
    const bool held = !none((row | 8) == ((int)fr1 | 8) || (row | 8) == (fr2 | 8));
#pragma unroll
    for (int p = 0; p < 2; ++p) __builtin_amdgcn_raw_buffer_store_b128(xres[p], rsu, row + 8 * p != (int)fr1 && row + 8 * p != fr2 ? ((row + 8 * p) * D_ + col) * 2 : S_ * D_ * 2, 0, OAUX);
    PIN();
    if (wid == 6) {
      if (rtr) {
#pragma unroll 1
        for (int i = 0; i < SPIN_MAX; ++i) {
          if (__builtin_amdgcn_readfirstlane(*LDS_AT(const volatile unsigned, lbase + SPM + 128)) == 7u) break;
          __builtin_amdgcn_s_sleep(1);
        }
        asm volatile("" ::: "memory");
      }
      while (!due()) __builtin_amdgcn_s_sleep(1);
    }
#pragma unroll 1
    for (int i = 0; i < 4 * SPIN_MAX && !best; ++i) {
      if (wid == 6) {
        LOG();
        keys(__builtin_amdgcn_raw_buffer_load_b128(rsk, (bid % NREP) * 1024 + lane * 16, 0, 16));
        if (!best) __builtin_amdgcn_s_sleep(KSLP);
      } else {
        best = __builtin_amdgcn_readfirstlane(*LDS_AT(const volatile unsigned, lbase + FLG + 40));
        if (!best) __builtin_amdgcn_s_sleep(1);
      }
      asm volatile("" ::: "memory");
    }
    const int idx2 = 2047 - (int)(best & 0x7ffu);
    if (held) {
#pragma unroll
      for (int p = 0; p < 2; ++p) {
        const int r = row + 8 * p;
        __builtin_amdgcn_raw_buffer_store_b128(xres[p], rsu, (r == (int)fr1 || r == fr2) && r != idx2 ? (r * D_ + col) * 2 : S_ * D_ * 2, 0, OAUX);
      }
    }
    if (__builtin_expect(idx2 != (int)fr1 && idx2 != fr2, 0) && !none((row | 8) == (idx2 | 8))) {
#pragma unroll
      for (int p = 0; p < 2; ++p)
        __builtin_amdgcn_raw_buffer_store_b128(u32x4{pack2(xf[p][0][0], xf[p][0][1]), pack2(xf[p][1][0], xf[p][1][1]), pack2(xf[p][2][0], xf[p][2][1]), pack2(xf[p][3][0], xf[p][3][1])},
            __builtin_amdgcn_make_buffer_rsrc(xout, 0, 3 * D_ * 2, 0x00020000), row + 8 * p == idx2 ? (2 * D_ + col) * 2 : S_ * D_ * 2, 0, 16);
      asm volatile("s_waitcnt vmcnt(0)" ::: "memory");
      if (lane == 0) st_ag(gran + GR_F + oni * 32, tag);
    }
  }
  }
}
struct Args2 { Args l[2]; };
extern "C" __global__ __launch_bounds__(768, 3) void layers_kernel(const Args2 args) {
  __shared__ __attribute__((aligned(128))) char smem[163840];
  const unsigned long long t_entry = 0;
  const int tid = threadIdx.x, bid = blockIdx.x;
  int wid = __builtin_amdgcn_readfirstlane(tid >> 6);
  asm volatile("" : "+s"(wid));
  const ArgP ap = (ArgP)__builtin_amdgcn_kernarg_segment_ptr();
  layer_body(wid, bid, (unsigned)(uintptr_t)smem, ap, t_entry);
  LAYER_HERE;
  if (!LAY2) {
    asm volatile(AGAIN :: "{s[0:1]}"(ap + 1), "{s2}"(bid), "{v0}"(wid * 64) : "s90", "s91", "scc", "memory");
    __builtin_unreachable();
  }
}

extern "C" __global__ void end_marker_kernel() {}

extern "C" void kernel_launch(void* const* d_in, const int* in_sizes, int n_in, void* d_out, int out_size,
                              void* d_ws, size_t ws_size, hipStream_t stream) {
  const u16* x0 = (const u16*)d_in[0];
  const u16* wqkv = (const u16*)d_in[1];
  const u16* wo = (const u16*)d_in[2];
  const u16* w_r = (const u16*)d_in[4];
  const u16* wg = (const u16*)d_in[5];
  const u16* wu = (const u16*)d_in[6];
  const u16* wd = (const u16*)d_in[7];
  u16* out = (u16*)d_out;

  char* ws = (char*)d_ws;
  const size_t MB4 = (size_t)S_ * D_ * 2;
  u16* kv1 = (u16*)(ws);
  u16* o1 = (u16*)(ws + 2 * MB4);
  u16* x1 = (u16*)(ws + 3 * MB4);
  unsigned* gran = (unsigned*)(ws + 4 * MB4);
  float* csf = (float*)(gran + GR_F + 16 * 32);
  double* stats = (double*)(csf + 8192);
  unsigned* hand = (unsigned*)(stats + 2 * STAT_N);
  static_assert((GR_F + 16 * 32) % 32 == 0 && (size_t)(GR_F + 16 * 32 + 8192 + 4 * STAT_N + 2 * HAND_N + PS_N) * 4 <= 6u << 20, "");
  u16* x3 = (u16*)(hand + 2 * HAND_N) - 3 * D_;
  u16* wc = (u16*)((((uintptr_t)ws + 4 * MB4 + (6u << 20) + 2047) & ~(uintptr_t)2047) + (((uintptr_t)wqkv + 128) & 2047));
  u16* kv2 = (u16*)(ws + 8 * MB4);
  u16* o2 = (u16*)(ws + 10 * MB4);
  Args2 a;
  for (int l = 0; l < 2; ++l) {
    Args& r = a.l[l];
    r.lay = l; r.a = l ? x1 : x0; r.kv = l ? kv2 : kv1; r.o = l ? o2 : o1; r.xo = l ? x3 : x1; r.so = l ? stats + STAT_N : stats;
    r.W = wqkv; r.wc = wc; r.wg = wg; r.wu = wu; r.wd = wd; r.wo = wo; r.w_r = w_r; r.gran = gran; r.csf = csf; r.stats = stats; r.hand = hand; r.out = out;
  }
  layers_kernel<<<256, 768, 0, stream>>>(a);
}
```

```cpp
#include <hip/hip_runtime.h>
#include <hip/hip_bf16.h>
#include <stdint.h>
#include <type_traits>
#include <utility>

typedef unsigned short u16;
typedef short s16x4 __attribute__((ext_vector_type(4)));
typedef short s16x8 __attribute__((ext_vector_type(8)));
typedef float f32x16 __attribute__((ext_vector_type(16)));
typedef float f32x4 __attribute__((ext_vector_type(4)));
typedef unsigned int u32x4 __attribute__((ext_vector_type(4)));
typedef unsigned int u32x2 __attribute__((ext_vector_type(2)));
typedef __bf16 bf16x8 __attribute__((ext_vector_type(8)));
typedef __bf16 bf16x16 __attribute__((ext_vector_type(16)));

__device__ __forceinline__ int fresh_lane() {
  int l;
  asm volatile("v_mbcnt_lo_u32_b32 %0, -1, 0\n\tv_mbcnt_hi_u32_b32 %0, -1, %0" : "=v"(l));
  return l;
}
#define STAMP(k, i)
#define STAMPV(k, i, v)
#define LOGDEF
#define LOG()
#define LOGAT(k)
#define lk_set(k)
#define LOGOUT(slot)
#define TE(grp, k)
#define TEARG
#define TEPASS
constexpr int S_ = 2048, D_ = 1024, H_ = 32, F_ = 3584;

__device__ __forceinline__ int kpos(int r) { return (r & ~12) | ((r & 4) << 1) | ((r & 8) >> 1); }
__device__ __forceinline__ float bf2f(u16 v) { return __uint_as_float(((unsigned)v) << 16); }
__device__ __forceinline__ u16 f2bf(float f) {
  __hip_bfloat16 b = __float2bfloat16(f);
  return *reinterpret_cast<u16*>(&b);
}
typedef float f32x2 __attribute__((ext_vector_type(2)));
typedef __bf16 bf16x2 __attribute__((ext_vector_type(2)));
__device__ __forceinline__ unsigned pack2(float lo, float hi) {
  f32x2 v = {lo, hi};
  return __builtin_bit_cast(unsigned, __builtin_convertvector(v, bf16x2));
}
__device__ __forceinline__ float rbf(float f) { return __uint_as_float(pack2(f, f) << 16); }
__device__ __forceinline__ void rbf2(float& a, float& b) {
  const unsigned p = pack2(a, b);
  a = __uint_as_float(p << 16); b = __uint_as_float(p & 0xffff0000u);
}

__device__ __forceinline__ float fadd(float a, float b) { float r = a + b; asm("" : "+v"(r)); return r; }
template <int CTRL, int RMASK>
__device__ __forceinline__ float dpp_add(float v) {
  const int t = __builtin_amdgcn_update_dpp(0, __float_as_int(v), CTRL, RMASK, 0xf, false);
  return v + __int_as_float(t);
}
__device__ __forceinline__ float wave_sum(float v) {
  v = dpp_add<0x111, 0xf>(v);
  v = dpp_add<0x112, 0xf>(v);
  v = dpp_add<0x114, 0xf>(v);
  v = dpp_add<0x118, 0xf>(v);
  v = dpp_add<0x142, 0xa>(v);
  v = dpp_add<0x143, 0xc>(v);
  return __int_as_float(__builtin_amdgcn_readlane(__float_as_int(v), 63));
}

template <int CTRL, int RMASK>
__device__ __forceinline__ unsigned dpp_umax(unsigned v) {
  const unsigned t = (unsigned)__builtin_amdgcn_update_dpp(0, (int)v, CTRL, RMASK, 0xf, false);
  return v > t ? v : t;
}
__device__ __forceinline__ unsigned wave_umax(unsigned v) {
  v = dpp_umax<0x111, 0xf>(v); v = dpp_umax<0x112, 0xf>(v); v = dpp_umax<0x114, 0xf>(v); v = dpp_umax<0x118, 0xf>(v);
  v = dpp_umax<0x142, 0xa>(v); v = dpp_umax<0x143, 0xc>(v);
  return (unsigned)__builtin_amdgcn_readlane((int)v, 63);
}

#define NREP 32
constexpr unsigned SENT = 0xFFFFFFFFu;
constexpr int GRAN_N = 256 * NREP;
constexpr int Y_N = F_ / 16 * D_;
constexpr int X2_N = D_ / 2;
constexpr int XS_N = 128;
constexpr int P4_N = 3 * D_ * 16;
constexpr int PA_N = H_ * 8 * 64;
constexpr int HAND_N = GRAN_N + Y_N + X2_N + XS_N + P4_N + PA_N;
constexpr int MD_O = 16 * 16 * 128 * 4;
constexpr int PK_O = MD_O;
constexpr int PP_O = PK_O + 128 * NREP;
constexpr int Y3_O = PP_O + H_ * S_;
constexpr int PS_N = Y3_O + Y_N;
constexpr int GR_ROW = 64, GR_COL = GR_ROW + 4 * S_, GR_KV = GR_COL + 16 * 16 * 192;
#define RO 16
#define RK 16
#define RX 1
constexpr int GR_O = GR_KV + 2 * 16 * 32 * RK, GR_X = GR_O + 2 * 16 * 64 * RO, GR_N = GR_X + 16 * 128 * RX;
constexpr int GR_F = GR_N + 256 * 64;
constexpr int STAT_N = 3 * S_;

__device__ __forceinline__ void st_ag(unsigned* p, unsigned v) { __hip_atomic_store(p, v, __ATOMIC_RELAXED, __HIP_MEMORY_SCOPE_AGENT); }
__device__ __forceinline__ unsigned ld_ag(const unsigned* p) { return __hip_atomic_load(p, __ATOMIC_RELAXED, __HIP_MEMORY_SCOPE_AGENT); }
__device__ __forceinline__ double ld_ag(const double* p) { return __hip_atomic_load(p, __ATOMIC_RELAXED, __HIP_MEMORY_SCOPE_AGENT); }
__device__ __forceinline__ unsigned f2word(float f) { const unsigned u = __float_as_uint(f); return u < 0xFFFFFFFEu ? u : 0xFFFFFFFEu; }
constexpr int SPIN_MAX = 1 << 18;
__device__ __forceinline__ unsigned umax(unsigned a, unsigned b) { return a > b ? a : b; }
__device__ __forceinline__ bool none(bool p) { return __builtin_amdgcn_ballot_w64(p) == 0ull; }

__device__ __forceinline__ void ln_from_sums(double s1, double s2, float& mean, float& var, float& mb, float& den) {
  const double md = s1 * (1.0 / 1024.0);
  const double vd = fma(-md, md, s2 * (1.0 / 1024.0));
  mean = (float)md; var = fmaxf((float)vd, 0.f);
  mb = rbf(mean);
  den = rbf(__builtin_amdgcn_sqrtf(rbf(rbf(var) + 1e-5f)));
}
__device__ __forceinline__ float frcp(float x) { return __builtin_amdgcn_rcpf(x); }
__device__ __forceinline__ void ln_apply2(float& a, float& b, float mb, float den, float rin) {
  a -= mb; b -= mb;
  rbf2(a, b);
  float qa = a * rin, qb = b * rin;
  qa = fmaf(fmaf(-qa, den, a), rin, qa);
  qb = fmaf(fmaf(-qb, den, b), rin, qb);
  rbf2(qa, qb);
  a = qa; b = qb;
}

#define CODE_ADDR(SYM, out64)                                                                                          \
  do {                                                                                                                 \
    unsigned lo_, hi_;                                                                                                 \
    asm volatile("s_getpc_b64 s[90:91]\n\ts_add_u32 s90, s90, " SYM "@rel32@lo+4\n\ts_addc_u32 s91, s91, " SYM          \
                 "@rel32@hi+12\n\ts_mov_b32 %0, s90\n\ts_mov_b32 %1, s91"                                             \
                 : "=s"(lo_), "=s"(hi_) :: "s90", "s91", "scc");                                                        \
    out64 = ((unsigned long long)hi_ << 32) | lo_;                                                                     \
  } while (0)

#define QNX 4
constexpr int QMX = 32 / QNX, QNG = 16 / QNX, QMG = 8 / QNG;

__device__ __forceinline__ unsigned touch(const void* p) { return *reinterpret_cast<const unsigned*>(p); }

__device__ __forceinline__ unsigned touch_code(unsigned long long lo, unsigned long long hi, int pc, int lane) {
  const unsigned long long p = lo + (unsigned)(pc * 8192 + lane * 128);
  return p + 4 <= hi ? *reinterpret_cast<__attribute__((address_space(1))) const unsigned*>(p) : 0u;
}

template <bool SC = false>
__device__ __forceinline__ void glds16s(const void* sbase, unsigned voff, unsigned lds_dst) {
  unsigned keep;
  if (SC)
    asm volatile("s_mov_b32 %0, m0\n\ts_mov_b32 m0, %3\n\ts_nop 2\n\tglobal_load_lds_dwordx4 %1, %2 sc1\n\ts_mov_b32 m0, %0"
                 : "=&s"(keep) : "v"(voff), "s"(sbase), "s"(lds_dst) : "memory");
  else
  asm volatile("s_mov_b32 %0, m0\n\ts_mov_b32 m0, %3\n\ts_nop 2\n\tglobal_load_lds_dwordx4 %1, %2\n\ts_mov_b32 m0, %0"
               : "=&s"(keep) : "v"(voff), "s"(sbase), "s"(lds_dst) : "memory");
}
template <bool SC>
__device__ __forceinline__ void glds16x4(const void* sbase, unsigned v0, unsigned v1, unsigned v2, unsigned v3, unsigned lds_dst) {
  unsigned keep;
  if (SC)
    asm volatile("s_mov_b32 %0, m0\n\ts_mov_b32 m0, %6\n\ts_nop 2\n\tglobal_load_lds_dwordx4 %1, %5 sc1\n\tglobal_load_lds_dwordx4 %2, %5 offset:1024 sc1\n\t"
                 "global_load_lds_dwordx4 %3, %5 offset:2048 sc1\n\tglobal_load_lds_dwordx4 %4, %5 offset:3072 sc1\n\ts_mov_b32 m0, %0"
                 : "=&s"(keep) : "v"(v0), "v"(v1), "v"(v2), "v"(v3), "s"(sbase), "s"(__builtin_amdgcn_readfirstlane(lds_dst)) : "memory");
  else
    asm volatile("s_mov_b32 %0, m0\n\ts_mov_b32 m0, %6\n\ts_nop 2\n\tglobal_load_lds_dwordx4 %1, %5\n\tglobal_load_lds_dwordx4 %2, %5 offset:1024\n\t"
                 "global_load_lds_dwordx4 %3, %5 offset:2048\n\tglobal_load_lds_dwordx4 %4, %5 offset:3072\n\ts_mov_b32 m0, %0"
                 : "=&s"(keep) : "v"(v0), "v"(v1), "v"(v2), "v"(v3), "s"(sbase), "s"(__builtin_amdgcn_readfirstlane(lds_dst)) : "memory");
}
constexpr float QSCALE = 0.17677669529663687f * 1.4426950408889634f;

__device__ __forceinline__ unsigned min4(u32x4 v) { const unsigned a = v[0] < v[1] ? v[0] : v[1], b = v[2] < v[3] ? v[2] : v[3]; return a < b ? a : b; }
#define LDS_AT(T, a) ((__attribute__((address_space(3))) T*)(uintptr_t)(a))
__device__ __forceinline__ u32x4 lds_rd4(unsigned a) { return *LDS_AT(const volatile u32x4, a); }
__device__ __forceinline__ void lds_wr(unsigned a, unsigned v) { *LDS_AT(volatile unsigned, a) = v; }
__device__ __forceinline__ void split3(float v, unsigned& p0, unsigned& p1, unsigned& p2) {
  p0 = pack2(v, v) & 0xffffu; v -= __uint_as_float(p0 << 16);
  p1 = pack2(v, v) & 0xffffu; v -= __uint_as_float(p1 << 16);
  p2 = pack2(v, v) & 0xffffu;
}

#define PIN() __builtin_amdgcn_sched_barrier(0)
template <int B, class F, int... I>
__device__ __forceinline__ void static_for_(F&& f, std::integer_sequence<int, I...>) { (f(std::integral_constant<int, B + I>{}), ...); }
template <int B, int E, class F>
__device__ __forceinline__ void static_for(F&& f) { static_for_<B>(f, std::make_integer_sequence<int, E - B>{}); }

#define TPRE 60
#define TPSL 0
#define FCPRE 44
#define FCSLP 2
#define SIGAT 24
#define XPRIO 1
#define OPD 8
#define LAUX 16
#define CSLP 2
#define EPRIO 3
#define OPRIO 1
#define XSLP 1
#define CSL1 8
#define XAUX 16
__device__ __forceinline__ void oproj_tail(const bool RES2, const int tid, const unsigned lbase, const unsigned flg, const int tmi, const int tni, const int idx,
                                           unsigned* osig, unsigned* xsig, const unsigned tag,
                                           const u16* A, u16* out,
                                           const u16* res, const u16* __restrict__ w_r, double* stats,
                                           const double* stats_in, unsigned* hand, u32x4 (&xres)[2] TEARG) {
  constexpr int ldw = D_, PD = OPD;
  const int lane = tid & 63, wid = __builtin_amdgcn_readfirstlane(tid >> 6);
  const int m0 = tmi * 128, n0 = tni * 64;
  const int dk = RES2 ? 7 : 3;
  if (wid == 0) STAMP(dk, 0);
  TE(1, 3);
  asm volatile("" : "+s"(A) :: "memory");
  PIN();
  const int row = m0 + (wid & 3) * 32 + (wid >> 2) * 16 + (lane >> 3), col = n0 + (lane & 7) * 8;
  const unsigned tb = lbase + 147456;
  typedef __attribute__((address_space(1))) double* gdp;
  typedef __attribute__((address_space(1))) unsigned* gup;
  unsigned tr, oad;
  gdp pst;
  gup pps;
  int xo;
  auto setup = [&]() __attribute__((always_inline)) {
    tr = tb + (row - m0) * 128 + (((lane ^ (lane >> 3)) & 7) << 4);
    pst = (gdp)(stats + ((lane & 3) == 3 ? 0 : (lane & 3)) * S_ + row + 8 * ((lane >> 2) & 1));
    pps = (gup)(hand + 2 * HAND_N + ((tmi * 16 + tni) * 128 + row - m0 + 8 * ((lane >> 2) & 1)) * 4 + (lane & 3));
    xo = (row * ldw + col) * 2;
    asm volatile("" : "+v"(tr), "+v"(pst), "+v"(pps), "+v"(xo));
    PIN();
  };
  {
    oad = lane == 0 ? flg + 24 : flg + 64 + lane * 4;
    gup ps = (gup)(osig + lane * 64);
    asm volatile("" : "+v"(oad), "+v"(ps));
    asm volatile("s_waitcnt vmcnt(1)" ::: "memory");
    TE(2, 1);
    const unsigned old = __hip_atomic_fetch_add(LDS_AT(unsigned, oad), 1u, __ATOMIC_RELAXED, __HIP_MEMORY_SCOPE_WORKGROUP);
    if (__builtin_amdgcn_readfirstlane(old) == 7u && lane < RO) __hip_atomic_store(ps, tag, __ATOMIC_RELAXED, __HIP_MEMORY_SCOPE_AGENT);
    PIN();
    TE(2, 2);
    if (wid == 0) STAMP(dk, 12);
  }
  auto announce = [&]() __attribute__((always_inline)) {
    asm volatile("" ::: "memory");
    PIN();
    __builtin_amdgcn_s_barrier();
    PIN();
    if (wid == 0) STAMP(dk, 1);
    TE(2, 3); TE(4, 0); TE(7, 1);
  };
  u32x4 rwr;
  auto sums = [&](const u32x4& x0, const u32x4& x1) __attribute__((always_inline)) {
    float f[2][3];
#pragma unroll
    for (int p = 0; p < 2; ++p) {
      f[p][0] = f[p][1] = f[p][2] = 0.f;
#pragma unroll
      for (int e = 0; e < 4; ++e) {
        const unsigned xu = p ? x1[e] : x0[e], wu = rwr[e];
        const bf16x2 x = __builtin_bit_cast(bf16x2, xu);
        f[p][0] = __builtin_amdgcn_fdot2_f32_bf16(x, __builtin_bit_cast(bf16x2, 0x3f803f80u), f[p][0], false);
        f[p][1] = __builtin_amdgcn_fdot2_f32_bf16(x, x, f[p][1], false);
        f[p][2] = __builtin_amdgcn_fdot2_f32_bf16(x, __builtin_bit_cast(bf16x2, wu), f[p][2], false);
      }
    }
    auto addx = [&](float keep, float give, auto ctl) {
      return keep + __int_as_float(__builtin_amdgcn_update_dpp(0, __float_as_int(give), decltype(ctl)::value, 0xf, 0xf, false));
    };
    const bool b2 = lane & 4, b1 = lane & 2, z0 = (lane & 3) == 0, z1 = (lane & 3) == 1;
    float g[3];
#pragma unroll
    for (int c = 0; c < 3; ++c) g[c] = addx(b2 ? f[1][c] : f[0][c], b2 ? f[0][c] : f[1][c], std::integral_constant<int, 0x141>{});
    const float u1 = addx(b1 ? g[2] : g[0], b1 ? g[0] : g[2], std::integral_constant<int, 0x4e>{});
    const float u2 = addx(g[1], g[1], std::integral_constant<int, 0x4e>{});
    return addx(z1 ? u2 : u1, z0 ? u2 : u1, std::integral_constant<int, 0xb1>{});
  };
  if (wid < 4) {
    const __amdgpu_buffer_rsrc_t rsa = __builtin_amdgcn_make_buffer_rsrc(const_cast<u16*>(A), 0, S_ * D_ * 2, 0x00020000);
    int sa0 = (tmi * 4 + wid) * 65536, va0 = (lane >> 4) * 512 + (lane & 15) * 16;
    asm volatile("" : "+v"(va0));
    asm volatile("" : "+s"(sa0));
    auto lda = [&](int i, int h) { return __builtin_amdgcn_raw_buffer_load_b128(rsa, va0 + (i & 1) * 2048 + h * 256, sa0 + (i >> 1) * 4096, LAUX); };
    unsigned wad[4], tw[4];
    f32x4 acc[2][4];
    const int k = (lane >> 5) * 16 + ((lane >> 2) & 7);
#pragma unroll
    for (int j = 0; j < 4; ++j) {
      wad[j] = lbase + k * 128 + (((2 * j) ^ (k & 6)) << 4) + 8 * (lane & 3);
      tw[j] = tb + wid * 4096 + (lane & 15) * 128 + (((2 * j + (lane >> 5)) ^ (lane & 7)) << 4) + ((lane >> 4) & 1) * 8;
      asm volatile("" : "+v"(wad[j]), "+v"(tw[j]));
    }
    auto ldw_ = [&](int i, int j) {
      const unsigned a = wad[j] + (i & 3) * 4096;
      s16x4 lo = __builtin_amdgcn_ds_read_tr16_b64_v4i16(LDS_AT(s16x4, a));
      s16x4 h4 = __builtin_amdgcn_ds_read_tr16_b64_v4i16(LDS_AT(s16x4, a + 1024));
      return (s16x8)__builtin_shufflevector(lo, h4, 0, 1, 2, 3, 4, 5, 6, 7);
    };
    u32x4 id[2];
#pragma unroll
    for (int j = 0; j < 2; ++j) {
#pragma unroll
      for (int e = 0; e < 4; ++e) id[j][e] = (lane >> 4) == 2 * j + ((lane >> 3) & 1) && ((lane & 7) >> 1) == e ? ((lane & 1) ? 0x3f800000u : 0x3f80u) : 0u;
      asm volatile("" : "+v"(id[j]));
    }
    unsigned rad = tb + wid * 4096 + (lane & 15) * 128 + (((lane >> 4) ^ (lane & 7)) << 4), rfa = flg + 36 + wid;
    asm volatile("" : "+v"(rad), "+v"(rfa));
    u32x4 rb[2][2];
    rwr = *reinterpret_cast<const u32x4*>(w_r + col);
    setup();
    asm volatile("" : "+v"(rwr));
    announce();
    u32x4 af[PD][2];
    s16x8 wf[2][4];
#pragma unroll
    for (int i = 0; i < PD; i += 2) { af[i][0] = lda(i, 0); af[i + 1][0] = lda(i + 1, 0); af[i][1] = lda(i, 1); af[i + 1][1] = lda(i + 1, 1); }
    PIN();
    __builtin_amdgcn_s_setprio(OPRIO);
#pragma unroll
    for (int q = 0; q < 4; ++q) rb[q >> 1][q & 1] = lds_rd4((rad ^ ((q & 1) * 64)) + (q >> 1) * 2048);
#pragma unroll
    for (int j = 0; j < 4; ++j) wf[j & 1][j >> 1] = ldw_(j & 1, j >> 1);
    PIN();
#pragma unroll
    for (int j = 0; j < 4; ++j)
#pragma unroll
      for (int h = 0; h < 2; ++h) { asm volatile("v_mfma_f32_16x16x32_bf16 %0, %1, %2, 0" : "=&v"(acc[h][j]) : "v"(id[j & 1]), "v"(rb[h][j >> 1])); PIN(); }
#pragma unroll 1
    for (int spin = 0; spin < SPIN_MAX; ++spin)
      if (__builtin_amdgcn_readfirstlane(*LDS_AT(const volatile unsigned, flg)) >= 40u) break;
    asm volatile("" : "+v"(va0) :: "memory");
    PIN();
    s16x4 wlo;
    static_for<0, 16>([&](auto ic) __attribute__((always_inline)) {
      constexpr int i = 2 * decltype(ic)::value;
      if (i == SIGAT) { __hip_atomic_fetch_add(LDS_AT(unsigned, oad + 4), 1u, __ATOMIC_RELAXED, __HIP_MEMORY_SCOPE_WORKGROUP); PIN(); }
#pragma unroll
      for (int g = 0; g < 16; ++g) {
        const int e = g & 1, j = g >> 2, h = (g >> 1) & 1, jn = (j + 2) & 3, in = j < 2 ? i : i + 2;
        asm volatile("v_mfma_f32_16x16x32_bf16 %0, %1, %2, %0" : "+v"(acc[h][j]) : "v"(wf[e][j]), "v"(af[(i + e) % PD][h]));
        PIN();
        if (in == 16 && !(g & 3)) asm volatile("v_add_u32 %0, 0x10000, %0" : "+v"(wad[jn]));
        if (in < 32) {
          const s16x4 v = __builtin_amdgcn_ds_read_tr16_b64_v4i16(LDS_AT(s16x4, wad[jn] + ((in + h) & 15) * 4096 + e * 1024));
          if (e) wf[h][jn] = (s16x8)__builtin_shufflevector(wlo, v, 0, 1, 2, 3, 4, 5, 6, 7);
          else wlo = v;
          PIN();
        }
        if ((g & 3) == 1 && i > 0 && i + PD - 2 < 32) { af[(i - 2 + (j & 1)) % PD][j >> 1] = lda(i + PD - 2 + (j & 1), j >> 1); PIN(); }
        if ((g & 3) == 3 && in < 32 && i != SIGAT) { __builtin_amdgcn_s_waitcnt(0xc47f); PIN(); }
      }
    });
    asm volatile("s_nop 3" : "+v"(acc[0][0]), "+v"(acc[1][0]));
    TE(4, 1); TE(7, 2);
    if (wid == 0) STAMP(dk, 17);
    if (wid == 1 && !RES2) STAMP(4, 0);
#pragma unroll
    for (int j = 0; j < 4; ++j)
#pragma unroll
      for (int h = 0; h < 2; ++h) {
        *LDS_AT(u32x2, tw[j] + h * 2048) = u32x2{pack2(acc[h][j][0], acc[h][j][1]), pack2(acc[h][j][2], acc[h][j][3])};
        PIN();
      }
    asm volatile("" ::: "memory");
    *LDS_AT(volatile unsigned char, rfa) = (unsigned char)2;
#pragma unroll
    for (int p = 0; p < 2; ++p) xres[p] = lds_rd4(tr + p * 1024);
    PIN();
    if (!RES2) {
      const __amdgpu_buffer_rsrc_t rso = __builtin_amdgcn_make_buffer_rsrc(out, 0, S_ * D_ * 2, 0x00020000);
      const __amdgpu_buffer_rsrc_t rsf = __builtin_amdgcn_make_buffer_rsrc(xsig + wid, 0, RX * 512, 0x00020000);
      const u32x4 x2 = lds_rd4(tr + 2048), x3 = lds_rd4(tr + 3072);
      PIN();
#pragma unroll
      for (int p = 0; p < 4; ++p) { __builtin_amdgcn_raw_buffer_store_b128(p < 2 ? xres[p] : (p < 3 ? x2 : x3), rso, xo + p * (8 * ldw * 2), 0, XAUX); PIN(); }
      TE(4, 2); TE(5, 0);
      const float s0 = sums(xres[0], xres[1]), s1 = sums(x2, x3);
      if ((lane & 3) != 3) { __builtin_amdgcn_global_atomic_fadd_f64(pst, (double)s0); __builtin_amdgcn_global_atomic_fadd_f64(pst + 16, (double)s1); }
      PIN();
      TE(4, 3); TE(5, 1);
      asm volatile("s_waitcnt vmcnt(2)" ::: "memory");
      __builtin_amdgcn_raw_buffer_store_b32(tag, rsf, lane * 512 + 256, 0, 16);
      *LDS_AT(volatile unsigned char, rfa) = (unsigned char)3;
      PIN();
      TE(5, 2);
      asm volatile("s_waitcnt vmcnt(1)" ::: "memory");
      __builtin_amdgcn_raw_buffer_store_b32(tag, rsf, lane * 512, 0, 16);
      PIN();
      TE(5, 3);
      __builtin_amdgcn_s_setprio(0);
      return;
    }
    TE(4, 2); TE(5, 0);
  } else {
    u32x4 rres[4];
    double is1[4], is2[4];
#pragma unroll
    for (int p = 0; p < 4; ++p)
      rres[p] = __builtin_amdgcn_raw_buffer_load_b128(__builtin_amdgcn_make_buffer_rsrc(const_cast<u16*>(res), 0, S_ * D_ * 2, 0x00020000), ((row - 16 + 8 * p) * ldw + col) * 2, 0, 16);
    rwr = *reinterpret_cast<const u32x4*>(w_r + col);
#pragma unroll
    for (int p = 0; p < 4; ++p) { is1[p] = ld_ag(stats_in + row - 16 + 8 * p); is2[p] = ld_ag(stats_in + S_ + row - 16 + 8 * p); }
    PIN();
    setup();
    if (RES2) {
#pragma unroll
      for (int p = 0; p < 4; ++p) {
        float mean, var, mb, den;
        ln_from_sums(is1[p], is2[p], mean, var, mb, den);
        const float rin = frcp(den);
#pragma unroll
        for (int e = 0; e < 4; ++e) {
          const f32x2 x = {__uint_as_float(rres[p][e] << 16), __uint_as_float(rres[p][e] & 0xffff0000u)};
          const f32x2 h = (x - f32x2{mb, mb}) * f32x2{rin, rin} + x;
          rres[p][e] = pack2(h[0], h[1]);
        }
        asm volatile("" : "+v"(rres[p]));
      }
      PIN();
    }
    if ((idx >> 5) == tmi * 4 + wid - 4) {
      const int dr = idx - (row - 16);
      u32x4 qq;
#pragma unroll 1
      for (int i = 0; i < SPIN_MAX; ++i) {
        qq = __builtin_amdgcn_raw_buffer_load_b128(__builtin_amdgcn_make_buffer_rsrc(hand, 0, HAND_N * 4, 0x00020000), (GRAN_N + Y_N) * 4 + col * 2, 0, 16);
        if (none(umax(umax(qq[0], qq[1]), umax(qq[2], qq[3])) == SENT)) break;
        asm volatile("" ::: "memory");
      }
#pragma unroll
      for (int p = 0; p < 4; ++p)
#pragma unroll
        for (int e = 0; e < 4; ++e) rres[p][e] = dr == 8 * p ? qq[e] : rres[p][e];
    }
#pragma unroll 1
    for (int i = 0; i < 4 * SPIN_MAX; ++i)
      if (__builtin_amdgcn_readfirstlane(*LDS_AT(const volatile unsigned, flg + 16)) >= 12u) break;
#pragma unroll
    for (int p = 0; p < 4; ++p) *LDS_AT(volatile u32x4, tr - 2048 + p * 1024) = rres[p];
    asm volatile("s_waitcnt lgkmcnt(0)" ::: "memory");
    announce();
    TE(4, 1); TE(7, 2);
    asm volatile("s_waitcnt vmcnt(0)" : "+v"(rwr) :: "memory");
    if (!RES2) return;
    __builtin_amdgcn_s_sleep(TPRE);
#pragma unroll 1
    for (int i = 0; i < 4 * SPIN_MAX; ++i) {
      if (__builtin_amdgcn_readfirstlane(*LDS_AT(const volatile unsigned char, flg + 32 + wid)) == 2u) break;
      __builtin_amdgcn_s_sleep(TPSL);
    }
    asm volatile("" ::: "memory");
    __builtin_amdgcn_s_setprio(XPRIO);
    PIN();
    TE(4, 2); TE(5, 0);
#pragma unroll
    for (int p = 0; p < 2; ++p) xres[p] = lds_rd4(tr + p * 1024);
  }
  TE(4, 3); TE(7, 3);
  const float sv = sums(xres[0], xres[1]);
  if (wid == 0) STAMP(dk, 14);
  if ((lane & 3) != 3) __hip_atomic_store(pps, f2word(sv), __ATOMIC_RELAXED, __HIP_MEMORY_SCOPE_AGENT);
  PIN();
  __builtin_amdgcn_s_setprio(0);
  __hip_atomic_fetch_add(LDS_AT(unsigned, oad), 1u, __ATOMIC_RELAXED, __HIP_MEMORY_SCOPE_WORKGROUP);
  TE(5, 1); TE(5, 2);
  PIN();
}

#define MFMA32(a, b, c) __builtin_amdgcn_mfma_f32_32x32x16_bf16(a, b, c, 0, 0, 0)
#define MFMA16(a, b, c) __builtin_amdgcn_mfma_f32_16x16x32_bf16(a, b, c, 0, 0, 0)

#define PLO(x) __builtin_bit_cast(bf16x16, __builtin_shufflevector(x, x, 0, 1, 2, 3, 4, 5, 6, 7))
#define PHI(x) __builtin_bit_cast(bf16x16, __builtin_shufflevector(x, x, 8, 9, 10, 11, 12, 13, 14, 15))
#define SMF32(a, b, c) __builtin_amdgcn_smfmac_f32_32x32x32_bf16(__builtin_bit_cast(bf16x8, a), b, c, sidx, 0, 0)
#define SMF16(a, b, c) __builtin_amdgcn_smfmac_f32_16x16x64_bf16(__builtin_bit_cast(bf16x8, a), b, c, sidx, 0, 0)

__device__ __forceinline__ void patch_kv(unsigned tab, int r, s16x8& k0, s16x8& k1, s16x8& v0, s16x8& v1, bool on = true) {
  const int lane = fresh_lane(), hi = lane >> 5;
  if (on && (lane & 31) == kpos(r)) {
    k0 = *LDS_AT(const s16x8, tab + hi * 16); k1 = *LDS_AT(const s16x8, tab + 32 + hi * 16);
  }
  const short vv = *LDS_AT(const short, tab + 128 + (lane & 31) * 2);
  const int js = (on && hi == ((r >> 2) & 1)) ? (r >> 4) * 8 + ((r >> 3) & 1) * 4 + (r & 3) : -1;
#pragma unroll
  for (int e = 0; e < 8; ++e) { v0[e] = js == e ? vv : v0[e]; v1[e] = js == 8 + e ? vv : v1[e]; }
}

__device__ __forceinline__ void attn_safe(const u16* KV, int tile0, s16x8 q0, s16x8 q1,
                                       f32x16& o, float& l_out, int ptile, int prow, unsigned ptab) {
  const int lane = fresh_lane();
  float m = -INFINITY, l = 0.f;
  const int sidx = 0xDDDDDDDD;
  const __amdgpu_buffer_rsrc_t rs = __builtin_amdgcn_make_buffer_rsrc(const_cast<u16*>(KV), 0, 2 * S_ * D_ * 2, 0x00020000);
#pragma unroll
  for (int e = 0; e < 16; ++e) o[e] = 0.f;
  for (int tt = 0; tt < 64; ++tt) {
    const int t = tile0 + tt, off = t * 2048 + lane * 16;
    s16x8 k0 = __builtin_bit_cast(s16x8, __builtin_amdgcn_raw_buffer_load_b128(rs, off, 0, 16));
    s16x8 k1 = __builtin_bit_cast(s16x8, __builtin_amdgcn_raw_buffer_load_b128(rs, off + 1024, 0, 16));
    s16x8 v0 = __builtin_bit_cast(s16x8, __builtin_amdgcn_raw_buffer_load_b128(rs, off + S_ * D_ * 2, 0, 16));
    s16x8 v1 = __builtin_bit_cast(s16x8, __builtin_amdgcn_raw_buffer_load_b128(rs, off + S_ * D_ * 2 + 1024, 0, 16));
    if (t == ptile) patch_kv(ptab, prow, k0, k1, v0, v1);
    f32x16 sc;
#pragma unroll
    for (int e = 0; e < 16; ++e) sc[e] = 0.f;
    sc = MFMA32(k0, q0, sc); sc = MFMA32(k1, q1, sc);
    float mt = sc[0];
#pragma unroll
    for (int e = 1; e < 16; ++e) mt = fmaxf(mt, sc[e]);
    mt = fmaxf(mt, __shfl_xor(mt, 32, 64));
    const float mn = fmaxf(m, mt);
    const float alpha = __builtin_amdgcn_exp2f(m - mn);
    m = mn;
#pragma unroll
    for (int e = 0; e < 16; ++e) sc[e] = __builtin_amdgcn_exp2f(sc[e] - mn);
    float ls = 0.f;
#pragma unroll
    for (int e = 0; e < 16; ++e) ls += __uint_as_float(__float_as_uint(sc[e]) & 0xffff0000u);
    l = l * alpha + ls;
#pragma unroll
    for (int e = 0; e < 16; ++e) o[e] *= alpha;
    o = SMF32(v0, PLO(sc), o); o = SMF32(v1, PHI(sc), o);
  }
  l_out = l + __shfl_xor(l, 32, 64);
}

__device__ __forceinline__ float wave_fmax(float v) {
  asm volatile("s_nop 1\n\tv_max_f32_dpp %0, %0, %0 row_shr:1 row_mask:0xf bank_mask:0xf\n\ts_nop 1\n\tv_max_f32_dpp %0, %0, %0 row_shr:2 row_mask:0xf bank_mask:0xf\n\t"
               "s_nop 1\n\tv_max_f32_dpp %0, %0, %0 row_shr:4 row_mask:0xf bank_mask:0xf\n\ts_nop 1\n\tv_max_f32_dpp %0, %0, %0 row_shr:8 row_mask:0xf bank_mask:0xf\n\t"
               "s_nop 1\n\tv_max_f32_dpp %0, %0, %0 row_bcast:15 row_mask:0xa bank_mask:0xf\n\ts_nop 1\n\tv_max_f32_dpp %0, %0, %0 row_bcast:31 row_mask:0xc bank_mask:0xf\n\ts_nop 0"
               : "+v"(v));
  return __int_as_float(__builtin_amdgcn_readlane(__float_as_int(v), 63));
}

#define PSLP 8
#define WSLP 3
#define PRE1 8
#define PRE5 8
#define W3SLP 4
#define FSTART 10
#define OAUX 16
#define OMX 4
#define T0 2u
#define TW 10u
#define HW 2
#define HP1 1
#define POLLAT 11
#define X2AT 2
#define HPRIO 3
#define SSLP 1
#define SPRE 6
#define WGAPS {0, 1, 7, 8, 9, 10, 11, 12}
#define KVS 4
#define PNS 12
#define G0 5u
#define GS 2u
#define IAT 1u
#define YSLP 2
#define ISLP 8
#define KSLP 2
#define UAT 13u
#define UHW 0
#define PPSLP 2
#define XRSLP 2
#define MPRE 20
#define YD1 150
#define YD2 180
#define KDT 200
#define XDT 45
#define R2PRIO 0
#define LNPRIO 1
#define MSLP 4
#define BARG 0
struct Args {
  long long lay;
  const u16* a; u16* kv; u16* o; u16* xo; double* so;
  const u16* W; u16* wc;
  const u16 *wg, *wu, *wd, *wo, *w_r;
  unsigned* gran; float* csf; double* stats; unsigned* hand; u16* out;
};
typedef __attribute__((address_space(4))) const Args* ArgP;
#define WCAT 3
#define CAP0 1
#define PRE4 50
#define PRE7 20
#define UARR 12u
#define GF0 10
#define XGATE 8
#define ASLP 1
#define B15 3
#define KO 5
#define PRO 2
#define HS 2
#define XD2 90
#define TFL 4u
#define WMAP 0x32100333
#define SMAP 0
#define STCH 6
#define KPOLL 10
#define TPRIO 2
#define TSLP 9
__device__ __forceinline__ bool slow_cols(const u16* W, int tni) { return ((((unsigned)(uintptr_t)W >> 7) + tni) & 7) == 3; }
#define ARGS_HERE                                                                                                      \
  ArgP ap_ = ap; asm volatile("" : "+s"(ap_));                                                                          \
  const u16* A = ap_->a; const u16* __restrict__ W = ap_->W; u16* KV = ap_->kv; u16* Of = ap_->o;        \
  const u16* __restrict__ wg = ap_->wg; const u16* __restrict__ wu = ap_->wu; const u16* __restrict__ wd = ap_->wd;       \
  const u16* __restrict__ wo = ap_->wo; const u16* __restrict__ w_r = ap_->w_r; unsigned* gran = ap_->gran;              \
  float* csf = ap_->csf; const double* stats = ap_->stats; double* stats_out = ap_->so; unsigned* hand = ap_->hand; \
  u16* xout = ap_->xo; u16* __restrict__ out = ap_->out
#define AGAIN "s_getpc_b64 s[90:91]\n\ts_add_u32 s90, s90, layers_kernel@rel32@lo+4\n\ts_addc_u32 s91, s91, layers_kernel@rel32@hi+12\n\ts_setpc_b64 s[90:91]"
#define KEPT(c)                                                                                                        \
  c "{v[104:107]}"(gu[0]), c "{v[108:111]}"(gu[1]), c "{v[112:115]}"(gu[2]), c "{v[116:119]}"(gu[3]), c "{v[120:123]}"(gu[4]),      \
  c "{v[124:127]}"(gu[5]), c "{v[128:131]}"(gu[6]), c "{v[132:135]}"(gu[7]), c "{v[136:139]}"(gu[8]), c "{v[140:143]}"(gu[9]),      \
  c "{v[144:147]}"(gu[10]), c "{v[148:151]}"(gu[11]), c "{v[152:155]}"(gu[12]), c "{v[156:159]}"(gu[13]), c "{v[160:163]}"(gu[14]), \
  c "{v[164:167]}"(gu[15])
#define LAYER_HERE ArgP apl_ = ap; asm volatile("" : "+s"(apl_)); const bool LAY2 = apl_->lay != 0
__device__ __forceinline__ void layer_body(const int wid, const int bid, const unsigned lbase, const ArgP ap, const unsigned long long t_entry) {
  constexpr int K = 1024, NKT = 16, NST = 3, ABYTES = 16384, WBYTES = 24576, STB = ABYTES + WBYTES, ldw = 3 * D_;
  constexpr int TABA = NST * STB, ALPHA = TABA + 2048, CRED = ALPHA + 512;
  constexpr int ASB = 16384, FLG = 8 * ASB + 256;
  constexpr int DUM = FLG + 64, TAB = DUM + 256, RED = TAB + 512, SACT = RED + 4096, XML = SACT + 3584, UGL = XML + 4096, SPM = UGL + 1024, QOFF = 9 * ASB;
  constexpr int TABW = RED;
  static_assert(SPM + 2048 <= QOFF && CRED <= 8 * ASB && RED + 8192 <= QOFF && RED % 16 == 0, "lds");
  const int xcd = (bid & 1) * 4 + ((bid >> 1) & 3), loc = bid >> 3;
  const int tmi = (xcd / QNG) * QMX + loc / QNX, tni = (xcd % QNG) * QNX + loc % QNX;
  const int m0 = tmi * 128, n0 = tni * 64;
  const int omi = (xcd / (OMX / 2)) * OMX + loc / (32 / OMX), oni = (xcd % (OMX / 2)) * (32 / OMX) + loc % (32 / OMX);
  constexpr unsigned VOFF = (unsigned)S_ * D_ * 2;
  auto tile_at = [&](unsigned n) __attribute__((always_inline)) {
#pragma unroll 1
    for (int i = 0; i < 4 * SPIN_MAX; ++i) {
      if (__builtin_amdgcn_readfirstlane(*LDS_AT(const volatile unsigned, lbase + FLG + 36)) == n * 0x01010101u) break;
      __builtin_amdgcn_s_sleep(1);
    }
    asm volatile("" ::: "memory");
    PIN();
  };
  auto all_ge = [&](unsigned w, unsigned n) { return ((w + (128u - n) * 0x01010101u) & 0x80808080u) == 0x80808080u; };
  if (wid >= 8) {
    u32x4 gu[16];
    LAYER_HERE;
    constexpr int dk = 6;
    const int hw = wid - 8, b = bid;
    int nb = 0;
    constexpr int lim = NKT - 1;
    auto tick_nw = [&]() {
      PIN();
      if (nb < lim) { __builtin_amdgcn_s_barrier(); ++nb; } else __builtin_amdgcn_s_sleep(2);
      asm volatile("" ::: "memory");
      PIN();
    };
    auto tick = [&]() {
      asm volatile("s_waitcnt lgkmcnt(0)" ::: "memory");
      tick_nw();
    };
    auto fold16 = [&](f32x2 y0, f32x2 y1) __attribute__((always_inline)) {
      const auto q0 = __builtin_amdgcn_permlane32_swap(__float_as_uint(y0[0]), __float_as_uint(y1[0]), false, false);
      const auto q1 = __builtin_amdgcn_permlane32_swap(__float_as_uint(y0[1]), __float_as_uint(y1[1]), false, false);
      const auto q = __builtin_amdgcn_permlane16_swap(__float_as_uint(__uint_as_float(q0[0]) + __uint_as_float(q0[1])),
                                                      __float_as_uint(__uint_as_float(q1[0]) + __uint_as_float(q1[1])), false, false);
      return __uint_as_float(q[0]) + __uint_as_float(q[1]);
    };
    unsigned best, tag, rmd;
    float alpha, m, wsum;
    u32x4 tw;
    u32x2 xq;
    s16x8 sl[8];
    const int kcap = CAP0 + (tmi & 7);
    if (!LAY2) {
      ARGS_HERE;
      const int lane = fresh_lane(), t = hw * 64 + lane;
      const unsigned ep = ld_ag(gran);
      const int srow = m0 + tni * 8 + hw * 2;
      u32x4 xa[2][2], wv[8];
      const int wr_ = (3 - ((unsigned)(uintptr_t)W >> 7) - (tni & ~3)) & 7;
      const bool cpy = hw == 3 && wr_ < 4 && wr_ != (tni & 3), wld = hw < 3 ? wr_ != (tni & 3) : cpy;
      const int wcn = cpy ? (tni & ~3) + wr_ : tni, wcp = cpy ? (tni - wr_ - 1) & 3 : hw;
      const int wof = ((tmi * 64 + (lane >> 3)) * ldw + wcp * D_ + wcn * 64 + (lane & 7) * 8) * 2;
      auto ld_w = [&]() {
        const __amdgpu_buffer_rsrc_t rw = __builtin_amdgcn_make_buffer_rsrc(const_cast<u16*>(W), 0, wld ? D_ * ldw * 2 : 0, 0x00020000);
#pragma unroll
        for (int r = 0; r < 8; ++r) wv[r] = __builtin_amdgcn_raw_buffer_load_b128(rw, wof, r * 8 * ldw * 2, 0);
        PIN();
      };
      auto wcopy = [&]() __attribute__((always_inline)) {
        if (cpy) {
          const __amdgpu_buffer_rsrc_t rw = __builtin_amdgcn_make_buffer_rsrc(ap_->wc, 0, D_ * ldw * 2, 0x00020000);
#pragma unroll
          for (int r = 0; r < 8; ++r) __builtin_amdgcn_raw_buffer_store_b128(wv[r], rw, wof + r * 8 * ldw * 2, 0, 16);
        }
        PIN();
      };
      const int wat = ((hw == 3 ? SMAP : WMAP) >> (4 * (tmi & 7))) & 3;
      unsigned ps = 0u;
      auto stouch = [&](int at) __attribute__((always_inline)) {
        if (at == STCH && cpy) ps = __builtin_amdgcn_raw_buffer_load_b32(__builtin_amdgcn_make_buffer_rsrc(const_cast<u16*>(W), 0, D_ * ldw * 2, 0x00020000),
                                                                        (((tmi ^ 8) * 64 + lane) * ldw + wcp * D_ + wcn * 64) * 2, 0, 0);
        PIN();
      };
      auto ld_x = [&](int h) {
#pragma unroll
        for (int r = 0; r < 2; ++r) xa[r][h] = *reinterpret_cast<const u32x4*>(A + (size_t)(srow + r) * D_ + (h ^ (tmi >> 3)) * 512 + lane * 8);
        PIN();
      };
      ld_x(0);
      if (X2AT == 0) ld_x(1);
      if (wat == 0) ld_w();
      unsigned pc = 0u;
      if (hw == 3) {
        unsigned long long a0, a1;
        CODE_ADDR("layers_kernel", a0);
        CODE_ADDR("end_marker_kernel", a1);
        pc = touch_code(a0, a1, loc, lane);
      }
      if (hw == 1) pc = ld_ag(gran + ((lane & 1) ? GR_ROW + (m0 + tni * 8) * 4 : GR_KV + (tni * RK + tmi % RK) * 32));
      if (hw == 2) st_ag(gran + GR_N + bid * 64 + lane, lane + 1u);
      {
        const int gi = bid * 256 + t;
        if (gi < GRAN_N / 4) __builtin_amdgcn_raw_buffer_store_b128(u32x4{SENT, SENT, SENT, SENT}, __builtin_amdgcn_make_buffer_rsrc(hand, 0, GRAN_N * 4, 0x00020000), gi * 16, 0, 16);
        if (t < 24) __hip_atomic_store(stats_out + (t >> 3) * S_ + m0 + tni * 8 + (t & 7), 0.0, __ATOMIC_RELAXED, __HIP_MEMORY_SCOPE_AGENT);
      }
      tick();
      if (wat == 1) ld_w();
      if (X2AT == 1) ld_x(1);
      tick();
      if (wat == 2) ld_w();
      if (WCAT == 2) wcopy();
      if (X2AT == 2) ld_x(1);
      asm volatile("" :: "v"(ep));
      tick();
      if (wat == 3) ld_w();
      if (WCAT == 3) wcopy();
      if (X2AT == 3) ld_x(1);
      stouch(3);
#pragma unroll
      for (int i = 0; i < HW; ++i) { tick(); stouch(4 + i); }
      __builtin_amdgcn_s_setprio(HP1);
      PIN();
      tag = ep * 1664525u + 1013904223u;
      const __amdgpu_buffer_rsrc_t rsc = __builtin_amdgcn_make_buffer_rsrc(gran, 0, GR_N * 4, 0x00020000);
      {
        u32x4 od;
#pragma unroll
        for (int q = 0; q < 4; ++q) od[q] = ((lane & 7) >> 1) == q ? ((lane & 1) ? 0x3f800000u : 0x00003f80u) : 0u;
        f32x4 d[2] = {{0.f, 0.f, 0.f, 0.f}, {0.f, 0.f, 0.f, 0.f}};
#pragma unroll
        for (int r = 0; r < 8; ++r) d[r & 1] = __builtin_amdgcn_mfma_f32_16x16x32_bf16(__builtin_bit_cast(s16x8, od), __builtin_bit_cast(s16x8, wv[r]), d[r & 1], 0, 0, 0);
        u32x4 gr;
#pragma unroll
        for (int q = 0; q < 4; ++q) gr[q] = ((__float_as_uint(dpp_add<0x128, 0xf>(fadd(d[0][q], d[1][q]))) + 0x80u) & ~0xffu) | ((tag >> (8 * q)) & 0xffu);
        asm volatile("s_waitcnt vmcnt(0)" ::: "memory");
        __builtin_amdgcn_raw_buffer_store_b128(gr, rsc, (lane & 40) || !wld ? GR_N * 4 : (GR_COL + (wcn * 16 + tmi) * 192 + wcp * 64 + (lane & 7) * 8 + (lane >> 4) * 4) * 4, 0, 16);
      }
      asm volatile("" :: "v"(pc));
      tick_nw();
      stouch(6);
      {
        float xv[2][16], mean[2];
#pragma unroll
        for (int r = 0; r < 2; ++r) {
          float sm = 0.f;
#pragma unroll
          for (int i = 0; i < 4; ++i) {
            xv[r][2 * i] = __uint_as_float(xa[r][0][i] << 16); xv[r][2 * i + 1] = __uint_as_float(xa[r][0][i] & 0xffff0000u);
            xv[r][8 + 2 * i] = __uint_as_float(xa[r][1][i] << 16); xv[r][8 + 2 * i + 1] = __uint_as_float(xa[r][1][i] & 0xffff0000u);
          }
#pragma unroll
          for (int i = 0; i < 16; ++i) sm += xv[r][i];
          mean[r] = wave_sum(sm) * (1.0f / 1024.0f);
          tick();
          stouch(7 + r);
        }
        float vr[2];
#pragma unroll
        for (int r = 0; r < 2; ++r) {
          float v = 0.f;
#pragma unroll
          for (int i = 0; i < 16; ++i) { const float d = xv[r][i] - mean[r]; v += d * d; }
          vr[r] = wave_sum(v) * (1.0f / 1024.0f);
          if (r == 0) tick();
        }
        const float vv = lane & 2 ? vr[1] : vr[0], mm = lane & 2 ? mean[1] : mean[0];
        const float rin = 1.0f / rbf(sqrtf(rbf(rbf(vv) + 1e-5f)));
        if (lane < 4) __builtin_amdgcn_raw_buffer_store_b64(u32x2{__float_as_uint((lane & 1) ? rbf(mm) : rin), tag}, rsc, (GR_ROW + srow * 4 + lane * 2) * 4, 0, 16);
      }
      tick();
      float cs;
      while (nb < POLLAT) tick();
      asm volatile("" :: "v"(ps));
#pragma unroll 1
      for (int spin = 0;; ++spin) {
        u32x4 csv[4];
        const __amdgpu_buffer_rsrc_t rsw = __builtin_amdgcn_make_buffer_rsrc(gran, 0, hw < 3 ? GR_N * 4 : 0, 0x00020000);
#pragma unroll
        for (int i = 0; i < 4; ++i) csv[i] = __builtin_amdgcn_raw_buffer_load_b128(rsw, (GR_COL + (tni * 16 + (lane >> 4)) * 192 + hw * 64 + (lane & 15) * 4) * 4, i * (4 * 192 * 4), 16);
        const u32x4 rp = __builtin_amdgcn_raw_buffer_load_b128(rsc, (GR_ROW + (m0 + (t & 127)) * 4) * 4, 0, 16);
        PIN();
        tick(); tick();
        unsigned bad = 0u;
        float z[4];
#pragma unroll
        for (int q = 0; q < 4; ++q) {
          const unsigned tq = tag >> (8 * q);
          bad |= ((csv[0][q] ^ tq) | (csv[1][q] ^ tq)) | ((csv[2][q] ^ tq) | (csv[3][q] ^ tq));
          z[q] = fadd(fadd(__uint_as_float(csv[0][q] & ~0xffu), __uint_as_float(csv[1][q] & ~0xffu)), fadd(__uint_as_float(csv[2][q] & ~0xffu), __uint_as_float(csv[3][q] & ~0xffu)));
        }
        cs = fold16(f32x2{z[0], z[1]}, f32x2{z[2], z[3]});
        bad = (hw < 3 ? bad & 0xffu : 0u) | (rp[1] ^ tag) | (rp[3] ^ tag);
        alpha = __uint_as_float(rp[0]); m = __uint_as_float(rp[2]);
        if (none(bad != 0u) || spin >= SPIN_MAX) break;
      }
      if (tmi == 0 && hw < 3) st_ag(reinterpret_cast<unsigned*>(csf) + hw * 1024 + n0 + (lane & 15) * 4 + (lane >> 4), __float_as_uint(cs));
      unsigned c0, c1, c2;
      split3(cs, c0, c1, c2);
      tw = u32x4{c0 | (c1 << 16), c0 | (c2 << 16), c1 | (c0 << 16), 0u};
      best = rmd = 0u;
      wsum = 0.f;
      asm volatile("" : "=v"(xq), KEPT("="));
#pragma unroll
      for (int i = 0; i < 8; ++i) asm volatile("" : "=v"(sl[i]));
    } else {
      asm volatile("; kept registers" : KEPT("="));
      PIN();
      ARGS_HERE;
      const int lane = fresh_lane(), t = hw * 64 + lane;
      best = rmd = 0u;
      tag = __builtin_amdgcn_readfirstlane(*LDS_AT(const volatile unsigned, lbase + FLG + 20));
#pragma unroll
      for (int i = 0; i < 8; ++i) asm volatile("" : "=v"(sl[i]));
      unsigned sad;
      auto tickc = [&]() __attribute__((always_inline)) {
        tick();
        if (nb == kcap + 1) {
#pragma unroll
          for (int i = 0; i < 8; ++i) {
            const unsigned a = (sad ^ ((i >> 2) * 64) ^ (((i >> 1) & 1) * 16)) + (i & 1) * 12288;
            sl[i] = __builtin_shufflevector(__builtin_amdgcn_ds_read_tr16_b64_v4i16(LDS_AT(s16x4, a)), __builtin_amdgcn_ds_read_tr16_b64_v4i16(LDS_AT(s16x4, a + 1536)), 0, 1, 2, 3, 4, 5, 6, 7);
          }
        }
        PIN();
      };
      static_assert(CAP0 + 8 <= KPOLL, "the stage is taken before that");
      if (hw == 1) STAMP(4, 4);
      if (hw == 0) {
        __builtin_amdgcn_s_sleep(TPRE);
        tile_at(3u);
        __builtin_amdgcn_s_sleep(XGATE);
        STAMP(4, 3);
#pragma unroll 1
        for (int spin = 0; spin < SPIN_MAX; ++spin) {
          if (none(ld_ag(gran + GR_X + (tmi * RX + tni % RX) * 128 + 64 + lane) != tag)) break;
          __builtin_amdgcn_s_sleep(1);
        }
      }
      static_assert(UHW == 0, "");
      if (hw == 0) asm volatile("s_waitcnt vmcnt(0)" ::: "memory");
      if (hw == 0) STAMP(4, 5);
      asm volatile("" ::: "memory");
      PIN();
      __builtin_amdgcn_s_barrier();
      PIN();
      if (hw == 1) STAMP(4, 6);
      {
        const int ln = fresh_lane(), g4 = ln >> 4, tp = ln & 3, k = 8 * g4 + ((ln & 15) >> 2);
        sad = lbase + (kcap % NST) * STB + ABYTES + k * 384 + (((2 * (tp >> 1)) ^ (4 * ((k >> 1) & 1)) ^ (g4 & 1)) << 4) + 8 * (tp & 1) + (hw % 3) * 128;
      }
      wsum = __uint_as_float(__builtin_amdgcn_readfirstlane(*LDS_AT(const volatile unsigned, lbase + FLG + 60)));
      asm volatile("" : "+v"(sad), "=v"(tw), "=v"(alpha), "=v"(m));
      PIN();
      const int row = m0 + (t & 127);
      const __amdgpu_buffer_rsrc_t rsg = __builtin_amdgcn_make_buffer_rsrc(hand, 0, GRAN_N * 4, 0x00020000);
      if (hw < 2 || tmi == tni) {
      while (nb < HS) tickc();
#pragma unroll 1
      for (int spin = 0; spin < SPIN_MAX; ++spin) {
        const unsigned v = ld_ag(gran + GR_X + (tmi * RX + tni % RX) * 128 + lane);
        tickc();
        if (none(v != tag)) break;
      }
      asm volatile("" ::: "memory");
      PIN();
      const double s1 = ld_ag(stats + row), s2 = ld_ag(stats + S_ + row), sw = ld_ag(stats + 2 * S_ + row);
      tickc();
      float mean, var, mb, den;
      ln_from_sums(s1, s2, mean, var, mb, den);
      const float rin = frcp(den), g1 = 1.0f + rin;
      const float mb2 = rbf(mean + (mean - mb) * rin);
      const float den2 = rbf(__builtin_amdgcn_sqrtf(rbf(rbf(var * g1 * g1) + 1e-5f)));
      alpha = g1 * frcp(den2); m = (mb * rin + mb2) * frcp(g1);
      if (tmi == tni && hw >= 2) {
        const float r = (float)fma(-(double)mb, (double)wsum, sw) * rin;
        const unsigned rb = pack2(r, r);
        unsigned rkey = (rb ^ ((unsigned)((int)rb >> 31) | 0x80000000u)) & 0xffff0000u;
        if (r != r) rkey = 0u;
        rkey = wave_umax(rkey | (unsigned)(2047 - row));
        const unsigned md = __builtin_amdgcn_readlane(f2word(__uint_as_float((__float_as_uint(mb) >> 16) | (__float_as_uint(den) & 0xffff0000u))), (2047 - (int)(rkey & 0xffffu)) & 63);
        __builtin_amdgcn_raw_buffer_store_b64(u32x2{rkey, md}, rsg, lane < NREP ? lane * 256 + (tmi * 2 + hw - 2) * 8 : GRAN_N * 4, 0, 16);
      }
      }
      while (nb < KPOLL) tickc();
#pragma unroll 1
      for (int spin = 0; spin < SPIN_MAX; ++spin) {
        const u32x2 v = __builtin_amdgcn_raw_buffer_load_b64(rsg, (bid % NREP) * 256 + (lane & 31) * 8, 0, 16);
        tick(); tick();
        if (none(umax(v[0], v[1]) == SENT)) {
          best = wave_umax(v[0]);
          rmd = __builtin_amdgcn_readlane(v[1], ((2047 - (int)(best & 0xffffu)) >> 6) & 31);
          break;
        }
      }
      const int ix = (2047 - (int)(best & 0xffffu)) & 2047;
      if (row == ix) alpha = 0.f;
      xq = __builtin_amdgcn_raw_buffer_load_b64(__builtin_amdgcn_make_buffer_rsrc(const_cast<u16*>(A), 0, S_ * D_ * 2, 0x00020000), (ix * D_ + hw * 256 + (lane & 3) * 64 + (lane >> 2) * 4) * 2, 0, 16);
      PIN();
    }
    tag = __builtin_amdgcn_readfirstlane(tag);
    wsum = __uint_as_float(__builtin_amdgcn_readfirstlane(__float_as_uint(wsum)));
    const int lane = fresh_lane(), t = hw * 64 + lane;
    {
      unsigned a0, a1, a2;
      split3(-m, a0, a1, a2);
      u32x4 ta = {a0 | (a0 << 16), a1 | (a0 << 16), a1 | (a2 << 16), 0u};
      asm volatile("" : "+v"(tw), "+v"(ta), "+v"(alpha));
      __builtin_amdgcn_s_setprio(TPRIO);
      if (nb < lim) {
        while (nb < lim) tick();
        __builtin_amdgcn_s_sleep(TSLP);
      }
      if (!LAY2 && hw < 3) *LDS_AT(u32x4, lbase + TABW + (hw * 64 + (lane & 15) * 4 + (lane >> 4)) * 16) = tw;
      if (t < 128) {
        *LDS_AT(u32x4, lbase + TABA + t * 16) = ta;
        *LDS_AT(float, lbase + ALPHA + t * 4) = alpha;
      }
    }
    if (t >= 192 && t < 196) *LDS_AT(u32x4, lbase + CRED + ((t >> 1) & 1) * 512 + (t & 1) * 128) = u32x4{0u, 0u, 0u, 0u};
    asm volatile("s_waitcnt lgkmcnt(0)" ::: "memory");
    PIN();
    __builtin_amdgcn_s_barrier();
    PIN();
    *LDS_AT(u32x4, lbase + XML + t * 16) = u32x4{SENT, SENT, SENT, SENT};
    *LDS_AT(u32x4, lbase + SPM + (t & 127) * 16) = u32x4{SENT, SENT, SENT, SENT};
    if (hw == 0) {
      unsigned v;
      asm volatile("v_mov_b32 %0, 0\n\tv_writelane_b32 %0, 2, 0\n\tv_writelane_b32 %0, %1, 5\n\tv_writelane_b32 %0, %2, 7"
                   : "=&v"(v) : "s"(tag), "s"(best));
      lds_wr(lbase + FLG + lane * 4, v);
    }
    const int idx = LAY2 ? (2047 - (int)(best & 0xffffu)) & 2047 : -64;
    const bool p1blk = (oni & 7) != 7, fast = tmi < 8, own = fast && (loc & 1);
    const int fg = xcd * 28 + (p1blk ? loc - (loc >> 3) : 0), ow = (xcd & 3) * 16 + (loc >> 1);
    static_assert(OMX == 4, "oni % 8 = loc % 8");
#define GLD(a, b) "global_load_dwordx4 v[" #a ":" #b "], %0, off\n\tv_lshl_add_u64 %0, %0, 0, %1\n\t"
#define GLD4(...) asm volatile(__VA_ARGS__ : "+v"(p) : "s"((unsigned long long)(F_ * 2)), KEPT() : "memory")
    auto gptr = [&](const u16* wg, const u16* wu) {
      const int ln = fresh_lane();
      return ((ln & 2) ? wu : wg) + (size_t)(hw * 256 + (ln >> 2) * 4) * F_ + fg * 16 + (ln & 1) * 8;
    };
    auto gather = [&](const u16*& p, int e) __attribute__((always_inline)) {
      if (e == 0) GLD4(GLD(104, 107) GLD(108, 111) GLD(112, 115) GLD(116, 119));
      else if (e == 1) GLD4(GLD(120, 123) GLD(124, 127) GLD(128, 131) GLD(132, 135));
      else if (e == 2) GLD4(GLD(136, 139) GLD(140, 143) GLD(144, 147) GLD(148, 151));
      else GLD4(GLD(152, 155) GLD(156, 159) GLD(160, 163) GLD(164, 167));
      p += 60 * F_;
    };
    auto dmask = [&](int q) { const int ln = fresh_lane(); return ((ln >> 1) & 1) == q ? ((ln & 1) ? 0xffff0000u : 0x0000ffffu) : 0u; };
    unsigned dm0, dm1, dad;
    auto gu_setup = [&]() __attribute__((always_inline)) {
      const int ln = fresh_lane();
      dm0 = dmask(0); dm1 = dmask(1);
      dad = lbase + SACT + ((ln & 3) * 8 + (ln >> 5) * 4 + ((ln >> 4) & 1)) * 16 + hw * 4;
      asm volatile("" : "+v"(dm0), "+v"(dm1), "+v"(dad));
    };
    auto gate_up = [&](u32x2 x, unsigned md) __attribute__((always_inline)) {
      const unsigned rin = __builtin_amdgcn_readfirstlane(__float_as_uint(frcp(__uint_as_float(md & 0xffff0000u))));
      asm volatile("s_getpc_b64 s[90:91]\n\ts_add_u32 s90, s90, gate_up_code@rel32@lo+4\n\ts_addc_u32 s91, s91, gate_up_code@rel32@hi+12\n\ts_swappc_b64 s[92:93], s[90:91]"
                   : "+{v[100:101]}"(x) : "{s84}"(md << 16), "{s85}"(md & 0xffff0000u), "{s86}"(rin), "{v99}"(dad), "{v102}"(dm0), "{v103}"(dm1), KEPT()
                   : "v84", "v85", "v86", "v87", "v88", "v89", "v90", "v91", "v92", "v93", "v94", "v95", "v96", "v97", "s88", "s89", "s90", "s91", "s92", "s93", "scc", "memory");
    };
    auto act_diag = [&](int so = 0) __attribute__((always_inline)) {
      const int ln = fresh_lane();
      const f32x4 g4 = *LDS_AT(const volatile f32x4, lbase + SACT + so + (ln >> 2) * 16), u4 = *LDS_AT(const volatile f32x4, lbase + SACT + so + 256 + (ln >> 2) * 16);
      float gv = fadd(fadd(g4[0], g4[1]), fadd(g4[2], g4[3])), uv = fadd(fadd(u4[0], u4[1]), fadd(u4[2], u4[3]));
      rbf2(gv, uv);
      const float av = rbf(gv * frcp(1.0f + __expf(-gv))) * uv;
      const unsigned ap2 = pack2(av, av);
      return __builtin_bit_cast(s16x4, u32x2{ap2 & dm0, ap2 & dm1});
    };
    auto down = [&](const s16x4& a, const u32x2 (&w)[16]) __attribute__((always_inline)) {
      f32x4 d[2] = {{0.f, 0.f, 0.f, 0.f}, {0.f, 0.f, 0.f, 0.f}};
      static_for<0, 16>([&](auto fc) __attribute__((always_inline)) {
        constexpr int f = decltype(fc)::value;
        d[f & 1] = __builtin_amdgcn_mfma_f32_4x4x4bf16_1k(a, __builtin_bit_cast(s16x4, w[f]), d[f & 1], 4, f, 0);
      });
      f32x4 z;
#pragma unroll
      for (int e = 0; e < 4; ++e) { z[e] = d[0][e] + d[1][e]; asm volatile("" : "+v"(z[e])); }
      return u32x4{f2word(z[0]), f2word(z[1]), f2word(z[2]), f2word(z[3])};
    };
    auto fold8 = [&](f32x2 y0, f32x2 y1) __attribute__((always_inline)) { return dpp_add<0x128, 0xf>(fold16(y0, y1)); };
    auto fold4 = [&](f32x2 y0, f32x2 y1) __attribute__((always_inline)) { return dpp_add<0x124, 0xf>(fold8(y0, y1)); };
    auto y_sum = [&](const __amdgpu_buffer_rsrc_t rs, auto nbc) __attribute__((always_inline)) {
      constexpr int NB = decltype(nbc)::value, N = 14 / NB;
      const int ln = fresh_lane(), src = (GRAN_N + (ln >> 2) * D_ + ow * 16 + (ln & 3) * 4) * 4;
      f32x2 y0 = {0.f, 0.f}, y1 = {0.f, 0.f};
#pragma unroll 1
      for (int bt = 0; bt < NB; ++bt) {
        f32x2 z0, z1;
#pragma unroll 1
        for (int spin = 0; spin < SPIN_MAX; ++spin) {
          u32x4 v[N];
#pragma unroll
          for (int i = 0; i < N; ++i) v[i] = __builtin_amdgcn_raw_buffer_load_b128(rs, src, (bt * N + i) * (16 * D_ * 4), 16);
          z0 = y0; z1 = y1;
#pragma unroll
          for (int i = 0; i < N; ++i) {
            z0 = f32x2{fadd(z0[0], __uint_as_float(v[i][0])), fadd(z0[1], __uint_as_float(v[i][1]))};
            z1 = f32x2{fadd(z1[0], __uint_as_float(v[i][2])), fadd(z1[1], __uint_as_float(v[i][3]))};
          }
          const float chk = fadd(fadd(z0[0], z0[1]), fadd(z1[0], z1[1]));
          if (none(chk != chk)) break;
          unsigned mx = 0u;
#pragma unroll
          for (int i = 0; i < N; ++i) mx = umax(umax(umax(mx, v[i][0]), v[i][1]), umax(v[i][2], v[i][3]));
          if (none(mx == SENT)) break;
          __builtin_amdgcn_s_sleep(YSLP);
          asm volatile("" ::: "memory");
        }
        y0 = z0; y1 = z1;
      }
      return fold4(y0, y1);
    };
    [&]() __attribute__((always_inline)) {
    ARGS_HERE;
    const unsigned l16 = lane * 16;
    unsigned* kvflag = gran + GR_KV + ((LAY2 ? 16 : 0) + tni) * (32 * RK);
    auto wait_sig = [&](int sh) {
#pragma unroll 1
      for (int i = 0; i < 4 * SPIN_MAX; ++i) {
        if (((__builtin_amdgcn_readfirstlane(*LDS_AT(const volatile unsigned, lbase + FLG + 32)) >> sh) & 7u) >= 4u) break;
        __builtin_amdgcn_s_sleep(SSLP);
      }
      asm volatile("" ::: "memory");
      PIN();
    };
    PIN();
    asm volatile("s_waitcnt lgkmcnt(0)" ::: "memory");
    if (!LAY2) asm volatile("s_waitcnt vmcnt(0)" ::: "memory");
    __builtin_amdgcn_s_barrier();
    PIN();
    __builtin_amdgcn_s_setprio(HPRIO);
    if (hw < 3) {
      __builtin_amdgcn_s_sleep(SPRE);
      wait_sig(0);
      __builtin_amdgcn_raw_buffer_store_b32(tag, __builtin_amdgcn_make_buffer_rsrc(kvflag, 0, hw == 0 ? RK * 128 : 0, 0x00020000), lane * 128 + tmi * 4, 0, 16);
      if (hw == 0) STAMP(LAY2 ? 5 : 1, 16);
    }
    if (hw == 3) {
      __builtin_amdgcn_s_sleep(KVS);
      const unsigned kv0 = ld_ag(kvflag + (tmi % RK) * 32 + (lane & 15));
      PIN();
      const char* cnt = reinterpret_cast<const char*>(gran + GR_N + bid * 64 + 2);
      unsigned vk[8], vp = lbase + FLG + 8 + (lane & 7), pw = 0u;
#pragma unroll
      for (int j = 0; j < 8; ++j) { vk[j] = l16 + (j >> 2) * 131072u + ((j >> 1) & 1) * 4096u + (j & 1) * (VOFF - 2048u); asm volatile("" : "+v"(vk[j])); }
#define G4(a, b, c, d, sc) "s_nop 0\n\tglobal_load_lds_dwordx4 %" #a ", %8" sc "\n\tglobal_load_lds_dwordx4 %" #b ", %8 offset:1024" sc "\n\t"             \
                       "global_load_lds_dwordx4 %" #c ", %8 offset:2048" sc "\n\tglobal_load_lds_dwordx4 %" #d ", %8 offset:3072" sc "\n\t"
#define M4 "s_addk_i32 m0, 0x1000\n\t"
      auto stage = [&](const char* sp, unsigned dst) __attribute__((always_inline)) {
        asm volatile("s_mov_b32 m0, %9\n\t" G4(0, 0, 1, 1, " sc1") M4 G4(2, 2, 3, 3, " sc1") M4 G4(4, 4, 5, 5, " sc1") M4 G4(6, 6, 7, 7, " sc1")
                     :: "v"(vk[0]), "v"(vk[1]), "v"(vk[2]), "v"(vk[3]), "v"(vk[4]), "v"(vk[5]), "v"(vk[6]), "v"(vk[7]), "s"(sp), "s"(dst) : "m0", "scc", "memory");
      };
      auto count = [&]() __attribute__((always_inline)) {
        asm volatile("s_mov_b32 m0, %2\n\ts_nop 0\n\tglobal_load_lds_dword %0, %1 sc1" :: "v"(0u), "s"(cnt), "s"(lbase + FLG - 252) : "m0", "memory");
        cnt += 8;
      };
      auto freed = [&](unsigned n) __attribute__((always_inline)) {
        if (!none(pw < n)) {
#pragma unroll 1
          for (int i = 0; i < 4 * SPIN_MAX; ++i) {
            pw = *LDS_AT(const volatile unsigned char, vp);
            if (none(pw < n)) break;
            __builtin_amdgcn_s_sleep(W3SLP);
          }
        }
        asm volatile("" ::: "memory");
      };
      unsigned long long ord = (0xfedcba9876543210ull >> 4 & ~((1ull << 4 * (tmi & 7)) - 1ull)) | (0xfedcba9876543210ull & ((1ull << 4 * (tmi & 7)) - 1ull));
      ord ^= (tmi & 8) * 0x1111111111111111ull;
      unsigned seen;
      const char* kvb = reinterpret_cast<const char*>(KV) + (size_t)tni * (128 * 2048);
      auto panel = [&]() __attribute__((always_inline)) {
        const unsigned pn = (unsigned)ord & 15u;
        ord >>= 4;
        if (__builtin_expect(!((seen >> pn) & 1u), 0)) {
#pragma unroll 1
          for (int i = 0; i < SPIN_MAX && !((seen >> pn) & 1u); ++i) seen = (unsigned)__ballot(ld_ag(kvflag + (tmi % RK) * 32 + (lane & 15)) == tag);
        }
        asm volatile("" ::: "memory");
        return kvb + pn * 8192u;
      };
      asm volatile("" : "+s"(ord), "+s"(kvb), "+s"(cnt));
      asm volatile("" : "+v"(vp), "+v"(pw));
      PIN();
      seen = (unsigned)__ballot(kv0 == tag);
      const char* sp = panel();
      wait_sig(16);
      stage(sp, lbase + 2 * ASB);
      count();
      const int ksp = LAY2 ? PNS / 2 : -1, ksg = LAY2 ? 64 : GF0;
      const u16* gp = gptr(wg, wu);
#pragma unroll 1
      for (int k = 1; k < 15; ++k) {
        if (__builtin_expect(k == ksp, 0)) {
          gu_setup();
          gate_up(xq, rmd);
          PIN();
          *LDS_AT(volatile unsigned char, lbase + FLG + 7) = (unsigned char)0x7f;
        }
        freed((unsigned)(0xba9876543211000ull >> 4 * k) & 15u);
        stage(sp + 2048, lbase + ((2 * k + 1) & 7) * ASB);
        if (__builtin_expect(k == 1, 0)) { cnt -= 4; count(); cnt -= 4; }
        sp = panel();
        stage(sp, lbase + ((2 * k + 2) & 7) * ASB);
        count();
        if (__builtin_expect((unsigned)(k - ksg) < 4u, 0)) gather(gp, k - ksg);
      }
      freed(12u);
      glds16s<true>(csf + 4096 + tni * 128, l16, lbase + UGL);
      stage(sp + 2048, lbase + 7 * ASB);
      count();
      const unsigned vwo = (unsigned)((lane >> 3) * D_ + oni * 64 + ((lane & 7) ^ ((lane >> 3) & 6)) * 8) * 2u;
      constexpr unsigned vs = 8u * D_ * 2u - 1024u;
#pragma unroll 1
      for (int ns = 0; ns < 6; ++ns) {
        freed((unsigned)(ns + 25) >> 1);
        const char* wp = reinterpret_cast<const char*>(wo) + (size_t)ns * (128 * D_ * 2);
#pragma unroll
        for (int j = 0; j < 4; ++j)
          glds16x4<false>(wp + j * (32u * D_ * 2u), vwo, vwo + vs, vwo + 2 * vs, vwo + 3 * vs, lbase + ns * ASB + j * 4096);
      }
      asm volatile("s_waitcnt vmcnt(0)" ::: "memory");
      return;
    }
    if (!LAY2) {
      auto until = [&](unsigned n) {
        while ((__builtin_amdgcn_readfirstlane(*LDS_AT(const volatile unsigned, lbase + FLG + 8)) & 255u) < n) __builtin_amdgcn_s_sleep(16);
        asm volatile("" ::: "memory");
        PIN();
      };
      if (hw != 1) {
        constexpr int N2 = HAND_N + Y_N, X3N = 3 * D_ / 2, N0 = X3N + PS_N;
        const __amdgpu_buffer_rsrc_t rsi = __builtin_amdgcn_make_buffer_rsrc(hand + (hw ? GRAN_N : 2 * HAND_N - X3N), 0, (hw ? N2 : N0) * 4, 0x00020000);
        until(IAT);
#pragma unroll 1
        for (int i = 0; i < ((hw ? N2 : N0) / 4 + 16383) / 16384; ++i) {
          __builtin_amdgcn_raw_buffer_store_b128(u32x4{SENT, SENT, SENT, SENT}, rsi, ((i * 256 + bid) * 64 + lane) * 16, 0, 16);
          __builtin_amdgcn_s_sleep(ISLP);
        }
      }
      const size_t og = (size_t)((fg - xcd * 28) / 7 * 256 + lane) * F_ + (xcd * 7 + fg % 7) * 64;
      const u16* p = gptr(wg, wu);
#pragma unroll 1
      for (unsigned n = T0; n < T0 + 13u; ++n) {
        until(n);
        if (hw == 1 && n < T0 + 8u) {
          const unsigned p0 = touch((((n - T0) & 1u) ? wu : wg) + og + (size_t)((n - T0) >> 1) * (64 * F_));
          asm volatile("" :: "v"(p0));
        }
        if (hw == 2 && n == TFL) {
          const int ln = fresh_lane() & 15;
          const unsigned p1 = ld_ag(gran + (ln < 4 ? GR_O + (((ln >> 1) * 16 + omi) * RO + oni % RO) * 64 + (ln & 1) * 32 :
                                            (ln < 8 ? GR_X + (tmi * RX + tni % RX) * 128 + (ln & 3) * 32 : GR_KV + ((16 + tni) * RK + tmi % RK) * 32)));
          asm volatile("" :: "v"(p1));
        }
        if (hw == 2 && n == TW) {
          const int ln = fresh_lane();
          unsigned long long a0, a1;
          CODE_ADDR("layers_kernel", a0); CODE_ADDR("end_marker_kernel", a1);
          const unsigned p1 = touch_code(a0, a1, 3 + loc, ln), p0 = touch(wo + (size_t)(768 + (loc / (32 / OMX)) * (256 / OMX) + ln) * D_ + oni * 64);
          asm volatile("" :: "v"(p0), "v"(p1));
        }
        if (n >= G0 && (n - G0) % GS == 0u && (n - G0) / GS < 4u) gather(p, (n - G0) / GS);
        if (n == UAT && hw == UHW) {
          u32x4 a[4][2], c[2];
#pragma unroll
          for (int i = 0; i < 4; ++i)
#pragma unroll
            for (int h = 0; h < 2; ++h) a[i][h] = *reinterpret_cast<const u32x4*>(wo + (size_t)(tni * 64 + tmi * 4 + i) * D_ + h * 512 + lane * 8);
#pragma unroll
          for (int h = 0; h < 2; ++h) c[h] = *reinterpret_cast<const u32x4*>(w_r + h * 512 + lane * 8);
          auto dot = [&](unsigned x, unsigned y, float z) { return __builtin_amdgcn_fdot2_f32_bf16(__builtin_bit_cast(bf16x2, x), __builtin_bit_cast(bf16x2, y), z, false); };
          PIN();
          __builtin_amdgcn_s_setprio(0);
#pragma unroll 1
          for (int i = 0; i < SPIN_MAX; ++i) {
            if (__builtin_amdgcn_readfirstlane(*LDS_AT(const volatile unsigned, lbase + FLG + 16)) >= UARR) break;
            __builtin_amdgcn_s_sleep(4);
          }
          asm volatile("s_waitcnt vmcnt(0)" ::: "memory");
          PIN();
          float wm = 0.f;
#pragma unroll
          for (int e = 0; e < 8; ++e) wm = dot(c[e >> 2][e & 3], 0x3f803f80u, wm);
          wm = wave_sum(wm);
          lds_wr(lbase + FLG + 60, __float_as_uint(wm));
          wm *= -1.0f / 1024.0f;
          f32x2 u[2], g[2];
#pragma unroll
          for (int i = 0; i < 4; ++i) {
            float p1 = 0.f, pw = 0.f, p2 = 0.f;
#pragma unroll
            for (int e = 0; e < 8; ++e) {
              const unsigned x = a[i][e >> 2][e & 3];
              p1 = dot(x, 0x3f803f80u, p1); pw = dot(x, c[e >> 2][e & 3], pw); p2 = dot(x, x, p2);
            }
            u[i >> 1][i & 1] = fmaf(p1, wm, pw); g[i >> 1][i & 1] = p2;
          }
          float us = fold4(u[0], u[1]), gs = fold4(g[0], g[1]);
          us = dpp_add<0x121, 0xf>(dpp_add<0x122, 0xf>(us)); gs = dpp_add<0x121, 0xf>(dpp_add<0x122, 0xf>(gs));
          __builtin_amdgcn_raw_buffer_store_b32(__float_as_uint((lane & 1) ? __builtin_amdgcn_sqrtf(gs) * (1.0f / 32.0f) : us),
              __builtin_amdgcn_make_buffer_rsrc(csf + 4096 + tni * 128, 0, 512, 0x00020000), (lane & 14) ? 512 : ((lane & 1) * 64 + tmi * 4 + (lane >> 4)) * 4, 0, 16);
        }
      }
      if (hw == 2 && bid == 0) {
        const __amdgpu_buffer_rsrc_t rsc = __builtin_amdgcn_make_buffer_rsrc(gran, 0, GR_N * 4, 0x00020000);
#pragma unroll 1
        for (int spin = 0; spin < SPIN_MAX; ++spin) {
          bool ok = true;
#pragma unroll
          for (int i = 0; i < 4; ++i) ok &= __builtin_amdgcn_raw_buffer_load_b32(rsc, (GR_ROW + (i * 64 + lane) * 32 + 1) * 4, 0, 16) == tag;
          if (__all(ok)) break;
          __builtin_amdgcn_s_sleep(8);
          asm volatile("" ::: "memory");
        }
        if (lane == 0) st_ag(gran, tag);
      }
      return;
    }
    unsigned* X2 = hand + GRAN_N + Y_N;
    unsigned* XS = X2 + X2_N;
    unsigned* P4 = XS + XS_N;
    unsigned* PA = P4 + P4_N;
    const __amdgpu_buffer_rsrc_t rsh = __builtin_amdgcn_make_buffer_rsrc(hand, 0, HAND_N * 4, 0x00020000);
    auto signal = [&](unsigned ph) {
      asm volatile("" ::: "memory");
      PIN();
      *LDS_AT(volatile unsigned char, lbase + FLG + 4 + hw) = (unsigned char)ph;
    };
    auto wait_all = [&](unsigned ph) {
#pragma unroll 1
      for (int i = 0; i < SPIN_MAX; ++i) {
        if (all_ge(__builtin_amdgcn_readfirstlane(*LDS_AT(const volatile unsigned, lbase + FLG + 4)), ph)) break;
        __builtin_amdgcn_s_sleep(WSLP);
      }
      asm volatile("" ::: "memory");
      PIN();
    };
    gu_setup();
    gate_up(xq, rmd);
    signal(hw == 2 ? 2u : 0x7fu);
    if (wid == 8) STAMP(dk, 2);
    if (hw < 2) {
#pragma unroll 1
      for (int i = 0; i < SPIN_MAX; ++i) {
        if (__builtin_amdgcn_readfirstlane(*LDS_AT(const volatile unsigned, lbase + FLG)) >= FSTART) break;
        __builtin_amdgcn_s_sleep(WSLP);
      }
      asm volatile("" ::: "memory");
      PIN();
      if (wid == 8) STAMP(dk, 10);
      u32x2 w[16];
      const __amdgpu_buffer_rsrc_t rd_ = __builtin_amdgcn_make_buffer_rsrc(const_cast<u16*>(wd), 0, D_ * F_ * 2, 0x00020000);
      auto ldw = [&](int c) __attribute__((always_inline)) {
#pragma unroll
        for (int f = 0; f < 16; ++f) w[f] = __builtin_amdgcn_raw_buffer_load_b64(rd_, lane * 8 + c * 512 + (f & 1) * 2048, (fg * 16 + (f & ~1)) * (D_ * 2) + hw * 1024, 0);
        PIN();
      };
      ldw(0);
      const unsigned pt = __builtin_amdgcn_raw_buffer_load_b32(rd_, (lane >> 2) * (D_ * 2) + (lane & 3) * 128 + 512, fg * 16 * (D_ * 2) + hw * 1024, 0);
      int yo = p1blk ? (GRAN_N + fg * D_ + hw * 512) * 4 + lane * 16 : HAND_N * 4;
      asm volatile("" : "+v"(yo));
      wait_all(2u);
      if (wid == 8) STAMP(dk, 3);
      const s16x4 a = act_diag();
      const u32x4 y0 = down(a, w);
      PIN();
      ldw(1);
      __builtin_amdgcn_raw_buffer_store_b128(y0, rsh, yo, 0, 16);
      PIN();
      __builtin_amdgcn_raw_buffer_store_b128(down(a, w), rsh, yo + 1024, 0, 16);
      asm volatile("" :: "v"(pt));
      if (wid == 8) STAMP(dk, 4);
    }
    PIN();
    [&]() __attribute__((always_inline)) {
    const int lane = fresh_lane();
    const float rmb = __uint_as_float(rmd << 16);
    auto first_words = [&](int src, auto nc) __attribute__((always_inline)) {
#pragma unroll 1
      for (int spin = 0; spin < SPIN_MAX; ++spin) {
        unsigned mx = 0u;
#pragma unroll
        for (int i = 0; i < decltype(nc)::value; ++i) mx = umax(mx, __builtin_amdgcn_raw_buffer_load_b32(rsh, src, i * (56 * D_ * 4), 16));
        if (none(mx == SENT)) break;
        __builtin_amdgcn_s_sleep(PSLP);
        asm volatile("" ::: "memory");
      }
    };
    if (hw == 2) {
      wait_all(2u);
      if (own) {
        const float rden = __uint_as_float(rmd & 0xffff0000u), rrin = frcp(rden);
        const int col = ow * 16 + (lane & 3) * 4 + (lane >> 4);
        const unsigned xo2 = ld_ag(reinterpret_cast<const unsigned*>(A + (size_t)idx * D_ + (col & ~1)));
        __builtin_amdgcn_s_sleep(PRE1);
        first_words((GRAN_N + (lane < 56 ? lane : 0) * D_ + ow * 16) * 4, std::integral_constant<int, 4>{});
        const float y = y_sum(rsh, std::integral_constant<int, 2>{});
        const unsigned rk = best >> 16;
        const float rv = __uint_as_float(((rk & 0x8000u) ? (rk & 0x7fffu) : (~rk & 0xffffu)) << 16);
        const float rs = rbf(frcp(1.0f + __expf(-rv)));
        const float x1v = __uint_as_float((col & 1) ? xo2 & 0xffff0000u : xo2 << 16);
        float h0 = x1v, h1 = x1v;
        ln_apply2(h0, h1, rmb, rden, rrin);
        const float x2v = rbf(x1v + rbf(h0 + rbf(rbf(y) * rs)));
        const auto pr = __builtin_amdgcn_permlane16_swap(__float_as_uint(x2v), __float_as_uint(x2v), false, false);
        __builtin_amdgcn_raw_buffer_store_b32(f2word(__uint_as_float((pr[0] >> 16) | pr[1])), rsh, (lane & 28) ? HAND_N * 4 : (int)(X2 - hand) * 4 + col * 2, 0, 16);
        const float d = x2v - rmb;
        const float sd = wave_sum(d), sq = wave_sum(d * d);
        if (lane < 2) st_ag(XS + ow * 2 + lane, f2word(lane ? sq : sd));
        STAMP(dk, 5);
      }
    }
    {
      const int ks = kcap ^ (tmi & 8), g4 = lane >> 4;
      const int src = (int)(X2 - hand) * 4 + ks * 128 + g4 * 16;
      int dst = (lane & 12) ? HAND_N * 4 : (int)((P4 - hand) + (tni * 16 + ks) * 192 + hw * 64 + (lane & 2) * 16 + (lane & 1) * 8 + (g4 >> 1) * 16 + (g4 & 1) * 4) * 4;
      asm volatile("" : "+v"(dst));
      u32x4 x0, x1;
      if (!(hw == 2 && own)) __builtin_amdgcn_s_sleep(PRE4);
#pragma unroll 1
      for (int spin = 0; spin < SPIN_MAX; ++spin) {
        x0 = __builtin_amdgcn_raw_buffer_load_b128(rsh, src, 0, 16); x1 = __builtin_amdgcn_raw_buffer_load_b128(rsh, src + 64, 0, 16);
        if (none(umax(umax(umax(x0[0], x0[1]), umax(x0[2], x0[3])), umax(umax(x1[0], x1[1]), umax(x1[2], x1[3]))) == SENT)) break;
        __builtin_amdgcn_s_sleep(PSLP);
        asm volatile("" ::: "memory");
      }
      f32x4 d[4];
#pragma unroll
      for (int i = 0; i < 4; ++i) d[i] = MFMA16(sl[2 * i], __builtin_bit_cast(s16x8, x0), (f32x4{0.f, 0.f, 0.f, 0.f}));
#pragma unroll
      for (int i = 0; i < 4; ++i) d[i] = MFMA16(sl[2 * i + 1], __builtin_bit_cast(s16x8, x1), d[i]);
      u32x4 o;
#pragma unroll
      for (int e = 0; e < 4; ++e) o[e] = f2word((lane & 2) ? ((lane & 1) ? d[3][e] : d[2][e]) : ((lane & 1) ? d[1][e] : d[0][e]));
      __builtin_amdgcn_raw_buffer_store_b128(o, rsh, dst, 0, 16);
      PIN();
      if (wid == 8) STAMP(dk, 6);
    }
    if (hw == 2) {
      const int col = (lane & 15) * 4 + (lane >> 4), src = (int)((P4 - hand) + (tni * 16 + (lane >> 4)) * 192 + (lane & 15) * 4) * 4;
      const __amdgpu_buffer_rsrc_t rsq = __builtin_amdgcn_make_buffer_rsrc(hand, 0, fast ? HAND_N * 4 : 0, 0x00020000);
      if (own) {
#pragma unroll
        for (int i = 0; i < 14; ++i)
          __builtin_amdgcn_raw_buffer_store_b128(u32x4{SENT, SENT, SENT, SENT}, rsh, (GRAN_N + (i * 16 + (lane >> 2)) * D_ + ow * 16 + (lane & 3) * 4) * 4, 0, 16);
      }
      float cs[3], val[3];
#pragma unroll
      for (int p = 0; p < 3; ++p) cs[p] = __uint_as_float(ld_ag(reinterpret_cast<const unsigned*>(csf) + p * 1024 + n0 + col));
      u32x2 xs;
      __builtin_amdgcn_s_sleep(PRE7);
      first_words((int)((P4 - hand) + (tni * 16 + (lane & 15)) * 192 + ((lane >> 4) % 3) * 64) * 4, std::integral_constant<int, 1>{});
#pragma unroll 1
      for (int spin = 0; spin < SPIN_MAX; ++spin) {
        u32x4 v[3][4];
        xs = __builtin_amdgcn_raw_buffer_load_b64(rsh, (int)((XS - hand) + lane * 2) * 4, 0, 16);
#pragma unroll
        for (int p = 2; p >= 0; --p)
#pragma unroll
          for (int i = 0; i < 4; ++i) v[p][i] = __builtin_amdgcn_raw_buffer_load_b128(p ? rsh : rsq, src + p * 256, i * (4 * 192 * 4), 16);
        auto part = [&](int p) __attribute__((always_inline)) {
          float z[4];
#pragma unroll
          for (int e = 0; e < 4; ++e) {
            z[e] = fadd(fadd(__uint_as_float(v[p][0][e]), __uint_as_float(v[p][1][e])), fadd(__uint_as_float(v[p][2][e]), __uint_as_float(v[p][3][e])));
          }
          val[p] = fold16(f32x2{z[0], z[1]}, f32x2{z[2], z[3]});
        };
        part(2); part(1);
        val[0] = 0.f;
        if (fast) part(0);
        const float chk = fadd(fadd(fadd(val[0], val[1]), fadd(val[2], __uint_as_float(xs[0]))), __uint_as_float(xs[1]));
        if (none(chk != chk)) break;
        unsigned mx = umax(xs[0], xs[1]);
#pragma unroll
        for (int i = 0; i < 12; ++i) mx = umax(umax(umax(mx, v[i >> 2][i & 3][0]), v[i >> 2][i & 3][1]), umax(v[i >> 2][i & 3][2], v[i >> 2][i & 3][3]));
        if (none(mx == SENT)) break;
        __builtin_amdgcn_s_sleep(PSLP);
        asm volatile("" ::: "memory");
      }
      const auto sw = __builtin_amdgcn_permlane32_swap(xs[0], xs[1], false, false);
      float sm = __uint_as_float(sw[0]) + __uint_as_float(sw[1]);
      sm = dpp_add<0x142, 0xa>(dpp_add<0x118, 0xf>(dpp_add<0x114, 0xf>(dpp_add<0x112, 0xf>(dpp_add<0x111, 0xf>(sm)))));
      const float dm = __int_as_float(__builtin_amdgcn_readlane(__float_as_int(sm), 31)) * (0.25f / 1024.0f);
      const float var = fmaxf(__int_as_float(__builtin_amdgcn_readlane(__float_as_int(sm), 63)) * (0.25f / 1024.0f) - dm * dm, 0.f);
      const float mb = rbf(rmb + dm);
      const float rin = frcp(rbf(__builtin_amdgcn_sqrtf(rbf(rbf(var) + 1e-5f))));
      const unsigned kv = pack2((val[1] - mb * cs[1]) * rin, (val[2] - mb * cs[2]) * rin);
      *LDS_AT(u16, lbase + TAB + 128 + kpos(col) * 2) = (u16)kv;
      *LDS_AT(u16, lbase + TAB + 256 + col * 2) = (u16)(kv >> 16);
      if (fast) *LDS_AT(u16, lbase + TAB + kpos(col) * 2) = f2bf((val[0] - mb * cs[0]) * rin * QSCALE);
      signal(7u);
      STAMP(dk, 7);
      return;
    }
    if (!fast) return;
    const int head = tni * 2 + hw;
    const unsigned tb = lbase + TAB + hw * 64;
    {
      const __amdgpu_buffer_rsrc_t rs = __builtin_amdgcn_make_buffer_rsrc(KV, 0, 2 * S_ * D_ * 2, 0x00020000);
      const int src = (head * 64 + tmi * 4) * 2048 + lane * 16;
      auto ld = [&](int i, unsigned off) { return __builtin_bit_cast(s16x8, __builtin_amdgcn_raw_buffer_load_b128(rs, src + (i & 3) * 1024, (i >> 3) * 65536 + ((i >> 2) & 1) * 4096 + off, 16)); };
#pragma unroll 1
      for (int i = 0; i < SPIN_MAX; ++i)
        if (__builtin_amdgcn_readfirstlane(ld_ag(kvflag + (tmi % RK) * 32 + tmi + 8)) == tag) break;
      asm volatile("" ::: "memory");
      s16x8 f[16];
#pragma unroll
      for (int i = 0; i < 16; ++i) f[i] = ld(i, 0u);
      PIN();
      const bool on = ((lane ^ (lane >> 4)) & 1) == 0;
      const unsigned pl = lbase + RED + hw * 1024;
      unsigned pw = pl + (lane >> 4) * 64 + (lane & 15) * 2, pr = pl + (on ? (lane >> 5) * 16 : 512), qr = on ? tb + (lane >> 5) * 16 : pl + 512;
      const int gone = (((idx >> 7) & 7) == tmi) ? ((idx >> 10) * 2 + (kpos(idx & 31) >> 4)) * 64 + ((idx >> 5) & 3) * 16 + (kpos(idx & 31) & 15) : -1;
      asm volatile("" : "+v"(pw), "+v"(pr), "+v"(qr));
      *LDS_AT(u32x2, pl + 512 + lane * 8) = u32x2{0u, 0u};
      wait_all(7u);
      u32x4 q[2];
#pragma unroll
      for (int sx = 0; sx < 2; ++sx) {
        q[sx] = *LDS_AT(const u32x4, qr + sx * 32);
      }
      float sc[4];
#pragma unroll
      for (int i = 0; i < 8; ++i) {
        f32x4 d = MFMA16(__builtin_bit_cast(s16x8, q[0]), f[2 * i], (f32x4{0.f, 0.f, 0.f, 0.f}));
        d = MFMA16(__builtin_bit_cast(s16x8, q[1]), f[2 * i + 1], d);
        PIN();
        f[2 * i] = ld(2 * i, VOFF); f[2 * i + 1] = ld(2 * i + 1, VOFF);
        PIN();
#pragma unroll
        for (int r = 0; r < 2; ++r) sc[(i >> 2) * 2 + r] = (i & 3) == 0 || (lane >> 4) == (i & 3) ? d[r] : sc[(i >> 2) * 2 + r];
      }
      if (gone >= 0) {
#pragma unroll
        for (int r = 0; r < 4; ++r) sc[r] = r * 64 + lane == gone ? -INFINITY : sc[r];
      }
      const float mx = wave_fmax(fmaxf(fmaxf(sc[0], sc[1]), fmaxf(sc[2], sc[3])));
      const unsigned p0 = pack2(__builtin_amdgcn_exp2f(sc[0] - mx), __builtin_amdgcn_exp2f(sc[1] - mx)), p1 = pack2(__builtin_amdgcn_exp2f(sc[2] - mx), __builtin_amdgcn_exp2f(sc[3] - mx));
      *LDS_AT(u16, pw) = (u16)p0; *LDS_AT(u16, pw + 32) = (u16)(p0 >> 16);
      *LDS_AT(u16, pw + 256) = (u16)p1; *LDS_AT(u16, pw + 288) = (u16)(p1 >> 16);
      asm volatile("" ::: "memory");
      const float ls = wave_sum((__uint_as_float(p0 << 16) + __uint_as_float(p0 & 0xffff0000u)) + (__uint_as_float(p1 << 16) + __uint_as_float(p1 & 0xffff0000u)));
      f32x4 o = {0.f, 0.f, 0.f, 0.f};
#pragma unroll
      for (int i = 0; i < 16; ++i) o = MFMA16(*LDS_AT(const s16x8, pr + i * 32), f[i], o);
      int pv = __float_as_int((lane & 16) ? o[1] : o[0]);
      asm volatile("s_nop 0\n\tv_writelane_b32 %0, %1, 32\n\tv_writelane_b32 %0, %2, 33" : "+v"(pv) : "s"(ls), "s"(mx));
      __builtin_amdgcn_raw_buffer_store_b32(f2word(__int_as_float(pv)), rsh, lane < 34 ? (int)((PA - hand) + (head * 8 + tmi) * 64 + lane) * 4 : HAND_N * 4, 0, 16);
    }
    if (wid == 8) STAMP(dk, 8);
    if (tmi != ((idx >> 7) & 7)) return;
    {
      const int src = (int)((PA - hand) + head * 512 + (lane < 34 ? lane : 0)) * 4;
      const unsigned ta = tb + (lane & 31) * 2;
      const float so = wave_sum(lane < 32 ? bf2f(*LDS_AT(const u16, ta)) * bf2f(*LDS_AT(const u16, ta + 128)) : 0.f);
      const float vo = lane < 32 ? bf2f(*LDS_AT(const u16, ta + 256)) : 1.0f;
      unsigned v[8];
      __builtin_amdgcn_s_sleep(PRE5);
#pragma unroll 1
      for (int spin = 0; spin < SPIN_MAX; ++spin) {
        unsigned mx = 0u;
#pragma unroll
        for (int i = 0; i < 8; ++i) v[i] = __builtin_amdgcn_raw_buffer_load_b32(rsh, src + i * 256, 0, 16);
#pragma unroll
        for (int i = 0; i < 8; ++i) mx = umax(mx, v[i]);
        if (none(mx == SENT)) break;
        __builtin_amdgcn_s_sleep(PSLP);
        asm volatile("" ::: "memory");
      }
      float m8[8], mx = so;
#pragma unroll
      for (int i = 0; i < 8; ++i) { m8[i] = __int_as_float(__builtin_amdgcn_readlane((int)v[i], 33)); mx = fmaxf(mx, m8[i]); }
      float acc = __builtin_amdgcn_exp2f(so - mx) * vo;
#pragma unroll
      for (int i = 0; i < 8; ++i) acc += __builtin_amdgcn_exp2f(m8[i] - mx) * __uint_as_float(v[i]);
      const float l = __int_as_float(__builtin_amdgcn_readlane(__float_as_int(acc), 32));
      const float ov = acc / l;
      const float o1 = __int_as_float(__builtin_amdgcn_update_dpp(0, __float_as_int(ov), 0x55, 0xf, 0xf, false));
      const float o2 = __int_as_float(__builtin_amdgcn_update_dpp(0, __float_as_int(ov), 0xaa, 0xf, 0xf, false));
      const float o3 = __int_as_float(__builtin_amdgcn_update_dpp(0, __float_as_int(ov), 0xff, 0xf, 0xf, false));
      const __amdgpu_buffer_rsrc_t rso = __builtin_amdgcn_make_buffer_rsrc(Of, 0, S_ * D_ * 2, 0x00020000);
      if (lane < 32 && (lane & 3) == 0)
        __builtin_amdgcn_raw_buffer_store_b64(u32x2{pack2(ov, o1), pack2(o2, o3)}, rso,
            (((idx >> 5) * 32 + head) * 1024 + (lane >> 4) * 512 + (((lane >> 2) & 1) * 32 + (idx & 31)) * 8 + ((lane >> 3) & 1) * 4) * 2, 0, 16);
      asm volatile("s_waitcnt vmcnt(0)" ::: "memory");
      if (lane < RO) st_ag(gran + GR_O + ((16 + (idx >> 7)) * RO + lane) * 64 + 16 + head, tag);
      STAMP(dk, 9);
    }
    }();
    }();
    __builtin_amdgcn_s_setprio(0);
    if (LAY2 || hw != UHW) asm volatile("s_waitcnt vmcnt(0)" ::: "memory");
    PIN();
    ARGS_HERE;
    u32x2 w[16];
    auto ld_wd = [&]() __attribute__((always_inline)) {
      const __amdgpu_buffer_rsrc_t rd_ = __builtin_amdgcn_make_buffer_rsrc(const_cast<u16*>(wd), 0, D_ * F_ * 2, 0x00020000);
#pragma unroll
      for (int f = 0; f < 16; ++f) w[f] = __builtin_amdgcn_raw_buffer_load_b64(rd_, t * 8 + (f & 1) * 2048, (fg * 16 + (f & ~1)) * (D_ * 2), 0);
      PIN();
    };
    float fnum = 0.f, fvar = 0.f;
    const int rrow = omi * 128 + (oni >> 3) * 64 + (hw & 1) * 32;
    if (LAY2) {
      ld_wd();
      if (!p1blk) {
        const int fr = rrow + (lane & 7) * 4 + (lane >> 4);
        const double s1 = ld_ag(stats + fr), s2 = ld_ag(stats + S_ + fr), sw = ld_ag(stats + 2 * S_ + fr);
        lds_wr(lbase + SPM + 132, __float_as_uint(wsum));
        float mean, var, mb, den;
        ln_from_sums(s1, s2, mean, var, mb, den);
        const float g1 = 1.0f + frcp(den);
        fnum = fr == idx ? __uint_as_float(0x7fc00000u) : fmaf(-mean, wsum, (float)sw) * g1;
        fvar = var * g1 * g1;
      }
      asm volatile("" : "+v"(fnum), "+v"(fvar));
      PIN();
    }
    if (hw == 3) {
      const int nfl = (LAY2 && (idx >> 7) == omi) ? 48 : 16;
      auto pfl = (__attribute__((address_space(1))) const unsigned*)(gran + GR_O + (((LAY2 ? 16 : 0) + omi) * RO + oni % RO) * 64 + (lane < nfl ? lane : 0));
      constexpr unsigned vs = 8u * D_ * 2u - 1024u;
      unsigned vw0 = (unsigned)((lane >> 3) * D_ + oni * 64 + ((lane & 7) ^ ((lane >> 3) & 6)) * 8) * 2u, vw1 = vw0 + vs, vw2 = vw0 + 2 * vs, vw3 = vw0 + 3 * vs;
      asm volatile("" : "+v"(pfl), "+v"(vw0), "+v"(vw1), "+v"(vw2), "+v"(vw3));
      auto wait_arr = [&](unsigned n) {
#pragma unroll 1
        for (int i = 0; i < 4 * SPIN_MAX; ++i) {
          if (__builtin_amdgcn_readfirstlane(*LDS_AT(const volatile unsigned, lbase + FLG + 16)) >= n) break;
          __builtin_amdgcn_s_sleep(1);
        }
        asm volatile("" ::: "memory");
        PIN();
      };
      wait_arr(12u);
      unsigned fl;
      asm volatile("global_load_dword %0, %1, off sc1" : "=v"(fl) : "v"(pfl) : "memory");
#pragma unroll 1
      for (int i = 0; i < 4 * SPIN_MAX; ++i) {
        if (__builtin_amdgcn_readfirstlane(*LDS_AT(const volatile unsigned, lbase + FLG + 24)) >= 8u) break;
        __builtin_amdgcn_s_sleep(1);
      }
      asm volatile("" ::: "memory");
      PIN();
#pragma unroll
      for (int j = 24; j < 32; ++j)
        glds16x4<false>(reinterpret_cast<const char*>(wo) + j * (32u * D_ * 2u), vw0, vw1, vw2, vw3, lbase + j * 4096);
      unsigned keep;
      asm volatile("s_mov_b32 %0, m0\n\ts_mov_b32 m0, %3\n\ts_nop 2\n\tglobal_load_lds_dword %1, %2 sc1\n\ts_mov_b32 m0, %0"
                   : "=&s"(keep) : "v"(0u), "s"(reinterpret_cast<const char*>(gran + GR_N + bid * 64 + 39)), "s"(lbase + FLG - 252) : "memory");
      asm volatile("s_waitcnt vmcnt(33)" : "+v"(fl) :: "memory");
      if (__builtin_amdgcn_ballot_w64(fl != tag)) {
        __builtin_amdgcn_s_sleep(CSL1);
#pragma unroll 1
        for (int spin = 0; spin < SPIN_MAX; ++spin) {
          if (!__builtin_amdgcn_ballot_w64(__hip_atomic_load(pfl, __ATOMIC_RELAXED, __HIP_MEMORY_SCOPE_AGENT) != tag)) break;
          __builtin_amdgcn_s_sleep(CSLP);
        }
      }
      asm volatile("" ::: "memory");
      PIN();
    }
    u32x4 pv[4];
    asm volatile("" : "=v"(pv[0]), "=v"(pv[1]), "=v"(pv[2]), "=v"(pv[3]));
    if (LAY2 && !p1blk && hw < 2) {
      const __amdgpu_buffer_rsrc_t rsp = __builtin_amdgcn_make_buffer_rsrc(hand + 2 * HAND_N + PP_O, 0, H_ * S_ * 4, 0x00020000);
      const int src = ((lane >> 3) * S_ + rrow + (lane & 7) * 4) * 4;
#pragma unroll 1
      for (int i = 0; i < 4 * SPIN_MAX; ++i) {
        if (__builtin_amdgcn_readfirstlane(*LDS_AT(const volatile unsigned, lbase + FLG + 24)) >= 8u) break;
        __builtin_amdgcn_s_sleep(2);
      }
#pragma unroll 1
      for (int spin = 0; spin < SPIN_MAX; ++spin) {
        asm volatile("" ::: "memory");
        unsigned mx = 0u;
#pragma unroll
        for (int i = 0; i < 4; ++i) pv[i] = __builtin_amdgcn_raw_buffer_load_b128(rsp, src, i * (8 * S_ * 4), 16);
#pragma unroll
        for (int i = 0; i < 4; ++i) mx = umax(umax(umax(mx, pv[i][0]), pv[i][1]), umax(pv[i][2], pv[i][3]));
        if (none(mx == SENT)) break;
        __builtin_amdgcn_s_sleep(PPSLP);
      }
    }
    asm volatile("s_waitcnt lgkmcnt(0)" ::: "memory");
    __builtin_amdgcn_s_barrier();
    if (!LAY2) {
      PIN();
      asm volatile(AGAIN "\ngate_up_code:\n\t"
                 "v_lshlrev_b32 v84, 16, v100\n\tv_and_b32 v85, 0xffff0000, v100\n\tv_lshlrev_b32 v86, 16, v101\n\tv_and_b32 v87, 0xffff0000, v101\n\t"
                 "v_subrev_f32 v84, s84, v84\n\tv_subrev_f32 v85, s84, v85\n\tv_subrev_f32 v86, s84, v86\n\tv_subrev_f32 v87, s84, v87\n\t"
                 "v_cvt_pk_bf16_f32 v85, v84, v85\n\tv_cvt_pk_bf16_f32 v87, v86, v87\n\ts_mov_b32 s88, 0x01000100\n\ts_mov_b32 s89, 0x03020302\n\t"
                 "v_lshlrev_b32 v84, 16, v85\n\tv_and_b32 v85, 0xffff0000, v85\n\tv_lshlrev_b32 v86, 16, v87\n\tv_and_b32 v87, 0xffff0000, v87\n\t"
                 "v_mul_f32 v88, s86, v84\n\tv_mul_f32 v89, s86, v85\n\tv_mul_f32 v90, s86, v86\n\tv_mul_f32 v91, s86, v87\n\tv_fma_f32 v84, -v88, s85, v84\n\t"
                 "v_fma_f32 v85, -v89, s85, v85\n\tv_fma_f32 v86, -v90, s85, v86\n\tv_fma_f32 v87, -v91, s85, v87\n\tv_fmac_f32 v88, s86, v84\n\t"
                 "v_fmac_f32 v89, s86, v85\n\tv_fmac_f32 v90, s86, v86\n\tv_fmac_f32 v91, s86, v87\n\tv_cvt_pk_bf16_f32 v88, v88, v89\n\t"
                 "v_cvt_pk_bf16_f32 v90, v90, v91\n\tv_perm_b32 v92, v88, v88, s88\n\tv_perm_b32 v93, v88, v88, s89\n\tv_perm_b32 v94, v90, v90, s88\n\t"
                 "v_perm_b32 v95, v90, v90, s89\n\ts_nop 0\n\tv_and_b32_dpp v96, v92, v102 quad_perm:[0,0,0,0] row_mask:0xf bank_mask:0xf\n\t"
                 "v_and_b32_dpp v97, v92, v103 quad_perm:[0,0,0,0] row_mask:0xf bank_mask:0xf\n\t"
                 "v_and_b32_dpp v100, v93, v102 quad_perm:[0,0,0,0] row_mask:0xf bank_mask:0xf\n\t"
                 "v_and_b32_dpp v101, v93, v103 quad_perm:[0,0,0,0] row_mask:0xf bank_mask:0xf\n\tv_mfma_f32_4x4x4_16b_bf16 v[84:87], v[96:97], v[104:105], 0\n\t"
                 "v_mfma_f32_4x4x4_16b_bf16 v[88:91], v[96:97], v[106:107], 0\n\tv_and_b32_dpp v96, v94, v102 quad_perm:[0,0,0,0] row_mask:0xf bank_mask:0xf\n\t"
                 "v_and_b32_dpp v97, v94, v103 quad_perm:[0,0,0,0] row_mask:0xf bank_mask:0xf\n\t"
                 "v_mfma_f32_4x4x4_16b_bf16 v[84:87], v[100:101], v[108:109], v[84:87]\n\tv_mfma_f32_4x4x4_16b_bf16 v[88:91], v[100:101], v[110:111], v[88:91]\n\t"
                 "v_and_b32_dpp v100, v95, v102 quad_perm:[0,0,0,0] row_mask:0xf bank_mask:0xf\n\t"
                 "v_and_b32_dpp v101, v95, v103 quad_perm:[0,0,0,0] row_mask:0xf bank_mask:0xf\n\tv_mfma_f32_4x4x4_16b_bf16 v[84:87], v[96:97], v[112:113], v[84:87]\n\t"
                 "v_mfma_f32_4x4x4_16b_bf16 v[88:91], v[96:97], v[114:115], v[88:91]\n\tv_and_b32_dpp v96, v92, v102 quad_perm:[1,1,1,1] row_mask:0xf bank_mask:0xf\n\t"
                 "v_and_b32_dpp v97, v92, v103 quad_perm:[1,1,1,1] row_mask:0xf bank_mask:0xf\n\t"
                 "v_mfma_f32_4x4x4_16b_bf16 v[84:87], v[100:101], v[116:117], v[84:87]\n\tv_mfma_f32_4x4x4_16b_bf16 v[88:91], v[100:101], v[118:119], v[88:91]\n\t"
                 "v_and_b32_dpp v100, v93, v102 quad_perm:[1,1,1,1] row_mask:0xf bank_mask:0xf\n\t"
                 "v_and_b32_dpp v101, v93, v103 quad_perm:[1,1,1,1] row_mask:0xf bank_mask:0xf\n\tv_mfma_f32_4x4x4_16b_bf16 v[84:87], v[96:97], v[120:121], v[84:87]\n\t"
                 "v_mfma_f32_4x4x4_16b_bf16 v[88:91], v[96:97], v[122:123], v[88:91]\n\tv_and_b32_dpp v96, v94, v102 quad_perm:[1,1,1,1] row_mask:0xf bank_mask:0xf\n\t"
                 "v_and_b32_dpp v97, v94, v103 quad_perm:[1,1,1,1] row_mask:0xf bank_mask:0xf\n\t"
                 "v_mfma_f32_4x4x4_16b_bf16 v[84:87], v[100:101], v[124:125], v[84:87]\n\tv_mfma_f32_4x4x4_16b_bf16 v[88:91], v[100:101], v[126:127], v[88:91]\n\t"
                 "v_and_b32_dpp v100, v95, v102 quad_perm:[1,1,1,1] row_mask:0xf bank_mask:0xf\n\t"
                 "v_and_b32_dpp v101, v95, v103 quad_perm:[1,1,1,1] row_mask:0xf bank_mask:0xf\n\tv_mfma_f32_4x4x4_16b_bf16 v[84:87], v[96:97], v[128:129], v[84:87]\n\t"
                 "v_mfma_f32_4x4x4_16b_bf16 v[88:91], v[96:97], v[130:131], v[88:91]\n\tv_and_b32_dpp v96, v92, v102 quad_perm:[2,2,2,2] row_mask:0xf bank_mask:0xf\n\t"
                 "v_and_b32_dpp v97, v92, v103 quad_perm:[2,2,2,2] row_mask:0xf bank_mask:0xf\n\t"
                 "v_mfma_f32_4x4x4_16b_bf16 v[84:87], v[100:101], v[132:133], v[84:87]\n\tv_mfma_f32_4x4x4_16b_bf16 v[88:91], v[100:101], v[134:135], v[88:91]\n\t"
                 "v_and_b32_dpp v100, v93, v102 quad_perm:[2,2,2,2] row_mask:0xf bank_mask:0xf\n\t"
                 "v_and_b32_dpp v101, v93, v103 quad_perm:[2,2,2,2] row_mask:0xf bank_mask:0xf\n\tv_mfma_f32_4x4x4_16b_bf16 v[84:87], v[96:97], v[136:137], v[84:87]\n\t"
                 "v_mfma_f32_4x4x4_16b_bf16 v[88:91], v[96:97], v[138:139], v[88:91]\n\tv_and_b32_dpp v96, v94, v102 quad_perm:[2,2,2,2] row_mask:0xf bank_mask:0xf\n\t"
                 "v_and_b32_dpp v97, v94, v103 quad_perm:[2,2,2,2] row_mask:0xf bank_mask:0xf\n\t"
                 "v_mfma_f32_4x4x4_16b_bf16 v[84:87], v[100:101], v[140:141], v[84:87]\n\tv_mfma_f32_4x4x4_16b_bf16 v[88:91], v[100:101], v[142:143], v[88:91]\n\t"
                 "v_and_b32_dpp v100, v95, v102 quad_perm:[2,2,2,2] row_mask:0xf bank_mask:0xf\n\t"
                 "v_and_b32_dpp v101, v95, v103 quad_perm:[2,2,2,2] row_mask:0xf bank_mask:0xf\n\tv_mfma_f32_4x4x4_16b_bf16 v[84:87], v[96:97], v[144:145], v[84:87]\n\t"
                 "v_mfma_f32_4x4x4_16b_bf16 v[88:91], v[96:97], v[146:147], v[88:91]\n\tv_and_b32_dpp v96, v92, v102 quad_perm:[3,3,3,3] row_mask:0xf bank_mask:0xf\n\t"
                 "v_and_b32_dpp v97, v92, v103 quad_perm:[3,3,3,3] row_mask:0xf bank_mask:0xf\n\t"
                 "v_mfma_f32_4x4x4_16b_bf16 v[84:87], v[100:101], v[148:149], v[84:87]\n\tv_mfma_f32_4x4x4_16b_bf16 v[88:91], v[100:101], v[150:151], v[88:91]\n\t"
                 "v_and_b32_dpp v100, v93, v102 quad_perm:[3,3,3,3] row_mask:0xf bank_mask:0xf\n\t"
                 "v_and_b32_dpp v101, v93, v103 quad_perm:[3,3,3,3] row_mask:0xf bank_mask:0xf\n\tv_mfma_f32_4x4x4_16b_bf16 v[84:87], v[96:97], v[152:153], v[84:87]\n\t"
                 "v_mfma_f32_4x4x4_16b_bf16 v[88:91], v[96:97], v[154:155], v[88:91]\n\tv_and_b32_dpp v96, v94, v102 quad_perm:[3,3,3,3] row_mask:0xf bank_mask:0xf\n\t"
                 "v_and_b32_dpp v97, v94, v103 quad_perm:[3,3,3,3] row_mask:0xf bank_mask:0xf\n\t"
                 "v_mfma_f32_4x4x4_16b_bf16 v[84:87], v[100:101], v[156:157], v[84:87]\n\tv_mfma_f32_4x4x4_16b_bf16 v[88:91], v[100:101], v[158:159], v[88:91]\n\t"
                 "v_and_b32_dpp v100, v95, v102 quad_perm:[3,3,3,3] row_mask:0xf bank_mask:0xf\n\t"
                 "v_and_b32_dpp v101, v95, v103 quad_perm:[3,3,3,3] row_mask:0xf bank_mask:0xf\n\tv_mfma_f32_4x4x4_16b_bf16 v[84:87], v[96:97], v[160:161], v[84:87]\n\t"
                 "v_mfma_f32_4x4x4_16b_bf16 v[88:91], v[96:97], v[162:163], v[88:91]\n\ts_nop 1\n\t"
                 "v_mfma_f32_4x4x4_16b_bf16 v[84:87], v[100:101], v[164:165], v[84:87]\n\tv_mfma_f32_4x4x4_16b_bf16 v[88:91], v[100:101], v[166:167], v[88:91]\n\t"
                 "s_nop 4\n\tv_permlane32_swap_b32 v84, v88\n\tv_permlane32_swap_b32 v85, v89\n\tv_permlane32_swap_b32 v86, v90\n\tv_permlane32_swap_b32 v87, v91\n\t"
                 "v_add_f32 v84, v84, v88\n\tv_add_f32 v85, v85, v89\n\tv_add_f32 v86, v86, v90\n\tv_add_f32 v87, v87, v91\n\ts_nop 0\n\t"
                 "v_permlane16_swap_b32 v84, v85\n\tv_permlane16_swap_b32 v86, v87\n\ts_nop 0\n\tv_add_f32 v84, v84, v85\n\tv_add_f32 v86, v86, v87\n\ts_nop 1\n\t"
                 "v_add_f32_dpp v84, v84, v84 row_ror:8 row_mask:0xf bank_mask:0xf\n\tv_add_f32_dpp v86, v86, v86 row_ror:8 row_mask:0xf bank_mask:0xf\n\ts_nop 0\n\t"
                 "v_add_f32_dpp v84, v84, v84 row_ror:4 row_mask:0xf bank_mask:0xf\n\tv_add_f32_dpp v86, v86, v86 row_ror:4 row_mask:0xf bank_mask:0xf\n\t"
                 "ds_write2_b32 v99, v84, v86 offset1:8\n\ts_setpc_b64 s[92:93]\n\t"
                   :: "{s[0:1]}"(ap + 1), "{s2}"(bid), "{v0}"(wid * 64), KEPT() : "s90", "s91", "scc", "memory");
      __builtin_unreachable();
    }
    {
    constexpr int dk = 8;
    constexpr int BEST = FLG + 40, MEET = FLG + 48;
    __builtin_amdgcn_s_setprio(HPRIO);
    if (hw == 0) STAMP(dk, 0);
    const unsigned tb1 = (unsigned)__builtin_amdgcn_s_memrealtime();
    unsigned* hand2 = hand + HAND_N;
    const __amdgpu_buffer_rsrc_t rsh = __builtin_amdgcn_make_buffer_rsrc(hand2, 0, (HAND_N + PS_N) * 4, 0x00020000);
    const __amdgpu_buffer_rsrc_t rsx = __builtin_amdgcn_make_buffer_rsrc(xout, 0, 3 * D_ * 2, 0x00020000);
    constexpr int OOR = (HAND_N + PS_N) * 4;
    if (!p1blk && hw < 2) {
      f32x2 a[2], c[2];
#pragma unroll
      for (int e = 0; e < 4; ++e) {
        a[e >> 1][e & 1] = (__uint_as_float(pv[0][e] << 16) + __uint_as_float(pv[1][e] << 16)) + (__uint_as_float(pv[2][e] << 16) + __uint_as_float(pv[3][e] << 16));
        c[e >> 1][e & 1] = (__uint_as_float(pv[0][e] & 0xffff0000u) + __uint_as_float(pv[1][e] & 0xffff0000u)) + (__uint_as_float(pv[2][e] & 0xffff0000u) + __uint_as_float(pv[3][e] & 0xffff0000u));
      }
      const float r = (fnum + fold8(a[0], a[1])) * __builtin_amdgcn_rsqf(fvar + fold8(c[0], c[1]));
      unsigned key = (__float_as_uint(r) ^ ((unsigned)(__float_as_int(r) >> 31) | 0x80000000u)) & 0xfffff800u;
      if (r != r) key = 0u;
      key = wave_umax(key | (unsigned)(2047 - (rrow + (lane & 7) * 4 + (lane >> 4))));
      __builtin_amdgcn_raw_buffer_store_b64(u32x2{key, tb1 & 0x7fffffffu}, rsh, lane < NREP ? (HAND_N + PK_O + lane * 128 + (rrow >> 5) * 2) * 4 : OOR, 0, 16);
      if (hw == 1) STAMP(dk, 2);
    }
    gu_setup();
    bool got = true;
    unsigned fra = lbase;
    int fro = S_ * D_ * 2;
    auto get_fc = [&]() __attribute__((always_inline)) {
      const u32x2 fw = __builtin_amdgcn_raw_buffer_load_b64(rsh, (HAND_N + PK_O + (bid % NREP) * 128 + lane * 2) * 4, 0, 16);
      got = none(umax(fw[0], fw[1]) == SENT);
      if (got) {
        const unsigned c1 = wave_umax(fw[0]), c2 = wave_umax(fw[0] == c1 ? 0u : fw[0]);
#define BADFC 0
        const unsigned r1 = (2047u - (c1 & 0x7ffu)) ^ BADFC, r2 = 2047u - (c2 & 0x7ffu);
        const unsigned r = (lane & 8) ? r2 : r1;
        fra = lbase + QOFF + (r & 127u) * 128 + (((r ^ lane) & 7u) << 4);
        fro = (r >> 7) == (unsigned)omi && lane < 16 ? ((lane >> 3) * D_ + oni * 64 + (lane & 7) * 8) * 2 : S_ * D_ * 2;
        unsigned dt = (unsigned)((int)((fw[1] - tb1) << 1) >> 1) + 512u;
        dt = dt < 1024u ? dt : 512u;
        *LDS_AT(volatile u32x2, lbase + SPM + 144) = u32x2{r1 | (r2 << 16), (unsigned)__builtin_amdgcn_readlane(dt, r1 >> 5) | (wave_umax(dt) << 16)};
      }
    };
    if (hw == 0) {
      __builtin_amdgcn_s_sleep(FCPRE);
#pragma unroll 1
      for (int i = 0; i < SPIN_MAX; ++i) {
        if (__builtin_amdgcn_readfirstlane(*LDS_AT(const volatile unsigned, lbase + FLG + 28)) - best >= 4u) break;
        __builtin_amdgcn_s_sleep(FCSLP);
      }
      asm volatile("" ::: "memory");
      get_fc();
    } else __builtin_amdgcn_s_sleep(TPRE - 10);
    PIN();
    tile_at(2u);
    const unsigned tb2 = (unsigned)__builtin_amdgcn_s_memrealtime() - 512u;
    LOGDEF;
    LOG();
#pragma unroll 1
    for (int spin = 0; spin < SPIN_MAX && !got; ++spin) { get_fc(); asm volatile("" ::: "memory"); }
    if (hw == 0) __builtin_amdgcn_raw_buffer_store_b128(lds_rd4(fra), rsx, fro, 0, XAUX);
    PIN();
    u32x2 fm = {SENT, 0u};
#pragma unroll 1
    for (int i = 0; i < 4 * SPIN_MAX && fm[0] == SENT; ++i) {
      const u32x2 q = *LDS_AT(const volatile u32x2, lbase + SPM + 144);
      fm = u32x2{(unsigned)__builtin_amdgcn_readfirstlane(q[0]), (unsigned)__builtin_amdgcn_readfirstlane(q[1])};
    }
    const int fr1 = fm[0] & 0x7ffu, fr2 = (fm[0] >> 16) & 0x7ffu;
    const unsigned t1 = tb2 + (fm[1] & 1023u), tmx = tb2 + ((fm[1] >> 16) & 1023u);
    auto until = [&](unsigned te) { while ((int)((unsigned)__builtin_amdgcn_s_memrealtime() - te) < 0) __builtin_amdgcn_s_sleep(1); asm volatile("" ::: "memory"); };
    lds_wr(lbase + SPM + 136, tmx);
    PIN();
    if (hw == 0) STAMP(dk, 3);
    const int xoff = (hw * 256 + (lane & 3) * 64 + (lane >> 2) * 4) * 2;
    const int oc = (omi * 2 + (oni >> 3)) * 32, col = oc + (lane & 7) * 4 + (lane >> 4);
    const int xoo = !p1blk && hw == 3 ? (col & ~1) * 2 : S_ * D_ * 2;
    auto get_row = [&](int r, int sl, u32x2& xr, unsigned& xo) __attribute__((always_inline)) {
      if ((r >> 7) == omi) {
#pragma unroll 1
        for (int i = 0; i < SPIN_MAX; ++i) {
          if (__builtin_amdgcn_readfirstlane(*LDS_AT(const volatile unsigned, lbase + FLG + 24)) >= 16u) break;
          __builtin_amdgcn_s_sleep(1);
        }
        asm volatile("" ::: "memory");
      }
#pragma unroll 1
      for (int spin = 0; spin < SPIN_MAX; ++spin) {
        LOG();
        xr = __builtin_amdgcn_raw_buffer_load_b64(rsx, xoff, sl * (D_ * 2), 16);
        xo = __builtin_amdgcn_raw_buffer_load_b32(rsx, xoo, sl * (D_ * 2), 16);
        LOG();
        if (none(umax(umax(xr[0], xr[1]), xo) == SENT)) break;
        __builtin_amdgcn_s_sleep(XRSLP);
        asm volatile("" ::: "memory");
      }
      PIN();
    };
    auto row_md = [&](const u32x2& xr, int k) __attribute__((always_inline)) {
      auto dot = [&](unsigned x, unsigned y, float z) { return __builtin_amdgcn_fdot2_f32_bf16(__builtin_bit_cast(bf16x2, x), __builtin_bit_cast(bf16x2, y), z, false); };
      const float p1 = dot(xr[1], 0x3f803f80u, dot(xr[0], 0x3f803f80u, 0.f)), p2 = dot(xr[1], xr[1], dot(xr[0], xr[0], 0.f));
      const auto q = __builtin_amdgcn_permlane32_swap(__float_as_uint(p1), __float_as_uint(p2), false, false);
      const float qs = __uint_as_float(q[0]) + __uint_as_float(q[1]);
      const auto q2 = __builtin_amdgcn_permlane16_swap(__float_as_uint(qs), __float_as_uint(qs), false, false);
      float sv = __uint_as_float(q2[0]) + __uint_as_float(q2[1]);
      sv = dpp_add<0x121, 0xf>(dpp_add<0x122, 0xf>(dpp_add<0x124, 0xf>(dpp_add<0x128, 0xf>(sv))));
      lds_wr(lbase + SPM + k * 64 + (lane >> 4) * 16 + hw * 4, f2word(sv));
      u32x4 a, c;
#pragma unroll 1
      for (int i = 0; i < SPIN_MAX; ++i) {
        a = lds_rd4(lbase + SPM + k * 64); c = lds_rd4(lbase + SPM + k * 64 + 32);
        if (__builtin_amdgcn_readfirstlane(umax(umax(a[0], a[1]), umax(a[2], a[3]))) != SENT) break;
      }
      auto sum4 = [&](const u32x4& v) { return (__uint_as_float(v[0]) + __uint_as_float(v[1])) + (__uint_as_float(v[2]) + __uint_as_float(v[3])); };
      const float mean = sum4(a) * (1.0f / 1024.0f), msq = sum4(c) * (1.0f / 1024.0f);
      const float den = rbf(__builtin_amdgcn_sqrtf(rbf(rbf(fmaxf(fmaf(-mean, mean, msq), 0.f)) + 1e-5f)));
      return (unsigned)__builtin_amdgcn_readfirstlane((pack2(mean, mean) & 0xffffu) | (__float_as_uint(den) & 0xffff0000u));
    };
    auto ffn = [&](const u32x2& xr, unsigned md, unsigned n, const __amdgpu_buffer_rsrc_t rs, int yo, auto&& off) __attribute__((always_inline)) {
      gate_up(xr, md);
      if (hw == 0 && n == 2u) STAMP(dk, 8);
      asm volatile("" ::: "memory");
      PIN();
      lds_wr(lbase + MEET + hw * 4, n + off());
      u32x4 mt;
#pragma unroll 1
      for (int i = 0; i < SPIN_MAX; ++i) {
        mt = lds_rd4(lbase + MEET);
        if (__builtin_amdgcn_readfirstlane(min4(mt)) >= n) break;
      }
      asm volatile("" ::: "memory");
      PIN();
      if (hw == 0 && n == 2u) STAMP(dk, 9);
      if (__builtin_amdgcn_readfirstlane(mt[0]) != n + 1u)
        __builtin_amdgcn_raw_buffer_store_b128(down(act_diag(n == 4u ? 512 : 0), w), rs, (yo + fg * D_) * 4 + t * 16, 0, 16);
      dad += n == 2u ? 512 : -512;
      if (hw == 0 && n == 2u) STAMP(dk, 4);
    };
    auto y_sum4 = [&](const __amdgpu_buffer_rsrc_t rs, int yo, int k) __attribute__((always_inline)) {
      const int src = (yo + (hw * 56 + (lane >> 3)) * D_ + oc + (lane & 7) * 4) * 4;
      u32x4 v[7];
#pragma unroll
      for (int i = 0; i < 7; ++i) asm volatile("" : "=v"(v[i]));
      unsigned todo = 0x7fu;
#pragma unroll 1
      for (int spin = 0; spin < SPIN_MAX; ++spin) {
        LOG();
#pragma unroll
        for (int i = 0; i < 7; ++i) if ((todo >> i) & 1u) v[i] = __builtin_amdgcn_raw_buffer_load_b128(rs, src, i * (8 * D_ * 4), 16);
#pragma unroll
        for (int i = 0; i < 7; ++i) if (none(umax(umax(v[i][0], v[i][1]), umax(v[i][2], v[i][3])) == SENT)) todo &= ~(1u << i);
        if (!todo) break;
        __builtin_amdgcn_s_sleep(YSLP);
        asm volatile("" ::: "memory");
      }
      f32x2 z0 = {0.f, 0.f}, z1 = {0.f, 0.f};
#pragma unroll
      for (int i = 0; i < 7; ++i) {
        z0 += f32x2{__uint_as_float(v[i][0]), __uint_as_float(v[i][1])};
        z1 += f32x2{__uint_as_float(v[i][2]), __uint_as_float(v[i][3])};
      }
      const unsigned ya = lbase + SPM + 256 + k * 512 + ((lane & 7) * 4 + (lane >> 4)) * 16;
      lds_wr((lane & 8) ? lbase + DUM + lane * 4 : ya + hw * 4, f2word(fold8(z0, z1)));
      u32x4 q = {0u, 0u, 0u, 0u};
      if (hw == 3) {
#pragma unroll 1
        for (int i = 0; i < 4 * SPIN_MAX; ++i) {
          q = lds_rd4(ya);
          if (none(umax(umax(q[0], q[1]), umax(q[2], q[3])) == SENT)) break;
        }
      }
      return (__uint_as_float(q[0]) + __uint_as_float(q[1])) + (__uint_as_float(q[2]) + __uint_as_float(q[3]));
    };
    const __amdgpu_buffer_rsrc_t rs1 = __builtin_amdgcn_make_buffer_rsrc(hand, 0, HAND_N * 4, 0x00020000);
    u32x2 xr;
    unsigned xo, xo2;
    until(t1 + XDT);
    get_row(fr1, 0, xr, xo);
    if (hw == 0) STAMP(dk, 7);
#define FAILFC 0
    const unsigned md1 = row_md(xr, 0) ^ (FAILFC & 1);
    if (hw == 0) STAMP(dk, 10);
    LOGAT(20);
    u32x2 xr2;
    unsigned md2 = 0u;
    if (p1blk) {
      xr2 = __builtin_amdgcn_raw_buffer_load_b64(rsx, (fr2 >> 7) == omi ? S_ * D_ * 2 : xoff, D_ * 2, 16);
      PIN();
      ffn(xr, md1, 2u, rsh, GRAN_N, [&]() { return 0u; });
      __builtin_amdgcn_s_setprio(R2PRIO);
      LOG();
      if ((fr2 >> 7) == omi || !none(umax(xr2[0], xr2[1]) == SENT)) get_row(fr2, 1, xr2, xo2);
      md2 = row_md(xr2, 1) ^ (FAILFC & 2);
      ffn(xr2, md2, 4u, rs1, GRAN_N, [&]() {
        const u32x2 v = *LDS_AT(const volatile u32x2, lbase + BEST);
        return v[0] != 0u && (2047 - (int)(v[0] & 0x7ffu) != fr2 || v[1] != md2) ? 1u : 0u; });
    }
    if (hw == 0) STAMP(dk, 5);
    LOG();
    float y = 0.f;
    auto put_row = [&](int r, unsigned md, unsigned rb) __attribute__((always_inline)) {
      const float rden = __uint_as_float(md & 0xffff0000u), rs = rbf(frcp(1.0f + __expf(-__uint_as_float(rb << 16))));
      const float xa = __uint_as_float((col & 1) ? xo & 0xffff0000u : xo << 16);
      float ha = xa, hb = xa;
      ln_apply2(ha, hb, __uint_as_float(md << 16), rden, frcp(rden));
      const float o = xa + rbf(ha + rbf(rbf(y) * rs));
      __builtin_amdgcn_raw_buffer_store_b16((unsigned short)(pack2(o, o) & 0xffffu), __builtin_amdgcn_make_buffer_rsrc(out, 0, S_ * D_ * 2, 0x00020000),
                                            (lane & 8) ? S_ * D_ * 2 : (r * D_ + col) * 2, 0, OAUX);
    };
    if (!p1blk) {
      until(tmx + XD2);
      get_row(fr2, 1, xr2, xo2);
      md2 = row_md(xr2, 1) ^ (FAILFC & 2);
      until(t1 + XDT + YD1); until(tmx + YD2);
      if (hw == 3) STAMP(dk, 13);
      LOGAT(8);
      y = y_sum4(rsh, GRAN_N, 0);
      if (hw == 3) STAMP(dk, 14);
      LOGAT(24);
    }
    unsigned bmd = 0u;
    best = 0u;
#pragma unroll 1
    for (int spin = 0; spin < 4 * SPIN_MAX; ++spin) {
      const u32x2 v = *LDS_AT(const volatile u32x2, lbase + BEST);
      best = __builtin_amdgcn_readfirstlane(v[0]); bmd = __builtin_amdgcn_readfirstlane(v[1]);
      if (best) break;
      __builtin_amdgcn_s_sleep(1);
    }
    asm volatile("" ::: "memory");
    const int idx = 2047 - (int)(best & 0x7ffu);
    const unsigned rk = best >> 16, rb = (rk & 0x8000u) ? (rk & 0x7fffu) : (~rk & 0xffffu);
    if (hw == 0) STAMP(dk, 1);
    LOGAT(25);
    LOGOUT(hw);
    if (__builtin_expect(idx != fr1 || bmd != md1, 0)) {
      if (idx == fr2 && bmd == md2) {
        if (p1blk) return;
        y = y_sum4(rs1, GRAN_N, 1);
        xo = xo2;
      } else {
        get_row(idx, idx == fr1 ? 0 : (idx == fr2 ? 1 : 2), xr, xo);
        if (p1blk) ffn(xr, bmd, 6u, rsh, HAND_N + Y3_O, [&]() { return 0u; });
        else y = y_sum4(rsh, HAND_N + Y3_O, 2);
        if (p1blk || hw != 3) return;
#pragma unroll 1
        for (int spin = 0; spin < SPIN_MAX && idx != fr1 && idx != fr2; ++spin) {
          if (__builtin_amdgcn_readfirstlane(ld_ag(gran + GR_F + (oc >> 6) * 32)) == tag) break;
          asm volatile("" ::: "memory");
        }
      }
    }
    if (p1blk || hw != 3) return;
    put_row(idx, bmd, rb);
    }
    return;
  }
  if (wid >= 4) {
    const int lane = fresh_lane();
    const int lw = wid - 4;
    const long long lay_ = ap->lay;
    const u16* A = ap->a; const u16* W = ap->W; const u16* wc = ap->wc;
#define LPRIO 3
#define LP0 3
    __builtin_amdgcn_s_setprio(LP0);
    unsigned va[4], vw[6];
    const unsigned ul = lane;
    va[0] = (unsigned)(m0 + lw * 32) * (K * 2) + (ul >> 3) * (K * 2) + (((ul & 7u) ^ (ul >> 4)) << 4);
    va[1] = va[0] + (8 * K * 2 - 1024);
    va[2] = (va[0] ^ 64u) + 2 * (8 * K * 2 - 1024);
    va[3] = va[2] + (8 * K * 2 - 1024);
#pragma unroll
    for (int i = 0; i < 3; ++i) {
      const unsigned n = ul + 64u * i, k = __umul24(n, 171u) >> 12, cp = n - __umul24(k, 24u);
      vw[i] = __umul24(k, ldw * 2) + (cp >> 3) * 2048u + (((cp & 7u) ^ ((k & 2u) << 1)) << 4) + ((unsigned)(lw * 16 * ldw + n0) * 2u - i * 1024u);
      vw[i + 3] = (vw[i] ^ 16u) + 8 * ldw * 2;
    }
    PIN();
    asm volatile("" : "+v"(va[0]), "+v"(va[1]), "+v"(va[2]), "+v"(va[3]), "+v"(vw[0]), "+v"(vw[1]), "+v"(vw[2]), "+v"(vw[3]), "+v"(vw[4]), "+v"(vw[5]));
    const bool LAY2 = lay_ != 0;
    const int krot = tmi & 8;
    if (LAY2 && slow_cols(W, tni)) W = wc;
    auto stage_a = [&](int kt, int slot) {
      const char* pa = reinterpret_cast<const char*>(A) + (size_t)(kt ^ krot) * 128;
      asm volatile("s_mov_b32 m0, %5\n\ts_nop 0\n\tglobal_load_lds_dwordx4 %0, %4 sc1\n\tglobal_load_lds_dwordx4 %1, %4 offset:1024 sc1\n\t"
                   "global_load_lds_dwordx4 %2, %4 offset:2048 sc1\n\tglobal_load_lds_dwordx4 %3, %4 offset:3072 sc1"
                   :: "v"(va[0]), "v"(va[1]), "v"(va[2]), "v"(va[3]), "s"(pa), "s"(lbase + slot * STB + lw * 4096) : "m0", "memory");
    };
    auto stage_w = [&](int kt, int slot) {
      const char* pw = reinterpret_cast<const char*>(W) + (size_t)(kt ^ krot) * 128 * ldw;
      asm volatile("s_mov_b32 m0, %7\n\ts_nop 0\n\tglobal_load_lds_dwordx4 %0, %6 sc1\n\tglobal_load_lds_dwordx4 %1, %6 offset:1024 sc1\n\t"
                   "global_load_lds_dwordx4 %2, %6 offset:2048 sc1\n\ts_addk_i32 m0, 0xc00\n\ts_nop 0\n\tglobal_load_lds_dwordx4 %3, %6 sc1\n\t"
                   "global_load_lds_dwordx4 %4, %6 offset:1024 sc1\n\tglobal_load_lds_dwordx4 %5, %6 offset:2048 sc1"
                   :: "v"(vw[0]), "v"(vw[1]), "v"(vw[2]), "v"(vw[3]), "v"(vw[4]), "v"(vw[5]), "s"(pw), "s"(lbase + slot * STB + ABYTES + lw * 6144) : "m0", "scc", "memory");
    };
    if (LAY2) {
      __builtin_amdgcn_s_setprio(0);
      __builtin_amdgcn_s_sleep(TPRE - 10);
      tile_at(3u);
      __builtin_amdgcn_s_setprio(LPRIO);
#pragma unroll
      for (int st = 0; st < NST; ++st) stage_w(st, st);
      PIN();
      __builtin_amdgcn_s_barrier();
      PIN();
#pragma unroll
      for (int st = 0; st < NST; ++st) stage_a(st, st);
      asm volatile("s_waitcnt vmcnt(8)" ::: "memory");
    } else {
      __builtin_amdgcn_s_setprio(LPRIO);
      stage_a(0, 0); stage_w(0, 0);
      if (PRO > 1) { stage_a(1, 1); stage_w(1, 1); }
      if (PRO > 2) { stage_a(2, 2); stage_w(2, 2); }
      asm volatile("s_waitcnt vmcnt(%0)" :: "n"(10 * PRO - 10) : "memory");
    }
    int slot = 0;
    __builtin_amdgcn_s_barrier();
    if (!LAY2) {
      if (PRO < 2) { stage_a(1, 1); stage_w(1, 1); }
      if (PRO < 3) { stage_a(2, 2); stage_w(2, 2); }
    }
    for (int kt = 0; kt + 1 < NKT; ++kt) {
      if (LAY2 && kt == 0) asm volatile("s_waitcnt vmcnt(4)" ::: "memory");
      else if (kt + 2 >= NKT) asm volatile("s_waitcnt vmcnt(0)" ::: "memory");
      else asm volatile("s_waitcnt vmcnt(10)" ::: "memory");
      __builtin_amdgcn_s_barrier();
      if (kt + NST < NKT) { stage_a(kt + NST, slot); stage_w(kt + NST, slot); }
      slot = slot == NST - 1 ? 0 : slot + 1;
    }
    __builtin_amdgcn_s_setprio(0);
    __builtin_amdgcn_s_barrier();
  } else {
  LAYER_HERE;
  const int dk = LAY2 ? 5 : 1;
  const int lane = fresh_lane(), wm = wid & 1, wn = wid >> 1;
  if (wid == 0) STAMP(dk, 0);
  f32x4 acc[4][6];
#pragma unroll
  for (int i = 0; i < 4; ++i)
#pragma unroll
    for (int j = 0; j < 6; ++j) acc[i][j] = f32x4{0.f, 0.f, 0.f, 0.f};
  const int q16 = lane & 15, g4 = lane >> 4;
  const int e0 = 16 * (q16 >> 3) + (q16 & 7);
  s16x8 af[2][4];
  unsigned aad[2], wad[2];
  {
    const int row = wm * 64 + e0, swz = ((row >> 1) & 3) | (((row >> 4) & 1) << 2);
#pragma unroll
    for (int h = 0; h < 2; ++h) aad[h] = lbase + row * 128 + (((4 * h + g4) ^ swz) << 4);
    const int tq = (lane & 15) >> 2, tp = lane & 3, k = 8 * g4 + tq, ch = wn * 4 + 2 * (tp >> 1);
    wad[0] = lbase + ABYTES + k * 384 + ((ch ^ (4 * ((k >> 1) & 1)) ^ (g4 & 1)) << 4) + 8 * (tp & 1);
    wad[1] = wad[0] ^ 16;
  }
  auto lda = [&](unsigned a, int i) { return *LDS_AT(const s16x8, a + (i >> 1) * 4096 + (i & 1) * 1024); };
  auto ldw_ = [&](int h, int j) {
    const unsigned w = wad[j & 1] + h * 12288 + (j >> 1) * 128;
    s16x4 lo = __builtin_amdgcn_ds_read_tr16_b64_v4i16(LDS_AT(s16x4, w));
    s16x4 h4 = __builtin_amdgcn_ds_read_tr16_b64_v4i16(LDS_AT(s16x4, w + 1536));
    return (s16x8)__builtin_shufflevector(lo, h4, 0, 1, 2, 3, 4, 5, 6, 7);
  };
  ARGS_HERE;
  unsigned lto = lbase + wid * 4096;
  char* kvb = reinterpret_cast<char*>(KV) + ((tni * 2 + wn) * 64 + tmi * 4 + wm * 2) * 2048 - lto;
  {
    const unsigned ato = lbase + ALPHA + wm * 256, dum = lbase + DUM + lane * 4;
    *LDS_AT(u32x4, lto + QOFF + lane * 16) = u32x4{lane >= 16 ? lbase + CRED : lbase + TABA + (wm * 64 + e0) * 16, lbase + TABW + (wn * 32 + e0) * 16,
                                                   ato + e0 * 4, lto + (lane >> 5) * 1024 + ((lane & 28) * 2 + (lane & 3)) * 16};
    *LDS_AT(u32x2, lto + QOFF + 2048 + lane * 16) = u32x2{aad[0], aad[1]};
    *LDS_AT(u32x4, lto + QOFF + 1024 + lane * 16) = u32x4{ato + g4 * 16 + (lane >> 5) * 32, lto + (lane >> 5) * 1024 + ((lane & 24) * 2 + (lane & 7)) * 16,
                                                          lane == 0 ? lbase + FLG + 32 : dum, lane == 0 ? lbase + FLG + 16 : dum};
  }
  asm volatile("" : "+s"(lto), "+s"(kvb) :: "memory");
#pragma unroll
  for (int i = 0; i < 4; ++i)
#pragma unroll
    for (int j = 0; j < 6; ++j) asm volatile("" : "+v"(acc[i][j]));
  asm volatile("" : "+v"(aad[0]), "+v"(aad[1]), "+v"(wad[0]), "+v"(wad[1]));
  if (LAY2) __builtin_amdgcn_s_barrier();
  __builtin_amdgcn_s_barrier();
  PIN();
  if (wid == 0) STAMP(dk, 1);
  s16x8 wq[4][2];
  wq[0][0] = ldw_(0, 0); af[0][0] = lda(aad[0], 0); wq[0][1] = ldw_(1, 0); af[1][0] = lda(aad[1], 0); wq[1][0] = ldw_(0, 1); wq[1][1] = ldw_(1, 1);
  af[0][1] = lda(aad[0], 1); af[1][1] = lda(aad[1], 1); af[0][2] = lda(aad[0], 2); af[1][2] = lda(aad[1], 2);
  PIN();
  s16x4 wlo;
  auto rw = [&](s16x8& d, int h, int j, int hi, int off = 0) __attribute__((always_inline)) {
    const s16x4 v = __builtin_amdgcn_ds_read_tr16_b64_v4i16(LDS_AT(s16x4, wad[j & 1] + h * 12288 + (j >> 1) * 128 + hi * 1536 + off));
    if (hi) d = (s16x8)__builtin_shufflevector(wlo, v, 0, 1, 2, 3, 4, 5, 6, 7);
    else wlo = v;
  };
  auto at = [&](s16x8& x, int r) __attribute__((always_inline)) {
    if (r == 0) asm volatile("" :: "{v[104:107]}"(x));
    if (r == 1) asm volatile("" :: "{v[108:111]}"(x));
    if (r == 2) asm volatile("" :: "{v[112:115]}"(x));
    if (r == 3) asm volatile("" :: "{v[116:119]}"(x));
    if (r == 4) asm volatile("" :: "{v[120:123]}"(x));
    if (r == 5) asm volatile("" :: "{v[124:127]}"(x));
    if (r == 6) asm volatile("" :: "{v[128:131]}"(x));
    if (r == 7) asm volatile("" :: "{v[132:135]}"(x));
    if (r == 8) asm volatile("" :: "{v[136:139]}"(x));
    if (r == 9) asm volatile("" :: "{v[140:143]}"(x));
    if (r == 10) asm volatile("" :: "{v[144:147]}"(x));
    if (r == 11) asm volatile("" :: "{v[148:151]}"(x));
    if (r == 12) asm volatile("" :: "{v[152:155]}"(x));
    if (r == 13) asm volatile("" :: "{v[156:159]}"(x));
    if (r == 14) asm volatile("" :: "{v[160:163]}"(x));
    if (r == 15) asm volatile("" :: "{v[164:167]}"(x));
  };
  auto mm = [&](int i, int j, int h, int sl, int hw_) __attribute__((always_inline)) {
    s16x8 &a = af[h][i], &w = wq[sl][hw_];
    at(a, h * 4 + i); at(w, 8 + sl * 2 + hw_);
    if (j >= 4) asm volatile("v_mfma_f32_16x16x32_bf16 %0, %1, %2, %0" : "+v"(acc[i][j]) : "v"(a), "v"(w));
    else asm volatile("v_mfma_f32_16x16x32_bf16 %0, %1, %2, %0" : "+v"(acc[i][j]) : "v"(w), "v"(a));
    PIN();
  };
  auto next = [&](int g, int J0, int s0, int s1, int off) __attribute__((always_inline)) {
    if (g == 2 || g == 3) rw(wq[s0][0], 0, J0, g & 1, off);
    if (g == 4 || g == 5) af[g & 1][0] = lda(aad[g & 1] + off, 0);
    if (g == 6 || g == 7) rw(wq[s0][1], 1, J0, g & 1, off);
    if (g >= 8 && g < 12) rw(wq[s1][(g - 8) >> 1], (g - 8) >> 1, J0 + 1, g & 1, off);
    if (g >= 12) af[g & 1][(g - 10) >> 1] = lda(aad[g & 1] + off, (g - 10) >> 1);
  };
  auto third = [&](int p, int J0, int delta) __attribute__((always_inline)) {
    auto mv = [&](unsigned& x) __attribute__((always_inline)) { asm volatile("v_add_u32 %0, %1, %0" : "+v"(x) : "s"(delta)); };
#pragma unroll
    for (int g = 0; g < 16; ++g) {
      const int h = g & 1, i = p == 1 ? (g >> 1) & 3 : g >> 2, t = p == 1 ? g >> 3 : (g >> 1) & 1, j = 2 * p + t, sl = p == 1 ? 2 - 2 * t : p + t;
      mm(i, j, h, sl, h);
      if (p == 0) {
        if (g < 2) af[g][3] = lda(aad[g], 3);
        else if (g < 6) rw(wq[2][(g - 2) >> 1], (g - 2) >> 1, 2, g & 1);
        else if (g < 10) rw(wq[3][(g - 6) >> 1], (g - 6) >> 1, 5, g & 1);
        else if (g < 12) mv(aad[g - 10]);
      }
      if (p == 1) {
        if (g < 4) rw(wq[0][g >> 1], g >> 1, 3, g & 1);
        if (g == 4) mv(wad[1]);
        if (g >= 8 && g < 12) rw(wq[2][(g - 8) >> 1], (g - 8) >> 1, 4, g & 1);
        if (g == 12) mv(wad[0]);
      }
      if (p == 2) {
        if (g == BARG) {
          asm volatile("" ::: "memory");
          PIN();
          __builtin_amdgcn_s_barrier();
          asm volatile("" ::: "memory");
        }
        next(g, J0, 0, 1, 0);
      }
      PIN();
      {
        if ((p == 2 && g == 15) || (p == 0 && g == 7)) __builtin_amdgcn_s_waitcnt(0xc67f);
        if (p == 0 && g == 2) __builtin_amdgcn_s_waitcnt(0xc57f);
        if ((p == 0 && g == 14) || (p == 1 && (g & 7) == 7)) __builtin_amdgcn_s_waitcnt(0xc07f);
        PIN();
      }
    }
  };
  third(0, 0, STB); third(1, 0, STB);
  {
    static_assert((NKT - NST - 1) % NST == 0, "");
#pragma unroll 1
    for (int kt = 0; kt < NKT - NST - 1; kt += NST) {
#pragma unroll
      for (int u = 0; u < NST; ++u) {
        const int delta = u == 1 ? -(NST - 1) * STB : STB;
        third(2, 0, 0); third(0, 0, delta); third(1, 0, delta);
      }
    }
  }
  static_assert(NKT % NST == 1, "slots of the last stages");
#define TT(i)
  auto bar = [&]() { PIN(); __builtin_amdgcn_s_barrier(); asm volatile("" ::: "memory"); PIN(); };
  TT(0);
  third(2, 2, 0);
  const __amdgpu_buffer_rsrc_t rsk = __builtin_amdgcn_make_buffer_rsrc(kvb, 0, 0x7fffffff, 0x00020000), rsv = __builtin_amdgcn_make_buffer_rsrc(kvb + VOFF, 0, 0x7fffffff, 0x00020000);
  unsigned pk;
  u32x2 t0, t1, t2, t3;
  auto park = [&](int i) { return *LDS_AT(const volatile u32x2, pk + (i >> 1) * 1024 + (i & 1) * 8); };
  float al[4];
  f32x4 av[4];
  u32x4 kvp[12];
  auto cacc = [&](int p, int x, int t) -> f32x4& { return p < 2 ? acc[x][2 * p + t] : acc[(x >> 1) * 2 + t][4 + (x & 1)]; };
  auto signal = [&](unsigned v) { __hip_atomic_fetch_add(LDS_AT(unsigned, t3[0]), v, __ATOMIC_RELAXED, __HIP_MEMORY_SCOPE_WORKGROUP); PIN(); };
  auto outop = [&](int p, int k) {
    if (k >= 0 && k < 16) {
      const int x = k >> 2, t = (k >> 1) & 1;
      f32x4& c = cacc(p, x, t);
#pragma unroll
      for (int o = 2 * (k & 1); o < 2 * (k & 1) + 2; ++o) {
        float r;
        asm volatile("v_mul_f32 %0, %1, %2" : "=v"(r) : "v"(c[o]), "v"(p < 2 ? al[x] : av[(x >> 1) * 2 + t][o]));
        c[o] = r;
      }
      PIN();
    }
    if (k >= 1 && k <= 16) {
      const int x = (k - 1) >> 2, t = ((k - 1) >> 1) & 1, o = (k - 1) & 1;
      kvp[p * 4 + x][2 * t + o] = pack2(cacc(p, x, t)[2 * o], cacc(p, x, t)[2 * o + 1]);
      PIN();
      if (t && o) {
        if (p) __builtin_amdgcn_raw_buffer_store_b128(kvp[p * 4 + x], p == 1 ? rsk : rsv, (p == 1 ? t1[1] : t2[1]) + (x >> 1) * 2048 + (x & 1) * (p == 1 ? 64 : 128), 0, 16);
        else *LDS_AT(u32x4, t2[1] + (x >> 1) * 2048 + (x & 1) * 128) = kvp[x];
        PIN();
      }
    }
  };
  static_for<0, 168>([&](auto nc) __attribute__((always_inline)) {
    constexpr int n = decltype(nc)::value, sec = n < 80 ? n >> 4 : (n < 104 ? 5 : 6 + ((n - 104) >> 4)), g = n < 104 ? n - 16 * sec : (n - 104) & 15;
    auto taba = [&](int i) { af[0][i] = *LDS_AT(const s16x8, t0[0] + (i >> 1) * 512 + (i & 1) * 128); };
    auto tabw = [&](int j) { wq[(2 + (j >> 1)) & 3][j & 1] = *LDS_AT(const s16x8, t0[1] + ((j >> 1) * 64 + (j & 1) * 8) * 16); };
    auto rwn = [&](int e, int sl, int j, int off) { if (e >= 0 && e < 4) rw(wq[sl][e >> 1], e >> 1, j, e & 1, off); };
    if (g == 0) TT(1 + sec);
    if (sec == 5) {
      const int i = (g / 12) * 2 + (g & 1), j = (g % 12) >> 1;
      mm(i, j, 0, (2 + (j >> 1)) & 3, j & 1);
    } else {
      constexpr int part[10] = {1, 2, 1, 2, 0, 0, 1, 2, 0, 0}, s0[10] = {0, 2, 0, 2, 0, 0, 1, 3, 1, 3};
      const int h = g & 1, i = sec == 7 ? (g >> 3) * 2 + ((g >> 1) & 1) : g >> 2, t = sec == 7 ? (g >> 2) & 1 : (g >> 1) & 1;
      mm(i, 2 * part[sec] + t, h, (s0[sec] + t) & 3, h);
    }
    if (sec == 0 || sec == 2) {
      if (g < 2) af[g][3] = lda(aad[g] + sec / 2 * STB, 3);
      rwn(g - 2, 2, 4, sec / 2 * STB); rwn(g - 6, 3, 5, sec / 2 * STB);
    }
    if (sec == 1) {
      if (g == 1) bar();
      next(g, 2, 0, 1, STB);
    }
    if ((sec == 3 && g == 12) || (sec == 6 && g == 13)) { pk = lto + QOFF + fresh_lane() * 16; asm volatile("" : "+v"(pk)); }
    if (sec == 3) {
      rwn(g, 0, 0, STB); rwn(g - 4, 1, 1, STB);
      if (g >= 8 && g < 12) asm volatile("v_subrev_u32 %0, %1, %0" : "+v"(g < 10 ? aad[g & 1] : wad[g & 1]) : "s"(STB));
      if (g == 13) t1 = park(1);
      if (g == 14) t0 = park(0);
    }
    if (sec == 4) {
      if (g == B15) bar();
      if (g == 4) taba(0);
      if (g == 5 || g == 6) tabw(g - 5);
      if (g == 7) taba(1);
      if (g == 8 || g == 9) tabw(g - 6);
      if (g == 10 || g == 11) af[1][g - 10] = lda(aad[1], g - 10);
      if (g == 12) taba(2);
      if (g == 15) taba(3);
    }
    if (sec == 5) {
      if (g < 2) tabw(4 + g);
      rwn(g - 2, 1, 2, 0);
      if (g == 12 || g == 13) af[0][g - 12] = lda(aad[0], g - 12);
      if (g == 22) { al[0] = *LDS_AT(const float, t1[0]); al[1] = *LDS_AT(const float, t1[0] + 32); }
      rwn(g - 16, 2, 3, 0);
      if (g == 20 || g == 21) af[1][g - 18] = lda(aad[1], g - 18);
    }
    if (sec == 6) {
      if (g < 2) af[0][2 + g] = lda(aad[0], 2 + g);
      rwn(g - 2, 3, 4, 0);
      if (g == 9) { al[2] = *LDS_AT(const float, t1[0] + 128); al[3] = *LDS_AT(const float, t1[0] + 160); }
      if (g == 14) t2 = park(2);
      if ((g & 3) >= 2) rwn((g - 10) / 4 * 2 + (g & 1) - (g < 10) * 8, 0, 5, 0);
    }
    if (sec == 7) {
      if (g == 2 || g == 3 || g == 10 || g == 11) av[(g >> 3) * 2 + (g & 1)] = *LDS_AT(const f32x4, t2[0] + ((g >> 3) * 32 + (g & 1) * 8) * 4);
      rwn(g < 2 ? g : (g < 4 ? -1 : g - 2), 1, 0, 0); rwn(g - 6, 2, 1, 0);
      if (g == 12) t3 = park(3);
      if (g == 13) { const u32x2 a = park(4); aad[0] = a[0]; aad[1] = a[1]; }
      if (g == 15) {
        asm volatile("" ::: "memory");
        __builtin_amdgcn_s_waitcnt(0xc07f);
        bar();
      }
    }
    if (sec == 8) next(g, 0, 3, 0, STB);
    if (sec == 9) {
      if (g < 2) af[g][3] = lda(aad[g] + STB, 3);
      if (g == 2) signal(0x10000u);
    }
    PIN();
    outop(1, n - 104 - KO);
    outop(2, n - 120 - KO);
    {
      constexpr int wg[8] = WGAPS;
#pragma unroll
      for (int e = 0; e < 8; ++e)
        if (n - 136 == wg[e]) {
          *LDS_AT(u32x4, (e < 4 ? t1[1] + (e & 1) * 64 : t2[1] + 2048 + (e & 1) * 128) + ((e >> 1) & 1) * ASB) = kvp[4 + e];
          PIN();
        }
    }
    if (n == 152 + 3 || n == 152 + 4) {
      al[2 * (n & 1)] *= QSCALE; al[2 * (n & 1) + 1] *= QSCALE;
      asm volatile("" : "+v"(al[2 * (n & 1)]), "+v"(al[2 * (n & 1) + 1]));
      if (n & 1) asm volatile("v_add_u32 %0, %1, %0" : "+v"(t2[1]) : "s"(QOFF));
      PIN();
    }
    outop(0, n - 152 - KO);
  });
  TT(13);
  if (wid == 0) STAMP(dk, 18);
#pragma unroll
  for (int k = 16 - KO; k <= 16; ++k) outop(0, k);
  __hip_atomic_fetch_add(LDS_AT(unsigned, t3[1]), 1u, __ATOMIC_RELAXED, __HIP_MEMORY_SCOPE_WORKGROUP);
  TT(14);
  asm volatile("s_waitcnt vmcnt(0)" ::: "memory");
  signal(1u);
  TT(15);
  if (wid == 0) STAMP(dk, 19);
  }
  PIN();
  {
  const int lane = fresh_lane(), tid = wid * 64 + lane;
  LAYER_HERE;
  const int dk = LAY2 ? 6 : 2;
  const int g = wid >> 2, hh = (wid >> 1) & 1, qg = wid & 1;
  const int head = tni * 2 + hh, qt = tmi * 4 + qg * 2;
  unsigned qaa = lbase + QOFF + (hh * 2 + qg) * 4096 + lane * 16 + (g ? 0 : 2048), qab = qaa ^ 2048;
  const unsigned wbase = lbase + (hh * 2 + g) * 4096 + lane * 16;
  auto sptr = [&](int sl) { unsigned a = wbase + sl * ASB; asm volatile("" : "+v"(a)); return LDS_AT(const s16x8, a); };
  auto rd = [&](__attribute__((address_space(3))) const s16x8* sp, int off) { return sp[off >> 4]; };
  auto sl0 = sptr(0);
  const unsigned arr_a = lane == 0 ? lbase + FLG + 16 : lbase + DUM + lane * 4;
  auto arrive = [&]() { __hip_atomic_fetch_add(LDS_AT(unsigned, arr_a), 1u, __ATOMIC_RELAXED, __HIP_MEMORY_SCOPE_WORKGROUP); };
  unsigned arr_w = lane == 0 ? lbase + FLG + 8 + g * 4 : lbase + DUM + lane * 4;
  unsigned arr_i = 1u << (8 * (wid & 3));
  auto arrive_w = [&]() { __hip_atomic_fetch_add(LDS_AT(unsigned, arr_w), arr_i, __ATOMIC_RELAXED, __HIP_MEMORY_SCOPE_WORKGROUP); };
  s16x8 sel;
#pragma unroll
  for (int e = 0; e < 8; ++e) sel[e] = ((lane & 7) == (e & 4)) ? (short)0x3F80 : (short)0;
  int sidx = 0xDDDDDDDD;
  f32x16 oa, ob, nn, sa, sb;
  f32x4 la = {0.f, 0.f, 0.f, 0.f}, lb = {0.f, 0.f, 0.f, 0.f};
#pragma unroll
  for (int e = 0; e < 16; ++e) { oa[e] = 0.f; ob[e] = 0.f; sa[e] = 0.f; sb[e] = 0.f; }
  asm volatile("" : "+v"(sidx), "+v"(sel), "+v"(oa), "+v"(ob), "+v"(la), "+v"(lb), "+v"(qaa), "+v"(qab), "+v"(arr_w), "+v"(arr_i));
  PIN();
  s16x8 qa0, qa1, qb0, qb1, k00, k01, v00, v01, v10, v11, k10, k11;
  u32x4 fw;
  auto first = [&]() {
    fw = lds_rd4(lbase + FLG + 16);
    asm volatile("" ::: "memory");
    k00 = rd(sl0, 0); qa0 = *LDS_AT(const s16x8, qaa); qb0 = *LDS_AT(const s16x8, qab);
    k01 = rd(sl0, 1024); qa1 = *LDS_AT(const s16x8, qaa + 1024); qb1 = *LDS_AT(const s16x8, qab + 1024);
    v00 = rd(sl0, 2048); v01 = rd(sl0, 3072);
    PIN();
  };
  first();
  if (__builtin_amdgcn_readfirstlane(fw[0]) < 4u) {
#pragma unroll 1
    for (int i = 0; i < SPIN_MAX; ++i) {
      if (__builtin_amdgcn_readfirstlane(*LDS_AT(const volatile unsigned, lbase + FLG + 16)) >= 4u) break;
      __builtin_amdgcn_s_sleep(ASLP);
    }
    asm volatile("" ::: "memory");
    first();
  }
  const unsigned tag = __builtin_amdgcn_readfirstlane(fw[1]);
  const int idx = LAY2 ? (2047 - (int)(__builtin_amdgcn_readfirstlane(fw[3]) & 0xffffu)) & 2047 : -64;
  float ma;
  const float zoff = __uint_as_float(__builtin_amdgcn_readfirstlane(LAY2 && g == ((idx >> 6) & 1) ? 0u : 0xff800000u));
  {
    sa = MFMA32(k00, qa0, sa); sb = MFMA32(k00, qb0, sb);
    sa = MFMA32(k01, qa1, sa); sb = MFMA32(k01, qb1, sb);
    float mb = sb[0];
    ma = sa[0];
#pragma unroll
    for (int e = 1; e < 16; ++e) { ma = fmaxf(ma, sa[e]); mb = fmaxf(mb, sb[e]); }
    ma = fmaxf(ma, mb);
    const auto sw = __builtin_amdgcn_permlane32_swap(__float_as_uint(ma), __float_as_uint(ma), false, false);
    ma = fmaxf(__uint_as_float(sw[0]), __uint_as_float(sw[1]));
    f32x2 n2 = {-ma, -ma};
    asm volatile("" : "+v"(n2));
#pragma unroll
    for (int e = 0; e < 16; ++e) nn[e] = n2[e & 1];
    sa = MFMA32(k00, qa0, nn); sa = MFMA32(k01, qa1, sa);
    la[0] = lb[0] = -__uint_as_float(__float_as_uint(__builtin_amdgcn_exp2f(zoff - ma)) & 0xffff0000u);
  }
#define SB() __builtin_amdgcn_sched_barrier(0)
#define EX(i) sc[i] = __builtin_amdgcn_exp2f(sc[i])
  auto unit = [&](f32x16& sc, f32x16& sn, const s16x8& kn0, const s16x8& kn1, const s16x8& qn0, const s16x8& qn1,
                  f32x16& op, f32x4& lp, const s16x8& vp,
                  f32x16& oc, f32x4& lc, const s16x8& vc, auto&& mid) {
    lp = SMF16(sel, PHI(sn), lp); SB();
    op = SMF32(vp, PHI(sn), op); SB();
    EX(0); EX(1); EX(2); EX(3); SB();
    sn = MFMA32(kn0, qn0, nn); SB();
    EX(4); EX(5); EX(6); EX(7); SB();
    sn = MFMA32(kn1, qn1, sn); SB();
    mid();
    EX(8); EX(9); EX(10); EX(11); SB();
    lc = SMF16(sel, PLO(sc), lc); SB();
    oc = SMF32(vc, PLO(sc), oc); SB();
    EX(12); EX(13); EX(14); EX(15); SB();
    asm volatile("" : "+v"(sc));
  };
  sl0 = sptr(1);
  unsigned f0 = 0u;
  auto check = [&](unsigned need) {
    if (__builtin_expect(__builtin_amdgcn_readfirstlane(f0) < need, 0)) {
#pragma unroll 1
      for (int i = 0; i < SPIN_MAX; ++i) {
        if (__builtin_amdgcn_readfirstlane(*LDS_AT(const volatile unsigned, lbase + FLG)) >= need) break;
        __builtin_amdgcn_s_sleep(1);
      }
    }
    asm volatile("" ::: "memory");
  };
#define PAT 0x22221110
#define SAW8(i) do { if (((PAT >> (28 - 4 * (i))) & 3) != ((PAT >> ((32 - 4 * (i)) & 31)) & 3)) { __builtin_amdgcn_s_setprio((PAT >> (28 - 4 * (i))) & 3); SB(); } } while (0)
#define SAW(i) do { if (odd) SAW8(4 + (i)); else SAW8(i); } while (0)
  auto three = [&](int st, bool odd) {
    SAW(0);
    unit(sa, sb, k00, k01, qb0, qb1, ob, lb, v11, oa, la, v00, [&]() { k10 = rd(sl0, 0); k11 = rd(sl0, 1024); SB(); });
    SAW(1);
    unit(sb, sa, k10, k11, qa0, qa1, oa, la, v01, ob, lb, v00, [&]() { v10 = rd(sl0, 2048); v11 = rd(sl0, 3072); SB(); });
    sl0 = sptr((2 * st + 2) & 7);
    SB();
    SAW(2);
    unit(sa, sb, k10, k11, qb0, qb1, ob, lb, v01, oa, la, v10, [&]() { k00 = rd(sl0, 0); k01 = rd(sl0, 1024); SB(); });
  };
  auto fourth = [&](int st, bool odd) {
    SAW(3);
    unit(sb, sa, k00, k01, qa0, qa1, oa, la, v11, ob, lb, v10, [&]() {
      v00 = rd(sl0, 2048); v01 = rd(sl0, 3072);
      f0 = *LDS_AT(const volatile unsigned, lbase + FLG);
      SB();
      arrive_w();
      SB(); });
    check(st >= 14 ? 32u : 2u * st + 5u);
    sl0 = sptr((2 * st + 3) & 7);
    SB();
  };
  SB();
  SAW8(0);
  {
    f32x16& sc = sa;
    sb = MFMA32(k00, qb0, nn); SB();
    k10 = rd(sl0, 0); k11 = rd(sl0, 1024); SB();
    sb = MFMA32(k01, qb1, sb); SB();
    EX(0); EX(1); EX(2); EX(3); EX(4); EX(5); EX(6); EX(7); EX(8); SB();
    oa = SMF32(v00, PLO(sa), oa); SB();
    EX(9); EX(10); EX(11); SB();
    la = SMF16(sel, PLO(sa), la); SB();
    EX(12); EX(13); EX(14); EX(15); SB();
    asm volatile("" : "+v"(sa));
  }
  SAW8(1);
  unit(sb, sa, k10, k11, qa0, qa1, oa, la, v01, ob, lb, v00, [&]() { v10 = rd(sl0, 2048); v11 = rd(sl0, 3072); SB(); });
  sl0 = sptr(2);
  SB();
  SAW8(2);
  unit(sa, sb, k10, k11, qb0, qb1, ob, lb, v01, oa, la, v10, [&]() {});
  {
    f32x16& sc = sb;
    SAW8(3);
    la = SMF16(sel, PHI(sa), la); SB();
    oa = SMF32(v11, PHI(sa), oa); SB();
    EX(0); EX(1); EX(2); EX(3); EX(4); EX(5); EX(6); EX(7); EX(8); asm volatile("" : "+v"(sc)); SB();
    lb = SMF16(sel, PLO(sb), lb); SB();
    ob = SMF32(v10, PLO(sb), ob); SB();
    f0 = *LDS_AT(const volatile unsigned, lbase + FLG);
    asm volatile("" ::: "memory");
    k00 = rd(sl0, 0); k01 = rd(sl0, 1024); SB();
    EX(9); EX(10); EX(11); EX(12); EX(13); EX(14); EX(15); asm volatile("" : "+v"(sc)); SB();
    if (__builtin_amdgcn_readfirstlane(f0) < 3u) { check(3u); k00 = rd(sl0, 0); k01 = rd(sl0, 1024); }
    SB();
    v00 = rd(sl0, 2048); v01 = rd(sl0, 3072); SB(); arrive_w(); SB();
    sa = MFMA32(k00, qa0, nn); sa = MFMA32(k01, qa1, sa); asm volatile("" : "+v"(sa)); SB();
  }
  sl0 = sptr(3);
  SB();
  SAW8(4);
  unit(sa, sb, k00, k01, qb0, qb1, ob, lb, v11, oa, la, v00, [&]() {
    f0 = *LDS_AT(const volatile unsigned, lbase + FLG);
    asm volatile("" ::: "memory");
    k10 = rd(sl0, 0); k11 = rd(sl0, 1024); SB(); });
  if (__builtin_amdgcn_readfirstlane(f0) < 4u) { check(4u); k10 = rd(sl0, 0); k11 = rd(sl0, 1024); }
  SB();
  SAW8(5);
  unit(sb, sa, k10, k11, qa0, qa1, oa, la, v01, ob, lb, v00, [&]() { v10 = rd(sl0, 2048); v11 = rd(sl0, 3072); f0 = *LDS_AT(const volatile unsigned, lbase + FLG); SB(); });
  check(5u);
  sl0 = sptr(4);
  SB();
  SAW8(6);
  unit(sa, sb, k10, k11, qb0, qb1, ob, lb, v01, oa, la, v10, [&]() { k00 = rd(sl0, 0); k01 = rd(sl0, 1024); SB(); });
#pragma unroll 1
  for (int st = 2; st < 16; st += 2) { fourth(st - 1, true); three(st, false); fourth(st, false); three(st + 1, true); }
  SAW8(7);
  unsigned vr, tk, tv;
  u32x4 f4;
  auto table = [&]() {
    f4 = lds_rd4(lbase + FLG);
    k00 = *LDS_AT(const volatile s16x8, tk); k01 = *LDS_AT(const volatile s16x8, tk + 32);
    vr = *LDS_AT(const volatile u16, tv);
  };
  unsigned xw, xm;
  u32x2 ml;
  u32x4 o2[4];
  auto take = [&]() {
    ml = *LDS_AT(const volatile u32x2, xm);
#pragma unroll
    for (int e = 0; e < 4; ++e) o2[e] = lds_rd4(xw + e * 1024);
  };
  float pzt;
  int dso;
  f32x16& sc = sb;
#define EXP() do { asm volatile("" : "+v"(sc)); SB(); } while (0)
  oa = SMF32(v11, PHI(sa), oa); SB();
  tk = lbase + TAB + 128 + hh * 64 + (lane >> 5) * 16; tv = lbase + TAB + 256 + hh * 64 + (lane & 31) * 2;
  table();
  SB();
  EX(0); EX(1); EXP();
  la = SMF16(sel, PHI(sa), la); SB();
  EX(2); EX(3); EX(4); EXP();
  xw = wbase + (qg ? QOFF : 6 * ASB); xm = lbase + XML + wid * 512 + lane * 8;
  pzt = __builtin_amdgcn_exp2f(nn[0] + (zoff - 8.f));
  asm volatile("" : "+v"(xw), "+v"(xm), "+v"(pzt), "+v"(f4));
  SB();
  {
    const unsigned ph = LAY2 ? 0x79797979u : 0x80808080u, pg = qg ? 0u : 0x80u << (16 * hh + 8);
    auto ready = [&]() {
      return (((__builtin_amdgcn_readfirstlane(f4[1]) + ph) & 0x80808080u) == 0x80808080u) & (((__builtin_amdgcn_readfirstlane(g ? f4[3] : f4[2]) + 0x71717171u) & pg) == pg);
    };
    if (__builtin_expect(!ready(), 0)) {
#pragma unroll 1
      for (int i = 0; i < SPIN_MAX; ++i) {
        __builtin_amdgcn_s_sleep(1);
        table();
        if (ready()) break;
      }
    }
    asm volatile("" ::: "memory");
  }
  SB();
  sa = MFMA32(k00, qb0, nn); asm volatile("" : "+v"(sa)); SB();
#pragma unroll
  for (int e = 0; e < 4; ++e) *LDS_AT(volatile f32x4, xw + e * 1024) = f32x4{oa[4 * e], oa[4 * e + 1], oa[4 * e + 2], oa[4 * e + 3]};
  *LDS_AT(volatile u32x2, xm) = u32x2{__float_as_uint(ma), f2word(pzt <= la[0] ? la[0] : 0.f)};
  SB();
  EX(5); EX(6); EX(7); EXP();
  sa = MFMA32(k01, qb1, sa); asm volatile("" : "+v"(sa), "+v"(vr)); SB();
  ARGS_HERE;
  EX(8); EX(9); EX(10); EXP();
  ob = SMF32(v10, PLO(sb), ob); SB();
  EX(11); EX(12); EX(13); EXP();
  lb = SMF16(sel, PLO(sb), lb); SB();
  const bool used = !LAY2 || (qt + g) * 32 + (lane & 31) != idx;
  dso = used ? (((qt + g) * 32 + head) * 1024 + lane * 8) * 2 : S_ * D_ * 2;
  xw += g ? -4096 : 4096; xm += g ? -2048 : 2048;
  asm volatile("" : "+v"(dso), "+v"(xw), "+v"(xm));
  SB();
  EX(14); EX(15); EXP();
  take();
  SB();
  lb = SMF16(sel, PHI(sb), lb); SB();
  ob = SMF32(v11, PHI(sb), ob); SB();
  __builtin_amdgcn_s_setprio(EPRIO);
  TE(0, 0); TE(1, 0); TE(2, 0); TE(3, 0); TE(7, 0);
  float lsb = lb[0];
  if (LAY2) {
    sa[0] = __builtin_amdgcn_exp2f(sa[0]);
    ob = SMF32((u32x4{lane < 32 ? vr : 0u, 0u, 0u, 0u}), PLO(sa), ob);
    lsb += __uint_as_float(__float_as_uint(sa[0]) & 0xffff0000u);
    lsb = pzt <= lsb ? lsb : 0.f;
  }
  const __amdgpu_buffer_rsrc_t rso = __builtin_amdgcn_make_buffer_rsrc(Of, 0, S_ * D_ * 2, 0x00020000);
  TE(0, 1);
  if (__builtin_amdgcn_ballot_w64(ml[1] == SENT) != 0ull) {
#pragma unroll 1
    for (int i = 0; i < SPIN_MAX; ++i) {
      __builtin_amdgcn_s_sleep(XSLP);
      if (!__builtin_amdgcn_ballot_w64(*LDS_AT(const volatile unsigned, xm + 4) == SENT)) break;
    }
    asm volatile("s_waitcnt lgkmcnt(0)" ::: "memory");
    take();
  }
  arrive();
  TE(0, 2);
  TE(0, 3); TE(1, 1);
  {
    const float m2 = __uint_as_float(ml[0]), l2 = __uint_as_float(ml[1]);
    const float mx = fmaxf(ma, m2);
    const float f1 = __builtin_amdgcn_exp2f(ma - mx), f2 = __builtin_amdgcn_exp2f(m2 - mx);
    const float inv = frcp(lsb * f1 + l2 * f2);
    const float c1 = f1 * inv, c2 = f2 * inv;
    f32x2 r[8];
#pragma unroll
    for (int e = 0; e < 8; ++e) {
      const unsigned x0 = o2[e >> 1][(e & 1) * 2], x1 = o2[e >> 1][(e & 1) * 2 + 1];
      r[e] = f32x2{__uint_as_float(x0) * c2, __uint_as_float(x1) * c2};
    }
#pragma unroll
    for (int e = 0; e < 8; ++e) r[e] = f32x2{ob[2 * e], ob[2 * e + 1]} * f32x2{c1, c1} + r[e];
    auto put = [&]() {
#pragma unroll
      for (int sx = 0; sx < 2; ++sx)
        __builtin_amdgcn_raw_buffer_store_b128(u32x4{pack2(r[4 * sx][0], r[4 * sx][1]), pack2(r[4 * sx + 1][0], r[4 * sx + 1][1]),
            pack2(r[4 * sx + 2][0], r[4 * sx + 2][1]), pack2(r[4 * sx + 3][0], r[4 * sx + 3][1])}, rso, dso + sx * 1024, 0, 16);
    };
    put();
    PIN();
    __builtin_amdgcn_s_setprio(0);
    PIN();
    f32x2 chk = {lsb - __builtin_amdgcn_fmed3f(lsb, 8.6736174e-19f, 1.2676506e30f), l2 - __builtin_amdgcn_fmed3f(l2, 8.6736174e-19f, 1.2676506e30f)};
#pragma unroll
    for (int e = 0; e < 8; ++e) chk = r[e] * f32x2{0.f, 0.f} + chk;
    if (__builtin_expect(__builtin_amdgcn_ballot_w64(used && !(chk[0] + chk[1] == 0.f)) != 0ull, 0)) {
      float l;
      attn_safe(KV, head * 64, qb0, qb1, ob, l, LAY2 ? head * 64 + (idx >> 5) : -1, idx & 31, lbase + TAB + 128 + hh * 64);
      const float inv2 = 1.0f / l;
#pragma unroll
      for (int e = 0; e < 8; ++e) r[e] = f32x2{ob[2 * e] * inv2, ob[2 * e + 1] * inv2};
      asm volatile("s_waitcnt vmcnt(0)" ::: "memory");
      put();
    }
    unsigned pp = 0u;
    if (LAY2) {
      const unsigned ua = lbase + UGL + hh * 128 + (lane >> 5) * 16;
      f32x2 du = {0.f, 0.f}, dn = {0.f, 0.f};
#pragma unroll
      for (int q = 0; q < 4; ++q) {
        const f32x4 uu = *LDS_AT(const f32x4, ua + q * 32), gg = *LDS_AT(const f32x4, ua + 256 + q * 32);
        du = r[2 * q] * f32x2{uu[0], uu[1]} + du; du = r[2 * q + 1] * f32x2{uu[2], uu[3]} + du;
        const f32x2 t0 = r[2 * q] * f32x2{gg[0], gg[1]}, t1 = r[2 * q + 1] * f32x2{gg[2], gg[3]};
        dn = t0 * t0 + dn; dn = t1 * t1 + dn;
      }
      const auto a = __builtin_amdgcn_permlane32_swap(__float_as_uint(du[0] + du[1]), __float_as_uint(du[0] + du[1]), false, false);
      const auto c = __builtin_amdgcn_permlane32_swap(__float_as_uint(dn[0] + dn[1]), __float_as_uint(dn[0] + dn[1]), false, false);
      pp = f2word(__uint_as_float(pack2(__uint_as_float(a[0]) + __uint_as_float(a[1]), __uint_as_float(c[0]) + __uint_as_float(c[1]))));
    }
    __builtin_amdgcn_raw_buffer_store_b32(pp, __builtin_amdgcn_make_buffer_rsrc(hand + 2 * HAND_N + PP_O, 0, LAY2 ? H_ * S_ * 4 : 0, 0x00020000),
                                          lane < 32 ? (head * S_ + (qt + g) * 32 + lane) * 4 : H_ * S_ * 4, 0, 16);
    PIN();
  }
  TE(1, 2);
  u32x4 xres[2];
  oproj_tail(LAY2, tid, lbase, lbase + FLG, omi, oni, idx, gran + GR_O + ((LAY2 ? 16 : 0) + tmi) * (64 * RO) + tni,
             gran + GR_X + omi * (128 * RX) + oni * 4, tag, Of, xout, A, w_r, stats_out, stats, hand, xres TEPASS);
  if (LAY2) {
    const int row = omi * 128 + (wid & 3) * 32 + (wid >> 2) * 16 + (lane >> 3), col = oni * 64 + (lane & 7) * 8;
    unsigned md[2], best = 0u;
    const __amdgpu_buffer_rsrc_t rsk = __builtin_amdgcn_make_buffer_rsrc(hand + HAND_N, 0, (HAND_N + PS_N) * 4, 0x00020000);
    f32x2 xf[2][4];
#pragma unroll
    for (int p = 0; p < 2; ++p)
#pragma unroll
      for (int e = 0; e < 4; ++e) {
        xf[p][e] = f32x2{__uint_as_float(xres[p][e] << 16), __uint_as_float(xres[p][e] & 0xffff0000u)};
        asm volatile("" : "+v"(xf[p][e]));
      }
    LOGDEF;
    LOG();
    const bool rtr = (oni & 7) == 7;
    {
      const int rl = (wid & 3) * 32 + (wid >> 2) * 16 + (lane & 15), rr = omi * 128 + rl;
      int src = (HAND_N + ((omi * 16 + (lane >> 4) * 4) * 128 + rl) * 4) * 4;
      int ko = rtr && (wid >> 2) == (oni >> 3) && lane < NREP ? lane * 1024 + (rr >> 4) * 8 : (HAND_N + PS_N) * 4;
      int bp = (lane >> 3) * 4;
      const float wsum = *LDS_AT(const volatile float, lbase + SPM + 132);
      asm volatile("" : "+v"(src), "+v"(ko), "+v"(bp));
      u32x4 v[4];
      __builtin_amdgcn_s_sleep(MPRE);
#pragma unroll 1
      for (int spin = 0; spin < SPIN_MAX; ++spin) {
        LOG();
#pragma unroll
        for (int i = 0; i < 4; ++i) v[i] = __builtin_amdgcn_raw_buffer_load_b128(rsk, src + (i & 1) * 2048, (i >> 1) * 4096, 16);
        unsigned mx = 0u;
#pragma unroll
        for (int i = 0; i < 4; ++i) mx = umax(umax(umax(mx, v[i][0]), v[i][1]), v[i][2]);
        if (none(mx == SENT)) break;
        __builtin_amdgcn_s_sleep(MSLP);
        asm volatile("" ::: "memory");
      }
      LOGAT(12);
      if (rtr) __builtin_amdgcn_s_setprio(3); else __builtin_amdgcn_s_setprio(LNPRIO);
      double sm[3];
#pragma unroll
      for (int c = 0; c < 3; ++c) {
        const double a = ((double)__uint_as_float(v[0][c]) + (double)__uint_as_float(v[1][c])) + ((double)__uint_as_float(v[2][c]) + (double)__uint_as_float(v[3][c]));
        u32x2 q = __builtin_bit_cast(u32x2, a);
        const auto lo = __builtin_amdgcn_permlane32_swap(q[0], q[0], false, false), hi = __builtin_amdgcn_permlane32_swap(q[1], q[1], false, false);
        q = __builtin_bit_cast(u32x2, __builtin_bit_cast(double, u32x2{lo[0], hi[0]}) + __builtin_bit_cast(double, u32x2{lo[1], hi[1]}));
        const auto l2 = __builtin_amdgcn_permlane16_swap(q[0], q[0], false, false), h2 = __builtin_amdgcn_permlane16_swap(q[1], q[1], false, false);
        sm[c] = __builtin_bit_cast(double, u32x2{l2[0], h2[0]}) + __builtin_bit_cast(double, u32x2{l2[1], h2[1]});
      }
      float mean, var, mb, den;
      ln_from_sums(sm[0], sm[1], mean, var, mb, den);
      const unsigned mdr = f2word(__uint_as_float((__float_as_uint(mb) >> 16) | (__float_as_uint(den) & 0xffff0000u)));
      md[0] = __builtin_amdgcn_ds_bpermute(bp, mdr); md[1] = __builtin_amdgcn_ds_bpermute(bp + 32, mdr);
      if (rtr) {
        const float r = (float)fma(-(double)mb, (double)wsum, sm[2]) * frcp(den);
        const unsigned rb = pack2(r, r);
        unsigned rkey = (rb ^ ((unsigned)((int)rb >> 31) | 0x80000000u)) & 0xffff0000u;
        if (r != r) rkey = 0u;
        const unsigned top = wave_umax(rkey | (unsigned)(2047 - rr));
        const unsigned mdt = __builtin_amdgcn_readlane(mdr, (2047 - (int)(top & 0xffffu)) & 15);
        __builtin_amdgcn_raw_buffer_store_b64(u32x2{top, mdt}, rsk, ko, 0, 16);
        PIN();
        __builtin_amdgcn_s_setprio(LNPRIO);
        __hip_atomic_fetch_add(LDS_AT(unsigned, lane == 0 ? lbase + SPM + 128 : lbase + DUM + lane * 4), 1u, __ATOMIC_RELAXED, __HIP_MEMORY_SCOPE_WORKGROUP);
        if (wid == 2 || wid == 5) STAMP(8, 6);
      }
      LOGAT(13);
    }
    auto keys = [&](const u32x4& v) __attribute__((always_inline)) {
      if (none(umax(umax(v[0], v[1]), umax(v[2], v[3])) == SENT)) {
        best = wave_umax(umax(v[0], v[2])) | 0x8000u;
        *LDS_AT(volatile u32x2, lbase + FLG + 40) = u32x2{best, (unsigned)__builtin_amdgcn_readlane(v[0] > v[2] ? v[1] : v[3], ((2047 - (int)(best & 0x7ffu)) >> 5) & 63)};
        STAMP(8, 11);
      }
    };
    auto ln_rows = [&]() __attribute__((always_inline)) {
#pragma unroll
      for (int p = 0; p < 2; ++p) {
        const float mb_ = __uint_as_float(md[p] << 16), rin = frcp(__uint_as_float(md[p] & 0xffff0000u));
#pragma unroll
        for (int e = 0; e < 4; ++e) {
          const f32x2 h = (xf[p][e] - f32x2{mb_, mb_}) * f32x2{rin, rin} + xf[p][e];
          xres[p][e] = pack2(h[0], h[1]);
        }
      }
      asm volatile("" : "+v"(xres[0]), "+v"(xres[1]));
    };
    if (wid == 0) STAMP(8, 12);
    ln_rows();
    LOGAT(14);
    const __amdgpu_buffer_rsrc_t rsu = __builtin_amdgcn_make_buffer_rsrc(out, 0, S_ * D_ * 2, 0x00020000);
    unsigned fr1 = SENT;
#pragma unroll 1
    for (int i = 0; i < 4 * SPIN_MAX && fr1 == SENT; ++i) fr1 = __builtin_amdgcn_readfirstlane(*LDS_AT(const volatile unsigned, lbase + SPM + 144));
    const int fr2 = (fr1 >> 16) & 0x7ffu;
    fr1 &= 0x7ffu;
    unsigned tmx = __builtin_amdgcn_readfirstlane(*LDS_AT(const volatile unsigned, lbase + SPM + 136)) + KDT;
    if (tmx - (unsigned)__builtin_amdgcn_s_memrealtime() > 1024u) tmx = 0u;
    auto due = [&]() { return tmx == 0u || (int)((unsigned)__builtin_amdgcn_s_memrealtime() - tmx) >= 0; };
    lk_set(15);
    const bool held = !none((row | 8) == ((int)fr1 | 8) || (row | 8) == (fr2 | 8));
#pragma unroll
    for (int p = 0; p < 2; ++p) __builtin_amdgcn_raw_buffer_store_b128(xres[p], rsu, row + 8 * p != (int)fr1 && row + 8 * p != fr2 ? ((row + 8 * p) * D_ + col) * 2 : S_ * D_ * 2, 0, OAUX);
    PIN();
    if (wid == 6) {
      if (rtr) {
#pragma unroll 1
        for (int i = 0; i < SPIN_MAX; ++i) {
          if (__builtin_amdgcn_readfirstlane(*LDS_AT(const volatile unsigned, lbase + SPM + 128)) == 7u) break;
          __builtin_amdgcn_s_sleep(1);
        }
        asm volatile("" ::: "memory");
      }
      while (!due()) __builtin_amdgcn_s_sleep(1);
    }
#pragma unroll 1
    for (int i = 0; i < 4 * SPIN_MAX && !best; ++i) {
      if (wid == 6) {
        LOG();
        keys(__builtin_amdgcn_raw_buffer_load_b128(rsk, (bid % NREP) * 1024 + lane * 16, 0, 16));
        if (!best) __builtin_amdgcn_s_sleep(KSLP);
      } else {
        best = __builtin_amdgcn_readfirstlane(*LDS_AT(const volatile unsigned, lbase + FLG + 40));
        if (!best) __builtin_amdgcn_s_sleep(1);
      }
      asm volatile("" ::: "memory");
    }
    const int idx2 = 2047 - (int)(best & 0x7ffu);
    if (held) {
#pragma unroll
      for (int p = 0; p < 2; ++p) {
        const int r = row + 8 * p;
        __builtin_amdgcn_raw_buffer_store_b128(xres[p], rsu, (r == (int)fr1 || r == fr2) && r != idx2 ? (r * D_ + col) * 2 : S_ * D_ * 2, 0, OAUX);
      }
    }
    if (__builtin_expect(idx2 != (int)fr1 && idx2 != fr2, 0) && !none((row | 8) == (idx2 | 8))) {
#pragma unroll
      for (int p = 0; p < 2; ++p)
        __builtin_amdgcn_raw_buffer_store_b128(u32x4{pack2(xf[p][0][0], xf[p][0][1]), pack2(xf[p][1][0], xf[p][1][1]), pack2(xf[p][2][0], xf[p][2][1]), pack2(xf[p][3][0], xf[p][3][1])},
            __builtin_amdgcn_make_buffer_rsrc(xout, 0, 3 * D_ * 2, 0x00020000), row + 8 * p == idx2 ? (2 * D_ + col) * 2 : S_ * D_ * 2, 0, 16);
      asm volatile("s_waitcnt vmcnt(0)" ::: "memory");
      if (lane == 0) st_ag(gran + GR_F + oni * 32, tag);
    }
  }
  }
}
struct Args2 { Args l[2]; };
extern "C" __global__ __launch_bounds__(768, 3) void layers_kernel(const Args2 args) {
  __shared__ __attribute__((aligned(128))) char smem[163840];
  const unsigned long long t_entry = 0;
  const int tid = threadIdx.x, bid = blockIdx.x;
  int wid = __builtin_amdgcn_readfirstlane(tid >> 6);
  asm volatile("" : "+s"(wid));
  const ArgP ap = (ArgP)__builtin_amdgcn_kernarg_segment_ptr();
  layer_body(wid, bid, (unsigned)(uintptr_t)smem, ap, t_entry);
  LAYER_HERE;
  if (!LAY2) {
    asm volatile(AGAIN :: "{s[0:1]}"(ap + 1), "{s2}"(bid), "{v0}"(wid * 64) : "s90", "s91", "scc", "memory");
    __builtin_unreachable();
  }
}

extern "C" __global__ void end_marker_kernel() {}

extern "C" void kernel_launch(void* const* d_in, const int* in_sizes, int n_in, void* d_out, int out_size,
                              void* d_ws, size_t ws_size, hipStream_t stream) {
  const u16* x0 = (const u16*)d_in[0];
  const u16* wqkv = (const u16*)d_in[1];
  const u16* wo = (const u16*)d_in[2];
  const u16* w_r = (const u16*)d_in[4];
  const u16* wg = (const u16*)d_in[5];
  const u16* wu = (const u16*)d_in[6];
  const u16* wd = (const u16*)d_in[7];
  u16* out = (u16*)d_out;

  char* ws = (char*)d_ws;
  const size_t MB4 = (size_t)S_ * D_ * 2;
  u16* kv = (u16*)(ws);
  u16* o = (u16*)(ws + 2 * MB4);
  u16* x1 = (u16*)(ws + 3 * MB4);
  unsigned* gran = (unsigned*)(ws + 4 * MB4);
  float* csf = (float*)(gran + GR_F + 16 * 32);
  double* stats = (double*)(csf + 8192);
  unsigned* hand = (unsigned*)(stats + 2 * STAT_N);
  static_assert((GR_F + 16 * 32) % 32 == 0 && (size_t)(GR_F + 16 * 32 + 8192 + 4 * STAT_N + 2 * HAND_N + PS_N) * 4 <= 6u << 20, "");
  u16* x3 = (u16*)(hand + 2 * HAND_N) - 3 * D_;
  u16* wc = (u16*)((((uintptr_t)ws + 4 * MB4 + (6u << 20) + 2047) & ~(uintptr_t)2047) + (((uintptr_t)wqkv + 128) & 2047));
  Args2 a;
  for (int l = 0; l < 2; ++l) {
    Args& r = a.l[l];
    r.lay = l; r.a = l ? x1 : x0; r.kv = kv; r.o = o; r.xo = l ? x3 : x1; r.so = l ? stats + STAT_N : stats;
    r.W = wqkv; r.wc = wc; r.wg = wg; r.wu = wu; r.wd = wd; r.wo = wo; r.w_r = w_r; r.gran = gran; r.csf = csf; r.stats = stats; r.hand = hand; r.out = out;
  }
  layers_kernel<<<256, 768, 0, stream>>>(a);
}
```

```cpp
#include <hip/hip_runtime.h>
#include <hip/hip_bf16.h>
#include <stdint.h>
#include <type_traits>
#include <utility>

typedef unsigned short u16;
typedef short s16x4 __attribute__((ext_vector_type(4)));
typedef short s16x8 __attribute__((ext_vector_type(8)));
typedef float f32x16 __attribute__((ext_vector_type(16)));
typedef float f32x4 __attribute__((ext_vector_type(4)));
typedef unsigned int u32x4 __attribute__((ext_vector_type(4)));
typedef unsigned int u32x2 __attribute__((ext_vector_type(2)));
typedef __bf16 bf16x8 __attribute__((ext_vector_type(8)));
typedef __bf16 bf16x16 __attribute__((ext_vector_type(16)));

__device__ __forceinline__ int fresh_lane() {
  int l;
  asm volatile("v_mbcnt_lo_u32_b32 %0, -1, 0\n\tv_mbcnt_hi_u32_b32 %0, -1, %0" : "=v"(l));
  return l;
}
#define STAMP(k, i)
#define STAMPV(k, i, v)
#define LOGDEF
#define LOG()
#define LOGAT(k)
#define lk_set(k)
#define LOGOUT(slot)
#define TE(grp, k)
#define TEARG
#define TEPASS
constexpr int S_ = 2048, D_ = 1024, H_ = 32, F_ = 3584;

__device__ __forceinline__ int kpos(int r) { return (r & ~12) | ((r & 4) << 1) | ((r & 8) >> 1); }
__device__ __forceinline__ float bf2f(u16 v) { return __uint_as_float(((unsigned)v) << 16); }
__device__ __forceinline__ u16 f2bf(float f) {
  __hip_bfloat16 b = __float2bfloat16(f);
  return *reinterpret_cast<u16*>(&b);
}
typedef float f32x2 __attribute__((ext_vector_type(2)));
typedef __bf16 bf16x2 __attribute__((ext_vector_type(2)));
__device__ __forceinline__ unsigned pack2(float lo, float hi) {
  f32x2 v = {lo, hi};
  return __builtin_bit_cast(unsigned, __builtin_convertvector(v, bf16x2));
}
__device__ __forceinline__ float rbf(float f) { return __uint_as_float(pack2(f, f) << 16); }
__device__ __forceinline__ void rbf2(float& a, float& b) {
  const unsigned p = pack2(a, b);
  a = __uint_as_float(p << 16); b = __uint_as_float(p & 0xffff0000u);
}

__device__ __forceinline__ float fadd(float a, float b) { float r = a + b; asm("" : "+v"(r)); return r; }
template <int CTRL, int RMASK>
__device__ __forceinline__ float dpp_add(float v) {
  const int t = __builtin_amdgcn_update_dpp(0, __float_as_int(v), CTRL, RMASK, 0xf, false);
  return v + __int_as_float(t);
}
__device__ __forceinline__ float wave_sum(float v) {
  v = dpp_add<0x111, 0xf>(v);
  v = dpp_add<0x112, 0xf>(v);
  v = dpp_add<0x114, 0xf>(v);
  v = dpp_add<0x118, 0xf>(v);
  v = dpp_add<0x142, 0xa>(v);
  v = dpp_add<0x143, 0xc>(v);
  return __int_as_float(__builtin_amdgcn_readlane(__float_as_int(v), 63));
}

template <int CTRL, int RMASK>
__device__ __forceinline__ unsigned dpp_umax(unsigned v) {
  const unsigned t = (unsigned)__builtin_amdgcn_update_dpp(0, (int)v, CTRL, RMASK, 0xf, false);
  return v > t ? v : t;
}
__device__ __forceinline__ unsigned wave_umax(unsigned v) {
  v = dpp_umax<0x111, 0xf>(v); v = dpp_umax<0x112, 0xf>(v); v = dpp_umax<0x114, 0xf>(v); v = dpp_umax<0x118, 0xf>(v);
  v = dpp_umax<0x142, 0xa>(v); v = dpp_umax<0x143, 0xc>(v);
  return (unsigned)__builtin_amdgcn_readlane((int)v, 63);
}

#define NREP 32
constexpr unsigned SENT = 0xFFFFFFFFu;
constexpr int GRAN_N = 256 * NREP;
constexpr int Y_N = F_ / 16 * D_;
constexpr int X2_N = D_ / 2;
constexpr int XS_N = 128;
constexpr int P4_N = 3 * D_ * 16;
constexpr int PA_N = H_ * 8 * 64;
constexpr int HAND_N = GRAN_N + Y_N + X2_N + XS_N + P4_N + PA_N;
constexpr int MD_O = 16 * 16 * 128 * 4;
constexpr int PK_O = MD_O;
constexpr int PP_O = PK_O + 128 * NREP;
constexpr int Y3_O = PP_O + H_ * S_;
constexpr int PS_N = Y3_O + Y_N;
constexpr int GR_ROW = 64, GR_COL = GR_ROW + 4 * S_, GR_KV = GR_COL + 16 * 16 * 192;
#define RO 16
#define RK 16
#define RX 1
constexpr int GR_O = GR_KV + 2 * 16 * 32 * RK, GR_X = GR_O + 2 * 16 * 64 * RO, GR_N = GR_X + 16 * 128 * RX;
constexpr int GR_F = GR_N + 256 * 64;
constexpr int STAT_N = 3 * S_;

__device__ __forceinline__ void st_ag(unsigned* p, unsigned v) { __hip_atomic_store(p, v, __ATOMIC_RELAXED, __HIP_MEMORY_SCOPE_AGENT); }
__device__ __forceinline__ unsigned ld_ag(const unsigned* p) { return __hip_atomic_load(p, __ATOMIC_RELAXED, __HIP_MEMORY_SCOPE_AGENT); }
__device__ __forceinline__ double ld_ag(const double* p) { return __hip_atomic_load(p, __ATOMIC_RELAXED, __HIP_MEMORY_SCOPE_AGENT); }
__device__ __forceinline__ unsigned f2word(float f) { const unsigned u = __float_as_uint(f); return u < 0xFFFFFFFEu ? u : 0xFFFFFFFEu; }
constexpr int SPIN_MAX = 1 << 18;
__device__ __forceinline__ unsigned umax(unsigned a, unsigned b) { return a > b ? a : b; }
__device__ __forceinline__ bool none(bool p) { return __builtin_amdgcn_ballot_w64(p) == 0ull; }

__device__ __forceinline__ void ln_from_sums(double s1, double s2, float& mean, float& var, float& mb, float& den) {
  const double md = s1 * (1.0 / 1024.0);
  const double vd = fma(-md, md, s2 * (1.0 / 1024.0));
  mean = (float)md; var = fmaxf((float)vd, 0.f);
  mb = rbf(mean);
  den = rbf(__builtin_amdgcn_sqrtf(rbf(rbf(var) + 1e-5f)));
}
__device__ __forceinline__ float frcp(float x) { return __builtin_amdgcn_rcpf(x); }
__device__ __forceinline__ void ln_apply2(float& a, float& b, float mb, float den, float rin) {
  a -= mb; b -= mb;
  rbf2(a, b);
  float qa = a * rin, qb = b * rin;
  qa = fmaf(fmaf(-qa, den, a), rin, qa);
  qb = fmaf(fmaf(-qb, den, b), rin, qb);
  rbf2(qa, qb);
  a = qa; b = qb;
}

#define CODE_ADDR(SYM, out64)                                                                                          \
  do {                                                                                                                 \
    unsigned lo_, hi_;                                                                                                 \
    asm volatile("s_getpc_b64 s[90:91]\n\ts_add_u32 s90, s90, " SYM "@rel32@lo+4\n\ts_addc_u32 s91, s91, " SYM          \
                 "@rel32@hi+12\n\ts_mov_b32 %0, s90\n\ts_mov_b32 %1, s91"                                             \
                 : "=s"(lo_), "=s"(hi_) :: "s90", "s91", "scc");                                                        \
    out64 = ((unsigned long long)hi_ << 32) | lo_;                                                                     \
  } while (0)

#define QNX 4
constexpr int QMX = 32 / QNX, QNG = 16 / QNX, QMG = 8 / QNG;

__device__ __forceinline__ unsigned touch(const void* p) { return *reinterpret_cast<const unsigned*>(p); }

__device__ __forceinline__ unsigned touch_code(unsigned long long lo, unsigned long long hi, int pc, int lane) {
  const unsigned long long p = lo + (unsigned)(pc * 8192 + lane * 128);
  return p + 4 <= hi ? *reinterpret_cast<__attribute__((address_space(1))) const unsigned*>(p) : 0u;
}

template <bool SC = false>
__device__ __forceinline__ void glds16s(const void* sbase, unsigned voff, unsigned lds_dst) {
  unsigned keep;
  if (SC)
    asm volatile("s_mov_b32 %0, m0\n\ts_mov_b32 m0, %3\n\ts_nop 2\n\tglobal_load_lds_dwordx4 %1, %2 sc1\n\ts_mov_b32 m0, %0"
                 : "=&s"(keep) : "v"(voff), "s"(sbase), "s"(lds_dst) : "memory");
  else
  asm volatile("s_mov_b32 %0, m0\n\ts_mov_b32 m0, %3\n\ts_nop 2\n\tglobal_load_lds_dwordx4 %1, %2\n\ts_mov_b32 m0, %0"
               : "=&s"(keep) : "v"(voff), "s"(sbase), "s"(lds_dst) : "memory");
}
template <bool SC>
__device__ __forceinline__ void glds16x4(const void* sbase, unsigned v0, unsigned v1, unsigned v2, unsigned v3, unsigned lds_dst) {
  unsigned keep;
  if (SC)
    asm volatile("s_mov_b32 %0, m0\n\ts_mov_b32 m0, %6\n\ts_nop 2\n\tglobal_load_lds_dwordx4 %1, %5 sc1\n\tglobal_load_lds_dwordx4 %2, %5 offset:1024 sc1\n\t"
                 "global_load_lds_dwordx4 %3, %5 offset:2048 sc1\n\tglobal_load_lds_dwordx4 %4, %5 offset:3072 sc1\n\ts_mov_b32 m0, %0"
                 : "=&s"(keep) : "v"(v0), "v"(v1), "v"(v2), "v"(v3), "s"(sbase), "s"(__builtin_amdgcn_readfirstlane(lds_dst)) : "memory");
  else
    asm volatile("s_mov_b32 %0, m0\n\ts_mov_b32 m0, %6\n\ts_nop 2\n\tglobal_load_lds_dwordx4 %1, %5\n\tglobal_load_lds_dwordx4 %2, %5 offset:1024\n\t"
                 "global_load_lds_dwordx4 %3, %5 offset:2048\n\tglobal_load_lds_dwordx4 %4, %5 offset:3072\n\ts_mov_b32 m0, %0"
                 : "=&s"(keep) : "v"(v0), "v"(v1), "v"(v2), "v"(v3), "s"(sbase), "s"(__builtin_amdgcn_readfirstlane(lds_dst)) : "memory");
}
constexpr float QSCALE = 0.17677669529663687f * 1.4426950408889634f;

__device__ __forceinline__ unsigned min4(u32x4 v) { const unsigned a = v[0] < v[1] ? v[0] : v[1], b = v[2] < v[3] ? v[2] : v[3]; return a < b ? a : b; }
#define LDS_AT(T, a) ((__attribute__((address_space(3))) T*)(uintptr_t)(a))
__device__ __forceinline__ u32x4 lds_rd4(unsigned a) { return *LDS_AT(const volatile u32x4, a); }
__device__ __forceinline__ void lds_wr(unsigned a, unsigned v) { *LDS_AT(volatile unsigned, a) = v; }
__device__ __forceinline__ void split3(float v, unsigned& p0, unsigned& p1, unsigned& p2) {
  p0 = pack2(v, v) & 0xffffu; v -= __uint_as_float(p0 << 16);
  p1 = pack2(v, v) & 0xffffu; v -= __uint_as_float(p1 << 16);
  p2 = pack2(v, v) & 0xffffu;
}

#define PIN() __builtin_amdgcn_sched_barrier(0)
template <int B, class F, int... I>
__device__ __forceinline__ void static_for_(F&& f, std::integer_sequence<int, I...>) { (f(std::integral_constant<int, B + I>{}), ...); }
template <int B, int E, class F>
__device__ __forceinline__ void static_for(F&& f) { static_for_<B>(f, std::make_integer_sequence<int, E - B>{}); }

#define TPRE 60
#define TPSL 0
#define FCPRE 44
#define FCSLP 2
#define SIGAT 24
#define XPRIO 1
#define OPD 8
#define LAUX 16
#define CSLP 2
#define EPRIO 3
#define OPRIO 1
#define XSLP 1
#define CSL1 8
#define XAUX 16
__device__ __forceinline__ void oproj_tail(const bool RES2, const int tid, const unsigned lbase, const unsigned flg, const unsigned fcm, const int tmi, const int tni, const int idx,
                                           unsigned* osig, unsigned* xsig, const unsigned tag,
                                           const u16* A, u16* out,
                                           const u16* res, const u16* __restrict__ w_r, double* stats,
                                           const double* stats_in, unsigned* hand, u32x4 (&xres)[2] TEARG) {
  constexpr int ldw = D_, PD = OPD;
  const int lane = tid & 63, wid = __builtin_amdgcn_readfirstlane(tid >> 6);
  const int m0 = tmi * 128, n0 = tni * 64;
  const int dk = RES2 ? 7 : 3;
  if (wid == 0) STAMP(dk, 0);
  TE(1, 3);
  asm volatile("" : "+s"(A) :: "memory");
  PIN();
  const int row = m0 + (wid & 3) * 32 + (wid >> 2) * 16 + (lane >> 3), col = n0 + (lane & 7) * 8;
  const unsigned tb = lbase + 147456;
  typedef __attribute__((address_space(1))) double* gdp;
  typedef __attribute__((address_space(1))) unsigned* gup;
  unsigned tr = tb + (row - m0) * 128 + (((lane ^ (lane >> 3)) & 7) << 4), oad;
  {
    oad = lane == 0 ? flg + 24 : flg + 64 + lane * 4;
    gup ps = (gup)(osig + lane * 64);
    asm volatile("" : "+v"(oad), "+v"(ps));
    asm volatile("s_waitcnt vmcnt(1)" ::: "memory");
    TE(2, 1);
    const unsigned old = __hip_atomic_fetch_add(LDS_AT(unsigned, oad), 1u, __ATOMIC_RELAXED, __HIP_MEMORY_SCOPE_WORKGROUP);
    if (__builtin_amdgcn_readfirstlane(old) == 7u && lane < RO) __hip_atomic_store(ps, tag, __ATOMIC_RELAXED, __HIP_MEMORY_SCOPE_AGENT);
    PIN();
    TE(2, 2);
    if (wid == 0) STAMP(dk, 12);
  }
  auto announce = [&]() __attribute__((always_inline)) {
    asm volatile("" ::: "memory");
    PIN();
    __builtin_amdgcn_s_barrier();
    PIN();
    if (wid == 0) STAMP(dk, 1);
    TE(2, 3); TE(4, 0); TE(7, 1);
  };
  if (wid < 4) {
    const __amdgpu_buffer_rsrc_t rsa = __builtin_amdgcn_make_buffer_rsrc(const_cast<u16*>(A), 0, S_ * D_ * 2, 0x00020000);
    int sa0 = (tmi * 4 + wid) * 65536, va0 = (lane >> 4) * 512 + (lane & 15) * 16;
    asm volatile("" : "+v"(va0));
    asm volatile("" : "+s"(sa0));
    auto lda = [&](int i, int h) { return __builtin_amdgcn_raw_buffer_load_b128(rsa, va0 + (i & 1) * 2048 + h * 256, sa0 + (i >> 1) * 4096, LAUX); };
    unsigned wad[4], tw[4];
    f32x4 acc[2][4];
    const int k = (lane >> 5) * 16 + ((lane >> 2) & 7);
#pragma unroll
    for (int j = 0; j < 4; ++j) {
      wad[j] = lbase + k * 128 + (((2 * j) ^ (k & 6)) << 4) + 8 * (lane & 3);
      tw[j] = tb + wid * 4096 + (lane & 15) * 128 + (((2 * j + (lane >> 5)) ^ (lane & 7)) << 4) + ((lane >> 4) & 1) * 8;
      asm volatile("" : "+v"(wad[j]), "+v"(tw[j]));
    }
    auto ldw_ = [&](int i, int j) {
      const unsigned a = wad[j] + (i & 3) * 4096;
      s16x4 lo = __builtin_amdgcn_ds_read_tr16_b64_v4i16(LDS_AT(s16x4, a));
      s16x4 h4 = __builtin_amdgcn_ds_read_tr16_b64_v4i16(LDS_AT(s16x4, a + 1024));
      return (s16x8)__builtin_shufflevector(lo, h4, 0, 1, 2, 3, 4, 5, 6, 7);
    };
    u32x4 id[2];
#pragma unroll
    for (int j = 0; j < 2; ++j) {
#pragma unroll
      for (int e = 0; e < 4; ++e) id[j][e] = (lane >> 4) == 2 * j + ((lane >> 3) & 1) && ((lane & 7) >> 1) == e ? ((lane & 1) ? 0x3f800000u : 0x3f80u) : 0u;
      asm volatile("" : "+v"(id[j]));
    }
    unsigned rad = tb + wid * 4096 + (lane & 15) * 128 + (((lane >> 4) ^ (lane & 7)) << 4), rfa = flg + 36 + wid;
    asm volatile("" : "+v"(rad), "+v"(rfa));
    u32x4 rb[2][2];
    u32x4 ow[2];
#pragma unroll
    for (int j = 0; j < 4; ++j) {
      const u32x2 v = *reinterpret_cast<const u32x2*>(w_r + n0 + j * 16 + (lane >> 4) * 4);
#pragma unroll
      for (int e = 0; e < 2; ++e) ow[j >> 1][(j & 1) * 2 + e] = (lane & 8) ? v[e] : 0x3f803f80u;
    }
    gdp pst = (gdp)(stats + (lane >> 4) % 3 * S_ + m0 + wid * 32 + (lane & 15));
    gup pps = (gup)(hand + 2 * HAND_N + ((tmi * 16 + tni) * 128 + wid * 32 + (lane & 15)) * 4 + (lane >> 4));
    int xo = (row * ldw + col) * 2;
    asm volatile("" : "+v"(tr), "+v"(pst), "+v"(pps), "+v"(xo), "+v"(ow[0]), "+v"(ow[1]));
    announce();
    u32x4 af[PD][2];
    s16x8 wf[2][4];
#pragma unroll
    for (int i = 0; i < PD; i += 2) { af[i][0] = lda(i, 0); af[i + 1][0] = lda(i + 1, 0); af[i][1] = lda(i, 1); af[i + 1][1] = lda(i + 1, 1); }
    PIN();
    __builtin_amdgcn_s_setprio(OPRIO);
#pragma unroll
    for (int q = 0; q < 4; ++q) rb[q >> 1][q & 1] = lds_rd4((rad ^ ((q & 1) * 64)) + (q >> 1) * 2048);
#pragma unroll
    for (int j = 0; j < 4; ++j) wf[j & 1][j >> 1] = ldw_(j & 1, j >> 1);
    PIN();
#pragma unroll
    for (int j = 0; j < 4; ++j)
#pragma unroll
      for (int h = 0; h < 2; ++h) { asm volatile("v_mfma_f32_16x16x32_bf16 %0, %1, %2, 0" : "=&v"(acc[h][j]) : "v"(id[j & 1]), "v"(rb[h][j >> 1])); PIN(); }
#pragma unroll 1
    for (int spin = 0; spin < SPIN_MAX; ++spin)
      if (__builtin_amdgcn_readfirstlane(*LDS_AT(const volatile unsigned, flg)) >= 40u) break;
    asm volatile("" : "+v"(va0) :: "memory");
    PIN();
    s16x4 wlo;
    static_for<0, 16>([&](auto ic) __attribute__((always_inline)) {
      constexpr int i = 2 * decltype(ic)::value;
      if (i == SIGAT) { __hip_atomic_fetch_add(LDS_AT(unsigned, oad + 4), 1u, __ATOMIC_RELAXED, __HIP_MEMORY_SCOPE_WORKGROUP); PIN(); }
#pragma unroll
      for (int g = 0; g < 16; ++g) {
        const int e = g & 1, j = g >> 2, h = (g >> 1) & 1, jn = (j + 2) & 3, in = j < 2 ? i : i + 2;
        asm volatile("v_mfma_f32_16x16x32_bf16 %0, %1, %2, %0" : "+v"(acc[h][j]) : "v"(wf[e][j]), "v"(af[(i + e) % PD][h]));
        PIN();
        if (in == 16 && !(g & 3)) asm volatile("v_add_u32 %0, 0x10000, %0" : "+v"(wad[jn]));
        if (in < 32) {
          const s16x4 v = __builtin_amdgcn_ds_read_tr16_b64_v4i16(LDS_AT(s16x4, wad[jn] + ((in + h) & 15) * 4096 + e * 1024));
          if (e) wf[h][jn] = (s16x8)__builtin_shufflevector(wlo, v, 0, 1, 2, 3, 4, 5, 6, 7);
          else wlo = v;
          PIN();
        }
        if ((g & 3) == 1 && i > 0 && i + PD - 2 < 32) { af[(i - 2 + (j & 1)) % PD][j >> 1] = lda(i + PD - 2 + (j & 1), j >> 1); PIN(); }
        if ((g & 3) == 3 && in < 32 && i != SIGAT) { __builtin_amdgcn_s_waitcnt(0xc47f); PIN(); }
      }
    });
    asm volatile("s_nop 3" : "+v"(acc[0][0]), "+v"(acc[1][0]));
    TE(4, 1); TE(7, 2);
    if (wid == 0) STAMP(dk, 17);
    if (wid == 1 && !RES2) STAMP(4, 0);
    u32x4 pk[2][2];
    float sv[2];
    auto sums = [&]() __attribute__((always_inline)) {
      f32x4 d[2];
      float q[2];
#pragma unroll
      for (int h = 0; h < 2; ++h) {
        d[h] = __builtin_amdgcn_mfma_f32_16x16x32_bf16(__builtin_bit_cast(s16x8, ow[0]), __builtin_bit_cast(s16x8, pk[h][0]), f32x4{0.f, 0.f, 0.f, 0.f}, 0, 0, 0);
        d[h] = __builtin_amdgcn_mfma_f32_16x16x32_bf16(__builtin_bit_cast(s16x8, ow[1]), __builtin_bit_cast(s16x8, pk[h][1]), d[h], 0, 0, 0);
        q[h] = 0.f;
      }
#pragma unroll
      for (int e = 0; e < 16; ++e) {
        const bf16x2 x = __builtin_bit_cast(bf16x2, (unsigned)pk[e & 1][e >> 3][(e >> 1) & 3]);
        q[e & 1] = __builtin_amdgcn_fdot2_f32_bf16(x, x, q[e & 1], false);
      }
      const auto a = __builtin_amdgcn_permlane32_swap(__float_as_uint(q[0]), __float_as_uint(q[1]), false, false);
      const unsigned b = __float_as_uint(__uint_as_float(a[0]) + __uint_as_float(a[1]));
      const auto c = __builtin_amdgcn_permlane16_swap(b, b, false, false);
      const unsigned e = __float_as_uint(__uint_as_float(c[0]) + __uint_as_float(c[1]));
      const auto f = __builtin_amdgcn_permlane32_swap(e, e, false, false);
#pragma unroll
      for (int h = 0; h < 2; ++h) sv[h] = (lane & 16) ? __uint_as_float(f[h]) : d[h][0];
    };
#pragma unroll
    for (int j = 0; j < 4; ++j)
#pragma unroll
      for (int h = 0; h < 2; ++h) {
        pk[h][j >> 1][(j & 1) * 2] = pack2(acc[h][j][0], acc[h][j][1]); pk[h][j >> 1][(j & 1) * 2 + 1] = pack2(acc[h][j][2], acc[h][j][3]);
        *LDS_AT(u32x2, tw[j] + h * 2048) = u32x2{pk[h][j >> 1][(j & 1) * 2], pk[h][j >> 1][(j & 1) * 2 + 1]};
        PIN();
      }
    asm volatile("" ::: "memory");
    *LDS_AT(volatile unsigned char, rfa) = (unsigned char)2;
#pragma unroll
    for (int p = 0; p < 2; ++p) xres[p] = lds_rd4(tr + p * 1024);
    PIN();
    if (!RES2) {
      const __amdgpu_buffer_rsrc_t rso = __builtin_amdgcn_make_buffer_rsrc(out, 0, S_ * D_ * 2, 0x00020000);
      const __amdgpu_buffer_rsrc_t rsf = __builtin_amdgcn_make_buffer_rsrc(xsig + wid, 0, RX * 512, 0x00020000);
      const u32x4 x2 = lds_rd4(tr + 2048), x3 = lds_rd4(tr + 3072);
      PIN();
#pragma unroll
      for (int p = 0; p < 4; ++p) { __builtin_amdgcn_raw_buffer_store_b128(p < 2 ? xres[p] : (p < 3 ? x2 : x3), rso, xo + p * (8 * ldw * 2), 0, XAUX); PIN(); }
      TE(4, 2); TE(5, 0);
      sums();
      if (lane < 48) { __builtin_amdgcn_global_atomic_fadd_f64(pst, (double)sv[0]); __builtin_amdgcn_global_atomic_fadd_f64(pst + 16, (double)sv[1]); }
      PIN();
      TE(4, 3); TE(5, 1);
      asm volatile("s_waitcnt vmcnt(2)" ::: "memory");
      __builtin_amdgcn_raw_buffer_store_b32(tag, rsf, lane * 512 + 256, 0, 16);
      *LDS_AT(volatile unsigned char, rfa) = (unsigned char)3;
      PIN();
      TE(5, 2);
      asm volatile("s_waitcnt vmcnt(1)" ::: "memory");
      __builtin_amdgcn_raw_buffer_store_b32(tag, rsf, lane * 512, 0, 16);
      PIN();
      TE(5, 3);
      __builtin_amdgcn_s_setprio(0);
      return;
    }
    TE(4, 2); TE(5, 0);
    sums();
    __hip_atomic_store(pps, f2word(sv[0]), __ATOMIC_RELAXED, __HIP_MEMORY_SCOPE_AGENT);
    __hip_atomic_store(pps + 64, f2word(sv[1]), __ATOMIC_RELAXED, __HIP_MEMORY_SCOPE_AGENT);
    if (wid == 0) STAMP(dk, 14);
  } else {
    u32x4 rres[4];
    double is1[4], is2[4];
#pragma unroll
    for (int p = 0; p < 4; ++p)
      rres[p] = __builtin_amdgcn_raw_buffer_load_b128(__builtin_amdgcn_make_buffer_rsrc(const_cast<u16*>(res), 0, S_ * D_ * 2, 0x00020000), ((row - 16 + 8 * p) * ldw + col) * 2, 0, 16);
#pragma unroll
    for (int p = 0; p < 4; ++p) { is1[p] = ld_ag(stats_in + row - 16 + 8 * p); is2[p] = ld_ag(stats_in + S_ + row - 16 + 8 * p); }
    PIN();
    if (RES2) {
#pragma unroll
      for (int p = 0; p < 4; ++p) {
        float mean, var, mb, den;
        ln_from_sums(is1[p], is2[p], mean, var, mb, den);
        const float rin = frcp(den);
#pragma unroll
        for (int e = 0; e < 4; ++e) {
          const f32x2 x = {__uint_as_float(rres[p][e] << 16), __uint_as_float(rres[p][e] & 0xffff0000u)};
          const f32x2 h = (x - f32x2{mb, mb}) * f32x2{rin, rin} + x;
          rres[p][e] = pack2(h[0], h[1]);
        }
        asm volatile("" : "+v"(rres[p]));
      }
      PIN();
    }
    if ((idx >> 5) == tmi * 4 + wid - 4) {
      const int dr = idx - (row - 16);
      u32x4 qq;
#pragma unroll 1
      for (int i = 0; i < SPIN_MAX; ++i) {
        qq = __builtin_amdgcn_raw_buffer_load_b128(__builtin_amdgcn_make_buffer_rsrc(hand, 0, HAND_N * 4, 0x00020000), (GRAN_N + Y_N) * 4 + col * 2, 0, 16);
        if (none(umax(umax(qq[0], qq[1]), umax(qq[2], qq[3])) == SENT)) break;
        asm volatile("" ::: "memory");
      }
#pragma unroll
      for (int p = 0; p < 4; ++p)
#pragma unroll
        for (int e = 0; e < 4; ++e) rres[p][e] = dr == 8 * p ? qq[e] : rres[p][e];
    }
#pragma unroll 1
    for (int i = 0; i < 4 * SPIN_MAX; ++i)
      if (__builtin_amdgcn_readfirstlane(*LDS_AT(const volatile unsigned, flg + 16)) >= 12u) break;
#pragma unroll
    for (int p = 0; p < 4; ++p) *LDS_AT(volatile u32x4, tr - 2048 + p * 1024) = rres[p];
    asm volatile("s_waitcnt lgkmcnt(0)" ::: "memory");
    announce();
    TE(4, 1); TE(7, 2);
    asm volatile("s_waitcnt vmcnt(0)" ::: "memory");
    if (!RES2) return;
    __builtin_amdgcn_s_sleep(TPRE);
    unsigned fm = SENT;
#pragma unroll 1
    for (int i = 0; i < 4 * SPIN_MAX; ++i) {
      fm = __builtin_amdgcn_readfirstlane(*LDS_AT(const volatile unsigned, fcm));
      if (fm != SENT) break;
      __builtin_amdgcn_s_sleep(1);
    }
    const unsigned r = (lane & 8) ? fm >> 16 : fm & 0xffffu;
    unsigned fra = tb + (r & 127u) * 128 + (((r ^ lane) & 7u) << 4);
    int fro = (r >> 5) == (unsigned)(tmi * 4 + wid - 4) && lane < 16 ? ((lane >> 3) * ldw + col) * 2 : S_ * D_ * 2;
    asm volatile("" : "+v"(fra), "+v"(fro));
#pragma unroll 1
    for (int i = 0; i < 4 * SPIN_MAX; ++i) {
      if (__builtin_amdgcn_readfirstlane(*LDS_AT(const volatile unsigned char, flg + 32 + wid)) == 2u) break;
      __builtin_amdgcn_s_sleep(TPSL);
    }
    asm volatile("" ::: "memory");
    __builtin_amdgcn_s_setprio(XPRIO);
    PIN();
    TE(4, 2); TE(5, 0);
    const u32x4 xr = lds_rd4(fra);
#pragma unroll
    for (int p = 0; p < 2; ++p) xres[p] = lds_rd4(tr + p * 1024);
    PIN();
    __builtin_amdgcn_raw_buffer_store_b128(xr, __builtin_amdgcn_make_buffer_rsrc(out, 0, 3 * D_ * 2, 0x00020000), fro, 0, XAUX);
  }
  TE(4, 3); TE(7, 3);
  PIN();
  __builtin_amdgcn_s_setprio(0);
  __hip_atomic_fetch_add(LDS_AT(unsigned, oad), 1u, __ATOMIC_RELAXED, __HIP_MEMORY_SCOPE_WORKGROUP);
  TE(5, 1); TE(5, 2);
  PIN();
}

#define MFMA32(a, b, c) __builtin_amdgcn_mfma_f32_32x32x16_bf16(a, b, c, 0, 0, 0)
#define MFMA16(a, b, c) __builtin_amdgcn_mfma_f32_16x16x32_bf16(a, b, c, 0, 0, 0)

#define PLO(x) __builtin_bit_cast(bf16x16, __builtin_shufflevector(x, x, 0, 1, 2, 3, 4, 5, 6, 7))
#define PHI(x) __builtin_bit_cast(bf16x16, __builtin_shufflevector(x, x, 8, 9, 10, 11, 12, 13, 14, 15))
#define SMF32(a, b, c) __builtin_amdgcn_smfmac_f32_32x32x32_bf16(__builtin_bit_cast(bf16x8, a), b, c, sidx, 0, 0)
#define SMF16(a, b, c) __builtin_amdgcn_smfmac_f32_16x16x64_bf16(__builtin_bit_cast(bf16x8, a), b, c, sidx, 0, 0)

__device__ __forceinline__ void patch_kv(unsigned tab, int r, s16x8& k0, s16x8& k1, s16x8& v0, s16x8& v1, bool on = true) {
  const int lane = fresh_lane(), hi = lane >> 5;
  if (on && (lane & 31) == kpos(r)) {
    k0 = *LDS_AT(const s16x8, tab + hi * 16); k1 = *LDS_AT(const s16x8, tab + 32 + hi * 16);
  }
  const short vv = *LDS_AT(const short, tab + 128 + (lane & 31) * 2);
  const int js = (on && hi == ((r >> 2) & 1)) ? (r >> 4) * 8 + ((r >> 3) & 1) * 4 + (r & 3) : -1;
#pragma unroll
  for (int e = 0; e < 8; ++e) { v0[e] = js == e ? vv : v0[e]; v1[e] = js == 8 + e ? vv : v1[e]; }
}

__device__ __forceinline__ void attn_safe(const u16* KV, int tile0, s16x8 q0, s16x8 q1,
                                       f32x16& o, float& l_out, int ptile, int prow, unsigned ptab) {
  const int lane = fresh_lane();
  float m = -INFINITY, l = 0.f;
  const int sidx = 0xDDDDDDDD;
  const __amdgpu_buffer_rsrc_t rs = __builtin_amdgcn_make_buffer_rsrc(const_cast<u16*>(KV), 0, 2 * S_ * D_ * 2, 0x00020000);
#pragma unroll
  for (int e = 0; e < 16; ++e) o[e] = 0.f;
  for (int tt = 0; tt < 64; ++tt) {
    const int t = tile0 + tt, off = t * 2048 + lane * 16;
    s16x8 k0 = __builtin_bit_cast(s16x8, __builtin_amdgcn_raw_buffer_load_b128(rs, off, 0, 16));
    s16x8 k1 = __builtin_bit_cast(s16x8, __builtin_amdgcn_raw_buffer_load_b128(rs, off + 1024, 0, 16));
    s16x8 v0 = __builtin_bit_cast(s16x8, __builtin_amdgcn_raw_buffer_load_b128(rs, off + S_ * D_ * 2, 0, 16));
    s16x8 v1 = __builtin_bit_cast(s16x8, __builtin_amdgcn_raw_buffer_load_b128(rs, off + S_ * D_ * 2 + 1024, 0, 16));
    if (t == ptile) patch_kv(ptab, prow, k0, k1, v0, v1);
    f32x16 sc;
#pragma unroll
    for (int e = 0; e < 16; ++e) sc[e] = 0.f;
    sc = MFMA32(k0, q0, sc); sc = MFMA32(k1, q1, sc);
    float mt = sc[0];
#pragma unroll
    for (int e = 1; e < 16; ++e) mt = fmaxf(mt, sc[e]);
    mt = fmaxf(mt, __shfl_xor(mt, 32, 64));
    const float mn = fmaxf(m, mt);
    const float alpha = __builtin_amdgcn_exp2f(m - mn);
    m = mn;
#pragma unroll
    for (int e = 0; e < 16; ++e) sc[e] = __builtin_amdgcn_exp2f(sc[e] - mn);
    float ls = 0.f;
#pragma unroll
    for (int e = 0; e < 16; ++e) ls += __uint_as_float(__float_as_uint(sc[e]) & 0xffff0000u);
    l = l * alpha + ls;
#pragma unroll
    for (int e = 0; e < 16; ++e) o[e] *= alpha;
    o = SMF32(v0, PLO(sc), o); o = SMF32(v1, PHI(sc), o);
  }
  l_out = l + __shfl_xor(l, 32, 64);
}

__device__ __forceinline__ float wave_fmax(float v) {
  asm volatile("s_nop 1\n\tv_max_f32_dpp %0, %0, %0 row_shr:1 row_mask:0xf bank_mask:0xf\n\ts_nop 1\n\tv_max_f32_dpp %0, %0, %0 row_shr:2 row_mask:0xf bank_mask:0xf\n\t"
               "s_nop 1\n\tv_max_f32_dpp %0, %0, %0 row_shr:4 row_mask:0xf bank_mask:0xf\n\ts_nop 1\n\tv_max_f32_dpp %0, %0, %0 row_shr:8 row_mask:0xf bank_mask:0xf\n\t"
               "s_nop 1\n\tv_max_f32_dpp %0, %0, %0 row_bcast:15 row_mask:0xa bank_mask:0xf\n\ts_nop 1\n\tv_max_f32_dpp %0, %0, %0 row_bcast:31 row_mask:0xc bank_mask:0xf\n\ts_nop 0"
               : "+v"(v));
  return __int_as_float(__builtin_amdgcn_readlane(__float_as_int(v), 63));
}

#define PSLP 8
#define WSLP 3
#define PRE1 8
#define PRE5 8
#define W3SLP 4
#define FSTART 10
#define OAUX 16
#define OMX 4
#define T0 2u
#define TW 10u
#define HW 2
#define HP1 1
#define POLLAT 11
#define X2AT 2
#define HPRIO 3
#define SSLP 1
#define SPRE 6
#define WGAPS {0, 1, 7, 8, 9, 10, 11, 12}
#define KVS 4
#define PNS 12
#define G0 5u
#define GS 2u
#define IAT 1u
#define YSLP 2
#define ISLP 8
#define KSLP 2
#define UAT 13u
#define UHW 0
#define PPSLP 2
#define XRSLP 2
#define MPRE 20
#define YD1 150
#define YD2 180
#define KDT 200
#define XDT 45
#define R2PRIO 0
#define LNPRIO 1
#define MSLP 4
#define BARG 0
struct Args {
  long long lay;
  const u16* a; u16* kv; u16* o; u16* xo; double* so;
  const u16* W; u16* wc;
  const u16 *wg, *wu, *wd, *wo, *w_r;
  unsigned* gran; float* csf; double* stats; unsigned* hand; u16* out;
};
typedef __attribute__((address_space(4))) const Args* ArgP;
#define WCAT 3
#define CAP0 1
#define PRE4 50
#define PRE7 20
#define UARR 12u
#define GF0 10
#define XGATE 8
#define ASLP 1
#define B15 3
#define KO 5
#define PRO 2
#define HS 2
#define XD2 90
#define TFL 4u
#define WMAP 0x32100333
#define SMAP 0
#define STCH 6
#define KPOLL 10
#define TPRIO 2
#define TSLP 9
__device__ __forceinline__ bool slow_cols(const u16* W, int tni) { return ((((unsigned)(uintptr_t)W >> 7) + tni) & 7) == 3; }
#define ARGS_HERE                                                                                                      \
  ArgP ap_ = ap; asm volatile("" : "+s"(ap_));                                                                          \
  const u16* A = ap_->a; const u16* __restrict__ W = ap_->W; u16* KV = ap_->kv; u16* Of = ap_->o;        \
  const u16* __restrict__ wg = ap_->wg; const u16* __restrict__ wu = ap_->wu; const u16* __restrict__ wd = ap_->wd;       \
  const u16* __restrict__ wo = ap_->wo; const u16* __restrict__ w_r = ap_->w_r; unsigned* gran = ap_->gran;              \
  float* csf = ap_->csf; const double* stats = ap_->stats; double* stats_out = ap_->so; unsigned* hand = ap_->hand; \
  u16* xout = ap_->xo; u16* __restrict__ out = ap_->out
#define AGAIN "s_getpc_b64 s[90:91]\n\ts_add_u32 s90, s90, layers_kernel@rel32@lo+4\n\ts_addc_u32 s91, s91, layers_kernel@rel32@hi+12\n\ts_setpc_b64 s[90:91]"
#define KEPT(c)                                                                                                        \
  c "{v[104:107]}"(gu[0]), c "{v[108:111]}"(gu[1]), c "{v[112:115]}"(gu[2]), c "{v[116:119]}"(gu[3]), c "{v[120:123]}"(gu[4]),      \
  c "{v[124:127]}"(gu[5]), c "{v[128:131]}"(gu[6]), c "{v[132:135]}"(gu[7]), c "{v[136:139]}"(gu[8]), c "{v[140:143]}"(gu[9]),      \
  c "{v[144:147]}"(gu[10]), c "{v[148:151]}"(gu[11]), c "{v[152:155]}"(gu[12]), c "{v[156:159]}"(gu[13]), c "{v[160:163]}"(gu[14]), \
  c "{v[164:167]}"(gu[15])
#define LAYER_HERE ArgP apl_ = ap; asm volatile("" : "+s"(apl_)); const bool LAY2 = apl_->lay != 0
__device__ __forceinline__ void layer_body(const int wid, const int bid, const unsigned lbase, const ArgP ap, const unsigned long long t_entry) {
  constexpr int K = 1024, NKT = 16, NST = 3, ABYTES = 16384, WBYTES = 24576, STB = ABYTES + WBYTES, ldw = 3 * D_;
  constexpr int TABA = NST * STB, ALPHA = TABA + 2048, CRED = ALPHA + 512;
  constexpr int ASB = 16384, FLG = 8 * ASB + 256;
  constexpr int DUM = FLG + 64, TAB = DUM + 256, RED = TAB + 512, SACT = RED + 4096, XML = SACT + 3584, UGL = XML + 4096, SPM = UGL + 1024, QOFF = 9 * ASB;
  constexpr int TABW = RED;
  static_assert(SPM + 2048 <= QOFF && CRED <= 8 * ASB && RED + 8192 <= QOFF && RED % 16 == 0, "lds");
  const int xcd = (bid & 1) * 4 + ((bid >> 1) & 3), loc = bid >> 3;
  const int tmi = (xcd / QNG) * QMX + loc / QNX, tni = (xcd % QNG) * QNX + loc % QNX;
  const int m0 = tmi * 128, n0 = tni * 64;
  const int omi = (xcd / (OMX / 2)) * OMX + loc / (32 / OMX), oni = (xcd % (OMX / 2)) * (32 / OMX) + loc % (32 / OMX);
  constexpr unsigned VOFF = (unsigned)S_ * D_ * 2;
  auto tile_at = [&](unsigned n) __attribute__((always_inline)) {
#pragma unroll 1
    for (int i = 0; i < 4 * SPIN_MAX; ++i) {
      if (__builtin_amdgcn_readfirstlane(*LDS_AT(const volatile unsigned, lbase + FLG + 36)) == n * 0x01010101u) break;
      __builtin_amdgcn_s_sleep(1);
    }
    asm volatile("" ::: "memory");
    PIN();
  };
  auto all_ge = [&](unsigned w, unsigned n) { return ((w + (128u - n) * 0x01010101u) & 0x80808080u) == 0x80808080u; };
  if (wid >= 8) {
    u32x4 gu[16];
    LAYER_HERE;
    constexpr int dk = 6;
    const int hw = wid - 8, b = bid;
    int nb = 0;
    constexpr int lim = NKT - 1;
    auto tick_nw = [&]() {
      PIN();
      if (nb < lim) { __builtin_amdgcn_s_barrier(); ++nb; } else __builtin_amdgcn_s_sleep(2);
      asm volatile("" ::: "memory");
      PIN();
    };
    auto tick = [&]() {
      asm volatile("s_waitcnt lgkmcnt(0)" ::: "memory");
      tick_nw();
    };
    auto fold16 = [&](f32x2 y0, f32x2 y1) __attribute__((always_inline)) {
      const auto q0 = __builtin_amdgcn_permlane32_swap(__float_as_uint(y0[0]), __float_as_uint(y1[0]), false, false);
      const auto q1 = __builtin_amdgcn_permlane32_swap(__float_as_uint(y0[1]), __float_as_uint(y1[1]), false, false);
      const auto q = __builtin_amdgcn_permlane16_swap(__float_as_uint(__uint_as_float(q0[0]) + __uint_as_float(q0[1])),
                                                      __float_as_uint(__uint_as_float(q1[0]) + __uint_as_float(q1[1])), false, false);
      return __uint_as_float(q[0]) + __uint_as_float(q[1]);
    };
    unsigned best, tag, rmd;
    float alpha, m, wsum;
    u32x4 tw;
    u32x2 xq;
    s16x8 sl[8];
    const int kcap = CAP0 + (tmi & 7);
    if (!LAY2) {
      ARGS_HERE;
      const int lane = fresh_lane(), t = hw * 64 + lane;
      const unsigned ep = ld_ag(gran);
      const int srow = m0 + tni * 8 + hw * 2;
      u32x4 xa[2][2], wv[8];
      const int wr_ = (3 - ((unsigned)(uintptr_t)W >> 7) - (tni & ~3)) & 7;
      const bool cpy = hw == 3 && wr_ < 4 && wr_ != (tni & 3), wld = hw < 3 ? wr_ != (tni & 3) : cpy;
      const int wcn = cpy ? (tni & ~3) + wr_ : tni, wcp = cpy ? (tni - wr_ - 1) & 3 : hw;
      const int wof = ((tmi * 64 + (lane >> 3)) * ldw + wcp * D_ + wcn * 64 + (lane & 7) * 8) * 2;
      auto ld_w = [&]() {
        const __amdgpu_buffer_rsrc_t rw = __builtin_amdgcn_make_buffer_rsrc(const_cast<u16*>(W), 0, wld ? D_ * ldw * 2 : 0, 0x00020000);
#pragma unroll
        for (int r = 0; r < 8; ++r) wv[r] = __builtin_amdgcn_raw_buffer_load_b128(rw, wof, r * 8 * ldw * 2, 0);
        PIN();
      };
      auto wcopy = [&]() __attribute__((always_inline)) {
        if (cpy) {
          const __amdgpu_buffer_rsrc_t rw = __builtin_amdgcn_make_buffer_rsrc(ap_->wc, 0, D_ * ldw * 2, 0x00020000);
#pragma unroll
          for (int r = 0; r < 8; ++r) __builtin_amdgcn_raw_buffer_store_b128(wv[r], rw, wof + r * 8 * ldw * 2, 0, 16);
        }
        PIN();
      };
      const int wat = ((hw == 3 ? SMAP : WMAP) >> (4 * (tmi & 7))) & 3;
      unsigned ps = 0u;
      auto stouch = [&](int at) __attribute__((always_inline)) {
        if (at == STCH && cpy) ps = __builtin_amdgcn_raw_buffer_load_b32(__builtin_amdgcn_make_buffer_rsrc(const_cast<u16*>(W), 0, D_ * ldw * 2, 0x00020000),
                                                                        (((tmi ^ 8) * 64 + lane) * ldw + wcp * D_ + wcn * 64) * 2, 0, 0);
        PIN();
      };
      auto ld_x = [&](int h) {
#pragma unroll
        for (int r = 0; r < 2; ++r) xa[r][h] = *reinterpret_cast<const u32x4*>(A + (size_t)(srow + r) * D_ + (h ^ (tmi >> 3)) * 512 + lane * 8);
        PIN();
      };
      ld_x(0);
      if (X2AT == 0) ld_x(1);
      if (wat == 0) ld_w();
      unsigned pc = 0u;
      if (hw == 3) {
        unsigned long long a0, a1;
        CODE_ADDR("layers_kernel", a0);
        CODE_ADDR("end_marker_kernel", a1);
        pc = touch_code(a0, a1, loc, lane);
      }
      if (hw == 1) pc = ld_ag(gran + ((lane & 1) ? GR_ROW + (m0 + tni * 8) * 4 : GR_KV + (tni * RK + tmi % RK) * 32));
      if (hw == 2) st_ag(gran + GR_N + bid * 64 + lane, lane + 1u);
      {
        const int gi = bid * 256 + t;
        if (gi < GRAN_N / 4) __builtin_amdgcn_raw_buffer_store_b128(u32x4{SENT, SENT, SENT, SENT}, __builtin_amdgcn_make_buffer_rsrc(hand, 0, GRAN_N * 4, 0x00020000), gi * 16, 0, 16);
        if (t < 24) __hip_atomic_store(stats_out + (t >> 3) * S_ + m0 + tni * 8 + (t & 7), 0.0, __ATOMIC_RELAXED, __HIP_MEMORY_SCOPE_AGENT);
      }
      tick();
      if (wat == 1) ld_w();
      if (X2AT == 1) ld_x(1);
      tick();
      if (wat == 2) ld_w();
      if (WCAT == 2) wcopy();
      if (X2AT == 2) ld_x(1);
      asm volatile("" :: "v"(ep));
      tick();
      if (wat == 3) ld_w();
      if (WCAT == 3) wcopy();
      if (X2AT == 3) ld_x(1);
      stouch(3);
#pragma unroll
      for (int i = 0; i < HW; ++i) { tick(); stouch(4 + i); }
      __builtin_amdgcn_s_setprio(HP1);
      PIN();
      tag = ep * 1664525u + 1013904223u;
      const __amdgpu_buffer_rsrc_t rsc = __builtin_amdgcn_make_buffer_rsrc(gran, 0, GR_N * 4, 0x00020000);
      {
        u32x4 od;
#pragma unroll
        for (int q = 0; q < 4; ++q) od[q] = ((lane & 7) >> 1) == q ? ((lane & 1) ? 0x3f800000u : 0x00003f80u) : 0u;
        f32x4 d[2] = {{0.f, 0.f, 0.f, 0.f}, {0.f, 0.f, 0.f, 0.f}};
#pragma unroll
        for (int r = 0; r < 8; ++r) d[r & 1] = __builtin_amdgcn_mfma_f32_16x16x32_bf16(__builtin_bit_cast(s16x8, od), __builtin_bit_cast(s16x8, wv[r]), d[r & 1], 0, 0, 0);
        u32x4 gr;
#pragma unroll
        for (int q = 0; q < 4; ++q) gr[q] = ((__float_as_uint(dpp_add<0x128, 0xf>(fadd(d[0][q], d[1][q]))) + 0x80u) & ~0xffu) | ((tag >> (8 * q)) & 0xffu);
        asm volatile("s_waitcnt vmcnt(0)" ::: "memory");
        __builtin_amdgcn_raw_buffer_store_b128(gr, rsc, (lane & 40) || !wld ? GR_N * 4 : (GR_COL + (wcn * 16 + tmi) * 192 + wcp * 64 + (lane & 7) * 8 + (lane >> 4) * 4) * 4, 0, 16);
      }
      asm volatile("" :: "v"(pc));
      tick_nw();
      stouch(6);
      {
        float xv[2][16], mean[2];
#pragma unroll
        for (int r = 0; r < 2; ++r) {
          float sm = 0.f;
#pragma unroll
          for (int i = 0; i < 4; ++i) {
            xv[r][2 * i] = __uint_as_float(xa[r][0][i] << 16); xv[r][2 * i + 1] = __uint_as_float(xa[r][0][i] & 0xffff0000u);
            xv[r][8 + 2 * i] = __uint_as_float(xa[r][1][i] << 16); xv[r][8 + 2 * i + 1] = __uint_as_float(xa[r][1][i] & 0xffff0000u);
          }
#pragma unroll
          for (int i = 0; i < 16; ++i) sm += xv[r][i];
          mean[r] = wave_sum(sm) * (1.0f / 1024.0f);
          tick();
          stouch(7 + r);
        }
        float vr[2];
#pragma unroll
        for (int r = 0; r < 2; ++r) {
          float v = 0.f;
#pragma unroll
          for (int i = 0; i < 16; ++i) { const float d = xv[r][i] - mean[r]; v += d * d; }
          vr[r] = wave_sum(v) * (1.0f / 1024.0f);
          if (r == 0) tick();
        }
        const float vv = lane & 2 ? vr[1] : vr[0], mm = lane & 2 ? mean[1] : mean[0];
        const float rin = 1.0f / rbf(sqrtf(rbf(rbf(vv) + 1e-5f)));
        if (lane < 4) __builtin_amdgcn_raw_buffer_store_b64(u32x2{__float_as_uint((lane & 1) ? rbf(mm) : rin), tag}, rsc, (GR_ROW + srow * 4 + lane * 2) * 4, 0, 16);
      }
      tick();
      float cs;
      while (nb < POLLAT) tick();
      asm volatile("" :: "v"(ps));
#pragma unroll 1
      for (int spin = 0;; ++spin) {
        u32x4 csv[4];
        const __amdgpu_buffer_rsrc_t rsw = __builtin_amdgcn_make_buffer_rsrc(gran, 0, hw < 3 ? GR_N * 4 : 0, 0x00020000);
#pragma unroll
        for (int i = 0; i < 4; ++i) csv[i] = __builtin_amdgcn_raw_buffer_load_b128(rsw, (GR_COL + (tni * 16 + (lane >> 4)) * 192 + hw * 64 + (lane & 15) * 4) * 4, i * (4 * 192 * 4), 16);
        const u32x4 rp = __builtin_amdgcn_raw_buffer_load_b128(rsc, (GR_ROW + (m0 + (t & 127)) * 4) * 4, 0, 16);
        PIN();
        tick(); tick();
        unsigned bad = 0u;
        float z[4];
#pragma unroll
        for (int q = 0; q < 4; ++q) {
          const unsigned tq = tag >> (8 * q);
          bad |= ((csv[0][q] ^ tq) | (csv[1][q] ^ tq)) | ((csv[2][q] ^ tq) | (csv[3][q] ^ tq));
          z[q] = fadd(fadd(__uint_as_float(csv[0][q] & ~0xffu), __uint_as_float(csv[1][q] & ~0xffu)), fadd(__uint_as_float(csv[2][q] & ~0xffu), __uint_as_float(csv[3][q] & ~0xffu)));
        }
        cs = fold16(f32x2{z[0], z[1]}, f32x2{z[2], z[3]});
        bad = (hw < 3 ? bad & 0xffu : 0u) | (rp[1] ^ tag) | (rp[3] ^ tag);
        alpha = __uint_as_float(rp[0]); m = __uint_as_float(rp[2]);
        if (none(bad != 0u) || spin >= SPIN_MAX) break;
      }
      if (tmi == 0 && hw < 3) st_ag(reinterpret_cast<unsigned*>(csf) + hw * 1024 + n0 + (lane & 15) * 4 + (lane >> 4), __float_as_uint(cs));
      unsigned c0, c1, c2;
      split3(cs, c0, c1, c2);
      tw = u32x4{c0 | (c1 << 16), c0 | (c2 << 16), c1 | (c0 << 16), 0u};
      best = rmd = 0u;
      wsum = 0.f;
      asm volatile("" : "=v"(xq), KEPT("="));
#pragma unroll
      for (int i = 0; i < 8; ++i) asm volatile("" : "=v"(sl[i]));
    } else {
      asm volatile("; kept registers" : KEPT("="));
      PIN();
      ARGS_HERE;
      const int lane = fresh_lane(), t = hw * 64 + lane;
      best = rmd = 0u;
      tag = __builtin_amdgcn_readfirstlane(*LDS_AT(const volatile unsigned, lbase + FLG + 20));
#pragma unroll
      for (int i = 0; i < 8; ++i) asm volatile("" : "=v"(sl[i]));
      unsigned sad;
      auto tickc = [&]() __attribute__((always_inline)) {
        tick();
        if (nb == kcap + 1) {
#pragma unroll
          for (int i = 0; i < 8; ++i) {
            const unsigned a = (sad ^ ((i >> 2) * 64) ^ (((i >> 1) & 1) * 16)) + (i & 1) * 12288;
            sl[i] = __builtin_shufflevector(__builtin_amdgcn_ds_read_tr16_b64_v4i16(LDS_AT(s16x4, a)), __builtin_amdgcn_ds_read_tr16_b64_v4i16(LDS_AT(s16x4, a + 1536)), 0, 1, 2, 3, 4, 5, 6, 7);
          }
        }
        PIN();
      };
      static_assert(CAP0 + 8 <= KPOLL, "the stage is taken before that");
      if (hw == 1) STAMP(4, 4);
      if (hw == 0) {
        __builtin_amdgcn_s_sleep(TPRE);
        tile_at(3u);
        __builtin_amdgcn_s_sleep(XGATE);
        STAMP(4, 3);
#pragma unroll 1
        for (int spin = 0; spin < SPIN_MAX; ++spin) {
          if (none(ld_ag(gran + GR_X + (tmi * RX + tni % RX) * 128 + 64 + lane) != tag)) break;
          __builtin_amdgcn_s_sleep(1);
        }
      }
      static_assert(UHW == 0, "");
      if (hw == 0) asm volatile("s_waitcnt vmcnt(0)" ::: "memory");
      if (hw == 0) STAMP(4, 5);
      asm volatile("" ::: "memory");
      PIN();
      __builtin_amdgcn_s_barrier();
      PIN();
      if (hw == 1) STAMP(4, 6);
      {
        const int ln = fresh_lane(), g4 = ln >> 4, tp = ln & 3, k = 8 * g4 + ((ln & 15) >> 2);
        sad = lbase + (kcap % NST) * STB + ABYTES + k * 384 + (((2 * (tp >> 1)) ^ (4 * ((k >> 1) & 1)) ^ (g4 & 1)) << 4) + 8 * (tp & 1) + (hw % 3) * 128;
      }
      wsum = __uint_as_float(__builtin_amdgcn_readfirstlane(*LDS_AT(const volatile unsigned, lbase + FLG + 60)));
      asm volatile("" : "+v"(sad), "=v"(tw), "=v"(alpha), "=v"(m));
      PIN();
      const int row = m0 + (t & 127);
      const __amdgpu_buffer_rsrc_t rsg = __builtin_amdgcn_make_buffer_rsrc(hand, 0, GRAN_N * 4, 0x00020000);
      if (hw < 2 || tmi == tni) {
      while (nb < HS) tickc();
#pragma unroll 1
      for (int spin = 0; spin < SPIN_MAX; ++spin) {
        const unsigned v = ld_ag(gran + GR_X + (tmi * RX + tni % RX) * 128 + lane);
        tickc();
        if (none(v != tag)) break;
      }
      asm volatile("" ::: "memory");
      PIN();
      const double s1 = ld_ag(stats + row), s2 = ld_ag(stats + S_ + row), sw = ld_ag(stats + 2 * S_ + row);
      tickc();
      float mean, var, mb, den;
      ln_from_sums(s1, s2, mean, var, mb, den);
      const float rin = frcp(den), g1 = 1.0f + rin;
      const float mb2 = rbf(mean + (mean - mb) * rin);
      const float den2 = rbf(__builtin_amdgcn_sqrtf(rbf(rbf(var * g1 * g1) + 1e-5f)));
      alpha = g1 * frcp(den2); m = (mb * rin + mb2) * frcp(g1);
      if (tmi == tni && hw >= 2) {
        const float r = (float)fma(-(double)mb, (double)wsum, sw) * rin;
        const unsigned rb = pack2(r, r);
        unsigned rkey = (rb ^ ((unsigned)((int)rb >> 31) | 0x80000000u)) & 0xffff0000u;
        if (r != r) rkey = 0u;
        rkey = wave_umax(rkey | (unsigned)(2047 - row));
        const unsigned md = __builtin_amdgcn_readlane(f2word(__uint_as_float((__float_as_uint(mb) >> 16) | (__float_as_uint(den) & 0xffff0000u))), (2047 - (int)(rkey & 0xffffu)) & 63);
        __builtin_amdgcn_raw_buffer_store_b64(u32x2{rkey, md}, rsg, lane < NREP ? lane * 256 + (tmi * 2 + hw - 2) * 8 : GRAN_N * 4, 0, 16);
      }
      }
      while (nb < KPOLL) tickc();
#pragma unroll 1
      for (int spin = 0; spin < SPIN_MAX; ++spin) {
        const u32x2 v = __builtin_amdgcn_raw_buffer_load_b64(rsg, (bid % NREP) * 256 + (lane & 31) * 8, 0, 16);
        tick(); tick();
        if (none(umax(v[0], v[1]) == SENT)) {
          best = wave_umax(v[0]);
          rmd = __builtin_amdgcn_readlane(v[1], ((2047 - (int)(best & 0xffffu)) >> 6) & 31);
          break;
        }
      }
      const int ix = (2047 - (int)(best & 0xffffu)) & 2047;
      if (row == ix) alpha = 0.f;
      xq = __builtin_amdgcn_raw_buffer_load_b64(__builtin_amdgcn_make_buffer_rsrc(const_cast<u16*>(A), 0, S_ * D_ * 2, 0x00020000), (ix * D_ + hw * 256 + (lane & 3) * 64 + (lane >> 2) * 4) * 2, 0, 16);
      PIN();
    }
    tag = __builtin_amdgcn_readfirstlane(tag);
    wsum = __uint_as_float(__builtin_amdgcn_readfirstlane(__float_as_uint(wsum)));
    const int lane = fresh_lane(), t = hw * 64 + lane;
    {
      unsigned a0, a1, a2;
      split3(-m, a0, a1, a2);
      u32x4 ta = {a0 | (a0 << 16), a1 | (a0 << 16), a1 | (a2 << 16), 0u};
      asm volatile("" : "+v"(tw), "+v"(ta), "+v"(alpha));
      __builtin_amdgcn_s_setprio(TPRIO);
      if (nb < lim) {
        while (nb < lim) tick();
        __builtin_amdgcn_s_sleep(TSLP);
      }
      if (!LAY2 && hw < 3) *LDS_AT(u32x4, lbase + TABW + (hw * 64 + (lane & 15) * 4 + (lane >> 4)) * 16) = tw;
      if (t < 128) {
        *LDS_AT(u32x4, lbase + TABA + t * 16) = ta;
        *LDS_AT(float, lbase + ALPHA + t * 4) = alpha;
      }
    }
    if (t >= 192 && t < 196) *LDS_AT(u32x4, lbase + CRED + ((t >> 1) & 1) * 512 + (t & 1) * 128) = u32x4{0u, 0u, 0u, 0u};
    asm volatile("s_waitcnt lgkmcnt(0)" ::: "memory");
    PIN();
    __builtin_amdgcn_s_barrier();
    PIN();
    *LDS_AT(u32x4, lbase + XML + t * 16) = u32x4{SENT, SENT, SENT, SENT};
    *LDS_AT(u32x4, lbase + SPM + (t & 127) * 16) = u32x4{SENT, SENT, SENT, SENT};
    if (hw == 0) {
      unsigned v;
      asm volatile("v_mov_b32 %0, 0\n\tv_writelane_b32 %0, 2, 0\n\tv_writelane_b32 %0, %1, 5\n\tv_writelane_b32 %0, %2, 7"
                   : "=&v"(v) : "s"(tag), "s"(best));
      lds_wr(lbase + FLG + lane * 4, v);
    }
    const int idx = LAY2 ? (2047 - (int)(best & 0xffffu)) & 2047 : -64;
    const bool p1blk = (oni & 7) != 7, fast = tmi < 8, own = fast && (loc & 1);
    const int fg = xcd * 28 + (p1blk ? loc - (loc >> 3) : 0), ow = (xcd & 3) * 16 + (loc >> 1);
    static_assert(OMX == 4, "oni % 8 = loc % 8");
#define GLD(a, b) "global_load_dwordx4 v[" #a ":" #b "], %0, off\n\tv_lshl_add_u64 %0, %0, 0, %1\n\t"
#define GLD4(...) asm volatile(__VA_ARGS__ : "+v"(p) : "s"((unsigned long long)(F_ * 2)), KEPT() : "memory")
    auto gptr = [&](const u16* wg, const u16* wu) {
      const int ln = fresh_lane();
      return ((ln & 2) ? wu : wg) + (size_t)(hw * 256 + (ln >> 2) * 4) * F_ + fg * 16 + (ln & 1) * 8;
    };
    auto gather = [&](const u16*& p, int e) __attribute__((always_inline)) {
      if (e == 0) GLD4(GLD(104, 107) GLD(108, 111) GLD(112, 115) GLD(116, 119));
      else if (e == 1) GLD4(GLD(120, 123) GLD(124, 127) GLD(128, 131) GLD(132, 135));
      else if (e == 2) GLD4(GLD(136, 139) GLD(140, 143) GLD(144, 147) GLD(148, 151));
      else GLD4(GLD(152, 155) GLD(156, 159) GLD(160, 163) GLD(164, 167));
      p += 60 * F_;
    };
    auto dmask = [&](int q) { const int ln = fresh_lane(); return ((ln >> 1) & 1) == q ? ((ln & 1) ? 0xffff0000u : 0x0000ffffu) : 0u; };
    unsigned dm0, dm1, dad;
    auto gu_setup = [&]() __attribute__((always_inline)) {
      const int ln = fresh_lane();
      dm0 = dmask(0); dm1 = dmask(1);
      dad = lbase + SACT + ((ln & 3) * 8 + (ln >> 5) * 4 + ((ln >> 4) & 1)) * 16 + hw * 4;
      asm volatile("" : "+v"(dm0), "+v"(dm1), "+v"(dad));
    };
    auto gate_up = [&](u32x2 x, unsigned md) __attribute__((always_inline)) {
      const unsigned rin = __builtin_amdgcn_readfirstlane(__float_as_uint(frcp(__uint_as_float(md & 0xffff0000u))));
      asm volatile("s_getpc_b64 s[90:91]\n\ts_add_u32 s90, s90, gate_up_code@rel32@lo+4\n\ts_addc_u32 s91, s91, gate_up_code@rel32@hi+12\n\ts_swappc_b64 s[92:93], s[90:91]"
                   : "+{v[100:101]}"(x) : "{s84}"(md << 16), "{s85}"(md & 0xffff0000u), "{s86}"(rin), "{v99}"(dad), "{v102}"(dm0), "{v103}"(dm1), KEPT()
                   : "v84", "v85", "v86", "v87", "v88", "v89", "v90", "v91", "v92", "v93", "v94", "v95", "v96", "v97", "s88", "s89", "s90", "s91", "s92", "s93", "scc", "memory");
    };
    auto act_diag = [&](int so = 0) __attribute__((always_inline)) {
      const int ln = fresh_lane();
      const f32x4 g4 = *LDS_AT(const volatile f32x4, lbase + SACT + so + (ln >> 2) * 16), u4 = *LDS_AT(const volatile f32x4, lbase + SACT + so + 256 + (ln >> 2) * 16);
      float gv = fadd(fadd(g4[0], g4[1]), fadd(g4[2], g4[3])), uv = fadd(fadd(u4[0], u4[1]), fadd(u4[2], u4[3]));
      rbf2(gv, uv);
      const float av = rbf(gv * frcp(1.0f + __expf(-gv))) * uv;
      const unsigned ap2 = pack2(av, av);
      return __builtin_bit_cast(s16x4, u32x2{ap2 & dm0, ap2 & dm1});
    };
    auto down = [&](const s16x4& a, const u32x2 (&w)[16]) __attribute__((always_inline)) {
      f32x4 d[2] = {{0.f, 0.f, 0.f, 0.f}, {0.f, 0.f, 0.f, 0.f}};
      static_for<0, 16>([&](auto fc) __attribute__((always_inline)) {
        constexpr int f = decltype(fc)::value;
        d[f & 1] = __builtin_amdgcn_mfma_f32_4x4x4bf16_1k(a, __builtin_bit_cast(s16x4, w[f]), d[f & 1], 4, f, 0);
      });
      f32x4 z;
#pragma unroll
      for (int e = 0; e < 4; ++e) { z[e] = d[0][e] + d[1][e]; asm volatile("" : "+v"(z[e])); }
      return u32x4{f2word(z[0]), f2word(z[1]), f2word(z[2]), f2word(z[3])};
    };
    auto fold8 = [&](f32x2 y0, f32x2 y1) __attribute__((always_inline)) { return dpp_add<0x128, 0xf>(fold16(y0, y1)); };
    auto fold4 = [&](f32x2 y0, f32x2 y1) __attribute__((always_inline)) { return dpp_add<0x124, 0xf>(fold8(y0, y1)); };
    auto y_sum = [&](const __amdgpu_buffer_rsrc_t rs, auto nbc) __attribute__((always_inline)) {
      constexpr int NB = decltype(nbc)::value, N = 14 / NB;
      const int ln = fresh_lane(), src = (GRAN_N + (ln >> 2) * D_ + ow * 16 + (ln & 3) * 4) * 4;
      f32x2 y0 = {0.f, 0.f}, y1 = {0.f, 0.f};
#pragma unroll 1
      for (int bt = 0; bt < NB; ++bt) {
        f32x2 z0, z1;
#pragma unroll 1
        for (int spin = 0; spin < SPIN_MAX; ++spin) {
          u32x4 v[N];
#pragma unroll
          for (int i = 0; i < N; ++i) v[i] = __builtin_amdgcn_raw_buffer_load_b128(rs, src, (bt * N + i) * (16 * D_ * 4), 16);
          z0 = y0; z1 = y1;
#pragma unroll
          for (int i = 0; i < N; ++i) {
            z0 = f32x2{fadd(z0[0], __uint_as_float(v[i][0])), fadd(z0[1], __uint_as_float(v[i][1]))};
            z1 = f32x2{fadd(z1[0], __uint_as_float(v[i][2])), fadd(z1[1], __uint_as_float(v[i][3]))};
          }
          const float chk = fadd(fadd(z0[0], z0[1]), fadd(z1[0], z1[1]));
          if (none(chk != chk)) break;
          unsigned mx = 0u;
#pragma unroll
          for (int i = 0; i < N; ++i) mx = umax(umax(umax(mx, v[i][0]), v[i][1]), umax(v[i][2], v[i][3]));
          if (none(mx == SENT)) break;
          __builtin_amdgcn_s_sleep(YSLP);
          asm volatile("" ::: "memory");
        }
        y0 = z0; y1 = z1;
      }
      return fold4(y0, y1);
    };
    [&]() __attribute__((always_inline)) {
    ARGS_HERE;
    const unsigned l16 = lane * 16;
    unsigned* kvflag = gran + GR_KV + ((LAY2 ? 16 : 0) + tni) * (32 * RK);
    auto wait_sig = [&](int sh) {
#pragma unroll 1
      for (int i = 0; i < 4 * SPIN_MAX; ++i) {
        if (((__builtin_amdgcn_readfirstlane(*LDS_AT(const volatile unsigned, lbase + FLG + 32)) >> sh) & 7u) >= 4u) break;
        __builtin_amdgcn_s_sleep(SSLP);
      }
      asm volatile("" ::: "memory");
      PIN();
    };
    PIN();
    asm volatile("s_waitcnt lgkmcnt(0)" ::: "memory");
    if (!LAY2) asm volatile("s_waitcnt vmcnt(0)" ::: "memory");
    __builtin_amdgcn_s_barrier();
    PIN();
    __builtin_amdgcn_s_setprio(HPRIO);
    if (hw < 3) {
      __builtin_amdgcn_s_sleep(SPRE);
      wait_sig(0);
      __builtin_amdgcn_raw_buffer_store_b32(tag, __builtin_amdgcn_make_buffer_rsrc(kvflag, 0, hw == 0 ? RK * 128 : 0, 0x00020000), lane * 128 + tmi * 4, 0, 16);
      if (hw == 0) STAMP(LAY2 ? 5 : 1, 16);
    }
    if (hw == 3) {
      __builtin_amdgcn_s_sleep(KVS);
      const unsigned kv0 = ld_ag(kvflag + (tmi % RK) * 32 + (lane & 15));
      PIN();
      const char* cnt = reinterpret_cast<const char*>(gran + GR_N + bid * 64 + 2);
      unsigned vk[8], vp = lbase + FLG + 8 + (lane & 7), pw = 0u;
#pragma unroll
      for (int j = 0; j < 8; ++j) { vk[j] = l16 + (j >> 2) * 131072u + ((j >> 1) & 1) * 4096u + (j & 1) * (VOFF - 2048u); asm volatile("" : "+v"(vk[j])); }
#define G4(a, b, c, d, sc) "s_nop 0\n\tglobal_load_lds_dwordx4 %" #a ", %8" sc "\n\tglobal_load_lds_dwordx4 %" #b ", %8 offset:1024" sc "\n\t"             \
                       "global_load_lds_dwordx4 %" #c ", %8 offset:2048" sc "\n\tglobal_load_lds_dwordx4 %" #d ", %8 offset:3072" sc "\n\t"
#define M4 "s_addk_i32 m0, 0x1000\n\t"
      auto stage = [&](const char* sp, unsigned dst) __attribute__((always_inline)) {
        asm volatile("s_mov_b32 m0, %9\n\t" G4(0, 0, 1, 1, " sc1") M4 G4(2, 2, 3, 3, " sc1") M4 G4(4, 4, 5, 5, " sc1") M4 G4(6, 6, 7, 7, " sc1")
                     :: "v"(vk[0]), "v"(vk[1]), "v"(vk[2]), "v"(vk[3]), "v"(vk[4]), "v"(vk[5]), "v"(vk[6]), "v"(vk[7]), "s"(sp), "s"(dst) : "m0", "scc", "memory");
      };
      auto count = [&]() __attribute__((always_inline)) {
        asm volatile("s_mov_b32 m0, %2\n\ts_nop 0\n\tglobal_load_lds_dword %0, %1 sc1" :: "v"(0u), "s"(cnt), "s"(lbase + FLG - 252) : "m0", "memory");
        cnt += 8;
      };
      auto freed = [&](unsigned n) __attribute__((always_inline)) {
        if (!none(pw < n)) {
#pragma unroll 1
          for (int i = 0; i < 4 * SPIN_MAX; ++i) {
            pw = *LDS_AT(const volatile unsigned char, vp);
            if (none(pw < n)) break;
            __builtin_amdgcn_s_sleep(W3SLP);
          }
        }
        asm volatile("" ::: "memory");
      };
      unsigned long long ord = (0xfedcba9876543210ull >> 4 & ~((1ull << 4 * (tmi & 7)) - 1ull)) | (0xfedcba9876543210ull & ((1ull << 4 * (tmi & 7)) - 1ull));
      ord ^= (tmi & 8) * 0x1111111111111111ull;
      unsigned seen;
      const char* kvb = reinterpret_cast<const char*>(KV) + (size_t)tni * (128 * 2048);
      auto panel = [&]() __attribute__((always_inline)) {
        const unsigned pn = (unsigned)ord & 15u;
        ord >>= 4;
        if (__builtin_expect(!((seen >> pn) & 1u), 0)) {
#pragma unroll 1
          for (int i = 0; i < SPIN_MAX && !((seen >> pn) & 1u); ++i) seen = (unsigned)__ballot(ld_ag(kvflag + (tmi % RK) * 32 + (lane & 15)) == tag);
        }
        asm volatile("" ::: "memory");
        return kvb + pn * 8192u;
      };
      asm volatile("" : "+s"(ord), "+s"(kvb), "+s"(cnt));
      asm volatile("" : "+v"(vp), "+v"(pw));
      PIN();
      seen = (unsigned)__ballot(kv0 == tag);
      const char* sp = panel();
      wait_sig(16);
      stage(sp, lbase + 2 * ASB);
      count();
      const int ksp = LAY2 ? PNS / 2 : -1, ksg = LAY2 ? 64 : GF0;
      const u16* gp = gptr(wg, wu);
#pragma unroll 1
      for (int k = 1; k < 15; ++k) {
        if (__builtin_expect(k == ksp, 0)) {
          gu_setup();
          gate_up(xq, rmd);
          PIN();
          *LDS_AT(volatile unsigned char, lbase + FLG + 7) = (unsigned char)0x7f;
        }
        freed((unsigned)(0xba9876543211000ull >> 4 * k) & 15u);
        stage(sp + 2048, lbase + ((2 * k + 1) & 7) * ASB);
        if (__builtin_expect(k == 1, 0)) { cnt -= 4; count(); cnt -= 4; }
        sp = panel();
        stage(sp, lbase + ((2 * k + 2) & 7) * ASB);
        count();
        if (__builtin_expect((unsigned)(k - ksg) < 4u, 0)) gather(gp, k - ksg);
      }
      freed(12u);
      glds16s<true>(csf + 4096 + tni * 128, l16, lbase + UGL);
      stage(sp + 2048, lbase + 7 * ASB);
      count();
      const unsigned vwo = (unsigned)((lane >> 3) * D_ + oni * 64 + ((lane & 7) ^ ((lane >> 3) & 6)) * 8) * 2u;
      constexpr unsigned vs = 8u * D_ * 2u - 1024u;
#pragma unroll 1
      for (int ns = 0; ns < 6; ++ns) {
        freed((unsigned)(ns + 25) >> 1);
        const char* wp = reinterpret_cast<const char*>(wo) + (size_t)ns * (128 * D_ * 2);
#pragma unroll
        for (int j = 0; j < 4; ++j)
          glds16x4<false>(wp + j * (32u * D_ * 2u), vwo, vwo + vs, vwo + 2 * vs, vwo + 3 * vs, lbase + ns * ASB + j * 4096);
      }
      asm volatile("s_waitcnt vmcnt(0)" ::: "memory");
      return;
    }
    if (!LAY2) {
      auto until = [&](unsigned n) {
        while ((__builtin_amdgcn_readfirstlane(*LDS_AT(const volatile unsigned, lbase + FLG + 8)) & 255u) < n) __builtin_amdgcn_s_sleep(16);
        asm volatile("" ::: "memory");
        PIN();
      };
      if (hw != 1) {
        constexpr int N2 = HAND_N + Y_N, X3N = 3 * D_ / 2, N0 = X3N + PS_N;
        const __amdgpu_buffer_rsrc_t rsi = __builtin_amdgcn_make_buffer_rsrc(hand + (hw ? GRAN_N : 2 * HAND_N - X3N), 0, (hw ? N2 : N0) * 4, 0x00020000);
        until(IAT);
#pragma unroll 1
        for (int i = 0; i < ((hw ? N2 : N0) / 4 + 16383) / 16384; ++i) {
          __builtin_amdgcn_raw_buffer_store_b128(u32x4{SENT, SENT, SENT, SENT}, rsi, ((i * 256 + bid) * 64 + lane) * 16, 0, 16);
          __builtin_amdgcn_s_sleep(ISLP);
        }
      }
      const size_t og = (size_t)((fg - xcd * 28) / 7 * 256 + lane) * F_ + (xcd * 7 + fg % 7) * 64;
      const u16* p = gptr(wg, wu);
#pragma unroll 1
      for (unsigned n = T0; n < T0 + 13u; ++n) {
        until(n);
        if (hw == 1 && n < T0 + 8u) {
          const unsigned p0 = touch((((n - T0) & 1u) ? wu : wg) + og + (size_t)((n - T0) >> 1) * (64 * F_));
          asm volatile("" :: "v"(p0));
        }
        if (hw == 2 && n == TFL) {
          const int ln = fresh_lane() & 15;
          const unsigned p1 = ld_ag(gran + (ln < 4 ? GR_O + (((ln >> 1) * 16 + omi) * RO + oni % RO) * 64 + (ln & 1) * 32 :
                                            (ln < 8 ? GR_X + (tmi * RX + tni % RX) * 128 + (ln & 3) * 32 : GR_KV + ((16 + tni) * RK + tmi % RK) * 32)));
          asm volatile("" :: "v"(p1));
        }
        if (hw == 2 && n == TW) {
          const int ln = fresh_lane();
          unsigned long long a0, a1;
          CODE_ADDR("layers_kernel", a0); CODE_ADDR("end_marker_kernel", a1);
          const unsigned p1 = touch_code(a0, a1, 3 + loc, ln), p0 = touch(wo + (size_t)(768 + (loc / (32 / OMX)) * (256 / OMX) + ln) * D_ + oni * 64);
          asm volatile("" :: "v"(p0), "v"(p1));
        }
        if (n >= G0 && (n - G0) % GS == 0u && (n - G0) / GS < 4u) gather(p, (n - G0) / GS);
        if (n == UAT && hw == UHW) {
          u32x4 a[4][2], c[2];
#pragma unroll
          for (int i = 0; i < 4; ++i)
#pragma unroll
            for (int h = 0; h < 2; ++h) a[i][h] = *reinterpret_cast<const u32x4*>(wo + (size_t)(tni * 64 + tmi * 4 + i) * D_ + h * 512 + lane * 8);
#pragma unroll
          for (int h = 0; h < 2; ++h) c[h] = *reinterpret_cast<const u32x4*>(w_r + h * 512 + lane * 8);
          auto dot = [&](unsigned x, unsigned y, float z) { return __builtin_amdgcn_fdot2_f32_bf16(__builtin_bit_cast(bf16x2, x), __builtin_bit_cast(bf16x2, y), z, false); };
          PIN();
          __builtin_amdgcn_s_setprio(0);
#pragma unroll 1
          for (int i = 0; i < SPIN_MAX; ++i) {
            if (__builtin_amdgcn_readfirstlane(*LDS_AT(const volatile unsigned, lbase + FLG + 16)) >= UARR) break;
            __builtin_amdgcn_s_sleep(4);
          }
          asm volatile("s_waitcnt vmcnt(0)" ::: "memory");
          PIN();
          float wm = 0.f;
#pragma unroll
          for (int e = 0; e < 8; ++e) wm = dot(c[e >> 2][e & 3], 0x3f803f80u, wm);
          wm = wave_sum(wm);
          lds_wr(lbase + FLG + 60, __float_as_uint(wm));
          wm *= -1.0f / 1024.0f;
          f32x2 u[2], g[2];
#pragma unroll
          for (int i = 0; i < 4; ++i) {
            float p1 = 0.f, pw = 0.f, p2 = 0.f;
#pragma unroll
            for (int e = 0; e < 8; ++e) {
              const unsigned x = a[i][e >> 2][e & 3];
              p1 = dot(x, 0x3f803f80u, p1); pw = dot(x, c[e >> 2][e & 3], pw); p2 = dot(x, x, p2);
            }
            u[i >> 1][i & 1] = fmaf(p1, wm, pw); g[i >> 1][i & 1] = p2;
          }
          float us = fold4(u[0], u[1]), gs = fold4(g[0], g[1]);
          us = dpp_add<0x121, 0xf>(dpp_add<0x122, 0xf>(us)); gs = dpp_add<0x121, 0xf>(dpp_add<0x122, 0xf>(gs));
          __builtin_amdgcn_raw_buffer_store_b32(__float_as_uint((lane & 1) ? __builtin_amdgcn_sqrtf(gs) * (1.0f / 32.0f) : us),
              __builtin_amdgcn_make_buffer_rsrc(csf + 4096 + tni * 128, 0, 512, 0x00020000), (lane & 14) ? 512 : ((lane & 1) * 64 + tmi * 4 + (lane >> 4)) * 4, 0, 16);
        }
      }
      if (hw == 2 && bid == 0) {
        const __amdgpu_buffer_rsrc_t rsc = __builtin_amdgcn_make_buffer_rsrc(gran, 0, GR_N * 4, 0x00020000);
#pragma unroll 1
        for (int spin = 0; spin < SPIN_MAX; ++spin) {
          bool ok = true;
#pragma unroll
          for (int i = 0; i < 4; ++i) ok &= __builtin_amdgcn_raw_buffer_load_b32(rsc, (GR_ROW + (i * 64 + lane) * 32 + 1) * 4, 0, 16) == tag;
          if (__all(ok)) break;
          __builtin_amdgcn_s_sleep(8);
          asm volatile("" ::: "memory");
        }
        if (lane == 0) st_ag(gran, tag);
      }
      return;
    }
    unsigned* X2 = hand + GRAN_N + Y_N;
    unsigned* XS = X2 + X2_N;
    unsigned* P4 = XS + XS_N;
    unsigned* PA = P4 + P4_N;
    const __amdgpu_buffer_rsrc_t rsh = __builtin_amdgcn_make_buffer_rsrc(hand, 0, HAND_N * 4, 0x00020000);
    auto signal = [&](unsigned ph) {
      asm volatile("" ::: "memory");
      PIN();
      *LDS_AT(volatile unsigned char, lbase + FLG + 4 + hw) = (unsigned char)ph;
    };
    auto wait_all = [&](unsigned ph) {
#pragma unroll 1
      for (int i = 0; i < SPIN_MAX; ++i) {
        if (all_ge(__builtin_amdgcn_readfirstlane(*LDS_AT(const volatile unsigned, lbase + FLG + 4)), ph)) break;
        __builtin_amdgcn_s_sleep(WSLP);
      }
      asm volatile("" ::: "memory");
      PIN();
    };
    gu_setup();
    gate_up(xq, rmd);
    signal(hw == 2 ? 2u : 0x7fu);
    if (wid == 8) STAMP(dk, 2);
    if (hw < 2) {
#pragma unroll 1
      for (int i = 0; i < SPIN_MAX; ++i) {
        if (__builtin_amdgcn_readfirstlane(*LDS_AT(const volatile unsigned, lbase + FLG)) >= FSTART) break;
        __builtin_amdgcn_s_sleep(WSLP);
      }
      asm volatile("" ::: "memory");
      PIN();
      if (wid == 8) STAMP(dk, 10);
      u32x2 w[16];
      const __amdgpu_buffer_rsrc_t rd_ = __builtin_amdgcn_make_buffer_rsrc(const_cast<u16*>(wd), 0, D_ * F_ * 2, 0x00020000);
      auto ldw = [&](int c) __attribute__((always_inline)) {
#pragma unroll
        for (int f = 0; f < 16; ++f) w[f] = __builtin_amdgcn_raw_buffer_load_b64(rd_, lane * 8 + c * 512 + (f & 1) * 2048, (fg * 16 + (f & ~1)) * (D_ * 2) + hw * 1024, 0);
        PIN();
      };
      ldw(0);
      const unsigned pt = __builtin_amdgcn_raw_buffer_load_b32(rd_, (lane >> 2) * (D_ * 2) + (lane & 3) * 128 + 512, fg * 16 * (D_ * 2) + hw * 1024, 0);
      int yo = p1blk ? (GRAN_N + fg * D_ + hw * 512) * 4 + lane * 16 : HAND_N * 4;
      asm volatile("" : "+v"(yo));
      wait_all(2u);
      if (wid == 8) STAMP(dk, 3);
      const s16x4 a = act_diag();
      const u32x4 y0 = down(a, w);
      PIN();
      ldw(1);
      __builtin_amdgcn_raw_buffer_store_b128(y0, rsh, yo, 0, 16);
      PIN();
      __builtin_amdgcn_raw_buffer_store_b128(down(a, w), rsh, yo + 1024, 0, 16);
      asm volatile("" :: "v"(pt));
      if (wid == 8) STAMP(dk, 4);
    }
    PIN();
    [&]() __attribute__((always_inline)) {
    const int lane = fresh_lane();
    const float rmb = __uint_as_float(rmd << 16);
    auto first_words = [&](int src, auto nc) __attribute__((always_inline)) {
#pragma unroll 1
      for (int spin = 0; spin < SPIN_MAX; ++spin) {
        unsigned mx = 0u;
#pragma unroll
        for (int i = 0; i < decltype(nc)::value; ++i) mx = umax(mx, __builtin_amdgcn_raw_buffer_load_b32(rsh, src, i * (56 * D_ * 4), 16));
        if (none(mx == SENT)) break;
        __builtin_amdgcn_s_sleep(PSLP);
        asm volatile("" ::: "memory");
      }
    };
    if (hw == 2) {
      wait_all(2u);
      if (own) {
        const float rden = __uint_as_float(rmd & 0xffff0000u), rrin = frcp(rden);
        const int col = ow * 16 + (lane & 3) * 4 + (lane >> 4);
        const unsigned xo2 = ld_ag(reinterpret_cast<const unsigned*>(A + (size_t)idx * D_ + (col & ~1)));
        __builtin_amdgcn_s_sleep(PRE1);
        first_words((GRAN_N + (lane < 56 ? lane : 0) * D_ + ow * 16) * 4, std::integral_constant<int, 4>{});
        const float y = y_sum(rsh, std::integral_constant<int, 2>{});
        const unsigned rk = best >> 16;
        const float rv = __uint_as_float(((rk & 0x8000u) ? (rk & 0x7fffu) : (~rk & 0xffffu)) << 16);
        const float rs = rbf(frcp(1.0f + __expf(-rv)));
        const float x1v = __uint_as_float((col & 1) ? xo2 & 0xffff0000u : xo2 << 16);
        float h0 = x1v, h1 = x1v;
        ln_apply2(h0, h1, rmb, rden, rrin);
        const float x2v = rbf(x1v + rbf(h0 + rbf(rbf(y) * rs)));
        const auto pr = __builtin_amdgcn_permlane16_swap(__float_as_uint(x2v), __float_as_uint(x2v), false, false);
        __builtin_amdgcn_raw_buffer_store_b32(f2word(__uint_as_float((pr[0] >> 16) | pr[1])), rsh, (lane & 28) ? HAND_N * 4 : (int)(X2 - hand) * 4 + col * 2, 0, 16);
        const float d = x2v - rmb;
        const float sd = wave_sum(d), sq = wave_sum(d * d);
        if (lane < 2) st_ag(XS + ow * 2 + lane, f2word(lane ? sq : sd));
        STAMP(dk, 5);
      }
    }
    {
      const int ks = kcap ^ (tmi & 8), g4 = lane >> 4;
      const int src = (int)(X2 - hand) * 4 + ks * 128 + g4 * 16;
      int dst = (lane & 12) ? HAND_N * 4 : (int)((P4 - hand) + (tni * 16 + ks) * 192 + hw * 64 + (lane & 2) * 16 + (lane & 1) * 8 + (g4 >> 1) * 16 + (g4 & 1) * 4) * 4;
      asm volatile("" : "+v"(dst));
      u32x4 x0, x1;
      if (!(hw == 2 && own)) __builtin_amdgcn_s_sleep(PRE4);
#pragma unroll 1
      for (int spin = 0; spin < SPIN_MAX; ++spin) {
        x0 = __builtin_amdgcn_raw_buffer_load_b128(rsh, src, 0, 16); x1 = __builtin_amdgcn_raw_buffer_load_b128(rsh, src + 64, 0, 16);
        if (none(umax(umax(umax(x0[0], x0[1]), umax(x0[2], x0[3])), umax(umax(x1[0], x1[1]), umax(x1[2], x1[3]))) == SENT)) break;
        __builtin_amdgcn_s_sleep(PSLP);
        asm volatile("" ::: "memory");
      }
      f32x4 d[4];
#pragma unroll
      for (int i = 0; i < 4; ++i) d[i] = MFMA16(sl[2 * i], __builtin_bit_cast(s16x8, x0), (f32x4{0.f, 0.f, 0.f, 0.f}));
#pragma unroll
      for (int i = 0; i < 4; ++i) d[i] = MFMA16(sl[2 * i + 1], __builtin_bit_cast(s16x8, x1), d[i]);
      u32x4 o;
#pragma unroll
      for (int e = 0; e < 4; ++e) o[e] = f2word((lane & 2) ? ((lane & 1) ? d[3][e] : d[2][e]) : ((lane & 1) ? d[1][e] : d[0][e]));
      __builtin_amdgcn_raw_buffer_store_b128(o, rsh, dst, 0, 16);
      PIN();
      if (wid == 8) STAMP(dk, 6);
    }
    if (hw == 2) {
      const int col = (lane & 15) * 4 + (lane >> 4), src = (int)((P4 - hand) + (tni * 16 + (lane >> 4)) * 192 + (lane & 15) * 4) * 4;
      const __amdgpu_buffer_rsrc_t rsq = __builtin_amdgcn_make_buffer_rsrc(hand, 0, fast ? HAND_N * 4 : 0, 0x00020000);
      if (own) {
#pragma unroll
        for (int i = 0; i < 14; ++i)
          __builtin_amdgcn_raw_buffer_store_b128(u32x4{SENT, SENT, SENT, SENT}, rsh, (GRAN_N + (i * 16 + (lane >> 2)) * D_ + ow * 16 + (lane & 3) * 4) * 4, 0, 16);
      }
      float cs[3], val[3];
#pragma unroll
      for (int p = 0; p < 3; ++p) cs[p] = __uint_as_float(ld_ag(reinterpret_cast<const unsigned*>(csf) + p * 1024 + n0 + col));
      u32x2 xs;
      __builtin_amdgcn_s_sleep(PRE7);
      first_words((int)((P4 - hand) + (tni * 16 + (lane & 15)) * 192 + ((lane >> 4) % 3) * 64) * 4, std::integral_constant<int, 1>{});
#pragma unroll 1
      for (int spin = 0; spin < SPIN_MAX; ++spin) {
        u32x4 v[3][4];
        xs = __builtin_amdgcn_raw_buffer_load_b64(rsh, (int)((XS - hand) + lane * 2) * 4, 0, 16);
#pragma unroll
        for (int p = 2; p >= 0; --p)
#pragma unroll
          for (int i = 0; i < 4; ++i) v[p][i] = __builtin_amdgcn_raw_buffer_load_b128(p ? rsh : rsq, src + p * 256, i * (4 * 192 * 4), 16);
        auto part = [&](int p) __attribute__((always_inline)) {
          float z[4];
#pragma unroll
          for (int e = 0; e < 4; ++e) {
            z[e] = fadd(fadd(__uint_as_float(v[p][0][e]), __uint_as_float(v[p][1][e])), fadd(__uint_as_float(v[p][2][e]), __uint_as_float(v[p][3][e])));
          }
          val[p] = fold16(f32x2{z[0], z[1]}, f32x2{z[2], z[3]});
        };
        part(2); part(1);
        val[0] = 0.f;
        if (fast) part(0);
        const float chk = fadd(fadd(fadd(val[0], val[1]), fadd(val[2], __uint_as_float(xs[0]))), __uint_as_float(xs[1]));
        if (none(chk != chk)) break;
        unsigned mx = umax(xs[0], xs[1]);
#pragma unroll
        for (int i = 0; i < 12; ++i) mx = umax(umax(umax(mx, v[i >> 2][i & 3][0]), v[i >> 2][i & 3][1]), umax(v[i >> 2][i & 3][2], v[i >> 2][i & 3][3]));
        if (none(mx == SENT)) break;
        __builtin_amdgcn_s_sleep(PSLP);
        asm volatile("" ::: "memory");
      }
      const auto sw = __builtin_amdgcn_permlane32_swap(xs[0], xs[1], false, false);
      float sm = __uint_as_float(sw[0]) + __uint_as_float(sw[1]);
      sm = dpp_add<0x142, 0xa>(dpp_add<0x118, 0xf>(dpp_add<0x114, 0xf>(dpp_add<0x112, 0xf>(dpp_add<0x111, 0xf>(sm)))));
      const float dm = __int_as_float(__builtin_amdgcn_readlane(__float_as_int(sm), 31)) * (0.25f / 1024.0f);
      const float var = fmaxf(__int_as_float(__builtin_amdgcn_readlane(__float_as_int(sm), 63)) * (0.25f / 1024.0f) - dm * dm, 0.f);
      const float mb = rbf(rmb + dm);
      const float rin = frcp(rbf(__builtin_amdgcn_sqrtf(rbf(rbf(var) + 1e-5f))));
      const unsigned kv = pack2((val[1] - mb * cs[1]) * rin, (val[2] - mb * cs[2]) * rin);
      *LDS_AT(u16, lbase + TAB + 128 + kpos(col) * 2) = (u16)kv;
      *LDS_AT(u16, lbase + TAB + 256 + col * 2) = (u16)(kv >> 16);
      if (fast) *LDS_AT(u16, lbase + TAB + kpos(col) * 2) = f2bf((val[0] - mb * cs[0]) * rin * QSCALE);
      signal(7u);
      STAMP(dk, 7);
      return;
    }
    if (!fast) return;
    const int head = tni * 2 + hw;
    const unsigned tb = lbase + TAB + hw * 64;
    {
      const __amdgpu_buffer_rsrc_t rs = __builtin_amdgcn_make_buffer_rsrc(KV, 0, 2 * S_ * D_ * 2, 0x00020000);
      const int src = (head * 64 + tmi * 4) * 2048 + lane * 16;
      auto ld = [&](int i, unsigned off) { return __builtin_bit_cast(s16x8, __builtin_amdgcn_raw_buffer_load_b128(rs, src + (i & 3) * 1024, (i >> 3) * 65536 + ((i >> 2) & 1) * 4096 + off, 16)); };
#pragma unroll 1
      for (int i = 0; i < SPIN_MAX; ++i)
        if (__builtin_amdgcn_readfirstlane(ld_ag(kvflag + (tmi % RK) * 32 + tmi + 8)) == tag) break;
      asm volatile("" ::: "memory");
      s16x8 f[16];
#pragma unroll
      for (int i = 0; i < 16; ++i) f[i] = ld(i, 0u);
      PIN();
      const bool on = ((lane ^ (lane >> 4)) & 1) == 0;
      const unsigned pl = lbase + RED + hw * 1024;
      unsigned pw = pl + (lane >> 4) * 64 + (lane & 15) * 2, pr = pl + (on ? (lane >> 5) * 16 : 512), qr = on ? tb + (lane >> 5) * 16 : pl + 512;
      const int gone = (((idx >> 7) & 7) == tmi) ? ((idx >> 10) * 2 + (kpos(idx & 31) >> 4)) * 64 + ((idx >> 5) & 3) * 16 + (kpos(idx & 31) & 15) : -1;
      asm volatile("" : "+v"(pw), "+v"(pr), "+v"(qr));
      *LDS_AT(u32x2, pl + 512 + lane * 8) = u32x2{0u, 0u};
      wait_all(7u);
      u32x4 q[2];
#pragma unroll
      for (int sx = 0; sx < 2; ++sx) {
        q[sx] = *LDS_AT(const u32x4, qr + sx * 32);
      }
      float sc[4];
#pragma unroll
      for (int i = 0; i < 8; ++i) {
        f32x4 d = MFMA16(__builtin_bit_cast(s16x8, q[0]), f[2 * i], (f32x4{0.f, 0.f, 0.f, 0.f}));
        d = MFMA16(__builtin_bit_cast(s16x8, q[1]), f[2 * i + 1], d);
        PIN();
        f[2 * i] = ld(2 * i, VOFF); f[2 * i + 1] = ld(2 * i + 1, VOFF);
        PIN();
#pragma unroll
        for (int r = 0; r < 2; ++r) sc[(i >> 2) * 2 + r] = (i & 3) == 0 || (lane >> 4) == (i & 3) ? d[r] : sc[(i >> 2) * 2 + r];
      }
      if (gone >= 0) {
#pragma unroll
        for (int r = 0; r < 4; ++r) sc[r] = r * 64 + lane == gone ? -INFINITY : sc[r];
      }
      const float mx = wave_fmax(fmaxf(fmaxf(sc[0], sc[1]), fmaxf(sc[2], sc[3])));
      const unsigned p0 = pack2(__builtin_amdgcn_exp2f(sc[0] - mx), __builtin_amdgcn_exp2f(sc[1] - mx)), p1 = pack2(__builtin_amdgcn_exp2f(sc[2] - mx), __builtin_amdgcn_exp2f(sc[3] - mx));
      *LDS_AT(u16, pw) = (u16)p0; *LDS_AT(u16, pw + 32) = (u16)(p0 >> 16);
      *LDS_AT(u16, pw + 256) = (u16)p1; *LDS_AT(u16, pw + 288) = (u16)(p1 >> 16);
      asm volatile("" ::: "memory");
      const float ls = wave_sum((__uint_as_float(p0 << 16) + __uint_as_float(p0 & 0xffff0000u)) + (__uint_as_float(p1 << 16) + __uint_as_float(p1 & 0xffff0000u)));
      f32x4 o = {0.f, 0.f, 0.f, 0.f};
#pragma unroll
      for (int i = 0; i < 16; ++i) o = MFMA16(*LDS_AT(const s16x8, pr + i * 32), f[i], o);
      int pv = __float_as_int((lane & 16) ? o[1] : o[0]);
      asm volatile("s_nop 0\n\tv_writelane_b32 %0, %1, 32\n\tv_writelane_b32 %0, %2, 33" : "+v"(pv) : "s"(ls), "s"(mx));
      __builtin_amdgcn_raw_buffer_store_b32(f2word(__int_as_float(pv)), rsh, lane < 34 ? (int)((PA - hand) + (head * 8 + tmi) * 64 + lane) * 4 : HAND_N * 4, 0, 16);
    }
    if (wid == 8) STAMP(dk, 8);
    if (tmi != ((idx >> 7) & 7)) return;
    {
      const int src = (int)((PA - hand) + head * 512 + (lane < 34 ? lane : 0)) * 4;
      const unsigned ta = tb + (lane & 31) * 2;
      const float so = wave_sum(lane < 32 ? bf2f(*LDS_AT(const u16, ta)) * bf2f(*LDS_AT(const u16, ta + 128)) : 0.f);
      const float vo = lane < 32 ? bf2f(*LDS_AT(const u16, ta + 256)) : 1.0f;
      unsigned v[8];
      __builtin_amdgcn_s_sleep(PRE5);
#pragma unroll 1
      for (int spin = 0; spin < SPIN_MAX; ++spin) {
        unsigned mx = 0u;
#pragma unroll
        for (int i = 0; i < 8; ++i) v[i] = __builtin_amdgcn_raw_buffer_load_b32(rsh, src + i * 256, 0, 16);
#pragma unroll
        for (int i = 0; i < 8; ++i) mx = umax(mx, v[i]);
        if (none(mx == SENT)) break;
        __builtin_amdgcn_s_sleep(PSLP);
        asm volatile("" ::: "memory");
      }
      float m8[8], mx = so;
#pragma unroll
      for (int i = 0; i < 8; ++i) { m8[i] = __int_as_float(__builtin_amdgcn_readlane((int)v[i], 33)); mx = fmaxf(mx, m8[i]); }
      float acc = __builtin_amdgcn_exp2f(so - mx) * vo;
#pragma unroll
      for (int i = 0; i < 8; ++i) acc += __builtin_amdgcn_exp2f(m8[i] - mx) * __uint_as_float(v[i]);
      const float l = __int_as_float(__builtin_amdgcn_readlane(__float_as_int(acc), 32));
      const float ov = acc / l;
      const float o1 = __int_as_float(__builtin_amdgcn_update_dpp(0, __float_as_int(ov), 0x55, 0xf, 0xf, false));
      const float o2 = __int_as_float(__builtin_amdgcn_update_dpp(0, __float_as_int(ov), 0xaa, 0xf, 0xf, false));
      const float o3 = __int_as_float(__builtin_amdgcn_update_dpp(0, __float_as_int(ov), 0xff, 0xf, 0xf, false));
      const __amdgpu_buffer_rsrc_t rso = __builtin_amdgcn_make_buffer_rsrc(Of, 0, S_ * D_ * 2, 0x00020000);
      if (lane < 32 && (lane & 3) == 0)
        __builtin_amdgcn_raw_buffer_store_b64(u32x2{pack2(ov, o1), pack2(o2, o3)}, rso,
            (((idx >> 5) * 32 + head) * 1024 + (lane >> 4) * 512 + (((lane >> 2) & 1) * 32 + (idx & 31)) * 8 + ((lane >> 3) & 1) * 4) * 2, 0, 16);
      asm volatile("s_waitcnt vmcnt(0)" ::: "memory");
      if (lane < RO) st_ag(gran + GR_O + ((16 + (idx >> 7)) * RO + lane) * 64 + 16 + head, tag);
      STAMP(dk, 9);
    }
    }();
    }();
    __builtin_amdgcn_s_setprio(0);
    if (LAY2 || hw != UHW) asm volatile("s_waitcnt vmcnt(0)" ::: "memory");
    PIN();
    ARGS_HERE;
    u32x2 w[16];
    auto ld_wd = [&]() __attribute__((always_inline)) {
      const __amdgpu_buffer_rsrc_t rd_ = __builtin_amdgcn_make_buffer_rsrc(const_cast<u16*>(wd), 0, D_ * F_ * 2, 0x00020000);
#pragma unroll
      for (int f = 0; f < 16; ++f) w[f] = __builtin_amdgcn_raw_buffer_load_b64(rd_, t * 8 + (f & 1) * 2048, (fg * 16 + (f & ~1)) * (D_ * 2), 0);
      PIN();
    };
    float fnum = 0.f, fvar = 0.f;
    const int rrow = omi * 128 + (oni >> 3) * 64 + (hw & 1) * 32;
    if (LAY2) {
      ld_wd();
      if (!p1blk) {
        const int fr = rrow + (lane & 7) * 4 + (lane >> 4);
        const double s1 = ld_ag(stats + fr), s2 = ld_ag(stats + S_ + fr), sw = ld_ag(stats + 2 * S_ + fr);
        lds_wr(lbase + SPM + 132, __float_as_uint(wsum));
        float mean, var, mb, den;
        ln_from_sums(s1, s2, mean, var, mb, den);
        const float g1 = 1.0f + frcp(den);
        fnum = fr == idx ? __uint_as_float(0x7fc00000u) : fmaf(-mean, wsum, (float)sw) * g1;
        fvar = var * g1 * g1;
      }
      asm volatile("" : "+v"(fnum), "+v"(fvar));
      PIN();
    }
    if (hw == 3) {
      const int nfl = (LAY2 && (idx >> 7) == omi) ? 48 : 16;
      auto pfl = (__attribute__((address_space(1))) const unsigned*)(gran + GR_O + (((LAY2 ? 16 : 0) + omi) * RO + oni % RO) * 64 + (lane < nfl ? lane : 0));
      constexpr unsigned vs = 8u * D_ * 2u - 1024u;
      unsigned vw0 = (unsigned)((lane >> 3) * D_ + oni * 64 + ((lane & 7) ^ ((lane >> 3) & 6)) * 8) * 2u, vw1 = vw0 + vs, vw2 = vw0 + 2 * vs, vw3 = vw0 + 3 * vs;
      asm volatile("" : "+v"(pfl), "+v"(vw0), "+v"(vw1), "+v"(vw2), "+v"(vw3));
      auto wait_arr = [&](unsigned n) {
#pragma unroll 1
        for (int i = 0; i < 4 * SPIN_MAX; ++i) {
          if (__builtin_amdgcn_readfirstlane(*LDS_AT(const volatile unsigned, lbase + FLG + 16)) >= n) break;
          __builtin_amdgcn_s_sleep(1);
        }
        asm volatile("" ::: "memory");
        PIN();
      };
      wait_arr(12u);
      unsigned fl;
      asm volatile("global_load_dword %0, %1, off sc1" : "=v"(fl) : "v"(pfl) : "memory");
#pragma unroll 1
      for (int i = 0; i < 4 * SPIN_MAX; ++i) {
        if (__builtin_amdgcn_readfirstlane(*LDS_AT(const volatile unsigned, lbase + FLG + 24)) >= 8u) break;
        __builtin_amdgcn_s_sleep(1);
      }
      asm volatile("" ::: "memory");
      PIN();
#pragma unroll
      for (int j = 24; j < 32; ++j)
        glds16x4<false>(reinterpret_cast<const char*>(wo) + j * (32u * D_ * 2u), vw0, vw1, vw2, vw3, lbase + j * 4096);
      unsigned keep;
      asm volatile("s_mov_b32 %0, m0\n\ts_mov_b32 m0, %3\n\ts_nop 2\n\tglobal_load_lds_dword %1, %2 sc1\n\ts_mov_b32 m0, %0"
                   : "=&s"(keep) : "v"(0u), "s"(reinterpret_cast<const char*>(gran + GR_N + bid * 64 + 39)), "s"(lbase + FLG - 252) : "memory");
      asm volatile("s_waitcnt vmcnt(33)" : "+v"(fl) :: "memory");
      if (__builtin_amdgcn_ballot_w64(fl != tag)) {
        __builtin_amdgcn_s_sleep(CSL1);
#pragma unroll 1
        for (int spin = 0; spin < SPIN_MAX; ++spin) {
          if (!__builtin_amdgcn_ballot_w64(__hip_atomic_load(pfl, __ATOMIC_RELAXED, __HIP_MEMORY_SCOPE_AGENT) != tag)) break;
          __builtin_amdgcn_s_sleep(CSLP);
        }
      }
      asm volatile("" ::: "memory");
      PIN();
    }
    u32x4 pv[4];
    asm volatile("" : "=v"(pv[0]), "=v"(pv[1]), "=v"(pv[2]), "=v"(pv[3]));
    if (LAY2 && !p1blk && hw < 2) {
      const __amdgpu_buffer_rsrc_t rsp = __builtin_amdgcn_make_buffer_rsrc(hand + 2 * HAND_N + PP_O, 0, H_ * S_ * 4, 0x00020000);
      const int src = ((lane >> 3) * S_ + rrow + (lane & 7) * 4) * 4;
#pragma unroll 1
      for (int i = 0; i < 4 * SPIN_MAX; ++i) {
        if (__builtin_amdgcn_readfirstlane(*LDS_AT(const volatile unsigned, lbase + FLG + 24)) >= 8u) break;
        __builtin_amdgcn_s_sleep(2);
      }
#pragma unroll 1
      for (int spin = 0; spin < SPIN_MAX; ++spin) {
        asm volatile("" ::: "memory");
        unsigned mx = 0u;
#pragma unroll
        for (int i = 0; i < 4; ++i) pv[i] = __builtin_amdgcn_raw_buffer_load_b128(rsp, src, i * (8 * S_ * 4), 16);
#pragma unroll
        for (int i = 0; i < 4; ++i) mx = umax(umax(umax(mx, pv[i][0]), pv[i][1]), umax(pv[i][2], pv[i][3]));
        if (none(mx == SENT)) break;
        __builtin_amdgcn_s_sleep(PPSLP);
      }
    }
    asm volatile("s_waitcnt lgkmcnt(0)" ::: "memory");
    __builtin_amdgcn_s_barrier();
    if (!LAY2) {
      PIN();
      asm volatile(AGAIN "\ngate_up_code:\n\t"
                 "v_lshlrev_b32 v84, 16, v100\n\tv_and_b32 v85, 0xffff0000, v100\n\tv_lshlrev_b32 v86, 16, v101\n\tv_and_b32 v87, 0xffff0000, v101\n\t"
                 "v_subrev_f32 v84, s84, v84\n\tv_subrev_f32 v85, s84, v85\n\tv_subrev_f32 v86, s84, v86\n\tv_subrev_f32 v87, s84, v87\n\t"
                 "v_cvt_pk_bf16_f32 v85, v84, v85\n\tv_cvt_pk_bf16_f32 v87, v86, v87\n\ts_mov_b32 s88, 0x01000100\n\ts_mov_b32 s89, 0x03020302\n\t"
                 "v_lshlrev_b32 v84, 16, v85\n\tv_and_b32 v85, 0xffff0000, v85\n\tv_lshlrev_b32 v86, 16, v87\n\tv_and_b32 v87, 0xffff0000, v87\n\t"
                 "v_mul_f32 v88, s86, v84\n\tv_mul_f32 v89, s86, v85\n\tv_mul_f32 v90, s86, v86\n\tv_mul_f32 v91, s86, v87\n\tv_fma_f32 v84, -v88, s85, v84\n\t"
                 "v_fma_f32 v85, -v89, s85, v85\n\tv_fma_f32 v86, -v90, s85, v86\n\tv_fma_f32 v87, -v91, s85, v87\n\tv_fmac_f32 v88, s86, v84\n\t"
                 "v_fmac_f32 v89, s86, v85\n\tv_fmac_f32 v90, s86, v86\n\tv_fmac_f32 v91, s86, v87\n\tv_cvt_pk_bf16_f32 v88, v88, v89\n\t"
                 "v_cvt_pk_bf16_f32 v90, v90, v91\n\tv_perm_b32 v92, v88, v88, s88\n\tv_perm_b32 v93, v88, v88, s89\n\tv_perm_b32 v94, v90, v90, s88\n\t"
                 "v_perm_b32 v95, v90, v90, s89\n\ts_nop 0\n\tv_and_b32_dpp v96, v92, v102 quad_perm:[0,0,0,0] row_mask:0xf bank_mask:0xf\n\t"
                 "v_and_b32_dpp v97, v92, v103 quad_perm:[0,0,0,0] row_mask:0xf bank_mask:0xf\n\t"
                 "v_and_b32_dpp v100, v93, v102 quad_perm:[0,0,0,0] row_mask:0xf bank_mask:0xf\n\t"
                 "v_and_b32_dpp v101, v93, v103 quad_perm:[0,0,0,0] row_mask:0xf bank_mask:0xf\n\tv_mfma_f32_4x4x4_16b_bf16 v[84:87], v[96:97], v[104:105], 0\n\t"
                 "v_mfma_f32_4x4x4_16b_bf16 v[88:91], v[96:97], v[106:107], 0\n\tv_and_b32_dpp v96, v94, v102 quad_perm:[0,0,0,0] row_mask:0xf bank_mask:0xf\n\t"
                 "v_and_b32_dpp v97, v94, v103 quad_perm:[0,0,0,0] row_mask:0xf bank_mask:0xf\n\t"
                 "v_mfma_f32_4x4x4_16b_bf16 v[84:87], v[100:101], v[108:109], v[84:87]\n\tv_mfma_f32_4x4x4_16b_bf16 v[88:91], v[100:101], v[110:111], v[88:91]\n\t"
                 "v_and_b32_dpp v100, v95, v102 quad_perm:[0,0,0,0] row_mask:0xf bank_mask:0xf\n\t"
                 "v_and_b32_dpp v101, v95, v103 quad_perm:[0,0,0,0] row_mask:0xf bank_mask:0xf\n\tv_mfma_f32_4x4x4_16b_bf16 v[84:87], v[96:97], v[112:113], v[84:87]\n\t"
                 "v_mfma_f32_4x4x4_16b_bf16 v[88:91], v[96:97], v[114:115], v[88:91]\n\tv_and_b32_dpp v96, v92, v102 quad_perm:[1,1,1,1] row_mask:0xf bank_mask:0xf\n\t"
                 "v_and_b32_dpp v97, v92, v103 quad_perm:[1,1,1,1] row_mask:0xf bank_mask:0xf\n\t"
                 "v_mfma_f32_4x4x4_16b_bf16 v[84:87], v[100:101], v[116:117], v[84:87]\n\tv_mfma_f32_4x4x4_16b_bf16 v[88:91], v[100:101], v[118:119], v[88:91]\n\t"
                 "v_and_b32_dpp v100, v93, v102 quad_perm:[1,1,1,1] row_mask:0xf bank_mask:0xf\n\t"
                 "v_and_b32_dpp v101, v93, v103 quad_perm:[1,1,1,1] row_mask:0xf bank_mask:0xf\n\tv_mfma_f32_4x4x4_16b_bf16 v[84:87], v[96:97], v[120:121], v[84:87]\n\t"
                 "v_mfma_f32_4x4x4_16b_bf16 v[88:91], v[96:97], v[122:123], v[88:91]\n\tv_and_b32_dpp v96, v94, v102 quad_perm:[1,1,1,1] row_mask:0xf bank_mask:0xf\n\t"
                 "v_and_b32_dpp v97, v94, v103 quad_perm:[1,1,1,1] row_mask:0xf bank_mask:0xf\n\t"
                 "v_mfma_f32_4x4x4_16b_bf16 v[84:87], v[100:101], v[124:125], v[84:87]\n\tv_mfma_f32_4x4x4_16b_bf16 v[88:91], v[100:101], v[126:127], v[88:91]\n\t"
                 "v_and_b32_dpp v100, v95, v102 quad_perm:[1,1,1,1] row_mask:0xf bank_mask:0xf\n\t"
                 "v_and_b32_dpp v101, v95, v103 quad_perm:[1,1,1,1] row_mask:0xf bank_mask:0xf\n\tv_mfma_f32_4x4x4_16b_bf16 v[84:87], v[96:97], v[128:129], v[84:87]\n\t"
                 "v_mfma_f32_4x4x4_16b_bf16 v[88:91], v[96:97], v[130:131], v[88:91]\n\tv_and_b32_dpp v96, v92, v102 quad_perm:[2,2,2,2] row_mask:0xf bank_mask:0xf\n\t"
                 "v_and_b32_dpp v97, v92, v103 quad_perm:[2,2,2,2] row_mask:0xf bank_mask:0xf\n\t"
                 "v_mfma_f32_4x4x4_16b_bf16 v[84:87], v[100:101], v[132:133], v[84:87]\n\tv_mfma_f32_4x4x4_16b_bf16 v[88:91], v[100:101], v[134:135], v[88:91]\n\t"
                 "v_and_b32_dpp v100, v93, v102 quad_perm:[2,2,2,2] row_mask:0xf bank_mask:0xf\n\t"
                 "v_and_b32_dpp v101, v93, v103 quad_perm:[2,2,2,2] row_mask:0xf bank_mask:0xf\n\tv_mfma_f32_4x4x4_16b_bf16 v[84:87], v[96:97], v[136:137], v[84:87]\n\t"
                 "v_mfma_f32_4x4x4_16b_bf16 v[88:91], v[96:97], v[138:139], v[88:91]\n\tv_and_b32_dpp v96, v94, v102 quad_perm:[2,2,2,2] row_mask:0xf bank_mask:0xf\n\t"
                 "v_and_b32_dpp v97, v94, v103 quad_perm:[2,2,2,2] row_mask:0xf bank_mask:0xf\n\t"
                 "v_mfma_f32_4x4x4_16b_bf16 v[84:87], v[100:101], v[140:141], v[84:87]\n\tv_mfma_f32_4x4x4_16b_bf16 v[88:91], v[100:101], v[142:143], v[88:91]\n\t"
                 "v_and_b32_dpp v100, v95, v102 quad_perm:[2,2,2,2] row_mask:0xf bank_mask:0xf\n\t"
                 "v_and_b32_dpp v101, v95, v103 quad_perm:[2,2,2,2] row_mask:0xf bank_mask:0xf\n\tv_mfma_f32_4x4x4_16b_bf16 v[84:87], v[96:97], v[144:145], v[84:87]\n\t"
                 "v_mfma_f32_4x4x4_16b_bf16 v[88:91], v[96:97], v[146:147], v[88:91]\n\tv_and_b32_dpp v96, v92, v102 quad_perm:[3,3,3,3] row_mask:0xf bank_mask:0xf\n\t"
                 "v_and_b32_dpp v97, v92, v103 quad_perm:[3,3,3,3] row_mask:0xf bank_mask:0xf\n\t"
                 "v_mfma_f32_4x4x4_16b_bf16 v[84:87], v[100:101], v[148:149], v[84:87]\n\tv_mfma_f32_4x4x4_16b_bf16 v[88:91], v[100:101], v[150:151], v[88:91]\n\t"
                 "v_and_b32_dpp v100, v93, v102 quad_perm:[3,3,3,3] row_mask:0xf bank_mask:0xf\n\t"
                 "v_and_b32_dpp v101, v93, v103 quad_perm:[3,3,3,3] row_mask:0xf bank_mask:0xf\n\tv_mfma_f32_4x4x4_16b_bf16 v[84:87], v[96:97], v[152:153], v[84:87]\n\t"
                 "v_mfma_f32_4x4x4_16b_bf16 v[88:91], v[96:97], v[154:155], v[88:91]\n\tv_and_b32_dpp v96, v94, v102 quad_perm:[3,3,3,3] row_mask:0xf bank_mask:0xf\n\t"
                 "v_and_b32_dpp v97, v94, v103 quad_perm:[3,3,3,3] row_mask:0xf bank_mask:0xf\n\t"
                 "v_mfma_f32_4x4x4_16b_bf16 v[84:87], v[100:101], v[156:157], v[84:87]\n\tv_mfma_f32_4x4x4_16b_bf16 v[88:91], v[100:101], v[158:159], v[88:91]\n\t"
                 "v_and_b32_dpp v100, v95, v102 quad_perm:[3,3,3,3] row_mask:0xf bank_mask:0xf\n\t"
                 "v_and_b32_dpp v101, v95, v103 quad_perm:[3,3,3,3] row_mask:0xf bank_mask:0xf\n\tv_mfma_f32_4x4x4_16b_bf16 v[84:87], v[96:97], v[160:161], v[84:87]\n\t"
                 "v_mfma_f32_4x4x4_16b_bf16 v[88:91], v[96:97], v[162:163], v[88:91]\n\ts_nop 1\n\t"
                 "v_mfma_f32_4x4x4_16b_bf16 v[84:87], v[100:101], v[164:165], v[84:87]\n\tv_mfma_f32_4x4x4_16b_bf16 v[88:91], v[100:101], v[166:167], v[88:91]\n\t"
                 "s_nop 4\n\tv_permlane32_swap_b32 v84, v88\n\tv_permlane32_swap_b32 v85, v89\n\tv_permlane32_swap_b32 v86, v90\n\tv_permlane32_swap_b32 v87, v91\n\t"
                 "v_add_f32 v84, v84, v88\n\tv_add_f32 v85, v85, v89\n\tv_add_f32 v86, v86, v90\n\tv_add_f32 v87, v87, v91\n\ts_nop 0\n\t"
                 "v_permlane16_swap_b32 v84, v85\n\tv_permlane16_swap_b32 v86, v87\n\ts_nop 0\n\tv_add_f32 v84, v84, v85\n\tv_add_f32 v86, v86, v87\n\ts_nop 1\n\t"
                 "v_add_f32_dpp v84, v84, v84 row_ror:8 row_mask:0xf bank_mask:0xf\n\tv_add_f32_dpp v86, v86, v86 row_ror:8 row_mask:0xf bank_mask:0xf\n\ts_nop 0\n\t"
                 "v_add_f32_dpp v84, v84, v84 row_ror:4 row_mask:0xf bank_mask:0xf\n\tv_add_f32_dpp v86, v86, v86 row_ror:4 row_mask:0xf bank_mask:0xf\n\t"
                 "ds_write2_b32 v99, v84, v86 offset1:8\n\ts_setpc_b64 s[92:93]\n\t"
                   :: "{s[0:1]}"(ap + 1), "{s2}"(bid), "{v0}"(wid * 64), KEPT() : "s90", "s91", "scc", "memory");
      __builtin_unreachable();
    }
    {
    constexpr int dk = 8;
    constexpr int BEST = FLG + 40, MEET = FLG + 48;
    __builtin_amdgcn_s_setprio(HPRIO);
    if (hw == 0) STAMP(dk, 0);
    const unsigned tb1 = (unsigned)__builtin_amdgcn_s_memrealtime();
    unsigned* hand2 = hand + HAND_N;
    const __amdgpu_buffer_rsrc_t rsh = __builtin_amdgcn_make_buffer_rsrc(hand2, 0, (HAND_N + PS_N) * 4, 0x00020000);
    const __amdgpu_buffer_rsrc_t rsx = __builtin_amdgcn_make_buffer_rsrc(xout, 0, 3 * D_ * 2, 0x00020000);
    constexpr int OOR = (HAND_N + PS_N) * 4;
    if (!p1blk && hw < 2) {
      f32x2 a[2], c[2];
#pragma unroll
      for (int e = 0; e < 4; ++e) {
        a[e >> 1][e & 1] = (__uint_as_float(pv[0][e] << 16) + __uint_as_float(pv[1][e] << 16)) + (__uint_as_float(pv[2][e] << 16) + __uint_as_float(pv[3][e] << 16));
        c[e >> 1][e & 1] = (__uint_as_float(pv[0][e] & 0xffff0000u) + __uint_as_float(pv[1][e] & 0xffff0000u)) + (__uint_as_float(pv[2][e] & 0xffff0000u) + __uint_as_float(pv[3][e] & 0xffff0000u));
      }
      const float r = (fnum + fold8(a[0], a[1])) * __builtin_amdgcn_rsqf(fvar + fold8(c[0], c[1]));
      unsigned key = (__float_as_uint(r) ^ ((unsigned)(__float_as_int(r) >> 31) | 0x80000000u)) & 0xfffff800u;
      if (r != r) key = 0u;
      key = wave_umax(key | (unsigned)(2047 - (rrow + (lane & 7) * 4 + (lane >> 4))));
      __builtin_amdgcn_raw_buffer_store_b64(u32x2{key, tb1 & 0x7fffffffu}, rsh, lane < NREP ? (HAND_N + PK_O + lane * 128 + (rrow >> 5) * 2) * 4 : OOR, 0, 16);
      if (hw == 1) STAMP(dk, 2);
    }
    gu_setup();
    bool got = true;
    auto get_fc = [&]() __attribute__((always_inline)) {
      const u32x2 fw = __builtin_amdgcn_raw_buffer_load_b64(rsh, (HAND_N + PK_O + (bid % NREP) * 128 + lane * 2) * 4, 0, 16);
      got = none(umax(fw[0], fw[1]) == SENT);
      if (got) {
        const unsigned c1 = wave_umax(fw[0]), c2 = wave_umax(fw[0] == c1 ? 0u : fw[0]);
#define BADFC 0
        const unsigned r1 = (2047u - (c1 & 0x7ffu)) ^ BADFC, r2 = 2047u - (c2 & 0x7ffu);
        unsigned dt = (unsigned)((int)((fw[1] - tb1) << 1) >> 1) + 512u;
        dt = dt < 1024u ? dt : 512u;
        *LDS_AT(volatile u32x2, lbase + SPM + 144) = u32x2{r1 | (r2 << 16), (unsigned)__builtin_amdgcn_readlane(dt, r1 >> 5) | (wave_umax(dt) << 16)};
      }
    };
    if (hw == 0) {
      __builtin_amdgcn_s_sleep(FCPRE);
#pragma unroll 1
      for (int i = 0; i < SPIN_MAX; ++i) {
        if (__builtin_amdgcn_readfirstlane(*LDS_AT(const volatile unsigned, lbase + FLG + 28)) - best >= 4u) break;
        __builtin_amdgcn_s_sleep(FCSLP);
      }
      asm volatile("" ::: "memory");
      get_fc();
    } else __builtin_amdgcn_s_sleep(TPRE - 10);
    PIN();
    tile_at(2u);
    const unsigned tb2 = (unsigned)__builtin_amdgcn_s_memrealtime() - 512u;
    LOGDEF;
    LOG();
#pragma unroll 1
    for (int spin = 0; spin < SPIN_MAX && !got; ++spin) { get_fc(); asm volatile("" ::: "memory"); }
    PIN();
    u32x2 fm = {SENT, 0u};
#pragma unroll 1
    for (int i = 0; i < 4 * SPIN_MAX && fm[0] == SENT; ++i) {
      const u32x2 q = *LDS_AT(const volatile u32x2, lbase + SPM + 144);
      fm = u32x2{(unsigned)__builtin_amdgcn_readfirstlane(q[0]), (unsigned)__builtin_amdgcn_readfirstlane(q[1])};
    }
    const int fr1 = fm[0] & 0x7ffu, fr2 = (fm[0] >> 16) & 0x7ffu;
    const unsigned t1 = tb2 + (fm[1] & 1023u), tmx = tb2 + ((fm[1] >> 16) & 1023u);
    auto until = [&](unsigned te) { while ((int)((unsigned)__builtin_amdgcn_s_memrealtime() - te) < 0) __builtin_amdgcn_s_sleep(1); asm volatile("" ::: "memory"); };
    lds_wr(lbase + SPM + 136, tmx);
    PIN();
    if (hw == 0) STAMP(dk, 3);
    const int xoff = (hw * 256 + (lane & 3) * 64 + (lane >> 2) * 4) * 2;
    const int oc = (omi * 2 + (oni >> 3)) * 32, col = oc + (lane & 7) * 4 + (lane >> 4);
    const int xoo = !p1blk && hw == 3 ? (col & ~1) * 2 : S_ * D_ * 2;
    auto get_row = [&](int r, int sl, u32x2& xr, unsigned& xo) __attribute__((always_inline)) {
      if ((r >> 7) == omi) {
#pragma unroll 1
        for (int i = 0; i < SPIN_MAX; ++i) {
          if (__builtin_amdgcn_readfirstlane(*LDS_AT(const volatile unsigned, lbase + FLG + 24)) >= 16u) break;
          __builtin_amdgcn_s_sleep(1);
        }
        asm volatile("" ::: "memory");
      }
#pragma unroll 1
      for (int spin = 0; spin < SPIN_MAX; ++spin) {
        LOG();
        xr = __builtin_amdgcn_raw_buffer_load_b64(rsx, xoff, sl * (D_ * 2), 16);
        xo = __builtin_amdgcn_raw_buffer_load_b32(rsx, xoo, sl * (D_ * 2), 16);
        LOG();
        if (none(umax(umax(xr[0], xr[1]), xo) == SENT)) break;
        __builtin_amdgcn_s_sleep(XRSLP);
        asm volatile("" ::: "memory");
      }
      PIN();
    };
    auto row_md = [&](const u32x2& xr, int k) __attribute__((always_inline)) {
      auto dot = [&](unsigned x, unsigned y, float z) { return __builtin_amdgcn_fdot2_f32_bf16(__builtin_bit_cast(bf16x2, x), __builtin_bit_cast(bf16x2, y), z, false); };
      const float p1 = dot(xr[1], 0x3f803f80u, dot(xr[0], 0x3f803f80u, 0.f)), p2 = dot(xr[1], xr[1], dot(xr[0], xr[0], 0.f));
      const auto q = __builtin_amdgcn_permlane32_swap(__float_as_uint(p1), __float_as_uint(p2), false, false);
      const float qs = __uint_as_float(q[0]) + __uint_as_float(q[1]);
      const auto q2 = __builtin_amdgcn_permlane16_swap(__float_as_uint(qs), __float_as_uint(qs), false, false);
      float sv = __uint_as_float(q2[0]) + __uint_as_float(q2[1]);
      sv = dpp_add<0x121, 0xf>(dpp_add<0x122, 0xf>(dpp_add<0x124, 0xf>(dpp_add<0x128, 0xf>(sv))));
      lds_wr(lbase + SPM + k * 64 + (lane >> 4) * 16 + hw * 4, f2word(sv));
      u32x4 a, c;
#pragma unroll 1
      for (int i = 0; i < SPIN_MAX; ++i) {
        a = lds_rd4(lbase + SPM + k * 64); c = lds_rd4(lbase + SPM + k * 64 + 32);
        if (__builtin_amdgcn_readfirstlane(umax(umax(a[0], a[1]), umax(a[2], a[3]))) != SENT) break;
      }
      auto sum4 = [&](const u32x4& v) { return (__uint_as_float(v[0]) + __uint_as_float(v[1])) + (__uint_as_float(v[2]) + __uint_as_float(v[3])); };
      const float mean = sum4(a) * (1.0f / 1024.0f), msq = sum4(c) * (1.0f / 1024.0f);
      const float den = rbf(__builtin_amdgcn_sqrtf(rbf(rbf(fmaxf(fmaf(-mean, mean, msq), 0.f)) + 1e-5f)));
      return (unsigned)__builtin_amdgcn_readfirstlane((pack2(mean, mean) & 0xffffu) | (__float_as_uint(den) & 0xffff0000u));
    };
    auto ffn = [&](const u32x2& xr, unsigned md, unsigned n, const __amdgpu_buffer_rsrc_t rs, int yo, auto&& off) __attribute__((always_inline)) {
      gate_up(xr, md);
      if (hw == 0 && n == 2u) STAMP(dk, 8);
      asm volatile("" ::: "memory");
      PIN();
      lds_wr(lbase + MEET + hw * 4, n + off());
      u32x4 mt;
#pragma unroll 1
      for (int i = 0; i < SPIN_MAX; ++i) {
        mt = lds_rd4(lbase + MEET);
        if (__builtin_amdgcn_readfirstlane(min4(mt)) >= n) break;
      }
      asm volatile("" ::: "memory");
      PIN();
      if (hw == 0 && n == 2u) STAMP(dk, 9);
      if (__builtin_amdgcn_readfirstlane(mt[0]) != n + 1u)
        __builtin_amdgcn_raw_buffer_store_b128(down(act_diag(n == 4u ? 512 : 0), w), rs, (yo + fg * D_) * 4 + t * 16, 0, 16);
      dad += n == 2u ? 512 : -512;
      if (hw == 0 && n == 2u) STAMP(dk, 4);
    };
    auto y_sum4 = [&](const __amdgpu_buffer_rsrc_t rs, int yo, int k) __attribute__((always_inline)) {
      const int src = (yo + (hw * 56 + (lane >> 3)) * D_ + oc + (lane & 7) * 4) * 4;
      u32x4 v[7];
#pragma unroll
      for (int i = 0; i < 7; ++i) asm volatile("" : "=v"(v[i]));
      unsigned todo = 0x7fu;
#pragma unroll 1
      for (int spin = 0; spin < SPIN_MAX; ++spin) {
        LOG();
#pragma unroll
        for (int i = 0; i < 7; ++i) if ((todo >> i) & 1u) v[i] = __builtin_amdgcn_raw_buffer_load_b128(rs, src, i * (8 * D_ * 4), 16);
#pragma unroll
        for (int i = 0; i < 7; ++i) if (none(umax(umax(v[i][0], v[i][1]), umax(v[i][2], v[i][3])) == SENT)) todo &= ~(1u << i);
        if (!todo) break;
        __builtin_amdgcn_s_sleep(YSLP);
        asm volatile("" ::: "memory");
      }
      f32x2 z0 = {0.f, 0.f}, z1 = {0.f, 0.f};
#pragma unroll
      for (int i = 0; i < 7; ++i) {
        z0 += f32x2{__uint_as_float(v[i][0]), __uint_as_float(v[i][1])};
        z1 += f32x2{__uint_as_float(v[i][2]), __uint_as_float(v[i][3])};
      }
      const unsigned ya = lbase + SPM + 256 + k * 512 + ((lane & 7) * 4 + (lane >> 4)) * 16;
      lds_wr((lane & 8) ? lbase + DUM + lane * 4 : ya + hw * 4, f2word(fold8(z0, z1)));
      u32x4 q = {0u, 0u, 0u, 0u};
      if (hw == 3) {
#pragma unroll 1
        for (int i = 0; i < 4 * SPIN_MAX; ++i) {
          q = lds_rd4(ya);
          if (none(umax(umax(q[0], q[1]), umax(q[2], q[3])) == SENT)) break;
        }
      }
      return (__uint_as_float(q[0]) + __uint_as_float(q[1])) + (__uint_as_float(q[2]) + __uint_as_float(q[3]));
    };
    const __amdgpu_buffer_rsrc_t rs1 = __builtin_amdgcn_make_buffer_rsrc(hand, 0, HAND_N * 4, 0x00020000);
    u32x2 xr;
    unsigned xo, xo2;
    until(t1 + XDT);
    get_row(fr1, 0, xr, xo);
    if (hw == 0) STAMP(dk, 7);
#define FAILFC 0
    const unsigned md1 = row_md(xr, 0) ^ (FAILFC & 1);
    if (hw == 0) STAMP(dk, 10);
    LOGAT(20);
    u32x2 xr2;
    unsigned md2 = 0u;
    if (p1blk) {
      xr2 = __builtin_amdgcn_raw_buffer_load_b64(rsx, (fr2 >> 7) == omi ? S_ * D_ * 2 : xoff, D_ * 2, 16);
      PIN();
      ffn(xr, md1, 2u, rsh, GRAN_N, [&]() { return 0u; });
      __builtin_amdgcn_s_setprio(R2PRIO);
      LOG();
      if ((fr2 >> 7) == omi || !none(umax(xr2[0], xr2[1]) == SENT)) get_row(fr2, 1, xr2, xo2);
      md2 = row_md(xr2, 1) ^ (FAILFC & 2);
      ffn(xr2, md2, 4u, rs1, GRAN_N, [&]() {
        const u32x2 v = *LDS_AT(const volatile u32x2, lbase + BEST);
        return v[0] != 0u && (2047 - (int)(v[0] & 0x7ffu) != fr2 || v[1] != md2) ? 1u : 0u; });
    }
    if (hw == 0) STAMP(dk, 5);
    LOG();
    float y = 0.f;
    auto put_row = [&](int r, unsigned md, unsigned rb) __attribute__((always_inline)) {
      const float rden = __uint_as_float(md & 0xffff0000u), rs = rbf(frcp(1.0f + __expf(-__uint_as_float(rb << 16))));
      const float xa = __uint_as_float((col & 1) ? xo & 0xffff0000u : xo << 16);
      float ha = xa, hb = xa;
      ln_apply2(ha, hb, __uint_as_float(md << 16), rden, frcp(rden));
      const float o = xa + rbf(ha + rbf(rbf(y) * rs));
      __builtin_amdgcn_raw_buffer_store_b16((unsigned short)(pack2(o, o) & 0xffffu), __builtin_amdgcn_make_buffer_rsrc(out, 0, S_ * D_ * 2, 0x00020000),
                                            (lane & 8) ? S_ * D_ * 2 : (r * D_ + col) * 2, 0, OAUX);
    };
    if (!p1blk) {
      until(tmx + XD2);
      get_row(fr2, 1, xr2, xo2);
      md2 = row_md(xr2, 1) ^ (FAILFC & 2);
      until(t1 + XDT + YD1); until(tmx + YD2);
      if (hw == 3) STAMP(dk, 13);
      LOGAT(8);
      y = y_sum4(rsh, GRAN_N, 0);
      if (hw == 3) STAMP(dk, 14);
      LOGAT(24);
    }
    unsigned bmd = 0u;
    best = 0u;
#pragma unroll 1
    for (int spin = 0; spin < 4 * SPIN_MAX; ++spin) {
      const u32x2 v = *LDS_AT(const volatile u32x2, lbase + BEST);
      best = __builtin_amdgcn_readfirstlane(v[0]); bmd = __builtin_amdgcn_readfirstlane(v[1]);
      if (best) break;
      __builtin_amdgcn_s_sleep(1);
    }
    asm volatile("" ::: "memory");
    const int idx = 2047 - (int)(best & 0x7ffu);
    const unsigned rk = best >> 16, rb = (rk & 0x8000u) ? (rk & 0x7fffu) : (~rk & 0xffffu);
    if (hw == 0) STAMP(dk, 1);
    LOGAT(25);
    LOGOUT(hw);
    if (__builtin_expect(idx != fr1 || bmd != md1, 0)) {
      if (idx == fr2 && bmd == md2) {
        if (p1blk) return;
        y = y_sum4(rs1, GRAN_N, 1);
        xo = xo2;
      } else {
        get_row(idx, idx == fr1 ? 0 : (idx == fr2 ? 1 : 2), xr, xo);
        if (p1blk) ffn(xr, bmd, 6u, rsh, HAND_N + Y3_O, [&]() { return 0u; });
        else y = y_sum4(rsh, HAND_N + Y3_O, 2);
        if (p1blk || hw != 3) return;
#pragma unroll 1
        for (int spin = 0; spin < SPIN_MAX && idx != fr1 && idx != fr2; ++spin) {
          if (__builtin_amdgcn_readfirstlane(ld_ag(gran + GR_F + (oc >> 6) * 32)) == tag) break;
          asm volatile("" ::: "memory");
        }
      }
    }
    if (p1blk || hw != 3) return;
    put_row(idx, bmd, rb);
    }
    return;
  }
  if (wid >= 4) {
    const int lane = fresh_lane();
    const int lw = wid - 4;
    const long long lay_ = ap->lay;
    const u16* A = ap->a; const u16* W = ap->W; const u16* wc = ap->wc;
#define LPRIO 3
#define LP0 3
    __builtin_amdgcn_s_setprio(LP0);
    unsigned va[4], vw[6];
    const unsigned ul = lane;
    va[0] = (unsigned)(m0 + lw * 32) * (K * 2) + (ul >> 3) * (K * 2) + (((ul & 7u) ^ (ul >> 4)) << 4);
    va[1] = va[0] + (8 * K * 2 - 1024);
    va[2] = (va[0] ^ 64u) + 2 * (8 * K * 2 - 1024);
    va[3] = va[2] + (8 * K * 2 - 1024);
#pragma unroll
    for (int i = 0; i < 3; ++i) {
      const unsigned n = ul + 64u * i, k = __umul24(n, 171u) >> 12, cp = n - __umul24(k, 24u);
      vw[i] = __umul24(k, ldw * 2) + (cp >> 3) * 2048u + (((cp & 7u) ^ ((k & 2u) << 1)) << 4) + ((unsigned)(lw * 16 * ldw + n0) * 2u - i * 1024u);
      vw[i + 3] = (vw[i] ^ 16u) + 8 * ldw * 2;
    }
    PIN();
    asm volatile("" : "+v"(va[0]), "+v"(va[1]), "+v"(va[2]), "+v"(va[3]), "+v"(vw[0]), "+v"(vw[1]), "+v"(vw[2]), "+v"(vw[3]), "+v"(vw[4]), "+v"(vw[5]));
    const bool LAY2 = lay_ != 0;
    const int krot = tmi & 8;
    if (LAY2 && slow_cols(W, tni)) W = wc;
    auto stage_a = [&](int kt, int slot) {
      const char* pa = reinterpret_cast<const char*>(A) + (size_t)(kt ^ krot) * 128;
      asm volatile("s_mov_b32 m0, %5\n\ts_nop 0\n\tglobal_load_lds_dwordx4 %0, %4 sc1\n\tglobal_load_lds_dwordx4 %1, %4 offset:1024 sc1\n\t"
                   "global_load_lds_dwordx4 %2, %4 offset:2048 sc1\n\tglobal_load_lds_dwordx4 %3, %4 offset:3072 sc1"
                   :: "v"(va[0]), "v"(va[1]), "v"(va[2]), "v"(va[3]), "s"(pa), "s"(lbase + slot * STB + lw * 4096) : "m0", "memory");
    };
    auto stage_w = [&](int kt, int slot) {
      const char* pw = reinterpret_cast<const char*>(W) + (size_t)(kt ^ krot) * 128 * ldw;
      asm volatile("s_mov_b32 m0, %7\n\ts_nop 0\n\tglobal_load_lds_dwordx4 %0, %6 sc1\n\tglobal_load_lds_dwordx4 %1, %6 offset:1024 sc1\n\t"
                   "global_load_lds_dwordx4 %2, %6 offset:2048 sc1\n\ts_addk_i32 m0, 0xc00\n\ts_nop 0\n\tglobal_load_lds_dwordx4 %3, %6 sc1\n\t"
                   "global_load_lds_dwordx4 %4, %6 offset:1024 sc1\n\tglobal_load_lds_dwordx4 %5, %6 offset:2048 sc1"
                   :: "v"(vw[0]), "v"(vw[1]), "v"(vw[2]), "v"(vw[3]), "v"(vw[4]), "v"(vw[5]), "s"(pw), "s"(lbase + slot * STB + ABYTES + lw * 6144) : "m0", "scc", "memory");
    };
    if (LAY2) {
      __builtin_amdgcn_s_setprio(0);
      __builtin_amdgcn_s_sleep(TPRE - 10);
      tile_at(3u);
      __builtin_amdgcn_s_setprio(LPRIO);
#pragma unroll
      for (int st = 0; st < NST; ++st) stage_w(st, st);
      PIN();
      __builtin_amdgcn_s_barrier();
      PIN();
#pragma unroll
      for (int st = 0; st < NST; ++st) stage_a(st, st);
      asm volatile("s_waitcnt vmcnt(8)" ::: "memory");
    } else {
      __builtin_amdgcn_s_setprio(LPRIO);
      stage_a(0, 0); stage_w(0, 0);
      if (PRO > 1) { stage_a(1, 1); stage_w(1, 1); }
      if (PRO > 2) { stage_a(2, 2); stage_w(2, 2); }
      asm volatile("s_waitcnt vmcnt(%0)" :: "n"(10 * PRO - 10) : "memory");
    }
    int slot = 0;
    __builtin_amdgcn_s_barrier();
    if (!LAY2) {
      if (PRO < 2) { stage_a(1, 1); stage_w(1, 1); }
      if (PRO < 3) { stage_a(2, 2); stage_w(2, 2); }
    }
    for (int kt = 0; kt + 1 < NKT; ++kt) {
      if (LAY2 && kt == 0) asm volatile("s_waitcnt vmcnt(4)" ::: "memory");
      else if (kt + 2 >= NKT) asm volatile("s_waitcnt vmcnt(0)" ::: "memory");
      else asm volatile("s_waitcnt vmcnt(10)" ::: "memory");
      __builtin_amdgcn_s_barrier();
      if (kt + NST < NKT) { stage_a(kt + NST, slot); stage_w(kt + NST, slot); }
      slot = slot == NST - 1 ? 0 : slot + 1;
    }
    __builtin_amdgcn_s_setprio(0);
    __builtin_amdgcn_s_barrier();
  } else {
  LAYER_HERE;
  const int dk = LAY2 ? 5 : 1;
  const int lane = fresh_lane(), wm = wid & 1, wn = wid >> 1;
  if (wid == 0) STAMP(dk, 0);
  f32x4 acc[4][6];
#pragma unroll
  for (int i = 0; i < 4; ++i)
#pragma unroll
    for (int j = 0; j < 6; ++j) acc[i][j] = f32x4{0.f, 0.f, 0.f, 0.f};
  const int q16 = lane & 15, g4 = lane >> 4;
  const int e0 = 16 * (q16 >> 3) + (q16 & 7);
  s16x8 af[2][4];
  unsigned aad[2], wad[2];
  {
    const int row = wm * 64 + e0, swz = ((row >> 1) & 3) | (((row >> 4) & 1) << 2);
#pragma unroll
    for (int h = 0; h < 2; ++h) aad[h] = lbase + row * 128 + (((4 * h + g4) ^ swz) << 4);
    const int tq = (lane & 15) >> 2, tp = lane & 3, k = 8 * g4 + tq, ch = wn * 4 + 2 * (tp >> 1);
    wad[0] = lbase + ABYTES + k * 384 + ((ch ^ (4 * ((k >> 1) & 1)) ^ (g4 & 1)) << 4) + 8 * (tp & 1);
    wad[1] = wad[0] ^ 16;
  }
  auto lda = [&](unsigned a, int i) { return *LDS_AT(const s16x8, a + (i >> 1) * 4096 + (i & 1) * 1024); };
  auto ldw_ = [&](int h, int j) {
    const unsigned w = wad[j & 1] + h * 12288 + (j >> 1) * 128;
    s16x4 lo = __builtin_amdgcn_ds_read_tr16_b64_v4i16(LDS_AT(s16x4, w));
    s16x4 h4 = __builtin_amdgcn_ds_read_tr16_b64_v4i16(LDS_AT(s16x4, w + 1536));
    return (s16x8)__builtin_shufflevector(lo, h4, 0, 1, 2, 3, 4, 5, 6, 7);
  };
  ARGS_HERE;
  unsigned lto = lbase + wid * 4096;
  char* kvb = reinterpret_cast<char*>(KV) + ((tni * 2 + wn) * 64 + tmi * 4 + wm * 2) * 2048 - lto;
  {
    const unsigned ato = lbase + ALPHA + wm * 256, dum = lbase + DUM + lane * 4;
    *LDS_AT(u32x4, lto + QOFF + lane * 16) = u32x4{lane >= 16 ? lbase + CRED : lbase + TABA + (wm * 64 + e0) * 16, lbase + TABW + (wn * 32 + e0) * 16,
                                                   ato + e0 * 4, lto + (lane >> 5) * 1024 + ((lane & 28) * 2 + (lane & 3)) * 16};
    *LDS_AT(u32x2, lto + QOFF + 2048 + lane * 16) = u32x2{aad[0], aad[1]};
    *LDS_AT(u32x4, lto + QOFF + 1024 + lane * 16) = u32x4{ato + g4 * 16 + (lane >> 5) * 32, lto + (lane >> 5) * 1024 + ((lane & 24) * 2 + (lane & 7)) * 16,
                                                          lane == 0 ? lbase + FLG + 32 : dum, lane == 0 ? lbase + FLG + 16 : dum};
  }
  asm volatile("" : "+s"(lto), "+s"(kvb) :: "memory");
#pragma unroll
  for (int i = 0; i < 4; ++i)
#pragma unroll
    for (int j = 0; j < 6; ++j) asm volatile("" : "+v"(acc[i][j]));
  asm volatile("" : "+v"(aad[0]), "+v"(aad[1]), "+v"(wad[0]), "+v"(wad[1]));
  if (LAY2) __builtin_amdgcn_s_barrier();
  __builtin_amdgcn_s_barrier();
  PIN();
  if (wid == 0) STAMP(dk, 1);
  s16x8 wq[4][2];
  wq[0][0] = ldw_(0, 0); af[0][0] = lda(aad[0], 0); wq[0][1] = ldw_(1, 0); af[1][0] = lda(aad[1], 0); wq[1][0] = ldw_(0, 1); wq[1][1] = ldw_(1, 1);
  af[0][1] = lda(aad[0], 1); af[1][1] = lda(aad[1], 1); af[0][2] = lda(aad[0], 2); af[1][2] = lda(aad[1], 2);
  PIN();
  s16x4 wlo;
  auto rw = [&](s16x8& d, int h, int j, int hi, int off = 0) __attribute__((always_inline)) {
    const s16x4 v = __builtin_amdgcn_ds_read_tr16_b64_v4i16(LDS_AT(s16x4, wad[j & 1] + h * 12288 + (j >> 1) * 128 + hi * 1536 + off));
    if (hi) d = (s16x8)__builtin_shufflevector(wlo, v, 0, 1, 2, 3, 4, 5, 6, 7);
    else wlo = v;
  };
  auto at = [&](s16x8& x, int r) __attribute__((always_inline)) {
    if (r == 0) asm volatile("" :: "{v[104:107]}"(x));
    if (r == 1) asm volatile("" :: "{v[108:111]}"(x));
    if (r == 2) asm volatile("" :: "{v[112:115]}"(x));
    if (r == 3) asm volatile("" :: "{v[116:119]}"(x));
    if (r == 4) asm volatile("" :: "{v[120:123]}"(x));
    if (r == 5) asm volatile("" :: "{v[124:127]}"(x));
    if (r == 6) asm volatile("" :: "{v[128:131]}"(x));
    if (r == 7) asm volatile("" :: "{v[132:135]}"(x));
    if (r == 8) asm volatile("" :: "{v[136:139]}"(x));
    if (r == 9) asm volatile("" :: "{v[140:143]}"(x));
    if (r == 10) asm volatile("" :: "{v[144:147]}"(x));
    if (r == 11) asm volatile("" :: "{v[148:151]}"(x));
    if (r == 12) asm volatile("" :: "{v[152:155]}"(x));
    if (r == 13) asm volatile("" :: "{v[156:159]}"(x));
    if (r == 14) asm volatile("" :: "{v[160:163]}"(x));
    if (r == 15) asm volatile("" :: "{v[164:167]}"(x));
  };
  auto mm = [&](int i, int j, int h, int sl, int hw_) __attribute__((always_inline)) {
    s16x8 &a = af[h][i], &w = wq[sl][hw_];
    at(a, h * 4 + i); at(w, 8 + sl * 2 + hw_);
    if (j >= 4) asm volatile("v_mfma_f32_16x16x32_bf16 %0, %1, %2, %0" : "+v"(acc[i][j]) : "v"(a), "v"(w));
    else asm volatile("v_mfma_f32_16x16x32_bf16 %0, %1, %2, %0" : "+v"(acc[i][j]) : "v"(w), "v"(a));
    PIN();
  };
  auto next = [&](int g, int J0, int s0, int s1, int off) __attribute__((always_inline)) {
    if (g == 2 || g == 3) rw(wq[s0][0], 0, J0, g & 1, off);
    if (g == 4 || g == 5) af[g & 1][0] = lda(aad[g & 1] + off, 0);
    if (g == 6 || g == 7) rw(wq[s0][1], 1, J0, g & 1, off);
    if (g >= 8 && g < 12) rw(wq[s1][(g - 8) >> 1], (g - 8) >> 1, J0 + 1, g & 1, off);
    if (g >= 12) af[g & 1][(g - 10) >> 1] = lda(aad[g & 1] + off, (g - 10) >> 1);
  };
  auto third = [&](int p, int J0, int delta) __attribute__((always_inline)) {
    auto mv = [&](unsigned& x) __attribute__((always_inline)) { asm volatile("v_add_u32 %0, %1, %0" : "+v"(x) : "s"(delta)); };
#pragma unroll
    for (int g = 0; g < 16; ++g) {
      const int h = g & 1, i = p == 1 ? (g >> 1) & 3 : g >> 2, t = p == 1 ? g >> 3 : (g >> 1) & 1, j = 2 * p + t, sl = p == 1 ? 2 - 2 * t : p + t;
      mm(i, j, h, sl, h);
      if (p == 0) {
        if (g < 2) af[g][3] = lda(aad[g], 3);
        else if (g < 6) rw(wq[2][(g - 2) >> 1], (g - 2) >> 1, 2, g & 1);
        else if (g < 10) rw(wq[3][(g - 6) >> 1], (g - 6) >> 1, 5, g & 1);
        else if (g < 12) mv(aad[g - 10]);
      }
      if (p == 1) {
        if (g < 4) rw(wq[0][g >> 1], g >> 1, 3, g & 1);
        if (g == 4) mv(wad[1]);
        if (g >= 8 && g < 12) rw(wq[2][(g - 8) >> 1], (g - 8) >> 1, 4, g & 1);
        if (g == 12) mv(wad[0]);
      }
      if (p == 2) {
        if (g == BARG) {
          asm volatile("" ::: "memory");
          PIN();
          __builtin_amdgcn_s_barrier();
          asm volatile("" ::: "memory");
        }
        next(g, J0, 0, 1, 0);
      }
      PIN();
      {
        if ((p == 2 && g == 15) || (p == 0 && g == 7)) __builtin_amdgcn_s_waitcnt(0xc67f);
        if (p == 0 && g == 2) __builtin_amdgcn_s_waitcnt(0xc57f);
        if ((p == 0 && g == 14) || (p == 1 && (g & 7) == 7)) __builtin_amdgcn_s_waitcnt(0xc07f);
        PIN();
      }
    }
  };
  third(0, 0, STB); third(1, 0, STB);
  {
    static_assert((NKT - NST - 1) % NST == 0, "");
#pragma unroll 1
    for (int kt = 0; kt < NKT - NST - 1; kt += NST) {
#pragma unroll
      for (int u = 0; u < NST; ++u) {
        const int delta = u == 1 ? -(NST - 1) * STB : STB;
        third(2, 0, 0); third(0, 0, delta); third(1, 0, delta);
      }
    }
  }
  static_assert(NKT % NST == 1, "slots of the last stages");
#define TT(i)
  auto bar = [&]() { PIN(); __builtin_amdgcn_s_barrier(); asm volatile("" ::: "memory"); PIN(); };
  TT(0);
  third(2, 2, 0);
  const __amdgpu_buffer_rsrc_t rsk = __builtin_amdgcn_make_buffer_rsrc(kvb, 0, 0x7fffffff, 0x00020000), rsv = __builtin_amdgcn_make_buffer_rsrc(kvb + VOFF, 0, 0x7fffffff, 0x00020000);
  unsigned pk;
  u32x2 t0, t1, t2, t3;
  auto park = [&](int i) { return *LDS_AT(const volatile u32x2, pk + (i >> 1) * 1024 + (i & 1) * 8); };
  float al[4];
  f32x4 av[4];
  u32x4 kvp[12];
  auto cacc = [&](int p, int x, int t) -> f32x4& { return p < 2 ? acc[x][2 * p + t] : acc[(x >> 1) * 2 + t][4 + (x & 1)]; };
  auto signal = [&](unsigned v) { __hip_atomic_fetch_add(LDS_AT(unsigned, t3[0]), v, __ATOMIC_RELAXED, __HIP_MEMORY_SCOPE_WORKGROUP); PIN(); };
  auto outop = [&](int p, int k) {
    if (k >= 0 && k < 16) {
      const int x = k >> 2, t = (k >> 1) & 1;
      f32x4& c = cacc(p, x, t);
#pragma unroll
      for (int o = 2 * (k & 1); o < 2 * (k & 1) + 2; ++o) {
        float r;
        asm volatile("v_mul_f32 %0, %1, %2" : "=v"(r) : "v"(c[o]), "v"(p < 2 ? al[x] : av[(x >> 1) * 2 + t][o]));
        c[o] = r;
      }
      PIN();
    }
    if (k >= 1 && k <= 16) {
      const int x = (k - 1) >> 2, t = ((k - 1) >> 1) & 1, o = (k - 1) & 1;
      kvp[p * 4 + x][2 * t + o] = pack2(cacc(p, x, t)[2 * o], cacc(p, x, t)[2 * o + 1]);
      PIN();
      if (t && o) {
        if (p) __builtin_amdgcn_raw_buffer_store_b128(kvp[p * 4 + x], p == 1 ? rsk : rsv, (p == 1 ? t1[1] : t2[1]) + (x >> 1) * 2048 + (x & 1) * (p == 1 ? 64 : 128), 0, 16);
        else *LDS_AT(u32x4, t2[1] + (x >> 1) * 2048 + (x & 1) * 128) = kvp[x];
        PIN();
      }
    }
  };
  static_for<0, 168>([&](auto nc) __attribute__((always_inline)) {
    constexpr int n = decltype(nc)::value, sec = n < 80 ? n >> 4 : (n < 104 ? 5 : 6 + ((n - 104) >> 4)), g = n < 104 ? n - 16 * sec : (n - 104) & 15;
    auto taba = [&](int i) { af[0][i] = *LDS_AT(const s16x8, t0[0] + (i >> 1) * 512 + (i & 1) * 128); };
    auto tabw = [&](int j) { wq[(2 + (j >> 1)) & 3][j & 1] = *LDS_AT(const s16x8, t0[1] + ((j >> 1) * 64 + (j & 1) * 8) * 16); };
    auto rwn = [&](int e, int sl, int j, int off) { if (e >= 0 && e < 4) rw(wq[sl][e >> 1], e >> 1, j, e & 1, off); };
    if (g == 0) TT(1 + sec);
    if (sec == 5) {
      const int i = (g / 12) * 2 + (g & 1), j = (g % 12) >> 1;
      mm(i, j, 0, (2 + (j >> 1)) & 3, j & 1);
    } else {
      constexpr int part[10] = {1, 2, 1, 2, 0, 0, 1, 2, 0, 0}, s0[10] = {0, 2, 0, 2, 0, 0, 1, 3, 1, 3};
      const int h = g & 1, i = sec == 7 ? (g >> 3) * 2 + ((g >> 1) & 1) : g >> 2, t = sec == 7 ? (g >> 2) & 1 : (g >> 1) & 1;
      mm(i, 2 * part[sec] + t, h, (s0[sec] + t) & 3, h);
    }
    if (sec == 0 || sec == 2) {
      if (g < 2) af[g][3] = lda(aad[g] + sec / 2 * STB, 3);
      rwn(g - 2, 2, 4, sec / 2 * STB); rwn(g - 6, 3, 5, sec / 2 * STB);
    }
    if (sec == 1) {
      if (g == 1) bar();
      next(g, 2, 0, 1, STB);
    }
    if ((sec == 3 && g == 12) || (sec == 6 && g == 13)) { pk = lto + QOFF + fresh_lane() * 16; asm volatile("" : "+v"(pk)); }
    if (sec == 3) {
      rwn(g, 0, 0, STB); rwn(g - 4, 1, 1, STB);
      if (g >= 8 && g < 12) asm volatile("v_subrev_u32 %0, %1, %0" : "+v"(g < 10 ? aad[g & 1] : wad[g & 1]) : "s"(STB));
      if (g == 13) t1 = park(1);
      if (g == 14) t0 = park(0);
    }
    if (sec == 4) {
      if (g == B15) bar();
      if (g == 4) taba(0);
      if (g == 5 || g == 6) tabw(g - 5);
      if (g == 7) taba(1);
      if (g == 8 || g == 9) tabw(g - 6);
      if (g == 10 || g == 11) af[1][g - 10] = lda(aad[1], g - 10);
      if (g == 12) taba(2);
      if (g == 15) taba(3);
    }
    if (sec == 5) {
      if (g < 2) tabw(4 + g);
      rwn(g - 2, 1, 2, 0);
      if (g == 12 || g == 13) af[0][g - 12] = lda(aad[0], g - 12);
      if (g == 22) { al[0] = *LDS_AT(const float, t1[0]); al[1] = *LDS_AT(const float, t1[0] + 32); }
      rwn(g - 16, 2, 3, 0);
      if (g == 20 || g == 21) af[1][g - 18] = lda(aad[1], g - 18);
    }
    if (sec == 6) {
      if (g < 2) af[0][2 + g] = lda(aad[0], 2 + g);
      rwn(g - 2, 3, 4, 0);
      if (g == 9) { al[2] = *LDS_AT(const float, t1[0] + 128); al[3] = *LDS_AT(const float, t1[0] + 160); }
      if (g == 14) t2 = park(2);
      if ((g & 3) >= 2) rwn((g - 10) / 4 * 2 + (g & 1) - (g < 10) * 8, 0, 5, 0);
    }
    if (sec == 7) {
      if (g == 2 || g == 3 || g == 10 || g == 11) av[(g >> 3) * 2 + (g & 1)] = *LDS_AT(const f32x4, t2[0] + ((g >> 3) * 32 + (g & 1) * 8) * 4);
      rwn(g < 2 ? g : (g < 4 ? -1 : g - 2), 1, 0, 0); rwn(g - 6, 2, 1, 0);
      if (g == 12) t3 = park(3);
      if (g == 13) { const u32x2 a = park(4); aad[0] = a[0]; aad[1] = a[1]; }
      if (g == 15) {
        asm volatile("" ::: "memory");
        __builtin_amdgcn_s_waitcnt(0xc07f);
        bar();
      }
    }
    if (sec == 8) next(g, 0, 3, 0, STB);
    if (sec == 9) {
      if (g < 2) af[g][3] = lda(aad[g] + STB, 3);
      if (g == 2) signal(0x10000u);
    }
    PIN();
    outop(1, n - 104 - KO);
    outop(2, n - 120 - KO);
    {
      constexpr int wg[8] = WGAPS;
#pragma unroll
      for (int e = 0; e < 8; ++e)
        if (n - 136 == wg[e]) {
          *LDS_AT(u32x4, (e < 4 ? t1[1] + (e & 1) * 64 : t2[1] + 2048 + (e & 1) * 128) + ((e >> 1) & 1) * ASB) = kvp[4 + e];
          PIN();
        }
    }
    if (n == 152 + 3 || n == 152 + 4) {
      al[2 * (n & 1)] *= QSCALE; al[2 * (n & 1) + 1] *= QSCALE;
      asm volatile("" : "+v"(al[2 * (n & 1)]), "+v"(al[2 * (n & 1) + 1]));
      if (n & 1) asm volatile("v_add_u32 %0, %1, %0" : "+v"(t2[1]) : "s"(QOFF));
      PIN();
    }
    outop(0, n - 152 - KO);
  });
  TT(13);
  if (wid == 0) STAMP(dk, 18);
#pragma unroll
  for (int k = 16 - KO; k <= 16; ++k) outop(0, k);
  __hip_atomic_fetch_add(LDS_AT(unsigned, t3[1]), 1u, __ATOMIC_RELAXED, __HIP_MEMORY_SCOPE_WORKGROUP);
  TT(14);
  asm volatile("s_waitcnt vmcnt(0)" ::: "memory");
  signal(1u);
  TT(15);
  if (wid == 0) STAMP(dk, 19);
  }
  PIN();
  {
  const int lane = fresh_lane(), tid = wid * 64 + lane;
  LAYER_HERE;
  const int dk = LAY2 ? 6 : 2;
  const int g = wid >> 2, hh = (wid >> 1) & 1, qg = wid & 1;
  const int head = tni * 2 + hh, qt = tmi * 4 + qg * 2;
  unsigned qaa = lbase + QOFF + (hh * 2 + qg) * 4096 + lane * 16 + (g ? 0 : 2048), qab = qaa ^ 2048;
  const unsigned wbase = lbase + (hh * 2 + g) * 4096 + lane * 16;
  auto sptr = [&](int sl) { unsigned a = wbase + sl * ASB; asm volatile("" : "+v"(a)); return LDS_AT(const s16x8, a); };
  auto rd = [&](__attribute__((address_space(3))) const s16x8* sp, int off) { return sp[off >> 4]; };
  auto sl0 = sptr(0);
  const unsigned arr_a = lane == 0 ? lbase + FLG + 16 : lbase + DUM + lane * 4;
  auto arrive = [&]() { __hip_atomic_fetch_add(LDS_AT(unsigned, arr_a), 1u, __ATOMIC_RELAXED, __HIP_MEMORY_SCOPE_WORKGROUP); };
  unsigned arr_w = lane == 0 ? lbase + FLG + 8 + g * 4 : lbase + DUM + lane * 4;
  unsigned arr_i = 1u << (8 * (wid & 3));
  auto arrive_w = [&]() { __hip_atomic_fetch_add(LDS_AT(unsigned, arr_w), arr_i, __ATOMIC_RELAXED, __HIP_MEMORY_SCOPE_WORKGROUP); };
  s16x8 sel;
#pragma unroll
  for (int e = 0; e < 8; ++e) sel[e] = ((lane & 7) == (e & 4)) ? (short)0x3F80 : (short)0;
  int sidx = 0xDDDDDDDD;
  f32x16 oa, ob, nn, sa, sb;
  f32x4 la = {0.f, 0.f, 0.f, 0.f}, lb = {0.f, 0.f, 0.f, 0.f};
#pragma unroll
  for (int e = 0; e < 16; ++e) { oa[e] = 0.f; ob[e] = 0.f; sa[e] = 0.f; sb[e] = 0.f; }
  asm volatile("" : "+v"(sidx), "+v"(sel), "+v"(oa), "+v"(ob), "+v"(la), "+v"(lb), "+v"(qaa), "+v"(qab), "+v"(arr_w), "+v"(arr_i));
  PIN();
  s16x8 qa0, qa1, qb0, qb1, k00, k01, v00, v01, v10, v11, k10, k11;
  u32x4 fw;
  auto first = [&]() {
    fw = lds_rd4(lbase + FLG + 16);
    asm volatile("" ::: "memory");
    k00 = rd(sl0, 0); qa0 = *LDS_AT(const s16x8, qaa); qb0 = *LDS_AT(const s16x8, qab);
    k01 = rd(sl0, 1024); qa1 = *LDS_AT(const s16x8, qaa + 1024); qb1 = *LDS_AT(const s16x8, qab + 1024);
    v00 = rd(sl0, 2048); v01 = rd(sl0, 3072);
    PIN();
  };
  first();
  if (__builtin_amdgcn_readfirstlane(fw[0]) < 4u) {
#pragma unroll 1
    for (int i = 0; i < SPIN_MAX; ++i) {
      if (__builtin_amdgcn_readfirstlane(*LDS_AT(const volatile unsigned, lbase + FLG + 16)) >= 4u) break;
      __builtin_amdgcn_s_sleep(ASLP);
    }
    asm volatile("" ::: "memory");
    first();
  }
  const unsigned tag = __builtin_amdgcn_readfirstlane(fw[1]);
  const int idx = LAY2 ? (2047 - (int)(__builtin_amdgcn_readfirstlane(fw[3]) & 0xffffu)) & 2047 : -64;
  float ma;
  const float zoff = __uint_as_float(__builtin_amdgcn_readfirstlane(LAY2 && g == ((idx >> 6) & 1) ? 0u : 0xff800000u));
  {
    sa = MFMA32(k00, qa0, sa); sb = MFMA32(k00, qb0, sb);
    sa = MFMA32(k01, qa1, sa); sb = MFMA32(k01, qb1, sb);
    float mb = sb[0];
    ma = sa[0];
#pragma unroll
    for (int e = 1; e < 16; ++e) { ma = fmaxf(ma, sa[e]); mb = fmaxf(mb, sb[e]); }
    ma = fmaxf(ma, mb);
    const auto sw = __builtin_amdgcn_permlane32_swap(__float_as_uint(ma), __float_as_uint(ma), false, false);
    ma = fmaxf(__uint_as_float(sw[0]), __uint_as_float(sw[1]));
    f32x2 n2 = {-ma, -ma};
    asm volatile("" : "+v"(n2));
#pragma unroll
    for (int e = 0; e < 16; ++e) nn[e] = n2[e & 1];
    sa = MFMA32(k00, qa0, nn); sa = MFMA32(k01, qa1, sa);
    la[0] = lb[0] = -__uint_as_float(__float_as_uint(__builtin_amdgcn_exp2f(zoff - ma)) & 0xffff0000u);
  }
#define SB() __builtin_amdgcn_sched_barrier(0)
#define EX(i) sc[i] = __builtin_amdgcn_exp2f(sc[i])
  auto unit = [&](f32x16& sc, f32x16& sn, const s16x8& kn0, const s16x8& kn1, const s16x8& qn0, const s16x8& qn1,
                  f32x16& op, f32x4& lp, const s16x8& vp,
                  f32x16& oc, f32x4& lc, const s16x8& vc, auto&& mid) {
    lp = SMF16(sel, PHI(sn), lp); SB();
    op = SMF32(vp, PHI(sn), op); SB();
    EX(0); EX(1); EX(2); EX(3); SB();
    sn = MFMA32(kn0, qn0, nn); SB();
    EX(4); EX(5); EX(6); EX(7); SB();
    sn = MFMA32(kn1, qn1, sn); SB();
    mid();
    EX(8); EX(9); EX(10); EX(11); SB();
    lc = SMF16(sel, PLO(sc), lc); SB();
    oc = SMF32(vc, PLO(sc), oc); SB();
    EX(12); EX(13); EX(14); EX(15); SB();
    asm volatile("" : "+v"(sc));
  };
  sl0 = sptr(1);
  unsigned f0 = 0u;
  auto check = [&](unsigned need) {
    if (__builtin_expect(__builtin_amdgcn_readfirstlane(f0) < need, 0)) {
#pragma unroll 1
      for (int i = 0; i < SPIN_MAX; ++i) {
        if (__builtin_amdgcn_readfirstlane(*LDS_AT(const volatile unsigned, lbase + FLG)) >= need) break;
        __builtin_amdgcn_s_sleep(1);
      }
    }
    asm volatile("" ::: "memory");
  };
#define PAT 0x22221110
#define SAW8(i) do { if (((PAT >> (28 - 4 * (i))) & 3) != ((PAT >> ((32 - 4 * (i)) & 31)) & 3)) { __builtin_amdgcn_s_setprio((PAT >> (28 - 4 * (i))) & 3); SB(); } } while (0)
#define SAW(i) do { if (odd) SAW8(4 + (i)); else SAW8(i); } while (0)
  auto three = [&](int st, bool odd) {
    SAW(0);
    unit(sa, sb, k00, k01, qb0, qb1, ob, lb, v11, oa, la, v00, [&]() { k10 = rd(sl0, 0); k11 = rd(sl0, 1024); SB(); });
    SAW(1);
    unit(sb, sa, k10, k11, qa0, qa1, oa, la, v01, ob, lb, v00, [&]() { v10 = rd(sl0, 2048); v11 = rd(sl0, 3072); SB(); });
    sl0 = sptr((2 * st + 2) & 7);
    SB();
    SAW(2);
    unit(sa, sb, k10, k11, qb0, qb1, ob, lb, v01, oa, la, v10, [&]() { k00 = rd(sl0, 0); k01 = rd(sl0, 1024); SB(); });
  };
  auto fourth = [&](int st, bool odd) {
    SAW(3);
    unit(sb, sa, k00, k01, qa0, qa1, oa, la, v11, ob, lb, v10, [&]() {
      v00 = rd(sl0, 2048); v01 = rd(sl0, 3072);
      f0 = *LDS_AT(const volatile unsigned, lbase + FLG);
      SB();
      arrive_w();
      SB(); });
    check(st >= 14 ? 32u : 2u * st + 5u);
    sl0 = sptr((2 * st + 3) & 7);
    SB();
  };
  SB();
  SAW8(0);
  {
    f32x16& sc = sa;
    sb = MFMA32(k00, qb0, nn); SB();
    k10 = rd(sl0, 0); k11 = rd(sl0, 1024); SB();
    sb = MFMA32(k01, qb1, sb); SB();
    EX(0); EX(1); EX(2); EX(3); EX(4); EX(5); EX(6); EX(7); EX(8); SB();
    oa = SMF32(v00, PLO(sa), oa); SB();
    EX(9); EX(10); EX(11); SB();
    la = SMF16(sel, PLO(sa), la); SB();
    EX(12); EX(13); EX(14); EX(15); SB();
    asm volatile("" : "+v"(sa));
  }
  SAW8(1);
  unit(sb, sa, k10, k11, qa0, qa1, oa, la, v01, ob, lb, v00, [&]() { v10 = rd(sl0, 2048); v11 = rd(sl0, 3072); SB(); });
  sl0 = sptr(2);
  SB();
  SAW8(2);
  unit(sa, sb, k10, k11, qb0, qb1, ob, lb, v01, oa, la, v10, [&]() {});
  {
    f32x16& sc = sb;
    SAW8(3);
    la = SMF16(sel, PHI(sa), la); SB();
    oa = SMF32(v11, PHI(sa), oa); SB();
    EX(0); EX(1); EX(2); EX(3); EX(4); EX(5); EX(6); EX(7); EX(8); asm volatile("" : "+v"(sc)); SB();
    lb = SMF16(sel, PLO(sb), lb); SB();
    ob = SMF32(v10, PLO(sb), ob); SB();
    f0 = *LDS_AT(const volatile unsigned, lbase + FLG);
    asm volatile("" ::: "memory");
    k00 = rd(sl0, 0); k01 = rd(sl0, 1024); SB();
    EX(9); EX(10); EX(11); EX(12); EX(13); EX(14); EX(15); asm volatile("" : "+v"(sc)); SB();
    if (__builtin_amdgcn_readfirstlane(f0) < 3u) { check(3u); k00 = rd(sl0, 0); k01 = rd(sl0, 1024); }
    SB();
    v00 = rd(sl0, 2048); v01 = rd(sl0, 3072); SB(); arrive_w(); SB();
    sa = MFMA32(k00, qa0, nn); sa = MFMA32(k01, qa1, sa); asm volatile("" : "+v"(sa)); SB();
  }
  sl0 = sptr(3);
  SB();
  SAW8(4);
  unit(sa, sb, k00, k01, qb0, qb1, ob, lb, v11, oa, la, v00, [&]() {
    f0 = *LDS_AT(const volatile unsigned, lbase + FLG);
    asm volatile("" ::: "memory");
    k10 = rd(sl0, 0); k11 = rd(sl0, 1024); SB(); });
  if (__builtin_amdgcn_readfirstlane(f0) < 4u) { check(4u); k10 = rd(sl0, 0); k11 = rd(sl0, 1024); }
  SB();
  SAW8(5);
  unit(sb, sa, k10, k11, qa0, qa1, oa, la, v01, ob, lb, v00, [&]() { v10 = rd(sl0, 2048); v11 = rd(sl0, 3072); f0 = *LDS_AT(const volatile unsigned, lbase + FLG); SB(); });
  check(5u);
  sl0 = sptr(4);
  SB();
  SAW8(6);
  unit(sa, sb, k10, k11, qb0, qb1, ob, lb, v01, oa, la, v10, [&]() { k00 = rd(sl0, 0); k01 = rd(sl0, 1024); SB(); });
#pragma unroll 1
  for (int st = 2; st < 16; st += 2) { fourth(st - 1, true); three(st, false); fourth(st, false); three(st + 1, true); }
  SAW8(7);
  unsigned vr, tk, tv;
  u32x4 f4;
  auto table = [&]() {
    f4 = lds_rd4(lbase + FLG);
    k00 = *LDS_AT(const volatile s16x8, tk); k01 = *LDS_AT(const volatile s16x8, tk + 32);
    vr = *LDS_AT(const volatile u16, tv);
  };
  unsigned xw, xm;
  u32x2 ml;
  u32x4 o2[4];
  auto take = [&]() {
    ml = *LDS_AT(const volatile u32x2, xm);
#pragma unroll
    for (int e = 0; e < 4; ++e) o2[e] = lds_rd4(xw + e * 1024);
  };
  float pzt;
  int dso;
  f32x16& sc = sb;
#define EXP() do { asm volatile("" : "+v"(sc)); SB(); } while (0)
  oa = SMF32(v11, PHI(sa), oa); SB();
  tk = lbase + TAB + 128 + hh * 64 + (lane >> 5) * 16; tv = lbase + TAB + 256 + hh * 64 + (lane & 31) * 2;
  table();
  SB();
  EX(0); EX(1); EXP();
  la = SMF16(sel, PHI(sa), la); SB();
  EX(2); EX(3); EX(4); EXP();
  xw = wbase + (qg ? QOFF : 6 * ASB); xm = lbase + XML + wid * 512 + lane * 8;
  pzt = __builtin_amdgcn_exp2f(nn[0] + (zoff - 8.f));
  asm volatile("" : "+v"(xw), "+v"(xm), "+v"(pzt), "+v"(f4));
  SB();
  {
    const unsigned ph = LAY2 ? 0x79797979u : 0x80808080u, pg = qg ? 0u : 0x80u << (16 * hh + 8);
    auto ready = [&]() {
      return (((__builtin_amdgcn_readfirstlane(f4[1]) + ph) & 0x80808080u) == 0x80808080u) & (((__builtin_amdgcn_readfirstlane(g ? f4[3] : f4[2]) + 0x71717171u) & pg) == pg);
    };
    if (__builtin_expect(!ready(), 0)) {
#pragma unroll 1
      for (int i = 0; i < SPIN_MAX; ++i) {
        __builtin_amdgcn_s_sleep(1);
        table();
        if (ready()) break;
      }
    }
    asm volatile("" ::: "memory");
  }
  SB();
  sa = MFMA32(k00, qb0, nn); asm volatile("" : "+v"(sa)); SB();
#pragma unroll
  for (int e = 0; e < 4; ++e) *LDS_AT(volatile f32x4, xw + e * 1024) = f32x4{oa[4 * e], oa[4 * e + 1], oa[4 * e + 2], oa[4 * e + 3]};
  *LDS_AT(volatile u32x2, xm) = u32x2{__float_as_uint(ma), f2word(pzt <= la[0] ? la[0] : 0.f)};
  SB();
  EX(5); EX(6); EX(7); EXP();
  sa = MFMA32(k01, qb1, sa); asm volatile("" : "+v"(sa), "+v"(vr)); SB();
  ARGS_HERE;
  EX(8); EX(9); EX(10); EXP();
  ob = SMF32(v10, PLO(sb), ob); SB();
  EX(11); EX(12); EX(13); EXP();
  lb = SMF16(sel, PLO(sb), lb); SB();
  const bool used = !LAY2 || (qt + g) * 32 + (lane & 31) != idx;
  dso = used ? (((qt + g) * 32 + head) * 1024 + lane * 8) * 2 : S_ * D_ * 2;
  xw += g ? -4096 : 4096; xm += g ? -2048 : 2048;
  asm volatile("" : "+v"(dso), "+v"(xw), "+v"(xm));
  SB();
  EX(14); EX(15); EXP();
  take();
  SB();
  lb = SMF16(sel, PHI(sb), lb); SB();
  ob = SMF32(v11, PHI(sb), ob); SB();
  __builtin_amdgcn_s_setprio(EPRIO);
  TE(0, 0); TE(1, 0); TE(2, 0); TE(3, 0); TE(7, 0);
  float lsb = lb[0];
  if (LAY2) {
    sa[0] = __builtin_amdgcn_exp2f(sa[0]);
    ob = SMF32((u32x4{lane < 32 ? vr : 0u, 0u, 0u, 0u}), PLO(sa), ob);
    lsb += __uint_as_float(__float_as_uint(sa[0]) & 0xffff0000u);
    lsb = pzt <= lsb ? lsb : 0.f;
  }
  const __amdgpu_buffer_rsrc_t rso = __builtin_amdgcn_make_buffer_rsrc(Of, 0, S_ * D_ * 2, 0x00020000);
  TE(0, 1);
  if (__builtin_amdgcn_ballot_w64(ml[1] == SENT) != 0ull) {
#pragma unroll 1
    for (int i = 0; i < SPIN_MAX; ++i) {
      __builtin_amdgcn_s_sleep(XSLP);
      if (!__builtin_amdgcn_ballot_w64(*LDS_AT(const volatile unsigned, xm + 4) == SENT)) break;
    }
    asm volatile("s_waitcnt lgkmcnt(0)" ::: "memory");
    take();
  }
  arrive();
  TE(0, 2);
  TE(0, 3); TE(1, 1);
  {
    const float m2 = __uint_as_float(ml[0]), l2 = __uint_as_float(ml[1]);
    const float mx = fmaxf(ma, m2);
    const float f1 = __builtin_amdgcn_exp2f(ma - mx), f2 = __builtin_amdgcn_exp2f(m2 - mx);
    const float inv = frcp(lsb * f1 + l2 * f2);
    const float c1 = f1 * inv, c2 = f2 * inv;
    f32x2 r[8];
#pragma unroll
    for (int e = 0; e < 8; ++e) {
      const unsigned x0 = o2[e >> 1][(e & 1) * 2], x1 = o2[e >> 1][(e & 1) * 2 + 1];
      r[e] = f32x2{__uint_as_float(x0) * c2, __uint_as_float(x1) * c2};
    }
#pragma unroll
    for (int e = 0; e < 8; ++e) r[e] = f32x2{ob[2 * e], ob[2 * e + 1]} * f32x2{c1, c1} + r[e];
    auto put = [&]() {
#pragma unroll
      for (int sx = 0; sx < 2; ++sx)
        __builtin_amdgcn_raw_buffer_store_b128(u32x4{pack2(r[4 * sx][0], r[4 * sx][1]), pack2(r[4 * sx + 1][0], r[4 * sx + 1][1]),
            pack2(r[4 * sx + 2][0], r[4 * sx + 2][1]), pack2(r[4 * sx + 3][0], r[4 * sx + 3][1])}, rso, dso + sx * 1024, 0, 16);
    };
    put();
    PIN();
    __builtin_amdgcn_s_setprio(0);
    PIN();
    f32x2 chk = {lsb - __builtin_amdgcn_fmed3f(lsb, 8.6736174e-19f, 1.2676506e30f), l2 - __builtin_amdgcn_fmed3f(l2, 8.6736174e-19f, 1.2676506e30f)};
#pragma unroll
    for (int e = 0; e < 8; ++e) chk = r[e] * f32x2{0.f, 0.f} + chk;
    if (__builtin_expect(__builtin_amdgcn_ballot_w64(used && !(chk[0] + chk[1] == 0.f)) != 0ull, 0)) {
      float l;
      attn_safe(KV, head * 64, qb0, qb1, ob, l, LAY2 ? head * 64 + (idx >> 5) : -1, idx & 31, lbase + TAB + 128 + hh * 64);
      const float inv2 = 1.0f / l;
#pragma unroll
      for (int e = 0; e < 8; ++e) r[e] = f32x2{ob[2 * e] * inv2, ob[2 * e + 1] * inv2};
      asm volatile("s_waitcnt vmcnt(0)" ::: "memory");
      put();
    }
    unsigned pp = 0u;
    if (LAY2) {
      const unsigned ua = lbase + UGL + hh * 128 + (lane >> 5) * 16;
      f32x2 du = {0.f, 0.f}, dn = {0.f, 0.f};
#pragma unroll
      for (int q = 0; q < 4; ++q) {
        const f32x4 uu = *LDS_AT(const f32x4, ua + q * 32), gg = *LDS_AT(const f32x4, ua + 256 + q * 32);
        du = r[2 * q] * f32x2{uu[0], uu[1]} + du; du = r[2 * q + 1] * f32x2{uu[2], uu[3]} + du;
        const f32x2 t0 = r[2 * q] * f32x2{gg[0], gg[1]}, t1 = r[2 * q + 1] * f32x2{gg[2], gg[3]};
        dn = t0 * t0 + dn; dn = t1 * t1 + dn;
      }
      const auto a = __builtin_amdgcn_permlane32_swap(__float_as_uint(du[0] + du[1]), __float_as_uint(du[0] + du[1]), false, false);
      const auto c = __builtin_amdgcn_permlane32_swap(__float_as_uint(dn[0] + dn[1]), __float_as_uint(dn[0] + dn[1]), false, false);
      pp = f2word(__uint_as_float(pack2(__uint_as_float(a[0]) + __uint_as_float(a[1]), __uint_as_float(c[0]) + __uint_as_float(c[1]))));
    }
    __builtin_amdgcn_raw_buffer_store_b32(pp, __builtin_amdgcn_make_buffer_rsrc(hand + 2 * HAND_N + PP_O, 0, LAY2 ? H_ * S_ * 4 : 0, 0x00020000),
                                          lane < 32 ? (head * S_ + (qt + g) * 32 + lane) * 4 : H_ * S_ * 4, 0, 16);
    PIN();
  }
  TE(1, 2);
  u32x4 xres[2];
  oproj_tail(LAY2, tid, lbase, lbase + FLG, lbase + SPM + 144, omi, oni, idx, gran + GR_O + ((LAY2 ? 16 : 0) + tmi) * (64 * RO) + tni,
             gran + GR_X + omi * (128 * RX) + oni * 4, tag, Of, xout, A, w_r, stats_out, stats, hand, xres TEPASS);
  if (LAY2) {
    const int row = omi * 128 + (wid & 3) * 32 + (wid >> 2) * 16 + (lane >> 3), col = oni * 64 + (lane & 7) * 8;
    unsigned md[2], best = 0u;
    const __amdgpu_buffer_rsrc_t rsk = __builtin_amdgcn_make_buffer_rsrc(hand + HAND_N, 0, (HAND_N + PS_N) * 4, 0x00020000);
    f32x2 xf[2][4];
#pragma unroll
    for (int p = 0; p < 2; ++p)
#pragma unroll
      for (int e = 0; e < 4; ++e) {
        xf[p][e] = f32x2{__uint_as_float(xres[p][e] << 16), __uint_as_float(xres[p][e] & 0xffff0000u)};
        asm volatile("" : "+v"(xf[p][e]));
      }
    LOGDEF;
    LOG();
    const bool rtr = (oni & 7) == 7;
    {
      const int rl = (wid & 3) * 32 + (wid >> 2) * 16 + (lane & 15), rr = omi * 128 + rl;
      int src = (HAND_N + ((omi * 16 + (lane >> 4) * 4) * 128 + rl) * 4) * 4;
      int ko = rtr && (wid >> 2) == (oni >> 3) && lane < NREP ? lane * 1024 + (rr >> 4) * 8 : (HAND_N + PS_N) * 4;
      int bp = (lane >> 3) * 4;
      const float wsum = *LDS_AT(const volatile float, lbase + SPM + 132);
      asm volatile("" : "+v"(src), "+v"(ko), "+v"(bp));
      u32x4 v[4];
      __builtin_amdgcn_s_sleep(MPRE);
#pragma unroll 1
      for (int spin = 0; spin < SPIN_MAX; ++spin) {
        LOG();
#pragma unroll
        for (int i = 0; i < 4; ++i) v[i] = __builtin_amdgcn_raw_buffer_load_b128(rsk, src + (i & 1) * 2048, (i >> 1) * 4096, 16);
        unsigned mx = 0u;
#pragma unroll
        for (int i = 0; i < 4; ++i) mx = umax(umax(umax(mx, v[i][0]), v[i][1]), v[i][2]);
        if (none(mx == SENT)) break;
        __builtin_amdgcn_s_sleep(MSLP);
        asm volatile("" ::: "memory");
      }
      LOGAT(12);
      if (rtr) __builtin_amdgcn_s_setprio(3); else __builtin_amdgcn_s_setprio(LNPRIO);
      double sm[3];
#pragma unroll
      for (int c = 0; c < 3; ++c) {
        const double a = ((double)__uint_as_float(v[0][c]) + (double)__uint_as_float(v[1][c])) + ((double)__uint_as_float(v[2][c]) + (double)__uint_as_float(v[3][c]));
        u32x2 q = __builtin_bit_cast(u32x2, a);
        const auto lo = __builtin_amdgcn_permlane32_swap(q[0], q[0], false, false), hi = __builtin_amdgcn_permlane32_swap(q[1], q[1], false, false);
        q = __builtin_bit_cast(u32x2, __builtin_bit_cast(double, u32x2{lo[0], hi[0]}) + __builtin_bit_cast(double, u32x2{lo[1], hi[1]}));
        const auto l2 = __builtin_amdgcn_permlane16_swap(q[0], q[0], false, false), h2 = __builtin_amdgcn_permlane16_swap(q[1], q[1], false, false);
        sm[c] = __builtin_bit_cast(double, u32x2{l2[0], h2[0]}) + __builtin_bit_cast(double, u32x2{l2[1], h2[1]});
      }
      float mean, var, mb, den;
      ln_from_sums(sm[0], sm[1], mean, var, mb, den);
      const unsigned mdr = f2word(__uint_as_float((__float_as_uint(mb) >> 16) | (__float_as_uint(den) & 0xffff0000u)));
      md[0] = __builtin_amdgcn_ds_bpermute(bp, mdr); md[1] = __builtin_amdgcn_ds_bpermute(bp + 32, mdr);
      if (rtr) {
        const float r = (float)fma(-(double)mb, (double)wsum, sm[2]) * frcp(den);
        const unsigned rb = pack2(r, r);
        unsigned rkey = (rb ^ ((unsigned)((int)rb >> 31) | 0x80000000u)) & 0xffff0000u;
        if (r != r) rkey = 0u;
        const unsigned top = wave_umax(rkey | (unsigned)(2047 - rr));
        const unsigned mdt = __builtin_amdgcn_readlane(mdr, (2047 - (int)(top & 0xffffu)) & 15);
        __builtin_amdgcn_raw_buffer_store_b64(u32x2{top, mdt}, rsk, ko, 0, 16);
        PIN();
        __builtin_amdgcn_s_setprio(LNPRIO);
        __hip_atomic_fetch_add(LDS_AT(unsigned, lane == 0 ? lbase + SPM + 128 : lbase + DUM + lane * 4), 1u, __ATOMIC_RELAXED, __HIP_MEMORY_SCOPE_WORKGROUP);
        if (wid == 2 || wid == 5) STAMP(8, 6);
      }
      LOGAT(13);
    }
    auto keys = [&](const u32x4& v) __attribute__((always_inline)) {
      if (none(umax(umax(v[0], v[1]), umax(v[2], v[3])) == SENT)) {
        best = wave_umax(umax(v[0], v[2])) | 0x8000u;
        *LDS_AT(volatile u32x2, lbase + FLG + 40) = u32x2{best, (unsigned)__builtin_amdgcn_readlane(v[0] > v[2] ? v[1] : v[3], ((2047 - (int)(best & 0x7ffu)) >> 5) & 63)};
        STAMP(8, 11);
      }
    };
    auto ln_rows = [&]() __attribute__((always_inline)) {
#pragma unroll
      for (int p = 0; p < 2; ++p) {
        const float mb_ = __uint_as_float(md[p] << 16), rin = frcp(__uint_as_float(md[p] & 0xffff0000u));
#pragma unroll
        for (int e = 0; e < 4; ++e) {
          const f32x2 h = (xf[p][e] - f32x2{mb_, mb_}) * f32x2{rin, rin} + xf[p][e];
          xres[p][e] = pack2(h[0], h[1]);
        }
      }
      asm volatile("" : "+v"(xres[0]), "+v"(xres[1]));
    };
    if (wid == 0) STAMP(8, 12);
    ln_rows();
    LOGAT(14);
    const __amdgpu_buffer_rsrc_t rsu = __builtin_amdgcn_make_buffer_rsrc(out, 0, S_ * D_ * 2, 0x00020000);
    unsigned fr1 = SENT;
#pragma unroll 1
    for (int i = 0; i < 4 * SPIN_MAX && fr1 == SENT; ++i) fr1 = __builtin_amdgcn_readfirstlane(*LDS_AT(const volatile unsigned, lbase + SPM + 144));
    const int fr2 = (fr1 >> 16) & 0x7ffu;
    fr1 &= 0x7ffu;
    unsigned tmx = __builtin_amdgcn_readfirstlane(*LDS_AT(const volatile unsigned, lbase + SPM + 136)) + KDT;
    if (tmx - (unsigned)__builtin_amdgcn_s_memrealtime() > 1024u) tmx = 0u;
    auto due = [&]() { return tmx == 0u || (int)((unsigned)__builtin_amdgcn_s_memrealtime() - tmx) >= 0; };
    lk_set(15);
    const bool held = !none((row | 8) == ((int)fr1 | 8) || (row | 8) == (fr2 | 8));
#pragma unroll
    for (int p = 0; p < 2; ++p) __builtin_amdgcn_raw_buffer_store_b128(xres[p], rsu, row + 8 * p != (int)fr1 && row + 8 * p != fr2 ? ((row + 8 * p) * D_ + col) * 2 : S_ * D_ * 2, 0, OAUX);
    PIN();
    if (wid == 6) {
      if (rtr) {
#pragma unroll 1
        for (int i = 0; i < SPIN_MAX; ++i) {
          if (__builtin_amdgcn_readfirstlane(*LDS_AT(const volatile unsigned, lbase + SPM + 128)) == 7u) break;
          __builtin_amdgcn_s_sleep(1);
        }
        asm volatile("" ::: "memory");
      }
      while (!due()) __builtin_amdgcn_s_sleep(1);
    }
#pragma unroll 1
    for (int i = 0; i < 4 * SPIN_MAX && !best; ++i) {
      if (wid == 6) {
        LOG();
        keys(__builtin_amdgcn_raw_buffer_load_b128(rsk, (bid % NREP) * 1024 + lane * 16, 0, 16));
        if (!best) __builtin_amdgcn_s_sleep(KSLP);
      } else {
        best = __builtin_amdgcn_readfirstlane(*LDS_AT(const volatile unsigned, lbase + FLG + 40));
        if (!best) __builtin_amdgcn_s_sleep(1);
      }
      asm volatile("" ::: "memory");
    }
    const int idx2 = 2047 - (int)(best & 0x7ffu);
    if (held) {
#pragma unroll
      for (int p = 0; p < 2; ++p) {
        const int r = row + 8 * p;
        __builtin_amdgcn_raw_buffer_store_b128(xres[p], rsu, (r == (int)fr1 || r == fr2) && r != idx2 ? (r * D_ + col) * 2 : S_ * D_ * 2, 0, OAUX);
      }
    }
    if (__builtin_expect(idx2 != (int)fr1 && idx2 != fr2, 0) && !none((row | 8) == (idx2 | 8))) {
#pragma unroll
      for (int p = 0; p < 2; ++p)
        __builtin_amdgcn_raw_buffer_store_b128(u32x4{pack2(xf[p][0][0], xf[p][0][1]), pack2(xf[p][1][0], xf[p][1][1]), pack2(xf[p][2][0], xf[p][2][1]), pack2(xf[p][3][0], xf[p][3][1])},
            __builtin_amdgcn_make_buffer_rsrc(xout, 0, 3 * D_ * 2, 0x00020000), row + 8 * p == idx2 ? (2 * D_ + col) * 2 : S_ * D_ * 2, 0, 16);
      asm volatile("s_waitcnt vmcnt(0)" ::: "memory");
      if (lane == 0) st_ag(gran + GR_F + oni * 32, tag);
    }
  }
  }
}
struct Args2 { Args l[2]; };
extern "C" __global__ __launch_bounds__(768, 3) void layers_kernel(const Args2 args) {
  __shared__ __attribute__((aligned(128))) char smem[163840];
  const unsigned long long t_entry = 0;
  const int tid = threadIdx.x, bid = blockIdx.x;
  int wid = __builtin_amdgcn_readfirstlane(tid >> 6);
  asm volatile("" : "+s"(wid));
  const ArgP ap = (ArgP)__builtin_amdgcn_kernarg_segment_ptr();
  layer_body(wid, bid, (unsigned)(uintptr_t)smem, ap, t_entry);
  LAYER_HERE;
  if (!LAY2) {
    asm volatile(AGAIN :: "{s[0:1]}"(ap + 1), "{s2}"(bid), "{v0}"(wid * 64) : "s90", "s91", "scc", "memory");
    __builtin_unreachable();
  }
}

extern "C" __global__ void end_marker_kernel() {}

extern "C" void kernel_launch(void* const* d_in, const int* in_sizes, int n_in, void* d_out, int out_size,
                              void* d_ws, size_t ws_size, hipStream_t stream) {
  const u16* x0 = (const u16*)d_in[0];
  const u16* wqkv = (const u16*)d_in[1];
  const u16* wo = (const u16*)d_in[2];
  const u16* w_r = (const u16*)d_in[4];
  const u16* wg = (const u16*)d_in[5];
  const u16* wu = (const u16*)d_in[6];
  const u16* wd = (const u16*)d_in[7];
  u16* out = (u16*)d_out;

  char* ws = (char*)d_ws;
  const size_t MB4 = (size_t)S_ * D_ * 2;
  u16* kv = (u16*)(ws);
  u16* o = (u16*)(ws + 2 * MB4);
  u16* x1 = (u16*)(ws + 3 * MB4);
  unsigned* gran = (unsigned*)(ws + 4 * MB4);
  float* csf = (float*)(gran + GR_F + 16 * 32);
  double* stats = (double*)(csf + 8192);
  unsigned* hand = (unsigned*)(stats + 2 * STAT_N);
  static_assert((GR_F + 16 * 32) % 32 == 0 && (size_t)(GR_F + 16 * 32 + 8192 + 4 * STAT_N + 2 * HAND_N + PS_N) * 4 <= 6u << 20, "");
  u16* x3 = (u16*)(hand + 2 * HAND_N) - 3 * D_;
  u16* wc = (u16*)((((uintptr_t)ws + 4 * MB4 + (6u << 20) + 2047) & ~(uintptr_t)2047) + (((uintptr_t)wqkv + 128) & 2047));
  Args2 a;
  for (int l = 0; l < 2; ++l) {
    Args& r = a.l[l];
    r.lay = l; r.a = l ? x1 : x0; r.kv = kv; r.o = o; r.xo = l ? x3 : x1; r.so = l ? stats + STAT_N : stats;
    r.W = wqkv; r.wc = wc; r.wg = wg; r.wu = wu; r.wd = wd; r.wo = wo; r.w_r = w_r; r.gran = gran; r.csf = csf; r.stats = stats; r.hand = hand; r.out = out;
  }
  layers_kernel<<<256, 768, 0, stream>>>(a);
}
```

```cpp
#include <hip/hip_runtime.h>
#include <hip/hip_bf16.h>
#include <stdint.h>
#include <type_traits>
#include <utility>

typedef unsigned short u16;
typedef short s16x4 __attribute__((ext_vector_type(4)));
typedef short s16x8 __attribute__((ext_vector_type(8)));
typedef float f32x16 __attribute__((ext_vector_type(16)));
typedef float f32x4 __attribute__((ext_vector_type(4)));
typedef unsigned int u32x4 __attribute__((ext_vector_type(4)));
typedef unsigned int u32x2 __attribute__((ext_vector_type(2)));
typedef __bf16 bf16x8 __attribute__((ext_vector_type(8)));
typedef __bf16 bf16x16 __attribute__((ext_vector_type(16)));

__device__ __forceinline__ int fresh_lane() {
  int l;
  asm volatile("v_mbcnt_lo_u32_b32 %0, -1, 0\n\tv_mbcnt_hi_u32_b32 %0, -1, %0" : "=v"(l));
  return l;
}
#define STAMP(k, i)
#define STAMPV(k, i, v)
#define LOGDEF
#define LOG()
#define LOGAT(k)
#define lk_set(k)
#define LOGOUT(slot)
#define TE(grp, k)
#define TEARG
#define TEPASS
constexpr int S_ = 2048, D_ = 1024, H_ = 32, F_ = 3584;

__device__ __forceinline__ int kpos(int r) { return (r & ~12) | ((r & 4) << 1) | ((r & 8) >> 1); }
__device__ __forceinline__ float bf2f(u16 v) { return __uint_as_float(((unsigned)v) << 16); }
__device__ __forceinline__ u16 f2bf(float f) {
  __hip_bfloat16 b = __float2bfloat16(f);
  return *reinterpret_cast<u16*>(&b);
}
typedef float f32x2 __attribute__((ext_vector_type(2)));
typedef __bf16 bf16x2 __attribute__((ext_vector_type(2)));
__device__ __forceinline__ unsigned pack2(float lo, float hi) {
  f32x2 v = {lo, hi};
  return __builtin_bit_cast(unsigned, __builtin_convertvector(v, bf16x2));
}
__device__ __forceinline__ float rbf(float f) { return __uint_as_float(pack2(f, f) << 16); }
__device__ __forceinline__ void rbf2(float& a, float& b) {
  const unsigned p = pack2(a, b);
  a = __uint_as_float(p << 16); b = __uint_as_float(p & 0xffff0000u);
}

__device__ __forceinline__ float fadd(float a, float b) { float r = a + b; asm("" : "+v"(r)); return r; }
template <int CTRL, int RMASK>
__device__ __forceinline__ float dpp_add(float v) {
  const int t = __builtin_amdgcn_update_dpp(0, __float_as_int(v), CTRL, RMASK, 0xf, false);
  return v + __int_as_float(t);
}
__device__ __forceinline__ float wave_sum(float v) {
  v = dpp_add<0x111, 0xf>(v);
  v = dpp_add<0x112, 0xf>(v);
  v = dpp_add<0x114, 0xf>(v);
  v = dpp_add<0x118, 0xf>(v);
  v = dpp_add<0x142, 0xa>(v);
  v = dpp_add<0x143, 0xc>(v);
  return __int_as_float(__builtin_amdgcn_readlane(__float_as_int(v), 63));
}

template <int CTRL, int RMASK>
__device__ __forceinline__ unsigned dpp_umax(unsigned v) {
  const unsigned t = (unsigned)__builtin_amdgcn_update_dpp(0, (int)v, CTRL, RMASK, 0xf, false);
  return v > t ? v : t;
}
__device__ __forceinline__ unsigned wave_umax(unsigned v) {
  v = dpp_umax<0x111, 0xf>(v); v = dpp_umax<0x112, 0xf>(v); v = dpp_umax<0x114, 0xf>(v); v = dpp_umax<0x118, 0xf>(v);
  v = dpp_umax<0x142, 0xa>(v); v = dpp_umax<0x143, 0xc>(v);
  return (unsigned)__builtin_amdgcn_readlane((int)v, 63);
}

#define NREP 32
constexpr unsigned SENT = 0xFFFFFFFFu;
constexpr int GRAN_N = 256 * NREP;
constexpr int Y_N = F_ / 16 * D_;
constexpr int X2_N = D_ / 2;
constexpr int XS_N = 128;
constexpr int P4_N = 3 * D_ * 16;
constexpr int PA_N = H_ * 8 * 64;
constexpr int HAND_N = GRAN_N + Y_N + X2_N + XS_N + P4_N + PA_N;
constexpr int MD_O = 16 * 16 * 128 * 4;
constexpr int PK_O = MD_O;
constexpr int PP_O = PK_O + 128 * NREP;
constexpr int Y3_O = PP_O + H_ * S_;
constexpr int PS_N = Y3_O + Y_N;
constexpr int GR_ROW = 64, GR_COL = GR_ROW + 4 * S_, GR_KV = GR_COL + 16 * 16 * 192;
#define RO 16
#define RK 16
#define RX 1
constexpr int GR_O = GR_KV + 2 * 16 * 32 * RK, GR_X = GR_O + 2 * 16 * 64 * RO, GR_Q = GR_X + 16 * 128 * RX, GR_N = GR_Q + 2 * 256 * 32;
constexpr unsigned QG = 30u << 20;
constexpr int GR_F = GR_N + 256 * 64;
constexpr int STAT_N = 3 * S_;

__device__ __forceinline__ void st_ag(unsigned* p, unsigned v) { __hip_atomic_store(p, v, __ATOMIC_RELAXED, __HIP_MEMORY_SCOPE_AGENT); }
__device__ __forceinline__ unsigned ld_ag(const unsigned* p) { return __hip_atomic_load(p, __ATOMIC_RELAXED, __HIP_MEMORY_SCOPE_AGENT); }
__device__ __forceinline__ double ld_ag(const double* p) { return __hip_atomic_load(p, __ATOMIC_RELAXED, __HIP_MEMORY_SCOPE_AGENT); }
__device__ __forceinline__ unsigned f2word(float f) { const unsigned u = __float_as_uint(f); return u < 0xFFFFFFFEu ? u : 0xFFFFFFFEu; }
constexpr int SPIN_MAX = 1 << 18;
__device__ __forceinline__ unsigned umax(unsigned a, unsigned b) { return a > b ? a : b; }
__device__ __forceinline__ bool none(bool p) { return __builtin_amdgcn_ballot_w64(p) == 0ull; }

__device__ __forceinline__ void ln_from_sums(double s1, double s2, float& mean, float& var, float& mb, float& den) {
  const double md = s1 * (1.0 / 1024.0);
  const double vd = fma(-md, md, s2 * (1.0 / 1024.0));
  mean = (float)md; var = fmaxf((float)vd, 0.f);
  mb = rbf(mean);
  den = rbf(__builtin_amdgcn_sqrtf(rbf(rbf(var) + 1e-5f)));
}
__device__ __forceinline__ float frcp(float x) { return __builtin_amdgcn_rcpf(x); }
__device__ __forceinline__ void ln_apply2(float& a, float& b, float mb, float den, float rin) {
  a -= mb; b -= mb;
  rbf2(a, b);
  float qa = a * rin, qb = b * rin;
  qa = fmaf(fmaf(-qa, den, a), rin, qa);
  qb = fmaf(fmaf(-qb, den, b), rin, qb);
  rbf2(qa, qb);
  a = qa; b = qb;
}

#define CODE_ADDR(SYM, out64)                                                                                          \
  do {                                                                                                                 \
    unsigned lo_, hi_;                                                                                                 \
    asm volatile("s_getpc_b64 s[90:91]\n\ts_add_u32 s90, s90, " SYM "@rel32@lo+4\n\ts_addc_u32 s91, s91, " SYM          \
                 "@rel32@hi+12\n\ts_mov_b32 %0, s90\n\ts_mov_b32 %1, s91"                                             \
                 : "=s"(lo_), "=s"(hi_) :: "s90", "s91", "scc");                                                        \
    out64 = ((unsigned long long)hi_ << 32) | lo_;                                                                     \
  } while (0)

#define QNX 4
constexpr int QMX = 32 / QNX, QNG = 16 / QNX, QMG = 8 / QNG;

__device__ __forceinline__ unsigned touch(const void* p) { return *reinterpret_cast<const unsigned*>(p); }

__device__ __forceinline__ unsigned touch_code(unsigned long long lo, unsigned long long hi, int pc, int lane) {
  const unsigned long long p = lo + (unsigned)(pc * 8192 + lane * 128);
  return p + 4 <= hi ? *reinterpret_cast<__attribute__((address_space(1))) const unsigned*>(p) : 0u;
}

template <bool SC = false>
__device__ __forceinline__ void glds16s(const void* sbase, unsigned voff, unsigned lds_dst) {
  unsigned keep;
  if (SC)
    asm volatile("s_mov_b32 %0, m0\n\ts_mov_b32 m0, %3\n\ts_nop 2\n\tglobal_load_lds_dwordx4 %1, %2 sc1\n\ts_mov_b32 m0, %0"
                 : "=&s"(keep) : "v"(voff), "s"(sbase), "s"(lds_dst) : "memory");
  else
  asm volatile("s_mov_b32 %0, m0\n\ts_mov_b32 m0, %3\n\ts_nop 2\n\tglobal_load_lds_dwordx4 %1, %2\n\ts_mov_b32 m0, %0"
               : "=&s"(keep) : "v"(voff), "s"(sbase), "s"(lds_dst) : "memory");
}
template <bool SC>
__device__ __forceinline__ void glds16x4(const void* sbase, unsigned v0, unsigned v1, unsigned v2, unsigned v3, unsigned lds_dst) {
  unsigned keep;
  if (SC)
    asm volatile("s_mov_b32 %0, m0\n\ts_mov_b32 m0, %6\n\ts_nop 2\n\tglobal_load_lds_dwordx4 %1, %5 sc1\n\tglobal_load_lds_dwordx4 %2, %5 offset:1024 sc1\n\t"
                 "global_load_lds_dwordx4 %3, %5 offset:2048 sc1\n\tglobal_load_lds_dwordx4 %4, %5 offset:3072 sc1\n\ts_mov_b32 m0, %0"
                 : "=&s"(keep) : "v"(v0), "v"(v1), "v"(v2), "v"(v3), "s"(sbase), "s"(__builtin_amdgcn_readfirstlane(lds_dst)) : "memory");
  else
    asm volatile("s_mov_b32 %0, m0\n\ts_mov_b32 m0, %6\n\ts_nop 2\n\tglobal_load_lds_dwordx4 %1, %5\n\tglobal_load_lds_dwordx4 %2, %5 offset:1024\n\t"
                 "global_load_lds_dwordx4 %3, %5 offset:2048\n\tglobal_load_lds_dwordx4 %4, %5 offset:3072\n\ts_mov_b32 m0, %0"
                 : "=&s"(keep) : "v"(v0), "v"(v1), "v"(v2), "v"(v3), "s"(sbase), "s"(__builtin_amdgcn_readfirstlane(lds_dst)) : "memory");
}
constexpr float QSCALE = 0.17677669529663687f * 1.4426950408889634f;

__device__ __forceinline__ unsigned min4(u32x4 v) { const unsigned a = v[0] < v[1] ? v[0] : v[1], b = v[2] < v[3] ? v[2] : v[3]; return a < b ? a : b; }
#define LDS_AT(T, a) ((__attribute__((address_space(3))) T*)(uintptr_t)(a))
__device__ __forceinline__ u32x4 lds_rd4(unsigned a) { return *LDS_AT(const volatile u32x4, a); }
__device__ __forceinline__ void lds_wr(unsigned a, unsigned v) { *LDS_AT(volatile unsigned, a) = v; }
__device__ __forceinline__ void split3(float v, unsigned& p0, unsigned& p1, unsigned& p2) {
  p0 = pack2(v, v) & 0xffffu; v -= __uint_as_float(p0 << 16);
  p1 = pack2(v, v) & 0xffffu; v -= __uint_as_float(p1 << 16);
  p2 = pack2(v, v) & 0xffffu;
}

#define PIN() __builtin_amdgcn_sched_barrier(0)
template <int B, class F, int... I>
__device__ __forceinline__ void static_for_(F&& f, std::integer_sequence<int, I...>) { (f(std::integral_constant<int, B + I>{}), ...); }
template <int B, int E, class F>
__device__ __forceinline__ void static_for(F&& f) { static_for_<B>(f, std::make_integer_sequence<int, E - B>{}); }

#define TPRE 60
#define TPSL 0
#define FCPRE 44
#define FCSLP 2
#define SIGAT 24
#define XPRIO 1
#define OPD 8
#define LAUX 16
#define CSLP 2
#define EPRIO 3
#define OPRIO 1
#define XSLP 1
#define CSL1 8
#define XAUX 16
__device__ __forceinline__ void oproj_tail(const bool RES2, const int tid, const unsigned lbase, const unsigned flg, const unsigned fcm, const int tmi, const int tni, const int idx,
                                           unsigned* osig, unsigned* xsig, const unsigned tag,
                                           const u16* A, u16* out,
                                           const u16* res, const u16* __restrict__ w_r, double* stats,
                                           const double* stats_in, unsigned* hand, u32x4 (&xres)[2] TEARG) {
  constexpr int ldw = D_, PD = OPD;
  const int lane = tid & 63, wid = __builtin_amdgcn_readfirstlane(tid >> 6);
  const int m0 = tmi * 128, n0 = tni * 64;
  const int dk = RES2 ? 7 : 3;
  if (wid == 0) STAMP(dk, 0);
  TE(1, 3);
  asm volatile("" : "+s"(A) :: "memory");
  PIN();
  const int row = m0 + (wid & 3) * 32 + (wid >> 2) * 16 + (lane >> 3), col = n0 + (lane & 7) * 8;
  const unsigned tb = lbase + 147456;
  typedef __attribute__((address_space(1))) double* gdp;
  typedef __attribute__((address_space(1))) unsigned* gup;
  unsigned tr = tb + (row - m0) * 128 + (((lane ^ (lane >> 3)) & 7) << 4), oad;
  {
    oad = lane == 0 ? flg + 24 : flg + 64 + lane * 4;
    gup ps = (gup)(osig + lane * 64);
    asm volatile("" : "+v"(oad), "+v"(ps));
    asm volatile("s_waitcnt vmcnt(1)" ::: "memory");
    TE(2, 1);
    const unsigned old = __hip_atomic_fetch_add(LDS_AT(unsigned, oad), 1u, __ATOMIC_RELAXED, __HIP_MEMORY_SCOPE_WORKGROUP);
    if (__builtin_amdgcn_readfirstlane(old) == 7u && lane < 2 * RO) __hip_atomic_store(ps, tag, __ATOMIC_RELAXED, __HIP_MEMORY_SCOPE_AGENT);
    PIN();
    TE(2, 2);
    if (wid == 0) STAMP(dk, 12);
  }
  auto announce = [&]() __attribute__((always_inline)) {
    asm volatile("" ::: "memory");
    PIN();
    __builtin_amdgcn_s_barrier();
    PIN();
    if (wid == 0) STAMP(dk, 1);
    TE(2, 3); TE(4, 0); TE(7, 1);
  };
  if (wid < 4) {
    const __amdgpu_buffer_rsrc_t rsa = __builtin_amdgcn_make_buffer_rsrc(const_cast<u16*>(A), 0, S_ * D_ * 2, 0x00020000);
    int sa0 = (tmi * 4 + wid) * 65536, va0 = (lane >> 4) * 512 + (lane & 15) * 16;
    asm volatile("" : "+v"(va0));
    asm volatile("" : "+s"(sa0));
    auto lda = [&](int i, int h) { return __builtin_amdgcn_raw_buffer_load_b128(rsa, va0 + (i & 1) * 2048 + h * 256, sa0 + (i >> 1) * 4096, LAUX); };
    unsigned wad[4], tw[4];
    f32x4 acc[2][4];
    const int k = (lane >> 5) * 16 + ((lane >> 2) & 7);
#pragma unroll
    for (int j = 0; j < 4; ++j) {
      wad[j] = lbase + k * 128 + (((2 * j) ^ (k & 6)) << 4) + 8 * (lane & 3);
      tw[j] = tb + wid * 4096 + (lane & 15) * 128 + (((2 * j + (lane >> 5)) ^ (lane & 7)) << 4) + ((lane >> 4) & 1) * 8;
      asm volatile("" : "+v"(wad[j]), "+v"(tw[j]));
    }
    auto ldw_ = [&](int i, int j) {
      const unsigned a = wad[j] + (i & 3) * 4096;
      s16x4 lo = __builtin_amdgcn_ds_read_tr16_b64_v4i16(LDS_AT(s16x4, a));
      s16x4 h4 = __builtin_amdgcn_ds_read_tr16_b64_v4i16(LDS_AT(s16x4, a + 1024));
      return (s16x8)__builtin_shufflevector(lo, h4, 0, 1, 2, 3, 4, 5, 6, 7);
    };
    u32x4 id[2];
#pragma unroll
    for (int j = 0; j < 2; ++j) {
#pragma unroll
      for (int e = 0; e < 4; ++e) id[j][e] = (lane >> 4) == 2 * j + ((lane >> 3) & 1) && ((lane & 7) >> 1) == e ? ((lane & 1) ? 0x3f800000u : 0x3f80u) : 0u;
      asm volatile("" : "+v"(id[j]));
    }
    unsigned rad = tb + wid * 4096 + (lane & 15) * 128 + (((lane >> 4) ^ (lane & 7)) << 4), rfa = flg + 36 + wid;
    asm volatile("" : "+v"(rad), "+v"(rfa));
    u32x4 rb[2][2];
    u32x4 ow[2];
#pragma unroll
    for (int j = 0; j < 4; ++j) {
      const u32x2 v = *reinterpret_cast<const u32x2*>(w_r + n0 + j * 16 + (lane >> 4) * 4);
#pragma unroll
      for (int e = 0; e < 2; ++e) ow[j >> 1][(j & 1) * 2 + e] = (lane & 8) ? v[e] : 0x3f803f80u;
    }
    gdp pst = (gdp)(stats + (lane >> 4) % 3 * S_ + m0 + wid * 32 + (lane & 15));
    gup pps = (gup)(hand + 2 * HAND_N + ((tmi * 16 + tni) * 128 + wid * 32 + (lane & 15)) * 4 + (lane >> 4));
    int xo = (row * ldw + col) * 2;
    asm volatile("" : "+v"(tr), "+v"(pst), "+v"(pps), "+v"(xo), "+v"(ow[0]), "+v"(ow[1]));
    announce();
    u32x4 af[PD][2];
    s16x8 wf[2][4];
#pragma unroll
    for (int i = 0; i < PD; i += 2) { af[i][0] = lda(i, 0); af[i + 1][0] = lda(i + 1, 0); af[i][1] = lda(i, 1); af[i + 1][1] = lda(i + 1, 1); }
    PIN();
    __builtin_amdgcn_s_setprio(OPRIO);
#pragma unroll
    for (int q = 0; q < 4; ++q) rb[q >> 1][q & 1] = lds_rd4((rad ^ ((q & 1) * 64)) + (q >> 1) * 2048);
#pragma unroll
    for (int j = 0; j < 4; ++j) wf[j & 1][j >> 1] = ldw_(j & 1, j >> 1);
    PIN();
#pragma unroll
    for (int j = 0; j < 4; ++j)
#pragma unroll
      for (int h = 0; h < 2; ++h) { asm volatile("v_mfma_f32_16x16x32_bf16 %0, %1, %2, 0" : "=&v"(acc[h][j]) : "v"(id[j & 1]), "v"(rb[h][j >> 1])); PIN(); }
#pragma unroll 1
    for (int spin = 0; spin < SPIN_MAX; ++spin)
      if (__builtin_amdgcn_readfirstlane(*LDS_AT(const volatile unsigned, flg)) >= 40u) break;
    asm volatile("" : "+v"(va0) :: "memory");
    PIN();
    s16x4 wlo;
    static_for<0, 16>([&](auto ic) __attribute__((always_inline)) {
      constexpr int i = 2 * decltype(ic)::value;
      if (i == SIGAT) { __hip_atomic_fetch_add(LDS_AT(unsigned, oad + 4), 1u, __ATOMIC_RELAXED, __HIP_MEMORY_SCOPE_WORKGROUP); PIN(); }
#pragma unroll
      for (int g = 0; g < 16; ++g) {
        const int e = g & 1, j = g >> 2, h = (g >> 1) & 1, jn = (j + 2) & 3, in = j < 2 ? i : i + 2;
        asm volatile("v_mfma_f32_16x16x32_bf16 %0, %1, %2, %0" : "+v"(acc[h][j]) : "v"(wf[e][j]), "v"(af[(i + e) % PD][h]));
        PIN();
        if (in == 16 && !(g & 3)) asm volatile("v_add_u32 %0, 0x10000, %0" : "+v"(wad[jn]));
        if (in < 32) {
          const s16x4 v = __builtin_amdgcn_ds_read_tr16_b64_v4i16(LDS_AT(s16x4, wad[jn] + ((in + h) & 15) * 4096 + e * 1024));
          if (e) wf[h][jn] = (s16x8)__builtin_shufflevector(wlo, v, 0, 1, 2, 3, 4, 5, 6, 7);
          else wlo = v;
          PIN();
        }
        if ((g & 3) == 1 && i > 0 && i + PD - 2 < 32) { af[(i - 2 + (j & 1)) % PD][j >> 1] = lda(i + PD - 2 + (j & 1), j >> 1); PIN(); }
        if ((g & 3) == 3 && in < 32 && i != SIGAT) { __builtin_amdgcn_s_waitcnt(0xc47f); PIN(); }
      }
    });
    asm volatile("s_nop 3" : "+v"(acc[0][0]), "+v"(acc[1][0]));
    TE(4, 1); TE(7, 2);
    if (wid == 0) STAMP(dk, 17);
    if (wid == 1 && !RES2) STAMP(4, 0);
    u32x4 pk[2][2];
    float sv[2];
    auto sums = [&]() __attribute__((always_inline)) {
      f32x4 d[2];
      float q[2];
#pragma unroll
      for (int h = 0; h < 2; ++h) {
        d[h] = __builtin_amdgcn_mfma_f32_16x16x32_bf16(__builtin_bit_cast(s16x8, ow[0]), __builtin_bit_cast(s16x8, pk[h][0]), f32x4{0.f, 0.f, 0.f, 0.f}, 0, 0, 0);
        d[h] = __builtin_amdgcn_mfma_f32_16x16x32_bf16(__builtin_bit_cast(s16x8, ow[1]), __builtin_bit_cast(s16x8, pk[h][1]), d[h], 0, 0, 0);
        q[h] = 0.f;
      }
#pragma unroll
      for (int e = 0; e < 16; ++e) {
        const bf16x2 x = __builtin_bit_cast(bf16x2, (unsigned)pk[e & 1][e >> 3][(e >> 1) & 3]);
        q[e & 1] = __builtin_amdgcn_fdot2_f32_bf16(x, x, q[e & 1], false);
      }
      const auto a = __builtin_amdgcn_permlane32_swap(__float_as_uint(q[0]), __float_as_uint(q[1]), false, false);
      const unsigned b = __float_as_uint(__uint_as_float(a[0]) + __uint_as_float(a[1]));
      const auto c = __builtin_amdgcn_permlane16_swap(b, b, false, false);
      const unsigned e = __float_as_uint(__uint_as_float(c[0]) + __uint_as_float(c[1]));
      const auto f = __builtin_amdgcn_permlane32_swap(e, e, false, false);
#pragma unroll
      for (int h = 0; h < 2; ++h) sv[h] = (lane & 16) ? __uint_as_float(f[h]) : d[h][0];
    };
#pragma unroll
    for (int j = 0; j < 4; ++j)
#pragma unroll
      for (int h = 0; h < 2; ++h) {
        pk[h][j >> 1][(j & 1) * 2] = pack2(acc[h][j][0], acc[h][j][1]); pk[h][j >> 1][(j & 1) * 2 + 1] = pack2(acc[h][j][2], acc[h][j][3]);
        *LDS_AT(u32x2, tw[j] + h * 2048) = u32x2{pk[h][j >> 1][(j & 1) * 2], pk[h][j >> 1][(j & 1) * 2 + 1]};
        PIN();
      }
    asm volatile("" ::: "memory");
    *LDS_AT(volatile unsigned char, rfa) = (unsigned char)2;
#pragma unroll
    for (int p = 0; p < 2; ++p) xres[p] = lds_rd4(tr + p * 1024);
    PIN();
    if (!RES2) {
      const __amdgpu_buffer_rsrc_t rso = __builtin_amdgcn_make_buffer_rsrc(out, 0, S_ * D_ * 2, 0x00020000);
      const __amdgpu_buffer_rsrc_t rsf = __builtin_amdgcn_make_buffer_rsrc(xsig + wid, 0, RX * 512, 0x00020000);
      const u32x4 x2 = lds_rd4(tr + 2048), x3 = lds_rd4(tr + 3072);
      PIN();
#pragma unroll
      for (int p = 0; p < 4; ++p) { __builtin_amdgcn_raw_buffer_store_b128(p < 2 ? xres[p] : (p < 3 ? x2 : x3), rso, xo + p * (8 * ldw * 2), 0, XAUX); PIN(); }
      TE(4, 2); TE(5, 0);
      sums();
      if (lane < 48) { __builtin_amdgcn_global_atomic_fadd_f64(pst, (double)sv[0]); __builtin_amdgcn_global_atomic_fadd_f64(pst + 16, (double)sv[1]); }
      PIN();
      TE(4, 3); TE(5, 1);
      asm volatile("s_waitcnt vmcnt(2)" ::: "memory");
      __builtin_amdgcn_raw_buffer_store_b32(tag, rsf, lane * 512 + 256, 0, 16);
      *LDS_AT(volatile unsigned char, rfa) = (unsigned char)3;
      PIN();
      TE(5, 2);
      asm volatile("s_waitcnt vmcnt(1)" ::: "memory");
      __builtin_amdgcn_raw_buffer_store_b32(tag, rsf, lane * 512, 0, 16);
      PIN();
      TE(5, 3);
      __builtin_amdgcn_s_setprio(0);
      return;
    }
    TE(4, 2); TE(5, 0);
    sums();
    __hip_atomic_store(pps, f2word(sv[0]), __ATOMIC_RELAXED, __HIP_MEMORY_SCOPE_AGENT);
    __hip_atomic_store(pps + 64, f2word(sv[1]), __ATOMIC_RELAXED, __HIP_MEMORY_SCOPE_AGENT);
    if (wid == 0) STAMP(dk, 14);
  } else {
    u32x4 rres[4];
    double is1[4], is2[4];
#pragma unroll
    for (int p = 0; p < 4; ++p)
      rres[p] = __builtin_amdgcn_raw_buffer_load_b128(__builtin_amdgcn_make_buffer_rsrc(const_cast<u16*>(res), 0, S_ * D_ * 2, 0x00020000), ((row - 16 + 8 * p) * ldw + col) * 2, 0, 16);
#pragma unroll
    for (int p = 0; p < 4; ++p) { is1[p] = ld_ag(stats_in + row - 16 + 8 * p); is2[p] = ld_ag(stats_in + S_ + row - 16 + 8 * p); }
    PIN();
    if (RES2) {
#pragma unroll
      for (int p = 0; p < 4; ++p) {
        float mean, var, mb, den;
        ln_from_sums(is1[p], is2[p], mean, var, mb, den);
        const float rin = frcp(den);
#pragma unroll
        for (int e = 0; e < 4; ++e) {
          const f32x2 x = {__uint_as_float(rres[p][e] << 16), __uint_as_float(rres[p][e] & 0xffff0000u)};
          const f32x2 h = (x - f32x2{mb, mb}) * f32x2{rin, rin} + x;
          rres[p][e] = pack2(h[0], h[1]);
        }
        asm volatile("" : "+v"(rres[p]));
      }
      PIN();
    }
    if ((idx >> 5) == tmi * 4 + wid - 4) {
      const int dr = idx - (row - 16);
      u32x4 qq;
#pragma unroll 1
      for (int i = 0; i < SPIN_MAX; ++i) {
        qq = __builtin_amdgcn_raw_buffer_load_b128(__builtin_amdgcn_make_buffer_rsrc(hand, 0, HAND_N * 4, 0x00020000), (GRAN_N + Y_N) * 4 + col * 2, 0, 16);
        if (none(umax(umax(qq[0], qq[1]), umax(qq[2], qq[3])) == SENT)) break;
        asm volatile("" ::: "memory");
      }
#pragma unroll
      for (int p = 0; p < 4; ++p)
#pragma unroll
        for (int e = 0; e < 4; ++e) rres[p][e] = dr == 8 * p ? qq[e] : rres[p][e];
    }
#pragma unroll 1
    for (int i = 0; i < 4 * SPIN_MAX; ++i)
      if (__builtin_amdgcn_readfirstlane(*LDS_AT(const volatile unsigned, flg + 16)) >= 12u) break;
#pragma unroll
    for (int p = 0; p < 4; ++p) *LDS_AT(volatile u32x4, tr - 2048 + p * 1024) = rres[p];
    asm volatile("s_waitcnt lgkmcnt(0)" ::: "memory");
    announce();
    TE(4, 1); TE(7, 2);
    asm volatile("s_waitcnt vmcnt(0)" ::: "memory");
    if (!RES2) return;
    __builtin_amdgcn_s_sleep(TPRE);
    unsigned fm = SENT;
#pragma unroll 1
    for (int i = 0; i < 4 * SPIN_MAX; ++i) {
      fm = __builtin_amdgcn_readfirstlane(*LDS_AT(const volatile unsigned, fcm));
      if (fm != SENT) break;
      __builtin_amdgcn_s_sleep(1);
    }
    const unsigned r = (lane & 8) ? fm >> 16 : fm & 0xffffu;
    unsigned fra = tb + (r & 127u) * 128 + (((r ^ lane) & 7u) << 4);
    int fro = (r >> 5) == (unsigned)(tmi * 4 + wid - 4) && lane < 16 ? ((lane >> 3) * ldw + col) * 2 : S_ * D_ * 2;
    asm volatile("" : "+v"(fra), "+v"(fro));
#pragma unroll 1
    for (int i = 0; i < 4 * SPIN_MAX; ++i) {
      if (__builtin_amdgcn_readfirstlane(*LDS_AT(const volatile unsigned char, flg + 32 + wid)) == 2u) break;
      __builtin_amdgcn_s_sleep(TPSL);
    }
    asm volatile("" ::: "memory");
    __builtin_amdgcn_s_setprio(XPRIO);
    PIN();
    TE(4, 2); TE(5, 0);
    const u32x4 xr = lds_rd4(fra);
#pragma unroll
    for (int p = 0; p < 2; ++p) xres[p] = lds_rd4(tr + p * 1024);
    PIN();
    __builtin_amdgcn_raw_buffer_store_b128(xr, __builtin_amdgcn_make_buffer_rsrc(out, 0, 3 * D_ * 2, 0x00020000), fro, 0, XAUX);
  }
  TE(4, 3); TE(7, 3);
  PIN();
  __builtin_amdgcn_s_setprio(0);
  __hip_atomic_fetch_add(LDS_AT(unsigned, oad), 1u, __ATOMIC_RELAXED, __HIP_MEMORY_SCOPE_WORKGROUP);
  TE(5, 1); TE(5, 2);
  PIN();
}

#define MFMA32(a, b, c) __builtin_amdgcn_mfma_f32_32x32x16_bf16(a, b, c, 0, 0, 0)
#define MFMA16(a, b, c) __builtin_amdgcn_mfma_f32_16x16x32_bf16(a, b, c, 0, 0, 0)

#define PLO(x) __builtin_bit_cast(bf16x16, __builtin_shufflevector(x, x, 0, 1, 2, 3, 4, 5, 6, 7))
#define PHI(x) __builtin_bit_cast(bf16x16, __builtin_shufflevector(x, x, 8, 9, 10, 11, 12, 13, 14, 15))
#define SMF32(a, b, c) __builtin_amdgcn_smfmac_f32_32x32x32_bf16(__builtin_bit_cast(bf16x8, a), b, c, sidx, 0, 0)
#define SMF16(a, b, c) __builtin_amdgcn_smfmac_f32_16x16x64_bf16(__builtin_bit_cast(bf16x8, a), b, c, sidx, 0, 0)

__device__ __forceinline__ void patch_kv(unsigned tab, int r, s16x8& k0, s16x8& k1, s16x8& v0, s16x8& v1, bool on = true) {
  const int lane = fresh_lane(), hi = lane >> 5;
  if (on && (lane & 31) == kpos(r)) {
    k0 = *LDS_AT(const s16x8, tab + hi * 16); k1 = *LDS_AT(const s16x8, tab + 32 + hi * 16);
  }
  const short vv = *LDS_AT(const short, tab + 128 + (lane & 31) * 2);
  const int js = (on && hi == ((r >> 2) & 1)) ? (r >> 4) * 8 + ((r >> 3) & 1) * 4 + (r & 3) : -1;
#pragma unroll
  for (int e = 0; e < 8; ++e) { v0[e] = js == e ? vv : v0[e]; v1[e] = js == 8 + e ? vv : v1[e]; }
}

__device__ __forceinline__ void attn_safe(const u16* KV, int tile0, s16x8 q0, s16x8 q1,
                                       f32x16& o, float& l_out, int ptile, int prow, unsigned ptab) {
  const int lane = fresh_lane();
  float m = -INFINITY, l = 0.f;
  const int sidx = 0xDDDDDDDD;
  const __amdgpu_buffer_rsrc_t rs = __builtin_amdgcn_make_buffer_rsrc(const_cast<u16*>(KV), 0, 2 * S_ * D_ * 2, 0x00020000);
#pragma unroll
  for (int e = 0; e < 16; ++e) o[e] = 0.f;
  for (int tt = 0; tt < 64; ++tt) {
    const int t = tile0 + tt, off = t * 2048 + lane * 16;
    s16x8 k0 = __builtin_bit_cast(s16x8, __builtin_amdgcn_raw_buffer_load_b128(rs, off, 0, 16));
    s16x8 k1 = __builtin_bit_cast(s16x8, __builtin_amdgcn_raw_buffer_load_b128(rs, off + 1024, 0, 16));
    s16x8 v0 = __builtin_bit_cast(s16x8, __builtin_amdgcn_raw_buffer_load_b128(rs, off + S_ * D_ * 2, 0, 16));
    s16x8 v1 = __builtin_bit_cast(s16x8, __builtin_amdgcn_raw_buffer_load_b128(rs, off + S_ * D_ * 2 + 1024, 0, 16));
    if (t == ptile) patch_kv(ptab, prow, k0, k1, v0, v1);
    f32x16 sc;
#pragma unroll
    for (int e = 0; e < 16; ++e) sc[e] = 0.f;
    sc = MFMA32(k0, q0, sc); sc = MFMA32(k1, q1, sc);
    float mt = sc[0];
#pragma unroll
    for (int e = 1; e < 16; ++e) mt = fmaxf(mt, sc[e]);
    mt = fmaxf(mt, __shfl_xor(mt, 32, 64));
    const float mn = fmaxf(m, mt);
    const float alpha = __builtin_amdgcn_exp2f(m - mn);
    m = mn;
#pragma unroll
    for (int e = 0; e < 16; ++e) sc[e] = __builtin_amdgcn_exp2f(sc[e] - mn);
    float ls = 0.f;
#pragma unroll
    for (int e = 0; e < 16; ++e) ls += __uint_as_float(__float_as_uint(sc[e]) & 0xffff0000u);
    l = l * alpha + ls;
#pragma unroll
    for (int e = 0; e < 16; ++e) o[e] *= alpha;
    o = SMF32(v0, PLO(sc), o); o = SMF32(v1, PHI(sc), o);
  }
  l_out = l + __shfl_xor(l, 32, 64);
}

__device__ __forceinline__ float wave_fmax(float v) {
  asm volatile("s_nop 1\n\tv_max_f32_dpp %0, %0, %0 row_shr:1 row_mask:0xf bank_mask:0xf\n\ts_nop 1\n\tv_max_f32_dpp %0, %0, %0 row_shr:2 row_mask:0xf bank_mask:0xf\n\t"
               "s_nop 1\n\tv_max_f32_dpp %0, %0, %0 row_shr:4 row_mask:0xf bank_mask:0xf\n\ts_nop 1\n\tv_max_f32_dpp %0, %0, %0 row_shr:8 row_mask:0xf bank_mask:0xf\n\t"
               "s_nop 1\n\tv_max_f32_dpp %0, %0, %0 row_bcast:15 row_mask:0xa bank_mask:0xf\n\ts_nop 1\n\tv_max_f32_dpp %0, %0, %0 row_bcast:31 row_mask:0xc bank_mask:0xf\n\ts_nop 0"
               : "+v"(v));
  return __int_as_float(__builtin_amdgcn_readlane(__float_as_int(v), 63));
}

#define PSLP 8
#define WSLP 3
#define PRE1 8
#define PRE5 8
#define W3SLP 4
#define FSTART 10
#define OAUX 16
#define OMX 4
#define T0 2u
#define TW 10u
#define HW 2
#define HP1 1
#define POLLAT 11
#define X2AT 2
#define HPRIO 3
#define SSLP 1
#define SPRE 6
#define WGAPS {0, 1, 7, 8, 9, 10, 11, 12}
#define KVS 4
#define PNS 12
#define G0 5u
#define GS 2u
#define IAT 1u
#define YSLP 2
#define ISLP 8
#define KSLP 2
#define UAT 13u
#define UHW 0
#define PPSLP 2
#define XRSLP 2
#define MPRE 20
#define YD1 150
#define YD2 180
#define KDT 200
#define XDT 45
#define R2PRIO 0
#define LNPRIO 1
#define MSLP 4
#define BARG 0
struct Args {
  long long lay;
  const u16* a; u16* kv; u16* o; u16* xo; double* so;
  const u16* W; u16* wc;
  const u16 *wg, *wu, *wd, *wo, *w_r;
  unsigned* gran; float* csf; double* stats; unsigned* hand; u16* out;
};
typedef __attribute__((address_space(4))) const Args* ArgP;
#define WCAT 3
#define CAP0 1
#define PRE4 50
#define PRE7 20
#define UARR 12u
#define GF0 10
#define XGATE 8
#define ASLP 1
#define B15 3
#define KO 5
#define PRO 2
#define HS 2
#define XD2 90
#define TFL 4u
#define QPRE 8
#define QSLP 1
#define WMAP 0x32100333
#define SMAP 0
#define STCH 6
#define KPOLL 10
#define TPRIO 2
#define TSLP 9
__device__ __forceinline__ bool slow_cols(const u16* W, int tni) { return ((((unsigned)(uintptr_t)W >> 7) + tni) & 7) == 3; }
#define ARGS_HERE                                                                                                      \
  ArgP ap_ = ap; asm volatile("" : "+s"(ap_));                                                                          \
  const u16* A = ap_->a; const u16* __restrict__ W = ap_->W; u16* KV = ap_->kv; u16* Of = ap_->o;        \
  const u16* __restrict__ wg = ap_->wg; const u16* __restrict__ wu = ap_->wu; const u16* __restrict__ wd = ap_->wd;       \
  const u16* __restrict__ wo = ap_->wo; const u16* __restrict__ w_r = ap_->w_r; unsigned* gran = ap_->gran;              \
  float* csf = ap_->csf; const double* stats = ap_->stats; double* stats_out = ap_->so; unsigned* hand = ap_->hand; \
  u16* xout = ap_->xo; u16* __restrict__ out = ap_->out
#define AGAIN "s_getpc_b64 s[90:91]\n\ts_add_u32 s90, s90, layers_kernel@rel32@lo+4\n\ts_addc_u32 s91, s91, layers_kernel@rel32@hi+12\n\ts_setpc_b64 s[90:91]"
#define KEPT(c)                                                                                                        \
  c "{v[104:107]}"(gu[0]), c "{v[108:111]}"(gu[1]), c "{v[112:115]}"(gu[2]), c "{v[116:119]}"(gu[3]), c "{v[120:123]}"(gu[4]),      \
  c "{v[124:127]}"(gu[5]), c "{v[128:131]}"(gu[6]), c "{v[132:135]}"(gu[7]), c "{v[136:139]}"(gu[8]), c "{v[140:143]}"(gu[9]),      \
  c "{v[144:147]}"(gu[10]), c "{v[148:151]}"(gu[11]), c "{v[152:155]}"(gu[12]), c "{v[156:159]}"(gu[13]), c "{v[160:163]}"(gu[14]), \
  c "{v[164:167]}"(gu[15])
#define LAYER_HERE ArgP apl_ = ap; asm volatile("" : "+s"(apl_)); const bool LAY2 = apl_->lay != 0
__device__ __forceinline__ void layer_body(const int wid, const int bid, const unsigned lbase, const ArgP ap, const unsigned long long t_entry) {
  constexpr int K = 1024, NKT = 16, NST = 3, ABYTES = 16384, WBYTES = 24576, STB = ABYTES + WBYTES, ldw = 3 * D_;
  constexpr int TABA = NST * STB, ALPHA = TABA + 2048, CRED = ALPHA + 512;
  constexpr int KSB = 8192, ASB = 16384, FLG = 8 * ASB + 256;
  constexpr int DUM = FLG + 64, TAB = DUM + 256, RED = TAB + 512, SACT = RED + 4096, XML = SACT + 3584, UGL = XML + 4096, SPM = UGL + 1024, QOFF = 9 * ASB;
  constexpr int TABW = RED;
  static_assert(SPM + 2048 <= QOFF && CRED <= 8 * ASB && RED + 8192 <= QOFF && RED % 16 == 0, "lds");
  const int xcd = (bid & 1) * 4 + ((bid >> 1) & 3), loc = bid >> 3;
  const int tmi = (xcd / QNG) * QMX + loc / QNX, tni = (xcd % QNG) * QNX + loc % QNX;
  const int m0 = tmi * 128, n0 = tni * 64;
  const int omi = (xcd / (OMX / 2)) * OMX + loc / (32 / OMX), oni = (xcd % (OMX / 2)) * (32 / OMX) + loc % (32 / OMX);
  constexpr unsigned VOFF = (unsigned)S_ * D_ * 2;
  auto tile_at = [&](unsigned n) __attribute__((always_inline)) {
#pragma unroll 1
    for (int i = 0; i < 4 * SPIN_MAX; ++i) {
      if (__builtin_amdgcn_readfirstlane(*LDS_AT(const volatile unsigned, lbase + FLG + 36)) == n * 0x01010101u) break;
      __builtin_amdgcn_s_sleep(1);
    }
    asm volatile("" ::: "memory");
    PIN();
  };
  auto all_ge = [&](unsigned w, unsigned n) { return ((w + (128u - n) * 0x01010101u) & 0x80808080u) == 0x80808080u; };
  if (wid >= 8) {
    u32x4 gu[16];
    LAYER_HERE;
    constexpr int dk = 6;
    const int hw = wid - 8, b = bid;
    int nb = 0;
    constexpr int lim = NKT - 1;
    auto tick_nw = [&]() {
      PIN();
      if (nb < lim) { __builtin_amdgcn_s_barrier(); ++nb; } else __builtin_amdgcn_s_sleep(2);
      asm volatile("" ::: "memory");
      PIN();
    };
    auto tick = [&]() {
      asm volatile("s_waitcnt lgkmcnt(0)" ::: "memory");
      tick_nw();
    };
    auto fold16 = [&](f32x2 y0, f32x2 y1) __attribute__((always_inline)) {
      const auto q0 = __builtin_amdgcn_permlane32_swap(__float_as_uint(y0[0]), __float_as_uint(y1[0]), false, false);
      const auto q1 = __builtin_amdgcn_permlane32_swap(__float_as_uint(y0[1]), __float_as_uint(y1[1]), false, false);
      const auto q = __builtin_amdgcn_permlane16_swap(__float_as_uint(__uint_as_float(q0[0]) + __uint_as_float(q0[1])),
                                                      __float_as_uint(__uint_as_float(q1[0]) + __uint_as_float(q1[1])), false, false);
      return __uint_as_float(q[0]) + __uint_as_float(q[1]);
    };
    unsigned best, tag, rmd;
    float alpha, m, wsum;
    u32x4 tw;
    u32x2 xq;
    s16x8 sl[8];
    const int kcap = CAP0 + (tmi & 7);
    if (!LAY2) {
      ARGS_HERE;
      const int lane = fresh_lane(), t = hw * 64 + lane;
      const unsigned ep = ld_ag(gran);
      const int srow = m0 + tni * 8 + hw * 2;
      u32x4 xa[2][2], wv[8];
      const int wr_ = (3 - ((unsigned)(uintptr_t)W >> 7) - (tni & ~3)) & 7;
      const bool cpy = hw == 3 && wr_ < 4 && wr_ != (tni & 3), wld = hw < 3 ? wr_ != (tni & 3) : cpy;
      const int wcn = cpy ? (tni & ~3) + wr_ : tni, wcp = cpy ? (tni - wr_ - 1) & 3 : hw;
      const int wof = ((tmi * 64 + (lane >> 3)) * ldw + wcp * D_ + wcn * 64 + (lane & 7) * 8) * 2;
      auto ld_w = [&]() {
        const __amdgpu_buffer_rsrc_t rw = __builtin_amdgcn_make_buffer_rsrc(const_cast<u16*>(W), 0, wld ? D_ * ldw * 2 : 0, 0x00020000);
#pragma unroll
        for (int r = 0; r < 8; ++r) wv[r] = __builtin_amdgcn_raw_buffer_load_b128(rw, wof, r * 8 * ldw * 2, 0);
        PIN();
      };
      auto wcopy = [&]() __attribute__((always_inline)) {
        if (cpy) {
          const __amdgpu_buffer_rsrc_t rw = __builtin_amdgcn_make_buffer_rsrc(ap_->wc, 0, D_ * ldw * 2, 0x00020000);
#pragma unroll
          for (int r = 0; r < 8; ++r) __builtin_amdgcn_raw_buffer_store_b128(wv[r], rw, wof + r * 8 * ldw * 2, 0, 16);
        }
        PIN();
      };
      const int wat = ((hw == 3 ? SMAP : WMAP) >> (4 * (tmi & 7))) & 3;
      unsigned ps = 0u;
      auto stouch = [&](int at) __attribute__((always_inline)) {
        if (at == STCH && cpy) ps = __builtin_amdgcn_raw_buffer_load_b32(__builtin_amdgcn_make_buffer_rsrc(const_cast<u16*>(W), 0, D_ * ldw * 2, 0x00020000),
                                                                        (((tmi ^ 8) * 64 + lane) * ldw + wcp * D_ + wcn * 64) * 2, 0, 0);
        PIN();
      };
      auto ld_x = [&](int h) {
#pragma unroll
        for (int r = 0; r < 2; ++r) xa[r][h] = *reinterpret_cast<const u32x4*>(A + (size_t)(srow + r) * D_ + (h ^ (tmi >> 3)) * 512 + lane * 8);
        PIN();
      };
      ld_x(0);
      if (X2AT == 0) ld_x(1);
      if (wat == 0) ld_w();
      unsigned pc = 0u;
      if (hw == 3) {
        unsigned long long a0, a1;
        CODE_ADDR("layers_kernel", a0);
        CODE_ADDR("end_marker_kernel", a1);
        pc = touch_code(a0, a1, loc, lane);
      }
      if (hw == 1) pc = ld_ag(gran + ((lane & 1) ? GR_ROW + (m0 + tni * 8) * 4 : ((lane & 2) ? GR_Q + ((tmi ^ 1) * 16 + tni) * 32 : GR_KV + (tni * RK + tmi % RK) * 32)));
      if (hw == 2) st_ag(gran + GR_N + bid * 64 + lane, lane + 1u);
      {
        const int gi = bid * 256 + t;
        if (gi < GRAN_N / 4) __builtin_amdgcn_raw_buffer_store_b128(u32x4{SENT, SENT, SENT, SENT}, __builtin_amdgcn_make_buffer_rsrc(hand, 0, GRAN_N * 4, 0x00020000), gi * 16, 0, 16);
        if (t < 24) __hip_atomic_store(stats_out + (t >> 3) * S_ + m0 + tni * 8 + (t & 7), 0.0, __ATOMIC_RELAXED, __HIP_MEMORY_SCOPE_AGENT);
      }
      tick();
      if (wat == 1) ld_w();
      if (X2AT == 1) ld_x(1);
      tick();
      if (wat == 2) ld_w();
      if (WCAT == 2) wcopy();
      if (X2AT == 2) ld_x(1);
      asm volatile("" :: "v"(ep));
      tick();
      if (wat == 3) ld_w();
      if (WCAT == 3) wcopy();
      if (X2AT == 3) ld_x(1);
      stouch(3);
#pragma unroll
      for (int i = 0; i < HW; ++i) { tick(); stouch(4 + i); }
      __builtin_amdgcn_s_setprio(HP1);
      PIN();
      tag = ep * 1664525u + 1013904223u;
      const __amdgpu_buffer_rsrc_t rsc = __builtin_amdgcn_make_buffer_rsrc(gran, 0, GR_N * 4, 0x00020000);
      {
        u32x4 od;
#pragma unroll
        for (int q = 0; q < 4; ++q) od[q] = ((lane & 7) >> 1) == q ? ((lane & 1) ? 0x3f800000u : 0x00003f80u) : 0u;
        f32x4 d[2] = {{0.f, 0.f, 0.f, 0.f}, {0.f, 0.f, 0.f, 0.f}};
#pragma unroll
        for (int r = 0; r < 8; ++r) d[r & 1] = __builtin_amdgcn_mfma_f32_16x16x32_bf16(__builtin_bit_cast(s16x8, od), __builtin_bit_cast(s16x8, wv[r]), d[r & 1], 0, 0, 0);
        u32x4 gr;
#pragma unroll
        for (int q = 0; q < 4; ++q) gr[q] = ((__float_as_uint(dpp_add<0x128, 0xf>(fadd(d[0][q], d[1][q]))) + 0x80u) & ~0xffu) | ((tag >> (8 * q)) & 0xffu);
        asm volatile("s_waitcnt vmcnt(0)" ::: "memory");
        __builtin_amdgcn_raw_buffer_store_b128(gr, rsc, (lane & 40) || !wld ? GR_N * 4 : (GR_COL + (wcn * 16 + tmi) * 192 + wcp * 64 + (lane & 7) * 8 + (lane >> 4) * 4) * 4, 0, 16);
      }
      asm volatile("" :: "v"(pc));
      tick_nw();
      stouch(6);
      {
        float xv[2][16], mean[2];
#pragma unroll
        for (int r = 0; r < 2; ++r) {
          float sm = 0.f;
#pragma unroll
          for (int i = 0; i < 4; ++i) {
            xv[r][2 * i] = __uint_as_float(xa[r][0][i] << 16); xv[r][2 * i + 1] = __uint_as_float(xa[r][0][i] & 0xffff0000u);
            xv[r][8 + 2 * i] = __uint_as_float(xa[r][1][i] << 16); xv[r][8 + 2 * i + 1] = __uint_as_float(xa[r][1][i] & 0xffff0000u);
          }
#pragma unroll
          for (int i = 0; i < 16; ++i) sm += xv[r][i];
          mean[r] = wave_sum(sm) * (1.0f / 1024.0f);
          tick();
          stouch(7 + r);
        }
        float vr[2];
#pragma unroll
        for (int r = 0; r < 2; ++r) {
          float v = 0.f;
#pragma unroll
          for (int i = 0; i < 16; ++i) { const float d = xv[r][i] - mean[r]; v += d * d; }
          vr[r] = wave_sum(v) * (1.0f / 1024.0f);
          if (r == 0) tick();
        }
        const float vv = lane & 2 ? vr[1] : vr[0], mm = lane & 2 ? mean[1] : mean[0];
        const float rin = 1.0f / rbf(sqrtf(rbf(rbf(vv) + 1e-5f)));
        if (lane < 4) __builtin_amdgcn_raw_buffer_store_b64(u32x2{__float_as_uint((lane & 1) ? rbf(mm) : rin), tag}, rsc, (GR_ROW + srow * 4 + lane * 2) * 4, 0, 16);
      }
      tick();
      float cs;
      while (nb < POLLAT) tick();
      asm volatile("" :: "v"(ps));
#pragma unroll 1
      for (int spin = 0;; ++spin) {
        u32x4 csv[4];
        const __amdgpu_buffer_rsrc_t rsw = __builtin_amdgcn_make_buffer_rsrc(gran, 0, hw < 3 ? GR_N * 4 : 0, 0x00020000);
#pragma unroll
        for (int i = 0; i < 4; ++i) csv[i] = __builtin_amdgcn_raw_buffer_load_b128(rsw, (GR_COL + (tni * 16 + (lane >> 4)) * 192 + hw * 64 + (lane & 15) * 4) * 4, i * (4 * 192 * 4), 16);
        const u32x4 rp = __builtin_amdgcn_raw_buffer_load_b128(rsc, (GR_ROW + (m0 + (t & 127)) * 4) * 4, 0, 16);
        PIN();
        tick(); tick();
        unsigned bad = 0u;
        float z[4];
#pragma unroll
        for (int q = 0; q < 4; ++q) {
          const unsigned tq = tag >> (8 * q);
          bad |= ((csv[0][q] ^ tq) | (csv[1][q] ^ tq)) | ((csv[2][q] ^ tq) | (csv[3][q] ^ tq));
          z[q] = fadd(fadd(__uint_as_float(csv[0][q] & ~0xffu), __uint_as_float(csv[1][q] & ~0xffu)), fadd(__uint_as_float(csv[2][q] & ~0xffu), __uint_as_float(csv[3][q] & ~0xffu)));
        }
        cs = fold16(f32x2{z[0], z[1]}, f32x2{z[2], z[3]});
        bad = (hw < 3 ? bad & 0xffu : 0u) | (rp[1] ^ tag) | (rp[3] ^ tag);
        alpha = __uint_as_float(rp[0]); m = __uint_as_float(rp[2]);
        if (none(bad != 0u) || spin >= SPIN_MAX) break;
      }
      if (tmi == 0 && hw < 3) st_ag(reinterpret_cast<unsigned*>(csf) + hw * 1024 + n0 + (lane & 15) * 4 + (lane >> 4), __float_as_uint(cs));
      unsigned c0, c1, c2;
      split3(cs, c0, c1, c2);
      tw = u32x4{c0 | (c1 << 16), c0 | (c2 << 16), c1 | (c0 << 16), 0u};
      best = rmd = 0u;
      wsum = 0.f;
      asm volatile("" : "=v"(xq), KEPT("="));
#pragma unroll
      for (int i = 0; i < 8; ++i) asm volatile("" : "=v"(sl[i]));
    } else {
      asm volatile("; kept registers" : KEPT("="));
      PIN();
      ARGS_HERE;
      const int lane = fresh_lane(), t = hw * 64 + lane;
      best = rmd = 0u;
      tag = __builtin_amdgcn_readfirstlane(*LDS_AT(const volatile unsigned, lbase + FLG + 20));
#pragma unroll
      for (int i = 0; i < 8; ++i) asm volatile("" : "=v"(sl[i]));
      unsigned sad;
      auto tickc = [&]() __attribute__((always_inline)) {
        tick();
        if (nb == kcap + 1) {
#pragma unroll
          for (int i = 0; i < 8; ++i) {
            const unsigned a = (sad ^ ((i >> 2) * 64) ^ (((i >> 1) & 1) * 16)) + (i & 1) * 12288;
            sl[i] = __builtin_shufflevector(__builtin_amdgcn_ds_read_tr16_b64_v4i16(LDS_AT(s16x4, a)), __builtin_amdgcn_ds_read_tr16_b64_v4i16(LDS_AT(s16x4, a + 1536)), 0, 1, 2, 3, 4, 5, 6, 7);
          }
        }
        PIN();
      };
      static_assert(CAP0 + 8 <= KPOLL, "the stage is taken before that");
      if (hw == 1) STAMP(4, 4);
      if (hw == 0) {
        __builtin_amdgcn_s_sleep(TPRE);
        tile_at(3u);
        __builtin_amdgcn_s_sleep(XGATE);
        STAMP(4, 3);
#pragma unroll 1
        for (int spin = 0; spin < SPIN_MAX; ++spin) {
          if (none(ld_ag(gran + GR_X + (tmi * RX + tni % RX) * 128 + 64 + lane) != tag)) break;
          __builtin_amdgcn_s_sleep(1);
        }
      }
      static_assert(UHW == 0, "");
      if (hw == 0) asm volatile("s_waitcnt vmcnt(0)" ::: "memory");
      if (hw == 0) STAMP(4, 5);
      asm volatile("" ::: "memory");
      PIN();
      __builtin_amdgcn_s_barrier();
      PIN();
      if (hw == 1) STAMP(4, 6);
      {
        const int ln = fresh_lane(), g4 = ln >> 4, tp = ln & 3, k = 8 * g4 + ((ln & 15) >> 2);
        sad = lbase + (kcap % NST) * STB + ABYTES + k * 384 + (((2 * (tp >> 1)) ^ (4 * ((k >> 1) & 1)) ^ (g4 & 1)) << 4) + 8 * (tp & 1) + (hw % 3) * 128;
      }
      wsum = __uint_as_float(__builtin_amdgcn_readfirstlane(*LDS_AT(const volatile unsigned, lbase + FLG + 60)));
      asm volatile("" : "+v"(sad), "=v"(tw), "=v"(alpha), "=v"(m));
      PIN();
      const int row = m0 + (t & 127);
      const __amdgpu_buffer_rsrc_t rsg = __builtin_amdgcn_make_buffer_rsrc(hand, 0, GRAN_N * 4, 0x00020000);
      if (hw < 2 || tmi == tni) {
      while (nb < HS) tickc();
#pragma unroll 1
      for (int spin = 0; spin < SPIN_MAX; ++spin) {
        const unsigned v = ld_ag(gran + GR_X + (tmi * RX + tni % RX) * 128 + lane);
        tickc();
        if (none(v != tag)) break;
      }
      asm volatile("" ::: "memory");
      PIN();
      const double s1 = ld_ag(stats + row), s2 = ld_ag(stats + S_ + row), sw = ld_ag(stats + 2 * S_ + row);
      tickc();
      float mean, var, mb, den;
      ln_from_sums(s1, s2, mean, var, mb, den);
      const float rin = frcp(den), g1 = 1.0f + rin;
      const float mb2 = rbf(mean + (mean - mb) * rin);
      const float den2 = rbf(__builtin_amdgcn_sqrtf(rbf(rbf(var * g1 * g1) + 1e-5f)));
      alpha = g1 * frcp(den2); m = (mb * rin + mb2) * frcp(g1);
      if (tmi == tni && hw >= 2) {
        const float r = (float)fma(-(double)mb, (double)wsum, sw) * rin;
        const unsigned rb = pack2(r, r);
        unsigned rkey = (rb ^ ((unsigned)((int)rb >> 31) | 0x80000000u)) & 0xffff0000u;
        if (r != r) rkey = 0u;
        rkey = wave_umax(rkey | (unsigned)(2047 - row));
        const unsigned md = __builtin_amdgcn_readlane(f2word(__uint_as_float((__float_as_uint(mb) >> 16) | (__float_as_uint(den) & 0xffff0000u))), (2047 - (int)(rkey & 0xffffu)) & 63);
        __builtin_amdgcn_raw_buffer_store_b64(u32x2{rkey, md}, rsg, lane < NREP ? lane * 256 + (tmi * 2 + hw - 2) * 8 : GRAN_N * 4, 0, 16);
      }
      }
      while (nb < KPOLL) tickc();
#pragma unroll 1
      for (int spin = 0; spin < SPIN_MAX; ++spin) {
        const u32x2 v = __builtin_amdgcn_raw_buffer_load_b64(rsg, (bid % NREP) * 256 + (lane & 31) * 8, 0, 16);
        tick(); tick();
        if (none(umax(v[0], v[1]) == SENT)) {
          best = wave_umax(v[0]);
          rmd = __builtin_amdgcn_readlane(v[1], ((2047 - (int)(best & 0xffffu)) >> 6) & 31);
          break;
        }
      }
      const int ix = (2047 - (int)(best & 0xffffu)) & 2047;
      if (row == ix) alpha = 0.f;
      xq = __builtin_amdgcn_raw_buffer_load_b64(__builtin_amdgcn_make_buffer_rsrc(const_cast<u16*>(A), 0, S_ * D_ * 2, 0x00020000), (ix * D_ + hw * 256 + (lane & 3) * 64 + (lane >> 2) * 4) * 2, 0, 16);
      PIN();
    }
    tag = __builtin_amdgcn_readfirstlane(tag);
    wsum = __uint_as_float(__builtin_amdgcn_readfirstlane(__float_as_uint(wsum)));
    const int lane = fresh_lane(), t = hw * 64 + lane;
    {
      unsigned a0, a1, a2;
      split3(-m, a0, a1, a2);
      u32x4 ta = {a0 | (a0 << 16), a1 | (a0 << 16), a1 | (a2 << 16), 0u};
      asm volatile("" : "+v"(tw), "+v"(ta), "+v"(alpha));
      __builtin_amdgcn_s_setprio(TPRIO);
      if (nb < lim) {
        while (nb < lim) tick();
        __builtin_amdgcn_s_sleep(TSLP);
      }
      if (!LAY2 && hw < 3) *LDS_AT(u32x4, lbase + TABW + (hw * 64 + (lane & 15) * 4 + (lane >> 4)) * 16) = tw;
      if (t < 128) {
        *LDS_AT(u32x4, lbase + TABA + t * 16) = ta;
        *LDS_AT(float, lbase + ALPHA + t * 4) = alpha;
      }
    }
    if (t >= 192 && t < 196) *LDS_AT(u32x4, lbase + CRED + ((t >> 1) & 1) * 512 + (t & 1) * 128) = u32x4{0u, 0u, 0u, 0u};
    asm volatile("s_waitcnt lgkmcnt(0)" ::: "memory");
    PIN();
    __builtin_amdgcn_s_barrier();
    PIN();
    *LDS_AT(u32x4, lbase + XML + t * 16) = u32x4{SENT, SENT, SENT, SENT};
    *LDS_AT(u32x4, lbase + SPM + (t & 127) * 16) = u32x4{SENT, SENT, SENT, SENT};
    if (hw == 0) {
      unsigned v;
      asm volatile("v_mov_b32 %0, 0\n\tv_writelane_b32 %0, 2, 0\n\tv_writelane_b32 %0, %1, 5\n\tv_writelane_b32 %0, %2, 7"
                   : "=&v"(v) : "s"(tag), "s"(best));
      lds_wr(lbase + FLG + lane * 4, v);
    }
    const int idx = LAY2 ? (2047 - (int)(best & 0xffffu)) & 2047 : -64;
    const bool p1blk = (oni & 7) != 7, fast = tmi < 8, own = fast && (loc & 1);
    const int fg = xcd * 28 + (p1blk ? loc - (loc >> 3) : 0), ow = (xcd & 3) * 16 + (loc >> 1);
    static_assert(OMX == 4, "oni % 8 = loc % 8");
#define GLD(a, b) "global_load_dwordx4 v[" #a ":" #b "], %0, off\n\tv_lshl_add_u64 %0, %0, 0, %1\n\t"
#define GLD4(...) asm volatile(__VA_ARGS__ : "+v"(p) : "s"((unsigned long long)(F_ * 2)), KEPT() : "memory")
    auto gptr = [&](const u16* wg, const u16* wu) {
      const int ln = fresh_lane();
      return ((ln & 2) ? wu : wg) + (size_t)(hw * 256 + (ln >> 2) * 4) * F_ + fg * 16 + (ln & 1) * 8;
    };
    auto gather = [&](const u16*& p, int e) __attribute__((always_inline)) {
      if (e == 0) GLD4(GLD(104, 107) GLD(108, 111) GLD(112, 115) GLD(116, 119));
      else if (e == 1) GLD4(GLD(120, 123) GLD(124, 127) GLD(128, 131) GLD(132, 135));
      else if (e == 2) GLD4(GLD(136, 139) GLD(140, 143) GLD(144, 147) GLD(148, 151));
      else GLD4(GLD(152, 155) GLD(156, 159) GLD(160, 163) GLD(164, 167));
      p += 60 * F_;
    };
    auto dmask = [&](int q) { const int ln = fresh_lane(); return ((ln >> 1) & 1) == q ? ((ln & 1) ? 0xffff0000u : 0x0000ffffu) : 0u; };
    unsigned dm0, dm1, dad;
    auto gu_setup = [&]() __attribute__((always_inline)) {
      const int ln = fresh_lane();
      dm0 = dmask(0); dm1 = dmask(1);
      dad = lbase + SACT + ((ln & 3) * 8 + (ln >> 5) * 4 + ((ln >> 4) & 1)) * 16 + hw * 4;
      asm volatile("" : "+v"(dm0), "+v"(dm1), "+v"(dad));
    };
    auto gate_up = [&](u32x2 x, unsigned md) __attribute__((always_inline)) {
      const unsigned rin = __builtin_amdgcn_readfirstlane(__float_as_uint(frcp(__uint_as_float(md & 0xffff0000u))));
      asm volatile("s_getpc_b64 s[90:91]\n\ts_add_u32 s90, s90, gate_up_code@rel32@lo+4\n\ts_addc_u32 s91, s91, gate_up_code@rel32@hi+12\n\ts_swappc_b64 s[92:93], s[90:91]"
                   : "+{v[100:101]}"(x) : "{s84}"(md << 16), "{s85}"(md & 0xffff0000u), "{s86}"(rin), "{v99}"(dad), "{v102}"(dm0), "{v103}"(dm1), KEPT()
                   : "v84", "v85", "v86", "v87", "v88", "v89", "v90", "v91", "v92", "v93", "v94", "v95", "v96", "v97", "s88", "s89", "s90", "s91", "s92", "s93", "scc", "memory");
    };
    auto act_diag = [&](int so = 0) __attribute__((always_inline)) {
      const int ln = fresh_lane();
      const f32x4 g4 = *LDS_AT(const volatile f32x4, lbase + SACT + so + (ln >> 2) * 16), u4 = *LDS_AT(const volatile f32x4, lbase + SACT + so + 256 + (ln >> 2) * 16);
      float gv = fadd(fadd(g4[0], g4[1]), fadd(g4[2], g4[3])), uv = fadd(fadd(u4[0], u4[1]), fadd(u4[2], u4[3]));
      rbf2(gv, uv);
      const float av = rbf(gv * frcp(1.0f + __expf(-gv))) * uv;
      const unsigned ap2 = pack2(av, av);
      return __builtin_bit_cast(s16x4, u32x2{ap2 & dm0, ap2 & dm1});
    };
    auto down = [&](const s16x4& a, const u32x2 (&w)[16]) __attribute__((always_inline)) {
      f32x4 d[2] = {{0.f, 0.f, 0.f, 0.f}, {0.f, 0.f, 0.f, 0.f}};
      static_for<0, 16>([&](auto fc) __attribute__((always_inline)) {
        constexpr int f = decltype(fc)::value;
        d[f & 1] = __builtin_amdgcn_mfma_f32_4x4x4bf16_1k(a, __builtin_bit_cast(s16x4, w[f]), d[f & 1], 4, f, 0);
      });
      f32x4 z;
#pragma unroll
      for (int e = 0; e < 4; ++e) { z[e] = d[0][e] + d[1][e]; asm volatile("" : "+v"(z[e])); }
      return u32x4{f2word(z[0]), f2word(z[1]), f2word(z[2]), f2word(z[3])};
    };
    auto fold8 = [&](f32x2 y0, f32x2 y1) __attribute__((always_inline)) { return dpp_add<0x128, 0xf>(fold16(y0, y1)); };
    auto fold4 = [&](f32x2 y0, f32x2 y1) __attribute__((always_inline)) { return dpp_add<0x124, 0xf>(fold8(y0, y1)); };
    auto y_sum = [&](const __amdgpu_buffer_rsrc_t rs, auto nbc) __attribute__((always_inline)) {
      constexpr int NB = decltype(nbc)::value, N = 14 / NB;
      const int ln = fresh_lane(), src = (GRAN_N + (ln >> 2) * D_ + ow * 16 + (ln & 3) * 4) * 4;
      f32x2 y0 = {0.f, 0.f}, y1 = {0.f, 0.f};
#pragma unroll 1
      for (int bt = 0; bt < NB; ++bt) {
        f32x2 z0, z1;
#pragma unroll 1
        for (int spin = 0; spin < SPIN_MAX; ++spin) {
          u32x4 v[N];
#pragma unroll
          for (int i = 0; i < N; ++i) v[i] = __builtin_amdgcn_raw_buffer_load_b128(rs, src, (bt * N + i) * (16 * D_ * 4), 16);
          z0 = y0; z1 = y1;
#pragma unroll
          for (int i = 0; i < N; ++i) {
            z0 = f32x2{fadd(z0[0], __uint_as_float(v[i][0])), fadd(z0[1], __uint_as_float(v[i][1]))};
            z1 = f32x2{fadd(z1[0], __uint_as_float(v[i][2])), fadd(z1[1], __uint_as_float(v[i][3]))};
          }
          const float chk = fadd(fadd(z0[0], z0[1]), fadd(z1[0], z1[1]));
          if (none(chk != chk)) break;
          unsigned mx = 0u;
#pragma unroll
          for (int i = 0; i < N; ++i) mx = umax(umax(umax(mx, v[i][0]), v[i][1]), umax(v[i][2], v[i][3]));
          if (none(mx == SENT)) break;
          __builtin_amdgcn_s_sleep(YSLP);
          asm volatile("" ::: "memory");
        }
        y0 = z0; y1 = z1;
      }
      return fold4(y0, y1);
    };
    [&]() __attribute__((always_inline)) {
    ARGS_HERE;
    const unsigned l16 = lane * 16;
    unsigned* kvflag = gran + GR_KV + ((LAY2 ? 16 : 0) + tni) * (32 * RK);
    auto wait_sig = [&](int sh) {
#pragma unroll 1
      for (int i = 0; i < 4 * SPIN_MAX; ++i) {
        if (((__builtin_amdgcn_readfirstlane(*LDS_AT(const volatile unsigned, lbase + FLG + 32)) >> sh) & 7u) >= 4u) break;
        __builtin_amdgcn_s_sleep(SSLP);
      }
      asm volatile("" ::: "memory");
      PIN();
    };
    PIN();
    asm volatile("s_waitcnt lgkmcnt(0)" ::: "memory");
    if (!LAY2) asm volatile("s_waitcnt vmcnt(0)" ::: "memory");
    __builtin_amdgcn_s_barrier();
    PIN();
    __builtin_amdgcn_s_setprio(HPRIO);
    if (hw < 3) {
      __builtin_amdgcn_s_sleep(SPRE);
      wait_sig(0);
      __builtin_amdgcn_raw_buffer_store_b32(tag, __builtin_amdgcn_make_buffer_rsrc(kvflag, 0, hw == 0 ? RK * 128 : 0, 0x00020000), lane * 128 + tmi * 4, 0, 16);
      if (hw == 0) STAMP(LAY2 ? 5 : 1, 16);
    }
    if (hw == 3) {
      __builtin_amdgcn_s_sleep(KVS);
      const unsigned kv0 = ld_ag(kvflag + (tmi % RK) * 32 + (lane & 15));
      PIN();
      const char* cnt = reinterpret_cast<const char*>(gran + GR_N + bid * 64 + 2);
      unsigned vk[4], vp = lbase + FLG + 8 + (lane & 7), pw = 0u;
#pragma unroll
      for (int j = 0; j < 4; ++j) { vk[j] = l16 + (j >> 1) * 4096u + (j & 1) * (VOFF - 2048u); asm volatile("" : "+v"(vk[j])); }
#define G4(a, b, c, d, sc) "s_nop 0\n\tglobal_load_lds_dwordx4 %" #a ", %4" sc "\n\tglobal_load_lds_dwordx4 %" #b ", %4 offset:1024" sc "\n\t"             \
                       "global_load_lds_dwordx4 %" #c ", %4 offset:2048" sc "\n\tglobal_load_lds_dwordx4 %" #d ", %4 offset:3072" sc "\n\t"
#define M4 "s_addk_i32 m0, 0x1000\n\t"
      auto stage = [&](const char* sp, unsigned dst) __attribute__((always_inline)) {
        asm volatile("s_mov_b32 m0, %5\n\t" G4(0, 0, 1, 1, " sc1") M4 G4(2, 2, 3, 3, " sc1")
                     :: "v"(vk[0]), "v"(vk[1]), "v"(vk[2]), "v"(vk[3]), "s"(sp), "s"(dst) : "m0", "scc", "memory");
      };
      auto count = [&]() __attribute__((always_inline)) {
        asm volatile("s_mov_b32 m0, %2\n\ts_nop 0\n\tglobal_load_lds_dword %0, %1 sc1" :: "v"(0u), "s"(cnt), "s"(lbase + FLG - 252) : "m0", "memory");
        cnt += 8;
      };
      auto freed = [&](unsigned n) __attribute__((always_inline)) {
        if (!none(pw < n)) {
#pragma unroll 1
          for (int i = 0; i < 4 * SPIN_MAX; ++i) {
            pw = *LDS_AT(const volatile unsigned char, vp);
            if (none(pw < n)) break;
            __builtin_amdgcn_s_sleep(W3SLP);
          }
        }
        asm volatile("" ::: "memory");
      };
      unsigned long long ord = (0xfedcba9876543210ull >> 4 & ~((1ull << 4 * (tmi & 7)) - 1ull)) | (0xfedcba9876543210ull & ((1ull << 4 * (tmi & 7)) - 1ull));
      ord ^= (tmi & 8) * 0x1111111111111111ull;
      unsigned seen;
      const char* kvb = reinterpret_cast<const char*>(KV) + (size_t)(tni * 2 + (tmi & 1)) * (64 * 2048);
      auto panel = [&]() __attribute__((always_inline)) {
        const unsigned pn = (unsigned)ord & 15u;
        ord >>= 4;
        if (__builtin_expect(!((seen >> pn) & 1u), 0)) {
#pragma unroll 1
          for (int i = 0; i < SPIN_MAX && !((seen >> pn) & 1u); ++i) seen = (unsigned)__ballot(ld_ag(kvflag + (tmi % RK) * 32 + (lane & 15)) == tag);
        }
        asm volatile("" ::: "memory");
        return kvb + pn * 8192u;
      };
      asm volatile("" : "+s"(ord), "+s"(kvb), "+s"(cnt));
      asm volatile("" : "+v"(vp), "+v"(pw));
      PIN();
      seen = (unsigned)__ballot(kv0 == tag);
      const char* sp = panel();
      wait_sig(16);
      stage(sp, lbase + 2 * KSB);
      count();
      const int ksp = LAY2 ? PNS / 2 : -1, ksg = LAY2 ? 64 : GF0;
      const u16* gp = gptr(wg, wu);
#pragma unroll 1
      for (int k = 1; k < 15; ++k) {
        if (__builtin_expect(k == ksp, 0)) {
          gu_setup();
          gate_up(xq, rmd);
          PIN();
          *LDS_AT(volatile unsigned char, lbase + FLG + 7) = (unsigned char)0x7f;
        }
        freed((unsigned)(0xba9876543211000ull >> 4 * k) & 15u);
        stage(sp + 2048, lbase + ((2 * k + 1) & 7) * KSB);
        if (__builtin_expect(k == 1, 0)) { cnt -= 4; count(); cnt -= 4; }
        sp = panel();
        stage(sp, lbase + ((2 * k + 2) & 7) * KSB);
        count();
        if (__builtin_expect((unsigned)(k - ksg) < 4u, 0)) gather(gp, k - ksg);
      }
      freed(12u);
      glds16s<true>(csf + 4096 + tni * 128, l16, lbase + UGL);
      stage(sp + 2048, lbase + 7 * KSB);
      count();
      const unsigned vwo = (unsigned)((lane >> 3) * D_ + oni * 64 + ((lane & 7) ^ ((lane >> 3) & 6)) * 8) * 2u;
      constexpr unsigned vs = 8u * D_ * 2u - 1024u;
#pragma unroll 1
      for (int i = 0; i < 6; ++i) {
        const int ns = (i + 5) & 7;
        freed(i < 3 ? 12u : 10u + i);
        const char* wp = reinterpret_cast<const char*>(wo) + (size_t)ns * (128 * D_ * 2);
#pragma unroll
        for (int j = 0; j < 4; ++j)
          glds16x4<false>(wp + j * (32u * D_ * 2u), vwo, vwo + vs, vwo + 2 * vs, vwo + 3 * vs, lbase + ns * ASB + j * 4096);
      }
      asm volatile("s_waitcnt vmcnt(0)" ::: "memory");
      return;
    }
    if (!LAY2) {
      auto until = [&](unsigned n) {
        while ((__builtin_amdgcn_readfirstlane(*LDS_AT(const volatile unsigned, lbase + FLG + 8)) & 255u) < n) __builtin_amdgcn_s_sleep(16);
        asm volatile("" ::: "memory");
        PIN();
      };
      if (hw != 1) {
        constexpr int N2 = HAND_N + Y_N, X3N = 3 * D_ / 2, N0 = X3N + PS_N;
        const __amdgpu_buffer_rsrc_t rsi = __builtin_amdgcn_make_buffer_rsrc(hand + (hw ? GRAN_N : 2 * HAND_N - X3N), 0, (hw ? N2 : N0) * 4, 0x00020000);
        until(IAT);
#pragma unroll 1
        for (int i = 0; i < ((hw ? N2 : N0) / 4 + 16383) / 16384; ++i) {
          __builtin_amdgcn_raw_buffer_store_b128(u32x4{SENT, SENT, SENT, SENT}, rsi, ((i * 256 + bid) * 64 + lane) * 16, 0, 16);
          __builtin_amdgcn_s_sleep(ISLP);
        }
      }
      const size_t og = (size_t)((fg - xcd * 28) / 7 * 256 + lane) * F_ + (xcd * 7 + fg % 7) * 64;
      const u16* p = gptr(wg, wu);
#pragma unroll 1
      for (unsigned n = T0; n < T0 + 13u; ++n) {
        until(n);
        if (hw == 1 && n < T0 + 8u) {
          const unsigned p0 = touch((((n - T0) & 1u) ? wu : wg) + og + (size_t)((n - T0) >> 1) * (64 * F_));
          asm volatile("" :: "v"(p0));
        }
        if (hw == 2 && n == TFL) {
          const int ln = fresh_lane() & 15;
          const unsigned p1 = ld_ag(gran + (ln < 4 ? GR_O + (((ln >> 1) * 16 + omi) * RO + oni % RO) * 64 + (ln & 1) * 32 :
                                            (ln < 8 ? GR_X + (tmi * RX + tni % RX) * 128 + (ln & 3) * 32 :
                                             (ln < 12 ? GR_Q + (256 + (tmi ^ 1) * 16 + tni) * 32 : GR_KV + ((16 + tni) * RK + tmi % RK) * 32))));
          asm volatile("" :: "v"(p1));
        }
        if (hw == 2 && n == TW) {
          const int ln = fresh_lane();
          unsigned long long a0, a1;
          CODE_ADDR("layers_kernel", a0); CODE_ADDR("end_marker_kernel", a1);
          const unsigned p1 = touch_code(a0, a1, 3 + loc, ln), p0 = touch(wo + (size_t)(384 + (loc / (32 / OMX)) * (256 / OMX) + ln) * D_ + oni * 64);
          asm volatile("" :: "v"(p0), "v"(p1));
        }
        if (n >= G0 && (n - G0) % GS == 0u && (n - G0) / GS < 4u) gather(p, (n - G0) / GS);
        if (n == UAT && hw == UHW) {
          u32x4 a[4][2], c[2];
#pragma unroll
          for (int i = 0; i < 4; ++i)
#pragma unroll
            for (int h = 0; h < 2; ++h) a[i][h] = *reinterpret_cast<const u32x4*>(wo + (size_t)(tni * 64 + tmi * 4 + i) * D_ + h * 512 + lane * 8);
#pragma unroll
          for (int h = 0; h < 2; ++h) c[h] = *reinterpret_cast<const u32x4*>(w_r + h * 512 + lane * 8);
          auto dot = [&](unsigned x, unsigned y, float z) { return __builtin_amdgcn_fdot2_f32_bf16(__builtin_bit_cast(bf16x2, x), __builtin_bit_cast(bf16x2, y), z, false); };
          PIN();
          __builtin_amdgcn_s_setprio(0);
#pragma unroll 1
          for (int i = 0; i < SPIN_MAX; ++i) {
            if (__builtin_amdgcn_readfirstlane(*LDS_AT(const volatile unsigned, lbase + FLG + 16)) >= UARR) break;
            __builtin_amdgcn_s_sleep(4);
          }
          asm volatile("s_waitcnt vmcnt(0)" ::: "memory");
          PIN();
          float wm = 0.f;
#pragma unroll
          for (int e = 0; e < 8; ++e) wm = dot(c[e >> 2][e & 3], 0x3f803f80u, wm);
          wm = wave_sum(wm);
          lds_wr(lbase + FLG + 60, __float_as_uint(wm));
          wm *= -1.0f / 1024.0f;
          f32x2 u[2], g[2];
#pragma unroll
          for (int i = 0; i < 4; ++i) {
            float p1 = 0.f, pw = 0.f, p2 = 0.f;
#pragma unroll
            for (int e = 0; e < 8; ++e) {
              const unsigned x = a[i][e >> 2][e & 3];
              p1 = dot(x, 0x3f803f80u, p1); pw = dot(x, c[e >> 2][e & 3], pw); p2 = dot(x, x, p2);
            }
            u[i >> 1][i & 1] = fmaf(p1, wm, pw); g[i >> 1][i & 1] = p2;
          }
          float us = fold4(u[0], u[1]), gs = fold4(g[0], g[1]);
          us = dpp_add<0x121, 0xf>(dpp_add<0x122, 0xf>(us)); gs = dpp_add<0x121, 0xf>(dpp_add<0x122, 0xf>(gs));
          __builtin_amdgcn_raw_buffer_store_b32(__float_as_uint((lane & 1) ? __builtin_amdgcn_sqrtf(gs) * (1.0f / 32.0f) : us),
              __builtin_amdgcn_make_buffer_rsrc(csf + 4096 + tni * 128, 0, 512, 0x00020000), (lane & 14) ? 512 : ((lane & 1) * 64 + tmi * 4 + (lane >> 4)) * 4, 0, 16);
        }
      }
      if (hw == 2 && bid == 0) {
        const __amdgpu_buffer_rsrc_t rsc = __builtin_amdgcn_make_buffer_rsrc(gran, 0, GR_N * 4, 0x00020000);
#pragma unroll 1
        for (int spin = 0; spin < SPIN_MAX; ++spin) {
          bool ok = true;
#pragma unroll
          for (int i = 0; i < 4; ++i) ok &= __builtin_amdgcn_raw_buffer_load_b32(rsc, (GR_ROW + (i * 64 + lane) * 32 + 1) * 4, 0, 16) == tag;
          if (__all(ok)) break;
          __builtin_amdgcn_s_sleep(8);
          asm volatile("" ::: "memory");
        }
        if (lane == 0) st_ag(gran, tag);
      }
      return;
    }
    unsigned* X2 = hand + GRAN_N + Y_N;
    unsigned* XS = X2 + X2_N;
    unsigned* P4 = XS + XS_N;
    unsigned* PA = P4 + P4_N;
    const __amdgpu_buffer_rsrc_t rsh = __builtin_amdgcn_make_buffer_rsrc(hand, 0, HAND_N * 4, 0x00020000);
    auto signal = [&](unsigned ph) {
      asm volatile("" ::: "memory");
      PIN();
      *LDS_AT(volatile unsigned char, lbase + FLG + 4 + hw) = (unsigned char)ph;
    };
    auto wait_all = [&](unsigned ph) {
#pragma unroll 1
      for (int i = 0; i < SPIN_MAX; ++i) {
        if (all_ge(__builtin_amdgcn_readfirstlane(*LDS_AT(const volatile unsigned, lbase + FLG + 4)), ph)) break;
        __builtin_amdgcn_s_sleep(WSLP);
      }
      asm volatile("" ::: "memory");
      PIN();
    };
    gu_setup();
    gate_up(xq, rmd);
    signal(hw == 2 ? 2u : 0x7fu);
    if (wid == 8) STAMP(dk, 2);
    if (hw < 2) {
#pragma unroll 1
      for (int i = 0; i < SPIN_MAX; ++i) {
        if (__builtin_amdgcn_readfirstlane(*LDS_AT(const volatile unsigned, lbase + FLG)) >= FSTART) break;
        __builtin_amdgcn_s_sleep(WSLP);
      }
      asm volatile("" ::: "memory");
      PIN();
      if (wid == 8) STAMP(dk, 10);
      u32x2 w[16];
      const __amdgpu_buffer_rsrc_t rd_ = __builtin_amdgcn_make_buffer_rsrc(const_cast<u16*>(wd), 0, D_ * F_ * 2, 0x00020000);
      auto ldw = [&](int c) __attribute__((always_inline)) {
#pragma unroll
        for (int f = 0; f < 16; ++f) w[f] = __builtin_amdgcn_raw_buffer_load_b64(rd_, lane * 8 + c * 512 + (f & 1) * 2048, (fg * 16 + (f & ~1)) * (D_ * 2) + hw * 1024, 0);
        PIN();
      };
      ldw(0);
      const unsigned pt = __builtin_amdgcn_raw_buffer_load_b32(rd_, (lane >> 2) * (D_ * 2) + (lane & 3) * 128 + 512, fg * 16 * (D_ * 2) + hw * 1024, 0);
      int yo = p1blk ? (GRAN_N + fg * D_ + hw * 512) * 4 + lane * 16 : HAND_N * 4;
      asm volatile("" : "+v"(yo));
      wait_all(2u);
      if (wid == 8) STAMP(dk, 3);
      const s16x4 a = act_diag();
      const u32x4 y0 = down(a, w);
      PIN();
      ldw(1);
      __builtin_amdgcn_raw_buffer_store_b128(y0, rsh, yo, 0, 16);
      PIN();
      __builtin_amdgcn_raw_buffer_store_b128(down(a, w), rsh, yo + 1024, 0, 16);
      asm volatile("" :: "v"(pt));
      if (wid == 8) STAMP(dk, 4);
    }
    PIN();
    [&]() __attribute__((always_inline)) {
    const int lane = fresh_lane();
    const float rmb = __uint_as_float(rmd << 16);
    auto first_words = [&](int src, auto nc) __attribute__((always_inline)) {
#pragma unroll 1
      for (int spin = 0; spin < SPIN_MAX; ++spin) {
        unsigned mx = 0u;
#pragma unroll
        for (int i = 0; i < decltype(nc)::value; ++i) mx = umax(mx, __builtin_amdgcn_raw_buffer_load_b32(rsh, src, i * (56 * D_ * 4), 16));
        if (none(mx == SENT)) break;
        __builtin_amdgcn_s_sleep(PSLP);
        asm volatile("" ::: "memory");
      }
    };
    if (hw == 2) {
      wait_all(2u);
      if (own) {
        const float rden = __uint_as_float(rmd & 0xffff0000u), rrin = frcp(rden);
        const int col = ow * 16 + (lane & 3) * 4 + (lane >> 4);
        const unsigned xo2 = ld_ag(reinterpret_cast<const unsigned*>(A + (size_t)idx * D_ + (col & ~1)));
        __builtin_amdgcn_s_sleep(PRE1);
        first_words((GRAN_N + (lane < 56 ? lane : 0) * D_ + ow * 16) * 4, std::integral_constant<int, 4>{});
        const float y = y_sum(rsh, std::integral_constant<int, 2>{});
        const unsigned rk = best >> 16;
        const float rv = __uint_as_float(((rk & 0x8000u) ? (rk & 0x7fffu) : (~rk & 0xffffu)) << 16);
        const float rs = rbf(frcp(1.0f + __expf(-rv)));
        const float x1v = __uint_as_float((col & 1) ? xo2 & 0xffff0000u : xo2 << 16);
        float h0 = x1v, h1 = x1v;
        ln_apply2(h0, h1, rmb, rden, rrin);
        const float x2v = rbf(x1v + rbf(h0 + rbf(rbf(y) * rs)));
        const auto pr = __builtin_amdgcn_permlane16_swap(__float_as_uint(x2v), __float_as_uint(x2v), false, false);
        __builtin_amdgcn_raw_buffer_store_b32(f2word(__uint_as_float((pr[0] >> 16) | pr[1])), rsh, (lane & 28) ? HAND_N * 4 : (int)(X2 - hand) * 4 + col * 2, 0, 16);
        const float d = x2v - rmb;
        const float sd = wave_sum(d), sq = wave_sum(d * d);
        if (lane < 2) st_ag(XS + ow * 2 + lane, f2word(lane ? sq : sd));
        STAMP(dk, 5);
      }
    }
    {
      const int ks = kcap ^ (tmi & 8), g4 = lane >> 4;
      const int src = (int)(X2 - hand) * 4 + ks * 128 + g4 * 16;
      int dst = (lane & 12) ? HAND_N * 4 : (int)((P4 - hand) + (tni * 16 + ks) * 192 + hw * 64 + (lane & 2) * 16 + (lane & 1) * 8 + (g4 >> 1) * 16 + (g4 & 1) * 4) * 4;
      asm volatile("" : "+v"(dst));
      u32x4 x0, x1;
      if (!(hw == 2 && own)) __builtin_amdgcn_s_sleep(PRE4);
#pragma unroll 1
      for (int spin = 0; spin < SPIN_MAX; ++spin) {
        x0 = __builtin_amdgcn_raw_buffer_load_b128(rsh, src, 0, 16); x1 = __builtin_amdgcn_raw_buffer_load_b128(rsh, src + 64, 0, 16);
        if (none(umax(umax(umax(x0[0], x0[1]), umax(x0[2], x0[3])), umax(umax(x1[0], x1[1]), umax(x1[2], x1[3]))) == SENT)) break;
        __builtin_amdgcn_s_sleep(PSLP);
        asm volatile("" ::: "memory");
      }
      f32x4 d[4];
#pragma unroll
      for (int i = 0; i < 4; ++i) d[i] = MFMA16(sl[2 * i], __builtin_bit_cast(s16x8, x0), (f32x4{0.f, 0.f, 0.f, 0.f}));
#pragma unroll
      for (int i = 0; i < 4; ++i) d[i] = MFMA16(sl[2 * i + 1], __builtin_bit_cast(s16x8, x1), d[i]);
      u32x4 o;
#pragma unroll
      for (int e = 0; e < 4; ++e) o[e] = f2word((lane & 2) ? ((lane & 1) ? d[3][e] : d[2][e]) : ((lane & 1) ? d[1][e] : d[0][e]));
      __builtin_amdgcn_raw_buffer_store_b128(o, rsh, dst, 0, 16);
      PIN();
      if (wid == 8) STAMP(dk, 6);
    }
    if (hw == 2) {
      const int col = (lane & 15) * 4 + (lane >> 4), src = (int)((P4 - hand) + (tni * 16 + (lane >> 4)) * 192 + (lane & 15) * 4) * 4;
      const __amdgpu_buffer_rsrc_t rsq = __builtin_amdgcn_make_buffer_rsrc(hand, 0, fast ? HAND_N * 4 : 0, 0x00020000);
      if (own) {
#pragma unroll
        for (int i = 0; i < 14; ++i)
          __builtin_amdgcn_raw_buffer_store_b128(u32x4{SENT, SENT, SENT, SENT}, rsh, (GRAN_N + (i * 16 + (lane >> 2)) * D_ + ow * 16 + (lane & 3) * 4) * 4, 0, 16);
      }
      float cs[3], val[3];
#pragma unroll
      for (int p = 0; p < 3; ++p) cs[p] = __uint_as_float(ld_ag(reinterpret_cast<const unsigned*>(csf) + p * 1024 + n0 + col));
      u32x2 xs;
      __builtin_amdgcn_s_sleep(PRE7);
      first_words((int)((P4 - hand) + (tni * 16 + (lane & 15)) * 192 + ((lane >> 4) % 3) * 64) * 4, std::integral_constant<int, 1>{});
#pragma unroll 1
      for (int spin = 0; spin < SPIN_MAX; ++spin) {
        u32x4 v[3][4];
        xs = __builtin_amdgcn_raw_buffer_load_b64(rsh, (int)((XS - hand) + lane * 2) * 4, 0, 16);
#pragma unroll
        for (int p = 2; p >= 0; --p)
#pragma unroll
          for (int i = 0; i < 4; ++i) v[p][i] = __builtin_amdgcn_raw_buffer_load_b128(p ? rsh : rsq, src + p * 256, i * (4 * 192 * 4), 16);
        auto part = [&](int p) __attribute__((always_inline)) {
          float z[4];
#pragma unroll
          for (int e = 0; e < 4; ++e) {
            z[e] = fadd(fadd(__uint_as_float(v[p][0][e]), __uint_as_float(v[p][1][e])), fadd(__uint_as_float(v[p][2][e]), __uint_as_float(v[p][3][e])));
          }
          val[p] = fold16(f32x2{z[0], z[1]}, f32x2{z[2], z[3]});
        };
        part(2); part(1);
        val[0] = 0.f;
        if (fast) part(0);
        const float chk = fadd(fadd(fadd(val[0], val[1]), fadd(val[2], __uint_as_float(xs[0]))), __uint_as_float(xs[1]));
        if (none(chk != chk)) break;
        unsigned mx = umax(xs[0], xs[1]);
#pragma unroll
        for (int i = 0; i < 12; ++i) mx = umax(umax(umax(mx, v[i >> 2][i & 3][0]), v[i >> 2][i & 3][1]), umax(v[i >> 2][i & 3][2], v[i >> 2][i & 3][3]));
        if (none(mx == SENT)) break;
        __builtin_amdgcn_s_sleep(PSLP);
        asm volatile("" ::: "memory");
      }
      const auto sw = __builtin_amdgcn_permlane32_swap(xs[0], xs[1], false, false);
      float sm = __uint_as_float(sw[0]) + __uint_as_float(sw[1]);
      sm = dpp_add<0x142, 0xa>(dpp_add<0x118, 0xf>(dpp_add<0x114, 0xf>(dpp_add<0x112, 0xf>(dpp_add<0x111, 0xf>(sm)))));
      const float dm = __int_as_float(__builtin_amdgcn_readlane(__float_as_int(sm), 31)) * (0.25f / 1024.0f);
      const float var = fmaxf(__int_as_float(__builtin_amdgcn_readlane(__float_as_int(sm), 63)) * (0.25f / 1024.0f) - dm * dm, 0.f);
      const float mb = rbf(rmb + dm);
      const float rin = frcp(rbf(__builtin_amdgcn_sqrtf(rbf(rbf(var) + 1e-5f))));
      const unsigned kv = pack2((val[1] - mb * cs[1]) * rin, (val[2] - mb * cs[2]) * rin);
      *LDS_AT(u16, lbase + TAB + 128 + kpos(col) * 2) = (u16)kv;
      *LDS_AT(u16, lbase + TAB + 256 + col * 2) = (u16)(kv >> 16);
      if (fast) *LDS_AT(u16, lbase + TAB + kpos(col) * 2) = f2bf((val[0] - mb * cs[0]) * rin * QSCALE);
      signal(7u);
      STAMP(dk, 7);
      return;
    }
    if (!fast) return;
    const int head = tni * 2 + hw;
    const unsigned tb = lbase + TAB + hw * 64;
    {
      const __amdgpu_buffer_rsrc_t rs = __builtin_amdgcn_make_buffer_rsrc(KV, 0, 2 * S_ * D_ * 2, 0x00020000);
      const int src = (head * 64 + tmi * 4) * 2048 + lane * 16;
      auto ld = [&](int i, unsigned off) { return __builtin_bit_cast(s16x8, __builtin_amdgcn_raw_buffer_load_b128(rs, src + (i & 3) * 1024, (i >> 3) * 65536 + ((i >> 2) & 1) * 4096 + off, 16)); };
#pragma unroll 1
      for (int i = 0; i < SPIN_MAX; ++i)
        if (__builtin_amdgcn_readfirstlane(ld_ag(kvflag + (tmi % RK) * 32 + tmi + 8)) == tag) break;
      asm volatile("" ::: "memory");
      s16x8 f[16];
#pragma unroll
      for (int i = 0; i < 16; ++i) f[i] = ld(i, 0u);
      PIN();
      const bool on = ((lane ^ (lane >> 4)) & 1) == 0;
      const unsigned pl = lbase + RED + hw * 1024;
      unsigned pw = pl + (lane >> 4) * 64 + (lane & 15) * 2, pr = pl + (on ? (lane >> 5) * 16 : 512), qr = on ? tb + (lane >> 5) * 16 : pl + 512;
      const int gone = (((idx >> 7) & 7) == tmi) ? ((idx >> 10) * 2 + (kpos(idx & 31) >> 4)) * 64 + ((idx >> 5) & 3) * 16 + (kpos(idx & 31) & 15) : -1;
      asm volatile("" : "+v"(pw), "+v"(pr), "+v"(qr));
      *LDS_AT(u32x2, pl + 512 + lane * 8) = u32x2{0u, 0u};
      wait_all(7u);
      u32x4 q[2];
#pragma unroll
      for (int sx = 0; sx < 2; ++sx) {
        q[sx] = *LDS_AT(const u32x4, qr + sx * 32);
      }
      float sc[4];
#pragma unroll
      for (int i = 0; i < 8; ++i) {
        f32x4 d = MFMA16(__builtin_bit_cast(s16x8, q[0]), f[2 * i], (f32x4{0.f, 0.f, 0.f, 0.f}));
        d = MFMA16(__builtin_bit_cast(s16x8, q[1]), f[2 * i + 1], d);
        PIN();
        f[2 * i] = ld(2 * i, VOFF); f[2 * i + 1] = ld(2 * i + 1, VOFF);
        PIN();
#pragma unroll
        for (int r = 0; r < 2; ++r) sc[(i >> 2) * 2 + r] = (i & 3) == 0 || (lane >> 4) == (i & 3) ? d[r] : sc[(i >> 2) * 2 + r];
      }
      if (gone >= 0) {
#pragma unroll
        for (int r = 0; r < 4; ++r) sc[r] = r * 64 + lane == gone ? -INFINITY : sc[r];
      }
      const float mx = wave_fmax(fmaxf(fmaxf(sc[0], sc[1]), fmaxf(sc[2], sc[3])));
      const unsigned p0 = pack2(__builtin_amdgcn_exp2f(sc[0] - mx), __builtin_amdgcn_exp2f(sc[1] - mx)), p1 = pack2(__builtin_amdgcn_exp2f(sc[2] - mx), __builtin_amdgcn_exp2f(sc[3] - mx));
      *LDS_AT(u16, pw) = (u16)p0; *LDS_AT(u16, pw + 32) = (u16)(p0 >> 16);
      *LDS_AT(u16, pw + 256) = (u16)p1; *LDS_AT(u16, pw + 288) = (u16)(p1 >> 16);
      asm volatile("" ::: "memory");
      const float ls = wave_sum((__uint_as_float(p0 << 16) + __uint_as_float(p0 & 0xffff0000u)) + (__uint_as_float(p1 << 16) + __uint_as_float(p1 & 0xffff0000u)));
      f32x4 o = {0.f, 0.f, 0.f, 0.f};
#pragma unroll
      for (int i = 0; i < 16; ++i) o = MFMA16(*LDS_AT(const s16x8, pr + i * 32), f[i], o);
      int pv = __float_as_int((lane & 16) ? o[1] : o[0]);
      asm volatile("s_nop 0\n\tv_writelane_b32 %0, %1, 32\n\tv_writelane_b32 %0, %2, 33" : "+v"(pv) : "s"(ls), "s"(mx));
      __builtin_amdgcn_raw_buffer_store_b32(f2word(__int_as_float(pv)), rsh, lane < 34 ? (int)((PA - hand) + (head * 8 + tmi) * 64 + lane) * 4 : HAND_N * 4, 0, 16);
    }
    if (wid == 8) STAMP(dk, 8);
    if (tmi != ((idx >> 7) & 7)) return;
    {
      const int src = (int)((PA - hand) + head * 512 + (lane < 34 ? lane : 0)) * 4;
      const unsigned ta = tb + (lane & 31) * 2;
      const float so = wave_sum(lane < 32 ? bf2f(*LDS_AT(const u16, ta)) * bf2f(*LDS_AT(const u16, ta + 128)) : 0.f);
      const float vo = lane < 32 ? bf2f(*LDS_AT(const u16, ta + 256)) : 1.0f;
      unsigned v[8];
      __builtin_amdgcn_s_sleep(PRE5);
#pragma unroll 1
      for (int spin = 0; spin < SPIN_MAX; ++spin) {
        unsigned mx = 0u;
#pragma unroll
        for (int i = 0; i < 8; ++i) v[i] = __builtin_amdgcn_raw_buffer_load_b32(rsh, src + i * 256, 0, 16);
#pragma unroll
        for (int i = 0; i < 8; ++i) mx = umax(mx, v[i]);
        if (none(mx == SENT)) break;
        __builtin_amdgcn_s_sleep(PSLP);
        asm volatile("" ::: "memory");
      }
      float m8[8], mx = so;
#pragma unroll
      for (int i = 0; i < 8; ++i) { m8[i] = __int_as_float(__builtin_amdgcn_readlane((int)v[i], 33)); mx = fmaxf(mx, m8[i]); }
      float acc = __builtin_amdgcn_exp2f(so - mx) * vo;
#pragma unroll
      for (int i = 0; i < 8; ++i) acc += __builtin_amdgcn_exp2f(m8[i] - mx) * __uint_as_float(v[i]);
      const float l = __int_as_float(__builtin_amdgcn_readlane(__float_as_int(acc), 32));
      const float ov = acc / l;
      const float o1 = __int_as_float(__builtin_amdgcn_update_dpp(0, __float_as_int(ov), 0x55, 0xf, 0xf, false));
      const float o2 = __int_as_float(__builtin_amdgcn_update_dpp(0, __float_as_int(ov), 0xaa, 0xf, 0xf, false));
      const float o3 = __int_as_float(__builtin_amdgcn_update_dpp(0, __float_as_int(ov), 0xff, 0xf, 0xf, false));
      const __amdgpu_buffer_rsrc_t rso = __builtin_amdgcn_make_buffer_rsrc(Of, 0, S_ * D_ * 2, 0x00020000);
      if (lane < 32 && (lane & 3) == 0)
        __builtin_amdgcn_raw_buffer_store_b64(u32x2{pack2(ov, o1), pack2(o2, o3)}, rso,
            (((idx >> 5) * 32 + head) * 1024 + (lane >> 4) * 512 + (((lane >> 2) & 1) * 32 + (idx & 31)) * 8 + ((lane >> 3) & 1) * 4) * 2, 0, 16);
      asm volatile("s_waitcnt vmcnt(0)" ::: "memory");
      if (lane < RO) st_ag(gran + GR_O + ((16 + (idx >> 7)) * RO + lane) * 64 + 32 + head, tag);
      STAMP(dk, 9);
    }
    }();
    }();
    __builtin_amdgcn_s_setprio(0);
    if (LAY2 || hw != UHW) asm volatile("s_waitcnt vmcnt(0)" ::: "memory");
    PIN();
    ARGS_HERE;
    u32x2 w[16];
    auto ld_wd = [&]() __attribute__((always_inline)) {
      const __amdgpu_buffer_rsrc_t rd_ = __builtin_amdgcn_make_buffer_rsrc(const_cast<u16*>(wd), 0, D_ * F_ * 2, 0x00020000);
#pragma unroll
      for (int f = 0; f < 16; ++f) w[f] = __builtin_amdgcn_raw_buffer_load_b64(rd_, t * 8 + (f & 1) * 2048, (fg * 16 + (f & ~1)) * (D_ * 2), 0);
      PIN();
    };
    float fnum = 0.f, fvar = 0.f;
    const int rrow = omi * 128 + (oni >> 3) * 64 + (hw & 1) * 32;
    if (LAY2) {
      ld_wd();
      if (!p1blk) {
        const int fr = rrow + (lane & 7) * 4 + (lane >> 4);
        const double s1 = ld_ag(stats + fr), s2 = ld_ag(stats + S_ + fr), sw = ld_ag(stats + 2 * S_ + fr);
        lds_wr(lbase + SPM + 132, __float_as_uint(wsum));
        float mean, var, mb, den;
        ln_from_sums(s1, s2, mean, var, mb, den);
        const float g1 = 1.0f + frcp(den);
        fnum = fr == idx ? __uint_as_float(0x7fc00000u) : fmaf(-mean, wsum, (float)sw) * g1;
        fvar = var * g1 * g1;
      }
      asm volatile("" : "+v"(fnum), "+v"(fvar));
      PIN();
    }
    if (hw == 3) {
      const int nfl = (LAY2 && (idx >> 7) == omi) ? 64 : 32;
      auto pfl = (__attribute__((address_space(1))) const unsigned*)(gran + GR_O + (((LAY2 ? 16 : 0) + omi) * RO + oni % RO) * 64 + (lane < nfl ? lane : 0));
      constexpr unsigned vs = 8u * D_ * 2u - 1024u;
      unsigned vw0 = (unsigned)((lane >> 3) * D_ + oni * 64 + ((lane & 7) ^ ((lane >> 3) & 6)) * 8) * 2u, vw1 = vw0 + vs, vw2 = vw0 + 2 * vs, vw3 = vw0 + 3 * vs;
      asm volatile("" : "+v"(pfl), "+v"(vw0), "+v"(vw1), "+v"(vw2), "+v"(vw3));
      auto wait_arr = [&](unsigned n) {
#pragma unroll 1
        for (int i = 0; i < 4 * SPIN_MAX; ++i) {
          if (__builtin_amdgcn_readfirstlane(*LDS_AT(const volatile unsigned, lbase + FLG + 16)) >= n) break;
          __builtin_amdgcn_s_sleep(1);
        }
        asm volatile("" ::: "memory");
        PIN();
      };
      wait_arr(12u);
      unsigned fl;
      asm volatile("global_load_dword %0, %1, off sc1" : "=v"(fl) : "v"(pfl) : "memory");
#pragma unroll 1
      for (int i = 0; i < 4 * SPIN_MAX; ++i) {
        if (__builtin_amdgcn_readfirstlane(*LDS_AT(const volatile unsigned, lbase + FLG + 24)) >= 8u) break;
        __builtin_amdgcn_s_sleep(1);
      }
      asm volatile("" ::: "memory");
      PIN();
#pragma unroll
      for (int j = 12; j < 20; ++j)
        glds16x4<false>(reinterpret_cast<const char*>(wo) + j * (32u * D_ * 2u), vw0, vw1, vw2, vw3, lbase + j * 4096);
      unsigned keep;
      asm volatile("s_mov_b32 %0, m0\n\ts_mov_b32 m0, %3\n\ts_nop 2\n\tglobal_load_lds_dword %1, %2 sc1\n\ts_mov_b32 m0, %0"
                   : "=&s"(keep) : "v"(0u), "s"(reinterpret_cast<const char*>(gran + GR_N + bid * 64 + 39)), "s"(lbase + FLG - 252) : "memory");
      asm volatile("s_waitcnt vmcnt(33)" : "+v"(fl) :: "memory");
      if (__builtin_amdgcn_ballot_w64(fl != tag)) {
        __builtin_amdgcn_s_sleep(CSL1);
#pragma unroll 1
        for (int spin = 0; spin < SPIN_MAX; ++spin) {
          if (!__builtin_amdgcn_ballot_w64(__hip_atomic_load(pfl, __ATOMIC_RELAXED, __HIP_MEMORY_SCOPE_AGENT) != tag)) break;
          __builtin_amdgcn_s_sleep(CSLP);
        }
      }
      asm volatile("" ::: "memory");
      PIN();
    }
    u32x4 pv[4];
    asm volatile("" : "=v"(pv[0]), "=v"(pv[1]), "=v"(pv[2]), "=v"(pv[3]));
    if (LAY2 && !p1blk && hw < 2) {
      const __amdgpu_buffer_rsrc_t rsp = __builtin_amdgcn_make_buffer_rsrc(hand + 2 * HAND_N + PP_O, 0, H_ * S_ * 4, 0x00020000);
      const int src = ((lane >> 3) * S_ + rrow + (lane & 7) * 4) * 4;
#pragma unroll 1
      for (int i = 0; i < 4 * SPIN_MAX; ++i) {
        if (__builtin_amdgcn_readfirstlane(*LDS_AT(const volatile unsigned, lbase + FLG + 24)) >= 8u) break;
        __builtin_amdgcn_s_sleep(2);
      }
#pragma unroll 1
      for (int spin = 0; spin < SPIN_MAX; ++spin) {
        asm volatile("" ::: "memory");
        unsigned mx = 0u;
#pragma unroll
        for (int i = 0; i < 4; ++i) pv[i] = __builtin_amdgcn_raw_buffer_load_b128(rsp, src, i * (8 * S_ * 4), 16);
#pragma unroll
        for (int i = 0; i < 4; ++i) mx = umax(umax(umax(mx, pv[i][0]), pv[i][1]), umax(pv[i][2], pv[i][3]));
        if (none(mx == SENT)) break;
        __builtin_amdgcn_s_sleep(PPSLP);
      }
    }
    asm volatile("s_waitcnt lgkmcnt(0)" ::: "memory");
    __builtin_amdgcn_s_barrier();
    if (!LAY2) {
      PIN();
      asm volatile(AGAIN "\ngate_up_code:\n\t"
                 "v_lshlrev_b32 v84, 16, v100\n\tv_and_b32 v85, 0xffff0000, v100\n\tv_lshlrev_b32 v86, 16, v101\n\tv_and_b32 v87, 0xffff0000, v101\n\t"
                 "v_subrev_f32 v84, s84, v84\n\tv_subrev_f32 v85, s84, v85\n\tv_subrev_f32 v86, s84, v86\n\tv_subrev_f32 v87, s84, v87\n\t"
                 "v_cvt_pk_bf16_f32 v85, v84, v85\n\tv_cvt_pk_bf16_f32 v87, v86, v87\n\ts_mov_b32 s88, 0x01000100\n\ts_mov_b32 s89, 0x03020302\n\t"
                 "v_lshlrev_b32 v84, 16, v85\n\tv_and_b32 v85, 0xffff0000, v85\n\tv_lshlrev_b32 v86, 16, v87\n\tv_and_b32 v87, 0xffff0000, v87\n\t"
                 "v_mul_f32 v88, s86, v84\n\tv_mul_f32 v89, s86, v85\n\tv_mul_f32 v90, s86, v86\n\tv_mul_f32 v91, s86, v87\n\tv_fma_f32 v84, -v88, s85, v84\n\t"
                 "v_fma_f32 v85, -v89, s85, v85\n\tv_fma_f32 v86, -v90, s85, v86\n\tv_fma_f32 v87, -v91, s85, v87\n\tv_fmac_f32 v88, s86, v84\n\t"
                 "v_fmac_f32 v89, s86, v85\n\tv_fmac_f32 v90, s86, v86\n\tv_fmac_f32 v91, s86, v87\n\tv_cvt_pk_bf16_f32 v88, v88, v89\n\t"
                 "v_cvt_pk_bf16_f32 v90, v90, v91\n\tv_perm_b32 v92, v88, v88, s88\n\tv_perm_b32 v93, v88, v88, s89\n\tv_perm_b32 v94, v90, v90, s88\n\t"
                 "v_perm_b32 v95, v90, v90, s89\n\ts_nop 0\n\tv_and_b32_dpp v96, v92, v102 quad_perm:[0,0,0,0] row_mask:0xf bank_mask:0xf\n\t"
                 "v_and_b32_dpp v97, v92, v103 quad_perm:[0,0,0,0] row_mask:0xf bank_mask:0xf\n\t"
                 "v_and_b32_dpp v100, v93, v102 quad_perm:[0,0,0,0] row_mask:0xf bank_mask:0xf\n\t"
                 "v_and_b32_dpp v101, v93, v103 quad_perm:[0,0,0,0] row_mask:0xf bank_mask:0xf\n\tv_mfma_f32_4x4x4_16b_bf16 v[84:87], v[96:97], v[104:105], 0\n\t"
                 "v_mfma_f32_4x4x4_16b_bf16 v[88:91], v[96:97], v[106:107], 0\n\tv_and_b32_dpp v96, v94, v102 quad_perm:[0,0,0,0] row_mask:0xf bank_mask:0xf\n\t"
                 "v_and_b32_dpp v97, v94, v103 quad_perm:[0,0,0,0] row_mask:0xf bank_mask:0xf\n\t"
                 "v_mfma_f32_4x4x4_16b_bf16 v[84:87], v[100:101], v[108:109], v[84:87]\n\tv_mfma_f32_4x4x4_16b_bf16 v[88:91], v[100:101], v[110:111], v[88:91]\n\t"
                 "v_and_b32_dpp v100, v95, v102 quad_perm:[0,0,0,0] row_mask:0xf bank_mask:0xf\n\t"
                 "v_and_b32_dpp v101, v95, v103 quad_perm:[0,0,0,0] row_mask:0xf bank_mask:0xf\n\tv_mfma_f32_4x4x4_16b_bf16 v[84:87], v[96:97], v[112:113], v[84:87]\n\t"
                 "v_mfma_f32_4x4x4_16b_bf16 v[88:91], v[96:97], v[114:115], v[88:91]\n\tv_and_b32_dpp v96, v92, v102 quad_perm:[1,1,1,1] row_mask:0xf bank_mask:0xf\n\t"
                 "v_and_b32_dpp v97, v92, v103 quad_perm:[1,1,1,1] row_mask:0xf bank_mask:0xf\n\t"
                 "v_mfma_f32_4x4x4_16b_bf16 v[84:87], v[100:101], v[116:117], v[84:87]\n\tv_mfma_f32_4x4x4_16b_bf16 v[88:91], v[100:101], v[118:119], v[88:91]\n\t"
                 "v_and_b32_dpp v100, v93, v102 quad_perm:[1,1,1,1] row_mask:0xf bank_mask:0xf\n\t"
                 "v_and_b32_dpp v101, v93, v103 quad_perm:[1,1,1,1] row_mask:0xf bank_mask:0xf\n\tv_mfma_f32_4x4x4_16b_bf16 v[84:87], v[96:97], v[120:121], v[84:87]\n\t"
                 "v_mfma_f32_4x4x4_16b_bf16 v[88:91], v[96:97], v[122:123], v[88:91]\n\tv_and_b32_dpp v96, v94, v102 quad_perm:[1,1,1,1] row_mask:0xf bank_mask:0xf\n\t"
                 "v_and_b32_dpp v97, v94, v103 quad_perm:[1,1,1,1] row_mask:0xf bank_mask:0xf\n\t"
                 "v_mfma_f32_4x4x4_16b_bf16 v[84:87], v[100:101], v[124:125], v[84:87]\n\tv_mfma_f32_4x4x4_16b_bf16 v[88:91], v[100:101], v[126:127], v[88:91]\n\t"
                 "v_and_b32_dpp v100, v95, v102 quad_perm:[1,1,1,1] row_mask:0xf bank_mask:0xf\n\t"
                 "v_and_b32_dpp v101, v95, v103 quad_perm:[1,1,1,1] row_mask:0xf bank_mask:0xf\n\tv_mfma_f32_4x4x4_16b_bf16 v[84:87], v[96:97], v[128:129], v[84:87]\n\t"
                 "v_mfma_f32_4x4x4_16b_bf16 v[88:91], v[96:97], v[130:131], v[88:91]\n\tv_and_b32_dpp v96, v92, v102 quad_perm:[2,2,2,2] row_mask:0xf bank_mask:0xf\n\t"
                 "v_and_b32_dpp v97, v92, v103 quad_perm:[2,2,2,2] row_mask:0xf bank_mask:0xf\n\t"
                 "v_mfma_f32_4x4x4_16b_bf16 v[84:87], v[100:101], v[132:133], v[84:87]\n\tv_mfma_f32_4x4x4_16b_bf16 v[88:91], v[100:101], v[134:135], v[88:91]\n\t"
                 "v_and_b32_dpp v100, v93, v102 quad_perm:[2,2,2,2] row_mask:0xf bank_mask:0xf\n\t"
                 "v_and_b32_dpp v101, v93, v103 quad_perm:[2,2,2,2] row_mask:0xf bank_mask:0xf\n\tv_mfma_f32_4x4x4_16b_bf16 v[84:87], v[96:97], v[136:137], v[84:87]\n\t"
                 "v_mfma_f32_4x4x4_16b_bf16 v[88:91], v[96:97], v[138:139], v[88:91]\n\tv_and_b32_dpp v96, v94, v102 quad_perm:[2,2,2,2] row_mask:0xf bank_mask:0xf\n\t"
                 "v_and_b32_dpp v97, v94, v103 quad_perm:[2,2,2,2] row_mask:0xf bank_mask:0xf\n\t"
                 "v_mfma_f32_4x4x4_16b_bf16 v[84:87], v[100:101], v[140:141], v[84:87]\n\tv_mfma_f32_4x4x4_16b_bf16 v[88:91], v[100:101], v[142:143], v[88:91]\n\t"
                 "v_and_b32_dpp v100, v95, v102 quad_perm:[2,2,2,2] row_mask:0xf bank_mask:0xf\n\t"
                 "v_and_b32_dpp v101, v95, v103 quad_perm:[2,2,2,2] row_mask:0xf bank_mask:0xf\n\tv_mfma_f32_4x4x4_16b_bf16 v[84:87], v[96:97], v[144:145], v[84:87]\n\t"
                 "v_mfma_f32_4x4x4_16b_bf16 v[88:91], v[96:97], v[146:147], v[88:91]\n\tv_and_b32_dpp v96, v92, v102 quad_perm:[3,3,3,3] row_mask:0xf bank_mask:0xf\n\t"
                 "v_and_b32_dpp v97, v92, v103 quad_perm:[3,3,3,3] row_mask:0xf bank_mask:0xf\n\t"
                 "v_mfma_f32_4x4x4_16b_bf16 v[84:87], v[100:101], v[148:149], v[84:87]\n\tv_mfma_f32_4x4x4_16b_bf16 v[88:91], v[100:101], v[150:151], v[88:91]\n\t"
                 "v_and_b32_dpp v100, v93, v102 quad_perm:[3,3,3,3] row_mask:0xf bank_mask:0xf\n\t"
                 "v_and_b32_dpp v101, v93, v103 quad_perm:[3,3,3,3] row_mask:0xf bank_mask:0xf\n\tv_mfma_f32_4x4x4_16b_bf16 v[84:87], v[96:97], v[152:153], v[84:87]\n\t"
                 "v_mfma_f32_4x4x4_16b_bf16 v[88:91], v[96:97], v[154:155], v[88:91]\n\tv_and_b32_dpp v96, v94, v102 quad_perm:[3,3,3,3] row_mask:0xf bank_mask:0xf\n\t"
                 "v_and_b32_dpp v97, v94, v103 quad_perm:[3,3,3,3] row_mask:0xf bank_mask:0xf\n\t"
                 "v_mfma_f32_4x4x4_16b_bf16 v[84:87], v[100:101], v[156:157], v[84:87]\n\tv_mfma_f32_4x4x4_16b_bf16 v[88:91], v[100:101], v[158:159], v[88:91]\n\t"
                 "v_and_b32_dpp v100, v95, v102 quad_perm:[3,3,3,3] row_mask:0xf bank_mask:0xf\n\t"
                 "v_and_b32_dpp v101, v95, v103 quad_perm:[3,3,3,3] row_mask:0xf bank_mask:0xf\n\tv_mfma_f32_4x4x4_16b_bf16 v[84:87], v[96:97], v[160:161], v[84:87]\n\t"
                 "v_mfma_f32_4x4x4_16b_bf16 v[88:91], v[96:97], v[162:163], v[88:91]\n\ts_nop 1\n\t"
                 "v_mfma_f32_4x4x4_16b_bf16 v[84:87], v[100:101], v[164:165], v[84:87]\n\tv_mfma_f32_4x4x4_16b_bf16 v[88:91], v[100:101], v[166:167], v[88:91]\n\t"
                 "s_nop 4\n\tv_permlane32_swap_b32 v84, v88\n\tv_permlane32_swap_b32 v85, v89\n\tv_permlane32_swap_b32 v86, v90\n\tv_permlane32_swap_b32 v87, v91\n\t"
                 "v_add_f32 v84, v84, v88\n\tv_add_f32 v85, v85, v89\n\tv_add_f32 v86, v86, v90\n\tv_add_f32 v87, v87, v91\n\ts_nop 0\n\t"
                 "v_permlane16_swap_b32 v84, v85\n\tv_permlane16_swap_b32 v86, v87\n\ts_nop 0\n\tv_add_f32 v84, v84, v85\n\tv_add_f32 v86, v86, v87\n\ts_nop 1\n\t"
                 "v_add_f32_dpp v84, v84, v84 row_ror:8 row_mask:0xf bank_mask:0xf\n\tv_add_f32_dpp v86, v86, v86 row_ror:8 row_mask:0xf bank_mask:0xf\n\ts_nop 0\n\t"
                 "v_add_f32_dpp v84, v84, v84 row_ror:4 row_mask:0xf bank_mask:0xf\n\tv_add_f32_dpp v86, v86, v86 row_ror:4 row_mask:0xf bank_mask:0xf\n\t"
                 "ds_write2_b32 v99, v84, v86 offset1:8\n\ts_setpc_b64 s[92:93]\n\t"
                   :: "{s[0:1]}"(ap + 1), "{s2}"(bid), "{v0}"(wid * 64), KEPT() : "s90", "s91", "scc", "memory");
      __builtin_unreachable();
    }
    {
    constexpr int dk = 8;
    constexpr int BEST = FLG + 40, MEET = FLG + 48;
    __builtin_amdgcn_s_setprio(HPRIO);
    if (hw == 0) STAMP(dk, 0);
    const unsigned tb1 = (unsigned)__builtin_amdgcn_s_memrealtime();
    unsigned* hand2 = hand + HAND_N;
    const __amdgpu_buffer_rsrc_t rsh = __builtin_amdgcn_make_buffer_rsrc(hand2, 0, (HAND_N + PS_N) * 4, 0x00020000);
    const __amdgpu_buffer_rsrc_t rsx = __builtin_amdgcn_make_buffer_rsrc(xout, 0, 3 * D_ * 2, 0x00020000);
    constexpr int OOR = (HAND_N + PS_N) * 4;
    if (!p1blk && hw < 2) {
      f32x2 a[2], c[2];
#pragma unroll
      for (int e = 0; e < 4; ++e) {
        a[e >> 1][e & 1] = (__uint_as_float(pv[0][e] << 16) + __uint_as_float(pv[1][e] << 16)) + (__uint_as_float(pv[2][e] << 16) + __uint_as_float(pv[3][e] << 16));
        c[e >> 1][e & 1] = (__uint_as_float(pv[0][e] & 0xffff0000u) + __uint_as_float(pv[1][e] & 0xffff0000u)) + (__uint_as_float(pv[2][e] & 0xffff0000u) + __uint_as_float(pv[3][e] & 0xffff0000u));
      }
      const float r = (fnum + fold8(a[0], a[1])) * __builtin_amdgcn_rsqf(fvar + fold8(c[0], c[1]));
      unsigned key = (__float_as_uint(r) ^ ((unsigned)(__float_as_int(r) >> 31) | 0x80000000u)) & 0xfffff800u;
      if (r != r) key = 0u;
      key = wave_umax(key | (unsigned)(2047 - (rrow + (lane & 7) * 4 + (lane >> 4))));
      __builtin_amdgcn_raw_buffer_store_b64(u32x2{key, tb1 & 0x7fffffffu}, rsh, lane < NREP ? (HAND_N + PK_O + lane * 128 + (rrow >> 5) * 2) * 4 : OOR, 0, 16);
      if (hw == 1) STAMP(dk, 2);
    }
    gu_setup();
    bool got = true;
    auto get_fc = [&]() __attribute__((always_inline)) {
      const u32x2 fw = __builtin_amdgcn_raw_buffer_load_b64(rsh, (HAND_N + PK_O + (bid % NREP) * 128 + lane * 2) * 4, 0, 16);
      got = none(umax(fw[0], fw[1]) == SENT);
      if (got) {
        const unsigned c1 = wave_umax(fw[0]), c2 = wave_umax(fw[0] == c1 ? 0u : fw[0]);
#define BADFC 0
        const unsigned r1 = (2047u - (c1 & 0x7ffu)) ^ BADFC, r2 = 2047u - (c2 & 0x7ffu);
        unsigned dt = (unsigned)((int)((fw[1] - tb1) << 1) >> 1) + 512u;
        dt = dt < 1024u ? dt : 512u;
        *LDS_AT(volatile u32x2, lbase + SPM + 144) = u32x2{r1 | (r2 << 16), (unsigned)__builtin_amdgcn_readlane(dt, r1 >> 5) | (wave_umax(dt) << 16)};
      }
    };
    if (hw == 0) {
      __builtin_amdgcn_s_sleep(FCPRE);
#pragma unroll 1
      for (int i = 0; i < SPIN_MAX; ++i) {
        if (__builtin_amdgcn_readfirstlane(*LDS_AT(const volatile unsigned, lbase + FLG + 28)) - best >= 4u) break;
        __builtin_amdgcn_s_sleep(FCSLP);
      }
      asm volatile("" ::: "memory");
      get_fc();
    } else __builtin_amdgcn_s_sleep(TPRE - 10);
    PIN();
    tile_at(2u);
    const unsigned tb2 = (unsigned)__builtin_amdgcn_s_memrealtime() - 512u;
    LOGDEF;
    LOG();
#pragma unroll 1
    for (int spin = 0; spin < SPIN_MAX && !got; ++spin) { get_fc(); asm volatile("" ::: "memory"); }
    PIN();
    u32x2 fm = {SENT, 0u};
#pragma unroll 1
    for (int i = 0; i < 4 * SPIN_MAX && fm[0] == SENT; ++i) {
      const u32x2 q = *LDS_AT(const volatile u32x2, lbase + SPM + 144);
      fm = u32x2{(unsigned)__builtin_amdgcn_readfirstlane(q[0]), (unsigned)__builtin_amdgcn_readfirstlane(q[1])};
    }
    const int fr1 = fm[0] & 0x7ffu, fr2 = (fm[0] >> 16) & 0x7ffu;
    const unsigned t1 = tb2 + (fm[1] & 1023u), tmx = tb2 + ((fm[1] >> 16) & 1023u);
    auto until = [&](unsigned te) { while ((int)((unsigned)__builtin_amdgcn_s_memrealtime() - te) < 0) __builtin_amdgcn_s_sleep(1); asm volatile("" ::: "memory"); };
    lds_wr(lbase + SPM + 136, tmx);
    PIN();
    if (hw == 0) STAMP(dk, 3);
    const int xoff = (hw * 256 + (lane & 3) * 64 + (lane >> 2) * 4) * 2;
    const int oc = (omi * 2 + (oni >> 3)) * 32, col = oc + (lane & 7) * 4 + (lane >> 4);
    const int xoo = !p1blk && hw == 3 ? (col & ~1) * 2 : S_ * D_ * 2;
    auto get_row = [&](int r, int sl, u32x2& xr, unsigned& xo) __attribute__((always_inline)) {
      if ((r >> 7) == omi) {
#pragma unroll 1
        for (int i = 0; i < SPIN_MAX; ++i) {
          if (__builtin_amdgcn_readfirstlane(*LDS_AT(const volatile unsigned, lbase + FLG + 24)) >= 16u) break;
          __builtin_amdgcn_s_sleep(1);
        }
        asm volatile("" ::: "memory");
      }
#pragma unroll 1
      for (int spin = 0; spin < SPIN_MAX; ++spin) {
        LOG();
        xr = __builtin_amdgcn_raw_buffer_load_b64(rsx, xoff, sl * (D_ * 2), 16);
        xo = __builtin_amdgcn_raw_buffer_load_b32(rsx, xoo, sl * (D_ * 2), 16);
        LOG();
        if (none(umax(umax(xr[0], xr[1]), xo) == SENT)) break;
        __builtin_amdgcn_s_sleep(XRSLP);
        asm volatile("" ::: "memory");
      }
      PIN();
    };
    auto row_md = [&](const u32x2& xr, int k) __attribute__((always_inline)) {
      auto dot = [&](unsigned x, unsigned y, float z) { return __builtin_amdgcn_fdot2_f32_bf16(__builtin_bit_cast(bf16x2, x), __builtin_bit_cast(bf16x2, y), z, false); };
      const float p1 = dot(xr[1], 0x3f803f80u, dot(xr[0], 0x3f803f80u, 0.f)), p2 = dot(xr[1], xr[1], dot(xr[0], xr[0], 0.f));
      const auto q = __builtin_amdgcn_permlane32_swap(__float_as_uint(p1), __float_as_uint(p2), false, false);
      const float qs = __uint_as_float(q[0]) + __uint_as_float(q[1]);
      const auto q2 = __builtin_amdgcn_permlane16_swap(__float_as_uint(qs), __float_as_uint(qs), false, false);
      float sv = __uint_as_float(q2[0]) + __uint_as_float(q2[1]);
      sv = dpp_add<0x121, 0xf>(dpp_add<0x122, 0xf>(dpp_add<0x124, 0xf>(dpp_add<0x128, 0xf>(sv))));
      lds_wr(lbase + SPM + k * 64 + (lane >> 4) * 16 + hw * 4, f2word(sv));
      u32x4 a, c;
#pragma unroll 1
      for (int i = 0; i < SPIN_MAX; ++i) {
        a = lds_rd4(lbase + SPM + k * 64); c = lds_rd4(lbase + SPM + k * 64 + 32);
        if (__builtin_amdgcn_readfirstlane(umax(umax(a[0], a[1]), umax(a[2], a[3]))) != SENT) break;
      }
      auto sum4 = [&](const u32x4& v) { return (__uint_as_float(v[0]) + __uint_as_float(v[1])) + (__uint_as_float(v[2]) + __uint_as_float(v[3])); };
      const float mean = sum4(a) * (1.0f / 1024.0f), msq = sum4(c) * (1.0f / 1024.0f);
      const float den = rbf(__builtin_amdgcn_sqrtf(rbf(rbf(fmaxf(fmaf(-mean, mean, msq), 0.f)) + 1e-5f)));
      return (unsigned)__builtin_amdgcn_readfirstlane((pack2(mean, mean) & 0xffffu) | (__float_as_uint(den) & 0xffff0000u));
    };
    auto ffn = [&](const u32x2& xr, unsigned md, unsigned n, const __amdgpu_buffer_rsrc_t rs, int yo, auto&& off) __attribute__((always_inline)) {
      gate_up(xr, md);
      if (hw == 0 && n == 2u) STAMP(dk, 8);
      asm volatile("" ::: "memory");
      PIN();
      lds_wr(lbase + MEET + hw * 4, n + off());
      u32x4 mt;
#pragma unroll 1
      for (int i = 0; i < SPIN_MAX; ++i) {
        mt = lds_rd4(lbase + MEET);
        if (__builtin_amdgcn_readfirstlane(min4(mt)) >= n) break;
      }
      asm volatile("" ::: "memory");
      PIN();
      if (hw == 0 && n == 2u) STAMP(dk, 9);
      if (__builtin_amdgcn_readfirstlane(mt[0]) != n + 1u)
        __builtin_amdgcn_raw_buffer_store_b128(down(act_diag(n == 4u ? 512 : 0), w), rs, (yo + fg * D_) * 4 + t * 16, 0, 16);
      dad += n == 2u ? 512 : -512;
      if (hw == 0 && n == 2u) STAMP(dk, 4);
    };
    auto y_sum4 = [&](const __amdgpu_buffer_rsrc_t rs, int yo, int k) __attribute__((always_inline)) {
      const int src = (yo + (hw * 56 + (lane >> 3)) * D_ + oc + (lane & 7) * 4) * 4;
      u32x4 v[7];
#pragma unroll
      for (int i = 0; i < 7; ++i) asm volatile("" : "=v"(v[i]));
      unsigned todo = 0x7fu;
#pragma unroll 1
      for (int spin = 0; spin < SPIN_MAX; ++spin) {
        LOG();
#pragma unroll
        for (int i = 0; i < 7; ++i) if ((todo >> i) & 1u) v[i] = __builtin_amdgcn_raw_buffer_load_b128(rs, src, i * (8 * D_ * 4), 16);
#pragma unroll
        for (int i = 0; i < 7; ++i) if (none(umax(umax(v[i][0], v[i][1]), umax(v[i][2], v[i][3])) == SENT)) todo &= ~(1u << i);
        if (!todo) break;
        __builtin_amdgcn_s_sleep(YSLP);
        asm volatile("" ::: "memory");
      }
      f32x2 z0 = {0.f, 0.f}, z1 = {0.f, 0.f};
#pragma unroll
      for (int i = 0; i < 7; ++i) {
        z0 += f32x2{__uint_as_float(v[i][0]), __uint_as_float(v[i][1])};
        z1 += f32x2{__uint_as_float(v[i][2]), __uint_as_float(v[i][3])};
      }
      const unsigned ya = lbase + SPM + 256 + k * 512 + ((lane & 7) * 4 + (lane >> 4)) * 16;
      lds_wr((lane & 8) ? lbase + DUM + lane * 4 : ya + hw * 4, f2word(fold8(z0, z1)));
      u32x4 q = {0u, 0u, 0u, 0u};
      if (hw == 3) {
#pragma unroll 1
        for (int i = 0; i < 4 * SPIN_MAX; ++i) {
          q = lds_rd4(ya);
          if (none(umax(umax(q[0], q[1]), umax(q[2], q[3])) == SENT)) break;
        }
      }
      return (__uint_as_float(q[0]) + __uint_as_float(q[1])) + (__uint_as_float(q[2]) + __uint_as_float(q[3]));
    };
    const __amdgpu_buffer_rsrc_t rs1 = __builtin_amdgcn_make_buffer_rsrc(hand, 0, HAND_N * 4, 0x00020000);
    u32x2 xr;
    unsigned xo, xo2;
    until(t1 + XDT);
    get_row(fr1, 0, xr, xo);
    if (hw == 0) STAMP(dk, 7);
#define FAILFC 0
    const unsigned md1 = row_md(xr, 0) ^ (FAILFC & 1);
    if (hw == 0) STAMP(dk, 10);
    LOGAT(20);
    u32x2 xr2;
    unsigned md2 = 0u;
    if (p1blk) {
      xr2 = __builtin_amdgcn_raw_buffer_load_b64(rsx, (fr2 >> 7) == omi ? S_ * D_ * 2 : xoff, D_ * 2, 16);
      PIN();
      ffn(xr, md1, 2u, rsh, GRAN_N, [&]() { return 0u; });
      __builtin_amdgcn_s_setprio(R2PRIO);
      LOG();
      if ((fr2 >> 7) == omi || !none(umax(xr2[0], xr2[1]) == SENT)) get_row(fr2, 1, xr2, xo2);
      md2 = row_md(xr2, 1) ^ (FAILFC & 2);
      ffn(xr2, md2, 4u, rs1, GRAN_N, [&]() {
        const u32x2 v = *LDS_AT(const volatile u32x2, lbase + BEST);
        return v[0] != 0u && (2047 - (int)(v[0] & 0x7ffu) != fr2 || v[1] != md2) ? 1u : 0u; });
    }
    if (hw == 0) STAMP(dk, 5);
    LOG();
    float y = 0.f;
    auto put_row = [&](int r, unsigned md, unsigned rb) __attribute__((always_inline)) {
      const float rden = __uint_as_float(md & 0xffff0000u), rs = rbf(frcp(1.0f + __expf(-__uint_as_float(rb << 16))));
      const float xa = __uint_as_float((col & 1) ? xo & 0xffff0000u : xo << 16);
      float ha = xa, hb = xa;
      ln_apply2(ha, hb, __uint_as_float(md << 16), rden, frcp(rden));
      const float o = xa + rbf(ha + rbf(rbf(y) * rs));
      __builtin_amdgcn_raw_buffer_store_b16((unsigned short)(pack2(o, o) & 0xffffu), __builtin_amdgcn_make_buffer_rsrc(out, 0, S_ * D_ * 2, 0x00020000),
                                            (lane & 8) ? S_ * D_ * 2 : (r * D_ + col) * 2, 0, OAUX);
    };
    if (!p1blk) {
      until(tmx + XD2);
      get_row(fr2, 1, xr2, xo2);
      md2 = row_md(xr2, 1) ^ (FAILFC & 2);
      until(t1 + XDT + YD1); until(tmx + YD2);
      if (hw == 3) STAMP(dk, 13);
      LOGAT(8);
      y = y_sum4(rsh, GRAN_N, 0);
      if (hw == 3) STAMP(dk, 14);
      LOGAT(24);
    }
    unsigned bmd = 0u;
    best = 0u;
#pragma unroll 1
    for (int spin = 0; spin < 4 * SPIN_MAX; ++spin) {
      const u32x2 v = *LDS_AT(const volatile u32x2, lbase + BEST);
      best = __builtin_amdgcn_readfirstlane(v[0]); bmd = __builtin_amdgcn_readfirstlane(v[1]);
      if (best) break;
      __builtin_amdgcn_s_sleep(1);
    }
    asm volatile("" ::: "memory");
    const int idx = 2047 - (int)(best & 0x7ffu);
    const unsigned rk = best >> 16, rb = (rk & 0x8000u) ? (rk & 0x7fffu) : (~rk & 0xffffu);
    if (hw == 0) STAMP(dk, 1);
    LOGAT(25);
    LOGOUT(hw);
    if (__builtin_expect(idx != fr1 || bmd != md1, 0)) {
      if (idx == fr2 && bmd == md2) {
        if (p1blk) return;
        y = y_sum4(rs1, GRAN_N, 1);
        xo = xo2;
      } else {
        get_row(idx, idx == fr1 ? 0 : (idx == fr2 ? 1 : 2), xr, xo);
        if (p1blk) ffn(xr, bmd, 6u, rsh, HAND_N + Y3_O, [&]() { return 0u; });
        else y = y_sum4(rsh, HAND_N + Y3_O, 2);
        if (p1blk || hw != 3) return;
#pragma unroll 1
        for (int spin = 0; spin < SPIN_MAX && idx != fr1 && idx != fr2; ++spin) {
          if (__builtin_amdgcn_readfirstlane(ld_ag(gran + GR_F + (oc >> 6) * 32)) == tag) break;
          asm volatile("" ::: "memory");
        }
      }
    }
    if (p1blk || hw != 3) return;
    put_row(idx, bmd, rb);
    }
    return;
  }
  if (wid >= 4) {
    const int lane = fresh_lane();
    const int lw = wid - 4;
    const long long lay_ = ap->lay;
    const u16* A = ap->a; const u16* W = ap->W; const u16* wc = ap->wc;
#define LPRIO 3
#define LP0 3
    __builtin_amdgcn_s_setprio(LP0);
    unsigned va[4], vw[6];
    const unsigned ul = lane;
    va[0] = (unsigned)(m0 + lw * 32) * (K * 2) + (ul >> 3) * (K * 2) + (((ul & 7u) ^ (ul >> 4)) << 4);
    va[1] = va[0] + (8 * K * 2 - 1024);
    va[2] = (va[0] ^ 64u) + 2 * (8 * K * 2 - 1024);
    va[3] = va[2] + (8 * K * 2 - 1024);
#pragma unroll
    for (int i = 0; i < 3; ++i) {
      const unsigned n = ul + 64u * i, k = __umul24(n, 171u) >> 12, cp = n - __umul24(k, 24u);
      vw[i] = __umul24(k, ldw * 2) + (cp >> 3) * 2048u + (((cp & 7u) ^ ((k & 2u) << 1)) << 4) + ((unsigned)(lw * 16 * ldw + n0) * 2u - i * 1024u);
      vw[i + 3] = (vw[i] ^ 16u) + 8 * ldw * 2;
    }
    PIN();
    asm volatile("" : "+v"(va[0]), "+v"(va[1]), "+v"(va[2]), "+v"(va[3]), "+v"(vw[0]), "+v"(vw[1]), "+v"(vw[2]), "+v"(vw[3]), "+v"(vw[4]), "+v"(vw[5]));
    const bool LAY2 = lay_ != 0;
    const int krot = tmi & 8;
    if (LAY2 && slow_cols(W, tni)) W = wc;
    auto stage_a = [&](int kt, int slot) {
      const char* pa = reinterpret_cast<const char*>(A) + (size_t)(kt ^ krot) * 128;
      asm volatile("s_mov_b32 m0, %5\n\ts_nop 0\n\tglobal_load_lds_dwordx4 %0, %4 sc1\n\tglobal_load_lds_dwordx4 %1, %4 offset:1024 sc1\n\t"
                   "global_load_lds_dwordx4 %2, %4 offset:2048 sc1\n\tglobal_load_lds_dwordx4 %3, %4 offset:3072 sc1"
                   :: "v"(va[0]), "v"(va[1]), "v"(va[2]), "v"(va[3]), "s"(pa), "s"(lbase + slot * STB + lw * 4096) : "m0", "memory");
    };
    auto stage_w = [&](int kt, int slot) {
      const char* pw = reinterpret_cast<const char*>(W) + (size_t)(kt ^ krot) * 128 * ldw;
      asm volatile("s_mov_b32 m0, %7\n\ts_nop 0\n\tglobal_load_lds_dwordx4 %0, %6 sc1\n\tglobal_load_lds_dwordx4 %1, %6 offset:1024 sc1\n\t"
                   "global_load_lds_dwordx4 %2, %6 offset:2048 sc1\n\ts_addk_i32 m0, 0xc00\n\ts_nop 0\n\tglobal_load_lds_dwordx4 %3, %6 sc1\n\t"
                   "global_load_lds_dwordx4 %4, %6 offset:1024 sc1\n\tglobal_load_lds_dwordx4 %5, %6 offset:2048 sc1"
                   :: "v"(vw[0]), "v"(vw[1]), "v"(vw[2]), "v"(vw[3]), "v"(vw[4]), "v"(vw[5]), "s"(pw), "s"(lbase + slot * STB + ABYTES + lw * 6144) : "m0", "scc", "memory");
    };
    if (LAY2) {
      __builtin_amdgcn_s_setprio(0);
      __builtin_amdgcn_s_sleep(TPRE - 10);
      tile_at(3u);
      __builtin_amdgcn_s_setprio(LPRIO);
#pragma unroll
      for (int st = 0; st < NST; ++st) stage_w(st, st);
      PIN();
      __builtin_amdgcn_s_barrier();
      PIN();
#pragma unroll
      for (int st = 0; st < NST; ++st) stage_a(st, st);
      asm volatile("s_waitcnt vmcnt(8)" ::: "memory");
    } else {
      __builtin_amdgcn_s_setprio(LPRIO);
      stage_a(0, 0); stage_w(0, 0);
      if (PRO > 1) { stage_a(1, 1); stage_w(1, 1); }
      if (PRO > 2) { stage_a(2, 2); stage_w(2, 2); }
      asm volatile("s_waitcnt vmcnt(%0)" :: "n"(10 * PRO - 10) : "memory");
    }
    int slot = 0;
    __builtin_amdgcn_s_barrier();
    if (!LAY2) {
      if (PRO < 2) { stage_a(1, 1); stage_w(1, 1); }
      if (PRO < 3) { stage_a(2, 2); stage_w(2, 2); }
    }
    for (int kt = 0; kt + 1 < NKT; ++kt) {
      if (LAY2 && kt == 0) asm volatile("s_waitcnt vmcnt(4)" ::: "memory");
      else if (kt + 2 >= NKT) asm volatile("s_waitcnt vmcnt(0)" ::: "memory");
      else asm volatile("s_waitcnt vmcnt(10)" ::: "memory");
      __builtin_amdgcn_s_barrier();
      if (kt + NST < NKT) { stage_a(kt + NST, slot); stage_w(kt + NST, slot); }
      slot = slot == NST - 1 ? 0 : slot + 1;
    }
    __builtin_amdgcn_s_setprio(0);
    __builtin_amdgcn_s_barrier();
  } else {
  LAYER_HERE;
  const int dk = LAY2 ? 5 : 1;
  const int lane = fresh_lane(), wm = wid & 1, wn = wid >> 1;
  if (wid == 0) STAMP(dk, 0);
  f32x4 acc[4][6];
#pragma unroll
  for (int i = 0; i < 4; ++i)
#pragma unroll
    for (int j = 0; j < 6; ++j) acc[i][j] = f32x4{0.f, 0.f, 0.f, 0.f};
  const int q16 = lane & 15, g4 = lane >> 4;
  const int e0 = 16 * (q16 >> 3) + (q16 & 7);
  s16x8 af[2][4];
  unsigned aad[2], wad[2];
  {
    const int row = wm * 64 + e0, swz = ((row >> 1) & 3) | (((row >> 4) & 1) << 2);
#pragma unroll
    for (int h = 0; h < 2; ++h) aad[h] = lbase + row * 128 + (((4 * h + g4) ^ swz) << 4);
    const int tq = (lane & 15) >> 2, tp = lane & 3, k = 8 * g4 + tq, ch = wn * 4 + 2 * (tp >> 1);
    wad[0] = lbase + ABYTES + k * 384 + ((ch ^ (4 * ((k >> 1) & 1)) ^ (g4 & 1)) << 4) + 8 * (tp & 1);
    wad[1] = wad[0] ^ 16;
  }
  auto lda = [&](unsigned a, int i) { return *LDS_AT(const s16x8, a + (i >> 1) * 4096 + (i & 1) * 1024); };
  auto ldw_ = [&](int h, int j) {
    const unsigned w = wad[j & 1] + h * 12288 + (j >> 1) * 128;
    s16x4 lo = __builtin_amdgcn_ds_read_tr16_b64_v4i16(LDS_AT(s16x4, w));
    s16x4 h4 = __builtin_amdgcn_ds_read_tr16_b64_v4i16(LDS_AT(s16x4, w + 1536));
    return (s16x8)__builtin_shufflevector(lo, h4, 0, 1, 2, 3, 4, 5, 6, 7);
  };
  ARGS_HERE;
  const bool give = wn != (tmi & 1);
  unsigned lto = lbase + (give ? 2 * KSB : 0) + wm * 4096, lqo = lbase + QOFF + wid * 4096;
  char* qgb = reinterpret_cast<char*>(KV) + QG + ((tmi * 16 + tni) * 2 + wm) * 4096 - lqo;
  unsigned* qfl = gran + GR_Q + ((LAY2 ? 256 : 0) + tmi * 16 + tni) * 32 + wm;
  char* kvb = reinterpret_cast<char*>(KV) + ((tni * 2 + wn) * 64 + tmi * 4 + wm * 2) * 2048 - lto;
  {
    const unsigned ato = lbase + ALPHA + wm * 256, dum = lbase + DUM + lane * 4;
    *LDS_AT(u32x4, lqo + lane * 16) = u32x4{lane >= 16 ? lbase + CRED : lbase + TABA + (wm * 64 + e0) * 16, lbase + TABW + (wn * 32 + e0) * 16,
                                                   ato + e0 * 4, lto + (lane >> 5) * 1024 + ((lane & 28) * 2 + (lane & 3)) * 16};
    *LDS_AT(u32x2, lqo + 2048 + lane * 16) = u32x2{aad[0], aad[1]};
    *LDS_AT(u32x4, lqo + 1024 + lane * 16) = u32x4{ato + g4 * 16 + (lane >> 5) * 32, lto + (lane >> 5) * 1024 + ((lane & 24) * 2 + (lane & 7)) * 16,
                                                          lane == 0 ? lbase + FLG + 32 : dum, lane == 0 ? lbase + FLG + 16 : dum};
  }
  unsigned qmv = lqo - lto;
  asm volatile("" : "+s"(lqo), "+s"(qmv), "+s"(kvb), "+s"(qgb), "+s"(qfl) :: "memory");
#pragma unroll
  for (int i = 0; i < 4; ++i)
#pragma unroll
    for (int j = 0; j < 6; ++j) asm volatile("" : "+v"(acc[i][j]));
  asm volatile("" : "+v"(aad[0]), "+v"(aad[1]), "+v"(wad[0]), "+v"(wad[1]));
  if (LAY2) __builtin_amdgcn_s_barrier();
  __builtin_amdgcn_s_barrier();
  PIN();
  if (wid == 0) STAMP(dk, 1);
  s16x8 wq[4][2];
  wq[0][0] = ldw_(0, 0); af[0][0] = lda(aad[0], 0); wq[0][1] = ldw_(1, 0); af[1][0] = lda(aad[1], 0); wq[1][0] = ldw_(0, 1); wq[1][1] = ldw_(1, 1);
  af[0][1] = lda(aad[0], 1); af[1][1] = lda(aad[1], 1); af[0][2] = lda(aad[0], 2); af[1][2] = lda(aad[1], 2);
  PIN();
  s16x4 wlo;
  auto rw = [&](s16x8& d, int h, int j, int hi, int off = 0) __attribute__((always_inline)) {
    const s16x4 v = __builtin_amdgcn_ds_read_tr16_b64_v4i16(LDS_AT(s16x4, wad[j & 1] + h * 12288 + (j >> 1) * 128 + hi * 1536 + off));
    if (hi) d = (s16x8)__builtin_shufflevector(wlo, v, 0, 1, 2, 3, 4, 5, 6, 7);
    else wlo = v;
  };
  auto at = [&](s16x8& x, int r) __attribute__((always_inline)) {
    if (r == 0) asm volatile("" :: "{v[104:107]}"(x));
    if (r == 1) asm volatile("" :: "{v[108:111]}"(x));
    if (r == 2) asm volatile("" :: "{v[112:115]}"(x));
    if (r == 3) asm volatile("" :: "{v[116:119]}"(x));
    if (r == 4) asm volatile("" :: "{v[120:123]}"(x));
    if (r == 5) asm volatile("" :: "{v[124:127]}"(x));
    if (r == 6) asm volatile("" :: "{v[128:131]}"(x));
    if (r == 7) asm volatile("" :: "{v[132:135]}"(x));
    if (r == 8) asm volatile("" :: "{v[136:139]}"(x));
    if (r == 9) asm volatile("" :: "{v[140:143]}"(x));
    if (r == 10) asm volatile("" :: "{v[144:147]}"(x));
    if (r == 11) asm volatile("" :: "{v[148:151]}"(x));
    if (r == 12) asm volatile("" :: "{v[152:155]}"(x));
    if (r == 13) asm volatile("" :: "{v[156:159]}"(x));
    if (r == 14) asm volatile("" :: "{v[160:163]}"(x));
    if (r == 15) asm volatile("" :: "{v[164:167]}"(x));
  };
  auto mm = [&](int i, int j, int h, int sl, int hw_) __attribute__((always_inline)) {
    s16x8 &a = af[h][i], &w = wq[sl][hw_];
    at(a, h * 4 + i); at(w, 8 + sl * 2 + hw_);
    if (j >= 4) asm volatile("v_mfma_f32_16x16x32_bf16 %0, %1, %2, %0" : "+v"(acc[i][j]) : "v"(a), "v"(w));
    else asm volatile("v_mfma_f32_16x16x32_bf16 %0, %1, %2, %0" : "+v"(acc[i][j]) : "v"(w), "v"(a));
    PIN();
  };
  auto next = [&](int g, int J0, int s0, int s1, int off) __attribute__((always_inline)) {
    if (g == 2 || g == 3) rw(wq[s0][0], 0, J0, g & 1, off);
    if (g == 4 || g == 5) af[g & 1][0] = lda(aad[g & 1] + off, 0);
    if (g == 6 || g == 7) rw(wq[s0][1], 1, J0, g & 1, off);
    if (g >= 8 && g < 12) rw(wq[s1][(g - 8) >> 1], (g - 8) >> 1, J0 + 1, g & 1, off);
    if (g >= 12) af[g & 1][(g - 10) >> 1] = lda(aad[g & 1] + off, (g - 10) >> 1);
  };
  auto third = [&](int p, int J0, int delta) __attribute__((always_inline)) {
    auto mv = [&](unsigned& x) __attribute__((always_inline)) { asm volatile("v_add_u32 %0, %1, %0" : "+v"(x) : "s"(delta)); };
#pragma unroll
    for (int g = 0; g < 16; ++g) {
      const int h = g & 1, i = p == 1 ? (g >> 1) & 3 : g >> 2, t = p == 1 ? g >> 3 : (g >> 1) & 1, j = 2 * p + t, sl = p == 1 ? 2 - 2 * t : p + t;
      mm(i, j, h, sl, h);
      if (p == 0) {
        if (g < 2) af[g][3] = lda(aad[g], 3);
        else if (g < 6) rw(wq[2][(g - 2) >> 1], (g - 2) >> 1, 2, g & 1);
        else if (g < 10) rw(wq[3][(g - 6) >> 1], (g - 6) >> 1, 5, g & 1);
        else if (g < 12) mv(aad[g - 10]);
      }
      if (p == 1) {
        if (g < 4) rw(wq[0][g >> 1], g >> 1, 3, g & 1);
        if (g == 4) mv(wad[1]);
        if (g >= 8 && g < 12) rw(wq[2][(g - 8) >> 1], (g - 8) >> 1, 4, g & 1);
        if (g == 12) mv(wad[0]);
      }
      if (p == 2) {
        if (g == BARG) {
          asm volatile("" ::: "memory");
          PIN();
          __builtin_amdgcn_s_barrier();
          asm volatile("" ::: "memory");
        }
        next(g, J0, 0, 1, 0);
      }
      PIN();
      {
        if ((p == 2 && g == 15) || (p == 0 && g == 7)) __builtin_amdgcn_s_waitcnt(0xc67f);
        if (p == 0 && g == 2) __builtin_amdgcn_s_waitcnt(0xc57f);
        if ((p == 0 && g == 14) || (p == 1 && (g & 7) == 7)) __builtin_amdgcn_s_waitcnt(0xc07f);
        PIN();
      }
    }
  };
  third(0, 0, STB); third(1, 0, STB);
  {
    static_assert((NKT - NST - 1) % NST == 0, "");
#pragma unroll 1
    for (int kt = 0; kt < NKT - NST - 1; kt += NST) {
#pragma unroll
      for (int u = 0; u < NST; ++u) {
        const int delta = u == 1 ? -(NST - 1) * STB : STB;
        third(2, 0, 0); third(0, 0, delta); third(1, 0, delta);
      }
    }
  }
  static_assert(NKT % NST == 1, "slots of the last stages");
#define TT(i)
  auto bar = [&]() { PIN(); __builtin_amdgcn_s_barrier(); asm volatile("" ::: "memory"); PIN(); };
  TT(0);
  third(2, 2, 0);
  const __amdgpu_buffer_rsrc_t rsk = __builtin_amdgcn_make_buffer_rsrc(kvb, 0, 0x7fffffff, 0x00020000), rsv = __builtin_amdgcn_make_buffer_rsrc(kvb + VOFF, 0, 0x7fffffff, 0x00020000);
  const __amdgpu_buffer_rsrc_t rsq = __builtin_amdgcn_make_buffer_rsrc(qgb, 0, give ? 0x7fffffff : 0, 0x00020000);
  unsigned pk;
  u32x2 t0, t1, t2, t3;
  auto park = [&](int i) { return *LDS_AT(const volatile u32x2, pk + (i >> 1) * 1024 + (i & 1) * 8); };
  float al[4];
  f32x4 av[4];
  u32x4 kvp[12];
  auto cacc = [&](int p, int x, int t) -> f32x4& { return p < 2 ? acc[x][2 * p + t] : acc[(x >> 1) * 2 + t][4 + (x & 1)]; };
  auto signal = [&](unsigned v) { __hip_atomic_fetch_add(LDS_AT(unsigned, t3[0]), v, __ATOMIC_RELAXED, __HIP_MEMORY_SCOPE_WORKGROUP); PIN(); };
  auto outop = [&](int p, int k) {
    if (k >= 0 && k < 16) {
      const int x = k >> 2, t = (k >> 1) & 1;
      f32x4& c = cacc(p, x, t);
#pragma unroll
      for (int o = 2 * (k & 1); o < 2 * (k & 1) + 2; ++o) {
        float r;
        asm volatile("v_mul_f32 %0, %1, %2" : "=v"(r) : "v"(c[o]), "v"(p < 2 ? al[x] : av[(x >> 1) * 2 + t][o]));
        c[o] = r;
      }
      PIN();
    }
    if (k >= 1 && k <= 16) {
      const int x = (k - 1) >> 2, t = ((k - 1) >> 1) & 1, o = (k - 1) & 1;
      kvp[p * 4 + x][2 * t + o] = pack2(cacc(p, x, t)[2 * o], cacc(p, x, t)[2 * o + 1]);
      PIN();
      if (t && o) {
        if (p) __builtin_amdgcn_raw_buffer_store_b128(kvp[p * 4 + x], p == 1 ? rsk : rsv, (p == 1 ? t1[1] : t2[1]) + (x >> 1) * 2048 + (x & 1) * (p == 1 ? 64 : 128), 0, 16);
        else {
          __builtin_amdgcn_raw_buffer_store_b128(kvp[x], rsq, t2[1] + (x >> 1) * 2048 + (x & 1) * 128, 0, 16);
          *LDS_AT(u32x4, t2[1] + (x >> 1) * 2048 + (x & 1) * 128) = kvp[x];
        }
        PIN();
      }
    }
  };
  static_for<0, 168>([&](auto nc) __attribute__((always_inline)) {
    constexpr int n = decltype(nc)::value, sec = n < 80 ? n >> 4 : (n < 104 ? 5 : 6 + ((n - 104) >> 4)), g = n < 104 ? n - 16 * sec : (n - 104) & 15;
    auto taba = [&](int i) { af[0][i] = *LDS_AT(const s16x8, t0[0] + (i >> 1) * 512 + (i & 1) * 128); };
    auto tabw = [&](int j) { wq[(2 + (j >> 1)) & 3][j & 1] = *LDS_AT(const s16x8, t0[1] + ((j >> 1) * 64 + (j & 1) * 8) * 16); };
    auto rwn = [&](int e, int sl, int j, int off) { if (e >= 0 && e < 4) rw(wq[sl][e >> 1], e >> 1, j, e & 1, off); };
    if (g == 0) TT(1 + sec);
    if (sec == 5) {
      const int i = (g / 12) * 2 + (g & 1), j = (g % 12) >> 1;
      mm(i, j, 0, (2 + (j >> 1)) & 3, j & 1);
    } else {
      constexpr int part[10] = {1, 2, 1, 2, 0, 0, 1, 2, 0, 0}, s0[10] = {0, 2, 0, 2, 0, 0, 1, 3, 1, 3};
      const int h = g & 1, i = sec == 7 ? (g >> 3) * 2 + ((g >> 1) & 1) : g >> 2, t = sec == 7 ? (g >> 2) & 1 : (g >> 1) & 1;
      mm(i, 2 * part[sec] + t, h, (s0[sec] + t) & 3, h);
    }
    if (sec == 0 || sec == 2) {
      if (g < 2) af[g][3] = lda(aad[g] + sec / 2 * STB, 3);
      rwn(g - 2, 2, 4, sec / 2 * STB); rwn(g - 6, 3, 5, sec / 2 * STB);
    }
    if (sec == 1) {
      if (g == 1) bar();
      next(g, 2, 0, 1, STB);
    }
    if ((sec == 3 && g == 12) || (sec == 6 && g == 13)) { pk = lqo + fresh_lane() * 16; asm volatile("" : "+v"(pk)); }
    if (sec == 3) {
      rwn(g, 0, 0, STB); rwn(g - 4, 1, 1, STB);
      if (g >= 8 && g < 12) asm volatile("v_subrev_u32 %0, %1, %0" : "+v"(g < 10 ? aad[g & 1] : wad[g & 1]) : "s"(STB));
      if (g == 13) t1 = park(1);
      if (g == 14) t0 = park(0);
    }
    if (sec == 4) {
      if (g == B15) bar();
      if (g == 4) taba(0);
      if (g == 5 || g == 6) tabw(g - 5);
      if (g == 7) taba(1);
      if (g == 8 || g == 9) tabw(g - 6);
      if (g == 10 || g == 11) af[1][g - 10] = lda(aad[1], g - 10);
      if (g == 12) taba(2);
      if (g == 15) taba(3);
    }
    if (sec == 5) {
      if (g < 2) tabw(4 + g);
      rwn(g - 2, 1, 2, 0);
      if (g == 12 || g == 13) af[0][g - 12] = lda(aad[0], g - 12);
      if (g == 22) { al[0] = *LDS_AT(const float, t1[0]); al[1] = *LDS_AT(const float, t1[0] + 32); }
      rwn(g - 16, 2, 3, 0);
      if (g == 20 || g == 21) af[1][g - 18] = lda(aad[1], g - 18);
    }
    if (sec == 6) {
      if (g < 2) af[0][2 + g] = lda(aad[0], 2 + g);
      rwn(g - 2, 3, 4, 0);
      if (g == 9) { al[2] = *LDS_AT(const float, t1[0] + 128); al[3] = *LDS_AT(const float, t1[0] + 160); }
      if (g == 14) t2 = park(2);
      if ((g & 3) >= 2) rwn((g - 10) / 4 * 2 + (g & 1) - (g < 10) * 8, 0, 5, 0);
    }
    if (sec == 7) {
      if (g == 2 || g == 3 || g == 10 || g == 11) av[(g >> 3) * 2 + (g & 1)] = *LDS_AT(const f32x4, t2[0] + ((g >> 3) * 32 + (g & 1) * 8) * 4);
      rwn(g < 2 ? g : (g < 4 ? -1 : g - 2), 1, 0, 0); rwn(g - 6, 2, 1, 0);
      if (g == 12) t3 = park(3);
      if (g == 13) { const u32x2 a = park(4); aad[0] = a[0]; aad[1] = a[1]; }
      if (g == 15) {
        asm volatile("" ::: "memory");
        __builtin_amdgcn_s_waitcnt(0xc07f);
        bar();
      }
    }
    if (sec == 8) next(g, 0, 3, 0, STB);
    if (sec == 9) {
      if (g < 2) af[g][3] = lda(aad[g] + STB, 3);
      if (g == 2) signal(0x10000u);
    }
    PIN();
    outop(1, n - 104 - KO);
    outop(2, n - 120 - KO);
    {
      constexpr int wg[8] = WGAPS;
#pragma unroll
      for (int e = 0; e < 8; ++e)
        if (n - 136 == wg[e]) {
          *LDS_AT(u32x4, (e < 4 ? t1[1] + (e & 1) * 64 : t2[1] + 2048 + (e & 1) * 128) + ((e >> 1) & 1) * KSB) = kvp[4 + e];
          PIN();
        }
    }
    if (n == 152 + 3 || n == 152 + 4) {
      al[2 * (n & 1)] *= QSCALE; al[2 * (n & 1) + 1] *= QSCALE;
      asm volatile("" : "+v"(al[2 * (n & 1)]), "+v"(al[2 * (n & 1) + 1]));
      if (n & 1) asm volatile("v_add_u32 %0, %1, %0" : "+v"(t2[1]) : "s"(qmv));
      PIN();
    }
    outop(0, n - 152 - KO);
  });
  TT(13);
  if (wid == 0) STAMP(dk, 18);
#pragma unroll
  for (int k = 16 - KO; k <= 16; ++k) outop(0, k);
  __hip_atomic_fetch_add(LDS_AT(unsigned, t3[1]), 1u, __ATOMIC_RELAXED, __HIP_MEMORY_SCOPE_WORKGROUP);
  TT(14);
  asm volatile("s_waitcnt vmcnt(4)" ::: "memory");
  signal(1u);
  if (give) {
    const unsigned tg = *LDS_AT(const volatile unsigned, lbase + FLG + 20);
    asm volatile("s_waitcnt vmcnt(0)" ::: "memory");
    if (fresh_lane() == 0) st_ag(qfl, tg);
  }
  TT(15);
  if (wid == 0) STAMP(dk, 19);
  }
  PIN();
  {
  const int lane = fresh_lane(), tid = wid * 64 + lane;
  LAYER_HERE;
  const int dk = LAY2 ? 6 : 2;
  const int g = (wid >> 1) & 1, hh = tmi & 1, qg = wid & 1, far = wid < 4;
  const int head = tni * 2 + hh, qt = (tmi ^ far) * 4 + qg * 2;
  unsigned qaa = lbase + QOFF + (hh * 2 + qg) * 4096 + lane * 16 + (g ? 0 : 2048), qab = qaa ^ 2048;
  const unsigned wbase = lbase + g * 4096 + lane * 16;
  auto sptr = [&](int sl) { unsigned a = wbase + sl * KSB; asm volatile("" : "+v"(a)); return LDS_AT(const s16x8, a); };
  auto rd = [&](__attribute__((address_space(3))) const s16x8* sp, int off) { return sp[off >> 4]; };
  auto sl0 = sptr(0);
  const unsigned arr_a = lane == 0 ? lbase + FLG + 16 : lbase + DUM + lane * 4;
  auto arrive = [&]() { __hip_atomic_fetch_add(LDS_AT(unsigned, arr_a), 1u, __ATOMIC_RELAXED, __HIP_MEMORY_SCOPE_WORKGROUP); };
  unsigned arr_w = lane == 0 ? lbase + FLG + 8 + (wid >> 2) * 4 : lbase + DUM + lane * 4;
  unsigned arr_i = 1u << (8 * (wid & 3));
  auto arrive_w = [&]() { __hip_atomic_fetch_add(LDS_AT(unsigned, arr_w), arr_i, __ATOMIC_RELAXED, __HIP_MEMORY_SCOPE_WORKGROUP); };
  s16x8 sel;
#pragma unroll
  for (int e = 0; e < 8; ++e) sel[e] = ((lane & 7) == (e & 4)) ? (short)0x3F80 : (short)0;
  int sidx = 0xDDDDDDDD;
  f32x16 oa, ob, nn, sa, sb;
  f32x4 la = {0.f, 0.f, 0.f, 0.f}, lb = {0.f, 0.f, 0.f, 0.f};
#pragma unroll
  for (int e = 0; e < 16; ++e) { oa[e] = 0.f; ob[e] = 0.f; sa[e] = 0.f; sb[e] = 0.f; }
  asm volatile("" : "+v"(sidx), "+v"(sel), "+v"(oa), "+v"(ob), "+v"(la), "+v"(lb), "+v"(qaa), "+v"(qab), "+v"(arr_w), "+v"(arr_i));
  PIN();
  s16x8 qa0, qa1, qb0, qb1, k00, k01, v00, v01, v10, v11, k10, k11;
  u32x4 fw;
  auto first = [&]() {
    fw = lds_rd4(lbase + FLG + 16);
    asm volatile("" ::: "memory");
    k00 = rd(sl0, 0); qa0 = *LDS_AT(const s16x8, qaa); qb0 = *LDS_AT(const s16x8, qab);
    k01 = rd(sl0, 1024); qa1 = *LDS_AT(const s16x8, qaa + 1024); qb1 = *LDS_AT(const s16x8, qab + 1024);
    v00 = rd(sl0, 2048); v01 = rd(sl0, 3072);
    PIN();
  };
  first();
  if (__builtin_amdgcn_readfirstlane(fw[0]) < 4u) {
#pragma unroll 1
    for (int i = 0; i < SPIN_MAX; ++i) {
      if (__builtin_amdgcn_readfirstlane(*LDS_AT(const volatile unsigned, lbase + FLG + 16)) >= 4u) break;
      __builtin_amdgcn_s_sleep(ASLP);
    }
    asm volatile("" ::: "memory");
    first();
  }
  const unsigned tag = __builtin_amdgcn_readfirstlane(fw[1]);
  if (far) {
    ArgP ap_ = ap; asm volatile("" : "+s"(ap_));
    const unsigned* qf = ap_->gran + GR_Q + ((LAY2 ? 256 : 0) + (tmi ^ 1) * 16 + tni) * 32 + qg;
    const __amdgpu_buffer_rsrc_t rq = __builtin_amdgcn_make_buffer_rsrc(reinterpret_cast<char*>(ap_->kv) + QG + (((tmi ^ 1) * 16 + tni) * 2 + qg) * 4096, 0, 4096, 0x00020000);
    __builtin_amdgcn_s_sleep(QPRE);
#pragma unroll 1
    for (int i = 0; i < SPIN_MAX; ++i) {
      if (__builtin_amdgcn_readfirstlane(ld_ag(qf)) == tag) break;
      __builtin_amdgcn_s_sleep(QSLP);
    }
    asm volatile("" ::: "memory");
    const int o = lane * 16 + (g ? 0 : 2048);
    qa0 = __builtin_bit_cast(s16x8, __builtin_amdgcn_raw_buffer_load_b128(rq, o, 0, 16)); qb0 = __builtin_bit_cast(s16x8, __builtin_amdgcn_raw_buffer_load_b128(rq, o ^ 2048, 0, 16));
    qa1 = __builtin_bit_cast(s16x8, __builtin_amdgcn_raw_buffer_load_b128(rq, o + 1024, 0, 16)); qb1 = __builtin_bit_cast(s16x8, __builtin_amdgcn_raw_buffer_load_b128(rq, (o ^ 2048) + 1024, 0, 16));
  }
  const int idx = LAY2 ? (2047 - (int)(__builtin_amdgcn_readfirstlane(fw[3]) & 0xffffu)) & 2047 : -64;
  float ma;
  const float zoff = __uint_as_float(__builtin_amdgcn_readfirstlane(LAY2 && g == ((idx >> 6) & 1) ? 0u : 0xff800000u));
  {
    sa = MFMA32(k00, qa0, sa); sb = MFMA32(k00, qb0, sb);
    sa = MFMA32(k01, qa1, sa); sb = MFMA32(k01, qb1, sb);
    float mb = sb[0];
    ma = sa[0];
#pragma unroll
    for (int e = 1; e < 16; ++e) { ma = fmaxf(ma, sa[e]); mb = fmaxf(mb, sb[e]); }
    ma = fmaxf(ma, mb);
    const auto sw = __builtin_amdgcn_permlane32_swap(__float_as_uint(ma), __float_as_uint(ma), false, false);
    ma = fmaxf(__uint_as_float(sw[0]), __uint_as_float(sw[1]));
    f32x2 n2 = {-ma, -ma};
    asm volatile("" : "+v"(n2));
#pragma unroll
    for (int e = 0; e < 16; ++e) nn[e] = n2[e & 1];
    sa = MFMA32(k00, qa0, nn); sa = MFMA32(k01, qa1, sa);
    la[0] = lb[0] = -__uint_as_float(__float_as_uint(__builtin_amdgcn_exp2f(zoff - ma)) & 0xffff0000u);
  }
#define SB() __builtin_amdgcn_sched_barrier(0)
#define EX(i) sc[i] = __builtin_amdgcn_exp2f(sc[i])
  auto unit = [&](f32x16& sc, f32x16& sn, const s16x8& kn0, const s16x8& kn1, const s16x8& qn0, const s16x8& qn1,
                  f32x16& op, f32x4& lp, const s16x8& vp,
                  f32x16& oc, f32x4& lc, const s16x8& vc, auto&& mid) {
    lp = SMF16(sel, PHI(sn), lp); SB();
    op = SMF32(vp, PHI(sn), op); SB();
    EX(0); EX(1); EX(2); EX(3); SB();
    sn = MFMA32(kn0, qn0, nn); SB();
    EX(4); EX(5); EX(6); EX(7); SB();
    sn = MFMA32(kn1, qn1, sn); SB();
    mid();
    EX(8); EX(9); EX(10); EX(11); SB();
    lc = SMF16(sel, PLO(sc), lc); SB();
    oc = SMF32(vc, PLO(sc), oc); SB();
    EX(12); EX(13); EX(14); EX(15); SB();
    asm volatile("" : "+v"(sc));
  };
  sl0 = sptr(1);
  unsigned f0 = 0u;
  auto check = [&](unsigned need) {
    if (__builtin_expect(__builtin_amdgcn_readfirstlane(f0) < need, 0)) {
#pragma unroll 1
      for (int i = 0; i < SPIN_MAX; ++i) {
        if (__builtin_amdgcn_readfirstlane(*LDS_AT(const volatile unsigned, lbase + FLG)) >= need) break;
        __builtin_amdgcn_s_sleep(1);
      }
    }
    asm volatile("" ::: "memory");
  };
#define PAT 0x22221110
#define SAW8(i) do { if (((PAT >> (28 - 4 * (i))) & 3) != ((PAT >> ((32 - 4 * (i)) & 31)) & 3)) { __builtin_amdgcn_s_setprio((PAT >> (28 - 4 * (i))) & 3); SB(); } } while (0)
#define SAW(i) do { if (odd) SAW8(4 + (i)); else SAW8(i); } while (0)
  auto three = [&](int st, bool odd) {
    SAW(0);
    unit(sa, sb, k00, k01, qb0, qb1, ob, lb, v11, oa, la, v00, [&]() { k10 = rd(sl0, 0); k11 = rd(sl0, 1024); SB(); });
    SAW(1);
    unit(sb, sa, k10, k11, qa0, qa1, oa, la, v01, ob, lb, v00, [&]() { v10 = rd(sl0, 2048); v11 = rd(sl0, 3072); SB(); });
    sl0 = sptr((2 * st + 2) & 7);
    SB();
    SAW(2);
    unit(sa, sb, k10, k11, qb0, qb1, ob, lb, v01, oa, la, v10, [&]() { k00 = rd(sl0, 0); k01 = rd(sl0, 1024); SB(); });
  };
  auto fourth = [&](int st, bool odd) {
    SAW(3);
    unit(sb, sa, k00, k01, qa0, qa1, oa, la, v11, ob, lb, v10, [&]() {
      v00 = rd(sl0, 2048); v01 = rd(sl0, 3072);
      f0 = *LDS_AT(const volatile unsigned, lbase + FLG);
      SB();
      arrive_w();
      SB(); });
    check(st >= 14 ? 32u : 2u * st + 5u);
    sl0 = sptr((2 * st + 3) & 7);
    SB();
  };
  SB();
  SAW8(0);
  {
    f32x16& sc = sa;
    sb = MFMA32(k00, qb0, nn); SB();
    k10 = rd(sl0, 0); k11 = rd(sl0, 1024); SB();
    sb = MFMA32(k01, qb1, sb); SB();
    EX(0); EX(1); EX(2); EX(3); EX(4); EX(5); EX(6); EX(7); EX(8); SB();
    oa = SMF32(v00, PLO(sa), oa); SB();
    EX(9); EX(10); EX(11); SB();
    la = SMF16(sel, PLO(sa), la); SB();
    EX(12); EX(13); EX(14); EX(15); SB();
    asm volatile("" : "+v"(sa));
  }
  SAW8(1);
  unit(sb, sa, k10, k11, qa0, qa1, oa, la, v01, ob, lb, v00, [&]() { v10 = rd(sl0, 2048); v11 = rd(sl0, 3072); SB(); });
  sl0 = sptr(2);
  SB();
  SAW8(2);
  unit(sa, sb, k10, k11, qb0, qb1, ob, lb, v01, oa, la, v10, [&]() {});
  {
    f32x16& sc = sb;
    SAW8(3);
    la = SMF16(sel, PHI(sa), la); SB();
    oa = SMF32(v11, PHI(sa), oa); SB();
    EX(0); EX(1); EX(2); EX(3); EX(4); EX(5); EX(6); EX(7); EX(8); asm volatile("" : "+v"(sc)); SB();
    lb = SMF16(sel, PLO(sb), lb); SB();
    ob = SMF32(v10, PLO(sb), ob); SB();
    f0 = *LDS_AT(const volatile unsigned, lbase + FLG);
    asm volatile("" ::: "memory");
    k00 = rd(sl0, 0); k01 = rd(sl0, 1024); SB();
    EX(9); EX(10); EX(11); EX(12); EX(13); EX(14); EX(15); asm volatile("" : "+v"(sc)); SB();
    if (__builtin_amdgcn_readfirstlane(f0) < 3u) { check(3u); k00 = rd(sl0, 0); k01 = rd(sl0, 1024); }
    SB();
    v00 = rd(sl0, 2048); v01 = rd(sl0, 3072); SB(); arrive_w(); SB();
    sa = MFMA32(k00, qa0, nn); sa = MFMA32(k01, qa1, sa); asm volatile("" : "+v"(sa)); SB();
  }
  sl0 = sptr(3);
  SB();
  SAW8(4);
  unit(sa, sb, k00, k01, qb0, qb1, ob, lb, v11, oa, la, v00, [&]() {
    f0 = *LDS_AT(const volatile unsigned, lbase + FLG);
    asm volatile("" ::: "memory");
    k10 = rd(sl0, 0); k11 = rd(sl0, 1024); SB(); });
  if (__builtin_amdgcn_readfirstlane(f0) < 4u) { check(4u); k10 = rd(sl0, 0); k11 = rd(sl0, 1024); }
  SB();
  SAW8(5);
  unit(sb, sa, k10, k11, qa0, qa1, oa, la, v01, ob, lb, v00, [&]() { v10 = rd(sl0, 2048); v11 = rd(sl0, 3072); f0 = *LDS_AT(const volatile unsigned, lbase + FLG); SB(); });
  check(5u);
  sl0 = sptr(4);
  SB();
  SAW8(6);
  unit(sa, sb, k10, k11, qb0, qb1, ob, lb, v01, oa, la, v10, [&]() { k00 = rd(sl0, 0); k01 = rd(sl0, 1024); SB(); });
#pragma unroll 1
  for (int st = 2; st < 16; st += 2) { fourth(st - 1, true); three(st, false); fourth(st, false); three(st + 1, true); }
  SAW8(7);
  unsigned vr, tk, tv;
  unsigned f4;
  auto table = [&]() {
    f4 = *LDS_AT(const volatile unsigned, lbase + FLG + 4);
    k00 = *LDS_AT(const volatile s16x8, tk); k01 = *LDS_AT(const volatile s16x8, tk + 32);
    vr = *LDS_AT(const volatile u16, tv);
  };
  unsigned xw, xm;
  u32x2 ml;
  u32x4 o2[4];
  auto take = [&]() {
    ml = *LDS_AT(const volatile u32x2, xm);
#pragma unroll
    for (int e = 0; e < 4; ++e) o2[e] = lds_rd4(xw + e * 1024);
  };
  float pzt;
  int dso;
  f32x16& sc = sb;
#define EXP() do { asm volatile("" : "+v"(sc)); SB(); } while (0)
  oa = SMF32(v11, PHI(sa), oa); SB();
  tk = lbase + TAB + 128 + hh * 64 + (lane >> 5) * 16; tv = lbase + TAB + 256 + hh * 64 + (lane & 31) * 2;
  table();
  SB();
  EX(0); EX(1); EXP();
  la = SMF16(sel, PHI(sa), la); SB();
  EX(2); EX(3); EX(4); EXP();
  xw = wbase + (qg ? QOFF : 4 * ASB) + far * 8192; xm = lbase + XML + wid * 512 + lane * 8;
  pzt = __builtin_amdgcn_exp2f(nn[0] + (zoff - 8.f));
  asm volatile("" : "+v"(xw), "+v"(xm), "+v"(pzt), "+v"(f4));
  SB();
  {
    const unsigned ph = LAY2 ? 0x79797979u : 0x80808080u;
    auto ready = [&]() { return ((__builtin_amdgcn_readfirstlane(f4) + ph) & 0x80808080u) == 0x80808080u; };
    if (__builtin_expect(!ready(), 0)) {
#pragma unroll 1
      for (int i = 0; i < SPIN_MAX; ++i) {
        __builtin_amdgcn_s_sleep(1);
        table();
        if (ready()) break;
      }
    }
    asm volatile("" ::: "memory");
  }
  SB();
  sa = MFMA32(k00, qb0, nn); asm volatile("" : "+v"(sa)); SB();
#pragma unroll
  for (int e = 0; e < 4; ++e) *LDS_AT(volatile f32x4, xw + e * 1024) = f32x4{oa[4 * e], oa[4 * e + 1], oa[4 * e + 2], oa[4 * e + 3]};
  *LDS_AT(volatile u32x2, xm) = u32x2{__float_as_uint(ma), f2word(pzt <= la[0] ? la[0] : 0.f)};
  SB();
  EX(5); EX(6); EX(7); EXP();
  sa = MFMA32(k01, qb1, sa); asm volatile("" : "+v"(sa), "+v"(vr)); SB();
  ARGS_HERE;
  EX(8); EX(9); EX(10); EXP();
  ob = SMF32(v10, PLO(sb), ob); SB();
  EX(11); EX(12); EX(13); EXP();
  lb = SMF16(sel, PLO(sb), lb); SB();
  const bool used = !LAY2 || (qt + g) * 32 + (lane & 31) != idx;
  dso = used ? (((qt + g) * 32 + head) * 1024 + lane * 8) * 2 : S_ * D_ * 2;
  xw += g ? -4096 : 4096; xm += g ? -1024 : 1024;
  asm volatile("" : "+v"(dso), "+v"(xw), "+v"(xm));
  SB();
  EX(14); EX(15); EXP();
  take();
  SB();
  lb = SMF16(sel, PHI(sb), lb); SB();
  ob = SMF32(v11, PHI(sb), ob); SB();
  __builtin_amdgcn_s_setprio(EPRIO);
  TE(0, 0); TE(1, 0); TE(2, 0); TE(3, 0); TE(7, 0);
  float lsb = lb[0];
  if (LAY2) {
    sa[0] = __builtin_amdgcn_exp2f(sa[0]);
    ob = SMF32((u32x4{lane < 32 ? vr : 0u, 0u, 0u, 0u}), PLO(sa), ob);
    lsb += __uint_as_float(__float_as_uint(sa[0]) & 0xffff0000u);
    lsb = pzt <= lsb ? lsb : 0.f;
  }
  const __amdgpu_buffer_rsrc_t rso = __builtin_amdgcn_make_buffer_rsrc(Of, 0, S_ * D_ * 2, 0x00020000);
  TE(0, 1);
  if (__builtin_amdgcn_ballot_w64(ml[1] == SENT) != 0ull) {
#pragma unroll 1
    for (int i = 0; i < SPIN_MAX; ++i) {
      __builtin_amdgcn_s_sleep(XSLP);
      if (!__builtin_amdgcn_ballot_w64(*LDS_AT(const volatile unsigned, xm + 4) == SENT)) break;
    }
    asm volatile("s_waitcnt lgkmcnt(0)" ::: "memory");
    take();
  }
  arrive();
  TE(0, 2);
  TE(0, 3); TE(1, 1);
  {
    const float m2 = __uint_as_float(ml[0]), l2 = __uint_as_float(ml[1]);
    const float mx = fmaxf(ma, m2);
    const float f1 = __builtin_amdgcn_exp2f(ma - mx), f2 = __builtin_amdgcn_exp2f(m2 - mx);
    const float inv = frcp(lsb * f1 + l2 * f2);
    const float c1 = f1 * inv, c2 = f2 * inv;
    f32x2 r[8];
#pragma unroll
    for (int e = 0; e < 8; ++e) {
      const unsigned x0 = o2[e >> 1][(e & 1) * 2], x1 = o2[e >> 1][(e & 1) * 2 + 1];
      r[e] = f32x2{__uint_as_float(x0) * c2, __uint_as_float(x1) * c2};
    }
#pragma unroll
    for (int e = 0; e < 8; ++e) r[e] = f32x2{ob[2 * e], ob[2 * e + 1]} * f32x2{c1, c1} + r[e];
    auto put = [&]() {
#pragma unroll
      for (int sx = 0; sx < 2; ++sx)
        __builtin_amdgcn_raw_buffer_store_b128(u32x4{pack2(r[4 * sx][0], r[4 * sx][1]), pack2(r[4 * sx + 1][0], r[4 * sx + 1][1]),
            pack2(r[4 * sx + 2][0], r[4 * sx + 2][1]), pack2(r[4 * sx + 3][0], r[4 * sx + 3][1])}, rso, dso + sx * 1024, 0, 16);
    };
    put();
    PIN();
    __builtin_amdgcn_s_setprio(0);
    PIN();
    f32x2 chk = {lsb - __builtin_amdgcn_fmed3f(lsb, 8.6736174e-19f, 1.2676506e30f), l2 - __builtin_amdgcn_fmed3f(l2, 8.6736174e-19f, 1.2676506e30f)};
#pragma unroll
    for (int e = 0; e < 8; ++e) chk = r[e] * f32x2{0.f, 0.f} + chk;
    if (__builtin_expect(__builtin_amdgcn_ballot_w64(used && !(chk[0] + chk[1] == 0.f)) != 0ull, 0)) {
      float l;
      attn_safe(KV, head * 64, qb0, qb1, ob, l, LAY2 ? head * 64 + (idx >> 5) : -1, idx & 31, lbase + TAB + 128 + hh * 64);
      const float inv2 = 1.0f / l;
#pragma unroll
      for (int e = 0; e < 8; ++e) r[e] = f32x2{ob[2 * e] * inv2, ob[2 * e + 1] * inv2};
      asm volatile("s_waitcnt vmcnt(0)" ::: "memory");
      put();
    }
    unsigned pp = 0u;
    if (LAY2) {
      const unsigned ua = lbase + UGL + hh * 128 + (lane >> 5) * 16;
      f32x2 du = {0.f, 0.f}, dn = {0.f, 0.f};
#pragma unroll
      for (int q = 0; q < 4; ++q) {
        const f32x4 uu = *LDS_AT(const f32x4, ua + q * 32), gg = *LDS_AT(const f32x4, ua + 256 + q * 32);
        du = r[2 * q] * f32x2{uu[0], uu[1]} + du; du = r[2 * q + 1] * f32x2{uu[2], uu[3]} + du;
        const f32x2 t0 = r[2 * q] * f32x2{gg[0], gg[1]}, t1 = r[2 * q + 1] * f32x2{gg[2], gg[3]};
        dn = t0 * t0 + dn; dn = t1 * t1 + dn;
      }
      const auto a = __builtin_amdgcn_permlane32_swap(__float_as_uint(du[0] + du[1]), __float_as_uint(du[0] + du[1]), false, false);
      const auto c = __builtin_amdgcn_permlane32_swap(__float_as_uint(dn[0] + dn[1]), __float_as_uint(dn[0] + dn[1]), false, false);
      pp = f2word(__uint_as_float(pack2(__uint_as_float(a[0]) + __uint_as_float(a[1]), __uint_as_float(c[0]) + __uint_as_float(c[1]))));
    }
    __builtin_amdgcn_raw_buffer_store_b32(pp, __builtin_amdgcn_make_buffer_rsrc(hand + 2 * HAND_N + PP_O, 0, LAY2 ? H_ * S_ * 4 : 0, 0x00020000),
                                          lane < 32 ? (head * S_ + (qt + g) * 32 + lane) * 4 : H_ * S_ * 4, 0, 16);
    PIN();
  }
  TE(1, 2);
  u32x4 xres[2];
  oproj_tail(LAY2, tid, lbase, lbase + FLG, lbase + SPM + 144, omi, oni, idx, gran + GR_O + ((LAY2 ? 16 : 0) + (tmi & ~1)) * (64 * RO) + head,
             gran + GR_X + omi * (128 * RX) + oni * 4, tag, Of, xout, A, w_r, stats_out, stats, hand, xres TEPASS);
  if (LAY2) {
    const int row = omi * 128 + (wid & 3) * 32 + (wid >> 2) * 16 + (lane >> 3), col = oni * 64 + (lane & 7) * 8;
    unsigned md[2], best = 0u;
    const __amdgpu_buffer_rsrc_t rsk = __builtin_amdgcn_make_buffer_rsrc(hand + HAND_N, 0, (HAND_N + PS_N) * 4, 0x00020000);
    f32x2 xf[2][4];
#pragma unroll
    for (int p = 0; p < 2; ++p)
#pragma unroll
      for (int e = 0; e < 4; ++e) {
        xf[p][e] = f32x2{__uint_as_float(xres[p][e] << 16), __uint_as_float(xres[p][e] & 0xffff0000u)};
        asm volatile("" : "+v"(xf[p][e]));
      }
    LOGDEF;
    LOG();
    const bool rtr = (oni & 7) == 7;
    {
      const int rl = (wid & 3) * 32 + (wid >> 2) * 16 + (lane & 15), rr = omi * 128 + rl;
      int src = (HAND_N + ((omi * 16 + (lane >> 4) * 4) * 128 + rl) * 4) * 4;
      int ko = rtr && (wid >> 2) == (oni >> 3) && lane < NREP ? lane * 1024 + (rr >> 4) * 8 : (HAND_N + PS_N) * 4;
      int bp = (lane >> 3) * 4;
      const float wsum = *LDS_AT(const volatile float, lbase + SPM + 132);
      asm volatile("" : "+v"(src), "+v"(ko), "+v"(bp));
      u32x4 v[4];
      __builtin_amdgcn_s_sleep(MPRE);
#pragma unroll 1
      for (int spin = 0; spin < SPIN_MAX; ++spin) {
        LOG();
#pragma unroll
        for (int i = 0; i < 4; ++i) v[i] = __builtin_amdgcn_raw_buffer_load_b128(rsk, src + (i & 1) * 2048, (i >> 1) * 4096, 16);
        unsigned mx = 0u;
#pragma unroll
        for (int i = 0; i < 4; ++i) mx = umax(umax(umax(mx, v[i][0]), v[i][1]), v[i][2]);
        if (none(mx == SENT)) break;
        __builtin_amdgcn_s_sleep(MSLP);
        asm volatile("" ::: "memory");
      }
      LOGAT(12);
      if (rtr) __builtin_amdgcn_s_setprio(3); else __builtin_amdgcn_s_setprio(LNPRIO);
      double sm[3];
#pragma unroll
      for (int c = 0; c < 3; ++c) {
        const double a = ((double)__uint_as_float(v[0][c]) + (double)__uint_as_float(v[1][c])) + ((double)__uint_as_float(v[2][c]) + (double)__uint_as_float(v[3][c]));
        u32x2 q = __builtin_bit_cast(u32x2, a);
        const auto lo = __builtin_amdgcn_permlane32_swap(q[0], q[0], false, false), hi = __builtin_amdgcn_permlane32_swap(q[1], q[1], false, false);
        q = __builtin_bit_cast(u32x2, __builtin_bit_cast(double, u32x2{lo[0], hi[0]}) + __builtin_bit_cast(double, u32x2{lo[1], hi[1]}));
        const auto l2 = __builtin_amdgcn_permlane16_swap(q[0], q[0], false, false), h2 = __builtin_amdgcn_permlane16_swap(q[1], q[1], false, false);
        sm[c] = __builtin_bit_cast(double, u32x2{l2[0], h2[0]}) + __builtin_bit_cast(double, u32x2{l2[1], h2[1]});
      }
      float mean, var, mb, den;
      ln_from_sums(sm[0], sm[1], mean, var, mb, den);
      const unsigned mdr = f2word(__uint_as_float((__float_as_uint(mb) >> 16) | (__float_as_uint(den) & 0xffff0000u)));
      md[0] = __builtin_amdgcn_ds_bpermute(bp, mdr); md[1] = __builtin_amdgcn_ds_bpermute(bp + 32, mdr);
      if (rtr) {
        const float r = (float)fma(-(double)mb, (double)wsum, sm[2]) * frcp(den);
        const unsigned rb = pack2(r, r);
        unsigned rkey = (rb ^ ((unsigned)((int)rb >> 31) | 0x80000000u)) & 0xffff0000u;
        if (r != r) rkey = 0u;
        const unsigned top = wave_umax(rkey | (unsigned)(2047 - rr));
        const unsigned mdt = __builtin_amdgcn_readlane(mdr, (2047 - (int)(top & 0xffffu)) & 15);
        __builtin_amdgcn_raw_buffer_store_b64(u32x2{top, mdt}, rsk, ko, 0, 16);
        PIN();
        __builtin_amdgcn_s_setprio(LNPRIO);
        __hip_atomic_fetch_add(LDS_AT(unsigned, lane == 0 ? lbase + SPM + 128 : lbase + DUM + lane * 4), 1u, __ATOMIC_RELAXED, __HIP_MEMORY_SCOPE_WORKGROUP);
        if (wid == 2 || wid == 5) STAMP(8, 6);
      }
      LOGAT(13);
    }
    auto keys = [&](const u32x4& v) __attribute__((always_inline)) {
      if (none(umax(umax(v[0], v[1]), umax(v[2], v[3])) == SENT)) {
        best = wave_umax(umax(v[0], v[2])) | 0x8000u;
        *LDS_AT(volatile u32x2, lbase + FLG + 40) = u32x2{best, (unsigned)__builtin_amdgcn_readlane(v[0] > v[2] ? v[1] : v[3], ((2047 - (int)(best & 0x7ffu)) >> 5) & 63)};
        STAMP(8, 11);
      }
    };
    auto ln_rows = [&]() __attribute__((always_inline)) {
#pragma unroll
      for (int p = 0; p < 2; ++p) {
        const float mb_ = __uint_as_float(md[p] << 16), rin = frcp(__uint_as_float(md[p] & 0xffff0000u));
#pragma unroll
        for (int e = 0; e < 4; ++e) {
          const f32x2 h = (xf[p][e] - f32x2{mb_, mb_}) * f32x2{rin, rin} + xf[p][e];
          xres[p][e] = pack2(h[0], h[1]);
        }
      }
      asm volatile("" : "+v"(xres[0]), "+v"(xres[1]));
    };
    if (wid == 0) STAMP(8, 12);
    ln_rows();
    LOGAT(14);
    const __amdgpu_buffer_rsrc_t rsu = __builtin_amdgcn_make_buffer_rsrc(out, 0, S_ * D_ * 2, 0x00020000);
    unsigned fr1 = SENT;
#pragma unroll 1
    for (int i = 0; i < 4 * SPIN_MAX && fr1 == SENT; ++i) fr1 = __builtin_amdgcn_readfirstlane(*LDS_AT(const volatile unsigned, lbase + SPM + 144));
    const int fr2 = (fr1 >> 16) & 0x7ffu;
    fr1 &= 0x7ffu;
    unsigned tmx = __builtin_amdgcn_readfirstlane(*LDS_AT(const volatile unsigned, lbase + SPM + 136)) + KDT;
    if (tmx - (unsigned)__builtin_amdgcn_s_memrealtime() > 1024u) tmx = 0u;
    auto due = [&]() { return tmx == 0u || (int)((unsigned)__builtin_amdgcn_s_memrealtime() - tmx) >= 0; };
    lk_set(15);
    const bool held = !none((row | 8) == ((int)fr1 | 8) || (row | 8) == (fr2 | 8));
#pragma unroll
    for (int p = 0; p < 2; ++p) __builtin_amdgcn_raw_buffer_store_b128(xres[p], rsu, row + 8 * p != (int)fr1 && row + 8 * p != fr2 ? ((row + 8 * p) * D_ + col) * 2 : S_ * D_ * 2, 0, OAUX);
    PIN();
    if (wid == 6) {
      if (rtr) {
#pragma unroll 1
        for (int i = 0; i < SPIN_MAX; ++i) {
          if (__builtin_amdgcn_readfirstlane(*LDS_AT(const volatile unsigned, lbase + SPM + 128)) == 7u) break;
          __builtin_amdgcn_s_sleep(1);
        }
        asm volatile("" ::: "memory");
      }
      while (!due()) __builtin_amdgcn_s_sleep(1);
    }
#pragma unroll 1
    for (int i = 0; i < 4 * SPIN_MAX && !best; ++i) {
      if (wid == 6) {
        LOG();
        keys(__builtin_amdgcn_raw_buffer_load_b128(rsk, (bid % NREP) * 1024 + lane * 16, 0, 16));
        if (!best) __builtin_amdgcn_s_sleep(KSLP);
      } else {
        best = __builtin_amdgcn_readfirstlane(*LDS_AT(const volatile unsigned, lbase + FLG + 40));
        if (!best) __builtin_amdgcn_s_sleep(1);
      }
      asm volatile("" ::: "memory");
    }
    const int idx2 = 2047 - (int)(best & 0x7ffu);
    if (held) {
#pragma unroll
      for (int p = 0; p < 2; ++p) {
        const int r = row + 8 * p;
        __builtin_amdgcn_raw_buffer_store_b128(xres[p], rsu, (r == (int)fr1 || r == fr2) && r != idx2 ? (r * D_ + col) * 2 : S_ * D_ * 2, 0, OAUX);
      }
    }
    if (__builtin_expect(idx2 != (int)fr1 && idx2 != fr2, 0) && !none((row | 8) == (idx2 | 8))) {
#pragma unroll
      for (int p = 0; p < 2; ++p)
        __builtin_amdgcn_raw_buffer_store_b128(u32x4{pack2(xf[p][0][0], xf[p][0][1]), pack2(xf[p][1][0], xf[p][1][1]), pack2(xf[p][2][0], xf[p][2][1]), pack2(xf[p][3][0], xf[p][3][1])},
            __builtin_amdgcn_make_buffer_rsrc(xout, 0, 3 * D_ * 2, 0x00020000), row + 8 * p == idx2 ? (2 * D_ + col) * 2 : S_ * D_ * 2, 0, 16);
      asm volatile("s_waitcnt vmcnt(0)" ::: "memory");
      if (lane == 0) st_ag(gran + GR_F + oni * 32, tag);
    }
  }
  }
}
struct Args2 { Args l[2]; };
extern "C" __global__ __launch_bounds__(768, 3) void layers_kernel(const Args2 args) {
  __shared__ __attribute__((aligned(128))) char smem[163840];
  const unsigned long long t_entry = 0;
  const int tid = threadIdx.x, bid = blockIdx.x;
  int wid = __builtin_amdgcn_readfirstlane(tid >> 6);
  asm volatile("" : "+s"(wid));
  const ArgP ap = (ArgP)__builtin_amdgcn_kernarg_segment_ptr();
  layer_body(wid, bid, (unsigned)(uintptr_t)smem, ap, t_entry);
  LAYER_HERE;
  if (!LAY2) {
    asm volatile(AGAIN :: "{s[0:1]}"(ap + 1), "{s2}"(bid), "{v0}"(wid * 64) : "s90", "s91", "scc", "memory");
    __builtin_unreachable();
  }
}

extern "C" __global__ void end_marker_kernel() {}

extern "C" void kernel_launch(void* const* d_in, const int* in_sizes, int n_in, void* d_out, int out_size,
                              void* d_ws, size_t ws_size, hipStream_t stream) {
  const u16* x0 = (const u16*)d_in[0];
  const u16* wqkv = (const u16*)d_in[1];
  const u16* wo = (const u16*)d_in[2];
  const u16* w_r = (const u16*)d_in[4];
  const u16* wg = (const u16*)d_in[5];
  const u16* wu = (const u16*)d_in[6];
  const u16* wd = (const u16*)d_in[7];
  u16* out = (u16*)d_out;

  char* ws = (char*)d_ws;
  const size_t MB4 = (size_t)S_ * D_ * 2;
  u16* kv = (u16*)(ws);
  u16* o = (u16*)(ws + 2 * MB4);
  u16* x1 = (u16*)(ws + 3 * MB4);
  unsigned* gran = (unsigned*)(ws + 4 * MB4);
  float* csf = (float*)(gran + GR_F + 16 * 32);
  double* stats = (double*)(csf + 8192);
  unsigned* hand = (unsigned*)(stats + 2 * STAT_N);
  static_assert((GR_F + 16 * 32) % 32 == 0 && (size_t)(GR_F + 16 * 32 + 8192 + 4 * STAT_N + 2 * HAND_N + PS_N) * 4 <= 6u << 20, "");
  u16* x3 = (u16*)(hand + 2 * HAND_N) - 3 * D_;
  u16* wc = (u16*)((((uintptr_t)ws + 4 * MB4 + (6u << 20) + 2047) & ~(uintptr_t)2047) + (((uintptr_t)wqkv + 128) & 2047));
  Args2 a;
  for (int l = 0; l < 2; ++l) {
    Args& r = a.l[l];
    r.lay = l; r.a = l ? x1 : x0; r.kv = kv; r.o = o; r.xo = l ? x3 : x1; r.so = l ? stats + STAT_N : stats;
    r.W = wqkv; r.wc = wc; r.wg = wg; r.wu = wu; r.wd = wd; r.wo = wo; r.w_r = w_r; r.gran = gran; r.csf = csf; r.stats = stats; r.hand = hand; r.out = out;
  }
  layers_kernel<<<256, 768, 0, stream>>>(a);
}
```

```cpp
#include <hip/hip_runtime.h>
#include <hip/hip_bf16.h>
#include <stdint.h>
#include <type_traits>
#include <utility>

typedef unsigned short u16;
typedef short s16x4 __attribute__((ext_vector_type(4)));
typedef short s16x8 __attribute__((ext_vector_type(8)));
typedef float f32x16 __attribute__((ext_vector_type(16)));
typedef float f32x4 __attribute__((ext_vector_type(4)));
typedef unsigned int u32x4 __attribute__((ext_vector_type(4)));
typedef unsigned int u32x2 __attribute__((ext_vector_type(2)));
typedef __bf16 bf16x8 __attribute__((ext_vector_type(8)));
typedef __bf16 bf16x16 __attribute__((ext_vector_type(16)));

__device__ __forceinline__ int fresh_lane() {
  int l;
  asm volatile("v_mbcnt_lo_u32_b32 %0, -1, 0\n\tv_mbcnt_hi_u32_b32 %0, -1, %0" : "=v"(l));
  return l;
}
#define STAMP(k, i)
#define STAMPV(k, i, v)
#define LOGDEF
#define LOG()
#define LOGAT(k)
#define lk_set(k)
#define LOGOUT(slot)
#define TE(grp, k)
#define TEARG
#define TEPASS
constexpr int S_ = 2048, D_ = 1024, H_ = 32, F_ = 3584;

__device__ __forceinline__ int kpos(int r) { return (r & ~12) | ((r & 4) << 1) | ((r & 8) >> 1); }
__device__ __forceinline__ float bf2f(u16 v) { return __uint_as_float(((unsigned)v) << 16); }
__device__ __forceinline__ u16 f2bf(float f) {
  __hip_bfloat16 b = __float2bfloat16(f);
  return *reinterpret_cast<u16*>(&b);
}
typedef float f32x2 __attribute__((ext_vector_type(2)));
typedef __bf16 bf16x2 __attribute__((ext_vector_type(2)));
__device__ __forceinline__ unsigned pack2(float lo, float hi) {
  f32x2 v = {lo, hi};
  return __builtin_bit_cast(unsigned, __builtin_convertvector(v, bf16x2));
}
__device__ __forceinline__ float rbf(float f) { return __uint_as_float(pack2(f, f) << 16); }
__device__ __forceinline__ void rbf2(float& a, float& b) {
  const unsigned p = pack2(a, b);
  a = __uint_as_float(p << 16); b = __uint_as_float(p & 0xffff0000u);
}

__device__ __forceinline__ float fadd(float a, float b) { float r = a + b; asm("" : "+v"(r)); return r; }
template <int CTRL, int RMASK>
__device__ __forceinline__ float dpp_add(float v) {
  const int t = __builtin_amdgcn_update_dpp(0, __float_as_int(v), CTRL, RMASK, 0xf, false);
  return v + __int_as_float(t);
}
__device__ __forceinline__ float wave_sum(float v) {
  v = dpp_add<0x111, 0xf>(v);
  v = dpp_add<0x112, 0xf>(v);
  v = dpp_add<0x114, 0xf>(v);
  v = dpp_add<0x118, 0xf>(v);
  v = dpp_add<0x142, 0xa>(v);
  v = dpp_add<0x143, 0xc>(v);
  return __int_as_float(__builtin_amdgcn_readlane(__float_as_int(v), 63));
}

template <int CTRL, int RMASK>
__device__ __forceinline__ unsigned dpp_umax(unsigned v) {
  const unsigned t = (unsigned)__builtin_amdgcn_update_dpp(0, (int)v, CTRL, RMASK, 0xf, false);
  return v > t ? v : t;
}
__device__ __forceinline__ unsigned wave_umax(unsigned v) {
  v = dpp_umax<0x111, 0xf>(v); v = dpp_umax<0x112, 0xf>(v); v = dpp_umax<0x114, 0xf>(v); v = dpp_umax<0x118, 0xf>(v);
  v = dpp_umax<0x142, 0xa>(v); v = dpp_umax<0x143, 0xc>(v);
  return (unsigned)__builtin_amdgcn_readlane((int)v, 63);
}

#define NREP 32
constexpr unsigned SENT = 0xFFFFFFFFu;
constexpr int GRAN_N = 256 * NREP;
constexpr int Y_N = F_ / 16 * D_;
constexpr int X2_N = D_ / 2;
constexpr int XS_N = 128;
constexpr int P4_N = 3 * D_ * 16;
constexpr int PA_N = H_ * 8 * 64;
constexpr int HAND_N = GRAN_N + Y_N + X2_N + XS_N + P4_N + PA_N;
constexpr int MD_O = 16 * 16 * 128 * 4;
constexpr int PK_O = MD_O;
constexpr int PP_O = PK_O + 128 * NREP;
constexpr int Y3_O = PP_O + H_ * S_;
constexpr int PS_N = Y3_O + Y_N;
constexpr int GR_ROW = 64, GR_COL = GR_ROW + 4 * S_, GR_KV = GR_COL + 16 * 16 * 192;
#define RO 8
#define RK 16
#define RX 1
constexpr int GR_O = GR_KV + 2 * 16 * 32 * RK, GR_X = GR_O + 2 * 16 * 64 * RO, GR_N = GR_X + 16 * 128 * RX;
constexpr unsigned QG = 30u << 20;
constexpr int GR_F = GR_N + 256 * 64;
constexpr int STAT_N = 3 * S_;

__device__ __forceinline__ void st_ag(unsigned* p, unsigned v) { __hip_atomic_store(p, v, __ATOMIC_RELAXED, __HIP_MEMORY_SCOPE_AGENT); }
__device__ __forceinline__ unsigned ld_ag(const unsigned* p) { return __hip_atomic_load(p, __ATOMIC_RELAXED, __HIP_MEMORY_SCOPE_AGENT); }
__device__ __forceinline__ double ld_ag(const double* p) { return __hip_atomic_load(p, __ATOMIC_RELAXED, __HIP_MEMORY_SCOPE_AGENT); }
__device__ __forceinline__ unsigned f2word(float f) { const unsigned u = __float_as_uint(f); return u < 0xFFFFFFFEu ? u : 0xFFFFFFFEu; }
constexpr int SPIN_MAX = 1 << 18;
__device__ __forceinline__ unsigned umax(unsigned a, unsigned b) { return a > b ? a : b; }
__device__ __forceinline__ bool none(bool p) { return __builtin_amdgcn_ballot_w64(p) == 0ull; }

__device__ __forceinline__ void ln_from_sums(double s1, double s2, float& mean, float& var, float& mb, float& den) {
  const double md = s1 * (1.0 / 1024.0);
  const double vd = fma(-md, md, s2 * (1.0 / 1024.0));
  mean = (float)md; var = fmaxf((float)vd, 0.f);
  mb = rbf(mean);
  den = rbf(__builtin_amdgcn_sqrtf(rbf(rbf(var) + 1e-5f)));
}
__device__ __forceinline__ float frcp(float x) { return __builtin_amdgcn_rcpf(x); }
__device__ __forceinline__ void ln_apply2(float& a, float& b, float mb, float den, float rin) {
  a -= mb; b -= mb;
  rbf2(a, b);
  float qa = a * rin, qb = b * rin;
  qa = fmaf(fmaf(-qa, den, a), rin, qa);
  qb = fmaf(fmaf(-qb, den, b), rin, qb);
  rbf2(qa, qb);
  a = qa; b = qb;
}

#define CODE_ADDR(SYM, out64)                                                                                          \
  do {                                                                                                                 \
    unsigned lo_, hi_;                                                                                                 \
    asm volatile("s_getpc_b64 s[90:91]\n\ts_add_u32 s90, s90, " SYM "@rel32@lo+4\n\ts_addc_u32 s91, s91, " SYM          \
                 "@rel32@hi+12\n\ts_mov_b32 %0, s90\n\ts_mov_b32 %1, s91"                                             \
                 : "=s"(lo_), "=s"(hi_) :: "s90", "s91", "scc");                                                        \
    out64 = ((unsigned long long)hi_ << 32) | lo_;                                                                     \
  } while (0)

#define QNX 4
constexpr int QMX = 32 / QNX, QNG = 16 / QNX, QMG = 8 / QNG;

__device__ __forceinline__ unsigned touch(const void* p) { return *reinterpret_cast<const unsigned*>(p); }

__device__ __forceinline__ unsigned touch_code(unsigned long long lo, unsigned long long hi, int pc, int lane) {
  const unsigned long long p = lo + (unsigned)(pc * 8192 + lane * 128);
  return p + 4 <= hi ? *reinterpret_cast<__attribute__((address_space(1))) const unsigned*>(p) : 0u;
}

template <bool SC = false>
__device__ __forceinline__ void glds16s(const void* sbase, unsigned voff, unsigned lds_dst) {
  unsigned keep;
  if (SC)
    asm volatile("s_mov_b32 %0, m0\n\ts_mov_b32 m0, %3\n\ts_nop 2\n\tglobal_load_lds_dwordx4 %1, %2 sc1\n\ts_mov_b32 m0, %0"
                 : "=&s"(keep) : "v"(voff), "s"(sbase), "s"(lds_dst) : "memory");
  else
  asm volatile("s_mov_b32 %0, m0\n\ts_mov_b32 m0, %3\n\ts_nop 2\n\tglobal_load_lds_dwordx4 %1, %2\n\ts_mov_b32 m0, %0"
               : "=&s"(keep) : "v"(voff), "s"(sbase), "s"(lds_dst) : "memory");
}
template <bool SC>
__device__ __forceinline__ void glds16x4(const void* sbase, unsigned v0, unsigned v1, unsigned v2, unsigned v3, unsigned lds_dst) {
  unsigned keep;
  if (SC)
    asm volatile("s_mov_b32 %0, m0\n\ts_mov_b32 m0, %6\n\ts_nop 2\n\tglobal_load_lds_dwordx4 %1, %5 sc1\n\tglobal_load_lds_dwordx4 %2, %5 offset:1024 sc1\n\t"
                 "global_load_lds_dwordx4 %3, %5 offset:2048 sc1\n\tglobal_load_lds_dwordx4 %4, %5 offset:3072 sc1\n\ts_mov_b32 m0, %0"
                 : "=&s"(keep) : "v"(v0), "v"(v1), "v"(v2), "v"(v3), "s"(sbase), "s"(__builtin_amdgcn_readfirstlane(lds_dst)) : "memory");
  else
    asm volatile("s_mov_b32 %0, m0\n\ts_mov_b32 m0, %6\n\ts_nop 2\n\tglobal_load_lds_dwordx4 %1, %5\n\tglobal_load_lds_dwordx4 %2, %5 offset:1024\n\t"
                 "global_load_lds_dwordx4 %3, %5 offset:2048\n\tglobal_load_lds_dwordx4 %4, %5 offset:3072\n\ts_mov_b32 m0, %0"
                 : "=&s"(keep) : "v"(v0), "v"(v1), "v"(v2), "v"(v3), "s"(sbase), "s"(__builtin_amdgcn_readfirstlane(lds_dst)) : "memory");
}
constexpr float QSCALE = 0.17677669529663687f * 1.4426950408889634f;

__device__ __forceinline__ unsigned min4(u32x4 v) { const unsigned a = v[0] < v[1] ? v[0] : v[1], b = v[2] < v[3] ? v[2] : v[3]; return a < b ? a : b; }
#define LDS_AT(T, a) ((__attribute__((address_space(3))) T*)(uintptr_t)(a))
__device__ __forceinline__ u32x4 lds_rd4(unsigned a) { return *LDS_AT(const volatile u32x4, a); }
__device__ __forceinline__ void lds_wr(unsigned a, unsigned v) { *LDS_AT(volatile unsigned, a) = v; }
__device__ __forceinline__ void split3(float v, unsigned& p0, unsigned& p1, unsigned& p2) {
  p0 = pack2(v, v) & 0xffffu; v -= __uint_as_float(p0 << 16);
  p1 = pack2(v, v) & 0xffffu; v -= __uint_as_float(p1 << 16);
  p2 = pack2(v, v) & 0xffffu;
}

#define PIN() __builtin_amdgcn_sched_barrier(0)
template <int B, class F, int... I>
__device__ __forceinline__ void static_for_(F&& f, std::integer_sequence<int, I...>) { (f(std::integral_constant<int, B + I>{}), ...); }
template <int B, int E, class F>
__device__ __forceinline__ void static_for(F&& f) { static_for_<B>(f, std::make_integer_sequence<int, E - B>{}); }

#define TPRE 60
#define TPSL 0
#define FCPRE 44
#define FCSLP 2
#define SIGAT 24
#define XPRIO 1
#define OPD 8
#define LAUX 16
#define CSLP 2
#define EPRIO 3
#define OPRIO 1
#define XSLP 1
#define CSL1 8
#define XAUX 16
__device__ __forceinline__ void oproj_tail(const bool RES2, const int tid, const unsigned lbase, const unsigned flg, const unsigned fcm, const int tmi, const int tni, const int idx,
                                           unsigned* osig, unsigned* xsig, const unsigned tag,
                                           const u16* A, u16* out,
                                           const u16* res, const u16* __restrict__ w_r, double* stats,
                                           const double* stats_in, unsigned* hand, u32x4 (&xres)[2] TEARG) {
  constexpr int ldw = D_, PD = OPD;
  const int lane = tid & 63, wid = __builtin_amdgcn_readfirstlane(tid >> 6);
  const int m0 = tmi * 128, n0 = tni * 64;
  const int dk = RES2 ? 7 : 3;
  if (wid == 0) STAMP(dk, 0);
  TE(1, 3);
  asm volatile("" : "+s"(A) :: "memory");
  PIN();
  const int row = m0 + (wid & 3) * 32 + (wid >> 2) * 16 + (lane >> 3), col = n0 + (lane & 7) * 8;
  const unsigned tb = lbase + 147456;
  typedef __attribute__((address_space(1))) double* gdp;
  typedef __attribute__((address_space(1))) unsigned* gup;
  unsigned tr = tb + (row - m0) * 128 + (((lane ^ (lane >> 3)) & 7) << 4), oad;
  {
    oad = lane == 0 ? flg + 24 : flg + 64 + lane * 4;
    gup ps = (gup)(osig + lane * 64);
    asm volatile("" : "+v"(oad), "+v"(ps));
    asm volatile("s_waitcnt vmcnt(1)" ::: "memory");
    TE(2, 1);
    const unsigned old = __hip_atomic_fetch_add(LDS_AT(unsigned, oad), wid < 4 ? 0x101u : 0x10001u, __ATOMIC_RELAXED, __HIP_MEMORY_SCOPE_WORKGROUP);
    if (((__builtin_amdgcn_readfirstlane(old) >> (wid < 4 ? 8 : 16)) & 255u) == 3u && lane < RO) __hip_atomic_store(ps, tag, __ATOMIC_RELAXED, __HIP_MEMORY_SCOPE_AGENT);
    PIN();
    TE(2, 2);
    if (wid == 0) STAMP(dk, 12);
  }
  auto announce = [&]() __attribute__((always_inline)) {
    asm volatile("" ::: "memory");
    PIN();
    __builtin_amdgcn_s_barrier();
    PIN();
    if (wid == 0) STAMP(dk, 1);
    TE(2, 3); TE(4, 0); TE(7, 1);
  };
  if (wid < 4) {
    const __amdgpu_buffer_rsrc_t rsa = __builtin_amdgcn_make_buffer_rsrc(const_cast<u16*>(A), 0, S_ * D_ * 2, 0x00020000);
    int sa0 = (tmi * 4 + wid) * 65536, va0 = (lane >> 4) * 512 + (lane & 15) * 16;
    asm volatile("" : "+v"(va0));
    asm volatile("" : "+s"(sa0));
    auto lda = [&](int i, int h) { return __builtin_amdgcn_raw_buffer_load_b128(rsa, va0 + (i & 1) * 2048 + h * 256, sa0 + (i >> 1) * 4096, LAUX); };
    unsigned wad[4], tw[4];
    f32x4 acc[2][4];
    const int k = (lane >> 5) * 16 + ((lane >> 2) & 7);
#pragma unroll
    for (int j = 0; j < 4; ++j) {
      wad[j] = lbase + k * 128 + (((2 * j) ^ (k & 6)) << 4) + 8 * (lane & 3);
      tw[j] = tb + wid * 4096 + (lane & 15) * 128 + (((2 * j + (lane >> 5)) ^ (lane & 7)) << 4) + ((lane >> 4) & 1) * 8;
      asm volatile("" : "+v"(wad[j]), "+v"(tw[j]));
    }
    auto ldw_ = [&](int i, int j) {
      const unsigned a = wad[j] + (i & 3) * 4096;
      s16x4 lo = __builtin_amdgcn_ds_read_tr16_b64_v4i16(LDS_AT(s16x4, a));
      s16x4 h4 = __builtin_amdgcn_ds_read_tr16_b64_v4i16(LDS_AT(s16x4, a + 1024));
      return (s16x8)__builtin_shufflevector(lo, h4, 0, 1, 2, 3, 4, 5, 6, 7);
    };
    u32x4 id[2];
#pragma unroll
    for (int j = 0; j < 2; ++j) {
#pragma unroll
      for (int e = 0; e < 4; ++e) id[j][e] = (lane >> 4) == 2 * j + ((lane >> 3) & 1) && ((lane & 7) >> 1) == e ? ((lane & 1) ? 0x3f800000u : 0x3f80u) : 0u;
      asm volatile("" : "+v"(id[j]));
    }
    unsigned rad = tb + wid * 4096 + (lane & 15) * 128 + (((lane >> 4) ^ (lane & 7)) << 4), rfa = flg + 36 + wid;
    asm volatile("" : "+v"(rad), "+v"(rfa));
    u32x4 rb[2][2];
    u32x4 ow[2];
#pragma unroll
    for (int j = 0; j < 4; ++j) {
      const u32x2 v = *reinterpret_cast<const u32x2*>(w_r + n0 + j * 16 + (lane >> 4) * 4);
#pragma unroll
      for (int e = 0; e < 2; ++e) ow[j >> 1][(j & 1) * 2 + e] = (lane & 8) ? v[e] : 0x3f803f80u;
    }
    gdp pst = (gdp)(stats + (lane >> 4) % 3 * S_ + m0 + wid * 32 + (lane & 15));
    gup pps = (gup)(hand + 2 * HAND_N + ((tmi * 16 + tni) * 128 + wid * 32 + (lane & 15)) * 4 + (lane >> 4));
    int xo = (row * ldw + col) * 2;
    asm volatile("" : "+v"(tr), "+v"(pst), "+v"(pps), "+v"(xo), "+v"(ow[0]), "+v"(ow[1]));
    announce();
    u32x4 af[PD][2];
    s16x8 wf[2][4];
#pragma unroll
    for (int i = 0; i < PD; i += 2) { af[i][0] = lda(i, 0); af[i + 1][0] = lda(i + 1, 0); af[i][1] = lda(i, 1); af[i + 1][1] = lda(i + 1, 1); }
    PIN();
    __builtin_amdgcn_s_setprio(OPRIO);
#pragma unroll
    for (int q = 0; q < 4; ++q) rb[q >> 1][q & 1] = lds_rd4((rad ^ ((q & 1) * 64)) + (q >> 1) * 2048);
#pragma unroll
    for (int j = 0; j < 4; ++j) wf[j & 1][j >> 1] = ldw_(j & 1, j >> 1);
    PIN();
#pragma unroll
    for (int j = 0; j < 4; ++j)
#pragma unroll
      for (int h = 0; h < 2; ++h) { asm volatile("v_mfma_f32_16x16x32_bf16 %0, %1, %2, 0" : "=&v"(acc[h][j]) : "v"(id[j & 1]), "v"(rb[h][j >> 1])); PIN(); }
#pragma unroll 1
    for (int spin = 0; spin < SPIN_MAX; ++spin)
      if (__builtin_amdgcn_readfirstlane(*LDS_AT(const volatile unsigned, flg)) >= 40u) break;
    asm volatile("" : "+v"(va0) :: "memory");
    PIN();
    s16x4 wlo;
    static_for<0, 16>([&](auto ic) __attribute__((always_inline)) {
      constexpr int i = 2 * decltype(ic)::value;
      if (i == SIGAT) { __hip_atomic_fetch_add(LDS_AT(unsigned, oad + 4), 1u, __ATOMIC_RELAXED, __HIP_MEMORY_SCOPE_WORKGROUP); PIN(); }
#pragma unroll
      for (int g = 0; g < 16; ++g) {
        const int e = g & 1, j = g >> 2, h = (g >> 1) & 1, jn = (j + 2) & 3, in = j < 2 ? i : i + 2;
        asm volatile("v_mfma_f32_16x16x32_bf16 %0, %1, %2, %0" : "+v"(acc[h][j]) : "v"(wf[e][j]), "v"(af[(i + e) % PD][h]));
        PIN();
        if (in == 16 && !(g & 3)) asm volatile("v_add_u32 %0, 0x10000, %0" : "+v"(wad[jn]));
        if (in < 32) {
          const s16x4 v = __builtin_amdgcn_ds_read_tr16_b64_v4i16(LDS_AT(s16x4, wad[jn] + ((in + h) & 15) * 4096 + e * 1024));
          if (e) wf[h][jn] = (s16x8)__builtin_shufflevector(wlo, v, 0, 1, 2, 3, 4, 5, 6, 7);
          else wlo = v;
          PIN();
        }
        if ((g & 3) == 1 && i > 0 && i + PD - 2 < 32) { af[(i - 2 + (j & 1)) % PD][j >> 1] = lda(i + PD - 2 + (j & 1), j >> 1); PIN(); }
        if ((g & 3) == 3 && in < 32 && i != SIGAT) { __builtin_amdgcn_s_waitcnt(0xc47f); PIN(); }
      }
    });
    asm volatile("s_nop 3" : "+v"(acc[0][0]), "+v"(acc[1][0]));
    TE(4, 1); TE(7, 2);
    if (wid == 0) STAMP(dk, 17);
    if (wid == 1 && !RES2) STAMP(4, 0);
    u32x4 pk[2][2];
    float sv[2];
    auto sums = [&]() __attribute__((always_inline)) {
      f32x4 d[2];
      float q[2];
#pragma unroll
      for (int h = 0; h < 2; ++h) {
        d[h] = __builtin_amdgcn_mfma_f32_16x16x32_bf16(__builtin_bit_cast(s16x8, ow[0]), __builtin_bit_cast(s16x8, pk[h][0]), f32x4{0.f, 0.f, 0.f, 0.f}, 0, 0, 0);
        d[h] = __builtin_amdgcn_mfma_f32_16x16x32_bf16(__builtin_bit_cast(s16x8, ow[1]), __builtin_bit_cast(s16x8, pk[h][1]), d[h], 0, 0, 0);
        q[h] = 0.f;
      }
#pragma unroll
      for (int e = 0; e < 16; ++e) {
        const bf16x2 x = __builtin_bit_cast(bf16x2, (unsigned)pk[e & 1][e >> 3][(e >> 1) & 3]);
        q[e & 1] = __builtin_amdgcn_fdot2_f32_bf16(x, x, q[e & 1], false);
      }
      const auto a = __builtin_amdgcn_permlane32_swap(__float_as_uint(q[0]), __float_as_uint(q[1]), false, false);
      const unsigned b = __float_as_uint(__uint_as_float(a[0]) + __uint_as_float(a[1]));
      const auto c = __builtin_amdgcn_permlane16_swap(b, b, false, false);
      const unsigned e = __float_as_uint(__uint_as_float(c[0]) + __uint_as_float(c[1]));
      const auto f = __builtin_amdgcn_permlane32_swap(e, e, false, false);
#pragma unroll
      for (int h = 0; h < 2; ++h) sv[h] = (lane & 16) ? __uint_as_float(f[h]) : d[h][0];
    };
#pragma unroll
    for (int j = 0; j < 4; ++j)
#pragma unroll
      for (int h = 0; h < 2; ++h) {
        pk[h][j >> 1][(j & 1) * 2] = pack2(acc[h][j][0], acc[h][j][1]); pk[h][j >> 1][(j & 1) * 2 + 1] = pack2(acc[h][j][2], acc[h][j][3]);
        *LDS_AT(u32x2, tw[j] + h * 2048) = u32x2{pk[h][j >> 1][(j & 1) * 2], pk[h][j >> 1][(j & 1) * 2 + 1]};
        PIN();
      }
    asm volatile("" ::: "memory");
    *LDS_AT(volatile unsigned char, rfa) = (unsigned char)2;
#pragma unroll
    for (int p = 0; p < 2; ++p) xres[p] = lds_rd4(tr + p * 1024);
    PIN();
    if (!RES2) {
      const __amdgpu_buffer_rsrc_t rso = __builtin_amdgcn_make_buffer_rsrc(out, 0, S_ * D_ * 2, 0x00020000);
      const __amdgpu_buffer_rsrc_t rsf = __builtin_amdgcn_make_buffer_rsrc(xsig + wid, 0, RX * 512, 0x00020000);
      const u32x4 x2 = lds_rd4(tr + 2048), x3 = lds_rd4(tr + 3072);
      PIN();
#pragma unroll
      for (int p = 0; p < 4; ++p) { __builtin_amdgcn_raw_buffer_store_b128(p < 2 ? xres[p] : (p < 3 ? x2 : x3), rso, xo + p * (8 * ldw * 2), 0, XAUX); PIN(); }
      TE(4, 2); TE(5, 0);
      sums();
      if (lane < 48) { __builtin_amdgcn_global_atomic_fadd_f64(pst, (double)sv[0]); __builtin_amdgcn_global_atomic_fadd_f64(pst + 16, (double)sv[1]); }
      PIN();
      TE(4, 3); TE(5, 1);
      asm volatile("s_waitcnt vmcnt(2)" ::: "memory");
      __builtin_amdgcn_raw_buffer_store_b32(tag, rsf, lane * 512 + 256, 0, 16);
      *LDS_AT(volatile unsigned char, rfa) = (unsigned char)3;
      PIN();
      TE(5, 2);
      asm volatile("s_waitcnt vmcnt(1)" ::: "memory");
      __builtin_amdgcn_raw_buffer_store_b32(tag, rsf, lane * 512, 0, 16);
      PIN();
      TE(5, 3);
      __builtin_amdgcn_s_setprio(0);
      return;
    }
    TE(4, 2); TE(5, 0);
    sums();
    __hip_atomic_store(pps, f2word(sv[0]), __ATOMIC_RELAXED, __HIP_MEMORY_SCOPE_AGENT);
    __hip_atomic_store(pps + 64, f2word(sv[1]), __ATOMIC_RELAXED, __HIP_MEMORY_SCOPE_AGENT);
    if (wid == 0) STAMP(dk, 14);
  } else {
    u32x4 rres[4];
    double is1[4], is2[4];
#pragma unroll
    for (int p = 0; p < 4; ++p)
      rres[p] = __builtin_amdgcn_raw_buffer_load_b128(__builtin_amdgcn_make_buffer_rsrc(const_cast<u16*>(res), 0, S_ * D_ * 2, 0x00020000), ((row - 16 + 8 * p) * ldw + col) * 2, 0, 16);
#pragma unroll
    for (int p = 0; p < 4; ++p) { is1[p] = ld_ag(stats_in + row - 16 + 8 * p); is2[p] = ld_ag(stats_in + S_ + row - 16 + 8 * p); }
    PIN();
    if (RES2) {
#pragma unroll
      for (int p = 0; p < 4; ++p) {
        float mean, var, mb, den;
        ln_from_sums(is1[p], is2[p], mean, var, mb, den);
        const float rin = frcp(den);
#pragma unroll
        for (int e = 0; e < 4; ++e) {
          const f32x2 x = {__uint_as_float(rres[p][e] << 16), __uint_as_float(rres[p][e] & 0xffff0000u)};
          const f32x2 h = (x - f32x2{mb, mb}) * f32x2{rin, rin} + x;
          rres[p][e] = pack2(h[0], h[1]);
        }
        asm volatile("" : "+v"(rres[p]));
      }
      PIN();
    }
    if ((idx >> 5) == tmi * 4 + wid - 4) {
      const int dr = idx - (row - 16);
      u32x4 qq;
#pragma unroll 1
      for (int i = 0; i < SPIN_MAX; ++i) {
        qq = __builtin_amdgcn_raw_buffer_load_b128(__builtin_amdgcn_make_buffer_rsrc(hand, 0, HAND_N * 4, 0x00020000), (GRAN_N + Y_N) * 4 + col * 2, 0, 16);
        if (none(umax(umax(qq[0], qq[1]), umax(qq[2], qq[3])) == SENT)) break;
        asm volatile("" ::: "memory");
      }
#pragma unroll
      for (int p = 0; p < 4; ++p)
#pragma unroll
        for (int e = 0; e < 4; ++e) rres[p][e] = dr == 8 * p ? qq[e] : rres[p][e];
    }
#pragma unroll 1
    for (int i = 0; i < 4 * SPIN_MAX; ++i)
      if (__builtin_amdgcn_readfirstlane(*LDS_AT(const volatile unsigned, flg + 16)) >= 12u) break;
#pragma unroll
    for (int p = 0; p < 4; ++p) *LDS_AT(volatile u32x4, tr - 2048 + p * 1024) = rres[p];
    asm volatile("s_waitcnt lgkmcnt(0)" ::: "memory");
    announce();
    TE(4, 1); TE(7, 2);
    asm volatile("s_waitcnt vmcnt(0)" ::: "memory");
    if (!RES2) return;
    __builtin_amdgcn_s_sleep(TPRE);
    unsigned fm = SENT;
#pragma unroll 1
    for (int i = 0; i < 4 * SPIN_MAX; ++i) {
      fm = __builtin_amdgcn_readfirstlane(*LDS_AT(const volatile unsigned, fcm));
      if (fm != SENT) break;
      __builtin_amdgcn_s_sleep(1);
    }
    const unsigned r = (lane & 8) ? fm >> 16 : fm & 0xffffu;
    unsigned fra = tb + (r & 127u) * 128 + (((r ^ lane) & 7u) << 4);
    int fro = (r >> 5) == (unsigned)(tmi * 4 + wid - 4) && lane < 16 ? ((lane >> 3) * ldw + col) * 2 : S_ * D_ * 2;
    asm volatile("" : "+v"(fra), "+v"(fro));
#pragma unroll 1
    for (int i = 0; i < 4 * SPIN_MAX; ++i) {
      if (__builtin_amdgcn_readfirstlane(*LDS_AT(const volatile unsigned char, flg + 32 + wid)) == 2u) break;
      __builtin_amdgcn_s_sleep(TPSL);
    }
    asm volatile("" ::: "memory");
    __builtin_amdgcn_s_setprio(XPRIO);
    PIN();
    TE(4, 2); TE(5, 0);
    const u32x4 xr = lds_rd4(fra);
#pragma unroll
    for (int p = 0; p < 2; ++p) xres[p] = lds_rd4(tr + p * 1024);
    PIN();
    __builtin_amdgcn_raw_buffer_store_b128(xr, __builtin_amdgcn_make_buffer_rsrc(out, 0, 3 * D_ * 2, 0x00020000), fro, 0, XAUX);
  }
  TE(4, 3); TE(7, 3);
  PIN();
  __builtin_amdgcn_s_setprio(0);
  __hip_atomic_fetch_add(LDS_AT(unsigned, oad), 1u, __ATOMIC_RELAXED, __HIP_MEMORY_SCOPE_WORKGROUP);
  TE(5, 1); TE(5, 2);
  PIN();
}

#define MFMA32(a, b, c) __builtin_amdgcn_mfma_f32_32x32x16_bf16(a, b, c, 0, 0, 0)
#define MFMA16(a, b, c) __builtin_amdgcn_mfma_f32_16x16x32_bf16(a, b, c, 0, 0, 0)

#define PLO(x) __builtin_bit_cast(bf16x16, __builtin_shufflevector(x, x, 0, 1, 2, 3, 4, 5, 6, 7))
#define PHI(x) __builtin_bit_cast(bf16x16, __builtin_shufflevector(x, x, 8, 9, 10, 11, 12, 13, 14, 15))
#define SMF32(a, b, c) __builtin_amdgcn_smfmac_f32_32x32x32_bf16(__builtin_bit_cast(bf16x8, a), b, c, sidx, 0, 0)
#define SMF16(a, b, c) __builtin_amdgcn_smfmac_f32_16x16x64_bf16(__builtin_bit_cast(bf16x8, a), b, c, sidx, 0, 0)

__device__ __forceinline__ void patch_kv(unsigned tab, int r, s16x8& k0, s16x8& k1, s16x8& v0, s16x8& v1, bool on = true) {
  const int lane = fresh_lane(), hi = lane >> 5;
  if (on && (lane & 31) == kpos(r)) {
    k0 = *LDS_AT(const s16x8, tab + hi * 16); k1 = *LDS_AT(const s16x8, tab + 32 + hi * 16);
  }
  const short vv = *LDS_AT(const short, tab + 128 + (lane & 31) * 2);
  const int js = (on && hi == ((r >> 2) & 1)) ? (r >> 4) * 8 + ((r >> 3) & 1) * 4 + (r & 3) : -1;
#pragma unroll
  for (int e = 0; e < 8; ++e) { v0[e] = js == e ? vv : v0[e]; v1[e] = js == 8 + e ? vv : v1[e]; }
}

__device__ __forceinline__ void attn_safe(const u16* KV, int tile0, s16x8 q0, s16x8 q1,
                                       f32x16& o, float& l_out, int ptile, int prow, unsigned ptab) {
  const int lane = fresh_lane();
  float m = -INFINITY, l = 0.f;
  const int sidx = 0xDDDDDDDD;
  const __amdgpu_buffer_rsrc_t rs = __builtin_amdgcn_make_buffer_rsrc(const_cast<u16*>(KV), 0, 2 * S_ * D_ * 2, 0x00020000);
#pragma unroll
  for (int e = 0; e < 16; ++e) o[e] = 0.f;
  for (int tt = 0; tt < 64; ++tt) {
    const int t = tile0 + tt, off = t * 2048 + lane * 16;
    s16x8 k0 = __builtin_bit_cast(s16x8, __builtin_amdgcn_raw_buffer_load_b128(rs, off, 0, 16));
    s16x8 k1 = __builtin_bit_cast(s16x8, __builtin_amdgcn_raw_buffer_load_b128(rs, off + 1024, 0, 16));
    s16x8 v0 = __builtin_bit_cast(s16x8, __builtin_amdgcn_raw_buffer_load_b128(rs, off + S_ * D_ * 2, 0, 16));
    s16x8 v1 = __builtin_bit_cast(s16x8, __builtin_amdgcn_raw_buffer_load_b128(rs, off + S_ * D_ * 2 + 1024, 0, 16));
    if (t == ptile) patch_kv(ptab, prow, k0, k1, v0, v1);
    f32x16 sc;
#pragma unroll
    for (int e = 0; e < 16; ++e) sc[e] = 0.f;
    sc = MFMA32(k0, q0, sc); sc = MFMA32(k1, q1, sc);
    float mt = sc[0];
#pragma unroll
    for (int e = 1; e < 16; ++e) mt = fmaxf(mt, sc[e]);
    mt = fmaxf(mt, __shfl_xor(mt, 32, 64));
    const float mn = fmaxf(m, mt);
    const float alpha = __builtin_amdgcn_exp2f(m - mn);
    m = mn;
#pragma unroll
    for (int e = 0; e < 16; ++e) sc[e] = __builtin_amdgcn_exp2f(sc[e] - mn);
    float ls = 0.f;
#pragma unroll
    for (int e = 0; e < 16; ++e) ls += __uint_as_float(__float_as_uint(sc[e]) & 0xffff0000u);
    l = l * alpha + ls;
#pragma unroll
    for (int e = 0; e < 16; ++e) o[e] *= alpha;
    o = SMF32(v0, PLO(sc), o); o = SMF32(v1, PHI(sc), o);
  }
  l_out = l + __shfl_xor(l, 32, 64);
}

__device__ __forceinline__ float wave_fmax(float v) {
  asm volatile("s_nop 1\n\tv_max_f32_dpp %0, %0, %0 row_shr:1 row_mask:0xf bank_mask:0xf\n\ts_nop 1\n\tv_max_f32_dpp %0, %0, %0 row_shr:2 row_mask:0xf bank_mask:0xf\n\t"
               "s_nop 1\n\tv_max_f32_dpp %0, %0, %0 row_shr:4 row_mask:0xf bank_mask:0xf\n\ts_nop 1\n\tv_max_f32_dpp %0, %0, %0 row_shr:8 row_mask:0xf bank_mask:0xf\n\t"
               "s_nop 1\n\tv_max_f32_dpp %0, %0, %0 row_bcast:15 row_mask:0xa bank_mask:0xf\n\ts_nop 1\n\tv_max_f32_dpp %0, %0, %0 row_bcast:31 row_mask:0xc bank_mask:0xf\n\ts_nop 0"
               : "+v"(v));
  return __int_as_float(__builtin_amdgcn_readlane(__float_as_int(v), 63));
}

#define PSLP 8
#define WSLP 3
#define PRE1 8
#define PRE5 8
#define W3SLP 4
#define FSTART 10
#define OAUX 16
#define OMX 4
#define T0 2u
#define TW 10u
#define HW 2
#define HP1 1
#define POLLAT 11
#define X2AT 2
#define HPRIO 3
#define SSLP 1
#define SPRE 6
#define WGAPS {0, 1, 7, 8, 9, 10, 11, 12}
#define KVS 4
#define PNS 12
#define G0 5u
#define GS 2u
#define IAT 1u
#define YSLP 2
#define ISLP 8
#define KSLP 2
#define UAT 13u
#define UHW 0
#define PPSLP 2
#define XRSLP 2
#define MPRE 20
#define YD1 150
#define YD2 180
#define KDT 200
#define XDT 45
#define R2PRIO 0
#define LNPRIO 1
#define MSLP 4
#define BARG 0
struct Args {
  long long lay;
  const u16* a; u16* kv; u16* o; u16* xo; double* so;
  const u16* W; u16* wc;
  const u16 *wg, *wu, *wd, *wo, *w_r;
  unsigned* gran; float* csf; double* stats; unsigned* hand; u16* out;
};
typedef __attribute__((address_space(4))) const Args* ArgP;
#define WCAT 3
#define CAP0 1
#define PRE4 50
#define PRE7 20
#define UARR 12u
#define GF0 10
#define XGATE 8
#define ASLP 1
#define B15 3
#define KO 5
#define PRO 2
#define HS 2
#define XD2 90
#define TFL 4u
#define QPRE 10
#define QSLP 1
#define WMAP 0x32100333
#define SMAP 0
#define STCH 6
#define KPOLL 10
#define TPRIO 2
#define TSLP 9
__device__ __forceinline__ bool slow_cols(const u16* W, int tni) { return ((((unsigned)(uintptr_t)W >> 7) + tni) & 7) == 3; }
#define ARGS_HERE                                                                                                      \
  ArgP ap_ = ap; asm volatile("" : "+s"(ap_));                                                                          \
  const u16* A = ap_->a; const u16* __restrict__ W = ap_->W; u16* KV = ap_->kv; u16* Of = ap_->o;        \
  const u16* __restrict__ wg = ap_->wg; const u16* __restrict__ wu = ap_->wu; const u16* __restrict__ wd = ap_->wd;       \
  const u16* __restrict__ wo = ap_->wo; const u16* __restrict__ w_r = ap_->w_r; unsigned* gran = ap_->gran;              \
  float* csf = ap_->csf; const double* stats = ap_->stats; double* stats_out = ap_->so; unsigned* hand = ap_->hand; \
  u16* xout = ap_->xo; u16* __restrict__ out = ap_->out
#define AGAIN "s_getpc_b64 s[90:91]\n\ts_add_u32 s90, s90, layers_kernel@rel32@lo+4\n\ts_addc_u32 s91, s91, layers_kernel@rel32@hi+12\n\ts_setpc_b64 s[90:91]"
#define KEPT(c)                                                                                                        \
  c "{v[104:107]}"(gu[0]), c "{v[108:111]}"(gu[1]), c "{v[112:115]}"(gu[2]), c "{v[116:119]}"(gu[3]), c "{v[120:123]}"(gu[4]),      \
  c "{v[124:127]}"(gu[5]), c "{v[128:131]}"(gu[6]), c "{v[132:135]}"(gu[7]), c "{v[136:139]}"(gu[8]), c "{v[140:143]}"(gu[9]),      \
  c "{v[144:147]}"(gu[10]), c "{v[148:151]}"(gu[11]), c "{v[152:155]}"(gu[12]), c "{v[156:159]}"(gu[13]), c "{v[160:163]}"(gu[14]), \
  c "{v[164:167]}"(gu[15])
#define LAYER_HERE ArgP apl_ = ap; asm volatile("" : "+s"(apl_)); const bool LAY2 = apl_->lay != 0
__device__ __forceinline__ void layer_body(const int wid, const int bid, const unsigned lbase, const ArgP ap, const unsigned long long t_entry) {
  constexpr int K = 1024, NKT = 16, NST = 3, ABYTES = 16384, WBYTES = 24576, STB = ABYTES + WBYTES, ldw = 3 * D_;
  constexpr int TABA = NST * STB, ALPHA = TABA + 2048, CRED = ALPHA + 1024;
  constexpr int KSB = 8192, ASB = 16384, FLG = 8 * ASB + 256;
  constexpr int DUM = FLG + 64, TAB = DUM + 256, RED = TAB + 512, SACT = RED + 4096, XML = SACT + 3584, UGL = XML + 4096, SPM = UGL + 1024, QOFF = 9 * ASB;
  constexpr int TABW = RED;
  static_assert(ALPHA % 1024 == 0 && RK == 16, "");
  static_assert(SPM + 2048 <= QOFF && CRED <= 8 * ASB && RED + 8192 <= QOFF && RED % 16 == 0, "lds");
  const int xcd = (bid & 1) * 4 + ((bid >> 1) & 3), loc = bid >> 3;
  const int tmi = (xcd / QNG) * QMX + loc / QNX, tni = (xcd % QNG) * QNX + loc % QNX;
  const int m0 = tmi * 128, n0 = tni * 64;
  const int omi = (xcd / (OMX / 2)) * OMX + loc / (32 / OMX), oni = (xcd % (OMX / 2)) * (32 / OMX) + loc % (32 / OMX);
  constexpr unsigned VOFF = (unsigned)S_ * D_ * 2;
  auto tile_at = [&](unsigned n) __attribute__((always_inline)) {
#pragma unroll 1
    for (int i = 0; i < 4 * SPIN_MAX; ++i) {
      if (__builtin_amdgcn_readfirstlane(*LDS_AT(const volatile unsigned, lbase + FLG + 36)) == n * 0x01010101u) break;
      __builtin_amdgcn_s_sleep(1);
    }
    asm volatile("" ::: "memory");
    PIN();
  };
  auto all_ge = [&](unsigned w, unsigned n) { return ((w + (128u - n) * 0x01010101u) & 0x80808080u) == 0x80808080u; };
  if (wid >= 8) {
    u32x4 gu[16];
    LAYER_HERE;
    constexpr int dk = 6;
    const int hw = wid - 8, b = bid;
    int nb = 0;
    constexpr int lim = NKT - 1;
    auto tick_nw = [&]() {
      PIN();
      if (nb < lim) { __builtin_amdgcn_s_barrier(); ++nb; } else __builtin_amdgcn_s_sleep(2);
      asm volatile("" ::: "memory");
      PIN();
    };
    auto tick = [&]() {
      asm volatile("s_waitcnt lgkmcnt(0)" ::: "memory");
      tick_nw();
    };
    auto fold16 = [&](f32x2 y0, f32x2 y1) __attribute__((always_inline)) {
      const auto q0 = __builtin_amdgcn_permlane32_swap(__float_as_uint(y0[0]), __float_as_uint(y1[0]), false, false);
      const auto q1 = __builtin_amdgcn_permlane32_swap(__float_as_uint(y0[1]), __float_as_uint(y1[1]), false, false);
      const auto q = __builtin_amdgcn_permlane16_swap(__float_as_uint(__uint_as_float(q0[0]) + __uint_as_float(q0[1])),
                                                      __float_as_uint(__uint_as_float(q1[0]) + __uint_as_float(q1[1])), false, false);
      return __uint_as_float(q[0]) + __uint_as_float(q[1]);
    };
    unsigned best, tag, rmd;
    float alpha, m, wsum;
    u32x4 tw;
    u32x2 xq;
    s16x8 sl[8];
    const int kcap = CAP0 + (tmi & 7);
    if (!LAY2) {
      ARGS_HERE;
      const int lane = fresh_lane(), t = hw * 64 + lane;
      const unsigned ep = ld_ag(gran);
      const int srow = m0 + tni * 8 + hw * 2;
      u32x4 xa[2][2], wv[8];
      const int wr_ = (3 - ((unsigned)(uintptr_t)W >> 7) - (tni & ~3)) & 7;
      const bool cpy = hw == 3 && wr_ < 4 && wr_ != (tni & 3), wld = hw < 3 ? wr_ != (tni & 3) : cpy;
      const int wcn = cpy ? (tni & ~3) + wr_ : tni, wcp = cpy ? (tni - wr_ - 1) & 3 : hw;
      const int wof = ((tmi * 64 + (lane >> 3)) * ldw + wcp * D_ + wcn * 64 + (lane & 7) * 8) * 2;
      auto ld_w = [&]() {
        const __amdgpu_buffer_rsrc_t rw = __builtin_amdgcn_make_buffer_rsrc(const_cast<u16*>(W), 0, wld ? D_ * ldw * 2 : 0, 0x00020000);
#pragma unroll
        for (int r = 0; r < 8; ++r) wv[r] = __builtin_amdgcn_raw_buffer_load_b128(rw, wof, r * 8 * ldw * 2, 0);
        PIN();
      };
      auto wcopy = [&]() __attribute__((always_inline)) {
        if (cpy) {
          const __amdgpu_buffer_rsrc_t rw = __builtin_amdgcn_make_buffer_rsrc(ap_->wc, 0, D_ * ldw * 2, 0x00020000);
#pragma unroll
          for (int r = 0; r < 8; ++r) __builtin_amdgcn_raw_buffer_store_b128(wv[r], rw, wof + r * 8 * ldw * 2, 0, 16);
        }
        PIN();
      };
      const int wat = ((hw == 3 ? SMAP : WMAP) >> (4 * (tmi & 7))) & 3;
      unsigned ps = 0u;
      auto stouch = [&](int at) __attribute__((always_inline)) {
        if (at == STCH && cpy) ps = __builtin_amdgcn_raw_buffer_load_b32(__builtin_amdgcn_make_buffer_rsrc(const_cast<u16*>(W), 0, D_ * ldw * 2, 0x00020000),
                                                                        (((tmi ^ 8) * 64 + lane) * ldw + wcp * D_ + wcn * 64) * 2, 0, 0);
        PIN();
      };
      auto ld_x = [&](int h) {
#pragma unroll
        for (int r = 0; r < 2; ++r) xa[r][h] = *reinterpret_cast<const u32x4*>(A + (size_t)(srow + r) * D_ + (h ^ (tmi >> 3)) * 512 + lane * 8);
        PIN();
      };
      ld_x(0);
      if (X2AT == 0) ld_x(1);
      if (wat == 0) ld_w();
      unsigned pc = 0u;
      if (hw == 3) {
        unsigned long long a0, a1;
        CODE_ADDR("layers_kernel", a0);
        CODE_ADDR("end_marker_kernel", a1);
        pc = touch_code(a0, a1, loc, lane);
      }
      if (hw == 1) pc = ld_ag(gran + ((lane & 1) ? GR_ROW + (m0 + tni * 8) * 4 : GR_KV + (tni * RK + tmi % RK) * 32));
      if (hw == 2) st_ag(gran + GR_N + bid * 64 + lane, lane + 1u);
      {
        const int gi = bid * 256 + t;
        if (gi < GRAN_N / 4) __builtin_amdgcn_raw_buffer_store_b128(u32x4{SENT, SENT, SENT, SENT}, __builtin_amdgcn_make_buffer_rsrc(hand, 0, GRAN_N * 4, 0x00020000), gi * 16, 0, 16);
        if (t < 24) __hip_atomic_store(stats_out + (t >> 3) * S_ + m0 + tni * 8 + (t & 7), 0.0, __ATOMIC_RELAXED, __HIP_MEMORY_SCOPE_AGENT);
      }
      tick();
      if (wat == 1) ld_w();
      if (X2AT == 1) ld_x(1);
      tick();
      if (wat == 2) ld_w();
      if (WCAT == 2) wcopy();
      if (X2AT == 2) ld_x(1);
      asm volatile("" :: "v"(ep));
      tick();
      if (wat == 3) ld_w();
      if (WCAT == 3) wcopy();
      if (X2AT == 3) ld_x(1);
      stouch(3);
#pragma unroll
      for (int i = 0; i < HW; ++i) { tick(); stouch(4 + i); }
      __builtin_amdgcn_s_setprio(HP1);
      PIN();
      tag = ep * 1664525u + 1013904223u;
      const __amdgpu_buffer_rsrc_t rsc = __builtin_amdgcn_make_buffer_rsrc(gran, 0, GR_N * 4, 0x00020000);
      {
        u32x4 od;
#pragma unroll
        for (int q = 0; q < 4; ++q) od[q] = ((lane & 7) >> 1) == q ? ((lane & 1) ? 0x3f800000u : 0x00003f80u) : 0u;
        f32x4 d[2] = {{0.f, 0.f, 0.f, 0.f}, {0.f, 0.f, 0.f, 0.f}};
#pragma unroll
        for (int r = 0; r < 8; ++r) d[r & 1] = __builtin_amdgcn_mfma_f32_16x16x32_bf16(__builtin_bit_cast(s16x8, od), __builtin_bit_cast(s16x8, wv[r]), d[r & 1], 0, 0, 0);
        u32x4 gr;
#pragma unroll
        for (int q = 0; q < 4; ++q) gr[q] = ((__float_as_uint(dpp_add<0x128, 0xf>(fadd(d[0][q], d[1][q]))) + 0x80u) & ~0xffu) | ((tag >> (8 * q)) & 0xffu);
        asm volatile("s_waitcnt vmcnt(0)" ::: "memory");
        __builtin_amdgcn_raw_buffer_store_b128(gr, rsc, (lane & 40) || !wld ? GR_N * 4 : (GR_COL + (wcn * 16 + tmi) * 192 + wcp * 64 + (lane & 7) * 8 + (lane >> 4) * 4) * 4, 0, 16);
      }
      asm volatile("" :: "v"(pc));
      tick_nw();
      stouch(6);
      {
        float xv[2][16], mean[2];
#pragma unroll
        for (int r = 0; r < 2; ++r) {
          float sm = 0.f;
#pragma unroll
          for (int i = 0; i < 4; ++i) {
            xv[r][2 * i] = __uint_as_float(xa[r][0][i] << 16); xv[r][2 * i + 1] = __uint_as_float(xa[r][0][i] & 0xffff0000u);
            xv[r][8 + 2 * i] = __uint_as_float(xa[r][1][i] << 16); xv[r][8 + 2 * i + 1] = __uint_as_float(xa[r][1][i] & 0xffff0000u);
          }
#pragma unroll
          for (int i = 0; i < 16; ++i) sm += xv[r][i];
          mean[r] = wave_sum(sm) * (1.0f / 1024.0f);
          tick();
          stouch(7 + r);
        }
        float vr[2];
#pragma unroll
        for (int r = 0; r < 2; ++r) {
          float v = 0.f;
#pragma unroll
          for (int i = 0; i < 16; ++i) { const float d = xv[r][i] - mean[r]; v += d * d; }
          vr[r] = wave_sum(v) * (1.0f / 1024.0f);
          if (r == 0) tick();
        }
        const float vv = lane & 2 ? vr[1] : vr[0], mm = lane & 2 ? mean[1] : mean[0];
        const float rin = 1.0f / rbf(sqrtf(rbf(rbf(vv) + 1e-5f)));
        if (lane < 4) __builtin_amdgcn_raw_buffer_store_b64(u32x2{__float_as_uint((lane & 1) ? rbf(mm) : rin), tag}, rsc, (GR_ROW + srow * 4 + lane * 2) * 4, 0, 16);
      }
      tick();
      float cs;
      while (nb < POLLAT) tick();
      asm volatile("" :: "v"(ps));
#pragma unroll 1
      for (int spin = 0;; ++spin) {
        u32x4 csv[4];
        const __amdgpu_buffer_rsrc_t rsw = __builtin_amdgcn_make_buffer_rsrc(gran, 0, hw < 3 ? GR_N * 4 : 0, 0x00020000);
#pragma unroll
        for (int i = 0; i < 4; ++i) csv[i] = __builtin_amdgcn_raw_buffer_load_b128(rsw, (GR_COL + (tni * 16 + (lane >> 4)) * 192 + hw * 64 + (lane & 15) * 4) * 4, i * (4 * 192 * 4), 16);
        const u32x4 rp = __builtin_amdgcn_raw_buffer_load_b128(rsc, (GR_ROW + (hw < 2 ? m0 + t : (m0 ^ 128) + tni * 8 + (t & 7)) * 4) * 4, 0, 16);
        PIN();
        tick(); tick();
        unsigned bad = 0u;
        float z[4];
#pragma unroll
        for (int q = 0; q < 4; ++q) {
          const unsigned tq = tag >> (8 * q);
          bad |= ((csv[0][q] ^ tq) | (csv[1][q] ^ tq)) | ((csv[2][q] ^ tq) | (csv[3][q] ^ tq));
          z[q] = fadd(fadd(__uint_as_float(csv[0][q] & ~0xffu), __uint_as_float(csv[1][q] & ~0xffu)), fadd(__uint_as_float(csv[2][q] & ~0xffu), __uint_as_float(csv[3][q] & ~0xffu)));
        }
        cs = fold16(f32x2{z[0], z[1]}, f32x2{z[2], z[3]});
        bad = (hw < 3 ? bad & 0xffu : 0u) | (rp[1] ^ tag) | (rp[3] ^ tag);
        alpha = __uint_as_float(rp[0]); m = __uint_as_float(rp[2]);
        if (none(bad != 0u) || spin >= SPIN_MAX) break;
      }
      if (tmi == 0 && hw < 3) st_ag(reinterpret_cast<unsigned*>(csf) + hw * 1024 + n0 + (lane & 15) * 4 + (lane >> 4), __float_as_uint(cs));
      unsigned c0, c1, c2;
      split3(cs, c0, c1, c2);
      tw = u32x4{c0 | (c1 << 16), c0 | (c2 << 16), c1 | (c0 << 16), 0u};
      best = rmd = 0u;
      wsum = 0.f;
      asm volatile("" : "=v"(xq), KEPT("="));
#pragma unroll
      for (int i = 0; i < 8; ++i) asm volatile("" : "=v"(sl[i]));
    } else {
      asm volatile("; kept registers" : KEPT("="));
      PIN();
      ARGS_HERE;
      const int lane = fresh_lane(), t = hw * 64 + lane;
      best = rmd = 0u;
      tag = __builtin_amdgcn_readfirstlane(*LDS_AT(const volatile unsigned, lbase + FLG + 20));
#pragma unroll
      for (int i = 0; i < 8; ++i) asm volatile("" : "=v"(sl[i]));
      unsigned sad;
      auto tickc = [&]() __attribute__((always_inline)) {
        tick();
        if (nb == kcap + 1) {
#pragma unroll
          for (int i = 0; i < 8; ++i) {
            const unsigned a = (sad ^ ((i >> 2) * 64) ^ (((i >> 1) & 1) * 16)) + (i & 1) * 12288;
            sl[i] = __builtin_shufflevector(__builtin_amdgcn_ds_read_tr16_b64_v4i16(LDS_AT(s16x4, a)), __builtin_amdgcn_ds_read_tr16_b64_v4i16(LDS_AT(s16x4, a + 1536)), 0, 1, 2, 3, 4, 5, 6, 7);
          }
        }
        PIN();
      };
      static_assert(CAP0 + 8 <= KPOLL, "the stage is taken before that");
      if (hw == 1) STAMP(4, 4);
      if (hw == 0) {
        __builtin_amdgcn_s_sleep(TPRE);
        tile_at(3u);
        __builtin_amdgcn_s_sleep(XGATE);
        STAMP(4, 3);
#pragma unroll 1
        for (int spin = 0; spin < SPIN_MAX; ++spin) {
          if (none(ld_ag(gran + GR_X + (tmi * RX + tni % RX) * 128 + 64 + lane) != tag)) break;
          __builtin_amdgcn_s_sleep(1);
        }
      }
      static_assert(UHW == 0, "");
      if (hw == 0) asm volatile("s_waitcnt vmcnt(0)" ::: "memory");
      if (hw == 0) STAMP(4, 5);
      asm volatile("" ::: "memory");
      PIN();
      __builtin_amdgcn_s_barrier();
      PIN();
      if (hw == 1) STAMP(4, 6);
      {
        const int ln = fresh_lane(), g4 = ln >> 4, tp = ln & 3, k = 8 * g4 + ((ln & 15) >> 2);
        sad = lbase + (kcap % NST) * STB + ABYTES + k * 384 + (((2 * (tp >> 1)) ^ (4 * ((k >> 1) & 1)) ^ (g4 & 1)) << 4) + 8 * (tp & 1) + (hw % 3) * 128;
      }
      wsum = __uint_as_float(__builtin_amdgcn_readfirstlane(*LDS_AT(const volatile unsigned, lbase + FLG + 60)));
      asm volatile("" : "+v"(sad), "=v"(tw), "=v"(alpha), "=v"(m));
      PIN();
      const int row = m0 + (t & 127);
      const __amdgpu_buffer_rsrc_t rsg = __builtin_amdgcn_make_buffer_rsrc(hand, 0, GRAN_N * 4, 0x00020000);
      if (hw < 2 || tmi == tni) {
      while (nb < HS) tickc();
#pragma unroll 1
      for (int spin = 0; spin < SPIN_MAX; ++spin) {
        const unsigned v = ld_ag(gran + GR_X + (tmi * RX + tni % RX) * 128 + lane);
        tickc();
        if (none(v != tag)) break;
      }
      asm volatile("" ::: "memory");
      PIN();
      const double s1 = ld_ag(stats + row), s2 = ld_ag(stats + S_ + row), sw = ld_ag(stats + 2 * S_ + row);
      tickc();
      float mean, var, mb, den;
      ln_from_sums(s1, s2, mean, var, mb, den);
      const float rin = frcp(den), g1 = 1.0f + rin;
      const float mb2 = rbf(mean + (mean - mb) * rin);
      const float den2 = rbf(__builtin_amdgcn_sqrtf(rbf(rbf(var * g1 * g1) + 1e-5f)));
      alpha = g1 * frcp(den2); m = (mb * rin + mb2) * frcp(g1);
      if (tmi == tni && hw >= 2) {
        const float r = (float)fma(-(double)mb, (double)wsum, sw) * rin;
        const unsigned rb = pack2(r, r);
        unsigned rkey = (rb ^ ((unsigned)((int)rb >> 31) | 0x80000000u)) & 0xffff0000u;
        if (r != r) rkey = 0u;
        rkey = wave_umax(rkey | (unsigned)(2047 - row));
        const unsigned md = __builtin_amdgcn_readlane(f2word(__uint_as_float((__float_as_uint(mb) >> 16) | (__float_as_uint(den) & 0xffff0000u))), (2047 - (int)(rkey & 0xffffu)) & 63);
        __builtin_amdgcn_raw_buffer_store_b64(u32x2{rkey, md}, rsg, lane < NREP ? lane * 256 + (tmi * 2 + hw - 2) * 8 : GRAN_N * 4, 0, 16);
      }
      }
      while (nb < KPOLL) tickc();
#pragma unroll 1
      for (int spin = 0; spin < SPIN_MAX; ++spin) {
        const u32x2 v = __builtin_amdgcn_raw_buffer_load_b64(rsg, (bid % NREP) * 256 + (lane & 31) * 8, 0, 16);
        tick(); tick();
        if (none(umax(v[0], v[1]) == SENT)) {
          best = wave_umax(v[0]);
          rmd = __builtin_amdgcn_readlane(v[1], ((2047 - (int)(best & 0xffffu)) >> 6) & 31);
          break;
        }
      }
      const int ix = (2047 - (int)(best & 0xffffu)) & 2047;
      if (row == ix) alpha = 0.f;
      xq = __builtin_amdgcn_raw_buffer_load_b64(__builtin_amdgcn_make_buffer_rsrc(const_cast<u16*>(A), 0, S_ * D_ * 2, 0x00020000), (ix * D_ + hw * 256 + (lane & 3) * 64 + (lane >> 2) * 4) * 2, 0, 16);
      PIN();
    }
    tag = __builtin_amdgcn_readfirstlane(tag);
    wsum = __uint_as_float(__builtin_amdgcn_readfirstlane(__float_as_uint(wsum)));
    const int lane = fresh_lane(), t = hw * 64 + lane;
    {
      unsigned a0, a1, a2;
      split3(-m, a0, a1, a2);
      u32x4 ta = {a0 | (a0 << 16), a1 | (a0 << 16), a1 | (a2 << 16), 0u};
      asm volatile("" : "+v"(tw), "+v"(ta), "+v"(alpha));
      __builtin_amdgcn_s_setprio(TPRIO);
      if (nb < lim) {
        while (nb < lim) tick();
        __builtin_amdgcn_s_sleep(TSLP);
      }
      if (!LAY2 && hw < 3) *LDS_AT(u32x4, lbase + TABW + ((hw ^ (hw < 2 ? ((lane >> 3) ^ tmi) & 1 : 0)) * 64 + (lane & 15) * 4 + (lane >> 4)) * 16) = tw;
      if (t < 128) {
        *LDS_AT(u32x4, lbase + TABA + t * 16) = ta;
        *LDS_AT(float, lbase + ALPHA + t * 4) = alpha;
        *LDS_AT(float, lbase + ALPHA + 512 + t * 4) = alpha * QSCALE;
      }
    }
    if (t >= 192 && t < 196) *LDS_AT(u32x4, lbase + CRED + ((t >> 1) & 1) * 512 + (t & 1) * 128) = u32x4{0u, 0u, 0u, 0u};
    asm volatile("s_waitcnt lgkmcnt(0)" ::: "memory");
    PIN();
    __builtin_amdgcn_s_barrier();
    PIN();
    *LDS_AT(u32x4, lbase + XML + t * 16) = u32x4{SENT, SENT, SENT, SENT};
    *LDS_AT(u32x4, lbase + SPM + (t & 127) * 16) = u32x4{SENT, SENT, SENT, SENT};
    if (hw == 0) {
      unsigned v;
      asm volatile("v_mov_b32 %0, 0\n\tv_writelane_b32 %0, 2, 0\n\tv_writelane_b32 %0, %1, 5\n\tv_writelane_b32 %0, %2, 7"
                   : "=&v"(v) : "s"(tag), "s"(best));
      lds_wr(lbase + FLG + lane * 4, v);
    }
    const int idx = LAY2 ? (2047 - (int)(best & 0xffffu)) & 2047 : -64;
    const bool p1blk = (oni & 7) != 7, fast = tmi < 8, own = fast && (loc & 1);
    const int fg = xcd * 28 + (p1blk ? loc - (loc >> 3) : 0), ow = (xcd & 3) * 16 + (loc >> 1);
    static_assert(OMX == 4, "oni % 8 = loc % 8");
#define GLD(a, b) "global_load_dwordx4 v[" #a ":" #b "], %0, off\n\tv_lshl_add_u64 %0, %0, 0, %1\n\t"
#define GLD4(...) asm volatile(__VA_ARGS__ : "+v"(p) : "s"((unsigned long long)(F_ * 2)), KEPT() : "memory")
    auto gptr = [&](const u16* wg, const u16* wu) {
      const int ln = fresh_lane();
      return ((ln & 2) ? wu : wg) + (size_t)(hw * 256 + (ln >> 2) * 4) * F_ + fg * 16 + (ln & 1) * 8;
    };
    auto gather = [&](const u16*& p, int e) __attribute__((always_inline)) {
      if (e == 0) GLD4(GLD(104, 107) GLD(108, 111) GLD(112, 115) GLD(116, 119));
      else if (e == 1) GLD4(GLD(120, 123) GLD(124, 127) GLD(128, 131) GLD(132, 135));
      else if (e == 2) GLD4(GLD(136, 139) GLD(140, 143) GLD(144, 147) GLD(148, 151));
      else GLD4(GLD(152, 155) GLD(156, 159) GLD(160, 163) GLD(164, 167));
      p += 60 * F_;
    };
    auto dmask = [&](int q) { const int ln = fresh_lane(); return ((ln >> 1) & 1) == q ? ((ln & 1) ? 0xffff0000u : 0x0000ffffu) : 0u; };
    unsigned dm0, dm1, dad;
    auto gu_setup = [&]() __attribute__((always_inline)) {
      const int ln = fresh_lane();
      dm0 = dmask(0); dm1 = dmask(1);
      dad = lbase + SACT + ((ln & 3) * 8 + (ln >> 5) * 4 + ((ln >> 4) & 1)) * 16 + hw * 4;
      asm volatile("" : "+v"(dm0), "+v"(dm1), "+v"(dad));
    };
    auto gate_up = [&](u32x2 x, unsigned md) __attribute__((always_inline)) {
      const unsigned rin = __builtin_amdgcn_readfirstlane(__float_as_uint(frcp(__uint_as_float(md & 0xffff0000u))));
      asm volatile("s_getpc_b64 s[90:91]\n\ts_add_u32 s90, s90, gate_up_code@rel32@lo+4\n\ts_addc_u32 s91, s91, gate_up_code@rel32@hi+12\n\ts_swappc_b64 s[92:93], s[90:91]"
                   : "+{v[100:101]}"(x) : "{s84}"(md << 16), "{s85}"(md & 0xffff0000u), "{s86}"(rin), "{v99}"(dad), "{v102}"(dm0), "{v103}"(dm1), KEPT()
                   : "v84", "v85", "v86", "v87", "v88", "v89", "v90", "v91", "v92", "v93", "v94", "v95", "v96", "v97", "s88", "s89", "s90", "s91", "s92", "s93", "scc", "memory");
    };
    auto act_diag = [&](int so = 0) __attribute__((always_inline)) {
      const int ln = fresh_lane();
      const f32x4 g4 = *LDS_AT(const volatile f32x4, lbase + SACT + so + (ln >> 2) * 16), u4 = *LDS_AT(const volatile f32x4, lbase + SACT + so + 256 + (ln >> 2) * 16);
      float gv = fadd(fadd(g4[0], g4[1]), fadd(g4[2], g4[3])), uv = fadd(fadd(u4[0], u4[1]), fadd(u4[2], u4[3]));
      rbf2(gv, uv);
      const float av = rbf(gv * frcp(1.0f + __expf(-gv))) * uv;
      const unsigned ap2 = pack2(av, av);
      return __builtin_bit_cast(s16x4, u32x2{ap2 & dm0, ap2 & dm1});
    };
    auto down = [&](const s16x4& a, const u32x2 (&w)[16]) __attribute__((always_inline)) {
      f32x4 d[2] = {{0.f, 0.f, 0.f, 0.f}, {0.f, 0.f, 0.f, 0.f}};
      static_for<0, 16>([&](auto fc) __attribute__((always_inline)) {
        constexpr int f = decltype(fc)::value;
        d[f & 1] = __builtin_amdgcn_mfma_f32_4x4x4bf16_1k(a, __builtin_bit_cast(s16x4, w[f]), d[f & 1], 4, f, 0);
      });
      f32x4 z;
#pragma unroll
      for (int e = 0; e < 4; ++e) { z[e] = d[0][e] + d[1][e]; asm volatile("" : "+v"(z[e])); }
      return u32x4{f2word(z[0]), f2word(z[1]), f2word(z[2]), f2word(z[3])};
    };
    auto fold8 = [&](f32x2 y0, f32x2 y1) __attribute__((always_inline)) { return dpp_add<0x128, 0xf>(fold16(y0, y1)); };
    auto fold4 = [&](f32x2 y0, f32x2 y1) __attribute__((always_inline)) { return dpp_add<0x124, 0xf>(fold8(y0, y1)); };
    auto y_sum = [&](const __amdgpu_buffer_rsrc_t rs, auto nbc) __attribute__((always_inline)) {
      constexpr int NB = decltype(nbc)::value, N = 14 / NB;
      const int ln = fresh_lane(), src = (GRAN_N + (ln >> 2) * D_ + ow * 16 + (ln & 3) * 4) * 4;
      f32x2 y0 = {0.f, 0.f}, y1 = {0.f, 0.f};
#pragma unroll 1
      for (int bt = 0; bt < NB; ++bt) {
        f32x2 z0, z1;
#pragma unroll 1
        for (int spin = 0; spin < SPIN_MAX; ++spin) {
          u32x4 v[N];
#pragma unroll
          for (int i = 0; i < N; ++i) v[i] = __builtin_amdgcn_raw_buffer_load_b128(rs, src, (bt * N + i) * (16 * D_ * 4), 16);
          z0 = y0; z1 = y1;
#pragma unroll
          for (int i = 0; i < N; ++i) {
            z0 = f32x2{fadd(z0[0], __uint_as_float(v[i][0])), fadd(z0[1], __uint_as_float(v[i][1]))};
            z1 = f32x2{fadd(z1[0], __uint_as_float(v[i][2])), fadd(z1[1], __uint_as_float(v[i][3]))};
          }
          const float chk = fadd(fadd(z0[0], z0[1]), fadd(z1[0], z1[1]));
          if (none(chk != chk)) break;
          unsigned mx = 0u;
#pragma unroll
          for (int i = 0; i < N; ++i) mx = umax(umax(umax(mx, v[i][0]), v[i][1]), umax(v[i][2], v[i][3]));
          if (none(mx == SENT)) break;
          __builtin_amdgcn_s_sleep(YSLP);
          asm volatile("" ::: "memory");
        }
        y0 = z0; y1 = z1;
      }
      return fold4(y0, y1);
    };
    [&]() __attribute__((always_inline)) {
    ARGS_HERE;
    const unsigned l16 = lane * 16;
    unsigned* kvflag = gran + GR_KV + ((LAY2 ? 16 : 0) + tni) * (32 * RK);
    auto wait_sig = [&](int sh, unsigned n) {
#pragma unroll 1
      for (int i = 0; i < 4 * SPIN_MAX; ++i) {
        if (((__builtin_amdgcn_readfirstlane(*LDS_AT(const volatile unsigned, lbase + FLG + 32)) >> sh) & 7u) >= n) break;
        __builtin_amdgcn_s_sleep(SSLP);
      }
      asm volatile("" ::: "memory");
      PIN();
    };
    PIN();
    asm volatile("s_waitcnt lgkmcnt(0)" ::: "memory");
    if (!LAY2) asm volatile("s_waitcnt vmcnt(0)" ::: "memory");
    __builtin_amdgcn_s_barrier();
    PIN();
    __builtin_amdgcn_s_setprio(HPRIO);
    if (hw < 3) {
      __builtin_amdgcn_s_sleep(SPRE);
      wait_sig((hw & 1) * 8, 2u);
      __builtin_amdgcn_raw_buffer_store_b32(tag, __builtin_amdgcn_make_buffer_rsrc(kvflag, 0, hw < 2 ? RK * 128 : 0, 0x00020000), lane * 256 + ((tmi ^ hw) & 1) * 128 + tmi * 4, 0, 16);
      if (hw == 0) STAMP(LAY2 ? 5 : 1, 16);
    }
    if (hw == 3) {
      __builtin_amdgcn_s_sleep(KVS);
      const unsigned kv0 = ld_ag(kvflag + (tmi % RK) * 32 + (lane & 15));
      PIN();
      const char* cnt = reinterpret_cast<const char*>(gran + GR_N + bid * 64 + 2);
      unsigned vk[4], vp = lbase + FLG + 8 + (lane & 7), pw = 0u;
#pragma unroll
      for (int j = 0; j < 4; ++j) { vk[j] = l16 + (j >> 1) * 4096u + (j & 1) * (VOFF - 2048u); asm volatile("" : "+v"(vk[j])); }
#define G4(a, b, c, d, sc) "s_nop 0\n\tglobal_load_lds_dwordx4 %" #a ", %4" sc "\n\tglobal_load_lds_dwordx4 %" #b ", %4 offset:1024" sc "\n\t"             \
                       "global_load_lds_dwordx4 %" #c ", %4 offset:2048" sc "\n\tglobal_load_lds_dwordx4 %" #d ", %4 offset:3072" sc "\n\t"
#define M4 "s_addk_i32 m0, 0x1000\n\t"
      auto stage = [&](const char* sp, unsigned dst) __attribute__((always_inline)) {
        asm volatile("s_mov_b32 m0, %5\n\t" G4(0, 0, 1, 1, " sc1") M4 G4(2, 2, 3, 3, " sc1")
                     :: "v"(vk[0]), "v"(vk[1]), "v"(vk[2]), "v"(vk[3]), "s"(sp), "s"(dst) : "m0", "scc", "memory");
      };
      auto count = [&]() __attribute__((always_inline)) {
        asm volatile("s_mov_b32 m0, %2\n\ts_nop 0\n\tglobal_load_lds_dword %0, %1 sc1" :: "v"(0u), "s"(cnt), "s"(lbase + FLG - 252) : "m0", "memory");
        cnt += 8;
      };
      auto freed = [&](unsigned n) __attribute__((always_inline)) {
        if (!none(pw < n)) {
#pragma unroll 1
          for (int i = 0; i < 4 * SPIN_MAX; ++i) {
            pw = *LDS_AT(const volatile unsigned char, vp);
            if (none(pw < n)) break;
            __builtin_amdgcn_s_sleep(W3SLP);
          }
        }
        asm volatile("" ::: "memory");
      };
      unsigned long long ord = (0xfedcba9875316420ull >> 4 & ~((1ull << 2 * (tmi & 6)) - 1ull)) | (0xfedcba9875316420ull & ((1ull << 2 * (tmi & 6)) - 1ull));
      ord ^= (tmi & 9) * 0x1111111111111111ull;
      unsigned seen;
      const char* kvb = reinterpret_cast<const char*>(KV) + (size_t)(tni * 2 + (tmi & 1)) * (64 * 2048);
      auto panel = [&]() __attribute__((always_inline)) {
        const unsigned pn = (unsigned)ord & 15u;
        ord >>= 4;
        if (__builtin_expect(!((seen >> pn) & 1u), 0)) {
#pragma unroll 1
          for (int i = 0; i < SPIN_MAX && !((seen >> pn) & 1u); ++i) seen = (unsigned)__ballot(ld_ag(kvflag + (tmi % RK) * 32 + (lane & 15)) == tag);
        }
        asm volatile("" ::: "memory");
        return kvb + pn * 8192u;
      };
      asm volatile("" : "+s"(ord), "+s"(kvb), "+s"(cnt));
      asm volatile("" : "+v"(vp), "+v"(pw));
      PIN();
      seen = (unsigned)__ballot(kv0 == tag);
      const char* sp = panel();
      wait_sig(16, 4u);
      stage(sp, lbase + 2 * KSB);
      count();
      const int ksp = LAY2 ? PNS / 2 : -1, ksg = LAY2 ? 64 : GF0;
      const u16* gp = gptr(wg, wu);
#pragma unroll 1
      for (int k = 1; k < 15; ++k) {
        if (__builtin_expect(k == ksp, 0)) {
          gu_setup();
          gate_up(xq, rmd);
          PIN();
          *LDS_AT(volatile unsigned char, lbase + FLG + 7) = (unsigned char)0x7f;
        }
        freed((unsigned)(0xba9876543211000ull >> 4 * k) & 15u);
        stage(sp + 2048, lbase + ((2 * k + 1) & 7) * KSB);
        if (__builtin_expect(k == 1, 0)) { cnt -= 4; count(); cnt -= 4; }
        sp = panel();
        stage(sp, lbase + ((2 * k + 2) & 7) * KSB);
        count();
        if (__builtin_expect((unsigned)(k - ksg) < 4u, 0)) gather(gp, k - ksg);
      }
      freed(12u);
      glds16s<true>(csf + 4096 + tni * 128, l16, lbase + UGL);
      stage(sp + 2048, lbase + 7 * KSB);
      count();
      const unsigned vwo = (unsigned)((lane >> 3) * D_ + oni * 64 + ((lane & 7) ^ ((lane >> 3) & 6)) * 8) * 2u;
      constexpr unsigned vs = 8u * D_ * 2u - 1024u;
#pragma unroll 1
      for (int i = 0; i < 6; ++i) {
        const int ns = (i + 5) & 7;
        freed(i < 3 ? 12u : 10u + i);
        const char* wp = reinterpret_cast<const char*>(wo) + (size_t)ns * (128 * D_ * 2);
#pragma unroll
        for (int j = 0; j < 4; ++j)
          glds16x4<false>(wp + j * (32u * D_ * 2u), vwo, vwo + vs, vwo + 2 * vs, vwo + 3 * vs, lbase + ns * ASB + j * 4096);
      }
      asm volatile("s_waitcnt vmcnt(0)" ::: "memory");
      return;
    }
    if (!LAY2) {
      auto until = [&](unsigned n) {
        while ((__builtin_amdgcn_readfirstlane(*LDS_AT(const volatile unsigned, lbase + FLG + 8)) & 255u) < n) __builtin_amdgcn_s_sleep(16);
        asm volatile("" ::: "memory");
        PIN();
      };
      if (hw != 1) {
        constexpr int N2 = HAND_N + Y_N, X3N = 3 * D_ / 2, N0 = X3N + PS_N;
        const __amdgpu_buffer_rsrc_t rsi = __builtin_amdgcn_make_buffer_rsrc(hand + (hw ? GRAN_N : 2 * HAND_N - X3N), 0, (hw ? N2 : N0) * 4, 0x00020000);
        until(IAT);
#pragma unroll 1
        for (int i = 0; i < ((hw ? N2 : N0) / 4 + 16383) / 16384; ++i) {
          __builtin_amdgcn_raw_buffer_store_b128(u32x4{SENT, SENT, SENT, SENT}, rsi, ((i * 256 + bid) * 64 + lane) * 16, 0, 16);
          __builtin_amdgcn_s_sleep(ISLP);
        }
      }
      if (hw != 1) {
        const __amdgpu_buffer_rsrc_t rq = __builtin_amdgcn_make_buffer_rsrc(reinterpret_cast<char*>(KV) + QG + ((256 + bid) * 2 + (hw >> 1)) * 4096, 0, 4096, 0x00020000);
#pragma unroll
        for (int i = 0; i < 4; ++i) __builtin_amdgcn_raw_buffer_store_b128(u32x4{SENT, SENT, SENT, SENT}, rq, lane * 16 + i * 1024, 0, 16);
      }
      const size_t og = (size_t)((fg - xcd * 28) / 7 * 256 + lane) * F_ + (xcd * 7 + fg % 7) * 64;
      const u16* p = gptr(wg, wu);
#pragma unroll 1
      for (unsigned n = T0; n < T0 + 13u; ++n) {
        until(n);
        if (hw == 1 && n < T0 + 8u) {
          const unsigned p0 = touch((((n - T0) & 1u) ? wu : wg) + og + (size_t)((n - T0) >> 1) * (64 * F_));
          asm volatile("" :: "v"(p0));
        }
        if (hw == 2 && n == TFL) {
          const int ln = fresh_lane() & 15;
          const unsigned p1 = ld_ag(gran + (ln < 4 ? GR_O + (((ln >> 1) * 16 + omi) * RO + oni % RO) * 64 + (ln & 1) * 32 :
                                            (ln < 8 ? GR_X + (tmi * RX + tni % RX) * 128 + (ln & 3) * 32 : GR_KV + ((16 + tni) * RK + tmi % RK) * 32)));
          asm volatile("" :: "v"(p1));
        }
        if (hw == 2 && n == TW) {
          const int ln = fresh_lane();
          unsigned long long a0, a1;
          CODE_ADDR("layers_kernel", a0); CODE_ADDR("end_marker_kernel", a1);
          const unsigned p1 = touch_code(a0, a1, 3 + loc, ln), p0 = touch(wo + (size_t)(384 + (loc / (32 / OMX)) * (256 / OMX) + ln) * D_ + oni * 64);
          asm volatile("" :: "v"(p0), "v"(p1));
        }
        if (n >= G0 && (n - G0) % GS == 0u && (n - G0) / GS < 4u) gather(p, (n - G0) / GS);
        if (n == UAT && hw == UHW) {
          u32x4 a[4][2], c[2];
#pragma unroll
          for (int i = 0; i < 4; ++i)
#pragma unroll
            for (int h = 0; h < 2; ++h) a[i][h] = *reinterpret_cast<const u32x4*>(wo + (size_t)(tni * 64 + tmi * 4 + i) * D_ + h * 512 + lane * 8);
#pragma unroll
          for (int h = 0; h < 2; ++h) c[h] = *reinterpret_cast<const u32x4*>(w_r + h * 512 + lane * 8);
          auto dot = [&](unsigned x, unsigned y, float z) { return __builtin_amdgcn_fdot2_f32_bf16(__builtin_bit_cast(bf16x2, x), __builtin_bit_cast(bf16x2, y), z, false); };
          PIN();
          __builtin_amdgcn_s_setprio(0);
#pragma unroll 1
          for (int i = 0; i < SPIN_MAX; ++i) {
            if (__builtin_amdgcn_readfirstlane(*LDS_AT(const volatile unsigned, lbase + FLG + 16)) >= UARR) break;
            __builtin_amdgcn_s_sleep(4);
          }
          asm volatile("s_waitcnt vmcnt(0)" ::: "memory");
          PIN();
          float wm = 0.f;
#pragma unroll
          for (int e = 0; e < 8; ++e) wm = dot(c[e >> 2][e & 3], 0x3f803f80u, wm);
          wm = wave_sum(wm);
          lds_wr(lbase + FLG + 60, __float_as_uint(wm));
          wm *= -1.0f / 1024.0f;
          f32x2 u[2], g[2];
#pragma unroll
          for (int i = 0; i < 4; ++i) {
            float p1 = 0.f, pw = 0.f, p2 = 0.f;
#pragma unroll
            for (int e = 0; e < 8; ++e) {
              const unsigned x = a[i][e >> 2][e & 3];
              p1 = dot(x, 0x3f803f80u, p1); pw = dot(x, c[e >> 2][e & 3], pw); p2 = dot(x, x, p2);
            }
            u[i >> 1][i & 1] = fmaf(p1, wm, pw); g[i >> 1][i & 1] = p2;
          }
          float us = fold4(u[0], u[1]), gs = fold4(g[0], g[1]);
          us = dpp_add<0x121, 0xf>(dpp_add<0x122, 0xf>(us)); gs = dpp_add<0x121, 0xf>(dpp_add<0x122, 0xf>(gs));
          __builtin_amdgcn_raw_buffer_store_b32(__float_as_uint((lane & 1) ? __builtin_amdgcn_sqrtf(gs) * (1.0f / 32.0f) : us),
              __builtin_amdgcn_make_buffer_rsrc(csf + 4096 + tni * 128, 0, 512, 0x00020000), (lane & 14) ? 512 : ((lane & 1) * 64 + tmi * 4 + (lane >> 4)) * 4, 0, 16);
        }
      }
      if (hw == 2 && bid == 0) {
        const __amdgpu_buffer_rsrc_t rsc = __builtin_amdgcn_make_buffer_rsrc(gran, 0, GR_N * 4, 0x00020000);
#pragma unroll 1
        for (int spin = 0; spin < SPIN_MAX; ++spin) {
          bool ok = true;
#pragma unroll
          for (int i = 0; i < 4; ++i) ok &= __builtin_amdgcn_raw_buffer_load_b32(rsc, (GR_ROW + (i * 64 + lane) * 32 + 1) * 4, 0, 16) == tag;
          if (__all(ok)) break;
          __builtin_amdgcn_s_sleep(8);
          asm volatile("" ::: "memory");
        }
        if (lane == 0) st_ag(gran, tag);
      }
      return;
    }
    unsigned* X2 = hand + GRAN_N + Y_N;
    unsigned* XS = X2 + X2_N;
    unsigned* P4 = XS + XS_N;
    unsigned* PA = P4 + P4_N;
    const __amdgpu_buffer_rsrc_t rsh = __builtin_amdgcn_make_buffer_rsrc(hand, 0, HAND_N * 4, 0x00020000);
    auto signal = [&](unsigned ph) {
      asm volatile("" ::: "memory");
      PIN();
      *LDS_AT(volatile unsigned char, lbase + FLG + 4 + hw) = (unsigned char)ph;
    };
    auto wait_all = [&](unsigned ph) {
#pragma unroll 1
      for (int i = 0; i < SPIN_MAX; ++i) {
        if (all_ge(__builtin_amdgcn_readfirstlane(*LDS_AT(const volatile unsigned, lbase + FLG + 4)), ph)) break;
        __builtin_amdgcn_s_sleep(WSLP);
      }
      asm volatile("" ::: "memory");
      PIN();
    };
    gu_setup();
    gate_up(xq, rmd);
    signal(hw == 2 ? 2u : 0x7fu);
    if (wid == 8) STAMP(dk, 2);
    if (hw < 2) {
#pragma unroll 1
      for (int i = 0; i < SPIN_MAX; ++i) {
        if (__builtin_amdgcn_readfirstlane(*LDS_AT(const volatile unsigned, lbase + FLG)) >= FSTART) break;
        __builtin_amdgcn_s_sleep(WSLP);
      }
      asm volatile("" ::: "memory");
      PIN();
      if (wid == 8) STAMP(dk, 10);
      u32x2 w[16];
      const __amdgpu_buffer_rsrc_t rd_ = __builtin_amdgcn_make_buffer_rsrc(const_cast<u16*>(wd), 0, D_ * F_ * 2, 0x00020000);
      auto ldw = [&](int c) __attribute__((always_inline)) {
#pragma unroll
        for (int f = 0; f < 16; ++f) w[f] = __builtin_amdgcn_raw_buffer_load_b64(rd_, lane * 8 + c * 512 + (f & 1) * 2048, (fg * 16 + (f & ~1)) * (D_ * 2) + hw * 1024, 0);
        PIN();
      };
      ldw(0);
      const unsigned pt = __builtin_amdgcn_raw_buffer_load_b32(rd_, (lane >> 2) * (D_ * 2) + (lane & 3) * 128 + 512, fg * 16 * (D_ * 2) + hw * 1024, 0);
      int yo = p1blk ? (GRAN_N + fg * D_ + hw * 512) * 4 + lane * 16 : HAND_N * 4;
      asm volatile("" : "+v"(yo));
      wait_all(2u);
      if (wid == 8) STAMP(dk, 3);
      const s16x4 a = act_diag();
      const u32x4 y0 = down(a, w);
      PIN();
      ldw(1);
      __builtin_amdgcn_raw_buffer_store_b128(y0, rsh, yo, 0, 16);
      PIN();
      __builtin_amdgcn_raw_buffer_store_b128(down(a, w), rsh, yo + 1024, 0, 16);
      asm volatile("" :: "v"(pt));
      if (wid == 8) STAMP(dk, 4);
    }
    PIN();
    [&]() __attribute__((always_inline)) {
    const int lane = fresh_lane();
    const float rmb = __uint_as_float(rmd << 16);
    auto first_words = [&](int src, auto nc) __attribute__((always_inline)) {
#pragma unroll 1
      for (int spin = 0; spin < SPIN_MAX; ++spin) {
        unsigned mx = 0u;
#pragma unroll
        for (int i = 0; i < decltype(nc)::value; ++i) mx = umax(mx, __builtin_amdgcn_raw_buffer_load_b32(rsh, src, i * (56 * D_ * 4), 16));
        if (none(mx == SENT)) break;
        __builtin_amdgcn_s_sleep(PSLP);
        asm volatile("" ::: "memory");
      }
    };
    if (hw == 2) {
      wait_all(2u);
      if (own) {
        const float rden = __uint_as_float(rmd & 0xffff0000u), rrin = frcp(rden);
        const int col = ow * 16 + (lane & 3) * 4 + (lane >> 4);
        const unsigned xo2 = ld_ag(reinterpret_cast<const unsigned*>(A + (size_t)idx * D_ + (col & ~1)));
        __builtin_amdgcn_s_sleep(PRE1);
        first_words((GRAN_N + (lane < 56 ? lane : 0) * D_ + ow * 16) * 4, std::integral_constant<int, 4>{});
        const float y = y_sum(rsh, std::integral_constant<int, 2>{});
        const unsigned rk = best >> 16;
        const float rv = __uint_as_float(((rk & 0x8000u) ? (rk & 0x7fffu) : (~rk & 0xffffu)) << 16);
        const float rs = rbf(frcp(1.0f + __expf(-rv)));
        const float x1v = __uint_as_float((col & 1) ? xo2 & 0xffff0000u : xo2 << 16);
        float h0 = x1v, h1 = x1v;
        ln_apply2(h0, h1, rmb, rden, rrin);
        const float x2v = rbf(x1v + rbf(h0 + rbf(rbf(y) * rs)));
        const auto pr = __builtin_amdgcn_permlane16_swap(__float_as_uint(x2v), __float_as_uint(x2v), false, false);
        __builtin_amdgcn_raw_buffer_store_b32(f2word(__uint_as_float((pr[0] >> 16) | pr[1])), rsh, (lane & 28) ? HAND_N * 4 : (int)(X2 - hand) * 4 + col * 2, 0, 16);
        const float d = x2v - rmb;
        const float sd = wave_sum(d), sq = wave_sum(d * d);
        if (lane < 2) st_ag(XS + ow * 2 + lane, f2word(lane ? sq : sd));
        STAMP(dk, 5);
      }
    }
    {
      const int ks = kcap ^ (tmi & 8), g4 = lane >> 4;
      const int src = (int)(X2 - hand) * 4 + ks * 128 + g4 * 16;
      int dst = (lane & 12) ? HAND_N * 4 : (int)((P4 - hand) + (tni * 16 + ks) * 192 + (hw ^ (hw < 2 ? ((lane >> 1) ^ tmi) & 1 : 0)) * 64 + (lane & 2) * 16 + (lane & 1) * 8 + (g4 >> 1) * 16 + (g4 & 1) * 4) * 4;
      asm volatile("" : "+v"(dst));
      u32x4 x0, x1;
      if (!(hw == 2 && own)) __builtin_amdgcn_s_sleep(PRE4);
#pragma unroll 1
      for (int spin = 0; spin < SPIN_MAX; ++spin) {
        x0 = __builtin_amdgcn_raw_buffer_load_b128(rsh, src, 0, 16); x1 = __builtin_amdgcn_raw_buffer_load_b128(rsh, src + 64, 0, 16);
        if (none(umax(umax(umax(x0[0], x0[1]), umax(x0[2], x0[3])), umax(umax(x1[0], x1[1]), umax(x1[2], x1[3]))) == SENT)) break;
        __builtin_amdgcn_s_sleep(PSLP);
        asm volatile("" ::: "memory");
      }
      f32x4 d[4];
#pragma unroll
      for (int i = 0; i < 4; ++i) d[i] = MFMA16(sl[2 * i], __builtin_bit_cast(s16x8, x0), (f32x4{0.f, 0.f, 0.f, 0.f}));
#pragma unroll
      for (int i = 0; i < 4; ++i) d[i] = MFMA16(sl[2 * i + 1], __builtin_bit_cast(s16x8, x1), d[i]);
      u32x4 o;
#pragma unroll
      for (int e = 0; e < 4; ++e) o[e] = f2word((lane & 2) ? ((lane & 1) ? d[3][e] : d[2][e]) : ((lane & 1) ? d[1][e] : d[0][e]));
      __builtin_amdgcn_raw_buffer_store_b128(o, rsh, dst, 0, 16);
      PIN();
      if (wid == 8) STAMP(dk, 6);
    }
    if (hw == 2) {
      const int col = (lane & 15) * 4 + (lane >> 4), src = (int)((P4 - hand) + (tni * 16 + (lane >> 4)) * 192 + (lane & 15) * 4) * 4;
      const __amdgpu_buffer_rsrc_t rsq = __builtin_amdgcn_make_buffer_rsrc(hand, 0, fast ? HAND_N * 4 : 0, 0x00020000);
      if (own) {
#pragma unroll
        for (int i = 0; i < 14; ++i)
          __builtin_amdgcn_raw_buffer_store_b128(u32x4{SENT, SENT, SENT, SENT}, rsh, (GRAN_N + (i * 16 + (lane >> 2)) * D_ + ow * 16 + (lane & 3) * 4) * 4, 0, 16);
      }
      float cs[3], val[3];
#pragma unroll
      for (int p = 0; p < 3; ++p) cs[p] = __uint_as_float(ld_ag(reinterpret_cast<const unsigned*>(csf) + p * 1024 + n0 + col));
      u32x2 xs;
      __builtin_amdgcn_s_sleep(PRE7);
      first_words((int)((P4 - hand) + (tni * 16 + (lane & 15)) * 192 + ((lane >> 4) % 3) * 64) * 4, std::integral_constant<int, 1>{});
#pragma unroll 1
      for (int spin = 0; spin < SPIN_MAX; ++spin) {
        u32x4 v[3][4];
        xs = __builtin_amdgcn_raw_buffer_load_b64(rsh, (int)((XS - hand) + lane * 2) * 4, 0, 16);
#pragma unroll
        for (int p = 2; p >= 0; --p)
#pragma unroll
          for (int i = 0; i < 4; ++i) v[p][i] = __builtin_amdgcn_raw_buffer_load_b128(p ? rsh : rsq, src + p * 256, i * (4 * 192 * 4), 16);
        auto part = [&](int p) __attribute__((always_inline)) {
          float z[4];
#pragma unroll
          for (int e = 0; e < 4; ++e) {
            z[e] = fadd(fadd(__uint_as_float(v[p][0][e]), __uint_as_float(v[p][1][e])), fadd(__uint_as_float(v[p][2][e]), __uint_as_float(v[p][3][e])));
          }
          val[p] = fold16(f32x2{z[0], z[1]}, f32x2{z[2], z[3]});
        };
        part(2); part(1);
        val[0] = 0.f;
        if (fast) part(0);
        const float chk = fadd(fadd(fadd(val[0], val[1]), fadd(val[2], __uint_as_float(xs[0]))), __uint_as_float(xs[1]));
        if (none(chk != chk)) break;
        unsigned mx = umax(xs[0], xs[1]);
#pragma unroll
        for (int i = 0; i < 12; ++i) mx = umax(umax(umax(mx, v[i >> 2][i & 3][0]), v[i >> 2][i & 3][1]), umax(v[i >> 2][i & 3][2], v[i >> 2][i & 3][3]));
        if (none(mx == SENT)) break;
        __builtin_amdgcn_s_sleep(PSLP);
        asm volatile("" ::: "memory");
      }
      const auto sw = __builtin_amdgcn_permlane32_swap(xs[0], xs[1], false, false);
      float sm = __uint_as_float(sw[0]) + __uint_as_float(sw[1]);
      sm = dpp_add<0x142, 0xa>(dpp_add<0x118, 0xf>(dpp_add<0x114, 0xf>(dpp_add<0x112, 0xf>(dpp_add<0x111, 0xf>(sm)))));
      const float dm = __int_as_float(__builtin_amdgcn_readlane(__float_as_int(sm), 31)) * (0.25f / 1024.0f);
      const float var = fmaxf(__int_as_float(__builtin_amdgcn_readlane(__float_as_int(sm), 63)) * (0.25f / 1024.0f) - dm * dm, 0.f);
      const float mb = rbf(rmb + dm);
      const float rin = frcp(rbf(__builtin_amdgcn_sqrtf(rbf(rbf(var) + 1e-5f))));
      const unsigned kv = pack2((val[1] - mb * cs[1]) * rin, (val[2] - mb * cs[2]) * rin);
      *LDS_AT(u16, lbase + TAB + 128 + kpos(col) * 2) = (u16)kv;
      *LDS_AT(u16, lbase + TAB + 256 + col * 2) = (u16)(kv >> 16);
      if (fast) *LDS_AT(u16, lbase + TAB + kpos(col) * 2) = f2bf((val[0] - mb * cs[0]) * rin * QSCALE);
      signal(7u);
      STAMP(dk, 7);
      return;
    }
    if (!fast) return;
    const int head = tni * 2 + hw;
    const unsigned tb = lbase + TAB + hw * 64;
    {
      const __amdgpu_buffer_rsrc_t rs = __builtin_amdgcn_make_buffer_rsrc(KV, 0, 2 * S_ * D_ * 2, 0x00020000);
      const int src = (head * 64 + tmi * 4) * 2048 + lane * 16;
      auto ld = [&](int i, unsigned off) { return __builtin_bit_cast(s16x8, __builtin_amdgcn_raw_buffer_load_b128(rs, src + (i & 3) * 1024, (i >> 3) * 65536 + ((i >> 2) & 1) * 4096 + off, 16)); };
#pragma unroll 1
      for (int i = 0; i < SPIN_MAX; ++i)
        if (none(ld_ag(kvflag + ((tmi & ~1) + hw) * 32 + tmi + (lane & 8)) != tag)) break;
      asm volatile("" ::: "memory");
      s16x8 f[16];
#pragma unroll
      for (int i = 0; i < 16; ++i) f[i] = ld(i, 0u);
      PIN();
      const bool on = ((lane ^ (lane >> 4)) & 1) == 0;
      const unsigned pl = lbase + RED + hw * 1024;
      unsigned pw = pl + (lane >> 4) * 64 + (lane & 15) * 2, pr = pl + (on ? (lane >> 5) * 16 : 512), qr = on ? tb + (lane >> 5) * 16 : pl + 512;
      const int gone = (((idx >> 7) & 7) == tmi) ? ((idx >> 10) * 2 + (kpos(idx & 31) >> 4)) * 64 + ((idx >> 5) & 3) * 16 + (kpos(idx & 31) & 15) : -1;
      asm volatile("" : "+v"(pw), "+v"(pr), "+v"(qr));
      *LDS_AT(u32x2, pl + 512 + lane * 8) = u32x2{0u, 0u};
      wait_all(7u);
      u32x4 q[2];
#pragma unroll
      for (int sx = 0; sx < 2; ++sx) {
        q[sx] = *LDS_AT(const u32x4, qr + sx * 32);
      }
      float sc[4];
#pragma unroll
      for (int i = 0; i < 8; ++i) {
        f32x4 d = MFMA16(__builtin_bit_cast(s16x8, q[0]), f[2 * i], (f32x4{0.f, 0.f, 0.f, 0.f}));
        d = MFMA16(__builtin_bit_cast(s16x8, q[1]), f[2 * i + 1], d);
        PIN();
        f[2 * i] = ld(2 * i, VOFF); f[2 * i + 1] = ld(2 * i + 1, VOFF);
        PIN();
#pragma unroll
        for (int r = 0; r < 2; ++r) sc[(i >> 2) * 2 + r] = (i & 3) == 0 || (lane >> 4) == (i & 3) ? d[r] : sc[(i >> 2) * 2 + r];
      }
      if (gone >= 0) {
#pragma unroll
        for (int r = 0; r < 4; ++r) sc[r] = r * 64 + lane == gone ? -INFINITY : sc[r];
      }
      const float mx = wave_fmax(fmaxf(fmaxf(sc[0], sc[1]), fmaxf(sc[2], sc[3])));
      const unsigned p0 = pack2(__builtin_amdgcn_exp2f(sc[0] - mx), __builtin_amdgcn_exp2f(sc[1] - mx)), p1 = pack2(__builtin_amdgcn_exp2f(sc[2] - mx), __builtin_amdgcn_exp2f(sc[3] - mx));
      *LDS_AT(u16, pw) = (u16)p0; *LDS_AT(u16, pw + 32) = (u16)(p0 >> 16);
      *LDS_AT(u16, pw + 256) = (u16)p1; *LDS_AT(u16, pw + 288) = (u16)(p1 >> 16);
      asm volatile("" ::: "memory");
      const float ls = wave_sum((__uint_as_float(p0 << 16) + __uint_as_float(p0 & 0xffff0000u)) + (__uint_as_float(p1 << 16) + __uint_as_float(p1 & 0xffff0000u)));
      f32x4 o = {0.f, 0.f, 0.f, 0.f};
#pragma unroll
      for (int i = 0; i < 16; ++i) o = MFMA16(*LDS_AT(const s16x8, pr + i * 32), f[i], o);
      int pv = __float_as_int((lane & 16) ? o[1] : o[0]);
      asm volatile("s_nop 0\n\tv_writelane_b32 %0, %1, 32\n\tv_writelane_b32 %0, %2, 33" : "+v"(pv) : "s"(ls), "s"(mx));
      __builtin_amdgcn_raw_buffer_store_b32(f2word(__int_as_float(pv)), rsh, lane < 34 ? (int)((PA - hand) + (head * 8 + tmi) * 64 + lane) * 4 : HAND_N * 4, 0, 16);
    }
    if (wid == 8) STAMP(dk, 8);
    if (tmi != ((idx >> 7) & 7)) return;
    {
      const int src = (int)((PA - hand) + head * 512 + (lane < 34 ? lane : 0)) * 4;
      const unsigned ta = tb + (lane & 31) * 2;
      const float so = wave_sum(lane < 32 ? bf2f(*LDS_AT(const u16, ta)) * bf2f(*LDS_AT(const u16, ta + 128)) : 0.f);
      const float vo = lane < 32 ? bf2f(*LDS_AT(const u16, ta + 256)) : 1.0f;
      unsigned v[8];
      __builtin_amdgcn_s_sleep(PRE5);
#pragma unroll 1
      for (int spin = 0; spin < SPIN_MAX; ++spin) {
        unsigned mx = 0u;
#pragma unroll
        for (int i = 0; i < 8; ++i) v[i] = __builtin_amdgcn_raw_buffer_load_b32(rsh, src + i * 256, 0, 16);
#pragma unroll
        for (int i = 0; i < 8; ++i) mx = umax(mx, v[i]);
        if (none(mx == SENT)) break;
        __builtin_amdgcn_s_sleep(PSLP);
        asm volatile("" ::: "memory");
      }
      float m8[8], mx = so;
#pragma unroll
      for (int i = 0; i < 8; ++i) { m8[i] = __int_as_float(__builtin_amdgcn_readlane((int)v[i], 33)); mx = fmaxf(mx, m8[i]); }
      float acc = __builtin_amdgcn_exp2f(so - mx) * vo;
#pragma unroll
      for (int i = 0; i < 8; ++i) acc += __builtin_amdgcn_exp2f(m8[i] - mx) * __uint_as_float(v[i]);
      const float l = __int_as_float(__builtin_amdgcn_readlane(__float_as_int(acc), 32));
      const float ov = acc / l;
      const float o1 = __int_as_float(__builtin_amdgcn_update_dpp(0, __float_as_int(ov), 0x55, 0xf, 0xf, false));
      const float o2 = __int_as_float(__builtin_amdgcn_update_dpp(0, __float_as_int(ov), 0xaa, 0xf, 0xf, false));
      const float o3 = __int_as_float(__builtin_amdgcn_update_dpp(0, __float_as_int(ov), 0xff, 0xf, 0xf, false));
      const __amdgpu_buffer_rsrc_t rso = __builtin_amdgcn_make_buffer_rsrc(Of, 0, S_ * D_ * 2, 0x00020000);
      if (lane < 32 && (lane & 3) == 0)
        __builtin_amdgcn_raw_buffer_store_b64(u32x2{pack2(ov, o1), pack2(o2, o3)}, rso,
            (((idx >> 5) * 32 + head) * 1024 + (lane >> 4) * 512 + (((lane >> 2) & 1) * 32 + (idx & 31)) * 8 + ((lane >> 3) & 1) * 4) * 2, 0, 16);
      asm volatile("s_waitcnt vmcnt(0)" ::: "memory");
      if (lane < RO) st_ag(gran + GR_O + ((16 + (idx >> 7)) * RO + lane) * 64 + 32 + head, tag);
      STAMP(dk, 9);
    }
    }();
    }();
    __builtin_amdgcn_s_setprio(0);
    if (LAY2 || hw != UHW) asm volatile("s_waitcnt vmcnt(0)" ::: "memory");
    PIN();
    ARGS_HERE;
    u32x2 w[16];
    auto ld_wd = [&]() __attribute__((always_inline)) {
      const __amdgpu_buffer_rsrc_t rd_ = __builtin_amdgcn_make_buffer_rsrc(const_cast<u16*>(wd), 0, D_ * F_ * 2, 0x00020000);
#pragma unroll
      for (int f = 0; f < 16; ++f) w[f] = __builtin_amdgcn_raw_buffer_load_b64(rd_, t * 8 + (f & 1) * 2048, (fg * 16 + (f & ~1)) * (D_ * 2), 0);
      PIN();
    };
    float fnum = 0.f, fvar = 0.f;
    const int rrow = omi * 128 + (oni >> 3) * 64 + (hw & 1) * 32;
    if (LAY2) {
      ld_wd();
      if (!p1blk) {
        const int fr = rrow + (lane & 7) * 4 + (lane >> 4);
        const double s1 = ld_ag(stats + fr), s2 = ld_ag(stats + S_ + fr), sw = ld_ag(stats + 2 * S_ + fr);
        lds_wr(lbase + SPM + 132, __float_as_uint(wsum));
        float mean, var, mb, den;
        ln_from_sums(s1, s2, mean, var, mb, den);
        const float g1 = 1.0f + frcp(den);
        fnum = fr == idx ? __uint_as_float(0x7fc00000u) : fmaf(-mean, wsum, (float)sw) * g1;
        fvar = var * g1 * g1;
      }
      asm volatile("" : "+v"(fnum), "+v"(fvar));
      PIN();
    }
    if (hw == 3) {
      const int nfl = (LAY2 && (idx >> 7) == omi) ? 64 : 32;
      auto pfl = (__attribute__((address_space(1))) const unsigned*)(gran + GR_O + (((LAY2 ? 16 : 0) + omi) * RO + oni % RO) * 64 + (lane < nfl ? lane : 0));
      constexpr unsigned vs = 8u * D_ * 2u - 1024u;
      unsigned vw0 = (unsigned)((lane >> 3) * D_ + oni * 64 + ((lane & 7) ^ ((lane >> 3) & 6)) * 8) * 2u, vw1 = vw0 + vs, vw2 = vw0 + 2 * vs, vw3 = vw0 + 3 * vs;
      asm volatile("" : "+v"(pfl), "+v"(vw0), "+v"(vw1), "+v"(vw2), "+v"(vw3));
      auto wait_arr = [&](unsigned n) {
#pragma unroll 1
        for (int i = 0; i < 4 * SPIN_MAX; ++i) {
          if (__builtin_amdgcn_readfirstlane(*LDS_AT(const volatile unsigned, lbase + FLG + 16)) >= n) break;
          __builtin_amdgcn_s_sleep(1);
        }
        asm volatile("" ::: "memory");
        PIN();
      };
      wait_arr(12u);
      unsigned fl;
      asm volatile("global_load_dword %0, %1, off sc1" : "=v"(fl) : "v"(pfl) : "memory");
#pragma unroll 1
      for (int i = 0; i < 4 * SPIN_MAX; ++i) {
        if (__builtin_amdgcn_readfirstlane(*LDS_AT(const volatile unsigned char, lbase + FLG + 24)) >= 8u) break;
        __builtin_amdgcn_s_sleep(1);
      }
      asm volatile("" ::: "memory");
      PIN();
#pragma unroll
      for (int j = 12; j < 20; ++j)
        glds16x4<false>(reinterpret_cast<const char*>(wo) + j * (32u * D_ * 2u), vw0, vw1, vw2, vw3, lbase + j * 4096);
      unsigned keep;
      asm volatile("s_mov_b32 %0, m0\n\ts_mov_b32 m0, %3\n\ts_nop 2\n\tglobal_load_lds_dword %1, %2 sc1\n\ts_mov_b32 m0, %0"
                   : "=&s"(keep) : "v"(0u), "s"(reinterpret_cast<const char*>(gran + GR_N + bid * 64 + 39)), "s"(lbase + FLG - 252) : "memory");
      asm volatile("s_waitcnt vmcnt(33)" : "+v"(fl) :: "memory");
      if (__builtin_amdgcn_ballot_w64(fl != tag)) {
        __builtin_amdgcn_s_sleep(CSL1);
#pragma unroll 1
        for (int spin = 0; spin < SPIN_MAX; ++spin) {
          if (!__builtin_amdgcn_ballot_w64(__hip_atomic_load(pfl, __ATOMIC_RELAXED, __HIP_MEMORY_SCOPE_AGENT) != tag)) break;
          __builtin_amdgcn_s_sleep(CSLP);
        }
      }
      asm volatile("" ::: "memory");
      PIN();
    }
    u32x4 pv[4];
    asm volatile("" : "=v"(pv[0]), "=v"(pv[1]), "=v"(pv[2]), "=v"(pv[3]));
    if (LAY2 && !p1blk && hw < 2) {
      const __amdgpu_buffer_rsrc_t rsp = __builtin_amdgcn_make_buffer_rsrc(hand + 2 * HAND_N + PP_O, 0, H_ * S_ * 4, 0x00020000);
      const int src = ((lane >> 3) * S_ + rrow + (lane & 7) * 4) * 4;
#pragma unroll 1
      for (int i = 0; i < 4 * SPIN_MAX; ++i) {
        if (__builtin_amdgcn_readfirstlane(*LDS_AT(const volatile unsigned char, lbase + FLG + 24)) >= 8u) break;
        __builtin_amdgcn_s_sleep(2);
      }
#pragma unroll 1
      for (int spin = 0; spin < SPIN_MAX; ++spin) {
        asm volatile("" ::: "memory");
        unsigned mx = 0u;
#pragma unroll
        for (int i = 0; i < 4; ++i) pv[i] = __builtin_amdgcn_raw_buffer_load_b128(rsp, src, i * (8 * S_ * 4), 16);
#pragma unroll
        for (int i = 0; i < 4; ++i) mx = umax(umax(umax(mx, pv[i][0]), pv[i][1]), umax(pv[i][2], pv[i][3]));
        if (none(mx == SENT)) break;
        __builtin_amdgcn_s_sleep(PPSLP);
      }
    }
    asm volatile("s_waitcnt lgkmcnt(0)" ::: "memory");
    __builtin_amdgcn_s_barrier();
    if (!LAY2) {
      PIN();
      asm volatile(AGAIN "\ngate_up_code:\n\t"
                 "v_lshlrev_b32 v84, 16, v100\n\tv_and_b32 v85, 0xffff0000, v100\n\tv_lshlrev_b32 v86, 16, v101\n\tv_and_b32 v87, 0xffff0000, v101\n\t"
                 "v_subrev_f32 v84, s84, v84\n\tv_subrev_f32 v85, s84, v85\n\tv_subrev_f32 v86, s84, v86\n\tv_subrev_f32 v87, s84, v87\n\t"
                 "v_cvt_pk_bf16_f32 v85, v84, v85\n\tv_cvt_pk_bf16_f32 v87, v86, v87\n\ts_mov_b32 s88, 0x01000100\n\ts_mov_b32 s89, 0x03020302\n\t"
                 "v_lshlrev_b32 v84, 16, v85\n\tv_and_b32 v85, 0xffff0000, v85\n\tv_lshlrev_b32 v86, 16, v87\n\tv_and_b32 v87, 0xffff0000, v87\n\t"
                 "v_mul_f32 v88, s86, v84\n\tv_mul_f32 v89, s86, v85\n\tv_mul_f32 v90, s86, v86\n\tv_mul_f32 v91, s86, v87\n\tv_fma_f32 v84, -v88, s85, v84\n\t"
                 "v_fma_f32 v85, -v89, s85, v85\n\tv_fma_f32 v86, -v90, s85, v86\n\tv_fma_f32 v87, -v91, s85, v87\n\tv_fmac_f32 v88, s86, v84\n\t"
                 "v_fmac_f32 v89, s86, v85\n\tv_fmac_f32 v90, s86, v86\n\tv_fmac_f32 v91, s86, v87\n\tv_cvt_pk_bf16_f32 v88, v88, v89\n\t"
                 "v_cvt_pk_bf16_f32 v90, v90, v91\n\tv_perm_b32 v92, v88, v88, s88\n\tv_perm_b32 v93, v88, v88, s89\n\tv_perm_b32 v94, v90, v90, s88\n\t"
                 "v_perm_b32 v95, v90, v90, s89\n\ts_nop 0\n\tv_and_b32_dpp v96, v92, v102 quad_perm:[0,0,0,0] row_mask:0xf bank_mask:0xf\n\t"
                 "v_and_b32_dpp v97, v92, v103 quad_perm:[0,0,0,0] row_mask:0xf bank_mask:0xf\n\t"
                 "v_and_b32_dpp v100, v93, v102 quad_perm:[0,0,0,0] row_mask:0xf bank_mask:0xf\n\t"
                 "v_and_b32_dpp v101, v93, v103 quad_perm:[0,0,0,0] row_mask:0xf bank_mask:0xf\n\tv_mfma_f32_4x4x4_16b_bf16 v[84:87], v[96:97], v[104:105], 0\n\t"
                 "v_mfma_f32_4x4x4_16b_bf16 v[88:91], v[96:97], v[106:107], 0\n\tv_and_b32_dpp v96, v94, v102 quad_perm:[0,0,0,0] row_mask:0xf bank_mask:0xf\n\t"
                 "v_and_b32_dpp v97, v94, v103 quad_perm:[0,0,0,0] row_mask:0xf bank_mask:0xf\n\t"
                 "v_mfma_f32_4x4x4_16b_bf16 v[84:87], v[100:101], v[108:109], v[84:87]\n\tv_mfma_f32_4x4x4_16b_bf16 v[88:91], v[100:101], v[110:111], v[88:91]\n\t"
                 "v_and_b32_dpp v100, v95, v102 quad_perm:[0,0,0,0] row_mask:0xf bank_mask:0xf\n\t"
                 "v_and_b32_dpp v101, v95, v103 quad_perm:[0,0,0,0] row_mask:0xf bank_mask:0xf\n\tv_mfma_f32_4x4x4_16b_bf16 v[84:87], v[96:97], v[112:113], v[84:87]\n\t"
                 "v_mfma_f32_4x4x4_16b_bf16 v[88:91], v[96:97], v[114:115], v[88:91]\n\tv_and_b32_dpp v96, v92, v102 quad_perm:[1,1,1,1] row_mask:0xf bank_mask:0xf\n\t"
                 "v_and_b32_dpp v97, v92, v103 quad_perm:[1,1,1,1] row_mask:0xf bank_mask:0xf\n\t"
                 "v_mfma_f32_4x4x4_16b_bf16 v[84:87], v[100:101], v[116:117], v[84:87]\n\tv_mfma_f32_4x4x4_16b_bf16 v[88:91], v[100:101], v[118:119], v[88:91]\n\t"
                 "v_and_b32_dpp v100, v93, v102 quad_perm:[1,1,1,1] row_mask:0xf bank_mask:0xf\n\t"
                 "v_and_b32_dpp v101, v93, v103 quad_perm:[1,1,1,1] row_mask:0xf bank_mask:0xf\n\tv_mfma_f32_4x4x4_16b_bf16 v[84:87], v[96:97], v[120:121], v[84:87]\n\t"
                 "v_mfma_f32_4x4x4_16b_bf16 v[88:91], v[96:97], v[122:123], v[88:91]\n\tv_and_b32_dpp v96, v94, v102 quad_perm:[1,1,1,1] row_mask:0xf bank_mask:0xf\n\t"
                 "v_and_b32_dpp v97, v94, v103 quad_perm:[1,1,1,1] row_mask:0xf bank_mask:0xf\n\t"
                 "v_mfma_f32_4x4x4_16b_bf16 v[84:87], v[100:101], v[124:125], v[84:87]\n\tv_mfma_f32_4x4x4_16b_bf16 v[88:91], v[100:101], v[126:127], v[88:91]\n\t"
                 "v_and_b32_dpp v100, v95, v102 quad_perm:[1,1,1,1] row_mask:0xf bank_mask:0xf\n\t"
                 "v_and_b32_dpp v101, v95, v103 quad_perm:[1,1,1,1] row_mask:0xf bank_mask:0xf\n\tv_mfma_f32_4x4x4_16b_bf16 v[84:87], v[96:97], v[128:129], v[84:87]\n\t"
                 "v_mfma_f32_4x4x4_16b_bf16 v[88:91], v[96:97], v[130:131], v[88:91]\n\tv_and_b32_dpp v96, v92, v102 quad_perm:[2,2,2,2] row_mask:0xf bank_mask:0xf\n\t"
                 "v_and_b32_dpp v97, v92, v103 quad_perm:[2,2,2,2] row_mask:0xf bank_mask:0xf\n\t"
                 "v_mfma_f32_4x4x4_16b_bf16 v[84:87], v[100:101], v[132:133], v[84:87]\n\tv_mfma_f32_4x4x4_16b_bf16 v[88:91], v[100:101], v[134:135], v[88:91]\n\t"
                 "v_and_b32_dpp v100, v93, v102 quad_perm:[2,2,2,2] row_mask:0xf bank_mask:0xf\n\t"
                 "v_and_b32_dpp v101, v93, v103 quad_perm:[2,2,2,2] row_mask:0xf bank_mask:0xf\n\tv_mfma_f32_4x4x4_16b_bf16 v[84:87], v[96:97], v[136:137], v[84:87]\n\t"
                 "v_mfma_f32_4x4x4_16b_bf16 v[88:91], v[96:97], v[138:139], v[88:91]\n\tv_and_b32_dpp v96, v94, v102 quad_perm:[2,2,2,2] row_mask:0xf bank_mask:0xf\n\t"
                 "v_and_b32_dpp v97, v94, v103 quad_perm:[2,2,2,2] row_mask:0xf bank_mask:0xf\n\t"
                 "v_mfma_f32_4x4x4_16b_bf16 v[84:87], v[100:101], v[140:141], v[84:87]\n\tv_mfma_f32_4x4x4_16b_bf16 v[88:91], v[100:101], v[142:143], v[88:91]\n\t"
                 "v_and_b32_dpp v100, v95, v102 quad_perm:[2,2,2,2] row_mask:0xf bank_mask:0xf\n\t"
                 "v_and_b32_dpp v101, v95, v103 quad_perm:[2,2,2,2] row_mask:0xf bank_mask:0xf\n\tv_mfma_f32_4x4x4_16b_bf16 v[84:87], v[96:97], v[144:145], v[84:87]\n\t"
                 "v_mfma_f32_4x4x4_16b_bf16 v[88:91], v[96:97], v[146:147], v[88:91]\n\tv_and_b32_dpp v96, v92, v102 quad_perm:[3,3,3,3] row_mask:0xf bank_mask:0xf\n\t"
                 "v_and_b32_dpp v97, v92, v103 quad_perm:[3,3,3,3] row_mask:0xf bank_mask:0xf\n\t"
                 "v_mfma_f32_4x4x4_16b_bf16 v[84:87], v[100:101], v[148:149], v[84:87]\n\tv_mfma_f32_4x4x4_16b_bf16 v[88:91], v[100:101], v[150:151], v[88:91]\n\t"
                 "v_and_b32_dpp v100, v93, v102 quad_perm:[3,3,3,3] row_mask:0xf bank_mask:0xf\n\t"
                 "v_and_b32_dpp v101, v93, v103 quad_perm:[3,3,3,3] row_mask:0xf bank_mask:0xf\n\tv_mfma_f32_4x4x4_16b_bf16 v[84:87], v[96:97], v[152:153], v[84:87]\n\t"
                 "v_mfma_f32_4x4x4_16b_bf16 v[88:91], v[96:97], v[154:155], v[88:91]\n\tv_and_b32_dpp v96, v94, v102 quad_perm:[3,3,3,3] row_mask:0xf bank_mask:0xf\n\t"
                 "v_and_b32_dpp v97, v94, v103 quad_perm:[3,3,3,3] row_mask:0xf bank_mask:0xf\n\t"
                 "v_mfma_f32_4x4x4_16b_bf16 v[84:87], v[100:101], v[156:157], v[84:87]\n\tv_mfma_f32_4x4x4_16b_bf16 v[88:91], v[100:101], v[158:159], v[88:91]\n\t"
                 "v_and_b32_dpp v100, v95, v102 quad_perm:[3,3,3,3] row_mask:0xf bank_mask:0xf\n\t"
                 "v_and_b32_dpp v101, v95, v103 quad_perm:[3,3,3,3] row_mask:0xf bank_mask:0xf\n\tv_mfma_f32_4x4x4_16b_bf16 v[84:87], v[96:97], v[160:161], v[84:87]\n\t"
                 "v_mfma_f32_4x4x4_16b_bf16 v[88:91], v[96:97], v[162:163], v[88:91]\n\ts_nop 1\n\t"
                 "v_mfma_f32_4x4x4_16b_bf16 v[84:87], v[100:101], v[164:165], v[84:87]\n\tv_mfma_f32_4x4x4_16b_bf16 v[88:91], v[100:101], v[166:167], v[88:91]\n\t"
                 "s_nop 4\n\tv_permlane32_swap_b32 v84, v88\n\tv_permlane32_swap_b32 v85, v89\n\tv_permlane32_swap_b32 v86, v90\n\tv_permlane32_swap_b32 v87, v91\n\t"
                 "v_add_f32 v84, v84, v88\n\tv_add_f32 v85, v85, v89\n\tv_add_f32 v86, v86, v90\n\tv_add_f32 v87, v87, v91\n\ts_nop 0\n\t"
                 "v_permlane16_swap_b32 v84, v85\n\tv_permlane16_swap_b32 v86, v87\n\ts_nop 0\n\tv_add_f32 v84, v84, v85\n\tv_add_f32 v86, v86, v87\n\ts_nop 1\n\t"
                 "v_add_f32_dpp v84, v84, v84 row_ror:8 row_mask:0xf bank_mask:0xf\n\tv_add_f32_dpp v86, v86, v86 row_ror:8 row_mask:0xf bank_mask:0xf\n\ts_nop 0\n\t"
                 "v_add_f32_dpp v84, v84, v84 row_ror:4 row_mask:0xf bank_mask:0xf\n\tv_add_f32_dpp v86, v86, v86 row_ror:4 row_mask:0xf bank_mask:0xf\n\t"
                 "ds_write2_b32 v99, v84, v86 offset1:8\n\ts_setpc_b64 s[92:93]\n\t"
                   :: "{s[0:1]}"(ap + 1), "{s2}"(bid), "{v0}"(wid * 64), KEPT() : "s90", "s91", "scc", "memory");
      __builtin_unreachable();
    }
    {
    constexpr int dk = 8;
    constexpr int BEST = FLG + 40, MEET = FLG + 48;
    __builtin_amdgcn_s_setprio(HPRIO);
    if (hw == 0) STAMP(dk, 0);
    const unsigned tb1 = (unsigned)__builtin_amdgcn_s_memrealtime();
    unsigned* hand2 = hand + HAND_N;
    const __amdgpu_buffer_rsrc_t rsh = __builtin_amdgcn_make_buffer_rsrc(hand2, 0, (HAND_N + PS_N) * 4, 0x00020000);
    const __amdgpu_buffer_rsrc_t rsx = __builtin_amdgcn_make_buffer_rsrc(xout, 0, 3 * D_ * 2, 0x00020000);
    constexpr int OOR = (HAND_N + PS_N) * 4;
    if (!p1blk && hw < 2) {
      f32x2 a[2], c[2];
#pragma unroll
      for (int e = 0; e < 4; ++e) {
        a[e >> 1][e & 1] = (__uint_as_float(pv[0][e] << 16) + __uint_as_float(pv[1][e] << 16)) + (__uint_as_float(pv[2][e] << 16) + __uint_as_float(pv[3][e] << 16));
        c[e >> 1][e & 1] = (__uint_as_float(pv[0][e] & 0xffff0000u) + __uint_as_float(pv[1][e] & 0xffff0000u)) + (__uint_as_float(pv[2][e] & 0xffff0000u) + __uint_as_float(pv[3][e] & 0xffff0000u));
      }
      const float r = (fnum + fold8(a[0], a[1])) * __builtin_amdgcn_rsqf(fvar + fold8(c[0], c[1]));
      unsigned key = (__float_as_uint(r) ^ ((unsigned)(__float_as_int(r) >> 31) | 0x80000000u)) & 0xfffff800u;
      if (r != r) key = 0u;
      key = wave_umax(key | (unsigned)(2047 - (rrow + (lane & 7) * 4 + (lane >> 4))));
      __builtin_amdgcn_raw_buffer_store_b64(u32x2{key, tb1 & 0x7fffffffu}, rsh, lane < NREP ? (HAND_N + PK_O + lane * 128 + (rrow >> 5) * 2) * 4 : OOR, 0, 16);
      if (hw == 1) STAMP(dk, 2);
    }
    gu_setup();
    bool got = true;
    auto get_fc = [&]() __attribute__((always_inline)) {
      const u32x2 fw = __builtin_amdgcn_raw_buffer_load_b64(rsh, (HAND_N + PK_O + (bid % NREP) * 128 + lane * 2) * 4, 0, 16);
      got = none(umax(fw[0], fw[1]) == SENT);
      if (got) {
        const unsigned c1 = wave_umax(fw[0]), c2 = wave_umax(fw[0] == c1 ? 0u : fw[0]);
#define BADFC 0
        const unsigned r1 = (2047u - (c1 & 0x7ffu)) ^ BADFC, r2 = 2047u - (c2 & 0x7ffu);
        unsigned dt = (unsigned)((int)((fw[1] - tb1) << 1) >> 1) + 512u;
        dt = dt < 1024u ? dt : 512u;
        *LDS_AT(volatile u32x2, lbase + SPM + 144) = u32x2{r1 | (r2 << 16), (unsigned)__builtin_amdgcn_readlane(dt, r1 >> 5) | (wave_umax(dt) << 16)};
      }
    };
    if (hw == 0) {
      __builtin_amdgcn_s_sleep(FCPRE);
#pragma unroll 1
      for (int i = 0; i < SPIN_MAX; ++i) {
        if (__builtin_amdgcn_readfirstlane(*LDS_AT(const volatile unsigned, lbase + FLG + 28)) - best >= 4u) break;
        __builtin_amdgcn_s_sleep(FCSLP);
      }
      asm volatile("" ::: "memory");
      get_fc();
    } else __builtin_amdgcn_s_sleep(TPRE - 10);
    PIN();
    tile_at(2u);
    const unsigned tb2 = (unsigned)__builtin_amdgcn_s_memrealtime() - 512u;
    LOGDEF;
    LOG();
#pragma unroll 1
    for (int spin = 0; spin < SPIN_MAX && !got; ++spin) { get_fc(); asm volatile("" ::: "memory"); }
    PIN();
    u32x2 fm = {SENT, 0u};
#pragma unroll 1
    for (int i = 0; i < 4 * SPIN_MAX && fm[0] == SENT; ++i) {
      const u32x2 q = *LDS_AT(const volatile u32x2, lbase + SPM + 144);
      fm = u32x2{(unsigned)__builtin_amdgcn_readfirstlane(q[0]), (unsigned)__builtin_amdgcn_readfirstlane(q[1])};
    }
    const int fr1 = fm[0] & 0x7ffu, fr2 = (fm[0] >> 16) & 0x7ffu;
    const unsigned t1 = tb2 + (fm[1] & 1023u), tmx = tb2 + ((fm[1] >> 16) & 1023u);
    auto until = [&](unsigned te) { while ((int)((unsigned)__builtin_amdgcn_s_memrealtime() - te) < 0) __builtin_amdgcn_s_sleep(1); asm volatile("" ::: "memory"); };
    lds_wr(lbase + SPM + 136, tmx);
    PIN();
    if (hw == 0) STAMP(dk, 3);
    const int xoff = (hw * 256 + (lane & 3) * 64 + (lane >> 2) * 4) * 2;
    const int oc = (omi * 2 + (oni >> 3)) * 32, col = oc + (lane & 7) * 4 + (lane >> 4);
    const int xoo = !p1blk && hw == 3 ? (col & ~1) * 2 : S_ * D_ * 2;
    auto get_row = [&](int r, int sl, u32x2& xr, unsigned& xo) __attribute__((always_inline)) {
      if ((r >> 7) == omi) {
#pragma unroll 1
        for (int i = 0; i < SPIN_MAX; ++i) {
          if (__builtin_amdgcn_readfirstlane(*LDS_AT(const volatile unsigned char, lbase + FLG + 24)) >= 16u) break;
          __builtin_amdgcn_s_sleep(1);
        }
        asm volatile("" ::: "memory");
      }
#pragma unroll 1
      for (int spin = 0; spin < SPIN_MAX; ++spin) {
        LOG();
        xr = __builtin_amdgcn_raw_buffer_load_b64(rsx, xoff, sl * (D_ * 2), 16);
        xo = __builtin_amdgcn_raw_buffer_load_b32(rsx, xoo, sl * (D_ * 2), 16);
        LOG();
        if (none(umax(umax(xr[0], xr[1]), xo) == SENT)) break;
        __builtin_amdgcn_s_sleep(XRSLP);
        asm volatile("" ::: "memory");
      }
      PIN();
    };
    auto row_md = [&](const u32x2& xr, int k) __attribute__((always_inline)) {
      auto dot = [&](unsigned x, unsigned y, float z) { return __builtin_amdgcn_fdot2_f32_bf16(__builtin_bit_cast(bf16x2, x), __builtin_bit_cast(bf16x2, y), z, false); };
      const float p1 = dot(xr[1], 0x3f803f80u, dot(xr[0], 0x3f803f80u, 0.f)), p2 = dot(xr[1], xr[1], dot(xr[0], xr[0], 0.f));
      const auto q = __builtin_amdgcn_permlane32_swap(__float_as_uint(p1), __float_as_uint(p2), false, false);
      const float qs = __uint_as_float(q[0]) + __uint_as_float(q[1]);
      const auto q2 = __builtin_amdgcn_permlane16_swap(__float_as_uint(qs), __float_as_uint(qs), false, false);
      float sv = __uint_as_float(q2[0]) + __uint_as_float(q2[1]);
      sv = dpp_add<0x121, 0xf>(dpp_add<0x122, 0xf>(dpp_add<0x124, 0xf>(dpp_add<0x128, 0xf>(sv))));
      lds_wr(lbase + SPM + k * 64 + (lane >> 4) * 16 + hw * 4, f2word(sv));
      u32x4 a, c;
#pragma unroll 1
      for (int i = 0; i < SPIN_MAX; ++i) {
        a = lds_rd4(lbase + SPM + k * 64); c = lds_rd4(lbase + SPM + k * 64 + 32);
        if (__builtin_amdgcn_readfirstlane(umax(umax(a[0], a[1]), umax(a[2], a[3]))) != SENT) break;
      }
      auto sum4 = [&](const u32x4& v) { return (__uint_as_float(v[0]) + __uint_as_float(v[1])) + (__uint_as_float(v[2]) + __uint_as_float(v[3])); };
      const float mean = sum4(a) * (1.0f / 1024.0f), msq = sum4(c) * (1.0f / 1024.0f);
      const float den = rbf(__builtin_amdgcn_sqrtf(rbf(rbf(fmaxf(fmaf(-mean, mean, msq), 0.f)) + 1e-5f)));
      return (unsigned)__builtin_amdgcn_readfirstlane((pack2(mean, mean) & 0xffffu) | (__float_as_uint(den) & 0xffff0000u));
    };
    auto ffn = [&](const u32x2& xr, unsigned md, unsigned n, const __amdgpu_buffer_rsrc_t rs, int yo, auto&& off) __attribute__((always_inline)) {
      gate_up(xr, md);
      if (hw == 0 && n == 2u) STAMP(dk, 8);
      asm volatile("" ::: "memory");
      PIN();
      lds_wr(lbase + MEET + hw * 4, n + off());
      u32x4 mt;
#pragma unroll 1
      for (int i = 0; i < SPIN_MAX; ++i) {
        mt = lds_rd4(lbase + MEET);
        if (__builtin_amdgcn_readfirstlane(min4(mt)) >= n) break;
      }
      asm volatile("" ::: "memory");
      PIN();
      if (hw == 0 && n == 2u) STAMP(dk, 9);
      if (__builtin_amdgcn_readfirstlane(mt[0]) != n + 1u)
        __builtin_amdgcn_raw_buffer_store_b128(down(act_diag(n == 4u ? 512 : 0), w), rs, (yo + fg * D_) * 4 + t * 16, 0, 16);
      dad += n == 2u ? 512 : -512;
      if (hw == 0 && n == 2u) STAMP(dk, 4);
    };
    auto y_sum4 = [&](const __amdgpu_buffer_rsrc_t rs, int yo, int k) __attribute__((always_inline)) {
      const int src = (yo + (hw * 56 + (lane >> 3)) * D_ + oc + (lane & 7) * 4) * 4;
      u32x4 v[7];
#pragma unroll
      for (int i = 0; i < 7; ++i) asm volatile("" : "=v"(v[i]));
      unsigned todo = 0x7fu;
#pragma unroll 1
      for (int spin = 0; spin < SPIN_MAX; ++spin) {
        LOG();
#pragma unroll
        for (int i = 0; i < 7; ++i) if ((todo >> i) & 1u) v[i] = __builtin_amdgcn_raw_buffer_load_b128(rs, src, i * (8 * D_ * 4), 16);
#pragma unroll
        for (int i = 0; i < 7; ++i) if (none(umax(umax(v[i][0], v[i][1]), umax(v[i][2], v[i][3])) == SENT)) todo &= ~(1u << i);
        if (!todo) break;
        __builtin_amdgcn_s_sleep(YSLP);
        asm volatile("" ::: "memory");
      }
      f32x2 z0 = {0.f, 0.f}, z1 = {0.f, 0.f};
#pragma unroll
      for (int i = 0; i < 7; ++i) {
        z0 += f32x2{__uint_as_float(v[i][0]), __uint_as_float(v[i][1])};
        z1 += f32x2{__uint_as_float(v[i][2]), __uint_as_float(v[i][3])};
      }
      const unsigned ya = lbase + SPM + 256 + k * 512 + ((lane & 7) * 4 + (lane >> 4)) * 16;
      lds_wr((lane & 8) ? lbase + DUM + lane * 4 : ya + hw * 4, f2word(fold8(z0, z1)));
      u32x4 q = {0u, 0u, 0u, 0u};
      if (hw == 3) {
#pragma unroll 1
        for (int i = 0; i < 4 * SPIN_MAX; ++i) {
          q = lds_rd4(ya);
          if (none(umax(umax(q[0], q[1]), umax(q[2], q[3])) == SENT)) break;
        }
      }
      return (__uint_as_float(q[0]) + __uint_as_float(q[1])) + (__uint_as_float(q[2]) + __uint_as_float(q[3]));
    };
    const __amdgpu_buffer_rsrc_t rs1 = __builtin_amdgcn_make_buffer_rsrc(hand, 0, HAND_N * 4, 0x00020000);
    u32x2 xr;
    unsigned xo, xo2;
    until(t1 + XDT);
    get_row(fr1, 0, xr, xo);
    if (hw == 0) STAMP(dk, 7);
#define FAILFC 0
    const unsigned md1 = row_md(xr, 0) ^ (FAILFC & 1);
    if (hw == 0) STAMP(dk, 10);
    LOGAT(20);
    u32x2 xr2;
    unsigned md2 = 0u;
    if (p1blk) {
      xr2 = __builtin_amdgcn_raw_buffer_load_b64(rsx, (fr2 >> 7) == omi ? S_ * D_ * 2 : xoff, D_ * 2, 16);
      PIN();
      ffn(xr, md1, 2u, rsh, GRAN_N, [&]() { return 0u; });
      __builtin_amdgcn_s_setprio(R2PRIO);
      LOG();
      if ((fr2 >> 7) == omi || !none(umax(xr2[0], xr2[1]) == SENT)) get_row(fr2, 1, xr2, xo2);
      md2 = row_md(xr2, 1) ^ (FAILFC & 2);
      ffn(xr2, md2, 4u, rs1, GRAN_N, [&]() {
        const u32x2 v = *LDS_AT(const volatile u32x2, lbase + BEST);
        return v[0] != 0u && (2047 - (int)(v[0] & 0x7ffu) != fr2 || v[1] != md2) ? 1u : 0u; });
    }
    if (hw == 0) STAMP(dk, 5);
    LOG();
    float y = 0.f;
    auto put_row = [&](int r, unsigned md, unsigned rb) __attribute__((always_inline)) {
      const float rden = __uint_as_float(md & 0xffff0000u), rs = rbf(frcp(1.0f + __expf(-__uint_as_float(rb << 16))));
      const float xa = __uint_as_float((col & 1) ? xo & 0xffff0000u : xo << 16);
      float ha = xa, hb = xa;
      ln_apply2(ha, hb, __uint_as_float(md << 16), rden, frcp(rden));
      const float o = xa + rbf(ha + rbf(rbf(y) * rs));
      __builtin_amdgcn_raw_buffer_store_b16((unsigned short)(pack2(o, o) & 0xffffu), __builtin_amdgcn_make_buffer_rsrc(out, 0, S_ * D_ * 2, 0x00020000),
                                            (lane & 8) ? S_ * D_ * 2 : (r * D_ + col) * 2, 0, OAUX);
    };
    if (!p1blk) {
      until(tmx + XD2);
      get_row(fr2, 1, xr2, xo2);
      md2 = row_md(xr2, 1) ^ (FAILFC & 2);
      until(t1 + XDT + YD1); until(tmx + YD2);
      if (hw == 3) STAMP(dk, 13);
      LOGAT(8);
      y = y_sum4(rsh, GRAN_N, 0);
      if (hw == 3) STAMP(dk, 14);
      LOGAT(24);
    }
    unsigned bmd = 0u;
    best = 0u;
#pragma unroll 1
    for (int spin = 0; spin < 4 * SPIN_MAX; ++spin) {
      const u32x2 v = *LDS_AT(const volatile u32x2, lbase + BEST);
      best = __builtin_amdgcn_readfirstlane(v[0]); bmd = __builtin_amdgcn_readfirstlane(v[1]);
      if (best) break;
      __builtin_amdgcn_s_sleep(1);
    }
    asm volatile("" ::: "memory");
    const int idx = 2047 - (int)(best & 0x7ffu);
    const unsigned rk = best >> 16, rb = (rk & 0x8000u) ? (rk & 0x7fffu) : (~rk & 0xffffu);
    if (hw == 0) STAMP(dk, 1);
    LOGAT(25);
    LOGOUT(hw);
    if (__builtin_expect(idx != fr1 || bmd != md1, 0)) {
      if (idx == fr2 && bmd == md2) {
        if (p1blk) return;
        y = y_sum4(rs1, GRAN_N, 1);
        xo = xo2;
      } else {
        get_row(idx, idx == fr1 ? 0 : (idx == fr2 ? 1 : 2), xr, xo);
        if (p1blk) ffn(xr, bmd, 6u, rsh, HAND_N + Y3_O, [&]() { return 0u; });
        else y = y_sum4(rsh, HAND_N + Y3_O, 2);
        if (p1blk || hw != 3) return;
#pragma unroll 1
        for (int spin = 0; spin < SPIN_MAX && idx != fr1 && idx != fr2; ++spin) {
          if (__builtin_amdgcn_readfirstlane(ld_ag(gran + GR_F + (oc >> 6) * 32)) == tag) break;
          asm volatile("" ::: "memory");
        }
      }
    }
    if (p1blk || hw != 3) return;
    put_row(idx, bmd, rb);
    }
    return;
  }
  if (wid >= 4) {
    const int lane = fresh_lane();
    const int lw = wid - 4;
    const long long lay_ = ap->lay;
    const u16* A = ap->a; const u16* W = ap->W; const u16* wc = ap->wc;
#define LPRIO 3
#define LP0 3
    __builtin_amdgcn_s_setprio(LP0);
    unsigned va[4], vw[6];
    const unsigned ul = lane;
    va[0] = (unsigned)(m0 + lw * 32) * (K * 2) + (ul >> 3) * (K * 2) + (((ul & 7u) ^ (ul >> 4)) << 4);
    va[1] = va[0] + (8 * K * 2 - 1024);
    va[2] = (va[0] ^ 64u) + 2 * (8 * K * 2 - 1024);
    va[3] = va[2] + (8 * K * 2 - 1024);
#pragma unroll
    for (int i = 0; i < 3; ++i) {
      const unsigned n = ul + 64u * i, k = __umul24(n, 171u) >> 12, cp = n - __umul24(k, 24u);
      const unsigned c = (cp & 7u) ^ ((k & 2u) << 1), part = (cp >> 3) ^ (cp < 16u ? ((c >> 2) ^ (unsigned)tmi) & 1u : 0u);
      vw[i] = __umul24(k, ldw * 2) + part * 2048u + (c << 4) + ((unsigned)(lw * 16 * ldw + n0) * 2u - i * 1024u);
      vw[i + 3] = (vw[i] ^ 16u) + 8 * ldw * 2;
    }
    PIN();
    asm volatile("" : "+v"(va[0]), "+v"(va[1]), "+v"(va[2]), "+v"(va[3]), "+v"(vw[0]), "+v"(vw[1]), "+v"(vw[2]), "+v"(vw[3]), "+v"(vw[4]), "+v"(vw[5]));
    const bool LAY2 = lay_ != 0;
    const int krot = tmi & 8;
    if (LAY2 && slow_cols(W, tni)) W = wc;
    auto stage_a = [&](int kt, int slot) {
      const char* pa = reinterpret_cast<const char*>(A) + (size_t)(kt ^ krot) * 128;
      asm volatile("s_mov_b32 m0, %5\n\ts_nop 0\n\tglobal_load_lds_dwordx4 %0, %4 sc1\n\tglobal_load_lds_dwordx4 %1, %4 offset:1024 sc1\n\t"
                   "global_load_lds_dwordx4 %2, %4 offset:2048 sc1\n\tglobal_load_lds_dwordx4 %3, %4 offset:3072 sc1"
                   :: "v"(va[0]), "v"(va[1]), "v"(va[2]), "v"(va[3]), "s"(pa), "s"(lbase + slot * STB + lw * 4096) : "m0", "memory");
    };
    auto stage_w = [&](int kt, int slot) {
      const char* pw = reinterpret_cast<const char*>(W) + (size_t)(kt ^ krot) * 128 * ldw;
      asm volatile("s_mov_b32 m0, %7\n\ts_nop 0\n\tglobal_load_lds_dwordx4 %0, %6 sc1\n\tglobal_load_lds_dwordx4 %1, %6 offset:1024 sc1\n\t"
                   "global_load_lds_dwordx4 %2, %6 offset:2048 sc1\n\ts_addk_i32 m0, 0xc00\n\ts_nop 0\n\tglobal_load_lds_dwordx4 %3, %6 sc1\n\t"
                   "global_load_lds_dwordx4 %4, %6 offset:1024 sc1\n\tglobal_load_lds_dwordx4 %5, %6 offset:2048 sc1"
                   :: "v"(vw[0]), "v"(vw[1]), "v"(vw[2]), "v"(vw[3]), "v"(vw[4]), "v"(vw[5]), "s"(pw), "s"(lbase + slot * STB + ABYTES + lw * 6144) : "m0", "scc", "memory");
    };
    if (LAY2) {
      __builtin_amdgcn_s_setprio(0);
      __builtin_amdgcn_s_sleep(TPRE - 10);
      tile_at(3u);
      __builtin_amdgcn_s_setprio(LPRIO);
#pragma unroll
      for (int st = 0; st < NST; ++st) stage_w(st, st);
      PIN();
      __builtin_amdgcn_s_barrier();
      PIN();
#pragma unroll
      for (int st = 0; st < NST; ++st) stage_a(st, st);
      asm volatile("s_waitcnt vmcnt(8)" ::: "memory");
    } else {
      __builtin_amdgcn_s_setprio(LPRIO);
      stage_a(0, 0); stage_w(0, 0);
      if (PRO > 1) { stage_a(1, 1); stage_w(1, 1); }
      if (PRO > 2) { stage_a(2, 2); stage_w(2, 2); }
      asm volatile("s_waitcnt vmcnt(%0)" :: "n"(10 * PRO - 10) : "memory");
    }
    int slot = 0;
    __builtin_amdgcn_s_barrier();
    if (!LAY2) {
      if (PRO < 2) { stage_a(1, 1); stage_w(1, 1); }
      if (PRO < 3) { stage_a(2, 2); stage_w(2, 2); }
    }
    for (int kt = 0; kt + 1 < NKT; ++kt) {
      if (LAY2 && kt == 0) asm volatile("s_waitcnt vmcnt(4)" ::: "memory");
      else if (kt + 2 >= NKT) asm volatile("s_waitcnt vmcnt(0)" ::: "memory");
      else asm volatile("s_waitcnt vmcnt(10)" ::: "memory");
      __builtin_amdgcn_s_barrier();
      if (kt + NST < NKT) { stage_a(kt + NST, slot); stage_w(kt + NST, slot); }
      slot = slot == NST - 1 ? 0 : slot + 1;
    }
    __builtin_amdgcn_s_setprio(0);
    __builtin_amdgcn_s_barrier();
  } else {
  LAYER_HERE;
  const int dk = LAY2 ? 5 : 1;
  const int lane = fresh_lane(), wm = wid & 1, wn = wid >> 1;
  if (wid == 0) STAMP(dk, 0);
  f32x4 acc[4][6];
#pragma unroll
  for (int i = 0; i < 4; ++i)
#pragma unroll
    for (int j = 0; j < 6; ++j) acc[i][j] = f32x4{0.f, 0.f, 0.f, 0.f};
  const int q16 = lane & 15, g4 = lane >> 4;
  const int e0 = 16 * (q16 >> 3) + (q16 & 7);
  s16x8 af[2][4];
  unsigned aad[2], wad[2];
  {
    const int row = wm * 64 + e0, swz = ((row >> 1) & 3) | (((row >> 4) & 1) << 2);
#pragma unroll
    for (int h = 0; h < 2; ++h) aad[h] = lbase + row * 128 + (((4 * h + g4) ^ swz) << 4);
    const int tq = (lane & 15) >> 2, tp = lane & 3, k = 8 * g4 + tq, ch = wn * 4 + 2 * (tp >> 1);
    wad[0] = lbase + ABYTES + k * 384 + ((ch ^ (4 * ((k >> 1) & 1)) ^ (g4 & 1)) << 4) + 8 * (tp & 1);
    wad[1] = wad[0] ^ 16;
  }
  auto lda = [&](unsigned a, int i) { return *LDS_AT(const s16x8, a + (i >> 1) * 4096 + (i & 1) * 1024); };
  auto ldw_ = [&](int h, int j) {
    const unsigned w = wad[j & 1] + h * 12288 + (j >> 1) * 128;
    s16x4 lo = __builtin_amdgcn_ds_read_tr16_b64_v4i16(LDS_AT(s16x4, w));
    s16x4 h4 = __builtin_amdgcn_ds_read_tr16_b64_v4i16(LDS_AT(s16x4, w + 1536));
    return (s16x8)__builtin_shufflevector(lo, h4, 0, 1, 2, 3, 4, 5, 6, 7);
  };
  ARGS_HERE;
  const bool give = wn != (tmi & 1);
  unsigned lto = lbase + (give ? 2 * KSB : 0) + wm * 4096, lqo = lbase + QOFF + wid * 4096;
  char* qgb = reinterpret_cast<char*>(KV) + QG + (((LAY2 ? 256 : 0) + tmi * 16 + tni) * 2 + wm) * 4096 - lto;
  char* kvb = reinterpret_cast<char*>(KV) + ((tni * 2 + wn) * 64 + tmi * 4 + wm * 2) * 2048 - lto;
  {
    const unsigned ato = lbase + ALPHA + wm * 256, dum = lbase + DUM + lane * 4;
    *LDS_AT(u32x4, lqo + lane * 16) = u32x4{lane >= 16 ? lbase + CRED : lbase + TABA + (wm * 64 + e0) * 16, lbase + TABW + (wn * 32 + e0) * 16,
                                                   ato + (give ? 512 : 0) + e0 * 4, lto + (lane >> 5) * 1024 + ((lane & 28) * 2 + (lane & 3)) * 16};
    *LDS_AT(u32x2, lqo + 2048 + lane * 16) = u32x2{aad[0], aad[1]};
    *LDS_AT(u32x4, lqo + 1024 + lane * 16) = u32x4{ato + g4 * 16 + (lane >> 5) * 32, lto + (lane >> 5) * 1024 + ((lane & 24) * 2 + (lane & 7)) * 16,
                                                          lane == 0 ? lbase + FLG + 32 : dum, lane == 0 ? lbase + FLG + 16 : dum};
  }
  {
    const __amdgpu_buffer_rsrc_t rq = __builtin_amdgcn_make_buffer_rsrc(qgb + lto, 0, give && !LAY2 ? 4096 : 0, 0x00020000);
#pragma unroll
    for (int i = 0; i < 4; ++i) __builtin_amdgcn_raw_buffer_store_b128(u32x4{SENT, SENT, SENT, SENT}, rq, lane * 16 + i * 1024, 0, 16);
  }
  unsigned qmv = lqo - lto;
  asm volatile("" : "+s"(lqo), "+s"(qmv), "+s"(kvb), "+s"(qgb) :: "memory");
#pragma unroll
  for (int i = 0; i < 4; ++i)
#pragma unroll
    for (int j = 0; j < 6; ++j) asm volatile("" : "+v"(acc[i][j]));
  asm volatile("" : "+v"(aad[0]), "+v"(aad[1]), "+v"(wad[0]), "+v"(wad[1]));
  if (LAY2) __builtin_amdgcn_s_barrier();
  __builtin_amdgcn_s_barrier();
  PIN();
  if (wid == 0) STAMP(dk, 1);
  s16x8 wq[4][2];
  wq[0][0] = ldw_(0, 0); af[0][0] = lda(aad[0], 0); wq[0][1] = ldw_(1, 0); af[1][0] = lda(aad[1], 0); wq[1][0] = ldw_(0, 1); wq[1][1] = ldw_(1, 1);
  af[0][1] = lda(aad[0], 1); af[1][1] = lda(aad[1], 1); af[0][2] = lda(aad[0], 2); af[1][2] = lda(aad[1], 2);
  PIN();
  s16x4 wlo;
  auto rw = [&](s16x8& d, int h, int j, int hi, int off = 0) __attribute__((always_inline)) {
    const s16x4 v = __builtin_amdgcn_ds_read_tr16_b64_v4i16(LDS_AT(s16x4, wad[j & 1] + h * 12288 + (j >> 1) * 128 + hi * 1536 + off));
    if (hi) d = (s16x8)__builtin_shufflevector(wlo, v, 0, 1, 2, 3, 4, 5, 6, 7);
    else wlo = v;
  };
  auto at = [&](s16x8& x, int r) __attribute__((always_inline)) {
    if (r == 0) asm volatile("" :: "{v[104:107]}"(x));
    if (r == 1) asm volatile("" :: "{v[108:111]}"(x));
    if (r == 2) asm volatile("" :: "{v[112:115]}"(x));
    if (r == 3) asm volatile("" :: "{v[116:119]}"(x));
    if (r == 4) asm volatile("" :: "{v[120:123]}"(x));
    if (r == 5) asm volatile("" :: "{v[124:127]}"(x));
    if (r == 6) asm volatile("" :: "{v[128:131]}"(x));
    if (r == 7) asm volatile("" :: "{v[132:135]}"(x));
    if (r == 8) asm volatile("" :: "{v[136:139]}"(x));
    if (r == 9) asm volatile("" :: "{v[140:143]}"(x));
    if (r == 10) asm volatile("" :: "{v[144:147]}"(x));
    if (r == 11) asm volatile("" :: "{v[148:151]}"(x));
    if (r == 12) asm volatile("" :: "{v[152:155]}"(x));
    if (r == 13) asm volatile("" :: "{v[156:159]}"(x));
    if (r == 14) asm volatile("" :: "{v[160:163]}"(x));
    if (r == 15) asm volatile("" :: "{v[164:167]}"(x));
  };
  auto mm = [&](int i, int j, int h, int sl, int hw_) __attribute__((always_inline)) {
    s16x8 &a = af[h][i], &w = wq[sl][hw_];
    at(a, h * 4 + i); at(w, 8 + sl * 2 + hw_);
    if (j >= 4) asm volatile("v_mfma_f32_16x16x32_bf16 %0, %1, %2, %0" : "+v"(acc[i][j]) : "v"(a), "v"(w));
    else asm volatile("v_mfma_f32_16x16x32_bf16 %0, %1, %2, %0" : "+v"(acc[i][j]) : "v"(w), "v"(a));
    PIN();
  };
  auto next = [&](int g, int J0, int s0, int s1, int off) __attribute__((always_inline)) {
    if (g == 2 || g == 3) rw(wq[s0][0], 0, J0, g & 1, off);
    if (g == 4 || g == 5) af[g & 1][0] = lda(aad[g & 1] + off, 0);
    if (g == 6 || g == 7) rw(wq[s0][1], 1, J0, g & 1, off);
    if (g >= 8 && g < 12) rw(wq[s1][(g - 8) >> 1], (g - 8) >> 1, J0 + 1, g & 1, off);
    if (g >= 12) af[g & 1][(g - 10) >> 1] = lda(aad[g & 1] + off, (g - 10) >> 1);
  };
  auto third = [&](int p, int J0, int delta) __attribute__((always_inline)) {
    auto mv = [&](unsigned& x) __attribute__((always_inline)) { asm volatile("v_add_u32 %0, %1, %0" : "+v"(x) : "s"(delta)); };
#pragma unroll
    for (int g = 0; g < 16; ++g) {
      const int h = g & 1, i = p == 1 ? (g >> 1) & 3 : g >> 2, t = p == 1 ? g >> 3 : (g >> 1) & 1, j = 2 * p + t, sl = p == 1 ? 2 - 2 * t : p + t;
      mm(i, j, h, sl, h);
      if (p == 0) {
        if (g < 2) af[g][3] = lda(aad[g], 3);
        else if (g < 6) rw(wq[2][(g - 2) >> 1], (g - 2) >> 1, 2, g & 1);
        else if (g < 10) rw(wq[3][(g - 6) >> 1], (g - 6) >> 1, 5, g & 1);
        else if (g < 12) mv(aad[g - 10]);
      }
      if (p == 1) {
        if (g < 4) rw(wq[0][g >> 1], g >> 1, 3, g & 1);
        if (g == 4) mv(wad[1]);
        if (g >= 8 && g < 12) rw(wq[2][(g - 8) >> 1], (g - 8) >> 1, 4, g & 1);
        if (g == 12) mv(wad[0]);
      }
      if (p == 2) {
        if (g == BARG) {
          asm volatile("" ::: "memory");
          PIN();
          __builtin_amdgcn_s_barrier();
          asm volatile("" ::: "memory");
        }
        next(g, J0, 0, 1, 0);
      }
      PIN();
      {
        if ((p == 2 && g == 15) || (p == 0 && g == 7)) __builtin_amdgcn_s_waitcnt(0xc67f);
        if (p == 0 && g == 2) __builtin_amdgcn_s_waitcnt(0xc57f);
        if ((p == 0 && g == 14) || (p == 1 && (g & 7) == 7)) __builtin_amdgcn_s_waitcnt(0xc07f);
        PIN();
      }
    }
  };
  third(0, 0, STB); third(1, 0, STB);
  {
    static_assert((NKT - NST - 1) % NST == 0, "");
#pragma unroll 1
    for (int kt = 0; kt < NKT - NST - 1; kt += NST) {
#pragma unroll
      for (int u = 0; u < NST; ++u) {
        const int delta = u == 1 ? -(NST - 1) * STB : STB;
        third(2, 0, 0); third(0, 0, delta); third(1, 0, delta);
      }
      asm volatile("s_waitcnt vmcnt(0)" ::: "memory");
    }
  }
  static_assert(NKT % NST == 1, "slots of the last stages");
#define TT(i)
  auto bar = [&]() { PIN(); __builtin_amdgcn_s_barrier(); asm volatile("" ::: "memory"); PIN(); };
  TT(0);
  third(2, 2, 0);
  const __amdgpu_buffer_rsrc_t rsk = __builtin_amdgcn_make_buffer_rsrc(give ? qgb : kvb, 0, 0x7fffffff, 0x00020000), rsv = __builtin_amdgcn_make_buffer_rsrc(kvb + VOFF, 0, 0x7fffffff, 0x00020000);
  const __amdgpu_buffer_rsrc_t rsq = __builtin_amdgcn_make_buffer_rsrc(kvb - qmv, 0, give ? 0x7fffffff : 0, 0x00020000);
  unsigned pk;
  u32x2 t0, t1, t2, t3;
  auto park = [&](int i) { return *LDS_AT(const volatile u32x2, pk + (i >> 1) * 1024 + (i & 1) * 8); };
  float al[4];
  f32x4 av[4];
  u32x4 kvp[12];
  auto cacc = [&](int p, int x, int t) -> f32x4& { return p < 2 ? acc[x][2 * p + t] : acc[(x >> 1) * 2 + t][4 + (x & 1)]; };
  auto signal = [&](unsigned v) { __hip_atomic_fetch_add(LDS_AT(unsigned, t3[0]), v, __ATOMIC_RELAXED, __HIP_MEMORY_SCOPE_WORKGROUP); PIN(); };
  auto outop = [&](int p, int k) {
    if (k >= 0 && k < 16) {
      const int x = k >> 2, t = (k >> 1) & 1;
      f32x4& c = cacc(p, x, t);
#pragma unroll
      for (int o = 2 * (k & 1); o < 2 * (k & 1) + 2; ++o) {
        float r;
        asm volatile("v_mul_f32 %0, %1, %2" : "=v"(r) : "v"(c[o]), "v"(p < 2 ? al[x] : av[(x >> 1) * 2 + t][o]));
        c[o] = r;
      }
      PIN();
    }
    if (k >= 1 && k <= 16) {
      const int x = (k - 1) >> 2, t = ((k - 1) >> 1) & 1, o = (k - 1) & 1;
      kvp[p * 4 + x][2 * t + o] = pack2(cacc(p, x, t)[2 * o], cacc(p, x, t)[2 * o + 1]);
      PIN();
      if (t && o) {
        if (p) __builtin_amdgcn_raw_buffer_store_b128(kvp[p * 4 + x], p == 1 ? rsk : rsv, (p == 1 ? t1[1] : t2[1]) + (x >> 1) * 2048 + (x & 1) * (p == 1 ? 64 : 128), 0, 16);
        else {
          __builtin_amdgcn_raw_buffer_store_b128(kvp[x], rsq, t1[1] + (x >> 1) * 2048 + (x & 1) * 64, 0, 16);
          *LDS_AT(u32x4, t1[1] + (x >> 1) * 2048 + (x & 1) * 64) = kvp[x];
        }
        PIN();
      }
    }
  };
  static_for<0, 168>([&](auto nc) __attribute__((always_inline)) {
    constexpr int n = decltype(nc)::value, sec = n < 80 ? n >> 4 : (n < 104 ? 5 : 6 + ((n - 104) >> 4)), g = n < 104 ? n - 16 * sec : (n - 104) & 15;
    auto taba = [&](int i) { af[0][i] = *LDS_AT(const s16x8, t0[0] + (i >> 1) * 512 + (i & 1) * 128); };
    auto tabw = [&](int j) { wq[(2 + (j >> 1)) & 3][j & 1] = *LDS_AT(const s16x8, t0[1] + ((j >> 1) * 64 + (j & 1) * 8) * 16); };
    auto rwn = [&](int e, int sl, int j, int off) { if (e >= 0 && e < 4) rw(wq[sl][e >> 1], e >> 1, j, e & 1, off); };
    if (g == 0) TT(1 + sec);
    if (sec == 5) {
      const int i = (g / 12) * 2 + (g & 1), j = (g % 12) >> 1;
      mm(i, j, 0, (2 + (j >> 1)) & 3, j & 1);
    } else {
      constexpr int part[10] = {1, 2, 1, 2, 0, 0, 1, 2, 0, 0}, s0[10] = {0, 2, 0, 2, 0, 0, 1, 3, 1, 3};
      const int h = g & 1, i = sec == 7 ? (g >> 3) * 2 + ((g >> 1) & 1) : g >> 2, t = sec == 7 ? (g >> 2) & 1 : (g >> 1) & 1;
      mm(i, 2 * part[sec] + t, h, (s0[sec] + t) & 3, h);
    }
    if (sec == 0 || sec == 2) {
      if (g < 2) af[g][3] = lda(aad[g] + sec / 2 * STB, 3);
      rwn(g - 2, 2, 4, sec / 2 * STB); rwn(g - 6, 3, 5, sec / 2 * STB);
    }
    if (sec == 1) {
      if (g == 1) bar();
      next(g, 2, 0, 1, STB);
    }
    if ((sec == 3 && g == 12) || (sec == 6 && g == 13)) { pk = lqo + fresh_lane() * 16; asm volatile("" : "+v"(pk)); }
    if (sec == 3) {
      rwn(g, 0, 0, STB); rwn(g - 4, 1, 1, STB);
      if (g >= 8 && g < 12) asm volatile("v_subrev_u32 %0, %1, %0" : "+v"(g < 10 ? aad[g & 1] : wad[g & 1]) : "s"(STB));
      if (g == 13) t1 = park(1);
      if (g == 14) t0 = park(0);
    }
    if (sec == 4) {
      if (g == B15) bar();
      if (g == 4) taba(0);
      if (g == 5 || g == 6) tabw(g - 5);
      if (g == 7) taba(1);
      if (g == 8 || g == 9) tabw(g - 6);
      if (g == 10 || g == 11) af[1][g - 10] = lda(aad[1], g - 10);
      if (g == 12) taba(2);
      if (g == 15) taba(3);
    }
    if (sec == 5) {
      if (g < 2) tabw(4 + g);
      rwn(g - 2, 1, 2, 0);
      if (g == 12 || g == 13) af[0][g - 12] = lda(aad[0], g - 12);
      if (g == 22) { al[0] = *LDS_AT(const float, t1[0]); al[1] = *LDS_AT(const float, t1[0] + 32); }
      rwn(g - 16, 2, 3, 0);
      if (g == 20 || g == 21) af[1][g - 18] = lda(aad[1], g - 18);
    }
    if (sec == 6) {
      if (g < 2) af[0][2 + g] = lda(aad[0], 2 + g);
      rwn(g - 2, 3, 4, 0);
      if (g == 9) { al[2] = *LDS_AT(const float, t1[0] + 128); al[3] = *LDS_AT(const float, t1[0] + 160); }
      if (g == 14) t2 = park(2);
      if ((g & 3) >= 2) rwn((g - 10) / 4 * 2 + (g & 1) - (g < 10) * 8, 0, 5, 0);
    }
    if (sec == 7) {
      if (g == 2 || g == 3 || g == 10 || g == 11) av[(g >> 3) * 2 + (g & 1)] = *LDS_AT(const f32x4, t2[0] + ((g >> 3) * 32 + (g & 1) * 8) * 4);
      rwn(g < 2 ? g : (g < 4 ? -1 : g - 2), 1, 0, 0); rwn(g - 6, 2, 1, 0);
      if (g == 7) asm volatile("v_xor_b32 %0, 0x200, %0" : "+v"(t1[0]));
      if (g == 14) {
#pragma unroll
        for (int x = 0; x < 4; ++x) al[x] = *LDS_AT(const float, t1[0] + (x >> 1) * 128 + (x & 1) * 32);
      }
      if (g == 12) t3 = park(3);
      if (g == 13) { const u32x2 a = park(4); aad[0] = a[0]; aad[1] = a[1]; }
      if (g == 15) {
        asm volatile("" ::: "memory");
        __builtin_amdgcn_s_waitcnt(0xc07f);
        bar();
      }
    }
    if (sec == 8) next(g, 0, 3, 0, STB);
    if (sec == 9) {
      if (g < 2) af[g][3] = lda(aad[g] + STB, 3);
      if (g == 2) signal(0x10000u);
    }
    PIN();
    outop(1, n - 104 - KO);
    outop(2, n - 120 - KO);
    {
      constexpr int wg[8] = WGAPS;
#pragma unroll
      for (int e = 0; e < 8; ++e)
        if (n - 136 == wg[e]) {
          *LDS_AT(u32x4, (e < 4 ? t1[1] + (e & 1) * 64 : t2[1] + 2048 + (e & 1) * 128) + ((e >> 1) & 1) * KSB) = kvp[4 + e];
          PIN();
        }
    }
    if (n == 152 + 3) { asm volatile("v_add_u32 %0, %1, %0" : "+v"(t1[1]) : "s"(qmv)); PIN(); }
    outop(0, n - 152 - KO);
  });
  TT(13);
  if (wid == 0) STAMP(dk, 18);
#pragma unroll
  for (int k = 16 - KO; k <= 16; ++k) outop(0, k);
  __hip_atomic_fetch_add(LDS_AT(unsigned, t3[1]), 1u, __ATOMIC_RELAXED, __HIP_MEMORY_SCOPE_WORKGROUP);
  TT(14);
  asm volatile("s_waitcnt vmcnt(4)" ::: "memory");
  signal(give ? 0u : 1u);
  TT(15);
  if (wid == 0) STAMP(dk, 19);
  }
  PIN();
  {
  const int lane = fresh_lane(), tid = wid * 64 + lane;
  LAYER_HERE;
  const int dk = LAY2 ? 6 : 2;
  const int g = (wid >> 1) & 1, hh = tmi & 1, qg = wid & 1, far = wid >> 2;
  const int head = tni * 2 + hh, qt = (tmi ^ far) * 4 + qg * 2;
  unsigned qaa = lbase + QOFF + (hh * 2 + qg) * 4096 + kpos(lane) * 16 + (g ? 0 : 2048), qab = qaa ^ 2048;
  const unsigned wbase = lbase + g * 4096 + lane * 16;
  auto sptr = [&](int sl) { unsigned a = wbase + sl * KSB; asm volatile("" : "+v"(a)); return LDS_AT(const s16x8, a); };
  auto rd = [&](__attribute__((address_space(3))) const s16x8* sp, int off) { return sp[off >> 4]; };
  auto sl0 = sptr(0);
  const unsigned arr_a = lane == 0 ? lbase + FLG + 16 : lbase + DUM + lane * 4;
  auto arrive = [&]() { __hip_atomic_fetch_add(LDS_AT(unsigned, arr_a), 1u, __ATOMIC_RELAXED, __HIP_MEMORY_SCOPE_WORKGROUP); };
  unsigned arr_w = lane == 0 ? lbase + FLG + 8 + (wid >> 2) * 4 : lbase + DUM + lane * 4;
  unsigned arr_i = 1u << (8 * (wid & 3));
  auto arrive_w = [&]() { __hip_atomic_fetch_add(LDS_AT(unsigned, arr_w), arr_i, __ATOMIC_RELAXED, __HIP_MEMORY_SCOPE_WORKGROUP); };
  s16x8 sel;
#pragma unroll
  for (int e = 0; e < 8; ++e) sel[e] = ((lane & 7) == (e & 4)) ? (short)0x3F80 : (short)0;
  int sidx = 0xDDDDDDDD;
  f32x16 oa, ob, nn, sa, sb;
  f32x4 la = {0.f, 0.f, 0.f, 0.f}, lb = {0.f, 0.f, 0.f, 0.f};
#pragma unroll
  for (int e = 0; e < 16; ++e) { oa[e] = 0.f; ob[e] = 0.f; sa[e] = 0.f; sb[e] = 0.f; }
  asm volatile("" : "+v"(sidx), "+v"(sel), "+v"(oa), "+v"(ob), "+v"(la), "+v"(lb), "+v"(qaa), "+v"(qab), "+v"(arr_w), "+v"(arr_i));
  PIN();
  s16x8 qa0, qa1, qb0, qb1, k00, k01, v00, v01, v10, v11, k10, k11;
  asm volatile("" : "=v"(qa0), "=v"(qa1), "=v"(qb0), "=v"(qb1));
  u32x4 fw;
  auto first = [&]() {
    fw = lds_rd4(lbase + FLG + 16);
    asm volatile("" ::: "memory");
    k00 = rd(sl0, 0); k01 = rd(sl0, 1024);
    if (!far) { qa0 = *LDS_AT(const s16x8, qaa); qb0 = *LDS_AT(const s16x8, qab); qa1 = *LDS_AT(const s16x8, qaa + 1024); qb1 = *LDS_AT(const s16x8, qab + 1024); }
    v00 = rd(sl0, 2048); v01 = rd(sl0, 3072);
    PIN();
  };
  if (far) {
    ArgP ap_ = ap; asm volatile("" : "+s"(ap_));
    const __amdgpu_buffer_rsrc_t rq = __builtin_amdgcn_make_buffer_rsrc(reinterpret_cast<char*>(ap_->kv) + QG + (((LAY2 ? 256 : 0) + (tmi ^ 1) * 16 + tni) * 2 + qg) * 4096, 0, 4096, 0x00020000);
    const int o = kpos(lane) * 16 + (g ? 0 : 2048);
    __builtin_amdgcn_s_sleep(QPRE);
#pragma unroll 1
    for (int i = 0; i < SPIN_MAX; ++i) {
      const u32x4 a0 = __builtin_amdgcn_raw_buffer_load_b128(rq, o, 0, 16), b0 = __builtin_amdgcn_raw_buffer_load_b128(rq, o ^ 2048, 0, 16);
      const u32x4 a1 = __builtin_amdgcn_raw_buffer_load_b128(rq, o + 1024, 0, 16), b1 = __builtin_amdgcn_raw_buffer_load_b128(rq, (o ^ 2048) + 1024, 0, 16);
      qa0 = __builtin_bit_cast(s16x8, a0); qb0 = __builtin_bit_cast(s16x8, b0); qa1 = __builtin_bit_cast(s16x8, a1); qb1 = __builtin_bit_cast(s16x8, b1);
      unsigned mx = 0u;
#pragma unroll
      for (int e = 0; e < 4; ++e) mx = umax(umax(umax(mx, a0[e]), b0[e]), umax(a1[e], b1[e]));
      if (none(mx == SENT)) break;
      __builtin_amdgcn_s_sleep(QSLP);
      asm volatile("" ::: "memory");
    }
  }
  first();
  if (__builtin_amdgcn_readfirstlane(fw[0]) < 4u) {
#pragma unroll 1
    for (int i = 0; i < SPIN_MAX; ++i) {
      if (__builtin_amdgcn_readfirstlane(*LDS_AT(const volatile unsigned, lbase + FLG + 16)) >= 4u) break;
      __builtin_amdgcn_s_sleep(ASLP);
    }
    asm volatile("" ::: "memory");
    first();
  }
  const unsigned tag = __builtin_amdgcn_readfirstlane(fw[1]);
  const int idx = LAY2 ? (2047 - (int)(__builtin_amdgcn_readfirstlane(fw[3]) & 0xffffu)) & 2047 : -64;
  float ma;
  const float zoff = __uint_as_float(__builtin_amdgcn_readfirstlane(LAY2 && g == ((idx >> 6) & 1) ? 0u : 0xff800000u));
  {
    sa = MFMA32(k00, qa0, sa); sb = MFMA32(k00, qb0, sb);
    sa = MFMA32(k01, qa1, sa); sb = MFMA32(k01, qb1, sb);
    float mb = sb[0];
    ma = sa[0];
#pragma unroll
    for (int e = 1; e < 16; ++e) { ma = fmaxf(ma, sa[e]); mb = fmaxf(mb, sb[e]); }
    ma = fmaxf(ma, mb);
    const auto sw = __builtin_amdgcn_permlane32_swap(__float_as_uint(ma), __float_as_uint(ma), false, false);
    ma = fmaxf(__uint_as_float(sw[0]), __uint_as_float(sw[1]));
    f32x2 n2 = {-ma, -ma};
    asm volatile("" : "+v"(n2));
#pragma unroll
    for (int e = 0; e < 16; ++e) nn[e] = n2[e & 1];
    sa = MFMA32(k00, qa0, nn); sa = MFMA32(k01, qa1, sa);
    la[0] = lb[0] = -__uint_as_float(__float_as_uint(__builtin_amdgcn_exp2f(zoff - ma)) & 0xffff0000u);
  }
#define SB() __builtin_amdgcn_sched_barrier(0)
#define EX(i) sc[i] = __builtin_amdgcn_exp2f(sc[i])
  auto unit = [&](f32x16& sc, f32x16& sn, const s16x8& kn0, const s16x8& kn1, const s16x8& qn0, const s16x8& qn1,
                  f32x16& op, f32x4& lp, const s16x8& vp,
                  f32x16& oc, f32x4& lc, const s16x8& vc, auto&& mid) {
    lp = SMF16(sel, PHI(sn), lp); SB();
    op = SMF32(vp, PHI(sn), op); SB();
    EX(0); EX(1); EX(2); EX(3); SB();
    sn = MFMA32(kn0, qn0, nn); SB();
    EX(4); EX(5); EX(6); EX(7); SB();
    sn = MFMA32(kn1, qn1, sn); SB();
    mid();
    EX(8); EX(9); EX(10); EX(11); SB();
    lc = SMF16(sel, PLO(sc), lc); SB();
    oc = SMF32(vc, PLO(sc), oc); SB();
    EX(12); EX(13); EX(14); EX(15); SB();
    asm volatile("" : "+v"(sc));
  };
  sl0 = sptr(1);
  unsigned f0 = 0u;
  auto check = [&](unsigned need) {
    if (__builtin_expect(__builtin_amdgcn_readfirstlane(f0) < need, 0)) {
#pragma unroll 1
      for (int i = 0; i < SPIN_MAX; ++i) {
        if (__builtin_amdgcn_readfirstlane(*LDS_AT(const volatile unsigned, lbase + FLG)) >= need) break;
        __builtin_amdgcn_s_sleep(1);
      }
    }
    asm volatile("" ::: "memory");
  };
#define PAT 0x22221110
#define SAW8(i) do { if (((PAT >> (28 - 4 * (i))) & 3) != ((PAT >> ((32 - 4 * (i)) & 31)) & 3)) { __builtin_amdgcn_s_setprio((PAT >> (28 - 4 * (i))) & 3); SB(); } } while (0)
#define SAW(i) do { if (odd) SAW8(4 + (i)); else SAW8(i); } while (0)
  auto three = [&](int st, bool odd) {
    SAW(0);
    unit(sa, sb, k00, k01, qb0, qb1, ob, lb, v11, oa, la, v00, [&]() { k10 = rd(sl0, 0); k11 = rd(sl0, 1024); SB(); });
    SAW(1);
    unit(sb, sa, k10, k11, qa0, qa1, oa, la, v01, ob, lb, v00, [&]() { v10 = rd(sl0, 2048); v11 = rd(sl0, 3072); SB(); });
    sl0 = sptr((2 * st + 2) & 7);
    SB();
    SAW(2);
    unit(sa, sb, k10, k11, qb0, qb1, ob, lb, v01, oa, la, v10, [&]() { k00 = rd(sl0, 0); k01 = rd(sl0, 1024); SB(); });
  };
  auto fourth = [&](int st, bool odd) {
    SAW(3);
    unit(sb, sa, k00, k01, qa0, qa1, oa, la, v11, ob, lb, v10, [&]() {
      v00 = rd(sl0, 2048); v01 = rd(sl0, 3072);
      f0 = *LDS_AT(const volatile unsigned, lbase + FLG);
      SB();
      arrive_w();
      SB(); });
    check(st >= 14 ? 32u : 2u * st + 5u);
    sl0 = sptr((2 * st + 3) & 7);
    SB();
  };
  SB();
  SAW8(0);
  {
    f32x16& sc = sa;
    sb = MFMA32(k00, qb0, nn); SB();
    k10 = rd(sl0, 0); k11 = rd(sl0, 1024); SB();
    sb = MFMA32(k01, qb1, sb); SB();
    EX(0); EX(1); EX(2); EX(3); EX(4); EX(5); EX(6); EX(7); EX(8); SB();
    oa = SMF32(v00, PLO(sa), oa); SB();
    EX(9); EX(10); EX(11); SB();
    la = SMF16(sel, PLO(sa), la); SB();
    EX(12); EX(13); EX(14); EX(15); SB();
    asm volatile("" : "+v"(sa));
  }
  auto given = [&]() {
    if (!far && g != hh) {
      asm volatile("s_waitcnt vmcnt(0)" ::: "memory");
      __hip_atomic_fetch_add(LDS_AT(unsigned, arr_w + 24), 256u, __ATOMIC_RELAXED, __HIP_MEMORY_SCOPE_WORKGROUP);
    }
    SB();
  };
  given();
  SAW8(1);
  unit(sb, sa, k10, k11, qa0, qa1, oa, la, v01, ob, lb, v00, [&]() { v10 = rd(sl0, 2048); v11 = rd(sl0, 3072); SB(); });
  sl0 = sptr(2);
  SB();
  SAW8(2);
  unit(sa, sb, k10, k11, qb0, qb1, ob, lb, v01, oa, la, v10, [&]() {});
  {
    f32x16& sc = sb;
    SAW8(3);
    la = SMF16(sel, PHI(sa), la); SB();
    oa = SMF32(v11, PHI(sa), oa); SB();
    EX(0); EX(1); EX(2); EX(3); EX(4); EX(5); EX(6); EX(7); EX(8); asm volatile("" : "+v"(sc)); SB();
    lb = SMF16(sel, PLO(sb), lb); SB();
    ob = SMF32(v10, PLO(sb), ob); SB();
    f0 = *LDS_AT(const volatile unsigned, lbase + FLG);
    asm volatile("" ::: "memory");
    k00 = rd(sl0, 0); k01 = rd(sl0, 1024); SB();
    EX(9); EX(10); EX(11); EX(12); EX(13); EX(14); EX(15); asm volatile("" : "+v"(sc)); SB();
    if (__builtin_amdgcn_readfirstlane(f0) < 3u) { check(3u); k00 = rd(sl0, 0); k01 = rd(sl0, 1024); }
    SB();
    v00 = rd(sl0, 2048); v01 = rd(sl0, 3072); SB(); arrive_w(); SB();
    sa = MFMA32(k00, qa0, nn); sa = MFMA32(k01, qa1, sa); asm volatile("" : "+v"(sa)); SB();
  }
  sl0 = sptr(3);
  SB();
  SAW8(4);
  unit(sa, sb, k00, k01, qb0, qb1, ob, lb, v11, oa, la, v00, [&]() {
    f0 = *LDS_AT(const volatile unsigned, lbase + FLG);
    asm volatile("" ::: "memory");
    k10 = rd(sl0, 0); k11 = rd(sl0, 1024); SB(); });
  if (__builtin_amdgcn_readfirstlane(f0) < 4u) { check(4u); k10 = rd(sl0, 0); k11 = rd(sl0, 1024); }
  SB();
  SAW8(5);
  unit(sb, sa, k10, k11, qa0, qa1, oa, la, v01, ob, lb, v00, [&]() { v10 = rd(sl0, 2048); v11 = rd(sl0, 3072); f0 = *LDS_AT(const volatile unsigned, lbase + FLG); SB(); });
  check(5u);
  sl0 = sptr(4);
  SB();
  SAW8(6);
  unit(sa, sb, k10, k11, qb0, qb1, ob, lb, v01, oa, la, v10, [&]() { k00 = rd(sl0, 0); k01 = rd(sl0, 1024); SB(); });
#pragma unroll 1
  for (int st = 2; st < 16; st += 2) { fourth(st - 1, true); three(st, false); fourth(st, false); three(st + 1, true); }
  SAW8(7);
  unsigned vr, tk, tv;
  unsigned f4;
  auto table = [&]() {
    f4 = *LDS_AT(const volatile unsigned, lbase + FLG + 4);
    k00 = *LDS_AT(const volatile s16x8, tk); k01 = *LDS_AT(const volatile s16x8, tk + 32);
    vr = *LDS_AT(const volatile u16, tv);
  };
  unsigned xw, xm;
  u32x2 ml;
  u32x4 o2[4];
  auto take = [&]() {
    ml = *LDS_AT(const volatile u32x2, xm);
#pragma unroll
    for (int e = 0; e < 4; ++e) o2[e] = lds_rd4(xw + e * 1024);
  };
  float pzt;
  int dso;
  f32x16& sc = sb;
#define EXP() do { asm volatile("" : "+v"(sc)); SB(); } while (0)
  oa = SMF32(v11, PHI(sa), oa); SB();
  tk = lbase + TAB + 128 + hh * 64 + (lane >> 5) * 16; tv = lbase + TAB + 256 + hh * 64 + (lane & 31) * 2;
  table();
  SB();
  EX(0); EX(1); EXP();
  la = SMF16(sel, PHI(sa), la); SB();
  EX(2); EX(3); EX(4); EXP();
  xw = wbase + (qg ? QOFF : 4 * ASB) + far * 8192; xm = lbase + XML + wid * 512 + lane * 8;
  pzt = __builtin_amdgcn_exp2f(nn[0] + (zoff - 8.f));
  asm volatile("" : "+v"(xw), "+v"(xm), "+v"(pzt), "+v"(f4));
  SB();
  {
    const unsigned ph = LAY2 ? 0x79797979u : 0x80808080u;
    auto ready = [&]() { return ((__builtin_amdgcn_readfirstlane(f4) + ph) & 0x80808080u) == 0x80808080u; };
    if (__builtin_expect(!ready(), 0)) {
#pragma unroll 1
      for (int i = 0; i < SPIN_MAX; ++i) {
        __builtin_amdgcn_s_sleep(1);
        table();
        if (ready()) break;
      }
    }
    asm volatile("" ::: "memory");
  }
  SB();
  sa = MFMA32(k00, qb0, nn); asm volatile("" : "+v"(sa)); SB();
#pragma unroll
  for (int e = 0; e < 4; ++e) *LDS_AT(volatile f32x4, xw + e * 1024) = f32x4{oa[4 * e], oa[4 * e + 1], oa[4 * e + 2], oa[4 * e + 3]};
  *LDS_AT(volatile u32x2, xm) = u32x2{__float_as_uint(ma), f2word(pzt <= la[0] ? la[0] : 0.f)};
  SB();
  EX(5); EX(6); EX(7); EXP();
  sa = MFMA32(k01, qb1, sa); asm volatile("" : "+v"(sa), "+v"(vr)); SB();
  ARGS_HERE;
  EX(8); EX(9); EX(10); EXP();
  ob = SMF32(v10, PLO(sb), ob); SB();
  EX(11); EX(12); EX(13); EXP();
  lb = SMF16(sel, PLO(sb), lb); SB();
  const bool used = !LAY2 || (qt + g) * 32 + (lane & 31) != idx;
  dso = used ? (((qt + g) * 32 + head) * 1024 + lane * 8) * 2 : S_ * D_ * 2;
  xw += g ? -4096 : 4096; xm += g ? -1024 : 1024;
  asm volatile("" : "+v"(dso), "+v"(xw), "+v"(xm));
  SB();
  EX(14); EX(15); EXP();
  take();
  SB();
  lb = SMF16(sel, PHI(sb), lb); SB();
  ob = SMF32(v11, PHI(sb), ob); SB();
  __builtin_amdgcn_s_setprio(EPRIO);
  TE(0, 0); TE(1, 0); TE(2, 0); TE(3, 0); TE(7, 0);
  float lsb = lb[0];
  if (LAY2) {
    sa[0] = __builtin_amdgcn_exp2f(sa[0]);
    ob = SMF32((u32x4{lane < 32 ? vr : 0u, 0u, 0u, 0u}), PLO(sa), ob);
    lsb += __uint_as_float(__float_as_uint(sa[0]) & 0xffff0000u);
    lsb = pzt <= lsb ? lsb : 0.f;
  }
  const __amdgpu_buffer_rsrc_t rso = __builtin_amdgcn_make_buffer_rsrc(Of, 0, S_ * D_ * 2, 0x00020000);
  TE(0, 1);
  if (__builtin_amdgcn_ballot_w64(ml[1] == SENT) != 0ull) {
#pragma unroll 1
    for (int i = 0; i < SPIN_MAX; ++i) {
      __builtin_amdgcn_s_sleep(XSLP);
      if (!__builtin_amdgcn_ballot_w64(*LDS_AT(const volatile unsigned, xm + 4) == SENT)) break;
    }
    asm volatile("s_waitcnt lgkmcnt(0)" ::: "memory");
    take();
  }
  arrive();
  TE(0, 2);
  TE(0, 3); TE(1, 1);
  {
    const float m2 = __uint_as_float(ml[0]), l2 = __uint_as_float(ml[1]);
    const float mx = fmaxf(ma, m2);
    const float f1 = __builtin_amdgcn_exp2f(ma - mx), f2 = __builtin_amdgcn_exp2f(m2 - mx);
    const float inv = frcp(lsb * f1 + l2 * f2);
    const float c1 = f1 * inv, c2 = f2 * inv;
    f32x2 r[8];
#pragma unroll
    for (int e = 0; e < 8; ++e) {
      const unsigned x0 = o2[e >> 1][(e & 1) * 2], x1 = o2[e >> 1][(e & 1) * 2 + 1];
      r[e] = f32x2{__uint_as_float(x0) * c2, __uint_as_float(x1) * c2};
    }
#pragma unroll
    for (int e = 0; e < 8; ++e) r[e] = f32x2{ob[2 * e], ob[2 * e + 1]} * f32x2{c1, c1} + r[e];
    auto put = [&]() {
#pragma unroll
      for (int sx = 0; sx < 2; ++sx)
        __builtin_amdgcn_raw_buffer_store_b128(u32x4{pack2(r[4 * sx][0], r[4 * sx][1]), pack2(r[4 * sx + 1][0], r[4 * sx + 1][1]),
            pack2(r[4 * sx + 2][0], r[4 * sx + 2][1]), pack2(r[4 * sx + 3][0], r[4 * sx + 3][1])}, rso, dso + sx * 1024, 0, 16);
    };
    put();
    PIN();
    __builtin_amdgcn_s_setprio(0);
    PIN();
    f32x2 chk = {lsb - __builtin_amdgcn_fmed3f(lsb, 8.6736174e-19f, 1.2676506e30f), l2 - __builtin_amdgcn_fmed3f(l2, 8.6736174e-19f, 1.2676506e30f)};
#pragma unroll
    for (int e = 0; e < 8; ++e) chk = r[e] * f32x2{0.f, 0.f} + chk;
    if (__builtin_expect(__builtin_amdgcn_ballot_w64(used && !(chk[0] + chk[1] == 0.f)) != 0ull, 0)) {
      float l;
      attn_safe(KV, head * 64, qb0, qb1, ob, l, LAY2 ? head * 64 + (idx >> 5) : -1, idx & 31, lbase + TAB + 128 + hh * 64);
      const float inv2 = 1.0f / l;
#pragma unroll
      for (int e = 0; e < 8; ++e) r[e] = f32x2{ob[2 * e] * inv2, ob[2 * e + 1] * inv2};
      asm volatile("s_waitcnt vmcnt(0)" ::: "memory");
      put();
    }
    unsigned pp = 0u;
    if (LAY2) {
      const unsigned ua = lbase + UGL + hh * 128 + (lane >> 5) * 16;
      f32x2 du = {0.f, 0.f}, dn = {0.f, 0.f};
#pragma unroll
      for (int q = 0; q < 4; ++q) {
        const f32x4 uu = *LDS_AT(const f32x4, ua + q * 32), gg = *LDS_AT(const f32x4, ua + 256 + q * 32);
        du = r[2 * q] * f32x2{uu[0], uu[1]} + du; du = r[2 * q + 1] * f32x2{uu[2], uu[3]} + du;
        const f32x2 t0 = r[2 * q] * f32x2{gg[0], gg[1]}, t1 = r[2 * q + 1] * f32x2{gg[2], gg[3]};
        dn = t0 * t0 + dn; dn = t1 * t1 + dn;
      }
      const auto a = __builtin_amdgcn_permlane32_swap(__float_as_uint(du[0] + du[1]), __float_as_uint(du[0] + du[1]), false, false);
      const auto c = __builtin_amdgcn_permlane32_swap(__float_as_uint(dn[0] + dn[1]), __float_as_uint(dn[0] + dn[1]), false, false);
      pp = f2word(__uint_as_float(pack2(__uint_as_float(a[0]) + __uint_as_float(a[1]), __uint_as_float(c[0]) + __uint_as_float(c[1]))));
    }
    __builtin_amdgcn_raw_buffer_store_b32(pp, __builtin_amdgcn_make_buffer_rsrc(hand + 2 * HAND_N + PP_O, 0, LAY2 ? H_ * S_ * 4 : 0, 0x00020000),
                                          lane < 32 ? (head * S_ + (qt + g) * 32 + lane) * 4 : H_ * S_ * 4, 0, 16);
    PIN();
  }
  TE(1, 2);
  u32x4 xres[2];
  oproj_tail(LAY2, tid, lbase, lbase + FLG, lbase + SPM + 144, omi, oni, idx, gran + GR_O + ((LAY2 ? 16 : 0) + (tmi ^ far)) * (64 * RO) + head,
             gran + GR_X + omi * (128 * RX) + oni * 4, tag, Of, xout, A, w_r, stats_out, stats, hand, xres TEPASS);
  if (LAY2) {
    const int row = omi * 128 + (wid & 3) * 32 + (wid >> 2) * 16 + (lane >> 3), col = oni * 64 + (lane & 7) * 8;
    unsigned md[2], best = 0u;
    const __amdgpu_buffer_rsrc_t rsk = __builtin_amdgcn_make_buffer_rsrc(hand + HAND_N, 0, (HAND_N + PS_N) * 4, 0x00020000);
    f32x2 xf[2][4];
#pragma unroll
    for (int p = 0; p < 2; ++p)
#pragma unroll
      for (int e = 0; e < 4; ++e) {
        xf[p][e] = f32x2{__uint_as_float(xres[p][e] << 16), __uint_as_float(xres[p][e] & 0xffff0000u)};
        asm volatile("" : "+v"(xf[p][e]));
      }
    LOGDEF;
    LOG();
    const bool rtr = (oni & 7) == 7;
    {
      const int rl = (wid & 3) * 32 + (wid >> 2) * 16 + (lane & 15), rr = omi * 128 + rl;
      int src = (HAND_N + ((omi * 16 + (lane >> 4) * 4) * 128 + rl) * 4) * 4;
      int ko = rtr && (wid >> 2) == (oni >> 3) && lane < NREP ? lane * 1024 + (rr >> 4) * 8 : (HAND_N + PS_N) * 4;
      int bp = (lane >> 3) * 4;
      const float wsum = *LDS_AT(const volatile float, lbase + SPM + 132);
      asm volatile("" : "+v"(src), "+v"(ko), "+v"(bp));
      u32x4 v[4];
      __builtin_amdgcn_s_sleep(MPRE);
#pragma unroll 1
      for (int spin = 0; spin < SPIN_MAX; ++spin) {
        LOG();
#pragma unroll
        for (int i = 0; i < 4; ++i) v[i] = __builtin_amdgcn_raw_buffer_load_b128(rsk, src + (i & 1) * 2048, (i >> 1) * 4096, 16);
        unsigned mx = 0u;
#pragma unroll
        for (int i = 0; i < 4; ++i) mx = umax(umax(umax(mx, v[i][0]), v[i][1]), v[i][2]);
        if (none(mx == SENT)) break;
        __builtin_amdgcn_s_sleep(MSLP);
        asm volatile("" ::: "memory");
      }
      LOGAT(12);
      if (rtr) __builtin_amdgcn_s_setprio(3); else __builtin_amdgcn_s_setprio(LNPRIO);
      double sm[3];
#pragma unroll
      for (int c = 0; c < 3; ++c) {
        const double a = ((double)__uint_as_float(v[0][c]) + (double)__uint_as_float(v[1][c])) + ((double)__uint_as_float(v[2][c]) + (double)__uint_as_float(v[3][c]));
        u32x2 q = __builtin_bit_cast(u32x2, a);
        const auto lo = __builtin_amdgcn_permlane32_swap(q[0], q[0], false, false), hi = __builtin_amdgcn_permlane32_swap(q[1], q[1], false, false);
        q = __builtin_bit_cast(u32x2, __builtin_bit_cast(double, u32x2{lo[0], hi[0]}) + __builtin_bit_cast(double, u32x2{lo[1], hi[1]}));
        const auto l2 = __builtin_amdgcn_permlane16_swap(q[0], q[0], false, false), h2 = __builtin_amdgcn_permlane16_swap(q[1], q[1], false, false);
        sm[c] = __builtin_bit_cast(double, u32x2{l2[0], h2[0]}) + __builtin_bit_cast(double, u32x2{l2[1], h2[1]});
      }
      float mean, var, mb, den;
      ln_from_sums(sm[0], sm[1], mean, var, mb, den);
      const unsigned mdr = f2word(__uint_as_float((__float_as_uint(mb) >> 16) | (__float_as_uint(den) & 0xffff0000u)));
      md[0] = __builtin_amdgcn_ds_bpermute(bp, mdr); md[1] = __builtin_amdgcn_ds_bpermute(bp + 32, mdr);
      if (rtr) {
        const float r = (float)fma(-(double)mb, (double)wsum, sm[2]) * frcp(den);
        const unsigned rb = pack2(r, r);
        unsigned rkey = (rb ^ ((unsigned)((int)rb >> 31) | 0x80000000u)) & 0xffff0000u;
        if (r != r) rkey = 0u;
        const unsigned top = wave_umax(rkey | (unsigned)(2047 - rr));
        const unsigned mdt = __builtin_amdgcn_readlane(mdr, (2047 - (int)(top & 0xffffu)) & 15);
        __builtin_amdgcn_raw_buffer_store_b64(u32x2{top, mdt}, rsk, ko, 0, 16);
        PIN();
        __builtin_amdgcn_s_setprio(LNPRIO);
        __hip_atomic_fetch_add(LDS_AT(unsigned, lane == 0 ? lbase + SPM + 128 : lbase + DUM + lane * 4), 1u, __ATOMIC_RELAXED, __HIP_MEMORY_SCOPE_WORKGROUP);
        if (wid == 2 || wid == 5) STAMP(8, 6);
      }
      LOGAT(13);
    }
    auto keys = [&](const u32x4& v) __attribute__((always_inline)) {
      if (none(umax(umax(v[0], v[1]), umax(v[2], v[3])) == SENT)) {
        best = wave_umax(umax(v[0], v[2])) | 0x8000u;
        *LDS_AT(volatile u32x2, lbase + FLG + 40) = u32x2{best, (unsigned)__builtin_amdgcn_readlane(v[0] > v[2] ? v[1] : v[3], ((2047 - (int)(best & 0x7ffu)) >> 5) & 63)};
        STAMP(8, 11);
      }
    };
    auto ln_rows = [&]() __attribute__((always_inline)) {
#pragma unroll
      for (int p = 0; p < 2; ++p) {
        const float mb_ = __uint_as_float(md[p] << 16), rin = frcp(__uint_as_float(md[p] & 0xffff0000u));
#pragma unroll
        for (int e = 0; e < 4; ++e) {
          const f32x2 h = (xf[p][e] - f32x2{mb_, mb_}) * f32x2{rin, rin} + xf[p][e];
          xres[p][e] = pack2(h[0], h[1]);
        }
      }
      asm volatile("" : "+v"(xres[0]), "+v"(xres[1]));
    };
    if (wid == 0) STAMP(8, 12);
    ln_rows();
    LOGAT(14);
    const __amdgpu_buffer_rsrc_t rsu = __builtin_amdgcn_make_buffer_rsrc(out, 0, S_ * D_ * 2, 0x00020000);
    unsigned fr1 = SENT;
#pragma unroll 1
    for (int i = 0; i < 4 * SPIN_MAX && fr1 == SENT; ++i) fr1 = __builtin_amdgcn_readfirstlane(*LDS_AT(const volatile unsigned, lbase + SPM + 144));
    const int fr2 = (fr1 >> 16) & 0x7ffu;
    fr1 &= 0x7ffu;
    unsigned tmx = __builtin_amdgcn_readfirstlane(*LDS_AT(const volatile unsigned, lbase + SPM + 136)) + KDT;
    if (tmx - (unsigned)__builtin_amdgcn_s_memrealtime() > 1024u) tmx = 0u;
    auto due = [&]() { return tmx == 0u || (int)((unsigned)__builtin_amdgcn_s_memrealtime() - tmx) >= 0; };
    lk_set(15);
    const bool held = !none((row | 8) == ((int)fr1 | 8) || (row | 8) == (fr2 | 8));
#pragma unroll
    for (int p = 0; p < 2; ++p) __builtin_amdgcn_raw_buffer_store_b128(xres[p], rsu, row + 8 * p != (int)fr1 && row + 8 * p != fr2 ? ((row + 8 * p) * D_ + col) * 2 : S_ * D_ * 2, 0, OAUX);
    PIN();
    if (wid == 6) {
      if (rtr) {
#pragma unroll 1
        for (int i = 0; i < SPIN_MAX; ++i) {
          if (__builtin_amdgcn_readfirstlane(*LDS_AT(const volatile unsigned, lbase + SPM + 128)) == 7u) break;
          __builtin_amdgcn_s_sleep(1);
        }
        asm volatile("" ::: "memory");
      }
      while (!due()) __builtin_amdgcn_s_sleep(1);
    }
#pragma unroll 1
    for (int i = 0; i < 4 * SPIN_MAX && !best; ++i) {
      if (wid == 6) {
        LOG();
        keys(__builtin_amdgcn_raw_buffer_load_b128(rsk, (bid % NREP) * 1024 + lane * 16, 0, 16));
        if (!best) __builtin_amdgcn_s_sleep(KSLP);
      } else {
        best = __builtin_amdgcn_readfirstlane(*LDS_AT(const volatile unsigned, lbase + FLG + 40));
        if (!best) __builtin_amdgcn_s_sleep(1);
      }
      asm volatile("" ::: "memory");
    }
    const int idx2 = 2047 - (int)(best & 0x7ffu);
    if (held) {
#pragma unroll
      for (int p = 0; p < 2; ++p) {
        const int r = row + 8 * p;
        __builtin_amdgcn_raw_buffer_store_b128(xres[p], rsu, (r == (int)fr1 || r == fr2) && r != idx2 ? (r * D_ + col) * 2 : S_ * D_ * 2, 0, OAUX);
      }
    }
    if (__builtin_expect(idx2 != (int)fr1 && idx2 != fr2, 0) && !none((row | 8) == (idx2 | 8))) {
#pragma unroll
      for (int p = 0; p < 2; ++p)
        __builtin_amdgcn_raw_buffer_store_b128(u32x4{pack2(xf[p][0][0], xf[p][0][1]), pack2(xf[p][1][0], xf[p][1][1]), pack2(xf[p][2][0], xf[p][2][1]), pack2(xf[p][3][0], xf[p][3][1])},
            __builtin_amdgcn_make_buffer_rsrc(xout, 0, 3 * D_ * 2, 0x00020000), row + 8 * p == idx2 ? (2 * D_ + col) * 2 : S_ * D_ * 2, 0, 16);
      asm volatile("s_waitcnt vmcnt(0)" ::: "memory");
      if (lane == 0) st_ag(gran + GR_F + oni * 32, tag);
    }
  }
  }
}
struct Args2 { Args l[2]; };
extern "C" __global__ __launch_bounds__(768, 3) void layers_kernel(const Args2 args) {
  __shared__ __attribute__((aligned(128))) char smem[163840];
  const unsigned long long t_entry = 0;
  const int tid = threadIdx.x, bid = blockIdx.x;
  int wid = __builtin_amdgcn_readfirstlane(tid >> 6);
  asm volatile("" : "+s"(wid));
  const ArgP ap = (ArgP)__builtin_amdgcn_kernarg_segment_ptr();
  layer_body(wid, bid, (unsigned)(uintptr_t)smem, ap, t_entry);
  LAYER_HERE;
  if (!LAY2) {
    asm volatile(AGAIN :: "{s[0:1]}"(ap + 1), "{s2}"(bid), "{v0}"(wid * 64) : "s90", "s91", "scc", "memory");
    __builtin_unreachable();
  }
}

extern "C" __global__ void end_marker_kernel() {}

extern "C" void kernel_launch(void* const* d_in, const int* in_sizes, int n_in, void* d_out, int out_size,
                              void* d_ws, size_t ws_size, hipStream_t stream) {
  const u16* x0 = (const u16*)d_in[0];
  const u16* wqkv = (const u16*)d_in[1];
  const u16* wo = (const u16*)d_in[2];
  const u16* w_r = (const u16*)d_in[4];
  const u16* wg = (const u16*)d_in[5];
  const u16* wu = (const u16*)d_in[6];
  const u16* wd = (const u16*)d_in[7];
  u16* out = (u16*)d_out;

  char* ws = (char*)d_ws;
  const size_t MB4 = (size_t)S_ * D_ * 2;
  u16* kv = (u16*)(ws);
  u16* o = (u16*)(ws + 2 * MB4);
  u16* x1 = (u16*)(ws + 3 * MB4);
  unsigned* gran = (unsigned*)(ws + 4 * MB4);
  float* csf = (float*)(gran + GR_F + 16 * 32);
  double* stats = (double*)(csf + 8192);
  unsigned* hand = (unsigned*)(stats + 2 * STAT_N);
  static_assert((GR_F + 16 * 32) % 32 == 0 && (size_t)(GR_F + 16 * 32 + 8192 + 4 * STAT_N + 2 * HAND_N + PS_N) * 4 <= 6u << 20, "");
  u16* x3 = (u16*)(hand + 2 * HAND_N) - 3 * D_;
  u16* wc = (u16*)((((uintptr_t)ws + 4 * MB4 + (6u << 20) + 2047) & ~(uintptr_t)2047) + (((uintptr_t)wqkv + 128) & 2047));
  Args2 a;
  for (int l = 0; l < 2; ++l) {
    Args& r = a.l[l];
    r.lay = l; r.a = l ? x1 : x0; r.kv = kv; r.o = o; r.xo = l ? x3 : x1; r.so = l ? stats + STAT_N : stats;
    r.W = wqkv; r.wc = wc; r.wg = wg; r.wu = wu; r.wd = wd; r.wo = wo; r.w_r = w_r; r.gran = gran; r.csf = csf; r.stats = stats; r.hand = hand; r.out = out;
  }
  layers_kernel<<<256, 768, 0, stream>>>(a);
}
```

```cpp
#include <hip/hip_runtime.h>
#include <hip/hip_bf16.h>
#include <stdint.h>
#include <type_traits>
#include <utility>

typedef unsigned short u16;
typedef short s16x4 __attribute__((ext_vector_type(4)));
typedef short s16x8 __attribute__((ext_vector_type(8)));
typedef float f32x16 __attribute__((ext_vector_type(16)));
typedef float f32x4 __attribute__((ext_vector_type(4)));
typedef unsigned int u32x4 __attribute__((ext_vector_type(4)));
typedef unsigned int u32x2 __attribute__((ext_vector_type(2)));
typedef __bf16 bf16x8 __attribute__((ext_vector_type(8)));
typedef __bf16 bf16x16 __attribute__((ext_vector_type(16)));

__device__ __forceinline__ int fresh_lane() {
  int l;
  asm volatile("v_mbcnt_lo_u32_b32 %0, -1, 0\n\tv_mbcnt_hi_u32_b32 %0, -1, %0" : "=v"(l));
  return l;
}
#define STAMP(k, i)
#define STAMPV(k, i, v)
#define LOGDEF
#define LOG()
#define LOGAT(k)
#define lk_set(k)
#define LOGOUT(slot)
#define TE(grp, k)
#define TEARG
#define TEPASS
constexpr int S_ = 2048, D_ = 1024, H_ = 32, F_ = 3584;

__device__ __forceinline__ int kpos(int r) { return (r & ~12) | ((r & 4) << 1) | ((r & 8) >> 1); }
__device__ __forceinline__ float bf2f(u16 v) { return __uint_as_float(((unsigned)v) << 16); }
__device__ __forceinline__ u16 f2bf(float f) {
  __hip_bfloat16 b = __float2bfloat16(f);
  return *reinterpret_cast<u16*>(&b);
}
typedef float f32x2 __attribute__((ext_vector_type(2)));
typedef __bf16 bf16x2 __attribute__((ext_vector_type(2)));
__device__ __forceinline__ unsigned pack2(float lo, float hi) {
  f32x2 v = {lo, hi};
  return __builtin_bit_cast(unsigned, __builtin_convertvector(v, bf16x2));
}
__device__ __forceinline__ float rbf(float f) { return __uint_as_float(pack2(f, f) << 16); }
__device__ __forceinline__ void rbf2(float& a, float& b) {
  const unsigned p = pack2(a, b);
  a = __uint_as_float(p << 16); b = __uint_as_float(p & 0xffff0000u);
}

__device__ __forceinline__ float fadd(float a, float b) { float r = a + b; asm("" : "+v"(r)); return r; }
template <int CTRL, int RMASK>
__device__ __forceinline__ float dpp_add(float v) {
  const int t = __builtin_amdgcn_update_dpp(0, __float_as_int(v), CTRL, RMASK, 0xf, false);
  return v + __int_as_float(t);
}
__device__ __forceinline__ float wave_sum(float v) {
  v = dpp_add<0x111, 0xf>(v);
  v = dpp_add<0x112, 0xf>(v);
  v = dpp_add<0x114, 0xf>(v);
  v = dpp_add<0x118, 0xf>(v);
  v = dpp_add<0x142, 0xa>(v);
  v = dpp_add<0x143, 0xc>(v);
  return __int_as_float(__builtin_amdgcn_readlane(__float_as_int(v), 63));
}

template <int CTRL, int RMASK>
__device__ __forceinline__ unsigned dpp_umax(unsigned v) {
  const unsigned t = (unsigned)__builtin_amdgcn_update_dpp(0, (int)v, CTRL, RMASK, 0xf, false);
  return v > t ? v : t;
}
__device__ __forceinline__ unsigned wave_umax(unsigned v) {
  v = dpp_umax<0x111, 0xf>(v); v = dpp_umax<0x112, 0xf>(v); v = dpp_umax<0x114, 0xf>(v); v = dpp_umax<0x118, 0xf>(v);
  v = dpp_umax<0x142, 0xa>(v); v = dpp_umax<0x143, 0xc>(v);
  return (unsigned)__builtin_amdgcn_readlane((int)v, 63);
}

#define NREP 32
constexpr unsigned SENT = 0xFFFFFFFFu;
constexpr int GRAN_N = 256 * NREP;
constexpr int Y_N = F_ / 16 * D_;
constexpr int X2_N = D_ / 2;
constexpr int XS_N = 128;
constexpr int P4_N = 3 * D_ * 16;
constexpr int PA_N = H_ * 8 * 64;
constexpr int HAND_N = GRAN_N + Y_N + X2_N + XS_N + P4_N + PA_N;
constexpr int MD_O = 16 * 16 * 128 * 4;
constexpr int PK_O = MD_O;
constexpr int PP_O = PK_O + 128 * NREP;
constexpr int Y3_O = PP_O + H_ * S_;
constexpr int PS_N = Y3_O + Y_N;
constexpr int GR_ROW = 64, GR_COL = GR_ROW + 4 * S_, GR_KV = GR_COL + 16 * 16 * 192;
#define RO 8
#define RK 16
#define RX 1
constexpr int GR_O = GR_KV + 2 * 16 * 32 * RK, GR_X = GR_O + 2 * 16 * 64 * RO, GR_N = GR_X + 16 * 128 * RX;
constexpr unsigned QG = 30u << 20;
constexpr int GR_F = GR_N + 256 * 64;
constexpr int STAT_N = 3 * S_;

__device__ __forceinline__ void st_ag(unsigned* p, unsigned v) { __hip_atomic_store(p, v, __ATOMIC_RELAXED, __HIP_MEMORY_SCOPE_AGENT); }
__device__ __forceinline__ unsigned ld_ag(const unsigned* p) { return __hip_atomic_load(p, __ATOMIC_RELAXED, __HIP_MEMORY_SCOPE_AGENT); }
__device__ __forceinline__ double ld_ag(const double* p) { return __hip_atomic_load(p, __ATOMIC_RELAXED, __HIP_MEMORY_SCOPE_AGENT); }
__device__ __forceinline__ unsigned f2word(float f) { const unsigned u = __float_as_uint(f); return u < 0xFFFFFFFEu ? u : 0xFFFFFFFEu; }
constexpr int SPIN_MAX = 1 << 18;
__device__ __forceinline__ unsigned umax(unsigned a, unsigned b) { return a > b ? a : b; }
__device__ __forceinline__ bool none(bool p) { return __builtin_amdgcn_ballot_w64(p) == 0ull; }

__device__ __forceinline__ void ln_from_sums(double s1, double s2, float& mean, float& var, float& mb, float& den) {
  const double md = s1 * (1.0 / 1024.0);
  const double vd = fma(-md, md, s2 * (1.0 / 1024.0));
  mean = (float)md; var = fmaxf((float)vd, 0.f);
  mb = rbf(mean);
  den = rbf(__builtin_amdgcn_sqrtf(rbf(rbf(var) + 1e-5f)));
}
__device__ __forceinline__ float frcp(float x) { return __builtin_amdgcn_rcpf(x); }
__device__ __forceinline__ void ln_apply2(float& a, float& b, float mb, float den, float rin) {
  a -= mb; b -= mb;
  rbf2(a, b);
  float qa = a * rin, qb = b * rin;
  qa = fmaf(fmaf(-qa, den, a), rin, qa);
  qb = fmaf(fmaf(-qb, den, b), rin, qb);
  rbf2(qa, qb);
  a = qa; b = qb;
}

#define CODE_ADDR(SYM, out64)                                                                                          \
  do {                                                                                                                 \
    unsigned lo_, hi_;                                                                                                 \
    asm volatile("s_getpc_b64 s[90:91]\n\ts_add_u32 s90, s90, " SYM "@rel32@lo+4\n\ts_addc_u32 s91, s91, " SYM          \
                 "@rel32@hi+12\n\ts_mov_b32 %0, s90\n\ts_mov_b32 %1, s91"                                             \
                 : "=s"(lo_), "=s"(hi_) :: "s90", "s91", "scc");                                                        \
    out64 = ((unsigned long long)hi_ << 32) | lo_;                                                                     \
  } while (0)

#define QNX 4
constexpr int QMX = 32 / QNX, QNG = 16 / QNX, QMG = 8 / QNG;

__device__ __forceinline__ unsigned touch(const void* p) { return *reinterpret_cast<const unsigned*>(p); }

__device__ __forceinline__ unsigned touch_code(unsigned long long lo, unsigned long long hi, int pc, int lane) {
  const unsigned long long p = lo + (unsigned)(pc * 8192 + lane * 128);
  return p + 4 <= hi ? *reinterpret_cast<__attribute__((address_space(1))) const unsigned*>(p) : 0u;
}

template <bool SC = false>
__device__ __forceinline__ void glds16s(const void* sbase, unsigned voff, unsigned lds_dst) {
  unsigned keep;
  if (SC)
    asm volatile("s_mov_b32 %0, m0\n\ts_mov_b32 m0, %3\n\ts_nop 2\n\tglobal_load_lds_dwordx4 %1, %2 sc1\n\ts_mov_b32 m0, %0"
                 : "=&s"(keep) : "v"(voff), "s"(sbase), "s"(lds_dst) : "memory");
  else
  asm volatile("s_mov_b32 %0, m0\n\ts_mov_b32 m0, %3\n\ts_nop 2\n\tglobal_load_lds_dwordx4 %1, %2\n\ts_mov_b32 m0, %0"
               : "=&s"(keep) : "v"(voff), "s"(sbase), "s"(lds_dst) : "memory");
}
template <bool SC>
__device__ __forceinline__ void glds16x4(const void* sbase, unsigned v0, unsigned v1, unsigned v2, unsigned v3, unsigned lds_dst) {
  unsigned keep;
  if (SC)
    asm volatile("s_mov_b32 %0, m0\n\ts_mov_b32 m0, %6\n\ts_nop 2\n\tglobal_load_lds_dwordx4 %1, %5 sc1\n\tglobal_load_lds_dwordx4 %2, %5 offset:1024 sc1\n\t"
                 "global_load_lds_dwordx4 %3, %5 offset:2048 sc1\n\tglobal_load_lds_dwordx4 %4, %5 offset:3072 sc1\n\ts_mov_b32 m0, %0"
                 : "=&s"(keep) : "v"(v0), "v"(v1), "v"(v2), "v"(v3), "s"(sbase), "s"(__builtin_amdgcn_readfirstlane(lds_dst)) : "memory");
  else
    asm volatile("s_mov_b32 %0, m0\n\ts_mov_b32 m0, %6\n\ts_nop 2\n\tglobal_load_lds_dwordx4 %1, %5\n\tglobal_load_lds_dwordx4 %2, %5 offset:1024\n\t"
                 "global_load_lds_dwordx4 %3, %5 offset:2048\n\tglobal_load_lds_dwordx4 %4, %5 offset:3072\n\ts_mov_b32 m0, %0"
                 : "=&s"(keep) : "v"(v0), "v"(v1), "v"(v2), "v"(v3), "s"(sbase), "s"(__builtin_amdgcn_readfirstlane(lds_dst)) : "memory");
}
constexpr float QSCALE = 0.17677669529663687f * 1.4426950408889634f;

__device__ __forceinline__ unsigned min4(u32x4 v) { const unsigned a = v[0] < v[1] ? v[0] : v[1], b = v[2] < v[3] ? v[2] : v[3]; return a < b ? a : b; }
#define LDS_AT(T, a) ((__attribute__((address_space(3))) T*)(uintptr_t)(a))
__device__ __forceinline__ u32x4 lds_rd4(unsigned a) { return *LDS_AT(const volatile u32x4, a); }
__device__ __forceinline__ void lds_wr(unsigned a, unsigned v) { *LDS_AT(volatile unsigned, a) = v; }
__device__ __forceinline__ void split3(float v, unsigned& p0, unsigned& p1, unsigned& p2) {
  p0 = pack2(v, v) & 0xffffu; v -= __uint_as_float(p0 << 16);
  p1 = pack2(v, v) & 0xffffu; v -= __uint_as_float(p1 << 16);
  p2 = pack2(v, v) & 0xffffu;
}

#define PIN() __builtin_amdgcn_sched_barrier(0)
template <int B, class F, int... I>
__device__ __forceinline__ void static_for_(F&& f, std::integer_sequence<int, I...>) { (f(std::integral_constant<int, B + I>{}), ...); }
template <int B, int E, class F>
__device__ __forceinline__ void static_for(F&& f) { static_for_<B>(f, std::make_integer_sequence<int, E - B>{}); }

#define TPRE 60
#define TPSL 0
#define FCPRE 44
#define FCSLP 2
#define SIGAT 24
#define XPRIO 1
#define OPD 8
#define LAUX 16
#define CSLP 2
#define EPRIO 3
#define OPRIO 1
#define XSLP 1
#define CSL1 8
#define XAUX 16
__device__ __forceinline__ void oproj_tail(const bool RES2, const int tid, const unsigned lbase, const unsigned flg, const unsigned fcm, const int tmi, const int tni, const int idx,
                                           unsigned* osig, unsigned* xsig, const unsigned tag,
                                           const u16* A, u16* out,
                                           const u16* res, const u16* __restrict__ w_r, double* stats,
                                           const double* stats_in, unsigned* hand, u32x4 (&xres)[2] TEARG) {
  constexpr int ldw = D_, PD = OPD;
  const int lane = tid & 63, wid = __builtin_amdgcn_readfirstlane(tid >> 6);
  const int m0 = tmi * 128, n0 = tni * 64;
  const int dk = RES2 ? 7 : 3;
  if (wid == 0) STAMP(dk, 0);
  TE(1, 3);
  asm volatile("" : "+s"(A) :: "memory");
  PIN();
  const int row = m0 + (wid & 3) * 32 + (wid >> 2) * 16 + (lane >> 3), col = n0 + (lane & 7) * 8;
  const unsigned tb = lbase + 147456;
  typedef __attribute__((address_space(1))) double* gdp;
  typedef __attribute__((address_space(1))) unsigned* gup;
  unsigned tr = tb + (row - m0) * 128 + (((lane ^ (lane >> 3)) & 7) << 4), oad;
  {
    oad = lane == 0 ? flg + 24 : flg + 64 + lane * 4;
    gup ps = (gup)(osig + lane * 64);
    asm volatile("" : "+v"(oad), "+v"(ps));
    asm volatile("s_waitcnt vmcnt(1)" ::: "memory");
    TE(2, 1);
    const unsigned old = __hip_atomic_fetch_add(LDS_AT(unsigned, oad), wid < 4 ? 0x101u : 0x10001u, __ATOMIC_RELAXED, __HIP_MEMORY_SCOPE_WORKGROUP);
    if (((__builtin_amdgcn_readfirstlane(old) >> (wid < 4 ? 8 : 16)) & 255u) == 3u && lane < RO) __hip_atomic_store(ps, tag, __ATOMIC_RELAXED, __HIP_MEMORY_SCOPE_AGENT);
    PIN();
    TE(2, 2);
    if (wid == 0) STAMP(dk, 12);
  }
  auto announce = [&]() __attribute__((always_inline)) {
    asm volatile("" ::: "memory");
    PIN();
    __builtin_amdgcn_s_barrier();
    PIN();
    if (wid == 0) STAMP(dk, 1);
    TE(2, 3); TE(4, 0); TE(7, 1);
  };
  if (wid < 4) {
    const __amdgpu_buffer_rsrc_t rsa = __builtin_amdgcn_make_buffer_rsrc(const_cast<u16*>(A), 0, S_ * D_ * 2, 0x00020000);
    int sa0 = (tmi * 4 + wid) * 65536, va0 = (lane >> 4) * 512 + (lane & 15) * 16;
    asm volatile("" : "+v"(va0));
    asm volatile("" : "+s"(sa0));
    auto lda = [&](int i, int h) { return __builtin_amdgcn_raw_buffer_load_b128(rsa, va0 + (i & 1) * 2048 + h * 256, sa0 + (i >> 1) * 4096, LAUX); };
    unsigned wad[4], tw[4];
    f32x4 acc[2][4];
    const int k = (lane >> 5) * 16 + ((lane >> 2) & 7);
#pragma unroll
    for (int j = 0; j < 4; ++j) {
      wad[j] = lbase + k * 128 + (((2 * j) ^ (k & 6)) << 4) + 8 * (lane & 3);
      tw[j] = tb + wid * 4096 + (lane & 15) * 128 + (((2 * j + (lane >> 5)) ^ (lane & 7)) << 4) + ((lane >> 4) & 1) * 8;
      asm volatile("" : "+v"(wad[j]), "+v"(tw[j]));
    }
    auto ldw_ = [&](int i, int j) {
      const unsigned a = wad[j] + (i & 3) * 4096;
      s16x4 lo = __builtin_amdgcn_ds_read_tr16_b64_v4i16(LDS_AT(s16x4, a));
      s16x4 h4 = __builtin_amdgcn_ds_read_tr16_b64_v4i16(LDS_AT(s16x4, a + 1024));
      return (s16x8)__builtin_shufflevector(lo, h4, 0, 1, 2, 3, 4, 5, 6, 7);
    };
    u32x4 id[2];
#pragma unroll
    for (int j = 0; j < 2; ++j) {
#pragma unroll
      for (int e = 0; e < 4; ++e) id[j][e] = (lane >> 4) == 2 * j + ((lane >> 3) & 1) && ((lane & 7) >> 1) == e ? ((lane & 1) ? 0x3f800000u : 0x3f80u) : 0u;
      asm volatile("" : "+v"(id[j]));
    }
    unsigned rad = tb + wid * 4096 + (lane & 15) * 128 + (((lane >> 4) ^ (lane & 7)) << 4), rfa = flg + 36 + wid;
    asm volatile("" : "+v"(rad), "+v"(rfa));
    u32x4 rb[2][2];
    u32x4 ow[2];
#pragma unroll
    for (int j = 0; j < 4; ++j) {
      const u32x2 v = *reinterpret_cast<const u32x2*>(w_r + n0 + j * 16 + (lane >> 4) * 4);
#pragma unroll
      for (int e = 0; e < 2; ++e) ow[j >> 1][(j & 1) * 2 + e] = (lane & 8) ? v[e] : 0x3f803f80u;
    }
    gdp pst = (gdp)(stats + (lane >> 4) % 3 * S_ + m0 + wid * 32 + (lane & 15));
    gup pps = (gup)(hand + 2 * HAND_N + ((tmi * 16 + tni) * 128 + wid * 32 + (lane & 15)) * 4 + (lane >> 4));
    int xo = (row * ldw + col) * 2;
    asm volatile("" : "+v"(tr), "+v"(pst), "+v"(pps), "+v"(xo), "+v"(ow[0]), "+v"(ow[1]));
    announce();
    u32x4 af[PD][2];
    s16x8 wf[2][4];
#pragma unroll
    for (int i = 0; i < PD; i += 2) { af[i][0] = lda(i, 0); af[i + 1][0] = lda(i + 1, 0); af[i][1] = lda(i, 1); af[i + 1][1] = lda(i + 1, 1); }
    PIN();
    __builtin_amdgcn_s_setprio(OPRIO);
#pragma unroll
    for (int q = 0; q < 4; ++q) rb[q >> 1][q & 1] = lds_rd4((rad ^ ((q & 1) * 64)) + (q >> 1) * 2048);
#pragma unroll
    for (int j = 0; j < 4; ++j) wf[j & 1][j >> 1] = ldw_(j & 1, j >> 1);
    PIN();
#pragma unroll
    for (int j = 0; j < 4; ++j)
#pragma unroll
      for (int h = 0; h < 2; ++h) { asm volatile("v_mfma_f32_16x16x32_bf16 %0, %1, %2, 0" : "=&v"(acc[h][j]) : "v"(id[j & 1]), "v"(rb[h][j >> 1])); PIN(); }
#pragma unroll 1
    for (int spin = 0; spin < SPIN_MAX; ++spin)
      if (__builtin_amdgcn_readfirstlane(*LDS_AT(const volatile unsigned, flg)) >= 40u) break;
    asm volatile("" : "+v"(va0) :: "memory");
    PIN();
    s16x4 wlo;
    static_for<0, 16>([&](auto ic) __attribute__((always_inline)) {
      constexpr int i = 2 * decltype(ic)::value;
      if (i == SIGAT) { __hip_atomic_fetch_add(LDS_AT(unsigned, oad + 4), 1u, __ATOMIC_RELAXED, __HIP_MEMORY_SCOPE_WORKGROUP); PIN(); }
#pragma unroll
      for (int g = 0; g < 16; ++g) {
        const int e = g & 1, j = g >> 2, h = (g >> 1) & 1, jn = (j + 2) & 3, in = j < 2 ? i : i + 2;
        asm volatile("v_mfma_f32_16x16x32_bf16 %0, %1, %2, %0" : "+v"(acc[h][j]) : "v"(wf[e][j]), "v"(af[(i + e) % PD][h]));
        PIN();
        if (in == 16 && !(g & 3)) asm volatile("v_add_u32 %0, 0x10000, %0" : "+v"(wad[jn]));
        if (in < 32) {
          const s16x4 v = __builtin_amdgcn_ds_read_tr16_b64_v4i16(LDS_AT(s16x4, wad[jn] + ((in + h) & 15) * 4096 + e * 1024));
          if (e) wf[h][jn] = (s16x8)__builtin_shufflevector(wlo, v, 0, 1, 2, 3, 4, 5, 6, 7);
          else wlo = v;
          PIN();
        }
        if ((g & 3) == 1 && i > 0 && i + PD - 2 < 32) { af[(i - 2 + (j & 1)) % PD][j >> 1] = lda(i + PD - 2 + (j & 1), j >> 1); PIN(); }
        if ((g & 3) == 3 && in < 32 && i != SIGAT) { __builtin_amdgcn_s_waitcnt(0xc47f); PIN(); }
      }
    });
    asm volatile("s_nop 3" : "+v"(acc[0][0]), "+v"(acc[1][0]));
    TE(4, 1); TE(7, 2);
    if (wid == 0) STAMP(dk, 17);
    if (wid == 1 && !RES2) STAMP(4, 0);
    u32x4 pk[2][2];
    float sv[2];
    auto sums = [&]() __attribute__((always_inline)) {
      f32x4 d[2];
      float q[2];
#pragma unroll
      for (int h = 0; h < 2; ++h) {
        d[h] = __builtin_amdgcn_mfma_f32_16x16x32_bf16(__builtin_bit_cast(s16x8, ow[0]), __builtin_bit_cast(s16x8, pk[h][0]), f32x4{0.f, 0.f, 0.f, 0.f}, 0, 0, 0);
        d[h] = __builtin_amdgcn_mfma_f32_16x16x32_bf16(__builtin_bit_cast(s16x8, ow[1]), __builtin_bit_cast(s16x8, pk[h][1]), d[h], 0, 0, 0);
        q[h] = 0.f;
      }
#pragma unroll
      for (int e = 0; e < 16; ++e) {
        const bf16x2 x = __builtin_bit_cast(bf16x2, (unsigned)pk[e & 1][e >> 3][(e >> 1) & 3]);
        q[e & 1] = __builtin_amdgcn_fdot2_f32_bf16(x, x, q[e & 1], false);
      }
      const auto a = __builtin_amdgcn_permlane32_swap(__float_as_uint(q[0]), __float_as_uint(q[1]), false, false);
      const unsigned b = __float_as_uint(__uint_as_float(a[0]) + __uint_as_float(a[1]));
      const auto c = __builtin_amdgcn_permlane16_swap(b, b, false, false);
      const unsigned e = __float_as_uint(__uint_as_float(c[0]) + __uint_as_float(c[1]));
      const auto f = __builtin_amdgcn_permlane32_swap(e, e, false, false);
#pragma unroll
      for (int h = 0; h < 2; ++h) sv[h] = (lane & 16) ? __uint_as_float(f[h]) : d[h][0];
    };
#pragma unroll
    for (int j = 0; j < 4; ++j)
#pragma unroll
      for (int h = 0; h < 2; ++h) {
        pk[h][j >> 1][(j & 1) * 2] = pack2(acc[h][j][0], acc[h][j][1]); pk[h][j >> 1][(j & 1) * 2 + 1] = pack2(acc[h][j][2], acc[h][j][3]);
        *LDS_AT(u32x2, tw[j] + h * 2048) = u32x2{pk[h][j >> 1][(j & 1) * 2], pk[h][j >> 1][(j & 1) * 2 + 1]};
        PIN();
      }
    asm volatile("" ::: "memory");
    *LDS_AT(volatile unsigned char, rfa) = (unsigned char)2;
#pragma unroll
    for (int p = 0; p < 2; ++p) xres[p] = lds_rd4(tr + p * 1024);
    PIN();
    if (!RES2) {
      const __amdgpu_buffer_rsrc_t rso = __builtin_amdgcn_make_buffer_rsrc(out, 0, S_ * D_ * 2, 0x00020000);
      const __amdgpu_buffer_rsrc_t rsf = __builtin_amdgcn_make_buffer_rsrc(xsig + wid, 0, RX * 512, 0x00020000);
      const u32x4 x2 = lds_rd4(tr + 2048), x3 = lds_rd4(tr + 3072);
      PIN();
#pragma unroll
      for (int p = 0; p < 4; ++p) { __builtin_amdgcn_raw_buffer_store_b128(p < 2 ? xres[p] : (p < 3 ? x2 : x3), rso, xo + p * (8 * ldw * 2), 0, XAUX); PIN(); }
      TE(4, 2); TE(5, 0);
      sums();
      if (lane < 48) { __builtin_amdgcn_global_atomic_fadd_f64(pst, (double)sv[0]); __builtin_amdgcn_global_atomic_fadd_f64(pst + 16, (double)sv[1]); }
      PIN();
      TE(4, 3); TE(5, 1);
      asm volatile("s_waitcnt vmcnt(2)" ::: "memory");
      __builtin_amdgcn_raw_buffer_store_b32(tag, rsf, lane * 512 + 256, 0, 16);
      *LDS_AT(volatile unsigned char, rfa) = (unsigned char)3;
      PIN();
      TE(5, 2);
      asm volatile("s_waitcnt vmcnt(1)" ::: "memory");
      __builtin_amdgcn_raw_buffer_store_b32(tag, rsf, lane * 512, 0, 16);
      PIN();
      TE(5, 3);
      __builtin_amdgcn_s_setprio(0);
      return;
    }
    TE(4, 2); TE(5, 0);
    sums();
    __hip_atomic_store(pps, f2word(sv[0]), __ATOMIC_RELAXED, __HIP_MEMORY_SCOPE_AGENT);
    __hip_atomic_store(pps + 64, f2word(sv[1]), __ATOMIC_RELAXED, __HIP_MEMORY_SCOPE_AGENT);
    if (wid == 0) STAMP(dk, 14);
  } else {
    u32x4 rres[4];
    double is1[4], is2[4];
#pragma unroll
    for (int p = 0; p < 4; ++p)
      rres[p] = __builtin_amdgcn_raw_buffer_load_b128(__builtin_amdgcn_make_buffer_rsrc(const_cast<u16*>(res), 0, S_ * D_ * 2, 0x00020000), ((row - 16 + 8 * p) * ldw + col) * 2, 0, 16);
#pragma unroll
    for (int p = 0; p < 4; ++p) { is1[p] = ld_ag(stats_in + row - 16 + 8 * p); is2[p] = ld_ag(stats_in + S_ + row - 16 + 8 * p); }
    PIN();
    if (RES2) {
#pragma unroll
      for (int p = 0; p < 4; ++p) {
        float mean, var, mb, den;
        ln_from_sums(is1[p], is2[p], mean, var, mb, den);
        const float rin = frcp(den);
#pragma unroll
        for (int e = 0; e < 4; ++e) {
          const f32x2 x = {__uint_as_float(rres[p][e] << 16), __uint_as_float(rres[p][e] & 0xffff0000u)};
          const f32x2 h = (x - f32x2{mb, mb}) * f32x2{rin, rin} + x;
          rres[p][e] = pack2(h[0], h[1]);
        }
        asm volatile("" : "+v"(rres[p]));
      }
      PIN();
    }
    if ((idx >> 5) == tmi * 4 + wid - 4) {
      const int dr = idx - (row - 16);
      u32x4 qq;
#pragma unroll 1
      for (int i = 0; i < SPIN_MAX; ++i) {
        qq = __builtin_amdgcn_raw_buffer_load_b128(__builtin_amdgcn_make_buffer_rsrc(hand, 0, HAND_N * 4, 0x00020000), (GRAN_N + Y_N) * 4 + col * 2, 0, 16);
        if (none(umax(umax(qq[0], qq[1]), umax(qq[2], qq[3])) == SENT)) break;
        asm volatile("" ::: "memory");
      }
#pragma unroll
      for (int p = 0; p < 4; ++p)
#pragma unroll
        for (int e = 0; e < 4; ++e) rres[p][e] = dr == 8 * p ? qq[e] : rres[p][e];
    }
#pragma unroll 1
    for (int i = 0; i < 4 * SPIN_MAX; ++i)
      if (__builtin_amdgcn_readfirstlane(*LDS_AT(const volatile unsigned, flg + 16)) >= 12u) break;
#pragma unroll
    for (int p = 0; p < 4; ++p) *LDS_AT(volatile u32x4, tr - 2048 + p * 1024) = rres[p];
    asm volatile("s_waitcnt lgkmcnt(0)" ::: "memory");
    announce();
    TE(4, 1); TE(7, 2);
    asm volatile("s_waitcnt vmcnt(0)" ::: "memory");
    if (!RES2) return;
    __builtin_amdgcn_s_sleep(TPRE);
    unsigned fm = SENT;
#pragma unroll 1
    for (int i = 0; i < 4 * SPIN_MAX; ++i) {
      fm = __builtin_amdgcn_readfirstlane(*LDS_AT(const volatile unsigned, fcm));
      if (fm != SENT) break;
      __builtin_amdgcn_s_sleep(1);
    }
    const unsigned r = (lane & 8) ? fm >> 16 : fm & 0xffffu;
    unsigned fra = tb + (r & 127u) * 128 + (((r ^ lane) & 7u) << 4);
    int fro = (r >> 5) == (unsigned)(tmi * 4 + wid - 4) && lane < 16 ? ((lane >> 3) * ldw + col) * 2 : S_ * D_ * 2;
    asm volatile("" : "+v"(fra), "+v"(fro));
#pragma unroll 1
    for (int i = 0; i < 4 * SPIN_MAX; ++i) {
      if (__builtin_amdgcn_readfirstlane(*LDS_AT(const volatile unsigned char, flg + 32 + wid)) == 2u) break;
      __builtin_amdgcn_s_sleep(TPSL);
    }
    asm volatile("" ::: "memory");
    __builtin_amdgcn_s_setprio(XPRIO);
    PIN();
    TE(4, 2); TE(5, 0);
    const u32x4 xr = lds_rd4(fra);
#pragma unroll
    for (int p = 0; p < 2; ++p) xres[p] = lds_rd4(tr + p * 1024);
    PIN();
    __builtin_amdgcn_raw_buffer_store_b128(xr, __builtin_amdgcn_make_buffer_rsrc(out, 0, 3 * D_ * 2, 0x00020000), fro, 0, XAUX);
  }
  TE(4, 3); TE(7, 3);
  PIN();
  __builtin_amdgcn_s_setprio(0);
  __hip_atomic_fetch_add(LDS_AT(unsigned, oad), 1u, __ATOMIC_RELAXED, __HIP_MEMORY_SCOPE_WORKGROUP);
  TE(5, 1); TE(5, 2);
  PIN();
}

#define MFMA32(a, b, c) __builtin_amdgcn_mfma_f32_32x32x16_bf16(a, b, c, 0, 0, 0)
#define MFMA16(a, b, c) __builtin_amdgcn_mfma_f32_16x16x32_bf16(a, b, c, 0, 0, 0)

#define PLO(x) __builtin_bit_cast(bf16x16, __builtin_shufflevector(x, x, 0, 1, 2, 3, 4, 5, 6, 7))
#define PHI(x) __builtin_bit_cast(bf16x16, __builtin_shufflevector(x, x, 8, 9, 10, 11, 12, 13, 14, 15))
#define SMF32(a, b, c) __builtin_amdgcn_smfmac_f32_32x32x32_bf16(__builtin_bit_cast(bf16x8, a), b, c, sidx, 0, 0)
#define SMF16(a, b, c) __builtin_amdgcn_smfmac_f32_16x16x64_bf16(__builtin_bit_cast(bf16x8, a), b, c, sidx, 0, 0)

__device__ __forceinline__ void patch_kv(unsigned tab, int r, s16x8& k0, s16x8& k1, s16x8& v0, s16x8& v1, bool on = true) {
  const int lane = fresh_lane(), hi = lane >> 5;
  if (on && (lane & 31) == kpos(r)) {
    k0 = *LDS_AT(const s16x8, tab + hi * 16); k1 = *LDS_AT(const s16x8, tab + 32 + hi * 16);
  }
  const short vv = *LDS_AT(const short, tab + 128 + (lane & 31) * 2);
  const int js = (on && hi == ((r >> 2) & 1)) ? (r >> 4) * 8 + ((r >> 3) & 1) * 4 + (r & 3) : -1;
#pragma unroll
  for (int e = 0; e < 8; ++e) { v0[e] = js == e ? vv : v0[e]; v1[e] = js == 8 + e ? vv : v1[e]; }
}

__device__ __forceinline__ void attn_safe(const u16* KV, int tile0, s16x8 q0, s16x8 q1,
                                       f32x16& o, float& l_out, int ptile, int prow, unsigned ptab) {
  const int lane = fresh_lane();
  float m = -INFINITY, l = 0.f;
  const int sidx = 0xDDDDDDDD;
  const __amdgpu_buffer_rsrc_t rs = __builtin_amdgcn_make_buffer_rsrc(const_cast<u16*>(KV), 0, 2 * S_ * D_ * 2, 0x00020000);
#pragma unroll
  for (int e = 0; e < 16; ++e) o[e] = 0.f;
  for (int tt = 0; tt < 64; ++tt) {
    const int t = tile0 + tt, off = t * 2048 + lane * 16;
    s16x8 k0 = __builtin_bit_cast(s16x8, __builtin_amdgcn_raw_buffer_load_b128(rs, off, 0, 16));
    s16x8 k1 = __builtin_bit_cast(s16x8, __builtin_amdgcn_raw_buffer_load_b128(rs, off + 1024, 0, 16));
    s16x8 v0 = __builtin_bit_cast(s16x8, __builtin_amdgcn_raw_buffer_load_b128(rs, off + S_ * D_ * 2, 0, 16));
    s16x8 v1 = __builtin_bit_cast(s16x8, __builtin_amdgcn_raw_buffer_load_b128(rs, off + S_ * D_ * 2 + 1024, 0, 16));
    if (t == ptile) patch_kv(ptab, prow, k0, k1, v0, v1);
    f32x16 sc;
#pragma unroll
    for (int e = 0; e < 16; ++e) sc[e] = 0.f;
    sc = MFMA32(k0, q0, sc); sc = MFMA32(k1, q1, sc);
    float mt = sc[0];
#pragma unroll
    for (int e = 1; e < 16; ++e) mt = fmaxf(mt, sc[e]);
    mt = fmaxf(mt, __shfl_xor(mt, 32, 64));
    const float mn = fmaxf(m, mt);
    const float alpha = __builtin_amdgcn_exp2f(m - mn);
    m = mn;
#pragma unroll
    for (int e = 0; e < 16; ++e) sc[e] = __builtin_amdgcn_exp2f(sc[e] - mn);
    float ls = 0.f;
#pragma unroll
    for (int e = 0; e < 16; ++e) ls += __uint_as_float(__float_as_uint(sc[e]) & 0xffff0000u);
    l = l * alpha + ls;
#pragma unroll
    for (int e = 0; e < 16; ++e) o[e] *= alpha;
    o = SMF32(v0, PLO(sc), o); o = SMF32(v1, PHI(sc), o);
  }
  l_out = l + __shfl_xor(l, 32, 64);
}

__device__ __forceinline__ float wave_fmax(float v) {
  asm volatile("s_nop 1\n\tv_max_f32_dpp %0, %0, %0 row_shr:1 row_mask:0xf bank_mask:0xf\n\ts_nop 1\n\tv_max_f32_dpp %0, %0, %0 row_shr:2 row_mask:0xf bank_mask:0xf\n\t"
               "s_nop 1\n\tv_max_f32_dpp %0, %0, %0 row_shr:4 row_mask:0xf bank_mask:0xf\n\ts_nop 1\n\tv_max_f32_dpp %0, %0, %0 row_shr:8 row_mask:0xf bank_mask:0xf\n\t"
               "s_nop 1\n\tv_max_f32_dpp %0, %0, %0 row_bcast:15 row_mask:0xa bank_mask:0xf\n\ts_nop 1\n\tv_max_f32_dpp %0, %0, %0 row_bcast:31 row_mask:0xc bank_mask:0xf\n\ts_nop 0"
               : "+v"(v));
  return __int_as_float(__builtin_amdgcn_readlane(__float_as_int(v), 63));
}

#define PSLP 8
#define WSLP 3
#define PRE1 8
#define PRE5 8
#define W3SLP 4
#define FSTART 10
#define OAUX 16
#define OMX 4
#define T0 2u
#define TW 10u
#define HW 2
#define HP1 1
#define POLLAT 11
#define X2AT 2
#define HPRIO 3
#define SSLP 1
#define SPRE 6
#define WGAPS {0, 1, 7, 8, 9, 10, 11, 12}
#define KVS 4
#define PNS 12
#define G0 3u
#define GS 2u
#define IAT 1u
#define YSLP 2
#define ISLP 8
#define KSLP 2
#define UAT 13u
#define UHW 0
#define PPSLP 2
#define XRSLP 2
#define MPRE 20
#define YD1 150
#define YD2 180
#define KDT 200
#define XDT 45
#define R2PRIO 0
#define LNPRIO 1
#define MSLP 4
#define BARG 0
struct Args {
  long long lay;
  const u16* a; u16* kv; u16* o; u16* xo; double* so;
  const u16* W; u16* wc;
  const u16 *wg, *wu, *wd, *wo, *w_r;
  unsigned* gran; float* csf; double* stats; unsigned* hand; u16* out;
};
typedef __attribute__((address_space(4))) const Args* ArgP;
#define WCAT 3
#define CAP0 1
#define PRE4 50
#define PRE7 20
#define UARR 12u
#define GF0 10
#define XGATE 8
#define ASLP 1
#define B15 3
#define KO 5
#define PRO 2
#define HS 2
#define XD2 90
#define TFL 4u
#define QPRE 10
#define QSLP 1
#define GW 1u
#define G1 8u
#define GFD 2
#define WMAP 0x32100333
#define SMAP 0
#define STCH 6
#define KPOLL 10
#define TPRIO 2
#define TSLP 9
__device__ __forceinline__ bool slow_cols(const u16* W, int tni) { return ((((unsigned)(uintptr_t)W >> 7) + tni) & 7) == 3; }
#define ARGS_HERE                                                                                                      \
  ArgP ap_ = ap; asm volatile("" : "+s"(ap_));                                                                          \
  const u16* A = ap_->a; const u16* __restrict__ W = ap_->W; u16* KV = ap_->kv; u16* Of = ap_->o;        \
  const u16* __restrict__ wg = ap_->wg; const u16* __restrict__ wu = ap_->wu; const u16* __restrict__ wd = ap_->wd;       \
  const u16* __restrict__ wo = ap_->wo; const u16* __restrict__ w_r = ap_->w_r; unsigned* gran = ap_->gran;              \
  float* csf = ap_->csf; const double* stats = ap_->stats; double* stats_out = ap_->so; unsigned* hand = ap_->hand; \
  u16* xout = ap_->xo; u16* __restrict__ out = ap_->out
#define AGAIN "s_getpc_b64 s[90:91]\n\ts_add_u32 s90, s90, layers_kernel@rel32@lo+4\n\ts_addc_u32 s91, s91, layers_kernel@rel32@hi+12\n\ts_setpc_b64 s[90:91]"
#define KEPT(c)                                                                                                        \
  c "{v[104:107]}"(gu[0]), c "{v[108:111]}"(gu[1]), c "{v[112:115]}"(gu[2]), c "{v[116:119]}"(gu[3]), c "{v[120:123]}"(gu[4]),      \
  c "{v[124:127]}"(gu[5]), c "{v[128:131]}"(gu[6]), c "{v[132:135]}"(gu[7]), c "{v[136:139]}"(gu[8]), c "{v[140:143]}"(gu[9]),      \
  c "{v[144:147]}"(gu[10]), c "{v[148:151]}"(gu[11]), c "{v[152:155]}"(gu[12]), c "{v[156:159]}"(gu[13]), c "{v[160:163]}"(gu[14]), \
  c "{v[164:167]}"(gu[15])
#define LAYER_HERE ArgP apl_ = ap; asm volatile("" : "+s"(apl_)); const bool LAY2 = apl_->lay != 0
__device__ __forceinline__ void layer_body(const int wid, const int bid, const unsigned lbase, const ArgP ap, const unsigned long long t_entry) {
  constexpr int K = 1024, NKT = 16, NST = 3, ABYTES = 16384, WBYTES = 24576, STB = ABYTES + WBYTES, ldw = 3 * D_;
  constexpr int TABA = NST * STB, ALPHA = TABA + 2048, CRED = ALPHA + 1024;
  constexpr int KSB = 8192, ASB = 16384, FLG = 8 * ASB + 256;
  constexpr int DUM = FLG + 64, TAB = DUM + 256, RED = TAB + 512, SACT = RED + 4096, XML = SACT + 3584, UGL = XML + 4096, SPM = UGL + 1024, QOFF = 9 * ASB;
  constexpr int TABW = RED;
  static_assert(ALPHA % 1024 == 0 && RK == 16, "");
  static_assert(SPM + 2048 <= QOFF && CRED <= 8 * ASB && RED + 8192 <= QOFF && RED % 16 == 0, "lds");
  const int xcd = (bid & 1) * 4 + ((bid >> 1) & 3), loc = bid >> 3;
  const int tmi = (xcd / QNG) * QMX + loc / QNX, tni = (xcd % QNG) * QNX + loc % QNX;
  const int m0 = tmi * 128, n0 = tni * 64;
  const int omi = (xcd / (OMX / 2)) * OMX + loc / (32 / OMX), oni = (xcd % (OMX / 2)) * (32 / OMX) + loc % (32 / OMX);
  constexpr unsigned VOFF = (unsigned)S_ * D_ * 2;
  auto tile_at = [&](unsigned n) __attribute__((always_inline)) {
#pragma unroll 1
    for (int i = 0; i < 4 * SPIN_MAX; ++i) {
      if (__builtin_amdgcn_readfirstlane(*LDS_AT(const volatile unsigned, lbase + FLG + 36)) == n * 0x01010101u) break;
      __builtin_amdgcn_s_sleep(1);
    }
    asm volatile("" ::: "memory");
    PIN();
  };
  auto all_ge = [&](unsigned w, unsigned n) { return ((w + (128u - n) * 0x01010101u) & 0x80808080u) == 0x80808080u; };
  if (wid >= 8) {
    u32x4 gu[16];
    LAYER_HERE;
    constexpr int dk = 6;
    const int hw = wid - 8, b = bid;
    int nb = 0;
    constexpr int lim = NKT - 1;
    auto tick_nw = [&]() {
      PIN();
      if (nb < lim) { __builtin_amdgcn_s_barrier(); ++nb; } else __builtin_amdgcn_s_sleep(2);
      asm volatile("" ::: "memory");
      PIN();
    };
    auto tick = [&]() {
      asm volatile("s_waitcnt lgkmcnt(0)" ::: "memory");
      tick_nw();
    };
    auto fold16 = [&](f32x2 y0, f32x2 y1) __attribute__((always_inline)) {
      const auto q0 = __builtin_amdgcn_permlane32_swap(__float_as_uint(y0[0]), __float_as_uint(y1[0]), false, false);
      const auto q1 = __builtin_amdgcn_permlane32_swap(__float_as_uint(y0[1]), __float_as_uint(y1[1]), false, false);
      const auto q = __builtin_amdgcn_permlane16_swap(__float_as_uint(__uint_as_float(q0[0]) + __uint_as_float(q0[1])),
                                                      __float_as_uint(__uint_as_float(q1[0]) + __uint_as_float(q1[1])), false, false);
      return __uint_as_float(q[0]) + __uint_as_float(q[1]);
    };
    unsigned best, tag, rmd;
    float alpha, m, wsum;
    u32x4 tw;
    u32x2 xq;
    s16x8 sl[8];
    const int kcap = CAP0 + (tmi & 7);
    if (!LAY2) {
      ARGS_HERE;
      const int lane = fresh_lane(), t = hw * 64 + lane;
      const unsigned ep = ld_ag(gran);
      const int srow = m0 + tni * 8 + hw * 2;
      u32x4 xa[2][2], wv[8];
      const int wr_ = (3 - ((unsigned)(uintptr_t)W >> 7) - (tni & ~3)) & 7;
      const bool cpy = hw == 3 && wr_ < 4 && wr_ != (tni & 3), wld = hw < 3 ? wr_ != (tni & 3) : cpy;
      const int wcn = cpy ? (tni & ~3) + wr_ : tni, wcp = cpy ? (tni - wr_ - 1) & 3 : hw;
      const int wof = ((tmi * 64 + (lane >> 3)) * ldw + wcp * D_ + wcn * 64 + (lane & 7) * 8) * 2;
      auto ld_w = [&]() {
        const __amdgpu_buffer_rsrc_t rw = __builtin_amdgcn_make_buffer_rsrc(const_cast<u16*>(W), 0, wld ? D_ * ldw * 2 : 0, 0x00020000);
#pragma unroll
        for (int r = 0; r < 8; ++r) wv[r] = __builtin_amdgcn_raw_buffer_load_b128(rw, wof, r * 8 * ldw * 2, 0);
        PIN();
      };
      auto wcopy = [&]() __attribute__((always_inline)) {
        if (cpy) {
          const __amdgpu_buffer_rsrc_t rw = __builtin_amdgcn_make_buffer_rsrc(ap_->wc, 0, D_ * ldw * 2, 0x00020000);
#pragma unroll
          for (int r = 0; r < 8; ++r) __builtin_amdgcn_raw_buffer_store_b128(wv[r], rw, wof + r * 8 * ldw * 2, 0, 16);
        }
        PIN();
      };
      const int wat = ((hw == 3 ? SMAP : WMAP) >> (4 * (tmi & 7))) & 3;
      unsigned ps = 0u;
      auto stouch = [&](int at) __attribute__((always_inline)) {
        if (at == STCH && cpy) ps = __builtin_amdgcn_raw_buffer_load_b32(__builtin_amdgcn_make_buffer_rsrc(const_cast<u16*>(W), 0, D_ * ldw * 2, 0x00020000),
                                                                        (((tmi ^ 8) * 64 + lane) * ldw + wcp * D_ + wcn * 64) * 2, 0, 0);
        PIN();
      };
      auto ld_x = [&](int h) {
#pragma unroll
        for (int r = 0; r < 2; ++r) xa[r][h] = *reinterpret_cast<const u32x4*>(A + (size_t)(srow + r) * D_ + (h ^ (tmi >> 3)) * 512 + lane * 8);
        PIN();
      };
      ld_x(0);
      if (X2AT == 0) ld_x(1);
      if (wat == 0) ld_w();
      unsigned pc = 0u;
      if (hw == 3) {
        unsigned long long a0, a1;
        CODE_ADDR("layers_kernel", a0);
        CODE_ADDR("end_marker_kernel", a1);
        pc = touch_code(a0, a1, loc, lane);
      }
      if (hw == 1) pc = ld_ag(gran + ((lane & 1) ? GR_ROW + (m0 + tni * 8) * 4 : GR_KV + (tni * RK + tmi % RK) * 32));
      if (hw == 2) st_ag(gran + GR_N + bid * 64 + lane, lane + 1u);
      {
        const int gi = bid * 256 + t;
        if (gi < GRAN_N / 4) __builtin_amdgcn_raw_buffer_store_b128(u32x4{SENT, SENT, SENT, SENT}, __builtin_amdgcn_make_buffer_rsrc(hand, 0, GRAN_N * 4, 0x00020000), gi * 16, 0, 16);
        if (t < 24) __hip_atomic_store(stats_out + (t >> 3) * S_ + m0 + tni * 8 + (t & 7), 0.0, __ATOMIC_RELAXED, __HIP_MEMORY_SCOPE_AGENT);
      }
      tick();
      if (wat == 1) ld_w();
      if (X2AT == 1) ld_x(1);
      tick();
      if (wat == 2) ld_w();
      if (WCAT == 2) wcopy();
      if (X2AT == 2) ld_x(1);
      asm volatile("" :: "v"(ep));
      tick();
      if (wat == 3) ld_w();
      if (WCAT == 3) wcopy();
      if (X2AT == 3) ld_x(1);
      stouch(3);
#pragma unroll
      for (int i = 0; i < HW; ++i) { tick(); stouch(4 + i); }
      __builtin_amdgcn_s_setprio(HP1);
      PIN();
      tag = ep * 1664525u + 1013904223u;
      const __amdgpu_buffer_rsrc_t rsc = __builtin_amdgcn_make_buffer_rsrc(gran, 0, GR_N * 4, 0x00020000);
      {
        u32x4 od;
#pragma unroll
        for (int q = 0; q < 4; ++q) od[q] = ((lane & 7) >> 1) == q ? ((lane & 1) ? 0x3f800000u : 0x00003f80u) : 0u;
        f32x4 d[2] = {{0.f, 0.f, 0.f, 0.f}, {0.f, 0.f, 0.f, 0.f}};
#pragma unroll
        for (int r = 0; r < 8; ++r) d[r & 1] = __builtin_amdgcn_mfma_f32_16x16x32_bf16(__builtin_bit_cast(s16x8, od), __builtin_bit_cast(s16x8, wv[r]), d[r & 1], 0, 0, 0);
        u32x4 gr;
#pragma unroll
        for (int q = 0; q < 4; ++q) gr[q] = ((__float_as_uint(dpp_add<0x128, 0xf>(fadd(d[0][q], d[1][q]))) + 0x80u) & ~0xffu) | ((tag >> (8 * q)) & 0xffu);
        asm volatile("s_waitcnt vmcnt(0)" ::: "memory");
        __builtin_amdgcn_raw_buffer_store_b128(gr, rsc, (lane & 40) || !wld ? GR_N * 4 : (GR_COL + (wcn * 16 + tmi) * 192 + wcp * 64 + (lane & 7) * 8 + (lane >> 4) * 4) * 4, 0, 16);
      }
      asm volatile("" :: "v"(pc));
      tick_nw();
      stouch(6);
      {
        float xv[2][16], mean[2];
#pragma unroll
        for (int r = 0; r < 2; ++r) {
          float sm = 0.f;
#pragma unroll
          for (int i = 0; i < 4; ++i) {
            xv[r][2 * i] = __uint_as_float(xa[r][0][i] << 16); xv[r][2 * i + 1] = __uint_as_float(xa[r][0][i] & 0xffff0000u);
            xv[r][8 + 2 * i] = __uint_as_float(xa[r][1][i] << 16); xv[r][8 + 2 * i + 1] = __uint_as_float(xa[r][1][i] & 0xffff0000u);
          }
#pragma unroll
          for (int i = 0; i < 16; ++i) sm += xv[r][i];
          mean[r] = wave_sum(sm) * (1.0f / 1024.0f);
          tick();
          stouch(7 + r);
        }
        float vr[2];
#pragma unroll
        for (int r = 0; r < 2; ++r) {
          float v = 0.f;
#pragma unroll
          for (int i = 0; i < 16; ++i) { const float d = xv[r][i] - mean[r]; v += d * d; }
          vr[r] = wave_sum(v) * (1.0f / 1024.0f);
          if (r == 0) tick();
        }
        const float vv = lane & 2 ? vr[1] : vr[0], mm = lane & 2 ? mean[1] : mean[0];
        const float rin = 1.0f / rbf(sqrtf(rbf(rbf(vv) + 1e-5f)));
        if (lane < 4) __builtin_amdgcn_raw_buffer_store_b64(u32x2{__float_as_uint((lane & 1) ? rbf(mm) : rin), tag}, rsc, (GR_ROW + srow * 4 + lane * 2) * 4, 0, 16);
      }
      tick();
      float cs;
      while (nb < POLLAT) tick();
      asm volatile("" :: "v"(ps));
#pragma unroll 1
      for (int spin = 0;; ++spin) {
        u32x4 csv[4];
        const __amdgpu_buffer_rsrc_t rsw = __builtin_amdgcn_make_buffer_rsrc(gran, 0, hw < 3 ? GR_N * 4 : 0, 0x00020000);
#pragma unroll
        for (int i = 0; i < 4; ++i) csv[i] = __builtin_amdgcn_raw_buffer_load_b128(rsw, (GR_COL + (tni * 16 + (lane >> 4)) * 192 + hw * 64 + (lane & 15) * 4) * 4, i * (4 * 192 * 4), 16);
        const u32x4 rp = __builtin_amdgcn_raw_buffer_load_b128(rsc, (GR_ROW + (hw < 2 ? m0 + t : (m0 ^ 128) + tni * 8 + (t & 7)) * 4) * 4, 0, 16);
        PIN();
        tick(); tick();
        unsigned bad = 0u;
        float z[4];
#pragma unroll
        for (int q = 0; q < 4; ++q) {
          const unsigned tq = tag >> (8 * q);
          bad |= ((csv[0][q] ^ tq) | (csv[1][q] ^ tq)) | ((csv[2][q] ^ tq) | (csv[3][q] ^ tq));
          z[q] = fadd(fadd(__uint_as_float(csv[0][q] & ~0xffu), __uint_as_float(csv[1][q] & ~0xffu)), fadd(__uint_as_float(csv[2][q] & ~0xffu), __uint_as_float(csv[3][q] & ~0xffu)));
        }
        cs = fold16(f32x2{z[0], z[1]}, f32x2{z[2], z[3]});
        bad = (hw < 3 ? bad & 0xffu : 0u) | (rp[1] ^ tag) | (rp[3] ^ tag);
        alpha = __uint_as_float(rp[0]); m = __uint_as_float(rp[2]);
        if (none(bad != 0u) || spin >= SPIN_MAX) break;
      }
      if (tmi == 0 && hw < 3) st_ag(reinterpret_cast<unsigned*>(csf) + hw * 1024 + n0 + (lane & 15) * 4 + (lane >> 4), __float_as_uint(cs));
      unsigned c0, c1, c2;
      split3(cs, c0, c1, c2);
      tw = u32x4{c0 | (c1 << 16), c0 | (c2 << 16), c1 | (c0 << 16), 0u};
      best = rmd = 0u;
      wsum = 0.f;
      asm volatile("" : "=v"(xq), KEPT("="));
#pragma unroll
      for (int i = 0; i < 8; ++i) asm volatile("" : "=v"(sl[i]));
    } else {
      asm volatile("; kept registers" : KEPT("="));
      PIN();
      ARGS_HERE;
      const int lane = fresh_lane(), t = hw * 64 + lane;
      best = rmd = 0u;
      tag = __builtin_amdgcn_readfirstlane(*LDS_AT(const volatile unsigned, lbase + FLG + 20));
#pragma unroll
      for (int i = 0; i < 8; ++i) asm volatile("" : "=v"(sl[i]));
      unsigned sad;
      auto tickc = [&]() __attribute__((always_inline)) {
        tick();
        if (nb == kcap + 1) {
#pragma unroll
          for (int i = 0; i < 8; ++i) {
            const unsigned a = (sad ^ ((i >> 2) * 64) ^ (((i >> 1) & 1) * 16)) + (i & 1) * 12288;
            sl[i] = __builtin_shufflevector(__builtin_amdgcn_ds_read_tr16_b64_v4i16(LDS_AT(s16x4, a)), __builtin_amdgcn_ds_read_tr16_b64_v4i16(LDS_AT(s16x4, a + 1536)), 0, 1, 2, 3, 4, 5, 6, 7);
          }
        }
        PIN();
      };
      static_assert(CAP0 + 8 <= KPOLL, "the stage is taken before that");
      if (hw == 1) STAMP(4, 4);
      if (hw == 0) {
        __builtin_amdgcn_s_sleep(TPRE);
        tile_at(3u);
        __builtin_amdgcn_s_sleep(XGATE);
        STAMP(4, 3);
#pragma unroll 1
        for (int spin = 0; spin < SPIN_MAX; ++spin) {
          if (none(ld_ag(gran + GR_X + (tmi * RX + tni % RX) * 128 + 64 + lane) != tag)) break;
          __builtin_amdgcn_s_sleep(1);
        }
      }
      static_assert(UHW == 0, "");
      if (hw == 0) asm volatile("s_waitcnt vmcnt(0)" ::: "memory");
      if (hw == 0) STAMP(4, 5);
      asm volatile("" ::: "memory");
      PIN();
      __builtin_amdgcn_s_barrier();
      PIN();
      if (hw == 1) STAMP(4, 6);
      {
        const int ln = fresh_lane(), g4 = ln >> 4, tp = ln & 3, k = 8 * g4 + ((ln & 15) >> 2);
        sad = lbase + (kcap % NST) * STB + ABYTES + k * 384 + (((2 * (tp >> 1)) ^ (4 * ((k >> 1) & 1)) ^ (g4 & 1)) << 4) + 8 * (tp & 1) + (hw % 3) * 128;
      }
      wsum = __uint_as_float(__builtin_amdgcn_readfirstlane(*LDS_AT(const volatile unsigned, lbase + FLG + 60)));
      asm volatile("" : "+v"(sad), "=v"(tw), "=v"(alpha), "=v"(m));
      PIN();
      const int row = m0 + (t & 127);
      const __amdgpu_buffer_rsrc_t rsg = __builtin_amdgcn_make_buffer_rsrc(hand, 0, GRAN_N * 4, 0x00020000);
      if (hw < 2 || tmi == tni) {
      while (nb < HS) tickc();
#pragma unroll 1
      for (int spin = 0; spin < SPIN_MAX; ++spin) {
        const unsigned v = ld_ag(gran + GR_X + (tmi * RX + tni % RX) * 128 + lane);
        tickc();
        if (none(v != tag)) break;
      }
      asm volatile("" ::: "memory");
      PIN();
      const double s1 = ld_ag(stats + row), s2 = ld_ag(stats + S_ + row), sw = ld_ag(stats + 2 * S_ + row);
      tickc();
      float mean, var, mb, den;
      ln_from_sums(s1, s2, mean, var, mb, den);
      const float rin = frcp(den), g1 = 1.0f + rin;
      const float mb2 = rbf(mean + (mean - mb) * rin);
      const float den2 = rbf(__builtin_amdgcn_sqrtf(rbf(rbf(var * g1 * g1) + 1e-5f)));
      alpha = g1 * frcp(den2); m = (mb * rin + mb2) * frcp(g1);
      if (tmi == tni && hw >= 2) {
        const float r = (float)fma(-(double)mb, (double)wsum, sw) * rin;
        const unsigned rb = __float_as_uint(r) + 0x8000u;
        unsigned rkey = (rb ^ ((unsigned)((int)rb >> 31) | 0x80000000u)) & 0xfffff800u;
        if (r != r) rkey = 0u;
        rkey = wave_umax(rkey | (unsigned)(2047 - row));
        const unsigned md = __builtin_amdgcn_readlane(f2word(__uint_as_float((__float_as_uint(mb) >> 16) | (__float_as_uint(den) & 0xffff0000u))), (2047 - (int)(rkey & 0xffffu)) & 63);
        __builtin_amdgcn_raw_buffer_store_b64(u32x2{rkey, md}, rsg, lane < NREP ? lane * 256 + (tmi * 2 + hw - 2) * 8 : GRAN_N * 4, 0, 16);
      }
      }
      while (nb < KPOLL) tickc();
#pragma unroll 1
      for (int spin = 0; spin < SPIN_MAX; ++spin) {
        const u32x2 v = __builtin_amdgcn_raw_buffer_load_b64(rsg, (bid % NREP) * 256 + (lane & 31) * 8, 0, 16);
        tick(); tick();
        if (none(umax(v[0], v[1]) == SENT)) {
          best = wave_umax(v[0]);
          rmd = __builtin_amdgcn_readlane(v[1], ((2047 - (int)(best & 0xffffu)) >> 6) & 31);
          break;
        }
      }
      const int ix = (2047 - (int)(best & 0xffffu)) & 2047;
      if (row == ix) alpha = 0.f;
      xq = __builtin_amdgcn_raw_buffer_load_b64(__builtin_amdgcn_make_buffer_rsrc(const_cast<u16*>(A), 0, S_ * D_ * 2, 0x00020000), (ix * D_ + hw * 256 + (lane & 3) * 64 + (lane >> 2) * 4) * 2, 0, 16);
      PIN();
    }
    tag = __builtin_amdgcn_readfirstlane(tag);
    wsum = __uint_as_float(__builtin_amdgcn_readfirstlane(__float_as_uint(wsum)));
    const int lane = fresh_lane(), t = hw * 64 + lane;
    {
      unsigned a0, a1, a2;
      split3(-m, a0, a1, a2);
      u32x4 ta = {a0 | (a0 << 16), a1 | (a0 << 16), a1 | (a2 << 16), 0u};
      asm volatile("" : "+v"(tw), "+v"(ta), "+v"(alpha));
      __builtin_amdgcn_s_setprio(TPRIO);
      if (nb < lim) {
        while (nb < lim) tick();
        __builtin_amdgcn_s_sleep(TSLP);
      }
      if (!LAY2 && hw < 3) *LDS_AT(u32x4, lbase + TABW + ((hw ^ (hw < 2 ? ((lane >> 3) ^ tmi) & 1 : 0)) * 64 + (lane & 15) * 4 + (lane >> 4)) * 16) = tw;
      if (t < 128) {
        *LDS_AT(u32x4, lbase + TABA + t * 16) = ta;
        *LDS_AT(float, lbase + ALPHA + t * 4) = alpha;
        *LDS_AT(float, lbase + ALPHA + 512 + t * 4) = alpha * QSCALE;
      }
    }
    if (t >= 192 && t < 196) *LDS_AT(u32x4, lbase + CRED + ((t >> 1) & 1) * 512 + (t & 1) * 128) = u32x4{0u, 0u, 0u, 0u};
    asm volatile("s_waitcnt lgkmcnt(0)" ::: "memory");
    PIN();
    __builtin_amdgcn_s_barrier();
    PIN();
    *LDS_AT(u32x4, lbase + XML + t * 16) = u32x4{SENT, SENT, SENT, SENT};
    *LDS_AT(u32x4, lbase + SPM + (t & 127) * 16) = u32x4{SENT, SENT, SENT, SENT};
    if (hw == 0) {
      unsigned v;
      asm volatile("v_mov_b32 %0, 0\n\tv_writelane_b32 %0, 2, 0\n\tv_writelane_b32 %0, %1, 5\n\tv_writelane_b32 %0, %2, 7"
                   : "=&v"(v) : "s"(tag), "s"(best));
      lds_wr(lbase + FLG + lane * 4, v);
    }
    const int idx = LAY2 ? (2047 - (int)(best & 0xffffu)) & 2047 : -64;
    const bool p1blk = (oni & 7) != 7, fast = tmi < 8, own = fast && (loc & 1);
    const int fg = xcd * 28 + (p1blk ? loc - (loc >> 3) : 0), ow = (xcd & 3) * 16 + (loc >> 1);
    static_assert(OMX == 4, "oni % 8 = loc % 8");
#define GLD(a, b) "global_load_dwordx4 v[" #a ":" #b "], %0, off\n\tv_lshl_add_u64 %0, %0, 0, %1\n\t"
#define GLD4(...) asm volatile(__VA_ARGS__ : "+v"(p) : "s"((unsigned long long)(F_ * 2)), KEPT() : "memory")
    auto gptr = [&](const u16* wg, const u16* wu) {
      const int ln = fresh_lane();
      return ((ln & 2) ? wu : wg) + (size_t)(hw * 256 + (ln >> 2) * 4) * F_ + fg * 16 + (ln & 1) * 8;
    };
    auto gather = [&](const u16* p, int st) __attribute__((always_inline)) {
      const int e = (st + fg) & 3;
      p += e * (64 * F_);
      if (e == 0) GLD4(GLD(104, 107) GLD(108, 111) GLD(112, 115) GLD(116, 119));
      else if (e == 1) GLD4(GLD(120, 123) GLD(124, 127) GLD(128, 131) GLD(132, 135));
      else if (e == 2) GLD4(GLD(136, 139) GLD(140, 143) GLD(144, 147) GLD(148, 151));
      else GLD4(GLD(152, 155) GLD(156, 159) GLD(160, 163) GLD(164, 167));
    };
    auto dmask = [&](int q) { const int ln = fresh_lane(); return ((ln >> 1) & 1) == q ? ((ln & 1) ? 0xffff0000u : 0x0000ffffu) : 0u; };
    unsigned dm0, dm1, dad;
    auto gu_setup = [&]() __attribute__((always_inline)) {
      const int ln = fresh_lane();
      dm0 = dmask(0); dm1 = dmask(1);
      dad = lbase + SACT + ((ln & 3) * 8 + (ln >> 5) * 4 + ((ln >> 4) & 1)) * 16 + hw * 4;
      asm volatile("" : "+v"(dm0), "+v"(dm1), "+v"(dad));
    };
    auto gate_up = [&](u32x2 x, unsigned md) __attribute__((always_inline)) {
      const unsigned rin = __builtin_amdgcn_readfirstlane(__float_as_uint(frcp(__uint_as_float(md & 0xffff0000u))));
      asm volatile("s_getpc_b64 s[90:91]\n\ts_add_u32 s90, s90, gate_up_code@rel32@lo+4\n\ts_addc_u32 s91, s91, gate_up_code@rel32@hi+12\n\ts_swappc_b64 s[92:93], s[90:91]"
                   : "+{v[100:101]}"(x) : "{s84}"(md << 16), "{s85}"(md & 0xffff0000u), "{s86}"(rin), "{v99}"(dad), "{v102}"(dm0), "{v103}"(dm1), KEPT()
                   : "v84", "v85", "v86", "v87", "v88", "v89", "v90", "v91", "v92", "v93", "v94", "v95", "v96", "v97", "s88", "s89", "s90", "s91", "s92", "s93", "scc", "memory");
    };
    auto act_diag = [&](int so = 0) __attribute__((always_inline)) {
      const int ln = fresh_lane();
      const f32x4 g4 = *LDS_AT(const volatile f32x4, lbase + SACT + so + (ln >> 2) * 16), u4 = *LDS_AT(const volatile f32x4, lbase + SACT + so + 256 + (ln >> 2) * 16);
      float gv = fadd(fadd(g4[0], g4[1]), fadd(g4[2], g4[3])), uv = fadd(fadd(u4[0], u4[1]), fadd(u4[2], u4[3]));
      rbf2(gv, uv);
      const float av = rbf(gv * frcp(1.0f + __expf(-gv))) * uv;
      const unsigned ap2 = pack2(av, av);
      return __builtin_bit_cast(s16x4, u32x2{ap2 & dm0, ap2 & dm1});
    };
    auto down = [&](const s16x4& a, const u32x2 (&w)[16]) __attribute__((always_inline)) {
      f32x4 d[2] = {{0.f, 0.f, 0.f, 0.f}, {0.f, 0.f, 0.f, 0.f}};
      static_for<0, 16>([&](auto fc) __attribute__((always_inline)) {
        constexpr int f = decltype(fc)::value;
        d[f & 1] = __builtin_amdgcn_mfma_f32_4x4x4bf16_1k(a, __builtin_bit_cast(s16x4, w[f]), d[f & 1], 4, f, 0);
      });
      f32x4 z;
#pragma unroll
      for (int e = 0; e < 4; ++e) { z[e] = d[0][e] + d[1][e]; asm volatile("" : "+v"(z[e])); }
      return u32x4{f2word(z[0]), f2word(z[1]), f2word(z[2]), f2word(z[3])};
    };
    auto fold8 = [&](f32x2 y0, f32x2 y1) __attribute__((always_inline)) { return dpp_add<0x128, 0xf>(fold16(y0, y1)); };
    auto fold4 = [&](f32x2 y0, f32x2 y1) __attribute__((always_inline)) { return dpp_add<0x124, 0xf>(fold8(y0, y1)); };
    auto y_sum = [&](const __amdgpu_buffer_rsrc_t rs, auto nbc) __attribute__((always_inline)) {
      constexpr int NB = decltype(nbc)::value, N = 14 / NB;
      const int ln = fresh_lane(), src = (GRAN_N + (ln >> 2) * D_ + ow * 16 + (ln & 3) * 4) * 4;
      f32x2 y0 = {0.f, 0.f}, y1 = {0.f, 0.f};
#pragma unroll 1
      for (int bt = 0; bt < NB; ++bt) {
        f32x2 z0, z1;
#pragma unroll 1
        for (int spin = 0; spin < SPIN_MAX; ++spin) {
          u32x4 v[N];
#pragma unroll
          for (int i = 0; i < N; ++i) v[i] = __builtin_amdgcn_raw_buffer_load_b128(rs, src, (bt * N + i) * (16 * D_ * 4), 16);
          z0 = y0; z1 = y1;
#pragma unroll
          for (int i = 0; i < N; ++i) {
            z0 = f32x2{fadd(z0[0], __uint_as_float(v[i][0])), fadd(z0[1], __uint_as_float(v[i][1]))};
            z1 = f32x2{fadd(z1[0], __uint_as_float(v[i][2])), fadd(z1[1], __uint_as_float(v[i][3]))};
          }
          const float chk = fadd(fadd(z0[0], z0[1]), fadd(z1[0], z1[1]));
          if (none(chk != chk)) break;
          unsigned mx = 0u;
#pragma unroll
          for (int i = 0; i < N; ++i) mx = umax(umax(umax(mx, v[i][0]), v[i][1]), umax(v[i][2], v[i][3]));
          if (none(mx == SENT)) break;
          __builtin_amdgcn_s_sleep(YSLP);
          asm volatile("" ::: "memory");
        }
        y0 = z0; y1 = z1;
      }
      return fold4(y0, y1);
    };
    [&]() __attribute__((always_inline)) {
    ARGS_HERE;
    const unsigned l16 = lane * 16;
    unsigned* kvflag = gran + GR_KV + ((LAY2 ? 16 : 0) + tni) * (32 * RK);
    auto wait_sig = [&](int sh, unsigned n) {
#pragma unroll 1
      for (int i = 0; i < 4 * SPIN_MAX; ++i) {
        if (((__builtin_amdgcn_readfirstlane(*LDS_AT(const volatile unsigned, lbase + FLG + 32)) >> sh) & 7u) >= n) break;
        __builtin_amdgcn_s_sleep(SSLP);
      }
      asm volatile("" ::: "memory");
      PIN();
    };
    PIN();
    asm volatile("s_waitcnt lgkmcnt(0)" ::: "memory");
    if (!LAY2) asm volatile("s_waitcnt vmcnt(0)" ::: "memory");
    __builtin_amdgcn_s_barrier();
    PIN();
    __builtin_amdgcn_s_setprio(HPRIO);
    if (hw < 3) {
      __builtin_amdgcn_s_sleep(SPRE);
      wait_sig((hw & 1) * 8, 2u);
      __builtin_amdgcn_raw_buffer_store_b32(tag, __builtin_amdgcn_make_buffer_rsrc(kvflag, 0, hw < 2 ? RK * 128 : 0, 0x00020000), lane * 256 + ((tmi ^ hw) & 1) * 128 + tmi * 4, 0, 16);
      if (hw == 0) STAMP(LAY2 ? 5 : 1, 16);
    }
    if (hw == 3) {
      __builtin_amdgcn_s_sleep(KVS);
      const unsigned kv0 = ld_ag(kvflag + (tmi % RK) * 32 + (lane & 15));
      PIN();
      const char* cnt = reinterpret_cast<const char*>(gran + GR_N + bid * 64 + 2);
      unsigned vk[4], vp = lbase + FLG + 8 + (lane & 7), pw = 0u;
#pragma unroll
      for (int j = 0; j < 4; ++j) { vk[j] = l16 + (j >> 1) * 4096u + (j & 1) * (VOFF - 2048u); asm volatile("" : "+v"(vk[j])); }
#define G4(a, b, c, d, sc) "s_nop 0\n\tglobal_load_lds_dwordx4 %" #a ", %4" sc "\n\tglobal_load_lds_dwordx4 %" #b ", %4 offset:1024" sc "\n\t"             \
                       "global_load_lds_dwordx4 %" #c ", %4 offset:2048" sc "\n\tglobal_load_lds_dwordx4 %" #d ", %4 offset:3072" sc "\n\t"
#define M4 "s_addk_i32 m0, 0x1000\n\t"
      auto stage = [&](const char* sp, unsigned dst) __attribute__((always_inline)) {
        asm volatile("s_mov_b32 m0, %5\n\t" G4(0, 0, 1, 1, " sc1") M4 G4(2, 2, 3, 3, " sc1")
                     :: "v"(vk[0]), "v"(vk[1]), "v"(vk[2]), "v"(vk[3]), "s"(sp), "s"(dst) : "m0", "scc", "memory");
      };
      auto count = [&]() __attribute__((always_inline)) {
        asm volatile("s_mov_b32 m0, %2\n\ts_nop 0\n\tglobal_load_lds_dword %0, %1 sc1" :: "v"(0u), "s"(cnt), "s"(lbase + FLG - 252) : "m0", "memory");
        cnt += 8;
      };
      auto freed = [&](unsigned n) __attribute__((always_inline)) {
        if (!none(pw < n)) {
#pragma unroll 1
          for (int i = 0; i < 4 * SPIN_MAX; ++i) {
            pw = *LDS_AT(const volatile unsigned char, vp);
            if (none(pw < n)) break;
            __builtin_amdgcn_s_sleep(W3SLP);
          }
        }
        asm volatile("" ::: "memory");
      };
      unsigned long long ord = (0xfedcba9875316420ull >> 4 & ~((1ull << 2 * (tmi & 6)) - 1ull)) | (0xfedcba9875316420ull & ((1ull << 2 * (tmi & 6)) - 1ull));
      ord ^= (tmi & 9) * 0x1111111111111111ull;
      unsigned seen;
      const char* kvb = reinterpret_cast<const char*>(KV) + (size_t)(tni * 2 + (tmi & 1)) * (64 * 2048);
      auto panel = [&]() __attribute__((always_inline)) {
        const unsigned pn = (unsigned)ord & 15u;
        ord >>= 4;
        if (__builtin_expect(!((seen >> pn) & 1u), 0)) {
#pragma unroll 1
          for (int i = 0; i < SPIN_MAX && !((seen >> pn) & 1u); ++i) seen = (unsigned)__ballot(ld_ag(kvflag + (tmi % RK) * 32 + (lane & 15)) == tag);
        }
        asm volatile("" ::: "memory");
        return kvb + pn * 8192u;
      };
      asm volatile("" : "+s"(ord), "+s"(kvb), "+s"(cnt));
      asm volatile("" : "+v"(vp), "+v"(pw));
      PIN();
      seen = (unsigned)__ballot(kv0 == tag);
      const char* sp = panel();
      wait_sig(16, 4u);
      stage(sp, lbase + 2 * KSB);
      count();
      const int ksp = LAY2 ? PNS / 2 : -1, ksg = LAY2 || !p1blk ? 64 : GF0;
      const u16* gp = gptr(wg, wu);
#pragma unroll 1
      for (int k = 1; k < 15; ++k) {
        if (__builtin_expect(k == ksp, 0)) {
          gu_setup();
          gate_up(xq, rmd);
          PIN();
          *LDS_AT(volatile unsigned char, lbase + FLG + 7) = (unsigned char)0x7f;
        }
        freed((unsigned)(0xba9876543211000ull >> 4 * k) & 15u);
        stage(sp + 2048, lbase + ((2 * k + 1) & 7) * KSB);
        if (__builtin_expect(k == 1, 0)) { cnt -= 4; count(); cnt -= 4; }
        sp = panel();
        stage(sp, lbase + ((2 * k + 2) & 7) * KSB);
        count();
        if (__builtin_expect(k == ksg || (unsigned)(k - ksg - GFD) < 3u, 0)) gather(gp, k == ksg ? 0 : k - ksg - GFD + 1);
      }
      freed(12u);
      glds16s<true>(csf + 4096 + tni * 128, l16, lbase + UGL);
      stage(sp + 2048, lbase + 7 * KSB);
      count();
      const unsigned vwo = (unsigned)((lane >> 3) * D_ + oni * 64 + ((lane & 7) ^ ((lane >> 3) & 6)) * 8) * 2u;
      constexpr unsigned vs = 8u * D_ * 2u - 1024u;
#pragma unroll 1
      for (int i = 0; i < 6; ++i) {
        const int ns = (i + 5) & 7;
        freed(i < 3 ? 12u : 10u + i);
        const char* wp = reinterpret_cast<const char*>(wo) + (size_t)ns * (128 * D_ * 2);
#pragma unroll
        for (int j = 0; j < 4; ++j)
          glds16x4<false>(wp + j * (32u * D_ * 2u), vwo, vwo + vs, vwo + 2 * vs, vwo + 3 * vs, lbase + ns * ASB + j * 4096);
      }
      asm volatile("s_waitcnt vmcnt(0)" ::: "memory");
      return;
    }
    if (!LAY2) {
      auto until = [&](unsigned n) {
        while ((__builtin_amdgcn_readfirstlane(*LDS_AT(const volatile unsigned, lbase + FLG + 8)) & 255u) < n) __builtin_amdgcn_s_sleep(16);
        asm volatile("" ::: "memory");
        PIN();
      };
      if (hw != 1) {
        constexpr int N2 = HAND_N + Y_N, X3N = 3 * D_ / 2, N0 = X3N + PS_N;
        const __amdgpu_buffer_rsrc_t rsi = __builtin_amdgcn_make_buffer_rsrc(hand + (hw ? GRAN_N : 2 * HAND_N - X3N), 0, (hw ? N2 : N0) * 4, 0x00020000);
        until(IAT);
#pragma unroll 1
        for (int i = 0; i < ((hw ? N2 : N0) / 4 + 16383) / 16384; ++i) {
          __builtin_amdgcn_raw_buffer_store_b128(u32x4{SENT, SENT, SENT, SENT}, rsi, ((i * 256 + bid) * 64 + lane) * 16, 0, 16);
          __builtin_amdgcn_s_sleep(ISLP);
        }
      }
      if (hw != 1) {
        const __amdgpu_buffer_rsrc_t rq = __builtin_amdgcn_make_buffer_rsrc(reinterpret_cast<char*>(KV) + QG + ((256 + bid) * 2 + (hw >> 1)) * 4096, 0, 4096, 0x00020000);
#pragma unroll
        for (int i = 0; i < 4; ++i) __builtin_amdgcn_raw_buffer_store_b128(u32x4{SENT, SENT, SENT, SENT}, rq, lane * 16 + i * 1024, 0, 16);
      }
      const u16* p = gptr(wg, wu);
#pragma unroll 1
      for (unsigned n = T0; n < T0 + 13u; ++n) {
        until(n);
        if (hw == 2 && n == TFL) {
          const int ln = fresh_lane() & 15;
          const unsigned p1 = ld_ag(gran + (ln < 4 ? GR_O + (((ln >> 1) * 16 + omi) * RO + oni % RO) * 64 + (ln & 1) * 32 :
                                            (ln < 8 ? GR_X + (tmi * RX + tni % RX) * 128 + (ln & 3) * 32 : GR_KV + ((16 + tni) * RK + tmi % RK) * 32)));
          asm volatile("" :: "v"(p1));
        }
        if (hw == 2 && n == TW) {
          const int ln = fresh_lane();
          unsigned long long a0, a1;
          CODE_ADDR("layers_kernel", a0); CODE_ADDR("end_marker_kernel", a1);
          const unsigned p1 = touch_code(a0, a1, 3 + loc, ln), p0 = touch(wo + (size_t)(384 + (loc / (32 / OMX)) * (256 / OMX) + ln) * D_ + oni * 64);
          asm volatile("" :: "v"(p0), "v"(p1));
        }
        {
          const int st = n == G0 + GW * ((hw + 2) % 3) ? 0 : (n >= G1 && (n - G1) % GS == 0u && (n - G1) / GS < 3u ? 1 + (n - G1) / GS : -1);
          if (st >= 0 && p1blk) gather(p, st);
        }
        if (n == UAT && hw == UHW) {
          u32x4 a[4][2], c[2];
#pragma unroll
          for (int i = 0; i < 4; ++i)
#pragma unroll
            for (int h = 0; h < 2; ++h) a[i][h] = *reinterpret_cast<const u32x4*>(wo + (size_t)(tni * 64 + tmi * 4 + i) * D_ + h * 512 + lane * 8);
#pragma unroll
          for (int h = 0; h < 2; ++h) c[h] = *reinterpret_cast<const u32x4*>(w_r + h * 512 + lane * 8);
          auto dot = [&](unsigned x, unsigned y, float z) { return __builtin_amdgcn_fdot2_f32_bf16(__builtin_bit_cast(bf16x2, x), __builtin_bit_cast(bf16x2, y), z, false); };
          PIN();
          __builtin_amdgcn_s_setprio(0);
#pragma unroll 1
          for (int i = 0; i < SPIN_MAX; ++i) {
            if (__builtin_amdgcn_readfirstlane(*LDS_AT(const volatile unsigned, lbase + FLG + 16)) >= UARR) break;
            __builtin_amdgcn_s_sleep(4);
          }
          asm volatile("s_waitcnt vmcnt(0)" ::: "memory");
          PIN();
          float wm = 0.f;
#pragma unroll
          for (int e = 0; e < 8; ++e) wm = dot(c[e >> 2][e & 3], 0x3f803f80u, wm);
          wm = wave_sum(wm);
          lds_wr(lbase + FLG + 60, __float_as_uint(wm));
          wm *= -1.0f / 1024.0f;
          f32x2 u[2], g[2];
#pragma unroll
          for (int i = 0; i < 4; ++i) {
            float p1 = 0.f, pw = 0.f, p2 = 0.f;
#pragma unroll
            for (int e = 0; e < 8; ++e) {
              const unsigned x = a[i][e >> 2][e & 3];
              p1 = dot(x, 0x3f803f80u, p1); pw = dot(x, c[e >> 2][e & 3], pw); p2 = dot(x, x, p2);
            }
            u[i >> 1][i & 1] = fmaf(p1, wm, pw); g[i >> 1][i & 1] = p2;
          }
          float us = fold4(u[0], u[1]), gs = fold4(g[0], g[1]);
          us = dpp_add<0x121, 0xf>(dpp_add<0x122, 0xf>(us)); gs = dpp_add<0x121, 0xf>(dpp_add<0x122, 0xf>(gs));
          __builtin_amdgcn_raw_buffer_store_b32(__float_as_uint((lane & 1) ? __builtin_amdgcn_sqrtf(gs) * (1.0f / 32.0f) : us),
              __builtin_amdgcn_make_buffer_rsrc(csf + 4096 + tni * 128, 0, 512, 0x00020000), (lane & 14) ? 512 : ((lane & 1) * 64 + tmi * 4 + (lane >> 4)) * 4, 0, 16);
        }
      }
      if (hw == 2 && bid == 0) {
        const __amdgpu_buffer_rsrc_t rsc = __builtin_amdgcn_make_buffer_rsrc(gran, 0, GR_N * 4, 0x00020000);
#pragma unroll 1
        for (int spin = 0; spin < SPIN_MAX; ++spin) {
          bool ok = true;
#pragma unroll
          for (int i = 0; i < 4; ++i) ok &= __builtin_amdgcn_raw_buffer_load_b32(rsc, (GR_ROW + (i * 64 + lane) * 32 + 1) * 4, 0, 16) == tag;
          if (__all(ok)) break;
          __builtin_amdgcn_s_sleep(8);
          asm volatile("" ::: "memory");
        }
        if (lane == 0) st_ag(gran, tag);
      }
      return;
    }
    unsigned* X2 = hand + GRAN_N + Y_N;
    unsigned* XS = X2 + X2_N;
    unsigned* P4 = XS + XS_N;
    unsigned* PA = P4 + P4_N;
    const __amdgpu_buffer_rsrc_t rsh = __builtin_amdgcn_make_buffer_rsrc(hand, 0, HAND_N * 4, 0x00020000);
    auto signal = [&](unsigned ph) {
      asm volatile("" ::: "memory");
      PIN();
      *LDS_AT(volatile unsigned char, lbase + FLG + 4 + hw) = (unsigned char)ph;
    };
    auto wait_all = [&](unsigned ph) {
#pragma unroll 1
      for (int i = 0; i < SPIN_MAX; ++i) {
        if (all_ge(__builtin_amdgcn_readfirstlane(*LDS_AT(const volatile unsigned, lbase + FLG + 4)), ph)) break;
        __builtin_amdgcn_s_sleep(WSLP);
      }
      asm volatile("" ::: "memory");
      PIN();
    };
    gu_setup();
    gate_up(xq, rmd);
    signal(hw == 2 ? 2u : 0x7fu);
    if (wid == 8) STAMP(dk, 2);
    if (hw < 2) {
#pragma unroll 1
      for (int i = 0; i < SPIN_MAX; ++i) {
        if (__builtin_amdgcn_readfirstlane(*LDS_AT(const volatile unsigned, lbase + FLG)) >= FSTART) break;
        __builtin_amdgcn_s_sleep(WSLP);
      }
      asm volatile("" ::: "memory");
      PIN();
      if (wid == 8) STAMP(dk, 10);
      u32x2 w[16];
      const __amdgpu_buffer_rsrc_t rd_ = __builtin_amdgcn_make_buffer_rsrc(const_cast<u16*>(wd), 0, D_ * F_ * 2, 0x00020000);
      auto ldw = [&](int c) __attribute__((always_inline)) {
#pragma unroll
        for (int f = 0; f < 16; ++f) w[f] = __builtin_amdgcn_raw_buffer_load_b64(rd_, lane * 8 + c * 512 + (f & 1) * 2048, (fg * 16 + (f & ~1)) * (D_ * 2) + hw * 1024, 0);
        PIN();
      };
      ldw(0);
      const unsigned pt = __builtin_amdgcn_raw_buffer_load_b32(rd_, (lane >> 2) * (D_ * 2) + (lane & 3) * 128 + 512, fg * 16 * (D_ * 2) + hw * 1024, 0);
      int yo = p1blk ? (GRAN_N + fg * D_ + hw * 512) * 4 + lane * 16 : HAND_N * 4;
      asm volatile("" : "+v"(yo));
      wait_all(2u);
      if (wid == 8) STAMP(dk, 3);
      const s16x4 a = act_diag();
      const u32x4 y0 = down(a, w);
      PIN();
      ldw(1);
      __builtin_amdgcn_raw_buffer_store_b128(y0, rsh, yo, 0, 16);
      PIN();
      __builtin_amdgcn_raw_buffer_store_b128(down(a, w), rsh, yo + 1024, 0, 16);
      asm volatile("" :: "v"(pt));
      if (wid == 8) STAMP(dk, 4);
    }
    PIN();
    [&]() __attribute__((always_inline)) {
    const int lane = fresh_lane();
    const float rmb = __uint_as_float(rmd << 16);
    auto first_words = [&](int src, auto nc) __attribute__((always_inline)) {
#pragma unroll 1
      for (int spin = 0; spin < SPIN_MAX; ++spin) {
        unsigned mx = 0u;
#pragma unroll
        for (int i = 0; i < decltype(nc)::value; ++i) mx = umax(mx, __builtin_amdgcn_raw_buffer_load_b32(rsh, src, i * (56 * D_ * 4), 16));
        if (none(mx == SENT)) break;
        __builtin_amdgcn_s_sleep(PSLP);
        asm volatile("" ::: "memory");
      }
    };
    if (hw == 2) {
      wait_all(2u);
      if (own) {
        const float rden = __uint_as_float(rmd & 0xffff0000u), rrin = frcp(rden);
        const int col = ow * 16 + (lane & 3) * 4 + (lane >> 4);
        const unsigned xo2 = ld_ag(reinterpret_cast<const unsigned*>(A + (size_t)idx * D_ + (col & ~1)));
        __builtin_amdgcn_s_sleep(PRE1);
        first_words((GRAN_N + (lane < 56 ? lane : 0) * D_ + ow * 16) * 4, std::integral_constant<int, 4>{});
        const float y = y_sum(rsh, std::integral_constant<int, 2>{});
        const unsigned rk = best >> 16;
        const float rv = __uint_as_float(((rk & 0x8000u) ? (rk & 0x7fffu) : (~rk & 0xffffu)) << 16);
        const float rs = rbf(frcp(1.0f + __expf(-rv)));
        const float x1v = __uint_as_float((col & 1) ? xo2 & 0xffff0000u : xo2 << 16);
        float h0 = x1v, h1 = x1v;
        ln_apply2(h0, h1, rmb, rden, rrin);
        const float x2v = rbf(x1v + rbf(h0 + rbf(rbf(y) * rs)));
        const auto pr = __builtin_amdgcn_permlane16_swap(__float_as_uint(x2v), __float_as_uint(x2v), false, false);
        __builtin_amdgcn_raw_buffer_store_b32(f2word(__uint_as_float((pr[0] >> 16) | pr[1])), rsh, (lane & 28) ? HAND_N * 4 : (int)(X2 - hand) * 4 + col * 2, 0, 16);
        const float d = x2v - rmb;
        const float sd = wave_sum(d), sq = wave_sum(d * d);
        if (lane < 2) st_ag(XS + ow * 2 + lane, f2word(lane ? sq : sd));
        STAMP(dk, 5);
      }
    }
    {
      const int ks = kcap ^ (tmi & 8), g4 = lane >> 4;
      const int src = (int)(X2 - hand) * 4 + ks * 128 + g4 * 16;
      int dst = (lane & 12) ? HAND_N * 4 : (int)((P4 - hand) + (tni * 16 + ks) * 192 + (hw ^ (hw < 2 ? ((lane >> 1) ^ tmi) & 1 : 0)) * 64 + (lane & 2) * 16 + (lane & 1) * 8 + (g4 >> 1) * 16 + (g4 & 1) * 4) * 4;
      asm volatile("" : "+v"(dst));
      u32x4 x0, x1;
      if (!(hw == 2 && own)) __builtin_amdgcn_s_sleep(PRE4);
#pragma unroll 1
      for (int spin = 0; spin < SPIN_MAX; ++spin) {
        x0 = __builtin_amdgcn_raw_buffer_load_b128(rsh, src, 0, 16); x1 = __builtin_amdgcn_raw_buffer_load_b128(rsh, src + 64, 0, 16);
        if (none(umax(umax(umax(x0[0], x0[1]), umax(x0[2], x0[3])), umax(umax(x1[0], x1[1]), umax(x1[2], x1[3]))) == SENT)) break;
        __builtin_amdgcn_s_sleep(PSLP);
        asm volatile("" ::: "memory");
      }
      f32x4 d[4];
#pragma unroll
      for (int i = 0; i < 4; ++i) d[i] = MFMA16(sl[2 * i], __builtin_bit_cast(s16x8, x0), (f32x4{0.f, 0.f, 0.f, 0.f}));
#pragma unroll
      for (int i = 0; i < 4; ++i) d[i] = MFMA16(sl[2 * i + 1], __builtin_bit_cast(s16x8, x1), d[i]);
      u32x4 o;
#pragma unroll
      for (int e = 0; e < 4; ++e) o[e] = f2word((lane & 2) ? ((lane & 1) ? d[3][e] : d[2][e]) : ((lane & 1) ? d[1][e] : d[0][e]));
      __builtin_amdgcn_raw_buffer_store_b128(o, rsh, dst, 0, 16);
      PIN();
      if (wid == 8) STAMP(dk, 6);
    }
    if (hw == 2) {
      const int col = (lane & 15) * 4 + (lane >> 4), src = (int)((P4 - hand) + (tni * 16 + (lane >> 4)) * 192 + (lane & 15) * 4) * 4;
      const __amdgpu_buffer_rsrc_t rsq = __builtin_amdgcn_make_buffer_rsrc(hand, 0, fast ? HAND_N * 4 : 0, 0x00020000);
      if (own) {
#pragma unroll
        for (int i = 0; i < 14; ++i)
          __builtin_amdgcn_raw_buffer_store_b128(u32x4{SENT, SENT, SENT, SENT}, rsh, (GRAN_N + (i * 16 + (lane >> 2)) * D_ + ow * 16 + (lane & 3) * 4) * 4, 0, 16);
      }
      float cs[3], val[3];
#pragma unroll
      for (int p = 0; p < 3; ++p) cs[p] = __uint_as_float(ld_ag(reinterpret_cast<const unsigned*>(csf) + p * 1024 + n0 + col));
      u32x2 xs;
      __builtin_amdgcn_s_sleep(PRE7);
      first_words((int)((P4 - hand) + (tni * 16 + (lane & 15)) * 192 + ((lane >> 4) % 3) * 64) * 4, std::integral_constant<int, 1>{});
#pragma unroll 1
      for (int spin = 0; spin < SPIN_MAX; ++spin) {
        u32x4 v[3][4];
        xs = __builtin_amdgcn_raw_buffer_load_b64(rsh, (int)((XS - hand) + lane * 2) * 4, 0, 16);
#pragma unroll
        for (int p = 2; p >= 0; --p)
#pragma unroll
          for (int i = 0; i < 4; ++i) v[p][i] = __builtin_amdgcn_raw_buffer_load_b128(p ? rsh : rsq, src + p * 256, i * (4 * 192 * 4), 16);
        auto part = [&](int p) __attribute__((always_inline)) {
          float z[4];
#pragma unroll
          for (int e = 0; e < 4; ++e) {
            z[e] = fadd(fadd(__uint_as_float(v[p][0][e]), __uint_as_float(v[p][1][e])), fadd(__uint_as_float(v[p][2][e]), __uint_as_float(v[p][3][e])));
          }
          val[p] = fold16(f32x2{z[0], z[1]}, f32x2{z[2], z[3]});
        };
        part(2); part(1);
        val[0] = 0.f;
        if (fast) part(0);
        const float chk = fadd(fadd(fadd(val[0], val[1]), fadd(val[2], __uint_as_float(xs[0]))), __uint_as_float(xs[1]));
        if (none(chk != chk)) break;
        unsigned mx = umax(xs[0], xs[1]);
#pragma unroll
        for (int i = 0; i < 12; ++i) mx = umax(umax(umax(mx, v[i >> 2][i & 3][0]), v[i >> 2][i & 3][1]), umax(v[i >> 2][i & 3][2], v[i >> 2][i & 3][3]));
        if (none(mx == SENT)) break;
        __builtin_amdgcn_s_sleep(PSLP);
        asm volatile("" ::: "memory");
      }
      const auto sw = __builtin_amdgcn_permlane32_swap(xs[0], xs[1], false, false);
      float sm = __uint_as_float(sw[0]) + __uint_as_float(sw[1]);
      sm = dpp_add<0x142, 0xa>(dpp_add<0x118, 0xf>(dpp_add<0x114, 0xf>(dpp_add<0x112, 0xf>(dpp_add<0x111, 0xf>(sm)))));
      const float dm = __int_as_float(__builtin_amdgcn_readlane(__float_as_int(sm), 31)) * (0.25f / 1024.0f);
      const float var = fmaxf(__int_as_float(__builtin_amdgcn_readlane(__float_as_int(sm), 63)) * (0.25f / 1024.0f) - dm * dm, 0.f);
      const float mb = rbf(rmb + dm);
      const float rin = frcp(rbf(__builtin_amdgcn_sqrtf(rbf(rbf(var) + 1e-5f))));
      const unsigned kv = pack2((val[1] - mb * cs[1]) * rin, (val[2] - mb * cs[2]) * rin);
      *LDS_AT(u16, lbase + TAB + 128 + kpos(col) * 2) = (u16)kv;
      *LDS_AT(u16, lbase + TAB + 256 + col * 2) = (u16)(kv >> 16);
      if (fast) *LDS_AT(u16, lbase + TAB + kpos(col) * 2) = f2bf((val[0] - mb * cs[0]) * rin * QSCALE);
      signal(7u);
      STAMP(dk, 7);
      return;
    }
    if (!fast) return;
    const int head = tni * 2 + hw;
    const unsigned tb = lbase + TAB + hw * 64;
    {
      const __amdgpu_buffer_rsrc_t rs = __builtin_amdgcn_make_buffer_rsrc(KV, 0, 2 * S_ * D_ * 2, 0x00020000);
      const int src = (head * 64 + tmi * 4) * 2048 + lane * 16;
      auto ld = [&](int i, unsigned off) { return __builtin_bit_cast(s16x8, __builtin_amdgcn_raw_buffer_load_b128(rs, src + (i & 3) * 1024, (i >> 3) * 65536 + ((i >> 2) & 1) * 4096 + off, 16)); };
#pragma unroll 1
      for (int i = 0; i < SPIN_MAX; ++i)
        if (none(ld_ag(kvflag + ((tmi & ~1) + hw) * 32 + tmi + (lane & 8)) != tag)) break;
      asm volatile("" ::: "memory");
      s16x8 f[16];
#pragma unroll
      for (int i = 0; i < 16; ++i) f[i] = ld(i, 0u);
      PIN();
      const bool on = ((lane ^ (lane >> 4)) & 1) == 0;
      const unsigned pl = lbase + RED + hw * 1024;
      unsigned pw = pl + (lane >> 4) * 64 + (lane & 15) * 2, pr = pl + (on ? (lane >> 5) * 16 : 512), qr = on ? tb + (lane >> 5) * 16 : pl + 512;
      const int gone = (((idx >> 7) & 7) == tmi) ? ((idx >> 10) * 2 + (kpos(idx & 31) >> 4)) * 64 + ((idx >> 5) & 3) * 16 + (kpos(idx & 31) & 15) : -1;
      asm volatile("" : "+v"(pw), "+v"(pr), "+v"(qr));
      *LDS_AT(u32x2, pl + 512 + lane * 8) = u32x2{0u, 0u};
      wait_all(7u);
      u32x4 q[2];
#pragma unroll
      for (int sx = 0; sx < 2; ++sx) {
        q[sx] = *LDS_AT(const u32x4, qr + sx * 32);
      }
      float sc[4];
#pragma unroll
      for (int i = 0; i < 8; ++i) {
        f32x4 d = MFMA16(__builtin_bit_cast(s16x8, q[0]), f[2 * i], (f32x4{0.f, 0.f, 0.f, 0.f}));
        d = MFMA16(__builtin_bit_cast(s16x8, q[1]), f[2 * i + 1], d);
        PIN();
        f[2 * i] = ld(2 * i, VOFF); f[2 * i + 1] = ld(2 * i + 1, VOFF);
        PIN();
#pragma unroll
        for (int r = 0; r < 2; ++r) sc[(i >> 2) * 2 + r] = (i & 3) == 0 || (lane >> 4) == (i & 3) ? d[r] : sc[(i >> 2) * 2 + r];
      }
      if (gone >= 0) {
#pragma unroll
        for (int r = 0; r < 4; ++r) sc[r] = r * 64 + lane == gone ? -INFINITY : sc[r];
      }
      const float mx = wave_fmax(fmaxf(fmaxf(sc[0], sc[1]), fmaxf(sc[2], sc[3])));
      const unsigned p0 = pack2(__builtin_amdgcn_exp2f(sc[0] - mx), __builtin_amdgcn_exp2f(sc[1] - mx)), p1 = pack2(__builtin_amdgcn_exp2f(sc[2] - mx), __builtin_amdgcn_exp2f(sc[3] - mx));
      *LDS_AT(u16, pw) = (u16)p0; *LDS_AT(u16, pw + 32) = (u16)(p0 >> 16);
      *LDS_AT(u16, pw + 256) = (u16)p1; *LDS_AT(u16, pw + 288) = (u16)(p1 >> 16);
      asm volatile("" ::: "memory");
      const float ls = wave_sum((__uint_as_float(p0 << 16) + __uint_as_float(p0 & 0xffff0000u)) + (__uint_as_float(p1 << 16) + __uint_as_float(p1 & 0xffff0000u)));
      f32x4 o = {0.f, 0.f, 0.f, 0.f};
#pragma unroll
      for (int i = 0; i < 16; ++i) o = MFMA16(*LDS_AT(const s16x8, pr + i * 32), f[i], o);
      int pv = __float_as_int((lane & 16) ? o[1] : o[0]);
      asm volatile("s_nop 0\n\tv_writelane_b32 %0, %1, 32\n\tv_writelane_b32 %0, %2, 33" : "+v"(pv) : "s"(ls), "s"(mx));
      __builtin_amdgcn_raw_buffer_store_b32(f2word(__int_as_float(pv)), rsh, lane < 34 ? (int)((PA - hand) + (head * 8 + tmi) * 64 + lane) * 4 : HAND_N * 4, 0, 16);
    }
    if (wid == 8) STAMP(dk, 8);
    if (tmi != ((idx >> 7) & 7)) return;
    {
      const int src = (int)((PA - hand) + head * 512 + (lane < 34 ? lane : 0)) * 4;
      const unsigned ta = tb + (lane & 31) * 2;
      const float so = wave_sum(lane < 32 ? bf2f(*LDS_AT(const u16, ta)) * bf2f(*LDS_AT(const u16, ta + 128)) : 0.f);
      const float vo = lane < 32 ? bf2f(*LDS_AT(const u16, ta + 256)) : 1.0f;
      unsigned v[8];
      __builtin_amdgcn_s_sleep(PRE5);
#pragma unroll 1
      for (int spin = 0; spin < SPIN_MAX; ++spin) {
        unsigned mx = 0u;
#pragma unroll
        for (int i = 0; i < 8; ++i) v[i] = __builtin_amdgcn_raw_buffer_load_b32(rsh, src + i * 256, 0, 16);
#pragma unroll
        for (int i = 0; i < 8; ++i) mx = umax(mx, v[i]);
        if (none(mx == SENT)) break;
        __builtin_amdgcn_s_sleep(PSLP);
        asm volatile("" ::: "memory");
      }
      float m8[8], mx = so;
#pragma unroll
      for (int i = 0; i < 8; ++i) { m8[i] = __int_as_float(__builtin_amdgcn_readlane((int)v[i], 33)); mx = fmaxf(mx, m8[i]); }
      float acc = __builtin_amdgcn_exp2f(so - mx) * vo;
#pragma unroll
      for (int i = 0; i < 8; ++i) acc += __builtin_amdgcn_exp2f(m8[i] - mx) * __uint_as_float(v[i]);
      const float l = __int_as_float(__builtin_amdgcn_readlane(__float_as_int(acc), 32));
      const float ov = acc / l;
      const float o1 = __int_as_float(__builtin_amdgcn_update_dpp(0, __float_as_int(ov), 0x55, 0xf, 0xf, false));
      const float o2 = __int_as_float(__builtin_amdgcn_update_dpp(0, __float_as_int(ov), 0xaa, 0xf, 0xf, false));
      const float o3 = __int_as_float(__builtin_amdgcn_update_dpp(0, __float_as_int(ov), 0xff, 0xf, 0xf, false));
      const __amdgpu_buffer_rsrc_t rso = __builtin_amdgcn_make_buffer_rsrc(Of, 0, S_ * D_ * 2, 0x00020000);
      if (lane < 32 && (lane & 3) == 0)
        __builtin_amdgcn_raw_buffer_store_b64(u32x2{pack2(ov, o1), pack2(o2, o3)}, rso,
            (((idx >> 5) * 32 + head) * 1024 + (lane >> 4) * 512 + (((lane >> 2) & 1) * 32 + (idx & 31)) * 8 + ((lane >> 3) & 1) * 4) * 2, 0, 16);
      asm volatile("s_waitcnt vmcnt(0)" ::: "memory");
      if (lane < RO) st_ag(gran + GR_O + ((16 + (idx >> 7)) * RO + lane) * 64 + 32 + head, tag);
      STAMP(dk, 9);
    }
    }();
    }();
    __builtin_amdgcn_s_setprio(0);
    if (LAY2 || hw != UHW) asm volatile("s_waitcnt vmcnt(0)" ::: "memory");
    PIN();
    ARGS_HERE;
    u32x2 w[16];
    auto ld_wd = [&]() __attribute__((always_inline)) {
      const __amdgpu_buffer_rsrc_t rd_ = __builtin_amdgcn_make_buffer_rsrc(const_cast<u16*>(wd), 0, D_ * F_ * 2, 0x00020000);
#pragma unroll
      for (int f = 0; f < 16; ++f) w[f] = __builtin_amdgcn_raw_buffer_load_b64(rd_, t * 8 + (f & 1) * 2048, (fg * 16 + (f & ~1)) * (D_ * 2), 0);
      PIN();
    };
    float fnum = 0.f, fvar = 0.f;
    const int rrow = omi * 128 + (oni >> 3) * 64 + (hw & 1) * 32;
    if (LAY2) {
      ld_wd();
      if (!p1blk) {
        const int fr = rrow + (lane & 7) * 4 + (lane >> 4);
        const double s1 = ld_ag(stats + fr), s2 = ld_ag(stats + S_ + fr), sw = ld_ag(stats + 2 * S_ + fr);
        lds_wr(lbase + SPM + 132, __float_as_uint(wsum));
        float mean, var, mb, den;
        ln_from_sums(s1, s2, mean, var, mb, den);
        const float g1 = 1.0f + frcp(den);
        fnum = fr == idx ? __uint_as_float(0x7fc00000u) : fmaf(-mean, wsum, (float)sw) * g1;
        fvar = var * g1 * g1;
      }
      asm volatile("" : "+v"(fnum), "+v"(fvar));
      PIN();
    }
    if (hw == 3) {
      const int nfl = (LAY2 && (idx >> 7) == omi) ? 64 : 32;
      auto pfl = (__attribute__((address_space(1))) const unsigned*)(gran + GR_O + (((LAY2 ? 16 : 0) + omi) * RO + oni % RO) * 64 + (lane < nfl ? lane : 0));
      constexpr unsigned vs = 8u * D_ * 2u - 1024u;
      unsigned vw0 = (unsigned)((lane >> 3) * D_ + oni * 64 + ((lane & 7) ^ ((lane >> 3) & 6)) * 8) * 2u, vw1 = vw0 + vs, vw2 = vw0 + 2 * vs, vw3 = vw0 + 3 * vs;
      asm volatile("" : "+v"(pfl), "+v"(vw0), "+v"(vw1), "+v"(vw2), "+v"(vw3));
      auto wait_arr = [&](unsigned n) {
#pragma unroll 1
        for (int i = 0; i < 4 * SPIN_MAX; ++i) {
          if (__builtin_amdgcn_readfirstlane(*LDS_AT(const volatile unsigned, lbase + FLG + 16)) >= n) break;
          __builtin_amdgcn_s_sleep(1);
        }
        asm volatile("" ::: "memory");
        PIN();
      };
      wait_arr(12u);
      unsigned fl;
      asm volatile("global_load_dword %0, %1, off sc1" : "=v"(fl) : "v"(pfl) : "memory");
#pragma unroll 1
      for (int i = 0; i < 4 * SPIN_MAX; ++i) {
        if (__builtin_amdgcn_readfirstlane(*LDS_AT(const volatile unsigned char, lbase + FLG + 24)) >= 8u) break;
        __builtin_amdgcn_s_sleep(1);
      }
      asm volatile("" ::: "memory");
      PIN();
#pragma unroll
      for (int j = 12; j < 20; ++j)
        glds16x4<false>(reinterpret_cast<const char*>(wo) + j * (32u * D_ * 2u), vw0, vw1, vw2, vw3, lbase + j * 4096);
      unsigned keep;
      asm volatile("s_mov_b32 %0, m0\n\ts_mov_b32 m0, %3\n\ts_nop 2\n\tglobal_load_lds_dword %1, %2 sc1\n\ts_mov_b32 m0, %0"
                   : "=&s"(keep) : "v"(0u), "s"(reinterpret_cast<const char*>(gran + GR_N + bid * 64 + 39)), "s"(lbase + FLG - 252) : "memory");
      asm volatile("s_waitcnt vmcnt(33)" : "+v"(fl) :: "memory");
      if (__builtin_amdgcn_ballot_w64(fl != tag)) {
        __builtin_amdgcn_s_sleep(CSL1);
#pragma unroll 1
        for (int spin = 0; spin < SPIN_MAX; ++spin) {
          if (!__builtin_amdgcn_ballot_w64(__hip_atomic_load(pfl, __ATOMIC_RELAXED, __HIP_MEMORY_SCOPE_AGENT) != tag)) break;
          __builtin_amdgcn_s_sleep(CSLP);
        }
      }
      asm volatile("" ::: "memory");
      PIN();
    }
    u32x4 pv[4];
    asm volatile("" : "=v"(pv[0]), "=v"(pv[1]), "=v"(pv[2]), "=v"(pv[3]));
    if (LAY2 && !p1blk && hw < 2) {
      const __amdgpu_buffer_rsrc_t rsp = __builtin_amdgcn_make_buffer_rsrc(hand + 2 * HAND_N + PP_O, 0, H_ * S_ * 4, 0x00020000);
      const int src = ((lane >> 3) * S_ + rrow + (lane & 7) * 4) * 4;
#pragma unroll 1
      for (int i = 0; i < 4 * SPIN_MAX; ++i) {
        if (__builtin_amdgcn_readfirstlane(*LDS_AT(const volatile unsigned char, lbase + FLG + 24)) >= 8u) break;
        __builtin_amdgcn_s_sleep(2);
      }
#pragma unroll 1
      for (int spin = 0; spin < SPIN_MAX; ++spin) {
        asm volatile("" ::: "memory");
        unsigned mx = 0u;
#pragma unroll
        for (int i = 0; i < 4; ++i) pv[i] = __builtin_amdgcn_raw_buffer_load_b128(rsp, src, i * (8 * S_ * 4), 16);
#pragma unroll
        for (int i = 0; i < 4; ++i) mx = umax(umax(umax(mx, pv[i][0]), pv[i][1]), umax(pv[i][2], pv[i][3]));
        if (none(mx == SENT)) break;
        __builtin_amdgcn_s_sleep(PPSLP);
      }
    }
    asm volatile("s_waitcnt lgkmcnt(0)" ::: "memory");
    __builtin_amdgcn_s_barrier();
    if (!LAY2) {
      PIN();
      asm volatile(AGAIN "\ngate_up_code:\n\t"
                 "v_lshlrev_b32 v84, 16, v100\n\tv_and_b32 v85, 0xffff0000, v100\n\tv_lshlrev_b32 v86, 16, v101\n\tv_and_b32 v87, 0xffff0000, v101\n\t"
                 "v_subrev_f32 v84, s84, v84\n\tv_subrev_f32 v85, s84, v85\n\tv_subrev_f32 v86, s84, v86\n\tv_subrev_f32 v87, s84, v87\n\t"
                 "v_cvt_pk_bf16_f32 v85, v84, v85\n\tv_cvt_pk_bf16_f32 v87, v86, v87\n\ts_mov_b32 s88, 0x01000100\n\ts_mov_b32 s89, 0x03020302\n\t"
                 "v_lshlrev_b32 v84, 16, v85\n\tv_and_b32 v85, 0xffff0000, v85\n\tv_lshlrev_b32 v86, 16, v87\n\tv_and_b32 v87, 0xffff0000, v87\n\t"
                 "v_mul_f32 v88, s86, v84\n\tv_mul_f32 v89, s86, v85\n\tv_mul_f32 v90, s86, v86\n\tv_mul_f32 v91, s86, v87\n\tv_fma_f32 v84, -v88, s85, v84\n\t"
                 "v_fma_f32 v85, -v89, s85, v85\n\tv_fma_f32 v86, -v90, s85, v86\n\tv_fma_f32 v87, -v91, s85, v87\n\tv_fmac_f32 v88, s86, v84\n\t"
                 "v_fmac_f32 v89, s86, v85\n\tv_fmac_f32 v90, s86, v86\n\tv_fmac_f32 v91, s86, v87\n\tv_cvt_pk_bf16_f32 v88, v88, v89\n\t"
                 "v_cvt_pk_bf16_f32 v90, v90, v91\n\tv_perm_b32 v92, v88, v88, s88\n\tv_perm_b32 v93, v88, v88, s89\n\tv_perm_b32 v94, v90, v90, s88\n\t"
                 "v_perm_b32 v95, v90, v90, s89\n\ts_nop 0\n\tv_and_b32_dpp v96, v92, v102 quad_perm:[0,0,0,0] row_mask:0xf bank_mask:0xf\n\t"
                 "v_and_b32_dpp v97, v92, v103 quad_perm:[0,0,0,0] row_mask:0xf bank_mask:0xf\n\t"
                 "v_and_b32_dpp v100, v93, v102 quad_perm:[0,0,0,0] row_mask:0xf bank_mask:0xf\n\t"
                 "v_and_b32_dpp v101, v93, v103 quad_perm:[0,0,0,0] row_mask:0xf bank_mask:0xf\n\tv_mfma_f32_4x4x4_16b_bf16 v[84:87], v[96:97], v[104:105], 0\n\t"
                 "v_mfma_f32_4x4x4_16b_bf16 v[88:91], v[96:97], v[106:107], 0\n\tv_and_b32_dpp v96, v94, v102 quad_perm:[0,0,0,0] row_mask:0xf bank_mask:0xf\n\t"
                 "v_and_b32_dpp v97, v94, v103 quad_perm:[0,0,0,0] row_mask:0xf bank_mask:0xf\n\t"
                 "v_mfma_f32_4x4x4_16b_bf16 v[84:87], v[100:101], v[108:109], v[84:87]\n\tv_mfma_f32_4x4x4_16b_bf16 v[88:91], v[100:101], v[110:111], v[88:91]\n\t"
                 "v_and_b32_dpp v100, v95, v102 quad_perm:[0,0,0,0] row_mask:0xf bank_mask:0xf\n\t"
                 "v_and_b32_dpp v101, v95, v103 quad_perm:[0,0,0,0] row_mask:0xf bank_mask:0xf\n\tv_mfma_f32_4x4x4_16b_bf16 v[84:87], v[96:97], v[112:113], v[84:87]\n\t"
                 "v_mfma_f32_4x4x4_16b_bf16 v[88:91], v[96:97], v[114:115], v[88:91]\n\tv_and_b32_dpp v96, v92, v102 quad_perm:[1,1,1,1] row_mask:0xf bank_mask:0xf\n\t"
                 "v_and_b32_dpp v97, v92, v103 quad_perm:[1,1,1,1] row_mask:0xf bank_mask:0xf\n\t"
                 "v_mfma_f32_4x4x4_16b_bf16 v[84:87], v[100:101], v[116:117], v[84:87]\n\tv_mfma_f32_4x4x4_16b_bf16 v[88:91], v[100:101], v[118:119], v[88:91]\n\t"
                 "v_and_b32_dpp v100, v93, v102 quad_perm:[1,1,1,1] row_mask:0xf bank_mask:0xf\n\t"
                 "v_and_b32_dpp v101, v93, v103 quad_perm:[1,1,1,1] row_mask:0xf bank_mask:0xf\n\tv_mfma_f32_4x4x4_16b_bf16 v[84:87], v[96:97], v[120:121], v[84:87]\n\t"
                 "v_mfma_f32_4x4x4_16b_bf16 v[88:91], v[96:97], v[122:123], v[88:91]\n\tv_and_b32_dpp v96, v94, v102 quad_perm:[1,1,1,1] row_mask:0xf bank_mask:0xf\n\t"
                 "v_and_b32_dpp v97, v94, v103 quad_perm:[1,1,1,1] row_mask:0xf bank_mask:0xf\n\t"
                 "v_mfma_f32_4x4x4_16b_bf16 v[84:87], v[100:101], v[124:125], v[84:87]\n\tv_mfma_f32_4x4x4_16b_bf16 v[88:91], v[100:101], v[126:127], v[88:91]\n\t"
                 "v_and_b32_dpp v100, v95, v102 quad_perm:[1,1,1,1] row_mask:0xf bank_mask:0xf\n\t"
                 "v_and_b32_dpp v101, v95, v103 quad_perm:[1,1,1,1] row_mask:0xf bank_mask:0xf\n\tv_mfma_f32_4x4x4_16b_bf16 v[84:87], v[96:97], v[128:129], v[84:87]\n\t"
                 "v_mfma_f32_4x4x4_16b_bf16 v[88:91], v[96:97], v[130:131], v[88:91]\n\tv_and_b32_dpp v96, v92, v102 quad_perm:[2,2,2,2] row_mask:0xf bank_mask:0xf\n\t"
                 "v_and_b32_dpp v97, v92, v103 quad_perm:[2,2,2,2] row_mask:0xf bank_mask:0xf\n\t"
                 "v_mfma_f32_4x4x4_16b_bf16 v[84:87], v[100:101], v[132:133], v[84:87]\n\tv_mfma_f32_4x4x4_16b_bf16 v[88:91], v[100:101], v[134:135], v[88:91]\n\t"
                 "v_and_b32_dpp v100, v93, v102 quad_perm:[2,2,2,2] row_mask:0xf bank_mask:0xf\n\t"
                 "v_and_b32_dpp v101, v93, v103 quad_perm:[2,2,2,2] row_mask:0xf bank_mask:0xf\n\tv_mfma_f32_4x4x4_16b_bf16 v[84:87], v[96:97], v[136:137], v[84:87]\n\t"
                 "v_mfma_f32_4x4x4_16b_bf16 v[88:91], v[96:97], v[138:139], v[88:91]\n\tv_and_b32_dpp v96, v94, v102 quad_perm:[2,2,2,2] row_mask:0xf bank_mask:0xf\n\t"
                 "v_and_b32_dpp v97, v94, v103 quad_perm:[2,2,2,2] row_mask:0xf bank_mask:0xf\n\t"
                 "v_mfma_f32_4x4x4_16b_bf16 v[84:87], v[100:101], v[140:141], v[84:87]\n\tv_mfma_f32_4x4x4_16b_bf16 v[88:91], v[100:101], v[142:143], v[88:91]\n\t"
                 "v_and_b32_dpp v100, v95, v102 quad_perm:[2,2,2,2] row_mask:0xf bank_mask:0xf\n\t"
                 "v_and_b32_dpp v101, v95, v103 quad_perm:[2,2,2,2] row_mask:0xf bank_mask:0xf\n\tv_mfma_f32_4x4x4_16b_bf16 v[84:87], v[96:97], v[144:145], v[84:87]\n\t"
                 "v_mfma_f32_4x4x4_16b_bf16 v[88:91], v[96:97], v[146:147], v[88:91]\n\tv_and_b32_dpp v96, v92, v102 quad_perm:[3,3,3,3] row_mask:0xf bank_mask:0xf\n\t"
                 "v_and_b32_dpp v97, v92, v103 quad_perm:[3,3,3,3] row_mask:0xf bank_mask:0xf\n\t"
                 "v_mfma_f32_4x4x4_16b_bf16 v[84:87], v[100:101], v[148:149], v[84:87]\n\tv_mfma_f32_4x4x4_16b_bf16 v[88:91], v[100:101], v[150:151], v[88:91]\n\t"
                 "v_and_b32_dpp v100, v93, v102 quad_perm:[3,3,3,3] row_mask:0xf bank_mask:0xf\n\t"
                 "v_and_b32_dpp v101, v93, v103 quad_perm:[3,3,3,3] row_mask:0xf bank_mask:0xf\n\tv_mfma_f32_4x4x4_16b_bf16 v[84:87], v[96:97], v[152:153], v[84:87]\n\t"
                 "v_mfma_f32_4x4x4_16b_bf16 v[88:91], v[96:97], v[154:155], v[88:91]\n\tv_and_b32_dpp v96, v94, v102 quad_perm:[3,3,3,3] row_mask:0xf bank_mask:0xf\n\t"
                 "v_and_b32_dpp v97, v94, v103 quad_perm:[3,3,3,3] row_mask:0xf bank_mask:0xf\n\t"
                 "v_mfma_f32_4x4x4_16b_bf16 v[84:87], v[100:101], v[156:157], v[84:87]\n\tv_mfma_f32_4x4x4_16b_bf16 v[88:91], v[100:101], v[158:159], v[88:91]\n\t"
                 "v_and_b32_dpp v100, v95, v102 quad_perm:[3,3,3,3] row_mask:0xf bank_mask:0xf\n\t"
                 "v_and_b32_dpp v101, v95, v103 quad_perm:[3,3,3,3] row_mask:0xf bank_mask:0xf\n\tv_mfma_f32_4x4x4_16b_bf16 v[84:87], v[96:97], v[160:161], v[84:87]\n\t"
                 "v_mfma_f32_4x4x4_16b_bf16 v[88:91], v[96:97], v[162:163], v[88:91]\n\ts_nop 1\n\t"
                 "v_mfma_f32_4x4x4_16b_bf16 v[84:87], v[100:101], v[164:165], v[84:87]\n\tv_mfma_f32_4x4x4_16b_bf16 v[88:91], v[100:101], v[166:167], v[88:91]\n\t"
                 "s_nop 4\n\tv_permlane32_swap_b32 v84, v88\n\tv_permlane32_swap_b32 v85, v89\n\tv_permlane32_swap_b32 v86, v90\n\tv_permlane32_swap_b32 v87, v91\n\t"
                 "v_add_f32 v84, v84, v88\n\tv_add_f32 v85, v85, v89\n\tv_add_f32 v86, v86, v90\n\tv_add_f32 v87, v87, v91\n\ts_nop 0\n\t"
                 "v_permlane16_swap_b32 v84, v85\n\tv_permlane16_swap_b32 v86, v87\n\ts_nop 0\n\tv_add_f32 v84, v84, v85\n\tv_add_f32 v86, v86, v87\n\ts_nop 1\n\t"
                 "v_add_f32_dpp v84, v84, v84 row_ror:8 row_mask:0xf bank_mask:0xf\n\tv_add_f32_dpp v86, v86, v86 row_ror:8 row_mask:0xf bank_mask:0xf\n\ts_nop 0\n\t"
                 "v_add_f32_dpp v84, v84, v84 row_ror:4 row_mask:0xf bank_mask:0xf\n\tv_add_f32_dpp v86, v86, v86 row_ror:4 row_mask:0xf bank_mask:0xf\n\t"
                 "ds_write2_b32 v99, v84, v86 offset1:8\n\ts_setpc_b64 s[92:93]\n\t"
                   :: "{s[0:1]}"(ap + 1), "{s2}"(bid), "{v0}"(wid * 64), KEPT() : "s90", "s91", "scc", "memory");
      __builtin_unreachable();
    }
    {
    constexpr int dk = 8;
    constexpr int BEST = FLG + 40, MEET = FLG + 48;
    __builtin_amdgcn_s_setprio(HPRIO);
    if (hw == 0) STAMP(dk, 0);
    const unsigned tb1 = (unsigned)__builtin_amdgcn_s_memrealtime();
    unsigned* hand2 = hand + HAND_N;
    const __amdgpu_buffer_rsrc_t rsh = __builtin_amdgcn_make_buffer_rsrc(hand2, 0, (HAND_N + PS_N) * 4, 0x00020000);
    const __amdgpu_buffer_rsrc_t rsx = __builtin_amdgcn_make_buffer_rsrc(xout, 0, 3 * D_ * 2, 0x00020000);
    constexpr int OOR = (HAND_N + PS_N) * 4;
    if (!p1blk && hw < 2) {
      f32x2 a[2], c[2];
#pragma unroll
      for (int e = 0; e < 4; ++e) {
        a[e >> 1][e & 1] = (__uint_as_float(pv[0][e] << 16) + __uint_as_float(pv[1][e] << 16)) + (__uint_as_float(pv[2][e] << 16) + __uint_as_float(pv[3][e] << 16));
        c[e >> 1][e & 1] = (__uint_as_float(pv[0][e] & 0xffff0000u) + __uint_as_float(pv[1][e] & 0xffff0000u)) + (__uint_as_float(pv[2][e] & 0xffff0000u) + __uint_as_float(pv[3][e] & 0xffff0000u));
      }
      const float r = (fnum + fold8(a[0], a[1])) * __builtin_amdgcn_rsqf(fvar + fold8(c[0], c[1]));
      unsigned key = (__float_as_uint(r) ^ ((unsigned)(__float_as_int(r) >> 31) | 0x80000000u)) & 0xfffff800u;
      if (r != r) key = 0u;
      key = wave_umax(key | (unsigned)(2047 - (rrow + (lane & 7) * 4 + (lane >> 4))));
      __builtin_amdgcn_raw_buffer_store_b64(u32x2{key, tb1 & 0x7fffffffu}, rsh, lane < NREP ? (HAND_N + PK_O + lane * 128 + (rrow >> 5) * 2) * 4 : OOR, 0, 16);
      if (hw == 1) STAMP(dk, 2);
    }
    gu_setup();
    bool got = true;
    auto get_fc = [&]() __attribute__((always_inline)) {
      const u32x2 fw = __builtin_amdgcn_raw_buffer_load_b64(rsh, (HAND_N + PK_O + (bid % NREP) * 128 + lane * 2) * 4, 0, 16);
      got = none(umax(fw[0], fw[1]) == SENT);
      if (got) {
        const unsigned c1 = wave_umax(fw[0]), c2 = wave_umax(fw[0] == c1 ? 0u : fw[0]);
#define BADFC 0
        const unsigned r1 = (2047u - (c1 & 0x7ffu)) ^ BADFC, r2 = 2047u - (c2 & 0x7ffu);
        unsigned dt = (unsigned)((int)((fw[1] - tb1) << 1) >> 1) + 512u;
        dt = dt < 1024u ? dt : 512u;
        *LDS_AT(volatile u32x2, lbase + SPM + 144) = u32x2{r1 | (r2 << 16), (unsigned)__builtin_amdgcn_readlane(dt, r1 >> 5) | (wave_umax(dt) << 16)};
      }
    };
    if (hw == 0) {
      __builtin_amdgcn_s_sleep(FCPRE);
#pragma unroll 1
      for (int i = 0; i < SPIN_MAX; ++i) {
        if (__builtin_amdgcn_readfirstlane(*LDS_AT(const volatile unsigned, lbase + FLG + 28)) - best >= 4u) break;
        __builtin_amdgcn_s_sleep(FCSLP);
      }
      asm volatile("" ::: "memory");
      get_fc();
    } else __builtin_amdgcn_s_sleep(TPRE - 10);
    PIN();
    tile_at(2u);
    const unsigned tb2 = (unsigned)__builtin_amdgcn_s_memrealtime() - 512u;
    LOGDEF;
    LOG();
#pragma unroll 1
    for (int spin = 0; spin < SPIN_MAX && !got; ++spin) { get_fc(); asm volatile("" ::: "memory"); }
    PIN();
    u32x2 fm = {SENT, 0u};
#pragma unroll 1
    for (int i = 0; i < 4 * SPIN_MAX && fm[0] == SENT; ++i) {
      const u32x2 q = *LDS_AT(const volatile u32x2, lbase + SPM + 144);
      fm = u32x2{(unsigned)__builtin_amdgcn_readfirstlane(q[0]), (unsigned)__builtin_amdgcn_readfirstlane(q[1])};
    }
    const int fr1 = fm[0] & 0x7ffu, fr2 = (fm[0] >> 16) & 0x7ffu;
    const unsigned t1 = tb2 + (fm[1] & 1023u), tmx = tb2 + ((fm[1] >> 16) & 1023u);
    auto until = [&](unsigned te) { while ((int)((unsigned)__builtin_amdgcn_s_memrealtime() - te) < 0) __builtin_amdgcn_s_sleep(1); asm volatile("" ::: "memory"); };
    lds_wr(lbase + SPM + 136, tmx);
    PIN();
    if (hw == 0) STAMP(dk, 3);
    const int xoff = (hw * 256 + (lane & 3) * 64 + (lane >> 2) * 4) * 2;
    const int oc = (omi * 2 + (oni >> 3)) * 32, col = oc + (lane & 7) * 4 + (lane >> 4);
    const int xoo = !p1blk && hw == 3 ? (col & ~1) * 2 : S_ * D_ * 2;
    auto get_row = [&](int r, int sl, u32x2& xr, unsigned& xo) __attribute__((always_inline)) {
      if ((r >> 7) == omi) {
#pragma unroll 1
        for (int i = 0; i < SPIN_MAX; ++i) {
          if (__builtin_amdgcn_readfirstlane(*LDS_AT(const volatile unsigned char, lbase + FLG + 24)) >= 16u) break;
          __builtin_amdgcn_s_sleep(1);
        }
        asm volatile("" ::: "memory");
      }
#pragma unroll 1
      for (int spin = 0; spin < SPIN_MAX; ++spin) {
        LOG();
        xr = __builtin_amdgcn_raw_buffer_load_b64(rsx, xoff, sl * (D_ * 2), 16);
        xo = __builtin_amdgcn_raw_buffer_load_b32(rsx, xoo, sl * (D_ * 2), 16);
        LOG();
        if (none(umax(umax(xr[0], xr[1]), xo) == SENT)) break;
        __builtin_amdgcn_s_sleep(XRSLP);
        asm volatile("" ::: "memory");
      }
      PIN();
    };
    auto row_md = [&](const u32x2& xr, int k) __attribute__((always_inline)) {
      auto dot = [&](unsigned x, unsigned y, float z) { return __builtin_amdgcn_fdot2_f32_bf16(__builtin_bit_cast(bf16x2, x), __builtin_bit_cast(bf16x2, y), z, false); };
      const float p1 = dot(xr[1], 0x3f803f80u, dot(xr[0], 0x3f803f80u, 0.f)), p2 = dot(xr[1], xr[1], dot(xr[0], xr[0], 0.f));
      const auto q = __builtin_amdgcn_permlane32_swap(__float_as_uint(p1), __float_as_uint(p2), false, false);
      const float qs = __uint_as_float(q[0]) + __uint_as_float(q[1]);
      const auto q2 = __builtin_amdgcn_permlane16_swap(__float_as_uint(qs), __float_as_uint(qs), false, false);
      float sv = __uint_as_float(q2[0]) + __uint_as_float(q2[1]);
      sv = dpp_add<0x121, 0xf>(dpp_add<0x122, 0xf>(dpp_add<0x124, 0xf>(dpp_add<0x128, 0xf>(sv))));
      lds_wr(lbase + SPM + k * 64 + (lane >> 4) * 16 + hw * 4, f2word(sv));
      u32x4 a, c;
#pragma unroll 1
      for (int i = 0; i < SPIN_MAX; ++i) {
        a = lds_rd4(lbase + SPM + k * 64); c = lds_rd4(lbase + SPM + k * 64 + 32);
        if (__builtin_amdgcn_readfirstlane(umax(umax(a[0], a[1]), umax(a[2], a[3]))) != SENT) break;
      }
      auto sum4 = [&](const u32x4& v) { return (__uint_as_float(v[0]) + __uint_as_float(v[1])) + (__uint_as_float(v[2]) + __uint_as_float(v[3])); };
      const float mean = sum4(a) * (1.0f / 1024.0f), msq = sum4(c) * (1.0f / 1024.0f);
      const float den = rbf(__builtin_amdgcn_sqrtf(rbf(rbf(fmaxf(fmaf(-mean, mean, msq), 0.f)) + 1e-5f)));
      return (unsigned)__builtin_amdgcn_readfirstlane((pack2(mean, mean) & 0xffffu) | (__float_as_uint(den) & 0xffff0000u));
    };
    auto ffn = [&](const u32x2& xr, unsigned md, unsigned n, const __amdgpu_buffer_rsrc_t rs, int yo, auto&& off) __attribute__((always_inline)) {
      gate_up(xr, md);
      if (hw == 0 && n == 2u) STAMP(dk, 8);
      asm volatile("" ::: "memory");
      PIN();
      lds_wr(lbase + MEET + hw * 4, n + off());
      u32x4 mt;
#pragma unroll 1
      for (int i = 0; i < SPIN_MAX; ++i) {
        mt = lds_rd4(lbase + MEET);
        if (__builtin_amdgcn_readfirstlane(min4(mt)) >= n) break;
      }
      asm volatile("" ::: "memory");
      PIN();
      if (hw == 0 && n == 2u) STAMP(dk, 9);
      if (__builtin_amdgcn_readfirstlane(mt[0]) != n + 1u)
        __builtin_amdgcn_raw_buffer_store_b128(down(act_diag(n == 4u ? 512 : 0), w), rs, (yo + fg * D_) * 4 + t * 16, 0, 16);
      dad += n == 2u ? 512 : -512;
      if (hw == 0 && n == 2u) STAMP(dk, 4);
    };
    auto y_sum4 = [&](const __amdgpu_buffer_rsrc_t rs, int yo, int k) __attribute__((always_inline)) {
      const int src = (yo + (hw * 56 + (lane >> 3)) * D_ + oc + (lane & 7) * 4) * 4;
      u32x4 v[7];
#pragma unroll
      for (int i = 0; i < 7; ++i) asm volatile("" : "=v"(v[i]));
      unsigned todo = 0x7fu;
#pragma unroll 1
      for (int spin = 0; spin < SPIN_MAX; ++spin) {
        LOG();
#pragma unroll
        for (int i = 0; i < 7; ++i) if ((todo >> i) & 1u) v[i] = __builtin_amdgcn_raw_buffer_load_b128(rs, src, i * (8 * D_ * 4), 16);
#pragma unroll
        for (int i = 0; i < 7; ++i) if (none(umax(umax(v[i][0], v[i][1]), umax(v[i][2], v[i][3])) == SENT)) todo &= ~(1u << i);
        if (!todo) break;
        __builtin_amdgcn_s_sleep(YSLP);
        asm volatile("" ::: "memory");
      }
      f32x2 z0 = {0.f, 0.f}, z1 = {0.f, 0.f};
#pragma unroll
      for (int i = 0; i < 7; ++i) {
        z0 += f32x2{__uint_as_float(v[i][0]), __uint_as_float(v[i][1])};
        z1 += f32x2{__uint_as_float(v[i][2]), __uint_as_float(v[i][3])};
      }
      const unsigned ya = lbase + SPM + 256 + k * 512 + ((lane & 7) * 4 + (lane >> 4)) * 16;
      lds_wr((lane & 8) ? lbase + DUM + lane * 4 : ya + hw * 4, f2word(fold8(z0, z1)));
      u32x4 q = {0u, 0u, 0u, 0u};
      if (hw == 3) {
#pragma unroll 1
        for (int i = 0; i < 4 * SPIN_MAX; ++i) {
          q = lds_rd4(ya);
          if (none(umax(umax(q[0], q[1]), umax(q[2], q[3])) == SENT)) break;
        }
      }
      return (__uint_as_float(q[0]) + __uint_as_float(q[1])) + (__uint_as_float(q[2]) + __uint_as_float(q[3]));
    };
    const __amdgpu_buffer_rsrc_t rs1 = __builtin_amdgcn_make_buffer_rsrc(hand, 0, HAND_N * 4, 0x00020000);
    u32x2 xr;
    unsigned xo, xo2;
    until(t1 + XDT);
    get_row(fr1, 0, xr, xo);
    if (hw == 0) STAMP(dk, 7);
#define FAILFC 0
    const unsigned md1 = row_md(xr, 0) ^ (FAILFC & 1);
    if (hw == 0) STAMP(dk, 10);
    LOGAT(20);
    u32x2 xr2;
    unsigned md2 = 0u;
    if (p1blk) {
      xr2 = __builtin_amdgcn_raw_buffer_load_b64(rsx, (fr2 >> 7) == omi ? S_ * D_ * 2 : xoff, D_ * 2, 16);
      PIN();
      ffn(xr, md1, 2u, rsh, GRAN_N, [&]() { return 0u; });
      __builtin_amdgcn_s_setprio(R2PRIO);
      LOG();
      if ((fr2 >> 7) == omi || !none(umax(xr2[0], xr2[1]) == SENT)) get_row(fr2, 1, xr2, xo2);
      md2 = row_md(xr2, 1) ^ (FAILFC & 2);
      ffn(xr2, md2, 4u, rs1, GRAN_N, [&]() {
        const u32x2 v = *LDS_AT(const volatile u32x2, lbase + BEST);
        return v[0] != 0u && (2047 - (int)(v[0] & 0x7ffu) != fr2 || v[1] != md2) ? 1u : 0u; });
    }
    if (hw == 0) STAMP(dk, 5);
    LOG();
    float y = 0.f;
    auto put_row = [&](int r, unsigned md, unsigned rb) __attribute__((always_inline)) {
      const float rden = __uint_as_float(md & 0xffff0000u), rs = rbf(frcp(1.0f + __expf(-__uint_as_float(rb << 16))));
      const float xa = __uint_as_float((col & 1) ? xo & 0xffff0000u : xo << 16);
      float ha = xa, hb = xa;
      ln_apply2(ha, hb, __uint_as_float(md << 16), rden, frcp(rden));
      const float o = xa + rbf(ha + rbf(rbf(y) * rs));
      __builtin_amdgcn_raw_buffer_store_b16((unsigned short)(pack2(o, o) & 0xffffu), __builtin_amdgcn_make_buffer_rsrc(out, 0, S_ * D_ * 2, 0x00020000),
                                            (lane & 8) ? S_ * D_ * 2 : (r * D_ + col) * 2, 0, OAUX);
    };
    if (!p1blk) {
      until(tmx + XD2);
      get_row(fr2, 1, xr2, xo2);
      md2 = row_md(xr2, 1) ^ (FAILFC & 2);
      until(t1 + XDT + YD1); until(tmx + YD2);
      if (hw == 3) STAMP(dk, 13);
      LOGAT(8);
      y = y_sum4(rsh, GRAN_N, 0);
      if (hw == 3) STAMP(dk, 14);
      LOGAT(24);
    }
    unsigned bmd = 0u;
    best = 0u;
#pragma unroll 1
    for (int spin = 0; spin < 4 * SPIN_MAX; ++spin) {
      const u32x2 v = *LDS_AT(const volatile u32x2, lbase + BEST);
      best = __builtin_amdgcn_readfirstlane(v[0]); bmd = __builtin_amdgcn_readfirstlane(v[1]);
      if (best) break;
      __builtin_amdgcn_s_sleep(1);
    }
    asm volatile("" ::: "memory");
    const int idx = 2047 - (int)(best & 0x7ffu);
    const unsigned rk = best >> 16, rb = (rk & 0x8000u) ? (rk & 0x7fffu) : (~rk & 0xffffu);
    if (hw == 0) STAMP(dk, 1);
    LOGAT(25);
    LOGOUT(hw);
    if (__builtin_expect(idx != fr1 || bmd != md1, 0)) {
      if (idx == fr2 && bmd == md2) {
        if (p1blk) return;
        y = y_sum4(rs1, GRAN_N, 1);
        xo = xo2;
      } else {
        get_row(idx, idx == fr1 ? 0 : (idx == fr2 ? 1 : 2), xr, xo);
        if (p1blk) ffn(xr, bmd, 6u, rsh, HAND_N + Y3_O, [&]() { return 0u; });
        else y = y_sum4(rsh, HAND_N + Y3_O, 2);
        if (p1blk || hw != 3) return;
#pragma unroll 1
        for (int spin = 0; spin < SPIN_MAX && idx != fr1 && idx != fr2; ++spin) {
          if (__builtin_amdgcn_readfirstlane(ld_ag(gran + GR_F + (oc >> 6) * 32)) == tag) break;
          asm volatile("" ::: "memory");
        }
      }
    }
    if (p1blk || hw != 3) return;
    put_row(idx, bmd, rb);
    }
    return;
  }
  if (wid >= 4) {
    const int lane = fresh_lane();
    const int lw = wid - 4;
    const long long lay_ = ap->lay;
    const u16* A = ap->a; const u16* W = ap->W; const u16* wc = ap->wc;
#define LPRIO 3
#define LP0 3
    __builtin_amdgcn_s_setprio(LP0);
    unsigned va[4], vw[6];
    const unsigned ul = lane;
    va[0] = (unsigned)(m0 + lw * 32) * (K * 2) + (ul >> 3) * (K * 2) + (((ul & 7u) ^ (ul >> 4)) << 4);
    va[1] = va[0] + (8 * K * 2 - 1024);
    va[2] = (va[0] ^ 64u) + 2 * (8 * K * 2 - 1024);
    va[3] = va[2] + (8 * K * 2 - 1024);
#pragma unroll
    for (int i = 0; i < 3; ++i) {
      const unsigned n = ul + 64u * i, k = __umul24(n, 171u) >> 12, cp = n - __umul24(k, 24u);
      const unsigned c = (cp & 7u) ^ ((k & 2u) << 1), part = (cp >> 3) ^ (cp < 16u ? ((c >> 2) ^ (unsigned)tmi) & 1u : 0u);
      vw[i] = __umul24(k, ldw * 2) + part * 2048u + (c << 4) + ((unsigned)(lw * 16 * ldw + n0) * 2u - i * 1024u);
      vw[i + 3] = (vw[i] ^ 16u) + 8 * ldw * 2;
    }
    PIN();
    asm volatile("" : "+v"(va[0]), "+v"(va[1]), "+v"(va[2]), "+v"(va[3]), "+v"(vw[0]), "+v"(vw[1]), "+v"(vw[2]), "+v"(vw[3]), "+v"(vw[4]), "+v"(vw[5]));
    const bool LAY2 = lay_ != 0;
    const int krot = tmi & 8;
    if (LAY2 && slow_cols(W, tni)) W = wc;
    auto stage_a = [&](int kt, int slot) {
      const char* pa = reinterpret_cast<const char*>(A) + (size_t)(kt ^ krot) * 128;
      asm volatile("s_mov_b32 m0, %5\n\ts_nop 0\n\tglobal_load_lds_dwordx4 %0, %4 sc1\n\tglobal_load_lds_dwordx4 %1, %4 offset:1024 sc1\n\t"
                   "global_load_lds_dwordx4 %2, %4 offset:2048 sc1\n\tglobal_load_lds_dwordx4 %3, %4 offset:3072 sc1"
                   :: "v"(va[0]), "v"(va[1]), "v"(va[2]), "v"(va[3]), "s"(pa), "s"(lbase + slot * STB + lw * 4096) : "m0", "memory");
    };
    auto stage_w = [&](int kt, int slot) {
      const char* pw = reinterpret_cast<const char*>(W) + (size_t)(kt ^ krot) * 128 * ldw;
      asm volatile("s_mov_b32 m0, %7\n\ts_nop 0\n\tglobal_load_lds_dwordx4 %0, %6 sc1\n\tglobal_load_lds_dwordx4 %1, %6 offset:1024 sc1\n\t"
                   "global_load_lds_dwordx4 %2, %6 offset:2048 sc1\n\ts_addk_i32 m0, 0xc00\n\ts_nop 0\n\tglobal_load_lds_dwordx4 %3, %6 sc1\n\t"
                   "global_load_lds_dwordx4 %4, %6 offset:1024 sc1\n\tglobal_load_lds_dwordx4 %5, %6 offset:2048 sc1"
                   :: "v"(vw[0]), "v"(vw[1]), "v"(vw[2]), "v"(vw[3]), "v"(vw[4]), "v"(vw[5]), "s"(pw), "s"(lbase + slot * STB + ABYTES + lw * 6144) : "m0", "scc", "memory");
    };
    if (LAY2) {
      __builtin_amdgcn_s_setprio(0);
      __builtin_amdgcn_s_sleep(TPRE - 10);
      tile_at(3u);
      __builtin_amdgcn_s_setprio(LPRIO);
#pragma unroll
      for (int st = 0; st < NST; ++st) stage_w(st, st);
      PIN();
      __builtin_amdgcn_s_barrier();
      PIN();
#pragma unroll
      for (int st = 0; st < NST; ++st) stage_a(st, st);
      asm volatile("s_waitcnt vmcnt(8)" ::: "memory");
    } else {
      __builtin_amdgcn_s_setprio(LPRIO);
      stage_a(0, 0); stage_w(0, 0);
      if (PRO > 1) { stage_a(1, 1); stage_w(1, 1); }
      if (PRO > 2) { stage_a(2, 2); stage_w(2, 2); }
      asm volatile("s_waitcnt vmcnt(%0)" :: "n"(10 * PRO - 10) : "memory");
    }
    int slot = 0;
    __builtin_amdgcn_s_barrier();
    if (!LAY2) {
      if (PRO < 2) { stage_a(1, 1); stage_w(1, 1); }
      if (PRO < 3) { stage_a(2, 2); stage_w(2, 2); }
    }
    for (int kt = 0; kt + 1 < NKT; ++kt) {
      if (LAY2 && kt == 0) asm volatile("s_waitcnt vmcnt(4)" ::: "memory");
      else if (kt + 2 >= NKT) asm volatile("s_waitcnt vmcnt(0)" ::: "memory");
      else asm volatile("s_waitcnt vmcnt(10)" ::: "memory");
      __builtin_amdgcn_s_barrier();
      if (kt + NST < NKT) { stage_a(kt + NST, slot); stage_w(kt + NST, slot); }
      slot = slot == NST - 1 ? 0 : slot + 1;
    }
    __builtin_amdgcn_s_setprio(0);
    __builtin_amdgcn_s_barrier();
  } else {
  LAYER_HERE;
  const int dk = LAY2 ? 5 : 1;
  const int lane = fresh_lane(), wm = wid & 1, wn = wid >> 1;
  if (wid == 0) STAMP(dk, 0);
  f32x4 acc[4][6];
#pragma unroll
  for (int i = 0; i < 4; ++i)
#pragma unroll
    for (int j = 0; j < 6; ++j) acc[i][j] = f32x4{0.f, 0.f, 0.f, 0.f};
  const int q16 = lane & 15, g4 = lane >> 4;
  const int e0 = 16 * (q16 >> 3) + (q16 & 7);
  s16x8 af[2][4];
  unsigned aad[2], wad[2];
  {
    const int row = wm * 64 + e0, swz = ((row >> 1) & 3) | (((row >> 4) & 1) << 2);
#pragma unroll
    for (int h = 0; h < 2; ++h) aad[h] = lbase + row * 128 + (((4 * h + g4) ^ swz) << 4);
    const int tq = (lane & 15) >> 2, tp = lane & 3, k = 8 * g4 + tq, ch = wn * 4 + 2 * (tp >> 1);
    wad[0] = lbase + ABYTES + k * 384 + ((ch ^ (4 * ((k >> 1) & 1)) ^ (g4 & 1)) << 4) + 8 * (tp & 1);
    wad[1] = wad[0] ^ 16;
  }
  auto lda = [&](unsigned a, int i) { return *LDS_AT(const s16x8, a + (i >> 1) * 4096 + (i & 1) * 1024); };
  auto ldw_ = [&](int h, int j) {
    const unsigned w = wad[j & 1] + h * 12288 + (j >> 1) * 128;
    s16x4 lo = __builtin_amdgcn_ds_read_tr16_b64_v4i16(LDS_AT(s16x4, w));
    s16x4 h4 = __builtin_amdgcn_ds_read_tr16_b64_v4i16(LDS_AT(s16x4, w + 1536));
    return (s16x8)__builtin_shufflevector(lo, h4, 0, 1, 2, 3, 4, 5, 6, 7);
  };
  ARGS_HERE;
  const bool give = wn != (tmi & 1);
  unsigned lto = lbase + (give ? 2 * KSB : 0) + wm * 4096, lqo = lbase + QOFF + wid * 4096;
  char* qgb = reinterpret_cast<char*>(KV) + QG + (((LAY2 ? 256 : 0) + tmi * 16 + tni) * 2 + wm) * 4096 - lto;
  char* kvb = reinterpret_cast<char*>(KV) + ((tni * 2 + wn) * 64 + tmi * 4 + wm * 2) * 2048 - lto;
  {
    const unsigned ato = lbase + ALPHA + wm * 256, dum = lbase + DUM + lane * 4;
    *LDS_AT(u32x4, lqo + lane * 16) = u32x4{lane >= 16 ? lbase + CRED : lbase + TABA + (wm * 64 + e0) * 16, lbase + TABW + (wn * 32 + e0) * 16,
                                                   ato + (give ? 512 : 0) + e0 * 4, lto + (lane >> 5) * 1024 + ((lane & 28) * 2 + (lane & 3)) * 16};
    *LDS_AT(u32x2, lqo + 2048 + lane * 16) = u32x2{aad[0], aad[1]};
    *LDS_AT(u32x4, lqo + 1024 + lane * 16) = u32x4{ato + g4 * 16 + (lane >> 5) * 32, lto + (lane >> 5) * 1024 + ((lane & 24) * 2 + (lane & 7)) * 16,
                                                          lane == 0 ? lbase + FLG + 32 : dum, lane == 0 ? lbase + FLG + 16 : dum};
  }
  {
    const __amdgpu_buffer_rsrc_t rq = __builtin_amdgcn_make_buffer_rsrc(qgb + lto, 0, give && !LAY2 ? 4096 : 0, 0x00020000);
#pragma unroll
    for (int i = 0; i < 4; ++i) __builtin_amdgcn_raw_buffer_store_b128(u32x4{SENT, SENT, SENT, SENT}, rq, lane * 16 + i * 1024, 0, 16);
  }
  unsigned qmv = lqo - lto;
  asm volatile("" : "+s"(lqo), "+s"(qmv), "+s"(kvb), "+s"(qgb) :: "memory");
#pragma unroll
  for (int i = 0; i < 4; ++i)
#pragma unroll
    for (int j = 0; j < 6; ++j) asm volatile("" : "+v"(acc[i][j]));
  asm volatile("" : "+v"(aad[0]), "+v"(aad[1]), "+v"(wad[0]), "+v"(wad[1]));
  if (LAY2) __builtin_amdgcn_s_barrier();
  __builtin_amdgcn_s_barrier();
  PIN();
  if (wid == 0) STAMP(dk, 1);
  s16x8 wq[4][2];
  wq[0][0] = ldw_(0, 0); af[0][0] = lda(aad[0], 0); wq[0][1] = ldw_(1, 0); af[1][0] = lda(aad[1], 0); wq[1][0] = ldw_(0, 1); wq[1][1] = ldw_(1, 1);
  af[0][1] = lda(aad[0], 1); af[1][1] = lda(aad[1], 1); af[0][2] = lda(aad[0], 2); af[1][2] = lda(aad[1], 2);
  PIN();
  s16x4 wlo;
  auto rw = [&](s16x8& d, int h, int j, int hi, int off = 0) __attribute__((always_inline)) {
    const s16x4 v = __builtin_amdgcn_ds_read_tr16_b64_v4i16(LDS_AT(s16x4, wad[j & 1] + h * 12288 + (j >> 1) * 128 + hi * 1536 + off));
    if (hi) d = (s16x8)__builtin_shufflevector(wlo, v, 0, 1, 2, 3, 4, 5, 6, 7);
    else wlo = v;
  };
  auto at = [&](s16x8& x, int r) __attribute__((always_inline)) {
    if (r == 0) asm volatile("" :: "{v[104:107]}"(x));
    if (r == 1) asm volatile("" :: "{v[108:111]}"(x));
    if (r == 2) asm volatile("" :: "{v[112:115]}"(x));
    if (r == 3) asm volatile("" :: "{v[116:119]}"(x));
    if (r == 4) asm volatile("" :: "{v[120:123]}"(x));
    if (r == 5) asm volatile("" :: "{v[124:127]}"(x));
    if (r == 6) asm volatile("" :: "{v[128:131]}"(x));
    if (r == 7) asm volatile("" :: "{v[132:135]}"(x));
    if (r == 8) asm volatile("" :: "{v[136:139]}"(x));
    if (r == 9) asm volatile("" :: "{v[140:143]}"(x));
    if (r == 10) asm volatile("" :: "{v[144:147]}"(x));
    if (r == 11) asm volatile("" :: "{v[148:151]}"(x));
    if (r == 12) asm volatile("" :: "{v[152:155]}"(x));
    if (r == 13) asm volatile("" :: "{v[156:159]}"(x));
    if (r == 14) asm volatile("" :: "{v[160:163]}"(x));
    if (r == 15) asm volatile("" :: "{v[164:167]}"(x));
  };
  auto mm = [&](int i, int j, int h, int sl, int hw_) __attribute__((always_inline)) {
    s16x8 &a = af[h][i], &w = wq[sl][hw_];
    at(a, h * 4 + i); at(w, 8 + sl * 2 + hw_);
    if (j >= 4) asm volatile("v_mfma_f32_16x16x32_bf16 %0, %1, %2, %0" : "+v"(acc[i][j]) : "v"(a), "v"(w));
    else asm volatile("v_mfma_f32_16x16x32_bf16 %0, %1, %2, %0" : "+v"(acc[i][j]) : "v"(w), "v"(a));
    PIN();
  };
  auto next = [&](int g, int J0, int s0, int s1, int off) __attribute__((always_inline)) {
    if (g == 2 || g == 3) rw(wq[s0][0], 0, J0, g & 1, off);
    if (g == 4 || g == 5) af[g & 1][0] = lda(aad[g & 1] + off, 0);
    if (g == 6 || g == 7) rw(wq[s0][1], 1, J0, g & 1, off);
    if (g >= 8 && g < 12) rw(wq[s1][(g - 8) >> 1], (g - 8) >> 1, J0 + 1, g & 1, off);
    if (g >= 12) af[g & 1][(g - 10) >> 1] = lda(aad[g & 1] + off, (g - 10) >> 1);
  };
  auto third = [&](int p, int J0, int delta) __attribute__((always_inline)) {
    auto mv = [&](unsigned& x) __attribute__((always_inline)) { asm volatile("v_add_u32 %0, %1, %0" : "+v"(x) : "s"(delta)); };
#pragma unroll
    for (int g = 0; g < 16; ++g) {
      const int h = g & 1, i = p == 1 ? (g >> 1) & 3 : g >> 2, t = p == 1 ? g >> 3 : (g >> 1) & 1, j = 2 * p + t, sl = p == 1 ? 2 - 2 * t : p + t;
      mm(i, j, h, sl, h);
      if (p == 0) {
        if (g < 2) af[g][3] = lda(aad[g], 3);
        else if (g < 6) rw(wq[2][(g - 2) >> 1], (g - 2) >> 1, 2, g & 1);
        else if (g < 10) rw(wq[3][(g - 6) >> 1], (g - 6) >> 1, 5, g & 1);
        else if (g < 12) mv(aad[g - 10]);
      }
      if (p == 1) {
        if (g < 4) rw(wq[0][g >> 1], g >> 1, 3, g & 1);
        if (g == 4) mv(wad[1]);
        if (g >= 8 && g < 12) rw(wq[2][(g - 8) >> 1], (g - 8) >> 1, 4, g & 1);
        if (g == 12) mv(wad[0]);
      }
      if (p == 2) {
        if (g == BARG) {
          asm volatile("" ::: "memory");
          PIN();
          __builtin_amdgcn_s_barrier();
          asm volatile("" ::: "memory");
        }
        next(g, J0, 0, 1, 0);
      }
      PIN();
      {
        if ((p == 2 && g == 15) || (p == 0 && g == 7)) __builtin_amdgcn_s_waitcnt(0xc67f);
        if (p == 0 && g == 2) __builtin_amdgcn_s_waitcnt(0xc57f);
        if ((p == 0 && g == 14) || (p == 1 && (g & 7) == 7)) __builtin_amdgcn_s_waitcnt(0xc07f);
        PIN();
      }
    }
  };
  third(0, 0, STB); third(1, 0, STB);
  {
    static_assert((NKT - NST - 1) % NST == 0, "");
#pragma unroll 1
    for (int kt = 0; kt < NKT - NST - 1; kt += NST) {
#pragma unroll
      for (int u = 0; u < NST; ++u) {
        const int delta = u == 1 ? -(NST - 1) * STB : STB;
        third(2, 0, 0); third(0, 0, delta); third(1, 0, delta);
      }
      asm volatile("s_waitcnt vmcnt(0)" ::: "memory");
    }
  }
  static_assert(NKT % NST == 1, "slots of the last stages");
#define TT(i)
  auto bar = [&]() { PIN(); __builtin_amdgcn_s_barrier(); asm volatile("" ::: "memory"); PIN(); };
  TT(0);
  third(2, 2, 0);
  const __amdgpu_buffer_rsrc_t rsk = __builtin_amdgcn_make_buffer_rsrc(give ? qgb : kvb, 0, 0x7fffffff, 0x00020000), rsv = __builtin_amdgcn_make_buffer_rsrc(kvb + VOFF, 0, 0x7fffffff, 0x00020000);
  const __amdgpu_buffer_rsrc_t rsq = __builtin_amdgcn_make_buffer_rsrc(kvb - qmv, 0, give ? 0x7fffffff : 0, 0x00020000);
  unsigned pk;
  u32x2 t0, t1, t2, t3;
  auto park = [&](int i) { return *LDS_AT(const volatile u32x2, pk + (i >> 1) * 1024 + (i & 1) * 8); };
  float al[4];
  f32x4 av[4];
  u32x4 kvp[12];
  auto cacc = [&](int p, int x, int t) -> f32x4& { return p < 2 ? acc[x][2 * p + t] : acc[(x >> 1) * 2 + t][4 + (x & 1)]; };
  auto signal = [&](unsigned v) { __hip_atomic_fetch_add(LDS_AT(unsigned, t3[0]), v, __ATOMIC_RELAXED, __HIP_MEMORY_SCOPE_WORKGROUP); PIN(); };
  auto outop = [&](int p, int k) {
    if (k >= 0 && k < 16) {
      const int x = k >> 2, t = (k >> 1) & 1;
      f32x4& c = cacc(p, x, t);
#pragma unroll
      for (int o = 2 * (k & 1); o < 2 * (k & 1) + 2; ++o) {
        float r;
        asm volatile("v_mul_f32 %0, %1, %2" : "=v"(r) : "v"(c[o]), "v"(p < 2 ? al[x] : av[(x >> 1) * 2 + t][o]));
        c[o] = r;
      }
      PIN();
    }
    if (k >= 1 && k <= 16) {
      const int x = (k - 1) >> 2, t = ((k - 1) >> 1) & 1, o = (k - 1) & 1;
      kvp[p * 4 + x][2 * t + o] = pack2(cacc(p, x, t)[2 * o], cacc(p, x, t)[2 * o + 1]);
      PIN();
      if (t && o) {
        if (p) __builtin_amdgcn_raw_buffer_store_b128(kvp[p * 4 + x], p == 1 ? rsk : rsv, (p == 1 ? t1[1] : t2[1]) + (x >> 1) * 2048 + (x & 1) * (p == 1 ? 64 : 128), 0, 16);
        else {
          __builtin_amdgcn_raw_buffer_store_b128(kvp[x], rsq, t1[1] + (x >> 1) * 2048 + (x & 1) * 64, 0, 16);
          *LDS_AT(u32x4, t1[1] + (x >> 1) * 2048 + (x & 1) * 64) = kvp[x];
        }
        PIN();
      }
    }
  };
  static_for<0, 168>([&](auto nc) __attribute__((always_inline)) {
    constexpr int n = decltype(nc)::value, sec = n < 80 ? n >> 4 : (n < 104 ? 5 : 6 + ((n - 104) >> 4)), g = n < 104 ? n - 16 * sec : (n - 104) & 15;
    auto taba = [&](int i) { af[0][i] = *LDS_AT(const s16x8, t0[0] + (i >> 1) * 512 + (i & 1) * 128); };
    auto tabw = [&](int j) { wq[(2 + (j >> 1)) & 3][j & 1] = *LDS_AT(const s16x8, t0[1] + ((j >> 1) * 64 + (j & 1) * 8) * 16); };
    auto rwn = [&](int e, int sl, int j, int off) { if (e >= 0 && e < 4) rw(wq[sl][e >> 1], e >> 1, j, e & 1, off); };
    if (g == 0) TT(1 + sec);
    if (sec == 5) {
      const int i = (g / 12) * 2 + (g & 1), j = (g % 12) >> 1;
      mm(i, j, 0, (2 + (j >> 1)) & 3, j & 1);
    } else {
      constexpr int part[10] = {1, 2, 1, 2, 0, 0, 1, 2, 0, 0}, s0[10] = {0, 2, 0, 2, 0, 0, 1, 3, 1, 3};
      const int h = g & 1, i = sec == 7 ? (g >> 3) * 2 + ((g >> 1) & 1) : g >> 2, t = sec == 7 ? (g >> 2) & 1 : (g >> 1) & 1;
      mm(i, 2 * part[sec] + t, h, (s0[sec] + t) & 3, h);
    }
    if (sec == 0 || sec == 2) {
      if (g < 2) af[g][3] = lda(aad[g] + sec / 2 * STB, 3);
      rwn(g - 2, 2, 4, sec / 2 * STB); rwn(g - 6, 3, 5, sec / 2 * STB);
    }
    if (sec == 1) {
      if (g == 1) bar();
      next(g, 2, 0, 1, STB);
    }
    if ((sec == 3 && g == 12) || (sec == 6 && g == 13)) { pk = lqo + fresh_lane() * 16; asm volatile("" : "+v"(pk)); }
    if (sec == 3) {
      rwn(g, 0, 0, STB); rwn(g - 4, 1, 1, STB);
      if (g >= 8 && g < 12) asm volatile("v_subrev_u32 %0, %1, %0" : "+v"(g < 10 ? aad[g & 1] : wad[g & 1]) : "s"(STB));
      if (g == 13) t1 = park(1);
      if (g == 14) t0 = park(0);
    }
    if (sec == 4) {
      if (g == B15) bar();
      if (g == 4) taba(0);
      if (g == 5 || g == 6) tabw(g - 5);
      if (g == 7) taba(1);
      if (g == 8 || g == 9) tabw(g - 6);
      if (g == 10 || g == 11) af[1][g - 10] = lda(aad[1], g - 10);
      if (g == 12) taba(2);
      if (g == 15) taba(3);
    }
    if (sec == 5) {
      if (g < 2) tabw(4 + g);
      rwn(g - 2, 1, 2, 0);
      if (g == 12 || g == 13) af[0][g - 12] = lda(aad[0], g - 12);
      if (g == 22) { al[0] = *LDS_AT(const float, t1[0]); al[1] = *LDS_AT(const float, t1[0] + 32); }
      rwn(g - 16, 2, 3, 0);
      if (g == 20 || g == 21) af[1][g - 18] = lda(aad[1], g - 18);
    }
    if (sec == 6) {
      if (g < 2) af[0][2 + g] = lda(aad[0], 2 + g);
      rwn(g - 2, 3, 4, 0);
      if (g == 9) { al[2] = *LDS_AT(const float, t1[0] + 128); al[3] = *LDS_AT(const float, t1[0] + 160); }
      if (g == 14) t2 = park(2);
      if ((g & 3) >= 2) rwn((g - 10) / 4 * 2 + (g & 1) - (g < 10) * 8, 0, 5, 0);
    }
    if (sec == 7) {
      if (g == 2 || g == 3 || g == 10 || g == 11) av[(g >> 3) * 2 + (g & 1)] = *LDS_AT(const f32x4, t2[0] + ((g >> 3) * 32 + (g & 1) * 8) * 4);
      rwn(g < 2 ? g : (g < 4 ? -1 : g - 2), 1, 0, 0); rwn(g - 6, 2, 1, 0);
      if (g == 7) asm volatile("v_xor_b32 %0, 0x200, %0" : "+v"(t1[0]));
      if (g == 14) {
#pragma unroll
        for (int x = 0; x < 4; ++x) al[x] = *LDS_AT(const float, t1[0] + (x >> 1) * 128 + (x & 1) * 32);
      }
      if (g == 12) t3 = park(3);
      if (g == 13) { const u32x2 a = park(4); aad[0] = a[0]; aad[1] = a[1]; }
      if (g == 15) {
        asm volatile("" ::: "memory");
        __builtin_amdgcn_s_waitcnt(0xc07f);
        bar();
      }
    }
    if (sec == 8) next(g, 0, 3, 0, STB);
    if (sec == 9) {
      if (g < 2) af[g][3] = lda(aad[g] + STB, 3);
      if (g == 2) signal(0x10000u);
    }
    PIN();
    outop(1, n - 104 - KO);
    outop(2, n - 120 - KO);
    {
      constexpr int wg[8] = WGAPS;
#pragma unroll
      for (int e = 0; e < 8; ++e)
        if (n - 136 == wg[e]) {
          *LDS_AT(u32x4, (e < 4 ? t1[1] + (e & 1) * 64 : t2[1] + 2048 + (e & 1) * 128) + ((e >> 1) & 1) * KSB) = kvp[4 + e];
          PIN();
        }
    }
    if (n == 152 + 3) { asm volatile("v_add_u32 %0, %1, %0" : "+v"(t1[1]) : "s"(qmv)); PIN(); }
    outop(0, n - 152 - KO);
  });
  TT(13);
  if (wid == 0) STAMP(dk, 18);
#pragma unroll
  for (int k = 16 - KO; k <= 16; ++k) outop(0, k);
  __hip_atomic_fetch_add(LDS_AT(unsigned, t3[1]), 1u, __ATOMIC_RELAXED, __HIP_MEMORY_SCOPE_WORKGROUP);
  TT(14);
  asm volatile("s_waitcnt vmcnt(4)" ::: "memory");
  signal(give ? 0u : 1u);
  TT(15);
  if (wid == 0) STAMP(dk, 19);
  }
  PIN();
  {
  const int lane = fresh_lane(), tid = wid * 64 + lane;
  LAYER_HERE;
  const int dk = LAY2 ? 6 : 2;
  const int g = (wid >> 1) & 1, hh = tmi & 1, qg = wid & 1, far = wid >> 2;
  const int head = tni * 2 + hh, qt = (tmi ^ far) * 4 + qg * 2;
  unsigned qaa = lbase + QOFF + (hh * 2 + qg) * 4096 + kpos(lane) * 16 + (g ? 0 : 2048), qab = qaa ^ 2048;
  const unsigned wbase = lbase + g * 4096 + lane * 16;
  auto sptr = [&](int sl) { unsigned a = wbase + sl * KSB; asm volatile("" : "+v"(a)); return LDS_AT(const s16x8, a); };
  auto rd = [&](__attribute__((address_space(3))) const s16x8* sp, int off) { return sp[off >> 4]; };
  auto sl0 = sptr(0);
  const unsigned arr_a = lane == 0 ? lbase + FLG + 16 : lbase + DUM + lane * 4;
  auto arrive = [&]() { __hip_atomic_fetch_add(LDS_AT(unsigned, arr_a), 1u, __ATOMIC_RELAXED, __HIP_MEMORY_SCOPE_WORKGROUP); };
  unsigned arr_w = lane == 0 ? lbase + FLG + 8 + (wid >> 2) * 4 : lbase + DUM + lane * 4;
  unsigned arr_i = 1u << (8 * (wid & 3));
  auto arrive_w = [&]() { __hip_atomic_fetch_add(LDS_AT(unsigned, arr_w), arr_i, __ATOMIC_RELAXED, __HIP_MEMORY_SCOPE_WORKGROUP); };
  s16x8 sel;
#pragma unroll
  for (int e = 0; e < 8; ++e) sel[e] = ((lane & 7) == (e & 4)) ? (short)0x3F80 : (short)0;
  int sidx = 0xDDDDDDDD;
  f32x16 oa, ob, nn, sa, sb;
  f32x4 la = {0.f, 0.f, 0.f, 0.f}, lb = {0.f, 0.f, 0.f, 0.f};
#pragma unroll
  for (int e = 0; e < 16; ++e) { oa[e] = 0.f; ob[e] = 0.f; sa[e] = 0.f; sb[e] = 0.f; }
  asm volatile("" : "+v"(sidx), "+v"(sel), "+v"(oa), "+v"(ob), "+v"(la), "+v"(lb), "+v"(qaa), "+v"(qab), "+v"(arr_w), "+v"(arr_i));
  PIN();
  s16x8 qa0, qa1, qb0, qb1, k00, k01, v00, v01, v10, v11, k10, k11;
  asm volatile("" : "=v"(qa0), "=v"(qa1), "=v"(qb0), "=v"(qb1));
  u32x4 fw;
  auto first = [&]() {
    fw = lds_rd4(lbase + FLG + 16);
    asm volatile("" ::: "memory");
    k00 = rd(sl0, 0); k01 = rd(sl0, 1024);
    if (!far) { qa0 = *LDS_AT(const s16x8, qaa); qb0 = *LDS_AT(const s16x8, qab); qa1 = *LDS_AT(const s16x8, qaa + 1024); qb1 = *LDS_AT(const s16x8, qab + 1024); }
    v00 = rd(sl0, 2048); v01 = rd(sl0, 3072);
    PIN();
  };
  if (far) {
    ArgP ap_ = ap; asm volatile("" : "+s"(ap_));
    const __amdgpu_buffer_rsrc_t rq = __builtin_amdgcn_make_buffer_rsrc(reinterpret_cast<char*>(ap_->kv) + QG + (((LAY2 ? 256 : 0) + (tmi ^ 1) * 16 + tni) * 2 + qg) * 4096, 0, 4096, 0x00020000);
    const int o = kpos(lane) * 16 + (g ? 0 : 2048);
    __builtin_amdgcn_s_sleep(QPRE);
#pragma unroll 1
    for (int i = 0; i < SPIN_MAX; ++i) {
      const u32x4 a0 = __builtin_amdgcn_raw_buffer_load_b128(rq, o, 0, 16), b0 = __builtin_amdgcn_raw_buffer_load_b128(rq, o ^ 2048, 0, 16);
      const u32x4 a1 = __builtin_amdgcn_raw_buffer_load_b128(rq, o + 1024, 0, 16), b1 = __builtin_amdgcn_raw_buffer_load_b128(rq, (o ^ 2048) + 1024, 0, 16);
      qa0 = __builtin_bit_cast(s16x8, a0); qb0 = __builtin_bit_cast(s16x8, b0); qa1 = __builtin_bit_cast(s16x8, a1); qb1 = __builtin_bit_cast(s16x8, b1);
      unsigned mx = 0u;
#pragma unroll
      for (int e = 0; e < 4; ++e) mx = umax(umax(umax(mx, a0[e]), b0[e]), umax(a1[e], b1[e]));
      if (none(mx == SENT)) break;
      __builtin_amdgcn_s_sleep(QSLP);
      asm volatile("" ::: "memory");
    }
  }
  first();
  if (__builtin_amdgcn_readfirstlane(fw[0]) < 4u) {
#pragma unroll 1
    for (int i = 0; i < SPIN_MAX; ++i) {
      if (__builtin_amdgcn_readfirstlane(*LDS_AT(const volatile unsigned, lbase + FLG + 16)) >= 4u) break;
      __builtin_amdgcn_s_sleep(ASLP);
    }
    asm volatile("" ::: "memory");
    first();
  }
  const unsigned tag = __builtin_amdgcn_readfirstlane(fw[1]);
  const int idx = LAY2 ? (2047 - (int)(__builtin_amdgcn_readfirstlane(fw[3]) & 0xffffu)) & 2047 : -64;
  float ma;
  const float zoff = __uint_as_float(__builtin_amdgcn_readfirstlane(LAY2 && g == ((idx >> 6) & 1) ? 0u : 0xff800000u));
  {
    sa = MFMA32(k00, qa0, sa); sb = MFMA32(k00, qb0, sb);
    sa = MFMA32(k01, qa1, sa); sb = MFMA32(k01, qb1, sb);
    float mb = sb[0];
    ma = sa[0];
#pragma unroll
    for (int e = 1; e < 16; ++e) { ma = fmaxf(ma, sa[e]); mb = fmaxf(mb, sb[e]); }
    ma = fmaxf(ma, mb);
    const auto sw = __builtin_amdgcn_permlane32_swap(__float_as_uint(ma), __float_as_uint(ma), false, false);
    ma = fmaxf(__uint_as_float(sw[0]), __uint_as_float(sw[1]));
    f32x2 n2 = {-ma, -ma};
    asm volatile("" : "+v"(n2));
#pragma unroll
    for (int e = 0; e < 16; ++e) nn[e] = n2[e & 1];
    sa = MFMA32(k00, qa0, nn); sa = MFMA32(k01, qa1, sa);
    la[0] = lb[0] = -__uint_as_float(__float_as_uint(__builtin_amdgcn_exp2f(zoff - ma)) & 0xffff0000u);
  }
#define SB() __builtin_amdgcn_sched_barrier(0)
#define EX(i) sc[i] = __builtin_amdgcn_exp2f(sc[i])
  auto unit = [&](f32x16& sc, f32x16& sn, const s16x8& kn0, const s16x8& kn1, const s16x8& qn0, const s16x8& qn1,
                  f32x16& op, f32x4& lp, const s16x8& vp,
                  f32x16& oc, f32x4& lc, const s16x8& vc, auto&& mid) {
    lp = SMF16(sel, PHI(sn), lp); SB();
    op = SMF32(vp, PHI(sn), op); SB();
    EX(0); EX(1); EX(2); EX(3); SB();
    sn = MFMA32(kn0, qn0, nn); SB();
    EX(4); EX(5); EX(6); EX(7); SB();
    sn = MFMA32(kn1, qn1, sn); SB();
    mid();
    EX(8); EX(9); EX(10); EX(11); SB();
    lc = SMF16(sel, PLO(sc), lc); SB();
    oc = SMF32(vc, PLO(sc), oc); SB();
    EX(12); EX(13); EX(14); EX(15); SB();
    asm volatile("" : "+v"(sc));
  };
  sl0 = sptr(1);
  unsigned f0 = 0u;
  auto check = [&](unsigned need) {
    if (__builtin_expect(__builtin_amdgcn_readfirstlane(f0) < need, 0)) {
#pragma unroll 1
      for (int i = 0; i < SPIN_MAX; ++i) {
        if (__builtin_amdgcn_readfirstlane(*LDS_AT(const volatile unsigned, lbase + FLG)) >= need) break;
        __builtin_amdgcn_s_sleep(1);
      }
    }
    asm volatile("" ::: "memory");
  };
#define PAT 0x22221110
#define SAW8(i) do { if (((PAT >> (28 - 4 * (i))) & 3) != ((PAT >> ((32 - 4 * (i)) & 31)) & 3)) { __builtin_amdgcn_s_setprio((PAT >> (28 - 4 * (i))) & 3); SB(); } } while (0)
#define SAW(i) do { if (odd) SAW8(4 + (i)); else SAW8(i); } while (0)
  auto three = [&](int st, bool odd) {
    SAW(0);
    unit(sa, sb, k00, k01, qb0, qb1, ob, lb, v11, oa, la, v00, [&]() { k10 = rd(sl0, 0); k11 = rd(sl0, 1024); SB(); });
    SAW(1);
    unit(sb, sa, k10, k11, qa0, qa1, oa, la, v01, ob, lb, v00, [&]() { v10 = rd(sl0, 2048); v11 = rd(sl0, 3072); SB(); });
    sl0 = sptr((2 * st + 2) & 7);
    SB();
    SAW(2);
    unit(sa, sb, k10, k11, qb0, qb1, ob, lb, v01, oa, la, v10, [&]() { k00 = rd(sl0, 0); k01 = rd(sl0, 1024); SB(); });
  };
  auto fourth = [&](int st, bool odd) {
    SAW(3);
    unit(sb, sa, k00, k01, qa0, qa1, oa, la, v11, ob, lb, v10, [&]() {
      v00 = rd(sl0, 2048); v01 = rd(sl0, 3072);
      f0 = *LDS_AT(const volatile unsigned, lbase + FLG);
      SB();
      arrive_w();
      SB(); });
    check(st >= 14 ? 32u : 2u * st + 5u);
    sl0 = sptr((2 * st + 3) & 7);
    SB();
  };
  SB();
  SAW8(0);
  {
    f32x16& sc = sa;
    sb = MFMA32(k00, qb0, nn); SB();
    k10 = rd(sl0, 0); k11 = rd(sl0, 1024); SB();
    sb = MFMA32(k01, qb1, sb); SB();
    EX(0); EX(1); EX(2); EX(3); EX(4); EX(5); EX(6); EX(7); EX(8); SB();
    oa = SMF32(v00, PLO(sa), oa); SB();
    EX(9); EX(10); EX(11); SB();
    la = SMF16(sel, PLO(sa), la); SB();
    EX(12); EX(13); EX(14); EX(15); SB();
    asm volatile("" : "+v"(sa));
  }
  auto given = [&]() {
    if (!far && g != hh) {
      asm volatile("s_waitcnt vmcnt(0)" ::: "memory");
      __hip_atomic_fetch_add(LDS_AT(unsigned, arr_w + 24), 256u, __ATOMIC_RELAXED, __HIP_MEMORY_SCOPE_WORKGROUP);
    }
    SB();
  };
  given();
  SAW8(1);
  unit(sb, sa, k10, k11, qa0, qa1, oa, la, v01, ob, lb, v00, [&]() { v10 = rd(sl0, 2048); v11 = rd(sl0, 3072); SB(); });
  sl0 = sptr(2);
  SB();
  SAW8(2);
  unit(sa, sb, k10, k11, qb0, qb1, ob, lb, v01, oa, la, v10, [&]() {});
  {
    f32x16& sc = sb;
    SAW8(3);
    la = SMF16(sel, PHI(sa), la); SB();
    oa = SMF32(v11, PHI(sa), oa); SB();
    EX(0); EX(1); EX(2); EX(3); EX(4); EX(5); EX(6); EX(7); EX(8); asm volatile("" : "+v"(sc)); SB();
    lb = SMF16(sel, PLO(sb), lb); SB();
    ob = SMF32(v10, PLO(sb), ob); SB();
    f0 = *LDS_AT(const volatile unsigned, lbase + FLG);
    asm volatile("" ::: "memory");
    k00 = rd(sl0, 0); k01 = rd(sl0, 1024); SB();
    EX(9); EX(10); EX(11); EX(12); EX(13); EX(14); EX(15); asm volatile("" : "+v"(sc)); SB();
    if (__builtin_amdgcn_readfirstlane(f0) < 3u) { check(3u); k00 = rd(sl0, 0); k01 = rd(sl0, 1024); }
    SB();
    v00 = rd(sl0, 2048); v01 = rd(sl0, 3072); SB(); arrive_w(); SB();
    sa = MFMA32(k00, qa0, nn); sa = MFMA32(k01, qa1, sa); asm volatile("" : "+v"(sa)); SB();
  }
  sl0 = sptr(3);
  SB();
  SAW8(4);
  unit(sa, sb, k00, k01, qb0, qb1, ob, lb, v11, oa, la, v00, [&]() {
    f0 = *LDS_AT(const volatile unsigned, lbase + FLG);
    asm volatile("" ::: "memory");
    k10 = rd(sl0, 0); k11 = rd(sl0, 1024); SB(); });
  if (__builtin_amdgcn_readfirstlane(f0) < 4u) { check(4u); k10 = rd(sl0, 0); k11 = rd(sl0, 1024); }
  SB();
  SAW8(5);
  unit(sb, sa, k10, k11, qa0, qa1, oa, la, v01, ob, lb, v00, [&]() { v10 = rd(sl0, 2048); v11 = rd(sl0, 3072); f0 = *LDS_AT(const volatile unsigned, lbase + FLG); SB(); });
  check(5u);
  sl0 = sptr(4);
  SB();
  SAW8(6);
  unit(sa, sb, k10, k11, qb0, qb1, ob, lb, v01, oa, la, v10, [&]() { k00 = rd(sl0, 0); k01 = rd(sl0, 1024); SB(); });
#pragma unroll 1
  for (int st = 2; st < 16; st += 2) { fourth(st - 1, true); three(st, false); fourth(st, false); three(st + 1, true); }
  SAW8(7);
  unsigned vr, tk, tv;
  unsigned f4;
  auto table = [&]() {
    f4 = *LDS_AT(const volatile unsigned, lbase + FLG + 4);
    k00 = *LDS_AT(const volatile s16x8, tk); k01 = *LDS_AT(const volatile s16x8, tk + 32);
    vr = *LDS_AT(const volatile u16, tv);
  };
  unsigned xw, xm;
  u32x2 ml;
  u32x4 o2[4];
  auto take = [&]() {
    ml = *LDS_AT(const volatile u32x2, xm);
#pragma unroll
    for (int e = 0; e < 4; ++e) o2[e] = lds_rd4(xw + e * 1024);
  };
  float pzt;
  int dso;
  f32x16& sc = sb;
#define EXP() do { asm volatile("" : "+v"(sc)); SB(); } while (0)
  oa = SMF32(v11, PHI(sa), oa); SB();
  tk = lbase + TAB + 128 + hh * 64 + (lane >> 5) * 16; tv = lbase + TAB + 256 + hh * 64 + (lane & 31) * 2;
  table();
  SB();
  EX(0); EX(1); EXP();
  la = SMF16(sel, PHI(sa), la); SB();
  EX(2); EX(3); EX(4); EXP();
  xw = wbase + (qg ? QOFF : 4 * ASB) + far * 8192; xm = lbase + XML + wid * 512 + lane * 8;
  pzt = __builtin_amdgcn_exp2f(nn[0] + (zoff - 8.f));
  asm volatile("" : "+v"(xw), "+v"(xm), "+v"(pzt), "+v"(f4));
  SB();
  {
    const unsigned ph = LAY2 ? 0x79797979u : 0x80808080u;
    auto ready = [&]() { return ((__builtin_amdgcn_readfirstlane(f4) + ph) & 0x80808080u) == 0x80808080u; };
    if (__builtin_expect(!ready(), 0)) {
#pragma unroll 1
      for (int i = 0; i < SPIN_MAX; ++i) {
        __builtin_amdgcn_s_sleep(1);
        table();
        if (ready()) break;
      }
    }
    asm volatile("" ::: "memory");
  }
  SB();
  sa = MFMA32(k00, qb0, nn); asm volatile("" : "+v"(sa)); SB();
#pragma unroll
  for (int e = 0; e < 4; ++e) *LDS_AT(volatile f32x4, xw + e * 1024) = f32x4{oa[4 * e], oa[4 * e + 1], oa[4 * e + 2], oa[4 * e + 3]};
  *LDS_AT(volatile u32x2, xm) = u32x2{__float_as_uint(ma), f2word(pzt <= la[0] ? la[0] : 0.f)};
  SB();
  EX(5); EX(6); EX(7); EXP();
  sa = MFMA32(k01, qb1, sa); asm volatile("" : "+v"(sa), "+v"(vr)); SB();
  ARGS_HERE;
  EX(8); EX(9); EX(10); EXP();
  ob = SMF32(v10, PLO(sb), ob); SB();
  EX(11); EX(12); EX(13); EXP();
  lb = SMF16(sel, PLO(sb), lb); SB();
  const bool used = !LAY2 || (qt + g) * 32 + (lane & 31) != idx;
  dso = used ? (((qt + g) * 32 + head) * 1024 + lane * 8) * 2 : S_ * D_ * 2;
  xw += g ? -4096 : 4096; xm += g ? -1024 : 1024;
  asm volatile("" : "+v"(dso), "+v"(xw), "+v"(xm));
  SB();
  EX(14); EX(15); EXP();
  take();
  SB();
  lb = SMF16(sel, PHI(sb), lb); SB();
  ob = SMF32(v11, PHI(sb), ob); SB();
  __builtin_amdgcn_s_setprio(EPRIO);
  TE(0, 0); TE(1, 0); TE(2, 0); TE(3, 0); TE(7, 0);
  float lsb = lb[0];
  if (LAY2) {
    sa[0] = __builtin_amdgcn_exp2f(sa[0]);
    ob = SMF32((u32x4{lane < 32 ? vr : 0u, 0u, 0u, 0u}), PLO(sa), ob);
    lsb += __uint_as_float(__float_as_uint(sa[0]) & 0xffff0000u);
    lsb = pzt <= lsb ? lsb : 0.f;
  }
  const __amdgpu_buffer_rsrc_t rso = __builtin_amdgcn_make_buffer_rsrc(Of, 0, S_ * D_ * 2, 0x00020000);
  TE(0, 1);
  if (__builtin_amdgcn_ballot_w64(ml[1] == SENT) != 0ull) {
#pragma unroll 1
    for (int i = 0; i < SPIN_MAX; ++i) {
      __builtin_amdgcn_s_sleep(XSLP);
      if (!__builtin_amdgcn_ballot_w64(*LDS_AT(const volatile unsigned, xm + 4) == SENT)) break;
    }
    asm volatile("s_waitcnt lgkmcnt(0)" ::: "memory");
    take();
  }
  arrive();
  TE(0, 2);
  TE(0, 3); TE(1, 1);
  {
    const float m2 = __uint_as_float(ml[0]), l2 = __uint_as_float(ml[1]);
    const float mx = fmaxf(ma, m2);
    const float f1 = __builtin_amdgcn_exp2f(ma - mx), f2 = __builtin_amdgcn_exp2f(m2 - mx);
    const float inv = frcp(lsb * f1 + l2 * f2);
    const float c1 = f1 * inv, c2 = f2 * inv;
    f32x2 r[8];
#pragma unroll
    for (int e = 0; e < 8; ++e) {
      const unsigned x0 = o2[e >> 1][(e & 1) * 2], x1 = o2[e >> 1][(e & 1) * 2 + 1];
      r[e] = f32x2{__uint_as_float(x0) * c2, __uint_as_float(x1) * c2};
    }
#pragma unroll
    for (int e = 0; e < 8; ++e) r[e] = f32x2{ob[2 * e], ob[2 * e + 1]} * f32x2{c1, c1} + r[e];
    auto put = [&]() {
#pragma unroll
      for (int sx = 0; sx < 2; ++sx)
        __builtin_amdgcn_raw_buffer_store_b128(u32x4{pack2(r[4 * sx][0], r[4 * sx][1]), pack2(r[4 * sx + 1][0], r[4 * sx + 1][1]),
            pack2(r[4 * sx + 2][0], r[4 * sx + 2][1]), pack2(r[4 * sx + 3][0], r[4 * sx + 3][1])}, rso, dso + sx * 1024, 0, 16);
    };
    put();
    PIN();
    __builtin_amdgcn_s_setprio(0);
    PIN();
    f32x2 chk = {lsb - __builtin_amdgcn_fmed3f(lsb, 8.6736174e-19f, 1.2676506e30f), l2 - __builtin_amdgcn_fmed3f(l2, 8.6736174e-19f, 1.2676506e30f)};
#pragma unroll
    for (int e = 0; e < 8; ++e) chk = r[e] * f32x2{0.f, 0.f} + chk;
    if (__builtin_expect(__builtin_amdgcn_ballot_w64(used && !(chk[0] + chk[1] == 0.f)) != 0ull, 0)) {
      float l;
      attn_safe(KV, head * 64, qb0, qb1, ob, l, LAY2 ? head * 64 + (idx >> 5) : -1, idx & 31, lbase + TAB + 128 + hh * 64);
      const float inv2 = 1.0f / l;
#pragma unroll
      for (int e = 0; e < 8; ++e) r[e] = f32x2{ob[2 * e] * inv2, ob[2 * e + 1] * inv2};
      asm volatile("s_waitcnt vmcnt(0)" ::: "memory");
      put();
    }
    unsigned pp = 0u;
    if (LAY2) {
      const unsigned ua = lbase + UGL + hh * 128 + (lane >> 5) * 16;
      f32x2 du = {0.f, 0.f}, dn = {0.f, 0.f};
#pragma unroll
      for (int q = 0; q < 4; ++q) {
        const f32x4 uu = *LDS_AT(const f32x4, ua + q * 32), gg = *LDS_AT(const f32x4, ua + 256 + q * 32);
        du = r[2 * q] * f32x2{uu[0], uu[1]} + du; du = r[2 * q + 1] * f32x2{uu[2], uu[3]} + du;
        const f32x2 t0 = r[2 * q] * f32x2{gg[0], gg[1]}, t1 = r[2 * q + 1] * f32x2{gg[2], gg[3]};
        dn = t0 * t0 + dn; dn = t1 * t1 + dn;
      }
      const auto a = __builtin_amdgcn_permlane32_swap(__float_as_uint(du[0] + du[1]), __float_as_uint(du[0] + du[1]), false, false);
      const auto c = __builtin_amdgcn_permlane32_swap(__float_as_uint(dn[0] + dn[1]), __float_as_uint(dn[0] + dn[1]), false, false);
      pp = f2word(__uint_as_float(pack2(__uint_as_float(a[0]) + __uint_as_float(a[1]), __uint_as_float(c[0]) + __uint_as_float(c[1]))));
    }
    __builtin_amdgcn_raw_buffer_store_b32(pp, __builtin_amdgcn_make_buffer_rsrc(hand + 2 * HAND_N + PP_O, 0, LAY2 ? H_ * S_ * 4 : 0, 0x00020000),
                                          lane < 32 ? (head * S_ + (qt + g) * 32 + lane) * 4 : H_ * S_ * 4, 0, 16);
    PIN();
  }
  TE(1, 2);
  u32x4 xres[2];
  oproj_tail(LAY2, tid, lbase, lbase + FLG, lbase + SPM + 144, omi, oni, idx, gran + GR_O + ((LAY2 ? 16 : 0) + (tmi ^ far)) * (64 * RO) + head,
             gran + GR_X + omi * (128 * RX) + oni * 4, tag, Of, xout, A, w_r, stats_out, stats, hand, xres TEPASS);
  if (LAY2) {
    const int row = omi * 128 + (wid & 3) * 32 + (wid >> 2) * 16 + (lane >> 3), col = oni * 64 + (lane & 7) * 8;
    unsigned md[2], best = 0u;
    const __amdgpu_buffer_rsrc_t rsk = __builtin_amdgcn_make_buffer_rsrc(hand + HAND_N, 0, (HAND_N + PS_N) * 4, 0x00020000);
    f32x2 xf[2][4];
#pragma unroll
    for (int p = 0; p < 2; ++p)
#pragma unroll
      for (int e = 0; e < 4; ++e) {
        xf[p][e] = f32x2{__uint_as_float(xres[p][e] << 16), __uint_as_float(xres[p][e] & 0xffff0000u)};
        asm volatile("" : "+v"(xf[p][e]));
      }
    LOGDEF;
    LOG();
    const bool rtr = (oni & 7) == 7;
    {
      const int rl = (wid & 3) * 32 + (wid >> 2) * 16 + (lane & 15), rr = omi * 128 + rl;
      int src = (HAND_N + ((omi * 16 + (lane >> 4) * 4) * 128 + rl) * 4) * 4;
      int ko = rtr && (wid >> 2) == (oni >> 3) && lane < NREP ? lane * 1024 + (rr >> 4) * 8 : (HAND_N + PS_N) * 4;
      int bp = (lane >> 3) * 4;
      const float wsum = *LDS_AT(const volatile float, lbase + SPM + 132);
      asm volatile("" : "+v"(src), "+v"(ko), "+v"(bp));
      u32x4 v[4];
      __builtin_amdgcn_s_sleep(MPRE);
#pragma unroll 1
      for (int spin = 0; spin < SPIN_MAX; ++spin) {
        LOG();
#pragma unroll
        for (int i = 0; i < 4; ++i) v[i] = __builtin_amdgcn_raw_buffer_load_b128(rsk, src + (i & 1) * 2048, (i >> 1) * 4096, 16);
        unsigned mx = 0u;
#pragma unroll
        for (int i = 0; i < 4; ++i) mx = umax(umax(umax(mx, v[i][0]), v[i][1]), v[i][2]);
        if (none(mx == SENT)) break;
        __builtin_amdgcn_s_sleep(MSLP);
        asm volatile("" ::: "memory");
      }
      LOGAT(12);
      if (rtr) __builtin_amdgcn_s_setprio(3); else __builtin_amdgcn_s_setprio(LNPRIO);
      double sm[3];
#pragma unroll
      for (int c = 0; c < 3; ++c) {
        const double a = ((double)__uint_as_float(v[0][c]) + (double)__uint_as_float(v[1][c])) + ((double)__uint_as_float(v[2][c]) + (double)__uint_as_float(v[3][c]));
        u32x2 q = __builtin_bit_cast(u32x2, a);
        const auto lo = __builtin_amdgcn_permlane32_swap(q[0], q[0], false, false), hi = __builtin_amdgcn_permlane32_swap(q[1], q[1], false, false);
        q = __builtin_bit_cast(u32x2, __builtin_bit_cast(double, u32x2{lo[0], hi[0]}) + __builtin_bit_cast(double, u32x2{lo[1], hi[1]}));
        const auto l2 = __builtin_amdgcn_permlane16_swap(q[0], q[0], false, false), h2 = __builtin_amdgcn_permlane16_swap(q[1], q[1], false, false);
        sm[c] = __builtin_bit_cast(double, u32x2{l2[0], h2[0]}) + __builtin_bit_cast(double, u32x2{l2[1], h2[1]});
      }
      float mean, var, mb, den;
      ln_from_sums(sm[0], sm[1], mean, var, mb, den);
      const unsigned mdr = f2word(__uint_as_float((__float_as_uint(mb) >> 16) | (__float_as_uint(den) & 0xffff0000u)));
      md[0] = __builtin_amdgcn_ds_bpermute(bp, mdr); md[1] = __builtin_amdgcn_ds_bpermute(bp + 32, mdr);
      if (rtr) {
        const float r = (float)fma(-(double)mb, (double)wsum, sm[2]) * frcp(den);
        const unsigned rb = __float_as_uint(r) + 0x8000u;
        unsigned rkey = (rb ^ ((unsigned)((int)rb >> 31) | 0x80000000u)) & 0xfffff800u;
        if (r != r) rkey = 0u;
        const unsigned top = wave_umax(rkey | (unsigned)(2047 - rr));
        const unsigned mdt = __builtin_amdgcn_readlane(mdr, (2047 - (int)(top & 0xffffu)) & 15);
        __builtin_amdgcn_raw_buffer_store_b64(u32x2{top, mdt}, rsk, ko, 0, 16);
        PIN();
        __builtin_amdgcn_s_setprio(LNPRIO);
        __hip_atomic_fetch_add(LDS_AT(unsigned, lane == 0 ? lbase + SPM + 128 : lbase + DUM + lane * 4), 1u, __ATOMIC_RELAXED, __HIP_MEMORY_SCOPE_WORKGROUP);
        if (wid == 2 || wid == 5) STAMP(8, 6);
      }
      LOGAT(13);
    }
    auto keys = [&](const u32x4& v) __attribute__((always_inline)) {
      if (none(umax(umax(v[0], v[1]), umax(v[2], v[3])) == SENT)) {
        best = wave_umax(umax(v[0], v[2])) | 0x8000u;
        *LDS_AT(volatile u32x2, lbase + FLG + 40) = u32x2{best, (unsigned)__builtin_amdgcn_readlane(v[0] > v[2] ? v[1] : v[3], ((2047 - (int)(best & 0x7ffu)) >> 5) & 63)};
        STAMP(8, 11);
      }
    };
    auto ln_rows = [&]() __attribute__((always_inline)) {
#pragma unroll
      for (int p = 0; p < 2; ++p) {
        const float mb_ = __uint_as_float(md[p] << 16), rin = frcp(__uint_as_float(md[p] & 0xffff0000u));
#pragma unroll
        for (int e = 0; e < 4; ++e) {
          const f32x2 h = (xf[p][e] - f32x2{mb_, mb_}) * f32x2{rin, rin} + xf[p][e];
          xres[p][e] = pack2(h[0], h[1]);
        }
      }
      asm volatile("" : "+v"(xres[0]), "+v"(xres[1]));
    };
    if (wid == 0) STAMP(8, 12);
    ln_rows();
    LOGAT(14);
    const __amdgpu_buffer_rsrc_t rsu = __builtin_amdgcn_make_buffer_rsrc(out, 0, S_ * D_ * 2, 0x00020000);
    unsigned fr1 = SENT;
#pragma unroll 1
    for (int i = 0; i < 4 * SPIN_MAX && fr1 == SENT; ++i) fr1 = __builtin_amdgcn_readfirstlane(*LDS_AT(const volatile unsigned, lbase + SPM + 144));
    const int fr2 = (fr1 >> 16) & 0x7ffu;
    fr1 &= 0x7ffu;
    unsigned tmx = __builtin_amdgcn_readfirstlane(*LDS_AT(const volatile unsigned, lbase + SPM + 136)) + KDT;
    if (tmx - (unsigned)__builtin_amdgcn_s_memrealtime() > 1024u) tmx = 0u;
    auto due = [&]() { return tmx == 0u || (int)((unsigned)__builtin_amdgcn_s_memrealtime() - tmx) >= 0; };
    lk_set(15);
    const bool held = !none((row | 8) == ((int)fr1 | 8) || (row | 8) == (fr2 | 8));
#pragma unroll
    for (int p = 0; p < 2; ++p) __builtin_amdgcn_raw_buffer_store_b128(xres[p], rsu, row + 8 * p != (int)fr1 && row + 8 * p != fr2 ? ((row + 8 * p) * D_ + col) * 2 : S_ * D_ * 2, 0, OAUX);
    PIN();
    if (wid == 6) {
      if (rtr) {
#pragma unroll 1
        for (int i = 0; i < SPIN_MAX; ++i) {
          if (__builtin_amdgcn_readfirstlane(*LDS_AT(const volatile unsigned, lbase + SPM + 128)) == 7u) break;
          __builtin_amdgcn_s_sleep(1);
        }
        asm volatile("" ::: "memory");
      }
      while (!due()) __builtin_amdgcn_s_sleep(1);
    }
#pragma unroll 1
    for (int i = 0; i < 4 * SPIN_MAX && !best; ++i) {
      if (wid == 6) {
        LOG();
        keys(__builtin_amdgcn_raw_buffer_load_b128(rsk, (bid % NREP) * 1024 + lane * 16, 0, 16));
        if (!best) __builtin_amdgcn_s_sleep(KSLP);
      } else {
        best = __builtin_amdgcn_readfirstlane(*LDS_AT(const volatile unsigned, lbase + FLG + 40));
        if (!best) __builtin_amdgcn_s_sleep(1);
      }
      asm volatile("" ::: "memory");
    }
    const int idx2 = 2047 - (int)(best & 0x7ffu);
    if (held) {
#pragma unroll
      for (int p = 0; p < 2; ++p) {
        const int r = row + 8 * p;
        __builtin_amdgcn_raw_buffer_store_b128(xres[p], rsu, (r == (int)fr1 || r == fr2) && r != idx2 ? (r * D_ + col) * 2 : S_ * D_ * 2, 0, OAUX);
      }
    }
    if (__builtin_expect(idx2 != (int)fr1 && idx2 != fr2, 0) && !none((row | 8) == (idx2 | 8))) {
#pragma unroll
      for (int p = 0; p < 2; ++p)
        __builtin_amdgcn_raw_buffer_store_b128(u32x4{pack2(xf[p][0][0], xf[p][0][1]), pack2(xf[p][1][0], xf[p][1][1]), pack2(xf[p][2][0], xf[p][2][1]), pack2(xf[p][3][0], xf[p][3][1])},
            __builtin_amdgcn_make_buffer_rsrc(xout, 0, 3 * D_ * 2, 0x00020000), row + 8 * p == idx2 ? (2 * D_ + col) * 2 : S_ * D_ * 2, 0, 16);
      asm volatile("s_waitcnt vmcnt(0)" ::: "memory");
      if (lane == 0) st_ag(gran + GR_F + oni * 32, tag);
    }
  }
  }
}
struct Args2 { Args l[2]; };
extern "C" __global__ __launch_bounds__(768, 3) void layers_kernel(const Args2 args) {
  __shared__ __attribute__((aligned(128))) char smem[163840];
  const unsigned long long t_entry = 0;
  const int tid = threadIdx.x, bid = blockIdx.x;
  int wid = __builtin_amdgcn_readfirstlane(tid >> 6);
  asm volatile("" : "+s"(wid));
  const ArgP ap = (ArgP)__builtin_amdgcn_kernarg_segment_ptr();
  layer_body(wid, bid, (unsigned)(uintptr_t)smem, ap, t_entry);
  LAYER_HERE;
  if (!LAY2) {
    asm volatile(AGAIN :: "{s[0:1]}"(ap + 1), "{s2}"(bid), "{v0}"(wid * 64) : "s90", "s91", "scc", "memory");
    __builtin_unreachable();
  }
}

extern "C" __global__ void end_marker_kernel() {}

extern "C" void kernel_launch(void* const* d_in, const int* in_sizes, int n_in, void* d_out, int out_size,
                              void* d_ws, size_t ws_size, hipStream_t stream) {
  const u16* x0 = (const u16*)d_in[0];
  const u16* wqkv = (const u16*)d_in[1];
  const u16* wo = (const u16*)d_in[2];
  const u16* w_r = (const u16*)d_in[4];
  const u16* wg = (const u16*)d_in[5];
  const u16* wu = (const u16*)d_in[6];
  const u16* wd = (const u16*)d_in[7];
  u16* out = (u16*)d_out;

  char* ws = (char*)d_ws;
  const size_t MB4 = (size_t)S_ * D_ * 2;
  u16* kv = (u16*)(ws);
  u16* o = (u16*)(ws + 2 * MB4);
  u16* x1 = (u16*)(ws + 3 * MB4);
  unsigned* gran = (unsigned*)(ws + 4 * MB4);
  float* csf = (float*)(gran + GR_F + 16 * 32);
  double* stats = (double*)(csf + 8192);
  unsigned* hand = (unsigned*)(stats + 2 * STAT_N);
  static_assert((GR_F + 16 * 32) % 32 == 0 && (size_t)(GR_F + 16 * 32 + 8192 + 4 * STAT_N + 2 * HAND_N + PS_N) * 4 <= 6u << 20, "");
  u16* x3 = (u16*)(hand + 2 * HAND_N) - 3 * D_;
  u16* wc = (u16*)((((uintptr_t)ws + 4 * MB4 + (6u << 20) + 2047) & ~(uintptr_t)2047) + (((uintptr_t)wqkv + 128) & 2047));
  Args2 a;
  for (int l = 0; l < 2; ++l) {
    Args& r = a.l[l];
    r.lay = l; r.a = l ? x1 : x0; r.kv = kv; r.o = o; r.xo = l ? x3 : x1; r.so = l ? stats + STAT_N : stats;
    r.W = wqkv; r.wc = wc; r.wg = wg; r.wu = wu; r.wd = wd; r.wo = wo; r.w_r = w_r; r.gran = gran; r.csf = csf; r.stats = stats; r.hand = hand; r.out = out;
  }
  layers_kernel<<<256, 768, 0, stream>>>(a);
}
```

```cpp
#include <hip/hip_runtime.h>
#include <hip/hip_bf16.h>
#include <stdint.h>
#include <type_traits>
#include <utility>

typedef unsigned short u16;
typedef short s16x4 __attribute__((ext_vector_type(4)));
typedef short s16x8 __attribute__((ext_vector_type(8)));
typedef float f32x16 __attribute__((ext_vector_type(16)));
typedef float f32x4 __attribute__((ext_vector_type(4)));
typedef unsigned int u32x4 __attribute__((ext_vector_type(4)));
typedef unsigned int u32x2 __attribute__((ext_vector_type(2)));
typedef __bf16 bf16x8 __attribute__((ext_vector_type(8)));
typedef __bf16 bf16x16 __attribute__((ext_vector_type(16)));

__device__ __forceinline__ int fresh_lane() {
  int l;
  asm volatile("v_mbcnt_lo_u32_b32 %0, -1, 0\n\tv_mbcnt_hi_u32_b32 %0, -1, %0" : "=v"(l));
  return l;
}
#define STAMP(k, i)
#define STAMPV(k, i, v)
#define LOGDEF
#define LOG()
#define LOGAT(k)
#define lk_set(k)
#define LOGOUT(slot)
#define TE(grp, k)
#define TEARG
#define TEPASS
constexpr int S_ = 2048, D_ = 1024, H_ = 32, F_ = 3584;

__device__ __forceinline__ int kpos(int r) { return (r & ~12) | ((r & 4) << 1) | ((r & 8) >> 1); }
__device__ __forceinline__ float bf2f(u16 v) { return __uint_as_float(((unsigned)v) << 16); }
__device__ __forceinline__ u16 f2bf(float f) {
  __hip_bfloat16 b = __float2bfloat16(f);
  return *reinterpret_cast<u16*>(&b);
}
typedef float f32x2 __attribute__((ext_vector_type(2)));
typedef __bf16 bf16x2 __attribute__((ext_vector_type(2)));
__device__ __forceinline__ unsigned pack2(float lo, float hi) {
  f32x2 v = {lo, hi};
  return __builtin_bit_cast(unsigned, __builtin_convertvector(v, bf16x2));
}
__device__ __forceinline__ float rbf(float f) { return __uint_as_float(pack2(f, f) << 16); }
__device__ __forceinline__ void rbf2(float& a, float& b) {
  const unsigned p = pack2(a, b);
  a = __uint_as_float(p << 16); b = __uint_as_float(p & 0xffff0000u);
}

__device__ __forceinline__ float fadd(float a, float b) { float r = a + b; asm("" : "+v"(r)); return r; }
template <int CTRL, int RMASK>
__device__ __forceinline__ float dpp_add(float v) {
  const int t = __builtin_amdgcn_update_dpp(0, __float_as_int(v), CTRL, RMASK, 0xf, false);
  return v + __int_as_float(t);
}
__device__ __forceinline__ float wave_sum(float v) {
  v = dpp_add<0x111, 0xf>(v);
  v = dpp_add<0x112, 0xf>(v);
  v = dpp_add<0x114, 0xf>(v);
  v = dpp_add<0x118, 0xf>(v);
  v = dpp_add<0x142, 0xa>(v);
  v = dpp_add<0x143, 0xc>(v);
  return __int_as_float(__builtin_amdgcn_readlane(__float_as_int(v), 63));
}

template <int CTRL, int RMASK>
__device__ __forceinline__ unsigned dpp_umax(unsigned v) {
  const unsigned t = (unsigned)__builtin_amdgcn_update_dpp(0, (int)v, CTRL, RMASK, 0xf, false);
  return v > t ? v : t;
}
__device__ __forceinline__ unsigned wave_umax(unsigned v) {
  v = dpp_umax<0x111, 0xf>(v); v = dpp_umax<0x112, 0xf>(v); v = dpp_umax<0x114, 0xf>(v); v = dpp_umax<0x118, 0xf>(v);
  v = dpp_umax<0x142, 0xa>(v); v = dpp_umax<0x143, 0xc>(v);
  return (unsigned)__builtin_amdgcn_readlane((int)v, 63);
}

#define NREP 32
constexpr unsigned SENT = 0xFFFFFFFFu;
constexpr int GRAN_N = 256 * NREP;
constexpr int Y_N = F_ / 16 * D_;
constexpr int X2_N = D_ / 2;
constexpr int XS_N = 128;
constexpr int P4_N = 3 * D_ * 16;
constexpr int PA_N = H_ * 8 * 64;
constexpr int HAND_N = GRAN_N + Y_N + X2_N + XS_N + P4_N + PA_N;
constexpr int MD_O = 16 * 16 * 128 * 4;
constexpr int PK_O = MD_O;
constexpr int PP_O = PK_O + 128 * NREP;
constexpr int Y3_O = PP_O + H_ * S_;
constexpr int PS_N = Y3_O + Y_N;
constexpr int GR_ROW = 64, GR_COL = GR_ROW + 4 * S_, GR_KV = GR_COL + 16 * 16 * 192;
#define RO 8
#define RK 16
#define RX 1
constexpr int GR_O = GR_KV + 2 * 16 * 32 * RK, GR_X = GR_O + 2 * 16 * 64 * RO, GR_N = GR_X + 16 * 128 * RX;
constexpr unsigned QG = 30u << 20;
constexpr int GR_F = GR_N + 256 * 64;
constexpr int STAT_N = 3 * S_;

__device__ __forceinline__ void st_ag(unsigned* p, unsigned v) { __hip_atomic_store(p, v, __ATOMIC_RELAXED, __HIP_MEMORY_SCOPE_AGENT); }
__device__ __forceinline__ unsigned ld_ag(const unsigned* p) { return __hip_atomic_load(p, __ATOMIC_RELAXED, __HIP_MEMORY_SCOPE_AGENT); }
__device__ __forceinline__ double ld_ag(const double* p) { return __hip_atomic_load(p, __ATOMIC_RELAXED, __HIP_MEMORY_SCOPE_AGENT); }
__device__ __forceinline__ unsigned f2word(float f) { const unsigned u = __float_as_uint(f); return u < 0xFFFFFFFEu ? u : 0xFFFFFFFEu; }
constexpr int SPIN_MAX = 1 << 18;
__device__ __forceinline__ unsigned umax(unsigned a, unsigned b) { return a > b ? a : b; }
__device__ __forceinline__ bool none(bool p) { return __builtin_amdgcn_ballot_w64(p) == 0ull; }

__device__ __forceinline__ void ln_from_sums(double s1, double s2, float& mean, float& var, float& mb, float& den) {
  const double md = s1 * (1.0 / 1024.0);
  const double vd = fma(-md, md, s2 * (1.0 / 1024.0));
  mean = (float)md; var = fmaxf((float)vd, 0.f);
  mb = rbf(mean);
  den = rbf(__builtin_amdgcn_sqrtf(rbf(rbf(var) + 1e-5f)));
}
__device__ __forceinline__ float frcp(float x) { return __builtin_amdgcn_rcpf(x); }
__device__ __forceinline__ void ln_apply2(float& a, float& b, float mb, float den, float rin) {
  a -= mb; b -= mb;
  rbf2(a, b);
  float qa = a * rin, qb = b * rin;
  qa = fmaf(fmaf(-qa, den, a), rin, qa);
  qb = fmaf(fmaf(-qb, den, b), rin, qb);
  rbf2(qa, qb);
  a = qa; b = qb;
}

#define CODE_ADDR(SYM, out64)                                                                                          \
  do {                                                                                                                 \
    unsigned lo_, hi_;                                                                                                 \
    asm volatile("s_getpc_b64 s[90:91]\n\ts_add_u32 s90, s90, " SYM "@rel32@lo+4\n\ts_addc_u32 s91, s91, " SYM          \
                 "@rel32@hi+12\n\ts_mov_b32 %0, s90\n\ts_mov_b32 %1, s91"                                             \
                 : "=s"(lo_), "=s"(hi_) :: "s90", "s91", "scc");                                                        \
    out64 = ((unsigned long long)hi_ << 32) | lo_;                                                                     \
  } while (0)

#define QNX 4
constexpr int QMX = 32 / QNX, QNG = 16 / QNX, QMG = 8 / QNG;

__device__ __forceinline__ unsigned touch(const void* p) { return *reinterpret_cast<const unsigned*>(p); }

__device__ __forceinline__ unsigned touch_code(unsigned long long lo, unsigned long long hi, int pc, int lane) {
  const unsigned long long p = lo + (unsigned)(pc * 8192 + lane * 128);
  return p + 4 <= hi ? *reinterpret_cast<__attribute__((address_space(1))) const unsigned*>(p) : 0u;
}

template <bool SC = false>
__device__ __forceinline__ void glds16s(const void* sbase, unsigned voff, unsigned lds_dst) {
  unsigned keep;
  if (SC)
    asm volatile("s_mov_b32 %0, m0\n\ts_mov_b32 m0, %3\n\ts_nop 2\n\tglobal_load_lds_dwordx4 %1, %2 sc1\n\ts_mov_b32 m0, %0"
                 : "=&s"(keep) : "v"(voff), "s"(sbase), "s"(lds_dst) : "memory");
  else
  asm volatile("s_mov_b32 %0, m0\n\ts_mov_b32 m0, %3\n\ts_nop 2\n\tglobal_load_lds_dwordx4 %1, %2\n\ts_mov_b32 m0, %0"
               : "=&s"(keep) : "v"(voff), "s"(sbase), "s"(lds_dst) : "memory");
}
template <bool SC>
__device__ __forceinline__ void glds16x4(const void* sbase, unsigned v0, unsigned v1, unsigned v2, unsigned v3, unsigned lds_dst) {
  unsigned keep;
  if (SC)
    asm volatile("s_mov_b32 %0, m0\n\ts_mov_b32 m0, %6\n\ts_nop 2\n\tglobal_load_lds_dwordx4 %1, %5 sc1\n\tglobal_load_lds_dwordx4 %2, %5 offset:1024 sc1\n\t"
                 "global_load_lds_dwordx4 %3, %5 offset:2048 sc1\n\tglobal_load_lds_dwordx4 %4, %5 offset:3072 sc1\n\ts_mov_b32 m0, %0"
                 : "=&s"(keep) : "v"(v0), "v"(v1), "v"(v2), "v"(v3), "s"(sbase), "s"(__builtin_amdgcn_readfirstlane(lds_dst)) : "memory");
  else
    asm volatile("s_mov_b32 %0, m0\n\ts_mov_b32 m0, %6\n\ts_nop 2\n\tglobal_load_lds_dwordx4 %1, %5\n\tglobal_load_lds_dwordx4 %2, %5 offset:1024\n\t"
                 "global_load_lds_dwordx4 %3, %5 offset:2048\n\tglobal_load_lds_dwordx4 %4, %5 offset:3072\n\ts_mov_b32 m0, %0"
                 : "=&s"(keep) : "v"(v0), "v"(v1), "v"(v2), "v"(v3), "s"(sbase), "s"(__builtin_amdgcn_readfirstlane(lds_dst)) : "memory");
}
constexpr float QSCALE = 0.17677669529663687f * 1.4426950408889634f;

__device__ __forceinline__ unsigned min4(u32x4 v) { const unsigned a = v[0] < v[1] ? v[0] : v[1], b = v[2] < v[3] ? v[2] : v[3]; return a < b ? a : b; }
#define LDS_AT(T, a) ((__attribute__((address_space(3))) T*)(uintptr_t)(a))
__device__ __forceinline__ u32x4 lds_rd4(unsigned a) { return *LDS_AT(const volatile u32x4, a); }
__device__ __forceinline__ void lds_wr(unsigned a, unsigned v) { *LDS_AT(volatile unsigned, a) = v; }
__device__ __forceinline__ void split3(float v, unsigned& p0, unsigned& p1, unsigned& p2) {
  p0 = pack2(v, v) & 0xffffu; v -= __uint_as_float(p0 << 16);
  p1 = pack2(v, v) & 0xffffu; v -= __uint_as_float(p1 << 16);
  p2 = pack2(v, v) & 0xffffu;
}

#define PIN() __builtin_amdgcn_sched_barrier(0)
template <int B, class F, int... I>
__device__ __forceinline__ void static_for_(F&& f, std::integer_sequence<int, I...>) { (f(std::integral_constant<int, B + I>{}), ...); }
template <int B, int E, class F>
__device__ __forceinline__ void static_for(F&& f) { static_for_<B>(f, std::make_integer_sequence<int, E - B>{}); }

#define TPRE 60
#define TPSL 0
#define FCPRE 44
#define FCSLP 2
#define SIGAT 24
#define XPRIO 1
#define OPD 8
#define LAUX 16
#define CSLP 2
#define EPRIO 3
#define OPRIO 1
#define XSLP 1
#define CSL1 8
#define XAUX 16
__device__ __forceinline__ void oproj_tail(const bool RES2, const int tid, const unsigned lbase, const unsigned flg, const unsigned fcm, const int tmi, const int tni, const int idx,
                                           unsigned* osig, unsigned* xsig, const unsigned tag,
                                           const u16* A, u16* out,
                                           const u16* res, const u16* __restrict__ w_r, double* stats,
                                           const double* stats_in, unsigned* hand, u32x4 (&xres)[2] TEARG) {
  constexpr int ldw = D_, PD = OPD;
  const int lane = tid & 63, wid = __builtin_amdgcn_readfirstlane(tid >> 6);
  const int m0 = tmi * 128, n0 = tni * 64;
  const int dk = RES2 ? 7 : 3;
  if (wid == 0) STAMP(dk, 0);
  TE(1, 3);
  asm volatile("" : "+s"(A) :: "memory");
  PIN();
  const int row = m0 + (wid & 3) * 32 + (wid >> 2) * 16 + (lane >> 3), col = n0 + (lane & 7) * 8;
  const unsigned tb = lbase + 147456;
  typedef __attribute__((address_space(1))) double* gdp;
  typedef __attribute__((address_space(1))) unsigned* gup;
  unsigned tr = tb + (row - m0) * 128 + (((lane ^ (lane >> 3)) & 7) << 4), oad;
  {
    oad = lane == 0 ? flg + 24 : flg + 64 + lane * 4;
    gup ps = (gup)(osig + lane * 64);
    asm volatile("" : "+v"(oad), "+v"(ps));
    asm volatile("s_waitcnt vmcnt(1)" ::: "memory");
    TE(2, 1);
    const unsigned old = __hip_atomic_fetch_add(LDS_AT(unsigned, oad), wid < 4 ? 0x101u : 0x10001u, __ATOMIC_RELAXED, __HIP_MEMORY_SCOPE_WORKGROUP);
    if (((__builtin_amdgcn_readfirstlane(old) >> (wid < 4 ? 8 : 16)) & 255u) == 3u && lane < RO) __hip_atomic_store(ps, tag, __ATOMIC_RELAXED, __HIP_MEMORY_SCOPE_AGENT);
    PIN();
    TE(2, 2);
    if (wid == 0) STAMP(dk, 12);
  }
  auto announce = [&]() __attribute__((always_inline)) {
    asm volatile("" ::: "memory");
    PIN();
    __builtin_amdgcn_s_barrier();
    PIN();
    if (wid == 0) STAMP(dk, 1);
    TE(2, 3); TE(4, 0); TE(7, 1);
  };
  if (wid < 4) {
    const __amdgpu_buffer_rsrc_t rsa = __builtin_amdgcn_make_buffer_rsrc(const_cast<u16*>(A), 0, S_ * D_ * 2, 0x00020000);
    int sa0 = (tmi * 4 + wid) * 65536, va0 = (lane >> 4) * 512 + (lane & 15) * 16;
    asm volatile("" : "+v"(va0));
    asm volatile("" : "+s"(sa0));
    auto lda = [&](int i, int h) { return __builtin_amdgcn_raw_buffer_load_b128(rsa, va0 + (i & 1) * 2048 + h * 256, sa0 + (i >> 1) * 4096, LAUX); };
    unsigned wad[4], tw[4];
    f32x4 acc[2][4];
    const int k = (lane >> 5) * 16 + ((lane >> 2) & 7);
#pragma unroll
    for (int j = 0; j < 4; ++j) {
      wad[j] = lbase + k * 128 + (((2 * j) ^ (k & 6)) << 4) + 8 * (lane & 3);
      tw[j] = tb + wid * 4096 + (lane & 15) * 128 + (((2 * j + (lane >> 5)) ^ (lane & 7)) << 4) + ((lane >> 4) & 1) * 8;
      asm volatile("" : "+v"(wad[j]), "+v"(tw[j]));
    }
    auto ldw_ = [&](int i, int j) {
      const unsigned a = wad[j] + (i & 3) * 4096;
      s16x4 lo = __builtin_amdgcn_ds_read_tr16_b64_v4i16(LDS_AT(s16x4, a));
      s16x4 h4 = __builtin_amdgcn_ds_read_tr16_b64_v4i16(LDS_AT(s16x4, a + 1024));
      return (s16x8)__builtin_shufflevector(lo, h4, 0, 1, 2, 3, 4, 5, 6, 7);
    };
    u32x4 id[2];
#pragma unroll
    for (int j = 0; j < 2; ++j) {
#pragma unroll
      for (int e = 0; e < 4; ++e) id[j][e] = (lane >> 4) == 2 * j + ((lane >> 3) & 1) && ((lane & 7) >> 1) == e ? ((lane & 1) ? 0x3f800000u : 0x3f80u) : 0u;
      asm volatile("" : "+v"(id[j]));
    }
    unsigned rad = tb + wid * 4096 + (lane & 15) * 128 + (((lane >> 4) ^ (lane & 7)) << 4), rfa = flg + 36 + wid;
    asm volatile("" : "+v"(rad), "+v"(rfa));
    u32x4 rb[2][2];
    u32x4 ow[2];
#pragma unroll
    for (int j = 0; j < 4; ++j) {
      const u32x2 v = *reinterpret_cast<const u32x2*>(w_r + n0 + j * 16 + (lane >> 4) * 4);
#pragma unroll
      for (int e = 0; e < 2; ++e) ow[j >> 1][(j & 1) * 2 + e] = (lane & 8) ? v[e] : 0x3f803f80u;
    }
    gdp pst = (gdp)(stats + (lane >> 4) % 3 * S_ + m0 + wid * 32 + (lane & 15));
    gup pps = (gup)(hand + 2 * HAND_N + ((tmi * 16 + tni) * 128 + wid * 32 + (lane & 15)) * 4 + (lane >> 4));
    int xo = (row * ldw + col) * 2;
    asm volatile("" : "+v"(tr), "+v"(pst), "+v"(pps), "+v"(xo), "+v"(ow[0]), "+v"(ow[1]));
    announce();
    u32x4 af[PD][2];
    s16x8 wf[2][4];
#pragma unroll
    for (int i = 0; i < PD; i += 2) { af[i][0] = lda(i, 0); af[i + 1][0] = lda(i + 1, 0); af[i][1] = lda(i, 1); af[i + 1][1] = lda(i + 1, 1); }
    PIN();
    __builtin_amdgcn_s_setprio(OPRIO);
#pragma unroll
    for (int q = 0; q < 4; ++q) rb[q >> 1][q & 1] = lds_rd4((rad ^ ((q & 1) * 64)) + (q >> 1) * 2048);
#pragma unroll
    for (int j = 0; j < 4; ++j) wf[j & 1][j >> 1] = ldw_(j & 1, j >> 1);
    PIN();
#pragma unroll
    for (int j = 0; j < 4; ++j)
#pragma unroll
      for (int h = 0; h < 2; ++h) { asm volatile("v_mfma_f32_16x16x32_bf16 %0, %1, %2, 0" : "=&v"(acc[h][j]) : "v"(id[j & 1]), "v"(rb[h][j >> 1])); PIN(); }
#pragma unroll 1
    for (int spin = 0; spin < SPIN_MAX; ++spin)
      if (__builtin_amdgcn_readfirstlane(*LDS_AT(const volatile unsigned, flg)) >= 40u) break;
    asm volatile("" : "+v"(va0) :: "memory");
    PIN();
    s16x4 wlo;
    static_for<0, 16>([&](auto ic) __attribute__((always_inline)) {
      constexpr int i = 2 * decltype(ic)::value;
      if (i == SIGAT) { __hip_atomic_fetch_add(LDS_AT(unsigned, oad + 4), 1u, __ATOMIC_RELAXED, __HIP_MEMORY_SCOPE_WORKGROUP); PIN(); }
#pragma unroll
      for (int g = 0; g < 16; ++g) {
        const int e = g & 1, j = g >> 2, h = (g >> 1) & 1, jn = (j + 2) & 3, in = j < 2 ? i : i + 2;
        asm volatile("v_mfma_f32_16x16x32_bf16 %0, %1, %2, %0" : "+v"(acc[h][j]) : "v"(wf[e][j]), "v"(af[(i + e) % PD][h]));
        PIN();
        if (in == 16 && !(g & 3)) asm volatile("v_add_u32 %0, 0x10000, %0" : "+v"(wad[jn]));
        if (in < 32) {
          const s16x4 v = __builtin_amdgcn_ds_read_tr16_b64_v4i16(LDS_AT(s16x4, wad[jn] + ((in + h) & 15) * 4096 + e * 1024));
          if (e) wf[h][jn] = (s16x8)__builtin_shufflevector(wlo, v, 0, 1, 2, 3, 4, 5, 6, 7);
          else wlo = v;
          PIN();
        }
        if ((g & 3) == 1 && i > 0 && i + PD - 2 < 32) { af[(i - 2 + (j & 1)) % PD][j >> 1] = lda(i + PD - 2 + (j & 1), j >> 1); PIN(); }
        if ((g & 3) == 3 && in < 32 && i != SIGAT) { __builtin_amdgcn_s_waitcnt(0xc47f); PIN(); }
      }
    });
    asm volatile("s_nop 3" : "+v"(acc[0][0]), "+v"(acc[1][0]));
    TE(4, 1); TE(7, 2);
    if (wid == 0) STAMP(dk, 17);
    if (wid == 1 && !RES2) STAMP(4, 0);
    u32x4 pk[2][2];
    float sv[2];
    auto sums = [&]() __attribute__((always_inline)) {
      f32x4 d[2];
      float q[2];
#pragma unroll
      for (int h = 0; h < 2; ++h) {
        d[h] = __builtin_amdgcn_mfma_f32_16x16x32_bf16(__builtin_bit_cast(s16x8, ow[0]), __builtin_bit_cast(s16x8, pk[h][0]), f32x4{0.f, 0.f, 0.f, 0.f}, 0, 0, 0);
        d[h] = __builtin_amdgcn_mfma_f32_16x16x32_bf16(__builtin_bit_cast(s16x8, ow[1]), __builtin_bit_cast(s16x8, pk[h][1]), d[h], 0, 0, 0);
        q[h] = 0.f;
      }
#pragma unroll
      for (int e = 0; e < 16; ++e) {
        const bf16x2 x = __builtin_bit_cast(bf16x2, (unsigned)pk[e & 1][e >> 3][(e >> 1) & 3]);
        q[e & 1] = __builtin_amdgcn_fdot2_f32_bf16(x, x, q[e & 1], false);
      }
      const auto a = __builtin_amdgcn_permlane32_swap(__float_as_uint(q[0]), __float_as_uint(q[1]), false, false);
      const unsigned b = __float_as_uint(__uint_as_float(a[0]) + __uint_as_float(a[1]));
      const auto c = __builtin_amdgcn_permlane16_swap(b, b, false, false);
      const unsigned e = __float_as_uint(__uint_as_float(c[0]) + __uint_as_float(c[1]));
      const auto f = __builtin_amdgcn_permlane32_swap(e, e, false, false);
#pragma unroll
      for (int h = 0; h < 2; ++h) sv[h] = (lane & 16) ? __uint_as_float(f[h]) : d[h][0];
    };
#pragma unroll
    for (int j = 0; j < 4; ++j)
#pragma unroll
      for (int h = 0; h < 2; ++h) {
        pk[h][j >> 1][(j & 1) * 2] = pack2(acc[h][j][0], acc[h][j][1]); pk[h][j >> 1][(j & 1) * 2 + 1] = pack2(acc[h][j][2], acc[h][j][3]);
        *LDS_AT(u32x2, tw[j] + h * 2048) = u32x2{pk[h][j >> 1][(j & 1) * 2], pk[h][j >> 1][(j & 1) * 2 + 1]};
        PIN();
      }
    asm volatile("" ::: "memory");
    *LDS_AT(volatile unsigned char, rfa) = (unsigned char)2;
#pragma unroll
    for (int p = 0; p < 2; ++p) xres[p] = lds_rd4(tr + p * 1024);
    PIN();
    if (!RES2) {
      const __amdgpu_buffer_rsrc_t rso = __builtin_amdgcn_make_buffer_rsrc(out, 0, S_ * D_ * 2, 0x00020000);
      const __amdgpu_buffer_rsrc_t rsf = __builtin_amdgcn_make_buffer_rsrc(xsig + wid, 0, RX * 512, 0x00020000);
      const u32x4 x2 = lds_rd4(tr + 2048), x3 = lds_rd4(tr + 3072);
      PIN();
#pragma unroll
      for (int p = 0; p < 4; ++p) { __builtin_amdgcn_raw_buffer_store_b128(p < 2 ? xres[p] : (p < 3 ? x2 : x3), rso, xo + p * (8 * ldw * 2), 0, XAUX); PIN(); }
      TE(4, 2); TE(5, 0);
      sums();
      if (lane < 48) { __builtin_amdgcn_global_atomic_fadd_f64(pst, (double)sv[0]); __builtin_amdgcn_global_atomic_fadd_f64(pst + 16, (double)sv[1]); }
      PIN();
      TE(4, 3); TE(5, 1);
      asm volatile("s_waitcnt vmcnt(2)" ::: "memory");
      __builtin_amdgcn_raw_buffer_store_b32(tag, rsf, lane * 512 + 256, 0, 16);
      *LDS_AT(volatile unsigned char, rfa) = (unsigned char)3;
      PIN();
      TE(5, 2);
      asm volatile("s_waitcnt vmcnt(1)" ::: "memory");
      __builtin_amdgcn_raw_buffer_store_b32(tag, rsf, lane * 512, 0, 16);
      PIN();
      TE(5, 3);
      __builtin_amdgcn_s_setprio(0);
      return;
    }
    TE(4, 2); TE(5, 0);
    sums();
    __hip_atomic_store(pps, f2word(sv[0]), __ATOMIC_RELAXED, __HIP_MEMORY_SCOPE_AGENT);
    __hip_atomic_store(pps + 64, f2word(sv[1]), __ATOMIC_RELAXED, __HIP_MEMORY_SCOPE_AGENT);
    if (wid == 0) STAMP(dk, 14);
  } else {
    u32x4 rres[4];
    double is1[4], is2[4];
#pragma unroll
    for (int p = 0; p < 4; ++p)
      rres[p] = __builtin_amdgcn_raw_buffer_load_b128(__builtin_amdgcn_make_buffer_rsrc(const_cast<u16*>(res), 0, S_ * D_ * 2, 0x00020000), ((row - 16 + 8 * p) * ldw + col) * 2, 0, 16);
#pragma unroll
    for (int p = 0; p < 4; ++p) { is1[p] = ld_ag(stats_in + row - 16 + 8 * p); is2[p] = ld_ag(stats_in + S_ + row - 16 + 8 * p); }
    PIN();
    if (RES2) {
#pragma unroll
      for (int p = 0; p < 4; ++p) {
        float mean, var, mb, den;
        ln_from_sums(is1[p], is2[p], mean, var, mb, den);
        const float rin = frcp(den);
#pragma unroll
        for (int e = 0; e < 4; ++e) {
          const f32x2 x = {__uint_as_float(rres[p][e] << 16), __uint_as_float(rres[p][e] & 0xffff0000u)};
          const f32x2 h = (x - f32x2{mb, mb}) * f32x2{rin, rin} + x;
          rres[p][e] = pack2(h[0], h[1]);
        }
        asm volatile("" : "+v"(rres[p]));
      }
      PIN();
    }
    if ((idx >> 5) == tmi * 4 + wid - 4) {
      const int dr = idx - (row - 16);
      u32x4 qq;
#pragma unroll 1
      for (int i = 0; i < SPIN_MAX; ++i) {
        qq = __builtin_amdgcn_raw_buffer_load_b128(__builtin_amdgcn_make_buffer_rsrc(hand, 0, HAND_N * 4, 0x00020000), (GRAN_N + Y_N) * 4 + col * 2, 0, 16);
        if (none(umax(umax(qq[0], qq[1]), umax(qq[2], qq[3])) == SENT)) break;
        asm volatile("" ::: "memory");
      }
#pragma unroll
      for (int p = 0; p < 4; ++p)
#pragma unroll
        for (int e = 0; e < 4; ++e) rres[p][e] = dr == 8 * p ? qq[e] : rres[p][e];
    }
#pragma unroll 1
    for (int i = 0; i < 4 * SPIN_MAX; ++i)
      if (__builtin_amdgcn_readfirstlane(*LDS_AT(const volatile unsigned, flg + 16)) >= 12u) break;
#pragma unroll
    for (int p = 0; p < 4; ++p) *LDS_AT(volatile u32x4, tr - 2048 + p * 1024) = rres[p];
    asm volatile("s_waitcnt lgkmcnt(0)" ::: "memory");
    announce();
    TE(4, 1); TE(7, 2);
    asm volatile("s_waitcnt vmcnt(0)" ::: "memory");
    if (!RES2) return;
    __builtin_amdgcn_s_sleep(TPRE);
    unsigned fm = SENT;
#pragma unroll 1
    for (int i = 0; i < 4 * SPIN_MAX; ++i) {
      fm = __builtin_amdgcn_readfirstlane(*LDS_AT(const volatile unsigned, fcm));
      if (fm != SENT) break;
      __builtin_amdgcn_s_sleep(1);
    }
    const unsigned r = (lane & 8) ? fm >> 16 : fm & 0xffffu;
    unsigned fra = tb + (r & 127u) * 128 + (((r ^ lane) & 7u) << 4);
    int fro = (r >> 5) == (unsigned)(tmi * 4 + wid - 4) && lane < 16 ? ((lane >> 3) * ldw + col) * 2 : S_ * D_ * 2;
    asm volatile("" : "+v"(fra), "+v"(fro));
#pragma unroll 1
    for (int i = 0; i < 4 * SPIN_MAX; ++i) {
      if (__builtin_amdgcn_readfirstlane(*LDS_AT(const volatile unsigned char, flg + 32 + wid)) == 2u) break;
      __builtin_amdgcn_s_sleep(TPSL);
    }
    asm volatile("" ::: "memory");
    __builtin_amdgcn_s_setprio(XPRIO);
    PIN();
    TE(4, 2); TE(5, 0);
    const u32x4 xr = lds_rd4(fra);
#pragma unroll
    for (int p = 0; p < 2; ++p) xres[p] = lds_rd4(tr + p * 1024);
    PIN();
    __builtin_amdgcn_raw_buffer_store_b128(xr, __builtin_amdgcn_make_buffer_rsrc(out, 0, 3 * D_ * 2, 0x00020000), fro, 0, XAUX);
  }
  TE(4, 3); TE(7, 3);
  PIN();
  __builtin_amdgcn_s_setprio(0);
  __hip_atomic_fetch_add(LDS_AT(unsigned, oad), 1u, __ATOMIC_RELAXED, __HIP_MEMORY_SCOPE_WORKGROUP);
  TE(5, 1); TE(5, 2);
  PIN();
}

#define MFMA32(a, b, c) __builtin_amdgcn_mfma_f32_32x32x16_bf16(a, b, c, 0, 0, 0)
#define MFMA16(a, b, c) __builtin_amdgcn_mfma_f32_16x16x32_bf16(a, b, c, 0, 0, 0)

#define PLO(x) __builtin_bit_cast(bf16x16, __builtin_shufflevector(x, x, 0, 1, 2, 3, 4, 5, 6, 7))
#define PHI(x) __builtin_bit_cast(bf16x16, __builtin_shufflevector(x, x, 8, 9, 10, 11, 12, 13, 14, 15))
#define SMF32(a, b, c) __builtin_amdgcn_smfmac_f32_32x32x32_bf16(__builtin_bit_cast(bf16x8, a), b, c, sidx, 0, 0)
#define SMF16(a, b, c) __builtin_amdgcn_smfmac_f32_16x16x64_bf16(__builtin_bit_cast(bf16x8, a), b, c, sidx, 0, 0)

__device__ __forceinline__ void patch_kv(unsigned tab, int r, s16x8& k0, s16x8& k1, s16x8& v0, s16x8& v1, bool on = true) {
  const int lane = fresh_lane(), hi = lane >> 5;
  if (on && (lane & 31) == kpos(r)) {
    k0 = *LDS_AT(const s16x8, tab + hi * 16); k1 = *LDS_AT(const s16x8, tab + 32 + hi * 16);
  }
  const short vv = *LDS_AT(const short, tab + 128 + (lane & 31) * 2);
  const int js = (on && hi == ((r >> 2) & 1)) ? (r >> 4) * 8 + ((r >> 3) & 1) * 4 + (r & 3) : -1;
#pragma unroll
  for (int e = 0; e < 8; ++e) { v0[e] = js == e ? vv : v0[e]; v1[e] = js == 8 + e ? vv : v1[e]; }
}

__device__ __forceinline__ void attn_safe(const u16* KV, int tile0, s16x8 q0, s16x8 q1,
                                       f32x16& o, float& l_out, int ptile, int prow, unsigned ptab) {
  const int lane = fresh_lane();
  float m = -INFINITY, l = 0.f;
  const int sidx = 0xDDDDDDDD;
  const __amdgpu_buffer_rsrc_t rs = __builtin_amdgcn_make_buffer_rsrc(const_cast<u16*>(KV), 0, 2 * S_ * D_ * 2, 0x00020000);
#pragma unroll
  for (int e = 0; e < 16; ++e) o[e] = 0.f;
  for (int tt = 0; tt < 64; ++tt) {
    const int t = tile0 + tt, off = t * 2048 + lane * 16;
    s16x8 k0 = __builtin_bit_cast(s16x8, __builtin_amdgcn_raw_buffer_load_b128(rs, off, 0, 16));
    s16x8 k1 = __builtin_bit_cast(s16x8, __builtin_amdgcn_raw_buffer_load_b128(rs, off + 1024, 0, 16));
    s16x8 v0 = __builtin_bit_cast(s16x8, __builtin_amdgcn_raw_buffer_load_b128(rs, off + S_ * D_ * 2, 0, 16));
    s16x8 v1 = __builtin_bit_cast(s16x8, __builtin_amdgcn_raw_buffer_load_b128(rs, off + S_ * D_ * 2 + 1024, 0, 16));
    if (t == ptile) patch_kv(ptab, prow, k0, k1, v0, v1);
    f32x16 sc;
#pragma unroll
    for (int e = 0; e < 16; ++e) sc[e] = 0.f;
    sc = MFMA32(k0, q0, sc); sc = MFMA32(k1, q1, sc);
    float mt = sc[0];
#pragma unroll
    for (int e = 1; e < 16; ++e) mt = fmaxf(mt, sc[e]);
    mt = fmaxf(mt, __shfl_xor(mt, 32, 64));
    const float mn = fmaxf(m, mt);
    const float alpha = __builtin_amdgcn_exp2f(m - mn);
    m = mn;
#pragma unroll
    for (int e = 0; e < 16; ++e) sc[e] = __builtin_amdgcn_exp2f(sc[e] - mn);
    float ls = 0.f;
#pragma unroll
    for (int e = 0; e < 16; ++e) ls += __uint_as_float(__float_as_uint(sc[e]) & 0xffff0000u);
    l = l * alpha + ls;
#pragma unroll
    for (int e = 0; e < 16; ++e) o[e] *= alpha;
    o = SMF32(v0, PLO(sc), o); o = SMF32(v1, PHI(sc), o);
  }
  l_out = l + __shfl_xor(l, 32, 64);
}

__device__ __forceinline__ float wave_fmax(float v) {
  asm volatile("s_nop 1\n\tv_max_f32_dpp %0, %0, %0 row_shr:1 row_mask:0xf bank_mask:0xf\n\ts_nop 1\n\tv_max_f32_dpp %0, %0, %0 row_shr:2 row_mask:0xf bank_mask:0xf\n\t"
               "s_nop 1\n\tv_max_f32_dpp %0, %0, %0 row_shr:4 row_mask:0xf bank_mask:0xf\n\ts_nop 1\n\tv_max_f32_dpp %0, %0, %0 row_shr:8 row_mask:0xf bank_mask:0xf\n\t"
               "s_nop 1\n\tv_max_f32_dpp %0, %0, %0 row_bcast:15 row_mask:0xa bank_mask:0xf\n\ts_nop 1\n\tv_max_f32_dpp %0, %0, %0 row_bcast:31 row_mask:0xc bank_mask:0xf\n\ts_nop 0"
               : "+v"(v));
  return __int_as_float(__builtin_amdgcn_readlane(__float_as_int(v), 63));
}

#define PSLP 8
#define WSLP 3
#define PRE1 8
#define PRE5 8
#define W3SLP 4
#define FSTART 10
#define OAUX 16
#define OMX 4
#define T0 2u
#define TW 10u
#define HW 2
#define HP1 1
#define POLLAT 11
#define X2AT 2
#define HPRIO 3
#define SSLP 1
#define SPRE 6
#define WGAPS {0, 1, 7, 8, 9, 10, 11, 12}
#define KVS 4
#define PNS 12
#define G0 3u
#define GS 2u
#define IAT 1u
#define YSLP 2
#define ISLP 8
#define KSLP 2
#define UAT 13u
#define UHW 0
#define PPSLP 2
#define XRSLP 2
#define MPRE 20
#define YD1 150
#define YD2 180
#define KDT 200
#define XDT 45
#define R2PRIO 0
#define LNPRIO 1
#define MSLP 4
#define BARG 0
struct Args {
  long long lay;
  const u16* a; u16* kv; u16* o; u16* xo; double* so;
  const u16* W; u16* wc;
  const u16 *wg, *wu, *wd, *wo, *w_r;
  unsigned* gran; float* csf; double* stats; unsigned* hand; u16* out;
};
typedef __attribute__((address_space(4))) const Args* ArgP;
#define WCAT 3
#define CAP0 1
#define PRE4 50
#define PRE7 20
#define UARR 12u
#define GF0 10
#define XGATE 8
#define ASLP 1
#define B15 3
#define KO 5
#define PRO 2
#define HS 2
#define XD2 90
#define TFL 4u
#define QPRE 10
#define QSLP 1
#define GW 1u
#define G1 8u
#define GFD 2
#define WMAP 0x32100333
#define SMAP 0
#define STCH 6
#define KPOLL 10
#define TPRIO 2
#define TSLP 9
__device__ __forceinline__ bool slow_cols(const u16* W, int tni) { return ((((unsigned)(uintptr_t)W >> 7) + tni) & 7) == 3; }
#define ARGS_HERE                                                                                                      \
  ArgP ap_ = ap; asm volatile("" : "+s"(ap_));                                                                          \
  const u16* A = ap_->a; const u16* __restrict__ W = ap_->W; u16* KV = ap_->kv; u16* Of = ap_->o;        \
  const u16* __restrict__ wg = ap_->wg; const u16* __restrict__ wu = ap_->wu; const u16* __restrict__ wd = ap_->wd;       \
  const u16* __restrict__ wo = ap_->wo; const u16* __restrict__ w_r = ap_->w_r; unsigned* gran = ap_->gran;              \
  float* csf = ap_->csf; const double* stats = ap_->stats; double* stats_out = ap_->so; unsigned* hand = ap_->hand; \
  u16* xout = ap_->xo; u16* __restrict__ out = ap_->out
#define AGAIN "s_getpc_b64 s[90:91]\n\ts_add_u32 s90, s90, layers_kernel@rel32@lo+4\n\ts_addc_u32 s91, s91, layers_kernel@rel32@hi+12\n\ts_setpc_b64 s[90:91]"
#define KEPT(c)                                                                                                        \
  c "{v[104:107]}"(gu[0]), c "{v[108:111]}"(gu[1]), c "{v[112:115]}"(gu[2]), c "{v[116:119]}"(gu[3]), c "{v[120:123]}"(gu[4]),      \
  c "{v[124:127]}"(gu[5]), c "{v[128:131]}"(gu[6]), c "{v[132:135]}"(gu[7]), c "{v[136:139]}"(gu[8]), c "{v[140:143]}"(gu[9]),      \
  c "{v[144:147]}"(gu[10]), c "{v[148:151]}"(gu[11]), c "{v[152:155]}"(gu[12]), c "{v[156:159]}"(gu[13]), c "{v[160:163]}"(gu[14]), \
  c "{v[164:167]}"(gu[15])
#define LAYER_HERE ArgP apl_ = ap; asm volatile("" : "+s"(apl_)); const bool LAY2 = apl_->lay != 0
__device__ __forceinline__ void layer_body(const int wid, const int bid, const unsigned lbase, const ArgP ap, const unsigned long long t_entry) {
  constexpr int K = 1024, NKT = 16, NST = 3, ABYTES = 16384, WBYTES = 24576, STB = ABYTES + WBYTES, ldw = 3 * D_;
  constexpr int TABA = NST * STB, ALPHA = TABA + 2048, CRED = ALPHA + 1024;
  constexpr int KSB = 8192, ASB = 16384, FLG = 8 * ASB + 256;
  constexpr int DUM = FLG + 64, TAB = DUM + 256, RED = TAB + 512, SACT = RED + 4096, XML = SACT + 3584, UGL = XML + 4096, SPM = UGL + 1024, QOFF = 9 * ASB;
  constexpr int TABW = RED;
  static_assert(ALPHA % 1024 == 0 && RK == 16, "");
  static_assert(SPM + 2048 <= QOFF && CRED <= 8 * ASB && RED + 8192 <= QOFF && RED % 16 == 0, "lds");
  const int xcd = (bid & 1) * 4 + ((bid >> 1) & 3), loc = bid >> 3;
  const int tmi = (xcd / QNG) * QMX + loc / QNX, tni = (xcd % QNG) * QNX + loc % QNX;
  const int m0 = tmi * 128, n0 = tni * 64;
  const int omi = (xcd / (OMX / 2)) * OMX + loc / (32 / OMX), oni = (xcd % (OMX / 2)) * (32 / OMX) + loc % (32 / OMX);
  constexpr unsigned VOFF = (unsigned)S_ * D_ * 2;
  auto tile_at = [&](unsigned n) __attribute__((always_inline)) {
#pragma unroll 1
    for (int i = 0; i < 4 * SPIN_MAX; ++i) {
      if (__builtin_amdgcn_readfirstlane(*LDS_AT(const volatile unsigned, lbase + FLG + 36)) == n * 0x01010101u) break;
      __builtin_amdgcn_s_sleep(1);
    }
    asm volatile("" ::: "memory");
    PIN();
  };
  auto all_ge = [&](unsigned w, unsigned n) { return ((w + (128u - n) * 0x01010101u) & 0x80808080u) == 0x80808080u; };
  if (wid >= 8) {
    u32x4 gu[16];
    LAYER_HERE;
    constexpr int dk = 6;
    const int hw = wid - 8, b = bid;
    int nb = 0;
    constexpr int lim = NKT - 1;
    auto tick_nw = [&]() {
      PIN();
      if (nb < lim) { __builtin_amdgcn_s_barrier(); ++nb; } else __builtin_amdgcn_s_sleep(2);
      asm volatile("" ::: "memory");
      PIN();
    };
    auto tick = [&]() {
      asm volatile("s_waitcnt lgkmcnt(0)" ::: "memory");
      tick_nw();
    };
    auto fold16 = [&](f32x2 y0, f32x2 y1) __attribute__((always_inline)) {
      const auto q0 = __builtin_amdgcn_permlane32_swap(__float_as_uint(y0[0]), __float_as_uint(y1[0]), false, false);
      const auto q1 = __builtin_amdgcn_permlane32_swap(__float_as_uint(y0[1]), __float_as_uint(y1[1]), false, false);
      const auto q = __builtin_amdgcn_permlane16_swap(__float_as_uint(__uint_as_float(q0[0]) + __uint_as_float(q0[1])),
                                                      __float_as_uint(__uint_as_float(q1[0]) + __uint_as_float(q1[1])), false, false);
      return __uint_as_float(q[0]) + __uint_as_float(q[1]);
    };
    unsigned best, tag, rmd;
    float alpha, m, wsum;
    u32x4 tw;
    u32x2 xq;
    s16x8 sl[8];
    const int kcap = CAP0 + (tmi & 7);
    if (!LAY2) {
      ARGS_HERE;
      const int lane = fresh_lane(), t = hw * 64 + lane;
      const unsigned ep = ld_ag(gran);
      const int srow = m0 + tni * 8 + hw * 2;
      u32x4 xa[2][2], wv[8];
      const int wr_ = (3 - ((unsigned)(uintptr_t)W >> 7) - (tni & ~3)) & 7;
      const bool cpy = hw == 3 && wr_ < 4 && wr_ != (tni & 3), wld = hw < 3 ? wr_ != (tni & 3) : cpy;
      const int wcn = cpy ? (tni & ~3) + wr_ : tni, wcp = cpy ? (tni - wr_ - 1) & 3 : hw;
      const int wof = ((tmi * 64 + (lane >> 3)) * ldw + wcp * D_ + wcn * 64 + (lane & 7) * 8) * 2;
      auto ld_w = [&]() {
        const __amdgpu_buffer_rsrc_t rw = __builtin_amdgcn_make_buffer_rsrc(const_cast<u16*>(W), 0, wld ? D_ * ldw * 2 : 0, 0x00020000);
#pragma unroll
        for (int r = 0; r < 8; ++r) wv[r] = __builtin_amdgcn_raw_buffer_load_b128(rw, wof, r * 8 * ldw * 2, 0);
        PIN();
      };
      auto wcopy = [&]() __attribute__((always_inline)) {
        if (cpy) {
          const __amdgpu_buffer_rsrc_t rw = __builtin_amdgcn_make_buffer_rsrc(ap_->wc, 0, D_ * ldw * 2, 0x00020000);
#pragma unroll
          for (int r = 0; r < 8; ++r) __builtin_amdgcn_raw_buffer_store_b128(wv[r], rw, wof + r * 8 * ldw * 2, 0, 16);
        }
        PIN();
      };
      const int wat = ((hw == 3 ? SMAP : WMAP) >> (4 * (tmi & 7))) & 3;
      unsigned ps = 0u;
      auto stouch = [&](int at) __attribute__((always_inline)) {
        if (at == STCH && cpy) ps = __builtin_amdgcn_raw_buffer_load_b32(__builtin_amdgcn_make_buffer_rsrc(const_cast<u16*>(W), 0, D_ * ldw * 2, 0x00020000),
                                                                        (((tmi ^ 8) * 64 + lane) * ldw + wcp * D_ + wcn * 64) * 2, 0, 0);
        PIN();
      };
      auto ld_x = [&](int h) {
#pragma unroll
        for (int r = 0; r < 2; ++r) xa[r][h] = *reinterpret_cast<const u32x4*>(A + (size_t)(srow + r) * D_ + (h ^ (tmi >> 3)) * 512 + lane * 8);
        PIN();
      };
      ld_x(0);
      if (X2AT == 0) ld_x(1);
      if (wat == 0) ld_w();
      unsigned pc = 0u;
      if (hw == 3) {
        unsigned long long a0, a1;
        CODE_ADDR("layers_kernel", a0);
        CODE_ADDR("end_marker_kernel", a1);
        pc = touch_code(a0, a1, loc, lane);
      }
      if (hw == 1) pc = ld_ag(gran + ((lane & 1) ? GR_ROW + (m0 + tni * 8) * 4 : GR_KV + (tni * RK + tmi % RK) * 32));
      if (hw == 2) st_ag(gran + GR_N + bid * 64 + lane, lane + 1u);
      {
        const int gi = bid * 256 + t;
        if (gi < GRAN_N / 4) __builtin_amdgcn_raw_buffer_store_b128(u32x4{SENT, SENT, SENT, SENT}, __builtin_amdgcn_make_buffer_rsrc(hand, 0, GRAN_N * 4, 0x00020000), gi * 16, 0, 16);
        if (t < 24) __hip_atomic_store(stats_out + (t >> 3) * S_ + m0 + tni * 8 + (t & 7), 0.0, __ATOMIC_RELAXED, __HIP_MEMORY_SCOPE_AGENT);
      }
      tick();
      if (wat == 1) ld_w();
      if (X2AT == 1) ld_x(1);
      tick();
      if (wat == 2) ld_w();
      if (WCAT == 2) wcopy();
      if (X2AT == 2) ld_x(1);
      asm volatile("" :: "v"(ep));
      tick();
      if (wat == 3) ld_w();
      if (WCAT == 3) wcopy();
      if (X2AT == 3) ld_x(1);
      stouch(3);
#pragma unroll
      for (int i = 0; i < HW; ++i) { tick(); stouch(4 + i); }
      __builtin_amdgcn_s_setprio(HP1);
      PIN();
      tag = ep * 1664525u + 1013904223u;
      const __amdgpu_buffer_rsrc_t rsc = __builtin_amdgcn_make_buffer_rsrc(gran, 0, GR_N * 4, 0x00020000);
      {
        u32x4 od;
#pragma unroll
        for (int q = 0; q < 4; ++q) od[q] = ((lane & 7) >> 1) == q ? ((lane & 1) ? 0x3f800000u : 0x00003f80u) : 0u;
        f32x4 d[2] = {{0.f, 0.f, 0.f, 0.f}, {0.f, 0.f, 0.f, 0.f}};
#pragma unroll
        for (int r = 0; r < 8; ++r) d[r & 1] = __builtin_amdgcn_mfma_f32_16x16x32_bf16(__builtin_bit_cast(s16x8, od), __builtin_bit_cast(s16x8, wv[r]), d[r & 1], 0, 0, 0);
        u32x4 gr;
#pragma unroll
        for (int q = 0; q < 4; ++q) gr[q] = ((__float_as_uint(dpp_add<0x128, 0xf>(fadd(d[0][q], d[1][q]))) + 0x80u) & ~0xffu) | ((tag >> (8 * q)) & 0xffu);
        asm volatile("s_waitcnt vmcnt(0)" ::: "memory");
        __builtin_amdgcn_raw_buffer_store_b128(gr, rsc, (lane & 40) || !wld ? GR_N * 4 : (GR_COL + (wcn * 16 + tmi) * 192 + wcp * 64 + (lane & 7) * 8 + (lane >> 4) * 4) * 4, 0, 16);
      }
      asm volatile("" :: "v"(pc));
      tick_nw();
      stouch(6);
      {
        float xv[2][16], mean[2];
#pragma unroll
        for (int r = 0; r < 2; ++r) {
          float sm = 0.f;
#pragma unroll
          for (int i = 0; i < 4; ++i) {
            xv[r][2 * i] = __uint_as_float(xa[r][0][i] << 16); xv[r][2 * i + 1] = __uint_as_float(xa[r][0][i] & 0xffff0000u);
            xv[r][8 + 2 * i] = __uint_as_float(xa[r][1][i] << 16); xv[r][8 + 2 * i + 1] = __uint_as_float(xa[r][1][i] & 0xffff0000u);
          }
#pragma unroll
          for (int i = 0; i < 16; ++i) sm += xv[r][i];
          mean[r] = wave_sum(sm) * (1.0f / 1024.0f);
          tick();
          stouch(7 + r);
        }
        float vr[2];
#pragma unroll
        for (int r = 0; r < 2; ++r) {
          float v = 0.f;
#pragma unroll
          for (int i = 0; i < 16; ++i) { const float d = xv[r][i] - mean[r]; v += d * d; }
          vr[r] = wave_sum(v) * (1.0f / 1024.0f);
          if (r == 0) tick();
        }
        const float vv = lane & 2 ? vr[1] : vr[0], mm = lane & 2 ? mean[1] : mean[0];
        const float rin = 1.0f / rbf(sqrtf(rbf(rbf(vv) + 1e-5f)));
        if (lane < 4) __builtin_amdgcn_raw_buffer_store_b64(u32x2{__float_as_uint((lane & 1) ? rbf(mm) : rin), tag}, rsc, (GR_ROW + srow * 4 + lane * 2) * 4, 0, 16);
      }
      tick();
      float cs;
      while (nb < POLLAT) tick();
      asm volatile("" :: "v"(ps));
#pragma unroll 1
      for (int spin = 0;; ++spin) {
        u32x4 csv[4];
        const __amdgpu_buffer_rsrc_t rsw = __builtin_amdgcn_make_buffer_rsrc(gran, 0, hw < 3 ? GR_N * 4 : 0, 0x00020000);
#pragma unroll
        for (int i = 0; i < 4; ++i) csv[i] = __builtin_amdgcn_raw_buffer_load_b128(rsw, (GR_COL + (tni * 16 + (lane >> 4)) * 192 + hw * 64 + (lane & 15) * 4) * 4, i * (4 * 192 * 4), 16);
        const u32x4 rp = __builtin_amdgcn_raw_buffer_load_b128(rsc, (GR_ROW + (hw < 2 ? m0 + t : (m0 ^ 128) + tni * 8 + (t & 7)) * 4) * 4, 0, 16);
        PIN();
        tick(); tick();
        unsigned bad = 0u;
        float z[4];
#pragma unroll
        for (int q = 0; q < 4; ++q) {
          const unsigned tq = tag >> (8 * q);
          bad |= ((csv[0][q] ^ tq) | (csv[1][q] ^ tq)) | ((csv[2][q] ^ tq) | (csv[3][q] ^ tq));
          z[q] = fadd(fadd(__uint_as_float(csv[0][q] & ~0xffu), __uint_as_float(csv[1][q] & ~0xffu)), fadd(__uint_as_float(csv[2][q] & ~0xffu), __uint_as_float(csv[3][q] & ~0xffu)));
        }
        cs = fold16(f32x2{z[0], z[1]}, f32x2{z[2], z[3]});
        bad = (hw < 3 ? bad & 0xffu : 0u) | (rp[1] ^ tag) | (rp[3] ^ tag);
        alpha = __uint_as_float(rp[0]); m = __uint_as_float(rp[2]);
        if (none(bad != 0u) || spin >= SPIN_MAX) break;
      }
      if (tmi == 0 && hw < 3) st_ag(reinterpret_cast<unsigned*>(csf) + hw * 1024 + n0 + (lane & 15) * 4 + (lane >> 4), __float_as_uint(cs));
      unsigned c0, c1, c2;
      split3(cs, c0, c1, c2);
      tw = u32x4{c0 | (c1 << 16), c0 | (c2 << 16), c1 | (c0 << 16), 0u};
      best = rmd = 0u;
      wsum = 0.f;
      asm volatile("" : "=v"(xq), KEPT("="));
#pragma unroll
      for (int i = 0; i < 8; ++i) asm volatile("" : "=v"(sl[i]));
    } else {
      asm volatile("; kept registers" : KEPT("="));
      PIN();
      ARGS_HERE;
      const int lane = fresh_lane(), t = hw * 64 + lane;
      best = rmd = 0u;
      tag = __builtin_amdgcn_readfirstlane(*LDS_AT(const volatile unsigned, lbase + FLG + 20));
#pragma unroll
      for (int i = 0; i < 8; ++i) asm volatile("" : "=v"(sl[i]));
      unsigned sad;
      auto tickc = [&]() __attribute__((always_inline)) {
        tick();
        if (nb == kcap + 1) {
#pragma unroll
          for (int i = 0; i < 8; ++i) {
            const unsigned a = (sad ^ ((i >> 2) * 64) ^ (((i >> 1) & 1) * 16)) + (i & 1) * 12288;
            sl[i] = __builtin_shufflevector(__builtin_amdgcn_ds_read_tr16_b64_v4i16(LDS_AT(s16x4, a)), __builtin_amdgcn_ds_read_tr16_b64_v4i16(LDS_AT(s16x4, a + 1536)), 0, 1, 2, 3, 4, 5, 6, 7);
          }
        }
        PIN();
      };
      static_assert(CAP0 + 8 <= KPOLL, "the stage is taken before that");
      if (hw == 1) STAMP(4, 4);
      if (hw == 0) {
        __builtin_amdgcn_s_sleep(TPRE);
        tile_at(3u);
        __builtin_amdgcn_s_sleep(XGATE);
        STAMP(4, 3);
#pragma unroll 1
        for (int spin = 0; spin < SPIN_MAX; ++spin) {
          if (none(ld_ag(gran + GR_X + (tmi * RX + tni % RX) * 128 + 64 + lane) != tag)) break;
          __builtin_amdgcn_s_sleep(1);
        }
      }
      static_assert(UHW == 0, "");
      if (hw == 0) asm volatile("s_waitcnt vmcnt(0)" ::: "memory");
      if (hw == 0) STAMP(4, 5);
      asm volatile("" ::: "memory");
      PIN();
      __builtin_amdgcn_s_barrier();
      PIN();
      if (hw == 1) STAMP(4, 6);
      {
        const int ln = fresh_lane(), g4 = ln >> 4, tp = ln & 3, k = 8 * g4 + ((ln & 15) >> 2);
        sad = lbase + (kcap % NST) * STB + ABYTES + k * 384 + (((2 * (tp >> 1)) ^ (4 * ((k >> 1) & 1)) ^ (g4 & 1)) << 4) + 8 * (tp & 1) + (hw % 3) * 128;
      }
      wsum = __uint_as_float(__builtin_amdgcn_readfirstlane(*LDS_AT(const volatile unsigned, lbase + FLG + 60)));
      asm volatile("" : "+v"(sad), "=v"(tw), "=v"(alpha), "=v"(m));
      PIN();
      const int row = m0 + (t & 127);
      const __amdgpu_buffer_rsrc_t rsg = __builtin_amdgcn_make_buffer_rsrc(hand, 0, GRAN_N * 4, 0x00020000);
      if (hw < 2 || tmi == tni) {
      while (nb < HS) tickc();
#pragma unroll 1
      for (int spin = 0; spin < SPIN_MAX; ++spin) {
        const unsigned v = ld_ag(gran + GR_X + (tmi * RX + tni % RX) * 128 + lane);
        tickc();
        if (none(v != tag)) break;
      }
      asm volatile("" ::: "memory");
      PIN();
      const double s1 = ld_ag(stats + row), s2 = ld_ag(stats + S_ + row), sw = ld_ag(stats + 2 * S_ + row);
      tickc();
      float mean, var, mb, den;
      ln_from_sums(s1, s2, mean, var, mb, den);
      const float rin = frcp(den), g1 = 1.0f + rin;
      const float mb2 = rbf(mean + (mean - mb) * rin);
      const float den2 = rbf(__builtin_amdgcn_sqrtf(rbf(rbf(var * g1 * g1) + 1e-5f)));
      alpha = g1 * frcp(den2); m = (mb * rin + mb2) * frcp(g1);
      if (tmi == tni && hw >= 2) {
        const float r = (float)fma(-(double)mb, (double)wsum, sw) * rin;
        const unsigned rb = __float_as_uint(r) + 0x8000u;
        unsigned rkey = (rb ^ ((unsigned)((int)rb >> 31) | 0x80000000u)) & 0xfffff800u;
        if (r != r) rkey = 0u;
        rkey = wave_umax(rkey | (unsigned)(2047 - row));
        const unsigned md = __builtin_amdgcn_readlane(f2word(__uint_as_float((__float_as_uint(mb) >> 16) | (__float_as_uint(den) & 0xffff0000u))), (2047 - (int)(rkey & 0xffffu)) & 63);
        __builtin_amdgcn_raw_buffer_store_b64(u32x2{rkey, md}, rsg, lane < NREP ? lane * 256 + (tmi * 2 + hw - 2) * 8 : GRAN_N * 4, 0, 16);
      }
      }
      while (nb < KPOLL) tickc();
#pragma unroll 1
      for (int spin = 0; spin < SPIN_MAX; ++spin) {
        const u32x2 v = __builtin_amdgcn_raw_buffer_load_b64(rsg, (bid % NREP) * 256 + (lane & 31) * 8, 0, 16);
        tick(); tick();
        if (none(umax(v[0], v[1]) == SENT)) {
          best = wave_umax(v[0]);
          rmd = __builtin_amdgcn_readlane(v[1], ((2047 - (int)(best & 0xffffu)) >> 6) & 31);
          break;
        }
      }
      const int ix = (2047 - (int)(best & 0xffffu)) & 2047;
      if (row == ix) alpha = 0.f;
      xq = __builtin_amdgcn_raw_buffer_load_b64(__builtin_amdgcn_make_buffer_rsrc(const_cast<u16*>(A), 0, S_ * D_ * 2, 0x00020000), (ix * D_ + hw * 256 + (lane & 3) * 64 + (lane >> 2) * 4) * 2, 0, 16);
      PIN();
    }
    tag = __builtin_amdgcn_readfirstlane(tag);
    wsum = __uint_as_float(__builtin_amdgcn_readfirstlane(__float_as_uint(wsum)));
    const int lane = fresh_lane(), t = hw * 64 + lane;
    {
      unsigned a0, a1, a2;
      split3(-m, a0, a1, a2);
      u32x4 ta = {a0 | (a0 << 16), a1 | (a0 << 16), a1 | (a2 << 16), 0u};
      asm volatile("" : "+v"(tw), "+v"(ta), "+v"(alpha));
      __builtin_amdgcn_s_setprio(TPRIO);
      if (nb < lim) {
        while (nb < lim) tick();
        __builtin_amdgcn_s_sleep(TSLP);
      }
      if (!LAY2 && hw < 3) *LDS_AT(u32x4, lbase + TABW + ((hw ^ (hw < 2 ? ((lane >> 3) ^ tmi) & 1 : 0)) * 64 + (lane & 15) * 4 + (lane >> 4)) * 16) = tw;
      if (t < 128) {
        *LDS_AT(u32x4, lbase + TABA + t * 16) = ta;
        *LDS_AT(float, lbase + ALPHA + t * 4) = alpha;
        *LDS_AT(float, lbase + ALPHA + 512 + t * 4) = alpha * QSCALE;
      }
    }
    if (t >= 192 && t < 196) *LDS_AT(u32x4, lbase + CRED + ((t >> 1) & 1) * 512 + (t & 1) * 128) = u32x4{0u, 0u, 0u, 0u};
    asm volatile("s_waitcnt lgkmcnt(0)" ::: "memory");
    PIN();
    __builtin_amdgcn_s_barrier();
    PIN();
    *LDS_AT(u32x4, lbase + XML + t * 16) = u32x4{SENT, SENT, SENT, SENT};
    *LDS_AT(u32x4, lbase + SPM + (t & 127) * 16) = u32x4{SENT, SENT, SENT, SENT};
    if (hw == 0) {
      unsigned v;
      asm volatile("v_mov_b32 %0, 0\n\tv_writelane_b32 %0, 2, 0\n\tv_writelane_b32 %0, %1, 5\n\tv_writelane_b32 %0, %2, 7"
                   : "=&v"(v) : "s"(tag), "s"(best));
      lds_wr(lbase + FLG + lane * 4, v);
    }
    const int idx = LAY2 ? (2047 - (int)(best & 0xffffu)) & 2047 : -64;
    const bool p1blk = (oni & 7) != 7, fast = tmi < 8, own = fast && (loc & 1);
    const int fg = xcd * 28 + (p1blk ? loc - (loc >> 3) : 0), ow = (xcd & 3) * 16 + (loc >> 1);
    static_assert(OMX == 4, "oni % 8 = loc % 8");
#define GLD(a, b) "global_load_dwordx4 v[" #a ":" #b "], %0, off\n\tv_lshl_add_u64 %0, %0, 0, %1\n\t"
#define GLD4(...) asm volatile(__VA_ARGS__ : "+v"(p) : "s"((unsigned long long)(F_ * 2)), KEPT() : "memory")
    auto gptr = [&](const u16* wg, const u16* wu) {
      const int ln = fresh_lane();
      return ((ln & 2) ? wu : wg) + (size_t)(hw * 256 + (ln >> 2) * 4) * F_ + fg * 16 + (ln & 1) * 8;
    };
    auto gather = [&](const u16* p, int st) __attribute__((always_inline)) {
      const int e = (st + fg) & 3;
      p += e * (64 * F_);
      if (e == 0) GLD4(GLD(104, 107) GLD(108, 111) GLD(112, 115) GLD(116, 119));
      else if (e == 1) GLD4(GLD(120, 123) GLD(124, 127) GLD(128, 131) GLD(132, 135));
      else if (e == 2) GLD4(GLD(136, 139) GLD(140, 143) GLD(144, 147) GLD(148, 151));
      else GLD4(GLD(152, 155) GLD(156, 159) GLD(160, 163) GLD(164, 167));
    };
    auto dmask = [&](int q) { const int ln = fresh_lane(); return ((ln >> 1) & 1) == q ? ((ln & 1) ? 0xffff0000u : 0x0000ffffu) : 0u; };
    unsigned dm0, dm1, dad;
    auto gu_setup = [&]() __attribute__((always_inline)) {
      const int ln = fresh_lane();
      dm0 = dmask(0); dm1 = dmask(1);
      dad = lbase + SACT + ((ln & 3) * 8 + (ln >> 5) * 4 + ((ln >> 4) & 1)) * 16 + hw * 4;
      asm volatile("" : "+v"(dm0), "+v"(dm1), "+v"(dad));
    };
    auto gate_up = [&](u32x2 x, unsigned md) __attribute__((always_inline)) {
      const unsigned rin = __builtin_amdgcn_readfirstlane(__float_as_uint(frcp(__uint_as_float(md & 0xffff0000u))));
      asm volatile("s_getpc_b64 s[90:91]\n\ts_add_u32 s90, s90, gate_up_code@rel32@lo+4\n\ts_addc_u32 s91, s91, gate_up_code@rel32@hi+12\n\ts_swappc_b64 s[92:93], s[90:91]"
                   : "+{v[100:101]}"(x) : "{s84}"(md << 16), "{s85}"(md & 0xffff0000u), "{s86}"(rin), "{v99}"(dad), "{v102}"(dm0), "{v103}"(dm1), KEPT()
                   : "v84", "v85", "v86", "v87", "v88", "v89", "v90", "v91", "v92", "v93", "v94", "v95", "v96", "v97", "s88", "s89", "s90", "s91", "s92", "s93", "scc", "memory");
    };
    auto act_diag = [&](int so = 0) __attribute__((always_inline)) {
      const int ln = fresh_lane();
      const f32x4 g4 = *LDS_AT(const volatile f32x4, lbase + SACT + so + (ln >> 2) * 16), u4 = *LDS_AT(const volatile f32x4, lbase + SACT + so + 256 + (ln >> 2) * 16);
      float gv = fadd(fadd(g4[0], g4[1]), fadd(g4[2], g4[3])), uv = fadd(fadd(u4[0], u4[1]), fadd(u4[2], u4[3]));
      rbf2(gv, uv);
      const float av = rbf(gv * frcp(1.0f + __expf(-gv))) * uv;
      const unsigned ap2 = pack2(av, av);
      return __builtin_bit_cast(s16x4, u32x2{ap2 & dm0, ap2 & dm1});
    };
    auto down = [&](const s16x4& a, const u32x2 (&w)[16]) __attribute__((always_inline)) {
      f32x4 d[2] = {{0.f, 0.f, 0.f, 0.f}, {0.f, 0.f, 0.f, 0.f}};
      static_for<0, 16>([&](auto fc) __attribute__((always_inline)) {
        constexpr int f = decltype(fc)::value;
        d[f & 1] = __builtin_amdgcn_mfma_f32_4x4x4bf16_1k(a, __builtin_bit_cast(s16x4, w[f]), d[f & 1], 4, f, 0);
      });
      f32x4 z;
#pragma unroll
      for (int e = 0; e < 4; ++e) { z[e] = d[0][e] + d[1][e]; asm volatile("" : "+v"(z[e])); }
      return u32x4{f2word(z[0]), f2word(z[1]), f2word(z[2]), f2word(z[3])};
    };
    auto fold8 = [&](f32x2 y0, f32x2 y1) __attribute__((always_inline)) { return dpp_add<0x128, 0xf>(fold16(y0, y1)); };
    auto fold4 = [&](f32x2 y0, f32x2 y1) __attribute__((always_inline)) { return dpp_add<0x124, 0xf>(fold8(y0, y1)); };
    auto y_sum = [&](const __amdgpu_buffer_rsrc_t rs, auto nbc) __attribute__((always_inline)) {
      constexpr int NB = decltype(nbc)::value, N = 14 / NB;
      const int ln = fresh_lane(), src = (GRAN_N + (ln >> 2) * D_ + ow * 16 + (ln & 3) * 4) * 4;
      f32x2 y0 = {0.f, 0.f}, y1 = {0.f, 0.f};
#pragma unroll 1
      for (int bt = 0; bt < NB; ++bt) {
        f32x2 z0, z1;
#pragma unroll 1
        for (int spin = 0; spin < SPIN_MAX; ++spin) {
          u32x4 v[N];
#pragma unroll
          for (int i = 0; i < N; ++i) v[i] = __builtin_amdgcn_raw_buffer_load_b128(rs, src, (bt * N + i) * (16 * D_ * 4), 16);
          z0 = y0; z1 = y1;
#pragma unroll
          for (int i = 0; i < N; ++i) {
            z0 = f32x2{fadd(z0[0], __uint_as_float(v[i][0])), fadd(z0[1], __uint_as_float(v[i][1]))};
            z1 = f32x2{fadd(z1[0], __uint_as_float(v[i][2])), fadd(z1[1], __uint_as_float(v[i][3]))};
          }
          const float chk = fadd(fadd(z0[0], z0[1]), fadd(z1[0], z1[1]));
          if (none(chk != chk)) break;
          unsigned mx = 0u;
#pragma unroll
          for (int i = 0; i < N; ++i) mx = umax(umax(umax(mx, v[i][0]), v[i][1]), umax(v[i][2], v[i][3]));
          if (none(mx == SENT)) break;
          __builtin_amdgcn_s_sleep(YSLP);
          asm volatile("" ::: "memory");
        }
        y0 = z0; y1 = z1;
      }
      return fold4(y0, y1);
    };
    [&]() __attribute__((always_inline)) {
    ARGS_HERE;
    const unsigned l16 = lane * 16;
    unsigned* kvflag = gran + GR_KV + ((LAY2 ? 16 : 0) + tni) * (32 * RK);
    auto wait_sig = [&](int sh, unsigned n) {
#pragma unroll 1
      for (int i = 0; i < 4 * SPIN_MAX; ++i) {
        if (((__builtin_amdgcn_readfirstlane(*LDS_AT(const volatile unsigned, lbase + FLG + 32)) >> sh) & 7u) >= n) break;
        __builtin_amdgcn_s_sleep(SSLP);
      }
      asm volatile("" ::: "memory");
      PIN();
    };
    PIN();
    asm volatile("s_waitcnt lgkmcnt(0)" ::: "memory");
    if (!LAY2) asm volatile("s_waitcnt vmcnt(0)" ::: "memory");
    __builtin_amdgcn_s_barrier();
    PIN();
    __builtin_amdgcn_s_setprio(HPRIO);
    if (hw < 3) {
      __builtin_amdgcn_s_sleep(SPRE);
      wait_sig((hw & 1) * 8, 2u);
      __builtin_amdgcn_raw_buffer_store_b32(tag, __builtin_amdgcn_make_buffer_rsrc(kvflag, 0, hw < 2 ? RK * 128 : 0, 0x00020000), lane * 256 + ((tmi ^ hw) & 1) * 128 + tmi * 4, 0, 16);
      if (hw == 0) STAMP(LAY2 ? 5 : 1, 16);
    }
    if (hw == 3) {
      __builtin_amdgcn_s_sleep(KVS);
      const unsigned kv0 = ld_ag(kvflag + (tmi % RK) * 32 + (lane & 15));
      PIN();
      const char* cnt = reinterpret_cast<const char*>(gran + GR_N + bid * 64 + 2);
      unsigned vk[4], vp = lbase + FLG + 8 + (lane & 7), pw = 0u;
#pragma unroll
      for (int j = 0; j < 4; ++j) { vk[j] = l16 + (j >> 1) * 4096u + (j & 1) * (VOFF - 2048u); asm volatile("" : "+v"(vk[j])); }
#define G4(a, b, c, d, sc) "s_nop 0\n\tglobal_load_lds_dwordx4 %" #a ", %4" sc "\n\tglobal_load_lds_dwordx4 %" #b ", %4 offset:1024" sc "\n\t"             \
                       "global_load_lds_dwordx4 %" #c ", %4 offset:2048" sc "\n\tglobal_load_lds_dwordx4 %" #d ", %4 offset:3072" sc "\n\t"
#define M4 "s_addk_i32 m0, 0x1000\n\t"
      auto stage = [&](const char* sp, unsigned dst) __attribute__((always_inline)) {
        asm volatile("s_mov_b32 m0, %5\n\t" G4(0, 0, 1, 1, " sc1") M4 G4(2, 2, 3, 3, " sc1")
                     :: "v"(vk[0]), "v"(vk[1]), "v"(vk[2]), "v"(vk[3]), "s"(sp), "s"(dst) : "m0", "scc", "memory");
      };
      auto count = [&]() __attribute__((always_inline)) {
        asm volatile("s_mov_b32 m0, %2\n\ts_nop 0\n\tglobal_load_lds_dword %0, %1 sc1" :: "v"(0u), "s"(cnt), "s"(lbase + FLG - 252) : "m0", "memory");
        cnt += 8;
      };
      auto freed = [&](unsigned n) __attribute__((always_inline)) {
        if (!none(pw < n)) {
#pragma unroll 1
          for (int i = 0; i < 4 * SPIN_MAX; ++i) {
            pw = *LDS_AT(const volatile unsigned char, vp);
            if (none(pw < n)) break;
            __builtin_amdgcn_s_sleep(W3SLP);
          }
        }
        asm volatile("" ::: "memory");
      };
      unsigned long long ord = (0xfedcba9875316420ull >> 4 & ~((1ull << 2 * (tmi & 6)) - 1ull)) | (0xfedcba9875316420ull & ((1ull << 2 * (tmi & 6)) - 1ull));
      ord ^= (tmi & 9) * 0x1111111111111111ull;
      unsigned seen;
      const char* kvb = reinterpret_cast<const char*>(KV) + (size_t)(tni * 2 + (tmi & 1)) * (64 * 2048);
      auto panel = [&]() __attribute__((always_inline)) {
        const unsigned pn = (unsigned)ord & 15u;
        ord >>= 4;
        if (__builtin_expect(!((seen >> pn) & 1u), 0)) {
#pragma unroll 1
          for (int i = 0; i < SPIN_MAX && !((seen >> pn) & 1u); ++i) seen = (unsigned)__ballot(ld_ag(kvflag + (tmi % RK) * 32 + (lane & 15)) == tag);
        }
        asm volatile("" ::: "memory");
        return kvb + pn * 8192u;
      };
      asm volatile("" : "+s"(ord), "+s"(kvb), "+s"(cnt));
      asm volatile("" : "+v"(vp), "+v"(pw));
      PIN();
      seen = (unsigned)__ballot(kv0 == tag);
      const char* sp = panel();
      wait_sig(16, 4u);
      stage(sp, lbase + 2 * KSB);
      count();
      const int ksp = LAY2 ? PNS / 2 : -1, ksg = LAY2 || !p1blk ? 64 : GF0;
      const u16* gp = gptr(wg, wu);
#pragma unroll 1
      for (int k = 1; k < 15; ++k) {
        if (__builtin_expect(k == ksp, 0)) {
          gu_setup();
          gate_up(xq, rmd);
          PIN();
          *LDS_AT(volatile unsigned char, lbase + FLG + 7) = (unsigned char)0x7f;
        }
        freed((unsigned)(0xba9876543211000ull >> 4 * k) & 15u);
        stage(sp + 2048, lbase + ((2 * k + 1) & 7) * KSB);
        if (__builtin_expect(k == 1, 0)) { cnt -= 4; count(); cnt -= 4; }
        sp = panel();
        stage(sp, lbase + ((2 * k + 2) & 7) * KSB);
        count();
        if (__builtin_expect(k == ksg || (unsigned)(k - ksg - GFD) < 3u, 0)) gather(gp, k == ksg ? 0 : k - ksg - GFD + 1);
      }
      freed(12u);
      glds16s<true>(csf + 4096 + tni * 128, l16, lbase + UGL);
      stage(sp + 2048, lbase + 7 * KSB);
      count();
      const unsigned vwo = (unsigned)((lane >> 3) * D_ + oni * 64 + ((lane & 7) ^ ((lane >> 3) & 6)) * 8) * 2u;
      constexpr unsigned vs = 8u * D_ * 2u - 1024u;
#pragma unroll 1
      for (int i = 0; i < 6; ++i) {
        const int ns = (i + 5) & 7;
        freed(i < 3 ? 12u : 10u + i);
        const char* wp = reinterpret_cast<const char*>(wo) + (size_t)ns * (128 * D_ * 2);
#pragma unroll
        for (int j = 0; j < 4; ++j)
          glds16x4<false>(wp + j * (32u * D_ * 2u), vwo, vwo + vs, vwo + 2 * vs, vwo + 3 * vs, lbase + ns * ASB + j * 4096);
      }
      asm volatile("s_waitcnt vmcnt(0)" ::: "memory");
      return;
    }
    if (!LAY2) {
      auto until = [&](unsigned n) {
        while ((__builtin_amdgcn_readfirstlane(*LDS_AT(const volatile unsigned, lbase + FLG + 8)) & 255u) < n) __builtin_amdgcn_s_sleep(16);
        asm volatile("" ::: "memory");
        PIN();
      };
      if (hw != 1) {
        constexpr int N2 = HAND_N + Y_N, X3N = 3 * D_ / 2, N0 = X3N + PS_N;
        const __amdgpu_buffer_rsrc_t rsi = __builtin_amdgcn_make_buffer_rsrc(hand + (hw ? GRAN_N : 2 * HAND_N - X3N), 0, (hw ? N2 : N0) * 4, 0x00020000);
        until(IAT);
#pragma unroll 1
        for (int i = 0; i < ((hw ? N2 : N0) / 4 + 16383) / 16384; ++i) {
          __builtin_amdgcn_raw_buffer_store_b128(u32x4{SENT, SENT, SENT, SENT}, rsi, ((i * 256 + bid) * 64 + lane) * 16, 0, 16);
          __builtin_amdgcn_s_sleep(ISLP);
        }
      }
      if (hw != 1) {
        const __amdgpu_buffer_rsrc_t rq = __builtin_amdgcn_make_buffer_rsrc(reinterpret_cast<char*>(KV) + QG + ((256 + bid) * 2 + (hw >> 1)) * 4096, 0, 4096, 0x00020000);
#pragma unroll
        for (int i = 0; i < 4; ++i) __builtin_amdgcn_raw_buffer_store_b128(u32x4{SENT, SENT, SENT, SENT}, rq, lane * 16 + i * 1024, 0, 16);
      }
      const u16* p = gptr(wg, wu);
#pragma unroll 1
      for (unsigned n = T0; n < T0 + 13u; ++n) {
        until(n);
        if (hw == 2 && n == TFL) {
          const int ln = fresh_lane() & 15;
          const unsigned p1 = ld_ag(gran + (ln < 4 ? GR_O + (((ln >> 1) * 16 + omi) * RO + oni % RO) * 64 + (ln & 1) * 32 :
                                            (ln < 8 ? GR_X + (tmi * RX + tni % RX) * 128 + (ln & 3) * 32 : GR_KV + ((16 + tni) * RK + tmi % RK) * 32)));
          asm volatile("" :: "v"(p1));
        }
        if (hw == 2 && n == TW) {
          const int ln = fresh_lane();
          unsigned long long a0, a1;
          CODE_ADDR("layers_kernel", a0); CODE_ADDR("end_marker_kernel", a1);
          const unsigned p1 = touch_code(a0, a1, 3 + loc, ln), p0 = touch(wo + (size_t)(384 + (loc / (32 / OMX)) * (256 / OMX) + ln) * D_ + oni * 64);
          asm volatile("" :: "v"(p0), "v"(p1));
        }
        {
          const int st = n == G0 + GW * ((hw + 2) % 3) ? 0 : (n >= G1 && (n - G1) % GS == 0u && (n - G1) / GS < 3u ? 1 + (n - G1) / GS : -1);
          if (st >= 0 && p1blk) gather(p, st);
        }
        if (n == UAT && hw == UHW) {
          u32x4 a[4][2], c[2];
#pragma unroll
          for (int i = 0; i < 4; ++i)
#pragma unroll
            for (int h = 0; h < 2; ++h) a[i][h] = *reinterpret_cast<const u32x4*>(wo + (size_t)(tni * 64 + tmi * 4 + i) * D_ + h * 512 + lane * 8);
#pragma unroll
          for (int h = 0; h < 2; ++h) c[h] = *reinterpret_cast<const u32x4*>(w_r + h * 512 + lane * 8);
          auto dot = [&](unsigned x, unsigned y, float z) { return __builtin_amdgcn_fdot2_f32_bf16(__builtin_bit_cast(bf16x2, x), __builtin_bit_cast(bf16x2, y), z, false); };
          PIN();
          __builtin_amdgcn_s_setprio(0);
#pragma unroll 1
          for (int i = 0; i < SPIN_MAX; ++i) {
            if (__builtin_amdgcn_readfirstlane(*LDS_AT(const volatile unsigned, lbase + FLG + 16)) >= UARR) break;
            __builtin_amdgcn_s_sleep(4);
          }
          asm volatile("s_waitcnt vmcnt(0)" ::: "memory");
          PIN();
          float wm = 0.f;
#pragma unroll
          for (int e = 0; e < 8; ++e) wm = dot(c[e >> 2][e & 3], 0x3f803f80u, wm);
          wm = wave_sum(wm);
          lds_wr(lbase + FLG + 60, __float_as_uint(wm));
          wm *= -1.0f / 1024.0f;
          f32x2 u[2], g[2];
#pragma unroll
          for (int i = 0; i < 4; ++i) {
            float p1 = 0.f, pw = 0.f, p2 = 0.f;
#pragma unroll
            for (int e = 0; e < 8; ++e) {
              const unsigned x = a[i][e >> 2][e & 3];
              p1 = dot(x, 0x3f803f80u, p1); pw = dot(x, c[e >> 2][e & 3], pw); p2 = dot(x, x, p2);
            }
            u[i >> 1][i & 1] = fmaf(p1, wm, pw); g[i >> 1][i & 1] = p2;
          }
          float us = fold4(u[0], u[1]), gs = fold4(g[0], g[1]);
          us = dpp_add<0x121, 0xf>(dpp_add<0x122, 0xf>(us)); gs = dpp_add<0x121, 0xf>(dpp_add<0x122, 0xf>(gs));
          __builtin_amdgcn_raw_buffer_store_b32(__float_as_uint((lane & 1) ? __builtin_amdgcn_sqrtf(gs) * (1.0f / 32.0f) : us),
              __builtin_amdgcn_make_buffer_rsrc(csf + 4096 + tni * 128, 0, 512, 0x00020000), (lane & 14) ? 512 : ((lane & 1) * 64 + tmi * 4 + (lane >> 4)) * 4, 0, 16);
        }
      }
      if (hw == 2 && bid == 0) {
        const __amdgpu_buffer_rsrc_t rsc = __builtin_amdgcn_make_buffer_rsrc(gran, 0, GR_N * 4, 0x00020000);
#pragma unroll 1
        for (int spin = 0; spin < SPIN_MAX; ++spin) {
          bool ok = true;
#pragma unroll
          for (int i = 0; i < 4; ++i) ok &= __builtin_amdgcn_raw_buffer_load_b32(rsc, (GR_ROW + (i * 64 + lane) * 32 + 1) * 4, 0, 16) == tag;
          if (__all(ok)) break;
          __builtin_amdgcn_s_sleep(8);
          asm volatile("" ::: "memory");
        }
        if (lane == 0) st_ag(gran, tag);
      }
      return;
    }
    unsigned* X2 = hand + GRAN_N + Y_N;
    unsigned* XS = X2 + X2_N;
    unsigned* P4 = XS + XS_N;
    unsigned* PA = P4 + P4_N;
    const __amdgpu_buffer_rsrc_t rsh = __builtin_amdgcn_make_buffer_rsrc(hand, 0, HAND_N * 4, 0x00020000);
    auto signal = [&](unsigned ph) {
      asm volatile("" ::: "memory");
      PIN();
      *LDS_AT(volatile unsigned char, lbase + FLG + 4 + hw) = (unsigned char)ph;
    };
    auto wait_all = [&](unsigned ph) {
#pragma unroll 1
      for (int i = 0; i < SPIN_MAX; ++i) {
        if (all_ge(__builtin_amdgcn_readfirstlane(*LDS_AT(const volatile unsigned, lbase + FLG + 4)), ph)) break;
        __builtin_amdgcn_s_sleep(WSLP);
      }
      asm volatile("" ::: "memory");
      PIN();
    };
    gu_setup();
    gate_up(xq, rmd);
    signal(hw == 2 ? 2u : 0x7fu);
    if (wid == 8) STAMP(dk, 2);
    if (hw < 2) {
#pragma unroll 1
      for (int i = 0; i < SPIN_MAX; ++i) {
        if (__builtin_amdgcn_readfirstlane(*LDS_AT(const volatile unsigned, lbase + FLG)) >= FSTART) break;
        __builtin_amdgcn_s_sleep(WSLP);
      }
      asm volatile("" ::: "memory");
      PIN();
      if (wid == 8) STAMP(dk, 10);
      u32x2 w[16];
      const __amdgpu_buffer_rsrc_t rd_ = __builtin_amdgcn_make_buffer_rsrc(const_cast<u16*>(wd), 0, D_ * F_ * 2, 0x00020000);
      auto ldw = [&](int c) __attribute__((always_inline)) {
#pragma unroll
        for (int f = 0; f < 16; ++f) w[f] = __builtin_amdgcn_raw_buffer_load_b64(rd_, lane * 8 + c * 512 + (f & 1) * 2048, (fg * 16 + (f & ~1)) * (D_ * 2) + hw * 1024, 0);
        PIN();
      };
      ldw(0);
      const unsigned pt = __builtin_amdgcn_raw_buffer_load_b32(rd_, (lane >> 2) * (D_ * 2) + (lane & 3) * 128 + 512, fg * 16 * (D_ * 2) + hw * 1024, 0);
      int yo = p1blk ? (GRAN_N + fg * D_ + hw * 512) * 4 + lane * 16 : HAND_N * 4;
      asm volatile("" : "+v"(yo));
      wait_all(2u);
      if (wid == 8) STAMP(dk, 3);
      const s16x4 a = act_diag();
      const u32x4 y0 = down(a, w);
      PIN();
      ldw(1);
      __builtin_amdgcn_raw_buffer_store_b128(y0, rsh, yo, 0, 16);
      PIN();
      __builtin_amdgcn_raw_buffer_store_b128(down(a, w), rsh, yo + 1024, 0, 16);
      asm volatile("" :: "v"(pt));
      if (wid == 8) STAMP(dk, 4);
    }
    PIN();
    [&]() __attribute__((always_inline)) {
    const int lane = fresh_lane();
    const float rmb = __uint_as_float(rmd << 16);
    auto first_words = [&](int src, auto nc) __attribute__((always_inline)) {
#pragma unroll 1
      for (int spin = 0; spin < SPIN_MAX; ++spin) {
        unsigned mx = 0u;
#pragma unroll
        for (int i = 0; i < decltype(nc)::value; ++i) mx = umax(mx, __builtin_amdgcn_raw_buffer_load_b32(rsh, src, i * (56 * D_ * 4), 16));
        if (none(mx == SENT)) break;
        __builtin_amdgcn_s_sleep(PSLP);
        asm volatile("" ::: "memory");
      }
    };
    if (hw == 2) {
      wait_all(2u);
      if (own) {
        const float rden = __uint_as_float(rmd & 0xffff0000u), rrin = frcp(rden);
        const int col = ow * 16 + (lane & 3) * 4 + (lane >> 4);
        const unsigned xo2 = ld_ag(reinterpret_cast<const unsigned*>(A + (size_t)idx * D_ + (col & ~1)));
        __builtin_amdgcn_s_sleep(PRE1);
        first_words((GRAN_N + (lane < 56 ? lane : 0) * D_ + ow * 16) * 4, std::integral_constant<int, 4>{});
        const float y = y_sum(rsh, std::integral_constant<int, 2>{});
        const unsigned rk = best >> 16;
        const float rv = __uint_as_float(((rk & 0x8000u) ? (rk & 0x7fffu) : (~rk & 0xffffu)) << 16);
        const float rs = rbf(frcp(1.0f + __expf(-rv)));
        const float x1v = __uint_as_float((col & 1) ? xo2 & 0xffff0000u : xo2 << 16);
        float h0 = x1v, h1 = x1v;
        ln_apply2(h0, h1, rmb, rden, rrin);
        const float x2v = rbf(x1v + rbf(h0 + rbf(rbf(y) * rs)));
        const auto pr = __builtin_amdgcn_permlane16_swap(__float_as_uint(x2v), __float_as_uint(x2v), false, false);
        __builtin_amdgcn_raw_buffer_store_b32(f2word(__uint_as_float((pr[0] >> 16) | pr[1])), rsh, (lane & 28) ? HAND_N * 4 : (int)(X2 - hand) * 4 + col * 2, 0, 16);
        const float d = x2v - rmb;
        const float sd = wave_sum(d), sq = wave_sum(d * d);
        if (lane < 2) st_ag(XS + ow * 2 + lane, f2word(lane ? sq : sd));
        STAMP(dk, 5);
      }
    }
    {
      const int ks = kcap ^ (tmi & 8), g4 = lane >> 4;
      const int src = (int)(X2 - hand) * 4 + ks * 128 + g4 * 16;
      int dst = (lane & 12) ? HAND_N * 4 : (int)((P4 - hand) + (tni * 16 + ks) * 192 + (hw ^ (hw < 2 ? ((lane >> 1) ^ tmi) & 1 : 0)) * 64 + (lane & 2) * 16 + (lane & 1) * 8 + (g4 >> 1) * 16 + (g4 & 1) * 4) * 4;
      asm volatile("" : "+v"(dst));
      u32x4 x0, x1;
      if (!(hw == 2 && own)) __builtin_amdgcn_s_sleep(PRE4);
#pragma unroll 1
      for (int spin = 0; spin < SPIN_MAX; ++spin) {
        x0 = __builtin_amdgcn_raw_buffer_load_b128(rsh, src, 0, 16); x1 = __builtin_amdgcn_raw_buffer_load_b128(rsh, src + 64, 0, 16);
        if (none(umax(umax(umax(x0[0], x0[1]), umax(x0[2], x0[3])), umax(umax(x1[0], x1[1]), umax(x1[2], x1[3]))) == SENT)) break;
        __builtin_amdgcn_s_sleep(PSLP);
        asm volatile("" ::: "memory");
      }
      f32x4 d[4];
#pragma unroll
      for (int i = 0; i < 4; ++i) d[i] = MFMA16(sl[2 * i], __builtin_bit_cast(s16x8, x0), (f32x4{0.f, 0.f, 0.f, 0.f}));
#pragma unroll
      for (int i = 0; i < 4; ++i) d[i] = MFMA16(sl[2 * i + 1], __builtin_bit_cast(s16x8, x1), d[i]);
      u32x4 o;
#pragma unroll
      for (int e = 0; e < 4; ++e) o[e] = f2word((lane & 2) ? ((lane & 1) ? d[3][e] : d[2][e]) : ((lane & 1) ? d[1][e] : d[0][e]));
      __builtin_amdgcn_raw_buffer_store_b128(o, rsh, dst, 0, 16);
      PIN();
      if (wid == 8) STAMP(dk, 6);
    }
    if (hw == 2) {
      const int col = (lane & 15) * 4 + (lane >> 4), src = (int)((P4 - hand) + (tni * 16 + (lane >> 4)) * 192 + (lane & 15) * 4) * 4;
      const __amdgpu_buffer_rsrc_t rsq = __builtin_amdgcn_make_buffer_rsrc(hand, 0, fast ? HAND_N * 4 : 0, 0x00020000);
      if (own) {
#pragma unroll
        for (int i = 0; i < 14; ++i)
          __builtin_amdgcn_raw_buffer_store_b128(u32x4{SENT, SENT, SENT, SENT}, rsh, (GRAN_N + (i * 16 + (lane >> 2)) * D_ + ow * 16 + (lane & 3) * 4) * 4, 0, 16);
      }
      float cs[3], val[3];
#pragma unroll
      for (int p = 0; p < 3; ++p) cs[p] = __uint_as_float(ld_ag(reinterpret_cast<const unsigned*>(csf) + p * 1024 + n0 + col));
      u32x2 xs;
      __builtin_amdgcn_s_sleep(PRE7);
      first_words((int)((P4 - hand) + (tni * 16 + (lane & 15)) * 192 + ((lane >> 4) % 3) * 64) * 4, std::integral_constant<int, 1>{});
#pragma unroll 1
      for (int spin = 0; spin < SPIN_MAX; ++spin) {
        u32x4 v[3][4];
        xs = __builtin_amdgcn_raw_buffer_load_b64(rsh, (int)((XS - hand) + lane * 2) * 4, 0, 16);
#pragma unroll
        for (int p = 2; p >= 0; --p)
#pragma unroll
          for (int i = 0; i < 4; ++i) v[p][i] = __builtin_amdgcn_raw_buffer_load_b128(p ? rsh : rsq, src + p * 256, i * (4 * 192 * 4), 16);
        auto part = [&](int p) __attribute__((always_inline)) {
          float z[4];
#pragma unroll
          for (int e = 0; e < 4; ++e) {
            z[e] = fadd(fadd(__uint_as_float(v[p][0][e]), __uint_as_float(v[p][1][e])), fadd(__uint_as_float(v[p][2][e]), __uint_as_float(v[p][3][e])));
          }
          val[p] = fold16(f32x2{z[0], z[1]}, f32x2{z[2], z[3]});
        };
        part(2); part(1);
        val[0] = 0.f;
        if (fast) part(0);
        const float chk = fadd(fadd(fadd(val[0], val[1]), fadd(val[2], __uint_as_float(xs[0]))), __uint_as_float(xs[1]));
        if (none(chk != chk)) break;
        unsigned mx = umax(xs[0], xs[1]);
#pragma unroll
        for (int i = 0; i < 12; ++i) mx = umax(umax(umax(mx, v[i >> 2][i & 3][0]), v[i >> 2][i & 3][1]), umax(v[i >> 2][i & 3][2], v[i >> 2][i & 3][3]));
        if (none(mx == SENT)) break;
        __builtin_amdgcn_s_sleep(PSLP);
        asm volatile("" ::: "memory");
      }
      const auto sw = __builtin_amdgcn_permlane32_swap(xs[0], xs[1], false, false);
      float sm = __uint_as_float(sw[0]) + __uint_as_float(sw[1]);
      sm = dpp_add<0x142, 0xa>(dpp_add<0x118, 0xf>(dpp_add<0x114, 0xf>(dpp_add<0x112, 0xf>(dpp_add<0x111, 0xf>(sm)))));
      const float dm = __int_as_float(__builtin_amdgcn_readlane(__float_as_int(sm), 31)) * (0.25f / 1024.0f);
      const float var = fmaxf(__int_as_float(__builtin_amdgcn_readlane(__float_as_int(sm), 63)) * (0.25f / 1024.0f) - dm * dm, 0.f);
      const float mb = rbf(rmb + dm);
      const float rin = frcp(rbf(__builtin_amdgcn_sqrtf(rbf(rbf(var) + 1e-5f))));
      const unsigned kv = pack2((val[1] - mb * cs[1]) * rin, (val[2] - mb * cs[2]) * rin);
      *LDS_AT(u16, lbase + TAB + 128 + kpos(col) * 2) = (u16)kv;
      *LDS_AT(u16, lbase + TAB + 256 + col * 2) = (u16)(kv >> 16);
      if (fast) *LDS_AT(u16, lbase + TAB + kpos(col) * 2) = f2bf((val[0] - mb * cs[0]) * rin * QSCALE);
      signal(7u);
      STAMP(dk, 7);
      return;
    }
    if (!fast) return;
    const int head = tni * 2 + hw;
    const unsigned tb = lbase + TAB + hw * 64;
    {
      const __amdgpu_buffer_rsrc_t rs = __builtin_amdgcn_make_buffer_rsrc(KV, 0, 2 * S_ * D_ * 2, 0x00020000);
      const int src = (head * 64 + tmi * 4) * 2048 + lane * 16;
      auto ld = [&](int i, unsigned off) { return __builtin_bit_cast(s16x8, __builtin_amdgcn_raw_buffer_load_b128(rs, src + (i & 3) * 1024, (i >> 3) * 65536 + ((i >> 2) & 1) * 4096 + off, 16)); };
#pragma unroll 1
      for (int i = 0; i < SPIN_MAX; ++i)
        if (none(ld_ag(kvflag + ((tmi & ~1) + hw) * 32 + tmi + (lane & 8)) != tag)) break;
      asm volatile("" ::: "memory");
      s16x8 f[16];
#pragma unroll
      for (int i = 0; i < 16; ++i) f[i] = ld(i, 0u);
      PIN();
      const bool on = ((lane ^ (lane >> 4)) & 1) == 0;
      const unsigned pl = lbase + RED + hw * 1024;
      unsigned pw = pl + (lane >> 4) * 64 + (lane & 15) * 2, pr = pl + (on ? (lane >> 5) * 16 : 512), qr = on ? tb + (lane >> 5) * 16 : pl + 512;
      const int gone = (((idx >> 7) & 7) == tmi) ? ((idx >> 10) * 2 + (kpos(idx & 31) >> 4)) * 64 + ((idx >> 5) & 3) * 16 + (kpos(idx & 31) & 15) : -1;
      asm volatile("" : "+v"(pw), "+v"(pr), "+v"(qr));
      *LDS_AT(u32x2, pl + 512 + lane * 8) = u32x2{0u, 0u};
      wait_all(7u);
      u32x4 q[2];
#pragma unroll
      for (int sx = 0; sx < 2; ++sx) {
        q[sx] = *LDS_AT(const u32x4, qr + sx * 32);
      }
      float sc[4];
#pragma unroll
      for (int i = 0; i < 8; ++i) {
        f32x4 d = MFMA16(__builtin_bit_cast(s16x8, q[0]), f[2 * i], (f32x4{0.f, 0.f, 0.f, 0.f}));
        d = MFMA16(__builtin_bit_cast(s16x8, q[1]), f[2 * i + 1], d);
        PIN();
        f[2 * i] = ld(2 * i, VOFF); f[2 * i + 1] = ld(2 * i + 1, VOFF);
        PIN();
#pragma unroll
        for (int r = 0; r < 2; ++r) sc[(i >> 2) * 2 + r] = (i & 3) == 0 || (lane >> 4) == (i & 3) ? d[r] : sc[(i >> 2) * 2 + r];
      }
      if (gone >= 0) {
#pragma unroll
        for (int r = 0; r < 4; ++r) sc[r] = r * 64 + lane == gone ? -INFINITY : sc[r];
      }
      const float mx = wave_fmax(fmaxf(fmaxf(sc[0], sc[1]), fmaxf(sc[2], sc[3])));
      const unsigned p0 = pack2(__builtin_amdgcn_exp2f(sc[0] - mx), __builtin_amdgcn_exp2f(sc[1] - mx)), p1 = pack2(__builtin_amdgcn_exp2f(sc[2] - mx), __builtin_amdgcn_exp2f(sc[3] - mx));
      *LDS_AT(u16, pw) = (u16)p0; *LDS_AT(u16, pw + 32) = (u16)(p0 >> 16);
      *LDS_AT(u16, pw + 256) = (u16)p1; *LDS_AT(u16, pw + 288) = (u16)(p1 >> 16);
      asm volatile("" ::: "memory");
      const float ls = wave_sum((__uint_as_float(p0 << 16) + __uint_as_float(p0 & 0xffff0000u)) + (__uint_as_float(p1 << 16) + __uint_as_float(p1 & 0xffff0000u)));
      f32x4 o = {0.f, 0.f, 0.f, 0.f};
#pragma unroll
      for (int i = 0; i < 16; ++i) o = MFMA16(*LDS_AT(const s16x8, pr + i * 32), f[i], o);
      int pv = __float_as_int((lane & 16) ? o[1] : o[0]);
      asm volatile("s_nop 0\n\tv_writelane_b32 %0, %1, 32\n\tv_writelane_b32 %0, %2, 33" : "+v"(pv) : "s"(ls), "s"(mx));
      __builtin_amdgcn_raw_buffer_store_b32(f2word(__int_as_float(pv)), rsh, lane < 34 ? (int)((PA - hand) + (head * 8 + tmi) * 64 + lane) * 4 : HAND_N * 4, 0, 16);
    }
    if (wid == 8) STAMP(dk, 8);
    if (tmi != ((idx >> 7) & 7)) return;
    {
      const int src = (int)((PA - hand) + head * 512 + (lane < 34 ? lane : 0)) * 4;
      const unsigned ta = tb + (lane & 31) * 2;
      const float so = wave_sum(lane < 32 ? bf2f(*LDS_AT(const u16, ta)) * bf2f(*LDS_AT(const u16, ta + 128)) : 0.f);
      const float vo = lane < 32 ? bf2f(*LDS_AT(const u16, ta + 256)) : 1.0f;
      unsigned v[8];
      __builtin_amdgcn_s_sleep(PRE5);
#pragma unroll 1
      for (int spin = 0; spin < SPIN_MAX; ++spin) {
        unsigned mx = 0u;
#pragma unroll
        for (int i = 0; i < 8; ++i) v[i] = __builtin_amdgcn_raw_buffer_load_b32(rsh, src + i * 256, 0, 16);
#pragma unroll
        for (int i = 0; i < 8; ++i) mx = umax(mx, v[i]);
        if (none(mx == SENT)) break;
        __builtin_amdgcn_s_sleep(PSLP);
        asm volatile("" ::: "memory");
      }
      float m8[8], mx = so;
#pragma unroll
      for (int i = 0; i < 8; ++i) { m8[i] = __int_as_float(__builtin_amdgcn_readlane((int)v[i], 33)); mx = fmaxf(mx, m8[i]); }
      float acc = __builtin_amdgcn_exp2f(so - mx) * vo;
#pragma unroll
      for (int i = 0; i < 8; ++i) acc += __builtin_amdgcn_exp2f(m8[i] - mx) * __uint_as_float(v[i]);
      const float l = __int_as_float(__builtin_amdgcn_readlane(__float_as_int(acc), 32));
      const float ov = acc / l;
      const float o1 = __int_as_float(__builtin_amdgcn_update_dpp(0, __float_as_int(ov), 0x55, 0xf, 0xf, false));
      const float o2 = __int_as_float(__builtin_amdgcn_update_dpp(0, __float_as_int(ov), 0xaa, 0xf, 0xf, false));
      const float o3 = __int_as_float(__builtin_amdgcn_update_dpp(0, __float_as_int(ov), 0xff, 0xf, 0xf, false));
      const __amdgpu_buffer_rsrc_t rso = __builtin_amdgcn_make_buffer_rsrc(Of, 0, S_ * D_ * 2, 0x00020000);
      if (lane < 32 && (lane & 3) == 0)
        __builtin_amdgcn_raw_buffer_store_b64(u32x2{pack2(ov, o1), pack2(o2, o3)}, rso,
            (((idx >> 5) * 32 + head) * 1024 + (lane >> 4) * 512 + (((lane >> 2) & 1) * 32 + (idx & 31)) * 8 + ((lane >> 3) & 1) * 4) * 2, 0, 16);
      asm volatile("s_waitcnt vmcnt(0)" ::: "memory");
      if (lane < RO) st_ag(gran + GR_O + ((16 + (idx >> 7)) * RO + lane) * 64 + 32 + head, tag);
      STAMP(dk, 9);
    }
    }();
    }();
    __builtin_amdgcn_s_setprio(0);
    if (LAY2 || hw != UHW) asm volatile("s_waitcnt vmcnt(0)" ::: "memory");
    PIN();
    ARGS_HERE;
    u32x2 w[16];
    auto ld_wd = [&]() __attribute__((always_inline)) {
      const __amdgpu_buffer_rsrc_t rd_ = __builtin_amdgcn_make_buffer_rsrc(const_cast<u16*>(wd), 0, D_ * F_ * 2, 0x00020000);
#pragma unroll
      for (int f = 0; f < 16; ++f) w[f] = __builtin_amdgcn_raw_buffer_load_b64(rd_, t * 8 + (f & 1) * 2048, (fg * 16 + (f & ~1)) * (D_ * 2), 0);
      PIN();
    };
    float fnum = 0.f, fvar = 0.f;
    const int rrow = omi * 128 + (oni >> 3) * 64 + (hw & 1) * 32;
    if (LAY2) {
      ld_wd();
      if (!p1blk) {
        const int fr = rrow + (lane & 7) * 4 + (lane >> 4);
        const double s1 = ld_ag(stats + fr), s2 = ld_ag(stats + S_ + fr), sw = ld_ag(stats + 2 * S_ + fr);
        lds_wr(lbase + SPM + 132, __float_as_uint(wsum));
        float mean, var, mb, den;
        ln_from_sums(s1, s2, mean, var, mb, den);
        const float g1 = 1.0f + frcp(den);
        fnum = fr == idx ? __uint_as_float(0x7fc00000u) : fmaf(-mean, wsum, (float)sw) * g1;
        fvar = var * g1 * g1;
      }
      asm volatile("" : "+v"(fnum), "+v"(fvar));
      PIN();
    }
    if (hw == 3) {
      const int nfl = (LAY2 && (idx >> 7) == omi) ? 64 : 32;
      auto pfl = (__attribute__((address_space(1))) const unsigned*)(gran + GR_O + (((LAY2 ? 16 : 0) + omi) * RO + oni % RO) * 64 + (lane < nfl ? lane : 0));
      constexpr unsigned vs = 8u * D_ * 2u - 1024u;
      unsigned vw0 = (unsigned)((lane >> 3) * D_ + oni * 64 + ((lane & 7) ^ ((lane >> 3) & 6)) * 8) * 2u, vw1 = vw0 + vs, vw2 = vw0 + 2 * vs, vw3 = vw0 + 3 * vs;
      asm volatile("" : "+v"(pfl), "+v"(vw0), "+v"(vw1), "+v"(vw2), "+v"(vw3));
      auto wait_arr = [&](unsigned n) {
#pragma unroll 1
        for (int i = 0; i < 4 * SPIN_MAX; ++i) {
          if (__builtin_amdgcn_readfirstlane(*LDS_AT(const volatile unsigned, lbase + FLG + 16)) >= n) break;
          __builtin_amdgcn_s_sleep(1);
        }
        asm volatile("" ::: "memory");
        PIN();
      };
      wait_arr(12u);
      unsigned fl;
      asm volatile("global_load_dword %0, %1, off sc1" : "=v"(fl) : "v"(pfl) : "memory");
#pragma unroll 1
      for (int i = 0; i < 4 * SPIN_MAX; ++i) {
        if (__builtin_amdgcn_readfirstlane(*LDS_AT(const volatile unsigned char, lbase + FLG + 24)) >= 8u) break;
        __builtin_amdgcn_s_sleep(1);
      }
      asm volatile("" ::: "memory");
      PIN();
#pragma unroll
      for (int j = 12; j < 20; ++j)
        glds16x4<false>(reinterpret_cast<const char*>(wo) + j * (32u * D_ * 2u), vw0, vw1, vw2, vw3, lbase + j * 4096);
      unsigned keep;
      asm volatile("s_mov_b32 %0, m0\n\ts_mov_b32 m0, %3\n\ts_nop 2\n\tglobal_load_lds_dword %1, %2 sc1\n\ts_mov_b32 m0, %0"
                   : "=&s"(keep) : "v"(0u), "s"(reinterpret_cast<const char*>(gran + GR_N + bid * 64 + 39)), "s"(lbase + FLG - 252) : "memory");
      asm volatile("s_waitcnt vmcnt(33)" : "+v"(fl) :: "memory");
      if (__builtin_amdgcn_ballot_w64(fl != tag)) {
        __builtin_amdgcn_s_sleep(CSL1);
#pragma unroll 1
        for (int spin = 0; spin < SPIN_MAX; ++spin) {
          if (!__builtin_amdgcn_ballot_w64(__hip_atomic_load(pfl, __ATOMIC_RELAXED, __HIP_MEMORY_SCOPE_AGENT) != tag)) break;
          __builtin_amdgcn_s_sleep(CSLP);
        }
      }
      asm volatile("" ::: "memory");
      PIN();
    }
    u32x4 pv[4];
    asm volatile("" : "=v"(pv[0]), "=v"(pv[1]), "=v"(pv[2]), "=v"(pv[3]));
    if (LAY2 && !p1blk && hw < 2) {
      const __amdgpu_buffer_rsrc_t rsp = __builtin_amdgcn_make_buffer_rsrc(hand + 2 * HAND_N + PP_O, 0, H_ * S_ * 4, 0x00020000);
      const int src = ((lane >> 3) * S_ + rrow + (lane & 7) * 4) * 4;
#pragma unroll 1
      for (int i = 0; i < 4 * SPIN_MAX; ++i) {
        if (__builtin_amdgcn_readfirstlane(*LDS_AT(const volatile unsigned char, lbase + FLG + 24)) >= 8u) break;
        __builtin_amdgcn_s_sleep(2);
      }
#pragma unroll 1
      for (int spin = 0; spin < SPIN_MAX; ++spin) {
        asm volatile("" ::: "memory");
        unsigned mx = 0u;
#pragma unroll
        for (int i = 0; i < 4; ++i) pv[i] = __builtin_amdgcn_raw_buffer_load_b128(rsp, src, i * (8 * S_ * 4), 16);
#pragma unroll
        for (int i = 0; i < 4; ++i) mx = umax(umax(umax(mx, pv[i][0]), pv[i][1]), umax(pv[i][2], pv[i][3]));
        if (none(mx == SENT)) break;
        __builtin_amdgcn_s_sleep(PPSLP);
      }
    }
    asm volatile("s_waitcnt lgkmcnt(0)" ::: "memory");
    __builtin_amdgcn_s_barrier();
    if (!LAY2) {
      PIN();
      asm volatile(AGAIN "\ngate_up_code:\n\t"
                 "v_lshlrev_b32 v84, 16, v100\n\tv_and_b32 v85, 0xffff0000, v100\n\tv_lshlrev_b32 v86, 16, v101\n\tv_and_b32 v87, 0xffff0000, v101\n\t"
                 "v_subrev_f32 v84, s84, v84\n\tv_subrev_f32 v85, s84, v85\n\tv_subrev_f32 v86, s84, v86\n\tv_subrev_f32 v87, s84, v87\n\t"
                 "v_cvt_pk_bf16_f32 v85, v84, v85\n\tv_cvt_pk_bf16_f32 v87, v86, v87\n\ts_mov_b32 s88, 0x01000100\n\ts_mov_b32 s89, 0x03020302\n\t"
                 "v_lshlrev_b32 v84, 16, v85\n\tv_and_b32 v85, 0xffff0000, v85\n\tv_lshlrev_b32 v86, 16, v87\n\tv_and_b32 v87, 0xffff0000, v87\n\t"
                 "v_mul_f32 v88, s86, v84\n\tv_mul_f32 v89, s86, v85\n\tv_mul_f32 v90, s86, v86\n\tv_mul_f32 v91, s86, v87\n\tv_fma_f32 v84, -v88, s85, v84\n\t"
                 "v_fma_f32 v85, -v89, s85, v85\n\tv_fma_f32 v86, -v90, s85, v86\n\tv_fma_f32 v87, -v91, s85, v87\n\tv_fmac_f32 v88, s86, v84\n\t"
                 "v_fmac_f32 v89, s86, v85\n\tv_fmac_f32 v90, s86, v86\n\tv_fmac_f32 v91, s86, v87\n\tv_cvt_pk_bf16_f32 v88, v88, v89\n\t"
                 "v_cvt_pk_bf16_f32 v90, v90, v91\n\tv_perm_b32 v92, v88, v88, s88\n\tv_perm_b32 v93, v88, v88, s89\n\tv_perm_b32 v94, v90, v90, s88\n\t"
                 "v_perm_b32 v95, v90, v90, s89\n\ts_nop 0\n\tv_and_b32_dpp v96, v92, v102 quad_perm:[0,0,0,0] row_mask:0xf bank_mask:0xf\n\t"
                 "v_and_b32_dpp v97, v92, v103 quad_perm:[0,0,0,0] row_mask:0xf bank_mask:0xf\n\t"
                 "v_and_b32_dpp v100, v93, v102 quad_perm:[0,0,0,0] row_mask:0xf bank_mask:0xf\n\t"
                 "v_and_b32_dpp v101, v93, v103 quad_perm:[0,0,0,0] row_mask:0xf bank_mask:0xf\n\tv_mfma_f32_4x4x4_16b_bf16 v[84:87], v[96:97], v[104:105], 0\n\t"
                 "v_mfma_f32_4x4x4_16b_bf16 v[88:91], v[96:97], v[106:107], 0\n\tv_and_b32_dpp v96, v94, v102 quad_perm:[0,0,0,0] row_mask:0xf bank_mask:0xf\n\t"
                 "v_and_b32_dpp v97, v94, v103 quad_perm:[0,0,0,0] row_mask:0xf bank_mask:0xf\n\t"
                 "v_mfma_f32_4x4x4_16b_bf16 v[84:87], v[100:101], v[108:109], v[84:87]\n\tv_mfma_f32_4x4x4_16b_bf16 v[88:91], v[100:101], v[110:111], v[88:91]\n\t"
                 "v_and_b32_dpp v100, v95, v102 quad_perm:[0,0,0,0] row_mask:0xf bank_mask:0xf\n\t"
                 "v_and_b32_dpp v101, v95, v103 quad_perm:[0,0,0,0] row_mask:0xf bank_mask:0xf\n\tv_mfma_f32_4x4x4_16b_bf16 v[84:87], v[96:97], v[112:113], v[84:87]\n\t"
                 "v_mfma_f32_4x4x4_16b_bf16 v[88:91], v[96:97], v[114:115], v[88:91]\n\tv_and_b32_dpp v96, v92, v102 quad_perm:[1,1,1,1] row_mask:0xf bank_mask:0xf\n\t"
                 "v_and_b32_dpp v97, v92, v103 quad_perm:[1,1,1,1] row_mask:0xf bank_mask:0xf\n\t"
                 "v_mfma_f32_4x4x4_16b_bf16 v[84:87], v[100:101], v[116:117], v[84:87]\n\tv_mfma_f32_4x4x4_16b_bf16 v[88:91], v[100:101], v[118:119], v[88:91]\n\t"
                 "v_and_b32_dpp v100, v93, v102 quad_perm:[1,1,1,1] row_mask:0xf bank_mask:0xf\n\t"
                 "v_and_b32_dpp v101, v93, v103 quad_perm:[1,1,1,1] row_mask:0xf bank_mask:0xf\n\tv_mfma_f32_4x4x4_16b_bf16 v[84:87], v[96:97], v[120:121], v[84:87]\n\t"
                 "v_mfma_f32_4x4x4_16b_bf16 v[88:91], v[96:97], v[122:123], v[88:91]\n\tv_and_b32_dpp v96, v94, v102 quad_perm:[1,1,1,1] row_mask:0xf bank_mask:0xf\n\t"
                 "v_and_b32_dpp v97, v94, v103 quad_perm:[1,1,1,1] row_mask:0xf bank_mask:0xf\n\t"
                 "v_mfma_f32_4x4x4_16b_bf16 v[84:87], v[100:101], v[124:125], v[84:87]\n\tv_mfma_f32_4x4x4_16b_bf16 v[88:91], v[100:101], v[126:127], v[88:91]\n\t"
                 "v_and_b32_dpp v100, v95, v102 quad_perm:[1,1,1,1] row_mask:0xf bank_mask:0xf\n\t"
                 "v_and_b32_dpp v101, v95, v103 quad_perm:[1,1,1,1] row_mask:0xf bank_mask:0xf\n\tv_mfma_f32_4x4x4_16b_bf16 v[84:87], v[96:97], v[128:129], v[84:87]\n\t"
                 "v_mfma_f32_4x4x4_16b_bf16 v[88:91], v[96:97], v[130:131], v[88:91]\n\tv_and_b32_dpp v96, v92, v102 quad_perm:[2,2,2,2] row_mask:0xf bank_mask:0xf\n\t"
                 "v_and_b32_dpp v97, v92, v103 quad_perm:[2,2,2,2] row_mask:0xf bank_mask:0xf\n\t"
                 "v_mfma_f32_4x4x4_16b_bf16 v[84:87], v[100:101], v[132:133], v[84:87]\n\tv_mfma_f32_4x4x4_16b_bf16 v[88:91], v[100:101], v[134:135], v[88:91]\n\t"
                 "v_and_b32_dpp v100, v93, v102 quad_perm:[2,2,2,2] row_mask:0xf bank_mask:0xf\n\t"
                 "v_and_b32_dpp v101, v93, v103 quad_perm:[2,2,2,2] row_mask:0xf bank_mask:0xf\n\tv_mfma_f32_4x4x4_16b_bf16 v[84:87], v[96:97], v[136:137], v[84:87]\n\t"
                 "v_mfma_f32_4x4x4_16b_bf16 v[88:91], v[96:97], v[138:139], v[88:91]\n\tv_and_b32_dpp v96, v94, v102 quad_perm:[2,2,2,2] row_mask:0xf bank_mask:0xf\n\t"
                 "v_and_b32_dpp v97, v94, v103 quad_perm:[2,2,2,2] row_mask:0xf bank_mask:0xf\n\t"
                 "v_mfma_f32_4x4x4_16b_bf16 v[84:87], v[100:101], v[140:141], v[84:87]\n\tv_mfma_f32_4x4x4_16b_bf16 v[88:91], v[100:101], v[142:143], v[88:91]\n\t"
                 "v_and_b32_dpp v100, v95, v102 quad_perm:[2,2,2,2] row_mask:0xf bank_mask:0xf\n\t"
                 "v_and_b32_dpp v101, v95, v103 quad_perm:[2,2,2,2] row_mask:0xf bank_mask:0xf\n\tv_mfma_f32_4x4x4_16b_bf16 v[84:87], v[96:97], v[144:145], v[84:87]\n\t"
                 "v_mfma_f32_4x4x4_16b_bf16 v[88:91], v[96:97], v[146:147], v[88:91]\n\tv_and_b32_dpp v96, v92, v102 quad_perm:[3,3,3,3] row_mask:0xf bank_mask:0xf\n\t"
                 "v_and_b32_dpp v97, v92, v103 quad_perm:[3,3,3,3] row_mask:0xf bank_mask:0xf\n\t"
                 "v_mfma_f32_4x4x4_16b_bf16 v[84:87], v[100:101], v[148:149], v[84:87]\n\tv_mfma_f32_4x4x4_16b_bf16 v[88:91], v[100:101], v[150:151], v[88:91]\n\t"
                 "v_and_b32_dpp v100, v93, v102 quad_perm:[3,3,3,3] row_mask:0xf bank_mask:0xf\n\t"
                 "v_and_b32_dpp v101, v93, v103 quad_perm:[3,3,3,3] row_mask:0xf bank_mask:0xf\n\tv_mfma_f32_4x4x4_16b_bf16 v[84:87], v[96:97], v[152:153], v[84:87]\n\t"
                 "v_mfma_f32_4x4x4_16b_bf16 v[88:91], v[96:97], v[154:155], v[88:91]\n\tv_and_b32_dpp v96, v94, v102 quad_perm:[3,3,3,3] row_mask:0xf bank_mask:0xf\n\t"
                 "v_and_b32_dpp v97, v94, v103 quad_perm:[3,3,3,3] row_mask:0xf bank_mask:0xf\n\t"
                 "v_mfma_f32_4x4x4_16b_bf16 v[84:87], v[100:101], v[156:157], v[84:87]\n\tv_mfma_f32_4x4x4_16b_bf16 v[88:91], v[100:101], v[158:159], v[88:91]\n\t"
                 "v_and_b32_dpp v100, v95, v102 quad_perm:[3,3,3,3] row_mask:0xf bank_mask:0xf\n\t"
                 "v_and_b32_dpp v101, v95, v103 quad_perm:[3,3,3,3] row_mask:0xf bank_mask:0xf\n\tv_mfma_f32_4x4x4_16b_bf16 v[84:87], v[96:97], v[160:161], v[84:87]\n\t"
                 "v_mfma_f32_4x4x4_16b_bf16 v[88:91], v[96:97], v[162:163], v[88:91]\n\ts_nop 1\n\t"
                 "v_mfma_f32_4x4x4_16b_bf16 v[84:87], v[100:101], v[164:165], v[84:87]\n\tv_mfma_f32_4x4x4_16b_bf16 v[88:91], v[100:101], v[166:167], v[88:91]\n\t"
                 "s_nop 4\n\tv_permlane32_swap_b32 v84, v88\n\tv_permlane32_swap_b32 v85, v89\n\tv_permlane32_swap_b32 v86, v90\n\tv_permlane32_swap_b32 v87, v91\n\t"
                 "v_add_f32 v84, v84, v88\n\tv_add_f32 v85, v85, v89\n\tv_add_f32 v86, v86, v90\n\tv_add_f32 v87, v87, v91\n\ts_nop 0\n\t"
                 "v_permlane16_swap_b32 v84, v85\n\tv_permlane16_swap_b32 v86, v87\n\ts_nop 0\n\tv_add_f32 v84, v84, v85\n\tv_add_f32 v86, v86, v87\n\ts_nop 1\n\t"
                 "v_add_f32_dpp v84, v84, v84 row_ror:8 row_mask:0xf bank_mask:0xf\n\tv_add_f32_dpp v86, v86, v86 row_ror:8 row_mask:0xf bank_mask:0xf\n\ts_nop 0\n\t"
                 "v_add_f32_dpp v84, v84, v84 row_ror:4 row_mask:0xf bank_mask:0xf\n\tv_add_f32_dpp v86, v86, v86 row_ror:4 row_mask:0xf bank_mask:0xf\n\t"
                 "ds_write2_b32 v99, v84, v86 offset1:8\n\ts_setpc_b64 s[92:93]\n\t"
                   :: "{s[0:1]}"(ap + 1), "{s2}"(bid), "{v0}"(wid * 64), KEPT() : "s90", "s91", "scc", "memory");
      __builtin_unreachable();
    }
    {
    constexpr int dk = 8;
    constexpr int BEST = FLG + 40, MEET = FLG + 48;
    __builtin_amdgcn_s_setprio(HPRIO);
    if (hw == 0) STAMP(dk, 0);
    const unsigned tb1 = (unsigned)__builtin_amdgcn_s_memrealtime();
    unsigned* hand2 = hand + HAND_N;
    const __amdgpu_buffer_rsrc_t rsh = __builtin_amdgcn_make_buffer_rsrc(hand2, 0, (HAND_N + PS_N) * 4, 0x00020000);
    const __amdgpu_buffer_rsrc_t rsx = __builtin_amdgcn_make_buffer_rsrc(xout, 0, 3 * D_ * 2, 0x00020000);
    constexpr int OOR = (HAND_N + PS_N) * 4;
    if (!p1blk && hw < 2) {
      f32x2 a[2], c[2];
#pragma unroll
      for (int e = 0; e < 4; ++e) {
        a[e >> 1][e & 1] = (__uint_as_float(pv[0][e] << 16) + __uint_as_float(pv[1][e] << 16)) + (__uint_as_float(pv[2][e] << 16) + __uint_as_float(pv[3][e] << 16));
        c[e >> 1][e & 1] = (__uint_as_float(pv[0][e] & 0xffff0000u) + __uint_as_float(pv[1][e] & 0xffff0000u)) + (__uint_as_float(pv[2][e] & 0xffff0000u) + __uint_as_float(pv[3][e] & 0xffff0000u));
      }
      const float r = (fnum + fold8(a[0], a[1])) * __builtin_amdgcn_rsqf(fvar + fold8(c[0], c[1]));
      unsigned key = (__float_as_uint(r) ^ ((unsigned)(__float_as_int(r) >> 31) | 0x80000000u)) & 0xfffff800u;
      if (r != r) key = 0u;
      key = wave_umax(key | (unsigned)(2047 - (rrow + (lane & 7) * 4 + (lane >> 4))));
      __builtin_amdgcn_raw_buffer_store_b64(u32x2{key, tb1 & 0x7fffffffu}, rsh, lane < NREP ? (HAND_N + PK_O + lane * 128 + (rrow >> 5) * 2) * 4 : OOR, 0, 16);
      if (hw == 1) STAMP(dk, 2);
    }
    gu_setup();
    bool got = true;
    auto get_fc = [&]() __attribute__((always_inline)) {
      const u32x2 fw = __builtin_amdgcn_raw_buffer_load_b64(rsh, (HAND_N + PK_O + (bid % NREP) * 128 + lane * 2) * 4, 0, 16);
      got = none(umax(fw[0], fw[1]) == SENT);
      if (got) {
        const unsigned c1 = wave_umax(fw[0]), c2 = wave_umax(fw[0] == c1 ? 0u : fw[0]);
#define BADFC 0
        const unsigned r1 = (2047u - (c1 & 0x7ffu)) ^ BADFC, r2 = 2047u - (c2 & 0x7ffu);
        unsigned dt = (unsigned)((int)((fw[1] - tb1) << 1) >> 1) + 512u;
        dt = dt < 1024u ? dt : 512u;
        *LDS_AT(volatile u32x2, lbase + SPM + 144) = u32x2{r1 | (r2 << 16), (unsigned)__builtin_amdgcn_readlane(dt, r1 >> 5) | (wave_umax(dt) << 16)};
      }
    };
    if (hw == 0) {
      __builtin_amdgcn_s_sleep(FCPRE);
#pragma unroll 1
      for (int i = 0; i < SPIN_MAX; ++i) {
        if (__builtin_amdgcn_readfirstlane(*LDS_AT(const volatile unsigned, lbase + FLG + 28)) - best >= 4u) break;
        __builtin_amdgcn_s_sleep(FCSLP);
      }
      asm volatile("" ::: "memory");
      get_fc();
    } else __builtin_amdgcn_s_sleep(TPRE - 10);
    PIN();
    tile_at(2u);
    const unsigned tb2 = (unsigned)__builtin_amdgcn_s_memrealtime() - 512u;
    LOGDEF;
    LOG();
#pragma unroll 1
    for (int spin = 0; spin < SPIN_MAX && !got; ++spin) { get_fc(); asm volatile("" ::: "memory"); }
    PIN();
    u32x2 fm = {SENT, 0u};
#pragma unroll 1
    for (int i = 0; i < 4 * SPIN_MAX && fm[0] == SENT; ++i) {
      const u32x2 q = *LDS_AT(const volatile u32x2, lbase + SPM + 144);
      fm = u32x2{(unsigned)__builtin_amdgcn_readfirstlane(q[0]), (unsigned)__builtin_amdgcn_readfirstlane(q[1])};
    }
    const int fr1 = fm[0] & 0x7ffu, fr2 = (fm[0] >> 16) & 0x7ffu;
    const unsigned t1 = tb2 + (fm[1] & 1023u), tmx = tb2 + ((fm[1] >> 16) & 1023u);
    auto until = [&](unsigned te) { while ((int)((unsigned)__builtin_amdgcn_s_memrealtime() - te) < 0) __builtin_amdgcn_s_sleep(1); asm volatile("" ::: "memory"); };
    lds_wr(lbase + SPM + 136, tmx);
    PIN();
    if (hw == 0) STAMP(dk, 3);
    const int xoff = (hw * 256 + (lane & 3) * 64 + (lane >> 2) * 4) * 2;
    const int oc = (omi * 2 + (oni >> 3)) * 32, col = oc + (lane & 7) * 4 + (lane >> 4);
    const int xoo = !p1blk && hw == 3 ? (col & ~1) * 2 : S_ * D_ * 2;
    auto get_row = [&](int r, int sl, u32x2& xr, unsigned& xo) __attribute__((always_inline)) {
      if ((r >> 7) == omi) {
#pragma unroll 1
        for (int i = 0; i < SPIN_MAX; ++i) {
          if (__builtin_amdgcn_readfirstlane(*LDS_AT(const volatile unsigned char, lbase + FLG + 24)) >= 16u) break;
          __builtin_amdgcn_s_sleep(1);
        }
        asm volatile("" ::: "memory");
      }
#pragma unroll 1
      for (int spin = 0; spin < SPIN_MAX; ++spin) {
        LOG();
        xr = __builtin_amdgcn_raw_buffer_load_b64(rsx, xoff, sl * (D_ * 2), 16);
        xo = __builtin_amdgcn_raw_buffer_load_b32(rsx, xoo, sl * (D_ * 2), 16);
        LOG();
        if (none(umax(umax(xr[0], xr[1]), xo) == SENT)) break;
        __builtin_amdgcn_s_sleep(XRSLP);
        asm volatile("" ::: "memory");
      }
      PIN();
    };
    auto row_md = [&](const u32x2& xr, int k) __attribute__((always_inline)) {
      auto dot = [&](unsigned x, unsigned y, float z) { return __builtin_amdgcn_fdot2_f32_bf16(__builtin_bit_cast(bf16x2, x), __builtin_bit_cast(bf16x2, y), z, false); };
      const float p1 = dot(xr[1], 0x3f803f80u, dot(xr[0], 0x3f803f80u, 0.f)), p2 = dot(xr[1], xr[1], dot(xr[0], xr[0], 0.f));
      const auto q = __builtin_amdgcn_permlane32_swap(__float_as_uint(p1), __float_as_uint(p2), false, false);
      const float qs = __uint_as_float(q[0]) + __uint_as_float(q[1]);
      const auto q2 = __builtin_amdgcn_permlane16_swap(__float_as_uint(qs), __float_as_uint(qs), false, false);
      float sv = __uint_as_float(q2[0]) + __uint_as_float(q2[1]);
      sv = dpp_add<0x121, 0xf>(dpp_add<0x122, 0xf>(dpp_add<0x124, 0xf>(dpp_add<0x128, 0xf>(sv))));
      lds_wr(lbase + SPM + k * 64 + (lane >> 4) * 16 + hw * 4, f2word(sv));
      u32x4 a, c;
#pragma unroll 1
      for (int i = 0; i < SPIN_MAX; ++i) {
        a = lds_rd4(lbase + SPM + k * 64); c = lds_rd4(lbase + SPM + k * 64 + 32);
        if (__builtin_amdgcn_readfirstlane(umax(umax(a[0], a[1]), umax(a[2], a[3]))) != SENT) break;
      }
      auto sum4 = [&](const u32x4& v) { return (__uint_as_float(v[0]) + __uint_as_float(v[1])) + (__uint_as_float(v[2]) + __uint_as_float(v[3])); };
      const float mean = sum4(a) * (1.0f / 1024.0f), msq = sum4(c) * (1.0f / 1024.0f);
      const float den = rbf(__builtin_amdgcn_sqrtf(rbf(rbf(fmaxf(fmaf(-mean, mean, msq), 0.f)) + 1e-5f)));
      return (unsigned)__builtin_amdgcn_readfirstlane((pack2(mean, mean) & 0xffffu) | (__float_as_uint(den) & 0xffff0000u));
    };
    auto ffn = [&](const u32x2& xr, unsigned md, unsigned n, const __amdgpu_buffer_rsrc_t rs, int yo, auto&& off) __attribute__((always_inline)) {
      gate_up(xr, md);
      if (hw == 0 && n == 2u) STAMP(dk, 8);
      asm volatile("" ::: "memory");
      PIN();
      lds_wr(lbase + MEET + hw * 4, n + off());
      u32x4 mt;
#pragma unroll 1
      for (int i = 0; i < SPIN_MAX; ++i) {
        mt = lds_rd4(lbase + MEET);
        if (__builtin_amdgcn_readfirstlane(min4(mt)) >= n) break;
      }
      asm volatile("" ::: "memory");
      PIN();
      if (hw == 0 && n == 2u) STAMP(dk, 9);
      if (__builtin_amdgcn_readfirstlane(mt[0]) != n + 1u)
        __builtin_amdgcn_raw_buffer_store_b128(down(act_diag(n == 4u ? 512 : 0), w), rs, (yo + fg * D_) * 4 + t * 16, 0, 16);
      dad += n == 2u ? 512 : -512;
      if (hw == 0 && n == 2u) STAMP(dk, 4);
    };
    auto y_sum4 = [&](const __amdgpu_buffer_rsrc_t rs, int yo, int k) __attribute__((always_inline)) {
      const int src = (yo + (hw * 56 + (lane >> 3)) * D_ + oc + (lane & 7) * 4) * 4;
      u32x4 v[7];
#pragma unroll
      for (int i = 0; i < 7; ++i) asm volatile("" : "=v"(v[i]));
      unsigned todo = 0x7fu;
#pragma unroll 1
      for (int spin = 0; spin < SPIN_MAX; ++spin) {
        LOG();
#pragma unroll
        for (int i = 0; i < 7; ++i) if ((todo >> i) & 1u) v[i] = __builtin_amdgcn_raw_buffer_load_b128(rs, src, i * (8 * D_ * 4), 16);
#pragma unroll
        for (int i = 0; i < 7; ++i) if (none(umax(umax(v[i][0], v[i][1]), umax(v[i][2], v[i][3])) == SENT)) todo &= ~(1u << i);
        if (!todo) break;
        __builtin_amdgcn_s_sleep(YSLP);
        asm volatile("" ::: "memory");
      }
      f32x2 z0 = {0.f, 0.f}, z1 = {0.f, 0.f};
#pragma unroll
      for (int i = 0; i < 7; ++i) {
        z0 += f32x2{__uint_as_float(v[i][0]), __uint_as_float(v[i][1])};
        z1 += f32x2{__uint_as_float(v[i][2]), __uint_as_float(v[i][3])};
      }
      const unsigned ya = lbase + SPM + 256 + k * 512 + ((lane & 7) * 4 + (lane >> 4)) * 16;
      lds_wr((lane & 8) ? lbase + DUM + lane * 4 : ya + hw * 4, f2word(fold8(z0, z1)));
      u32x4 q = {0u, 0u, 0u, 0u};
      if (hw == 3) {
#pragma unroll 1
        for (int i = 0; i < 4 * SPIN_MAX; ++i) {
          q = lds_rd4(ya);
          if (none(umax(umax(q[0], q[1]), umax(q[2], q[3])) == SENT)) break;
        }
      }
      return (__uint_as_float(q[0]) + __uint_as_float(q[1])) + (__uint_as_float(q[2]) + __uint_as_float(q[3]));
    };
    const __amdgpu_buffer_rsrc_t rs1 = __builtin_amdgcn_make_buffer_rsrc(hand, 0, HAND_N * 4, 0x00020000);
    u32x2 xr;
    unsigned xo, xo2;
    until(t1 + XDT);
    get_row(fr1, 0, xr, xo);
    if (hw == 0) STAMP(dk, 7);
#define FAILFC 0
    const unsigned md1 = row_md(xr, 0) ^ (FAILFC & 1);
    if (hw == 0) STAMP(dk, 10);
    LOGAT(20);
    u32x2 xr2;
    unsigned md2 = 0u;
    if (p1blk) {
      xr2 = __builtin_amdgcn_raw_buffer_load_b64(rsx, (fr2 >> 7) == omi ? S_ * D_ * 2 : xoff, D_ * 2, 16);
      PIN();
      ffn(xr, md1, 2u, rsh, GRAN_N, [&]() { return 0u; });
      __builtin_amdgcn_s_setprio(R2PRIO);
      LOG();
      if ((fr2 >> 7) == omi || !none(umax(xr2[0], xr2[1]) == SENT)) get_row(fr2, 1, xr2, xo2);
      md2 = row_md(xr2, 1) ^ (FAILFC & 2);
      ffn(xr2, md2, 4u, rs1, GRAN_N, [&]() {
        const u32x2 v = *LDS_AT(const volatile u32x2, lbase + BEST);
        return v[0] != 0u && (2047 - (int)(v[0] & 0x7ffu) != fr2 || v[1] != md2) ? 1u : 0u; });
    }
    if (hw == 0) STAMP(dk, 5);
    LOG();
    float y = 0.f;
    auto put_row = [&](int r, unsigned md, unsigned rb) __attribute__((always_inline)) {
      const float rden = __uint_as_float(md & 0xffff0000u), rs = rbf(frcp(1.0f + __expf(-__uint_as_float(rb << 16))));
      const float xa = __uint_as_float((col & 1) ? xo & 0xffff0000u : xo << 16);
      float ha = xa, hb = xa;
      ln_apply2(ha, hb, __uint_as_float(md << 16), rden, frcp(rden));
      const float o = xa + rbf(ha + rbf(rbf(y) * rs));
      __builtin_amdgcn_raw_buffer_store_b16((unsigned short)(pack2(o, o) & 0xffffu), __builtin_amdgcn_make_buffer_rsrc(out, 0, S_ * D_ * 2, 0x00020000),
                                            (lane & 8) ? S_ * D_ * 2 : (r * D_ + col) * 2, 0, OAUX);
    };
    if (!p1blk) {
      until(tmx + XD2);
      get_row(fr2, 1, xr2, xo2);
      md2 = row_md(xr2, 1) ^ (FAILFC & 2);
      until(t1 + XDT + YD1); until(tmx + YD2);
      if (hw == 3) STAMP(dk, 13);
      LOGAT(8);
      y = y_sum4(rsh, GRAN_N, 0);
      if (hw == 3) STAMP(dk, 14);
      LOGAT(24);
    }
    unsigned bmd = 0u;
    best = 0u;
#pragma unroll 1
    for (int spin = 0; spin < 4 * SPIN_MAX; ++spin) {
      const u32x2 v = *LDS_AT(const volatile u32x2, lbase + BEST);
      best = __builtin_amdgcn_readfirstlane(v[0]); bmd = __builtin_amdgcn_readfirstlane(v[1]);
      if (best) break;
      __builtin_amdgcn_s_sleep(1);
    }
    asm volatile("" ::: "memory");
    const int idx = 2047 - (int)(best & 0x7ffu);
    const unsigned rk = best >> 16, rb = (rk & 0x8000u) ? (rk & 0x7fffu) : (~rk & 0xffffu);
    if (hw == 0) STAMP(dk, 1);
    LOGAT(25);
    LOGOUT(hw);
    if (__builtin_expect(idx != fr1 || bmd != md1, 0)) {
      if (idx == fr2 && bmd == md2) {
        if (p1blk) return;
        y = y_sum4(rs1, GRAN_N, 1);
        xo = xo2;
      } else {
        get_row(idx, idx == fr1 ? 0 : (idx == fr2 ? 1 : 2), xr, xo);
        if (p1blk) ffn(xr, bmd, 6u, rsh, HAND_N + Y3_O, [&]() { return 0u; });
        else y = y_sum4(rsh, HAND_N + Y3_O, 2);
        if (p1blk || hw != 3) return;
#pragma unroll 1
        for (int spin = 0; spin < SPIN_MAX && idx != fr1 && idx != fr2; ++spin) {
          if (__builtin_amdgcn_readfirstlane(ld_ag(gran + GR_F + (oc >> 6) * 32)) == tag) break;
          asm volatile("" ::: "memory");
        }
      }
    }
    if (p1blk || hw != 3) return;
    put_row(idx, bmd, rb);
    }
    return;
  }
  if (wid >= 4) {
    const int lane = fresh_lane();
    const int lw = wid - 4;
    const long long lay_ = ap->lay;
    const u16* A = ap->a; const u16* W = ap->W; const u16* wc = ap->wc;
#define LPRIO 3
#define LP0 3
    __builtin_amdgcn_s_setprio(LP0);
    unsigned va[4], vw[6];
    const unsigned ul = lane;
    va[0] = (unsigned)(m0 + lw * 32) * (K * 2) + (ul >> 3) * (K * 2) + (((ul & 7u) ^ (ul >> 4)) << 4);
    va[1] = va[0] + (8 * K * 2 - 1024);
    va[2] = (va[0] ^ 64u) + 2 * (8 * K * 2 - 1024);
    va[3] = va[2] + (8 * K * 2 - 1024);
#pragma unroll
    for (int i = 0; i < 3; ++i) {
      const unsigned n = ul + 64u * i, k = __umul24(n, 171u) >> 12, cp = n - __umul24(k, 24u);
      const unsigned c = (cp & 7u) ^ ((k & 2u) << 1), part = (cp >> 3) ^ (cp < 16u ? ((c >> 2) ^ (unsigned)tmi) & 1u : 0u);
      vw[i] = __umul24(k, ldw * 2) + part * 2048u + (c << 4) + ((unsigned)(lw * 16 * ldw + n0) * 2u - i * 1024u);
      vw[i + 3] = (vw[i] ^ 16u) + 8 * ldw * 2;
    }
    PIN();
    asm volatile("" : "+v"(va[0]), "+v"(va[1]), "+v"(va[2]), "+v"(va[3]), "+v"(vw[0]), "+v"(vw[1]), "+v"(vw[2]), "+v"(vw[3]), "+v"(vw[4]), "+v"(vw[5]));
    const bool LAY2 = lay_ != 0;
    const int krot = tmi & 8;
    if (LAY2 && slow_cols(W, tni)) W = wc;
    auto stage_a = [&](int kt, int slot) {
      const char* pa = reinterpret_cast<const char*>(A) + (size_t)(kt ^ krot) * 128;
      asm volatile("s_mov_b32 m0, %5\n\ts_nop 0\n\tglobal_load_lds_dwordx4 %0, %4 sc1\n\tglobal_load_lds_dwordx4 %1, %4 offset:1024 sc1\n\t"
                   "global_load_lds_dwordx4 %2, %4 offset:2048 sc1\n\tglobal_load_lds_dwordx4 %3, %4 offset:3072 sc1"
                   :: "v"(va[0]), "v"(va[1]), "v"(va[2]), "v"(va[3]), "s"(pa), "s"(lbase + slot * STB + lw * 4096) : "m0", "memory");
    };
    auto stage_w = [&](int kt, int slot) {
      const char* pw = reinterpret_cast<const char*>(W) + (size_t)(kt ^ krot) * 128 * ldw;
      asm volatile("s_mov_b32 m0, %7\n\ts_nop 0\n\tglobal_load_lds_dwordx4 %0, %6 sc1\n\tglobal_load_lds_dwordx4 %1, %6 offset:1024 sc1\n\t"
                   "global_load_lds_dwordx4 %2, %6 offset:2048 sc1\n\ts_addk_i32 m0, 0xc00\n\ts_nop 0\n\tglobal_load_lds_dwordx4 %3, %6 sc1\n\t"
                   "global_load_lds_dwordx4 %4, %6 offset:1024 sc1\n\tglobal_load_lds_dwordx4 %5, %6 offset:2048 sc1"
                   :: "v"(vw[0]), "v"(vw[1]), "v"(vw[2]), "v"(vw[3]), "v"(vw[4]), "v"(vw[5]), "s"(pw), "s"(lbase + slot * STB + ABYTES + lw * 6144) : "m0", "scc", "memory");
    };
    if (LAY2) {
      __builtin_amdgcn_s_setprio(0);
      __builtin_amdgcn_s_sleep(TPRE - 10);
      tile_at(3u);
      __builtin_amdgcn_s_setprio(LPRIO);
#pragma unroll
      for (int st = 0; st < NST; ++st) stage_w(st, st);
      PIN();
      __builtin_amdgcn_s_barrier();
      PIN();
#pragma unroll
      for (int st = 0; st < NST; ++st) stage_a(st, st);
      asm volatile("s_waitcnt vmcnt(8)" ::: "memory");
    } else {
      __builtin_amdgcn_s_setprio(LPRIO);
      stage_a(0, 0); stage_w(0, 0);
      if (PRO > 1) { stage_a(1, 1); stage_w(1, 1); }
      if (PRO > 2) { stage_a(2, 2); stage_w(2, 2); }
      asm volatile("s_waitcnt vmcnt(%0)" :: "n"(10 * PRO - 10) : "memory");
    }
    int slot = 0;
    __builtin_amdgcn_s_barrier();
    if (!LAY2) {
      if (PRO < 2) { stage_a(1, 1); stage_w(1, 1); }
      if (PRO < 3) { stage_a(2, 2); stage_w(2, 2); }
    }
    for (int kt = 0; kt + 1 < NKT; ++kt) {
      if (LAY2 && kt == 0) asm volatile("s_waitcnt vmcnt(4)" ::: "memory");
      else if (kt + 2 >= NKT) asm volatile("s_waitcnt vmcnt(0)" ::: "memory");
      else asm volatile("s_waitcnt vmcnt(10)" ::: "memory");
      __builtin_amdgcn_s_barrier();
      if (kt + NST < NKT) { stage_a(kt + NST, slot); stage_w(kt + NST, slot); }
      slot = slot == NST - 1 ? 0 : slot + 1;
    }
    __builtin_amdgcn_s_setprio(0);
    __builtin_amdgcn_s_barrier();
  } else {
  LAYER_HERE;
  const int dk = LAY2 ? 5 : 1;
  const int lane = fresh_lane(), wm = wid & 1, wn = wid >> 1;
  if (wid == 0) STAMP(dk, 0);
  f32x4 acc[4][6];
#pragma unroll
  for (int i = 0; i < 4; ++i)
#pragma unroll
    for (int j = 0; j < 6; ++j) acc[i][j] = f32x4{0.f, 0.f, 0.f, 0.f};
  const int q16 = lane & 15, g4 = lane >> 4;
  const int e0 = 16 * (q16 >> 3) + (q16 & 7);
  s16x8 af[2][4];
  unsigned aad[2], wad[2];
  {
    const int row = wm * 64 + e0, swz = ((row >> 1) & 3) | (((row >> 4) & 1) << 2);
#pragma unroll
    for (int h = 0; h < 2; ++h) aad[h] = lbase + row * 128 + (((4 * h + g4) ^ swz) << 4);
    const int tq = (lane & 15) >> 2, tp = lane & 3, k = 8 * g4 + tq, ch = wn * 4 + 2 * (tp >> 1);
    wad[0] = lbase + ABYTES + k * 384 + ((ch ^ (4 * ((k >> 1) & 1)) ^ (g4 & 1)) << 4) + 8 * (tp & 1);
    wad[1] = wad[0] ^ 16;
  }
  auto lda = [&](unsigned a, int i) { return *LDS_AT(const s16x8, a + (i >> 1) * 4096 + (i & 1) * 1024); };
  auto ldw_ = [&](int h, int j) {
    const unsigned w = wad[j & 1] + h * 12288 + (j >> 1) * 128;
    s16x4 lo = __builtin_amdgcn_ds_read_tr16_b64_v4i16(LDS_AT(s16x4, w));
    s16x4 h4 = __builtin_amdgcn_ds_read_tr16_b64_v4i16(LDS_AT(s16x4, w + 1536));
    return (s16x8)__builtin_shufflevector(lo, h4, 0, 1, 2, 3, 4, 5, 6, 7);
  };
  ARGS_HERE;
  const bool give = wn != (tmi & 1);
  unsigned lto = lbase + (give ? 2 * KSB : 0) + wm * 4096, lqo = lbase + QOFF + wid * 4096;
  char* qgb = reinterpret_cast<char*>(KV) + QG + (((LAY2 ? 256 : 0) + tmi * 16 + tni) * 2 + wm) * 4096 - lto;
  char* kvb = reinterpret_cast<char*>(KV) + ((tni * 2 + wn) * 64 + tmi * 4 + wm * 2) * 2048 - lto;
  {
    const unsigned ato = lbase + ALPHA + wm * 256, dum = lbase + DUM + lane * 4;
    *LDS_AT(u32x4, lqo + lane * 16) = u32x4{lane >= 16 ? lbase + CRED : lbase + TABA + (wm * 64 + e0) * 16, lbase + TABW + (wn * 32 + e0) * 16,
                                                   ato + (give ? 512 : 0) + e0 * 4, lto + (lane >> 5) * 1024 + ((lane & 28) * 2 + (lane & 3)) * 16};
    *LDS_AT(u32x2, lqo + 2048 + lane * 16) = u32x2{aad[0], aad[1]};
    *LDS_AT(u32x4, lqo + 1024 + lane * 16) = u32x4{ato + g4 * 16 + (lane >> 5) * 32, lto + (lane >> 5) * 1024 + ((lane & 24) * 2 + (lane & 7)) * 16,
                                                          lane == 0 ? lbase + FLG + 32 : dum, lane == 0 ? lbase + FLG + 16 : dum};
  }
  {
    const __amdgpu_buffer_rsrc_t rq = __builtin_amdgcn_make_buffer_rsrc(qgb + lto, 0, give && !LAY2 ? 4096 : 0, 0x00020000);
#pragma unroll
    for (int i = 0; i < 4; ++i) __builtin_amdgcn_raw_buffer_store_b128(u32x4{SENT, SENT, SENT, SENT}, rq, lane * 16 + i * 1024, 0, 16);
  }
  unsigned qmv = lqo - lto;
  asm volatile("" : "+s"(lqo), "+s"(qmv), "+s"(kvb), "+s"(qgb) :: "memory");
#pragma unroll
  for (int i = 0; i < 4; ++i)
#pragma unroll
    for (int j = 0; j < 6; ++j) asm volatile("" : "+v"(acc[i][j]));
  asm volatile("" : "+v"(aad[0]), "+v"(aad[1]), "+v"(wad[0]), "+v"(wad[1]));
  if (LAY2) __builtin_amdgcn_s_barrier();
  __builtin_amdgcn_s_barrier();
  PIN();
  if (wid == 0) STAMP(dk, 1);
  s16x8 wq[4][2];
  wq[0][0] = ldw_(0, 0); af[0][0] = lda(aad[0], 0); wq[0][1] = ldw_(1, 0); af[1][0] = lda(aad[1], 0); wq[1][0] = ldw_(0, 1); wq[1][1] = ldw_(1, 1);
  af[0][1] = lda(aad[0], 1); af[1][1] = lda(aad[1], 1); af[0][2] = lda(aad[0], 2); af[1][2] = lda(aad[1], 2);
  PIN();
  s16x4 wlo;
  auto rw = [&](s16x8& d, int h, int j, int hi, int off = 0) __attribute__((always_inline)) {
    const s16x4 v = __builtin_amdgcn_ds_read_tr16_b64_v4i16(LDS_AT(s16x4, wad[j & 1] + h * 12288 + (j >> 1) * 128 + hi * 1536 + off));
    if (hi) d = (s16x8)__builtin_shufflevector(wlo, v, 0, 1, 2, 3, 4, 5, 6, 7);
    else wlo = v;
  };
  auto at = [&](s16x8& x, int r) __attribute__((always_inline)) {
    if (r == 0) asm volatile("" :: "{v[104:107]}"(x));
    if (r == 1) asm volatile("" :: "{v[108:111]}"(x));
    if (r == 2) asm volatile("" :: "{v[112:115]}"(x));
    if (r == 3) asm volatile("" :: "{v[116:119]}"(x));
    if (r == 4) asm volatile("" :: "{v[120:123]}"(x));
    if (r == 5) asm volatile("" :: "{v[124:127]}"(x));
    if (r == 6) asm volatile("" :: "{v[128:131]}"(x));
    if (r == 7) asm volatile("" :: "{v[132:135]}"(x));
    if (r == 8) asm volatile("" :: "{v[136:139]}"(x));
    if (r == 9) asm volatile("" :: "{v[140:143]}"(x));
    if (r == 10) asm volatile("" :: "{v[144:147]}"(x));
    if (r == 11) asm volatile("" :: "{v[148:151]}"(x));
    if (r == 12) asm volatile("" :: "{v[152:155]}"(x));
    if (r == 13) asm volatile("" :: "{v[156:159]}"(x));
    if (r == 14) asm volatile("" :: "{v[160:163]}"(x));
    if (r == 15) asm volatile("" :: "{v[164:167]}"(x));
  };
  auto mm = [&](int i, int j, int h, int sl, int hw_) __attribute__((always_inline)) {
    s16x8 &a = af[h][i], &w = wq[sl][hw_];
    at(a, h * 4 + i); at(w, 8 + sl * 2 + hw_);
    if (j >= 4) asm volatile("v_mfma_f32_16x16x32_bf16 %0, %1, %2, %0" : "+v"(acc[i][j]) : "v"(a), "v"(w));
    else asm volatile("v_mfma_f32_16x16x32_bf16 %0, %1, %2, %0" : "+v"(acc[i][j]) : "v"(w), "v"(a));
    PIN();
  };
  auto next = [&](int g, int J0, int s0, int s1, int off) __attribute__((always_inline)) {
    if (g == 2 || g == 3) rw(wq[s0][0], 0, J0, g & 1, off);
    if (g == 4 || g == 5) af[g & 1][0] = lda(aad[g & 1] + off, 0);
    if (g == 6 || g == 7) rw(wq[s0][1], 1, J0, g & 1, off);
    if (g >= 8 && g < 12) rw(wq[s1][(g - 8) >> 1], (g - 8) >> 1, J0 + 1, g & 1, off);
    if (g >= 12) af[g & 1][(g - 10) >> 1] = lda(aad[g & 1] + off, (g - 10) >> 1);
  };
  auto third = [&](int p, int J0, int delta) __attribute__((always_inline)) {
    auto mv = [&](unsigned& x) __attribute__((always_inline)) { asm volatile("v_add_u32 %0, %1, %0" : "+v"(x) : "s"(delta)); };
#pragma unroll
    for (int g = 0; g < 16; ++g) {
      const int h = g & 1, i = p == 1 ? (g >> 1) & 3 : g >> 2, t = p == 1 ? g >> 3 : (g >> 1) & 1, j = 2 * p + t, sl = p == 1 ? 2 - 2 * t : p + t;
      mm(i, j, h, sl, h);
      if (p == 0) {
        if (g < 2) af[g][3] = lda(aad[g], 3);
        else if (g < 6) rw(wq[2][(g - 2) >> 1], (g - 2) >> 1, 2, g & 1);
        else if (g < 10) rw(wq[3][(g - 6) >> 1], (g - 6) >> 1, 5, g & 1);
        else if (g < 12) mv(aad[g - 10]);
      }
      if (p == 1) {
        if (g < 4) rw(wq[0][g >> 1], g >> 1, 3, g & 1);
        if (g == 4) mv(wad[1]);
        if (g >= 8 && g < 12) rw(wq[2][(g - 8) >> 1], (g - 8) >> 1, 4, g & 1);
        if (g == 12) mv(wad[0]);
      }
      if (p == 2) {
        if (g == BARG) {
          asm volatile("" ::: "memory");
          PIN();
          __builtin_amdgcn_s_barrier();
          asm volatile("" ::: "memory");
        }
        next(g, J0, 0, 1, 0);
      }
      PIN();
      {
        if ((p == 2 && g == 15) || (p == 0 && g == 7)) __builtin_amdgcn_s_waitcnt(0xc67f);
        if (p == 0 && g == 2) __builtin_amdgcn_s_waitcnt(0xc57f);
        if ((p == 0 && g == 14) || (p == 1 && (g & 7) == 7)) __builtin_amdgcn_s_waitcnt(0xc07f);
        PIN();
      }
    }
  };
  third(0, 0, STB); third(1, 0, STB);
  {
    static_assert((NKT - NST - 1) % NST == 0, "");
#pragma unroll 1
    for (int kt = 0; kt < NKT - NST - 1; kt += NST) {
#pragma unroll
      for (int u = 0; u < NST; ++u) {
        const int delta = u == 1 ? -(NST - 1) * STB : STB;
        third(2, 0, 0); third(0, 0, delta); third(1, 0, delta);
      }
      asm volatile("s_waitcnt vmcnt(0)" ::: "memory");
    }
  }
  static_assert(NKT % NST == 1, "slots of the last stages");
#define TT(i)
  auto bar = [&]() { PIN(); __builtin_amdgcn_s_barrier(); asm volatile("" ::: "memory"); PIN(); };
  TT(0);
  third(2, 2, 0);
  const __amdgpu_buffer_rsrc_t rsk = __builtin_amdgcn_make_buffer_rsrc(give ? qgb : kvb, 0, 0x7fffffff, 0x00020000), rsv = __builtin_amdgcn_make_buffer_rsrc(kvb + VOFF, 0, 0x7fffffff, 0x00020000);
  const __amdgpu_buffer_rsrc_t rsq = __builtin_amdgcn_make_buffer_rsrc(kvb - qmv, 0, give ? 0x7fffffff : 0, 0x00020000);
  unsigned pk;
  u32x2 t0, t1, t2, t3;
  auto park = [&](int i) { return *LDS_AT(const volatile u32x2, pk + (i >> 1) * 1024 + (i & 1) * 8); };
  float al[4];
  f32x4 av[4];
  u32x4 kvp[12];
  auto cacc = [&](int p, int x, int t) -> f32x4& { return p < 2 ? acc[x][2 * p + t] : acc[(x >> 1) * 2 + t][4 + (x & 1)]; };
  auto signal = [&](unsigned v) { __hip_atomic_fetch_add(LDS_AT(unsigned, t3[0]), v, __ATOMIC_RELAXED, __HIP_MEMORY_SCOPE_WORKGROUP); PIN(); };
  auto outop = [&](int p, int k) {
    if (k >= 0 && k < 16) {
      const int x = k >> 2, t = (k >> 1) & 1;
      f32x4& c = cacc(p, x, t);
#pragma unroll
      for (int o = 2 * (k & 1); o < 2 * (k & 1) + 2; ++o) {
        float r;
        asm volatile("v_mul_f32 %0, %1, %2" : "=v"(r) : "v"(c[o]), "v"(p < 2 ? al[x] : av[(x >> 1) * 2 + t][o]));
        c[o] = r;
      }
      PIN();
    }
    if (k >= 1 && k <= 16) {
      const int x = (k - 1) >> 2, t = ((k - 1) >> 1) & 1, o = (k - 1) & 1;
      kvp[p * 4 + x][2 * t + o] = pack2(cacc(p, x, t)[2 * o], cacc(p, x, t)[2 * o + 1]);
      PIN();
      if (t && o) {
        if (p) __builtin_amdgcn_raw_buffer_store_b128(kvp[p * 4 + x], p == 1 ? rsk : rsv, (p == 1 ? t1[1] : t2[1]) + (x >> 1) * 2048 + (x & 1) * (p == 1 ? 64 : 128), 0, 16);
        else {
          __builtin_amdgcn_raw_buffer_store_b128(kvp[x], rsq, t1[1] + (x >> 1) * 2048 + (x & 1) * 64, 0, 16);
          *LDS_AT(u32x4, t1[1] + (x >> 1) * 2048 + (x & 1) * 64) = kvp[x];
        }
        PIN();
      }
    }
  };
  static_for<0, 168>([&](auto nc) __attribute__((always_inline)) {
    constexpr int n = decltype(nc)::value, sec = n < 80 ? n >> 4 : (n < 104 ? 5 : 6 + ((n - 104) >> 4)), g = n < 104 ? n - 16 * sec : (n - 104) & 15;
    auto taba = [&](int i) { af[0][i] = *LDS_AT(const s16x8, t0[0] + (i >> 1) * 512 + (i & 1) * 128); };
    auto tabw = [&](int j) { wq[(2 + (j >> 1)) & 3][j & 1] = *LDS_AT(const s16x8, t0[1] + ((j >> 1) * 64 + (j & 1) * 8) * 16); };
    auto rwn = [&](int e, int sl, int j, int off) { if (e >= 0 && e < 4) rw(wq[sl][e >> 1], e >> 1, j, e & 1, off); };
    if (g == 0) TT(1 + sec);
    if (sec == 5) {
      const int i = (g / 12) * 2 + (g & 1), j = (g % 12) >> 1;
      mm(i, j, 0, (2 + (j >> 1)) & 3, j & 1);
    } else {
      constexpr int part[10] = {1, 2, 1, 2, 0, 0, 1, 2, 0, 0}, s0[10] = {0, 2, 0, 2, 0, 0, 1, 3, 1, 3};
      const int h = g & 1, i = sec == 7 ? (g >> 3) * 2 + ((g >> 1) & 1) : g >> 2, t = sec == 7 ? (g >> 2) & 1 : (g >> 1) & 1;
      mm(i, 2 * part[sec] + t, h, (s0[sec] + t) & 3, h);
    }
    if (sec == 0 || sec == 2) {
      if (g < 2) af[g][3] = lda(aad[g] + sec / 2 * STB, 3);
      rwn(g - 2, 2, 4, sec / 2 * STB); rwn(g - 6, 3, 5, sec / 2 * STB);
    }
    if (sec == 1) {
      if (g == 1) bar();
      next(g, 2, 0, 1, STB);
    }
    if ((sec == 3 && g == 12) || (sec == 6 && g == 13)) { pk = lqo + fresh_lane() * 16; asm volatile("" : "+v"(pk)); }
    if (sec == 3) {
      rwn(g, 0, 0, STB); rwn(g - 4, 1, 1, STB);
      if (g >= 8 && g < 12) asm volatile("v_subrev_u32 %0, %1, %0" : "+v"(g < 10 ? aad[g & 1] : wad[g & 1]) : "s"(STB));
      if (g == 13) t1 = park(1);
      if (g == 14) t0 = park(0);
    }
    if (sec == 4) {
      if (g == B15) bar();
      if (g == 4) taba(0);
      if (g == 5 || g == 6) tabw(g - 5);
      if (g == 7) taba(1);
      if (g == 8 || g == 9) tabw(g - 6);
      if (g == 10 || g == 11) af[1][g - 10] = lda(aad[1], g - 10);
      if (g == 12) taba(2);
      if (g == 15) taba(3);
    }
    if (sec == 5) {
      if (g < 2) tabw(4 + g);
      rwn(g - 2, 1, 2, 0);
      if (g == 12 || g == 13) af[0][g - 12] = lda(aad[0], g - 12);
      if (g == 22) { al[0] = *LDS_AT(const float, t1[0]); al[1] = *LDS_AT(const float, t1[0] + 32); }
      rwn(g - 16, 2, 3, 0);
      if (g == 20 || g == 21) af[1][g - 18] = lda(aad[1], g - 18);
    }
    if (sec == 6) {
      if (g < 2) af[0][2 + g] = lda(aad[0], 2 + g);
      rwn(g - 2, 3, 4, 0);
      if (g == 9) { al[2] = *LDS_AT(const float, t1[0] + 128); al[3] = *LDS_AT(const float, t1[0] + 160); }
      if (g == 14) t2 = park(2);
      if ((g & 3) >= 2) rwn((g - 10) / 4 * 2 + (g & 1) - (g < 10) * 8, 0, 5, 0);
    }
    if (sec == 7) {
      if (g == 2 || g == 3 || g == 10 || g == 11) av[(g >> 3) * 2 + (g & 1)] = *LDS_AT(const f32x4, t2[0] + ((g >> 3) * 32 + (g & 1) * 8) * 4);
      rwn(g < 2 ? g : (g < 4 ? -1 : g - 2), 1, 0, 0); rwn(g - 6, 2, 1, 0);
      if (g == 7) asm volatile("v_xor_b32 %0, 0x200, %0" : "+v"(t1[0]));
      if (g == 14) {
#pragma unroll
        for (int x = 0; x < 4; ++x) al[x] = *LDS_AT(const float, t1[0] + (x >> 1) * 128 + (x & 1) * 32);
      }
      if (g == 12) t3 = park(3);
      if (g == 13) { const u32x2 a = park(4); aad[0] = a[0]; aad[1] = a[1]; }
      if (g == 15) {
        asm volatile("" ::: "memory");
        __builtin_amdgcn_s_waitcnt(0xc07f);
        bar();
      }
    }
    if (sec == 8) next(g, 0, 3, 0, STB);
    if (sec == 9) {
      if (g < 2) af[g][3] = lda(aad[g] + STB, 3);
      if (g == 2) signal(0x10000u);
    }
    PIN();
    outop(1, n - 104 - KO);
    outop(2, n - 120 - KO);
    {
      constexpr int wg[8] = WGAPS;
#pragma unroll
      for (int e = 0; e < 8; ++e)
        if (n - 136 == wg[e]) {
          *LDS_AT(u32x4, (e < 4 ? t1[1] + (e & 1) * 64 : t2[1] + 2048 + (e & 1) * 128) + ((e >> 1) & 1) * KSB) = kvp[4 + e];
          PIN();
        }
    }
    if (n == 152 + 3) { asm volatile("v_add_u32 %0, %1, %0" : "+v"(t1[1]) : "s"(qmv)); PIN(); }
    outop(0, n - 152 - KO);
  });
  TT(13);
  if (wid == 0) STAMP(dk, 18);
#pragma unroll
  for (int k = 16 - KO; k <= 16; ++k) outop(0, k);
  __hip_atomic_fetch_add(LDS_AT(unsigned, t3[1]), 1u, __ATOMIC_RELAXED, __HIP_MEMORY_SCOPE_WORKGROUP);
  TT(14);
  asm volatile("s_waitcnt vmcnt(4)" ::: "memory");
  signal(give ? 0u : 1u);
  TT(15);
  if (wid == 0) STAMP(dk, 19);
  }
  PIN();
  {
  const int lane = fresh_lane(), tid = wid * 64 + lane;
  LAYER_HERE;
  const int dk = LAY2 ? 6 : 2;
  const int g = (wid >> 1) & 1, hh = tmi & 1, qg = wid & 1, far = wid >> 2;
  const int head = tni * 2 + hh, qt = (tmi ^ far) * 4 + qg * 2;
  unsigned qaa = lbase + QOFF + (hh * 2 + qg) * 4096 + kpos(lane) * 16 + (g ? 0 : 2048), qab = qaa ^ 2048;
  unsigned wbase = lbase + g * 4096 + lane * 16;
  asm volatile("" : "+v"(wbase));
  auto sptr = [&](int sl) { return sl * KSB; };
  auto rd = [&](int sp, int off) { return *LDS_AT(const s16x8, wbase + sp + off); };
  int sl0 = sptr(0);
  const unsigned arr_a = lane == 0 ? lbase + FLG + 16 : lbase + DUM + lane * 4;
  auto arrive = [&]() { __hip_atomic_fetch_add(LDS_AT(unsigned, arr_a), 1u, __ATOMIC_RELAXED, __HIP_MEMORY_SCOPE_WORKGROUP); };
  unsigned arr_w = lane == 0 ? lbase + FLG + 8 + (wid >> 2) * 4 : lbase + DUM + lane * 4;
  unsigned arr_i = 1u << (8 * (wid & 3));
  auto arrive_w = [&]() { __hip_atomic_fetch_add(LDS_AT(unsigned, arr_w), arr_i, __ATOMIC_RELAXED, __HIP_MEMORY_SCOPE_WORKGROUP); };
  s16x8 sel;
#pragma unroll
  for (int e = 0; e < 8; ++e) sel[e] = ((lane & 7) == (e & 4)) ? (short)0x3F80 : (short)0;
  int sidx = 0xDDDDDDDD;
  f32x16 oa, ob, nn, sa, sb;
  f32x4 la = {0.f, 0.f, 0.f, 0.f}, lb = {0.f, 0.f, 0.f, 0.f};
#pragma unroll
  for (int e = 0; e < 16; ++e) { oa[e] = 0.f; ob[e] = 0.f; sa[e] = 0.f; sb[e] = 0.f; }
  asm volatile("" : "+v"(sidx), "+v"(sel), "+v"(oa), "+v"(ob), "+v"(la), "+v"(lb), "+v"(qaa), "+v"(qab), "+v"(arr_w), "+v"(arr_i));
  PIN();
  s16x8 qa0, qa1, qb0, qb1, k00, k01, v00, v01, v10, v11, k10, k11;
  asm volatile("" : "=v"(qa0), "=v"(qa1), "=v"(qb0), "=v"(qb1));
  u32x4 fw;
  auto first = [&]() {
    fw = lds_rd4(lbase + FLG + 16);
    asm volatile("" ::: "memory");
    k00 = rd(sl0, 0); k01 = rd(sl0, 1024);
    if (!far) { qa0 = *LDS_AT(const s16x8, qaa); qb0 = *LDS_AT(const s16x8, qab); qa1 = *LDS_AT(const s16x8, qaa + 1024); qb1 = *LDS_AT(const s16x8, qab + 1024); }
    v00 = rd(sl0, 2048); v01 = rd(sl0, 3072);
    PIN();
  };
  if (far) {
    ArgP ap_ = ap; asm volatile("" : "+s"(ap_));
    const __amdgpu_buffer_rsrc_t rq = __builtin_amdgcn_make_buffer_rsrc(reinterpret_cast<char*>(ap_->kv) + QG + (((LAY2 ? 256 : 0) + (tmi ^ 1) * 16 + tni) * 2 + qg) * 4096, 0, 4096, 0x00020000);
    const int o = kpos(lane) * 16 + (g ? 0 : 2048);
    __builtin_amdgcn_s_sleep(QPRE);
#pragma unroll 1
    for (int i = 0; i < SPIN_MAX; ++i) {
      const u32x4 a0 = __builtin_amdgcn_raw_buffer_load_b128(rq, o, 0, 16), b0 = __builtin_amdgcn_raw_buffer_load_b128(rq, o ^ 2048, 0, 16);
      const u32x4 a1 = __builtin_amdgcn_raw_buffer_load_b128(rq, o + 1024, 0, 16), b1 = __builtin_amdgcn_raw_buffer_load_b128(rq, (o ^ 2048) + 1024, 0, 16);
      qa0 = __builtin_bit_cast(s16x8, a0); qb0 = __builtin_bit_cast(s16x8, b0); qa1 = __builtin_bit_cast(s16x8, a1); qb1 = __builtin_bit_cast(s16x8, b1);
      unsigned mx = 0u;
#pragma unroll
      for (int e = 0; e < 4; ++e) mx = umax(umax(umax(mx, a0[e]), b0[e]), umax(a1[e], b1[e]));
      if (none(mx == SENT)) break;
      __builtin_amdgcn_s_sleep(QSLP);
      asm volatile("" ::: "memory");
    }
  }
  first();
  if (__builtin_amdgcn_readfirstlane(fw[0]) < 4u) {
#pragma unroll 1
    for (int i = 0; i < SPIN_MAX; ++i) {
      if (__builtin_amdgcn_readfirstlane(*LDS_AT(const volatile unsigned, lbase + FLG + 16)) >= 4u) break;
      __builtin_amdgcn_s_sleep(ASLP);
    }
    asm volatile("" ::: "memory");
    first();
  }
  const unsigned tag = __builtin_amdgcn_readfirstlane(fw[1]);
  const int idx = LAY2 ? (2047 - (int)(__builtin_amdgcn_readfirstlane(fw[3]) & 0xffffu)) & 2047 : -64;
  float ma;
  const float zoff = __uint_as_float(__builtin_amdgcn_readfirstlane(LAY2 && g == ((idx >> 6) & 1) ? 0u : 0xff800000u));
  {
    sa = MFMA32(k00, qa0, sa); sb = MFMA32(k00, qb0, sb);
    sa = MFMA32(k01, qa1, sa); sb = MFMA32(k01, qb1, sb);
    float mb = sb[0];
    ma = sa[0];
#pragma unroll
    for (int e = 1; e < 16; ++e) { ma = fmaxf(ma, sa[e]); mb = fmaxf(mb, sb[e]); }
    ma = fmaxf(ma, mb);
    const auto sw = __builtin_amdgcn_permlane32_swap(__float_as_uint(ma), __float_as_uint(ma), false, false);
    ma = fmaxf(__uint_as_float(sw[0]), __uint_as_float(sw[1]));
    f32x2 n2 = {-ma, -ma};
    asm volatile("" : "+v"(n2));
#pragma unroll
    for (int e = 0; e < 16; ++e) nn[e] = n2[e & 1];
    sa = MFMA32(k00, qa0, nn); sa = MFMA32(k01, qa1, sa);
    la[0] = lb[0] = -__uint_as_float(__float_as_uint(__builtin_amdgcn_exp2f(zoff - ma)) & 0xffff0000u);
  }
#define SB() __builtin_amdgcn_sched_barrier(0)
#define EX(i) sc[i] = __builtin_amdgcn_exp2f(sc[i])
  auto unit = [&](f32x16& sc, f32x16& sn, const s16x8& kn0, const s16x8& kn1, const s16x8& qn0, const s16x8& qn1,
                  f32x16& op, f32x4& lp, const s16x8& vp,
                  f32x16& oc, f32x4& lc, const s16x8& vc, auto&& mid) {
    lp = SMF16(sel, PHI(sn), lp); SB();
    op = SMF32(vp, PHI(sn), op); SB();
    EX(0); EX(1); EX(2); EX(3); SB();
    sn = MFMA32(kn0, qn0, nn); SB();
    EX(4); EX(5); EX(6); EX(7); SB();
    sn = MFMA32(kn1, qn1, sn); SB();
    mid();
    EX(8); EX(9); EX(10); EX(11); SB();
    lc = SMF16(sel, PLO(sc), lc); SB();
    oc = SMF32(vc, PLO(sc), oc); SB();
    EX(12); EX(13); EX(14); EX(15); SB();
    asm volatile("" : "+v"(sc));
  };
  sl0 = sptr(1);
  unsigned f0 = 0u;
  auto check = [&](unsigned need) {
    if (__builtin_expect(__builtin_amdgcn_readfirstlane(f0) < need, 0)) {
#pragma unroll 1
      for (int i = 0; i < SPIN_MAX; ++i) {
        if (__builtin_amdgcn_readfirstlane(*LDS_AT(const volatile unsigned, lbase + FLG)) >= need) break;
        __builtin_amdgcn_s_sleep(1);
      }
    }
    asm volatile("" ::: "memory");
  };
#define PAT 0x22221110
#define SAW8(i) do { if (((PAT >> (28 - 4 * (i))) & 3) != ((PAT >> ((32 - 4 * (i)) & 31)) & 3)) { __builtin_amdgcn_s_setprio((PAT >> (28 - 4 * (i))) & 3); SB(); } } while (0)
#define SAW(i) do { if (odd) SAW8(4 + (i)); else SAW8(i); } while (0)
  auto three = [&](int q, bool odd) __attribute__((always_inline)) {
    sl0 = sptr((2 * q + 1) & 7);
    SAW(0);
    unit(sa, sb, k00, k01, qb0, qb1, ob, lb, v11, oa, la, v00, [&]() { k10 = rd(sl0, 0); k11 = rd(sl0, 1024); SB(); });
    SAW(1);
    unit(sb, sa, k10, k11, qa0, qa1, oa, la, v01, ob, lb, v00, [&]() { v10 = rd(sl0, 2048); v11 = rd(sl0, 3072); SB(); });
    sl0 = sptr((2 * q + 2) & 7);
    SAW(2);
    unit(sa, sb, k10, k11, qb0, qb1, ob, lb, v01, oa, la, v10, [&]() { k00 = rd(sl0, 0); k01 = rd(sl0, 1024); SB(); });
  };
  auto fourth = [&](int st, int q, bool odd) __attribute__((always_inline)) {
    sl0 = sptr((2 * q + 2) & 7);
    SAW(3);
    unit(sb, sa, k00, k01, qa0, qa1, oa, la, v11, ob, lb, v10, [&]() {
      v00 = rd(sl0, 2048); v01 = rd(sl0, 3072);
      f0 = *LDS_AT(const volatile unsigned, lbase + FLG);
      SB();
      arrive_w();
      SB(); });
    check(st >= 14 ? 32u : 2u * st + 5u);
    SB();
  };
  SB();
  SAW8(0);
  {
    f32x16& sc = sa;
    sb = MFMA32(k00, qb0, nn); SB();
    k10 = rd(sl0, 0); k11 = rd(sl0, 1024); SB();
    sb = MFMA32(k01, qb1, sb); SB();
    EX(0); EX(1); EX(2); EX(3); EX(4); EX(5); EX(6); EX(7); EX(8); SB();
    oa = SMF32(v00, PLO(sa), oa); SB();
    EX(9); EX(10); EX(11); SB();
    la = SMF16(sel, PLO(sa), la); SB();
    EX(12); EX(13); EX(14); EX(15); SB();
    asm volatile("" : "+v"(sa));
  }
  auto given = [&]() {
    if (!far && g != hh) {
      asm volatile("s_waitcnt vmcnt(0)" ::: "memory");
      __hip_atomic_fetch_add(LDS_AT(unsigned, arr_w + 24), 256u, __ATOMIC_RELAXED, __HIP_MEMORY_SCOPE_WORKGROUP);
    }
    SB();
  };
  given();
  SAW8(1);
  unit(sb, sa, k10, k11, qa0, qa1, oa, la, v01, ob, lb, v00, [&]() { v10 = rd(sl0, 2048); v11 = rd(sl0, 3072); SB(); });
  sl0 = sptr(2);
  SB();
  SAW8(2);
  unit(sa, sb, k10, k11, qb0, qb1, ob, lb, v01, oa, la, v10, [&]() {});
  {
    f32x16& sc = sb;
    SAW8(3);
    la = SMF16(sel, PHI(sa), la); SB();
    oa = SMF32(v11, PHI(sa), oa); SB();
    EX(0); EX(1); EX(2); EX(3); EX(4); EX(5); EX(6); EX(7); EX(8); asm volatile("" : "+v"(sc)); SB();
    lb = SMF16(sel, PLO(sb), lb); SB();
    ob = SMF32(v10, PLO(sb), ob); SB();
    f0 = *LDS_AT(const volatile unsigned, lbase + FLG);
    asm volatile("" ::: "memory");
    k00 = rd(sl0, 0); k01 = rd(sl0, 1024); SB();
    EX(9); EX(10); EX(11); EX(12); EX(13); EX(14); EX(15); asm volatile("" : "+v"(sc)); SB();
    if (__builtin_amdgcn_readfirstlane(f0) < 3u) { check(3u); k00 = rd(sl0, 0); k01 = rd(sl0, 1024); }
    SB();
    v00 = rd(sl0, 2048); v01 = rd(sl0, 3072); SB(); arrive_w(); SB();
    sa = MFMA32(k00, qa0, nn); sa = MFMA32(k01, qa1, sa); asm volatile("" : "+v"(sa)); SB();
  }
  sl0 = sptr(3);
  SB();
  SAW8(4);
  unit(sa, sb, k00, k01, qb0, qb1, ob, lb, v11, oa, la, v00, [&]() {
    f0 = *LDS_AT(const volatile unsigned, lbase + FLG);
    asm volatile("" ::: "memory");
    k10 = rd(sl0, 0); k11 = rd(sl0, 1024); SB(); });
  if (__builtin_amdgcn_readfirstlane(f0) < 4u) { check(4u); k10 = rd(sl0, 0); k11 = rd(sl0, 1024); }
  SB();
  SAW8(5);
  unit(sb, sa, k10, k11, qa0, qa1, oa, la, v01, ob, lb, v00, [&]() { v10 = rd(sl0, 2048); v11 = rd(sl0, 3072); f0 = *LDS_AT(const volatile unsigned, lbase + FLG); SB(); });
  check(5u);
  sl0 = sptr(4);
  SB();
  SAW8(6);
  unit(sa, sb, k10, k11, qb0, qb1, ob, lb, v01, oa, la, v10, [&]() { k00 = rd(sl0, 0); k01 = rd(sl0, 1024); SB(); });
#pragma unroll 1
  for (int st = 2; st < 14; st += 4) {
    fourth(st - 1, 1, true); three(2, false); fourth(st, 2, false); three(3, true);
    fourth(st + 1, 3, true); three(0, false); fourth(st + 2, 0, false); three(1, true);
  }
  fourth(13, 1, true); three(2, false); fourth(14, 2, false); three(3, true);
  SAW8(7);
  unsigned vr, tk, tv;
  unsigned f4;
  auto table = [&]() {
    f4 = *LDS_AT(const volatile unsigned, lbase + FLG + 4);
    k00 = *LDS_AT(const volatile s16x8, tk); k01 = *LDS_AT(const volatile s16x8, tk + 32);
    vr = *LDS_AT(const volatile u16, tv);
  };
  unsigned xw, xm;
  u32x2 ml;
  u32x4 o2[4];
  auto take = [&]() {
    ml = *LDS_AT(const volatile u32x2, xm);
#pragma unroll
    for (int e = 0; e < 4; ++e) o2[e] = lds_rd4(xw + e * 1024);
  };
  float pzt;
  int dso;
  f32x16& sc = sb;
#define EXP() do { asm volatile("" : "+v"(sc)); SB(); } while (0)
  oa = SMF32(v11, PHI(sa), oa); SB();
  tk = lbase + TAB + 128 + hh * 64 + (lane >> 5) * 16; tv = lbase + TAB + 256 + hh * 64 + (lane & 31) * 2;
  table();
  SB();
  EX(0); EX(1); EXP();
  la = SMF16(sel, PHI(sa), la); SB();
  EX(2); EX(3); EX(4); EXP();
  xw = wbase + (qg ? QOFF : 4 * ASB) + far * 8192; xm = lbase + XML + wid * 512 + lane * 8;
  pzt = __builtin_amdgcn_exp2f(nn[0] + (zoff - 8.f));
  asm volatile("" : "+v"(xw), "+v"(xm), "+v"(pzt), "+v"(f4));
  SB();
  {
    const unsigned ph = LAY2 ? 0x79797979u : 0x80808080u;
    auto ready = [&]() { return ((__builtin_amdgcn_readfirstlane(f4) + ph) & 0x80808080u) == 0x80808080u; };
    if (__builtin_expect(!ready(), 0)) {
#pragma unroll 1
      for (int i = 0; i < SPIN_MAX; ++i) {
        __builtin_amdgcn_s_sleep(1);
        table();
        if (ready()) break;
      }
    }
    asm volatile("" ::: "memory");
  }
  SB();
  sa = MFMA32(k00, qb0, nn); asm volatile("" : "+v"(sa)); SB();
#pragma unroll
  for (int e = 0; e < 4; ++e) *LDS_AT(volatile f32x4, xw + e * 1024) = f32x4{oa[4 * e], oa[4 * e + 1], oa[4 * e + 2], oa[4 * e + 3]};
  *LDS_AT(volatile u32x2, xm) = u32x2{__float_as_uint(ma), f2word(pzt <= la[0] ? la[0] : 0.f)};
  SB();
  EX(5); EX(6); EX(7); EXP();
  sa = MFMA32(k01, qb1, sa); asm volatile("" : "+v"(sa), "+v"(vr)); SB();
  ARGS_HERE;
  EX(8); EX(9); EX(10); EXP();
  ob = SMF32(v10, PLO(sb), ob); SB();
  EX(11); EX(12); EX(13); EXP();
  lb = SMF16(sel, PLO(sb), lb); SB();
  const bool used = !LAY2 || (qt + g) * 32 + (lane & 31) != idx;
  dso = used ? (((qt + g) * 32 + head) * 1024 + lane * 8) * 2 : S_ * D_ * 2;
  xw += g ? -4096 : 4096; xm += g ? -1024 : 1024;
  asm volatile("" : "+v"(dso), "+v"(xw), "+v"(xm));
  SB();
  EX(14); EX(15); EXP();
  take();
  SB();
  lb = SMF16(sel, PHI(sb), lb); SB();
  ob = SMF32(v11, PHI(sb), ob); SB();
  __builtin_amdgcn_s_setprio(EPRIO);
  TE(0, 0); TE(1, 0); TE(2, 0); TE(3, 0); TE(7, 0);
  float lsb = lb[0];
  if (LAY2) {
    sa[0] = __builtin_amdgcn_exp2f(sa[0]);
    ob = SMF32((u32x4{lane < 32 ? vr : 0u, 0u, 0u, 0u}), PLO(sa), ob);
    lsb += __uint_as_float(__float_as_uint(sa[0]) & 0xffff0000u);
    lsb = pzt <= lsb ? lsb : 0.f;
  }
  const __amdgpu_buffer_rsrc_t rso = __builtin_amdgcn_make_buffer_rsrc(Of, 0, S_ * D_ * 2, 0x00020000);
  TE(0, 1);
  if (__builtin_amdgcn_ballot_w64(ml[1] == SENT) != 0ull) {
#pragma unroll 1
    for (int i = 0; i < SPIN_MAX; ++i) {
      __builtin_amdgcn_s_sleep(XSLP);
      if (!__builtin_amdgcn_ballot_w64(*LDS_AT(const volatile unsigned, xm + 4) == SENT)) break;
    }
    asm volatile("s_waitcnt lgkmcnt(0)" ::: "memory");
    take();
  }
  arrive();
  TE(0, 2);
  TE(0, 3); TE(1, 1);
  {
    const float m2 = __uint_as_float(ml[0]), l2 = __uint_as_float(ml[1]);
    const float mx = fmaxf(ma, m2);
    const float f1 = __builtin_amdgcn_exp2f(ma - mx), f2 = __builtin_amdgcn_exp2f(m2 - mx);
    const float inv = frcp(lsb * f1 + l2 * f2);
    const float c1 = f1 * inv, c2 = f2 * inv;
    f32x2 r[8];
#pragma unroll
    for (int e = 0; e < 8; ++e) {
      const unsigned x0 = o2[e >> 1][(e & 1) * 2], x1 = o2[e >> 1][(e & 1) * 2 + 1];
      r[e] = f32x2{__uint_as_float(x0) * c2, __uint_as_float(x1) * c2};
    }
#pragma unroll
    for (int e = 0; e < 8; ++e) r[e] = f32x2{ob[2 * e], ob[2 * e + 1]} * f32x2{c1, c1} + r[e];
    auto put = [&]() {
#pragma unroll
      for (int sx = 0; sx < 2; ++sx)
        __builtin_amdgcn_raw_buffer_store_b128(u32x4{pack2(r[4 * sx][0], r[4 * sx][1]), pack2(r[4 * sx + 1][0], r[4 * sx + 1][1]),
            pack2(r[4 * sx + 2][0], r[4 * sx + 2][1]), pack2(r[4 * sx + 3][0], r[4 * sx + 3][1])}, rso, dso + sx * 1024, 0, 16);
    };
    put();
    PIN();
    __builtin_amdgcn_s_setprio(0);
    PIN();
    f32x2 chk = {lsb - __builtin_amdgcn_fmed3f(lsb, 8.6736174e-19f, 1.2676506e30f), l2 - __builtin_amdgcn_fmed3f(l2, 8.6736174e-19f, 1.2676506e30f)};
#pragma unroll
    for (int e = 0; e < 8; ++e) chk = r[e] * f32x2{0.f, 0.f} + chk;
    if (__builtin_expect(__builtin_amdgcn_ballot_w64(used && !(chk[0] + chk[1] == 0.f)) != 0ull, 0)) {
      float l;
      attn_safe(KV, head * 64, qb0, qb1, ob, l, LAY2 ? head * 64 + (idx >> 5) : -1, idx & 31, lbase + TAB + 128 + hh * 64);
      const float inv2 = 1.0f / l;
#pragma unroll
      for (int e = 0; e < 8; ++e) r[e] = f32x2{ob[2 * e] * inv2, ob[2 * e + 1] * inv2};
      asm volatile("s_waitcnt vmcnt(0)" ::: "memory");
      put();
    }
    unsigned pp = 0u;
    if (LAY2) {
      const unsigned ua = lbase + UGL + hh * 128 + (lane >> 5) * 16;
      f32x2 du = {0.f, 0.f}, dn = {0.f, 0.f};
#pragma unroll
      for (int q = 0; q < 4; ++q) {
        const f32x4 uu = *LDS_AT(const f32x4, ua + q * 32), gg = *LDS_AT(const f32x4, ua + 256 + q * 32);
        du = r[2 * q] * f32x2{uu[0], uu[1]} + du; du = r[2 * q + 1] * f32x2{uu[2], uu[3]} + du;
        const f32x2 t0 = r[2 * q] * f32x2{gg[0], gg[1]}, t1 = r[2 * q + 1] * f32x2{gg[2], gg[3]};
        dn = t0 * t0 + dn; dn = t1 * t1 + dn;
      }
      const auto a = __builtin_amdgcn_permlane32_swap(__float_as_uint(du[0] + du[1]), __float_as_uint(du[0] + du[1]), false, false);
      const auto c = __builtin_amdgcn_permlane32_swap(__float_as_uint(dn[0] + dn[1]), __float_as_uint(dn[0] + dn[1]), false, false);
      pp = f2word(__uint_as_float(pack2(__uint_as_float(a[0]) + __uint_as_float(a[1]), __uint_as_float(c[0]) + __uint_as_float(c[1]))));
    }
    __builtin_amdgcn_raw_buffer_store_b32(pp, __builtin_amdgcn_make_buffer_rsrc(hand + 2 * HAND_N + PP_O, 0, LAY2 ? H_ * S_ * 4 : 0, 0x00020000),
                                          lane < 32 ? (head * S_ + (qt + g) * 32 + lane) * 4 : H_ * S_ * 4, 0, 16);
    PIN();
  }
  TE(1, 2);
  u32x4 xres[2];
  oproj_tail(LAY2, tid, lbase, lbase + FLG, lbase + SPM + 144, omi, oni, idx, gran + GR_O + ((LAY2 ? 16 : 0) + (tmi ^ far)) * (64 * RO) + head,
             gran + GR_X + omi * (128 * RX) + oni * 4, tag, Of, xout, A, w_r, stats_out, stats, hand, xres TEPASS);
  if (LAY2) {
    const int row = omi * 128 + (wid & 3) * 32 + (wid >> 2) * 16 + (lane >> 3), col = oni * 64 + (lane & 7) * 8;
    unsigned md[2], best = 0u;
    const __amdgpu_buffer_rsrc_t rsk = __builtin_amdgcn_make_buffer_rsrc(hand + HAND_N, 0, (HAND_N + PS_N) * 4, 0x00020000);
    f32x2 xf[2][4];
#pragma unroll
    for (int p = 0; p < 2; ++p)
#pragma unroll
      for (int e = 0; e < 4; ++e) {
        xf[p][e] = f32x2{__uint_as_float(xres[p][e] << 16), __uint_as_float(xres[p][e] & 0xffff0000u)};
        asm volatile("" : "+v"(xf[p][e]));
      }
    LOGDEF;
    LOG();
    const bool rtr = (oni & 7) == 7;
    {
      const int rl = (wid & 3) * 32 + (wid >> 2) * 16 + (lane & 15), rr = omi * 128 + rl;
      int src = (HAND_N + ((omi * 16 + (lane >> 4) * 4) * 128 + rl) * 4) * 4;
      int ko = rtr && (wid >> 2) == (oni >> 3) && lane < NREP ? lane * 1024 + (rr >> 4) * 8 : (HAND_N + PS_N) * 4;
      int bp = (lane >> 3) * 4;
      const float wsum = *LDS_AT(const volatile float, lbase + SPM + 132);
      asm volatile("" : "+v"(src), "+v"(ko), "+v"(bp));
      u32x4 v[4];
      __builtin_amdgcn_s_sleep(MPRE);
#pragma unroll 1
      for (int spin = 0; spin < SPIN_MAX; ++spin) {
        LOG();
#pragma unroll
        for (int i = 0; i < 4; ++i) v[i] = __builtin_amdgcn_raw_buffer_load_b128(rsk, src + (i & 1) * 2048, (i >> 1) * 4096, 16);
        unsigned mx = 0u;
#pragma unroll
        for (int i = 0; i < 4; ++i) mx = umax(umax(umax(mx, v[i][0]), v[i][1]), v[i][2]);
        if (none(mx == SENT)) break;
        __builtin_amdgcn_s_sleep(MSLP);
        asm volatile("" ::: "memory");
      }
      LOGAT(12);
      if (rtr) __builtin_amdgcn_s_setprio(3); else __builtin_amdgcn_s_setprio(LNPRIO);
      double sm[3];
#pragma unroll
      for (int c = 0; c < 3; ++c) {
        const double a = ((double)__uint_as_float(v[0][c]) + (double)__uint_as_float(v[1][c])) + ((double)__uint_as_float(v[2][c]) + (double)__uint_as_float(v[3][c]));
        u32x2 q = __builtin_bit_cast(u32x2, a);
        const auto lo = __builtin_amdgcn_permlane32_swap(q[0], q[0], false, false), hi = __builtin_amdgcn_permlane32_swap(q[1], q[1], false, false);
        q = __builtin_bit_cast(u32x2, __builtin_bit_cast(double, u32x2{lo[0], hi[0]}) + __builtin_bit_cast(double, u32x2{lo[1], hi[1]}));
        const auto l2 = __builtin_amdgcn_permlane16_swap(q[0], q[0], false, false), h2 = __builtin_amdgcn_permlane16_swap(q[1], q[1], false, false);
        sm[c] = __builtin_bit_cast(double, u32x2{l2[0], h2[0]}) + __builtin_bit_cast(double, u32x2{l2[1], h2[1]});
      }
      float mean, var, mb, den;
      ln_from_sums(sm[0], sm[1], mean, var, mb, den);
      const unsigned mdr = f2word(__uint_as_float((__float_as_uint(mb) >> 16) | (__float_as_uint(den) & 0xffff0000u)));
      md[0] = __builtin_amdgcn_ds_bpermute(bp, mdr); md[1] = __builtin_amdgcn_ds_bpermute(bp + 32, mdr);
      if (rtr) {
        const float r = (float)fma(-(double)mb, (double)wsum, sm[2]) * frcp(den);
        const unsigned rb = __float_as_uint(r) + 0x8000u;
        unsigned rkey = (rb ^ ((unsigned)((int)rb >> 31) | 0x80000000u)) & 0xfffff800u;
        if (r != r) rkey = 0u;
        const unsigned top = wave_umax(rkey | (unsigned)(2047 - rr));
        const unsigned mdt = __builtin_amdgcn_readlane(mdr, (2047 - (int)(top & 0xffffu)) & 15);
        __builtin_amdgcn_raw_buffer_store_b64(u32x2{top, mdt}, rsk, ko, 0, 16);
        PIN();
        __builtin_amdgcn_s_setprio(LNPRIO);
        __hip_atomic_fetch_add(LDS_AT(unsigned, lane == 0 ? lbase + SPM + 128 : lbase + DUM + lane * 4), 1u, __ATOMIC_RELAXED, __HIP_MEMORY_SCOPE_WORKGROUP);
        if (wid == 2 || wid == 5) STAMP(8, 6);
      }
      LOGAT(13);
    }
    auto keys = [&](const u32x4& v) __attribute__((always_inline)) {
      if (none(umax(umax(v[0], v[1]), umax(v[2], v[3])) == SENT)) {
        best = wave_umax(umax(v[0], v[2])) | 0x8000u;
        *LDS_AT(volatile u32x2, lbase + FLG + 40) = u32x2{best, (unsigned)__builtin_amdgcn_readlane(v[0] > v[2] ? v[1] : v[3], ((2047 - (int)(best & 0x7ffu)) >> 5) & 63)};
        STAMP(8, 11);
      }
    };
    auto ln_rows = [&]() __attribute__((always_inline)) {
#pragma unroll
      for (int p = 0; p < 2; ++p) {
        const float mb_ = __uint_as_float(md[p] << 16), rin = frcp(__uint_as_float(md[p] & 0xffff0000u));
#pragma unroll
        for (int e = 0; e < 4; ++e) {
          const f32x2 h = (xf[p][e] - f32x2{mb_, mb_}) * f32x2{rin, rin} + xf[p][e];
          xres[p][e] = pack2(h[0], h[1]);
        }
      }
      asm volatile("" : "+v"(xres[0]), "+v"(xres[1]));
    };
    if (wid == 0) STAMP(8, 12);
    ln_rows();
    LOGAT(14);
    const __amdgpu_buffer_rsrc_t rsu = __builtin_amdgcn_make_buffer_rsrc(out, 0, S_ * D_ * 2, 0x00020000);
    unsigned fr1 = SENT;
#pragma unroll 1
    for (int i = 0; i < 4 * SPIN_MAX && fr1 == SENT; ++i) fr1 = __builtin_amdgcn_readfirstlane(*LDS_AT(const volatile unsigned, lbase + SPM + 144));
    const int fr2 = (fr1 >> 16) & 0x7ffu;
    fr1 &= 0x7ffu;
    unsigned tmx = __builtin_amdgcn_readfirstlane(*LDS_AT(const volatile unsigned, lbase + SPM + 136)) + KDT;
    if (tmx - (unsigned)__builtin_amdgcn_s_memrealtime() > 1024u) tmx = 0u;
    auto due = [&]() { return tmx == 0u || (int)((unsigned)__builtin_amdgcn_s_memrealtime() - tmx) >= 0; };
    lk_set(15);
    const bool held = !none((row | 8) == ((int)fr1 | 8) || (row | 8) == (fr2 | 8));
#pragma unroll
    for (int p = 0; p < 2; ++p) __builtin_amdgcn_raw_buffer_store_b128(xres[p], rsu, row + 8 * p != (int)fr1 && row + 8 * p != fr2 ? ((row + 8 * p) * D_ + col) * 2 : S_ * D_ * 2, 0, OAUX);
    PIN();
    if (wid == 6) {
      if (rtr) {
#pragma unroll 1
        for (int i = 0; i < SPIN_MAX; ++i) {
          if (__builtin_amdgcn_readfirstlane(*LDS_AT(const volatile unsigned, lbase + SPM + 128)) == 7u) break;
          __builtin_amdgcn_s_sleep(1);
        }
        asm volatile("" ::: "memory");
      }
      while (!due()) __builtin_amdgcn_s_sleep(1);
    }
#pragma unroll 1
    for (int i = 0; i < 4 * SPIN_MAX && !best; ++i) {
      if (wid == 6) {
        LOG();
        keys(__builtin_amdgcn_raw_buffer_load_b128(rsk, (bid % NREP) * 1024 + lane * 16, 0, 16));
        if (!best) __builtin_amdgcn_s_sleep(KSLP);
      } else {
        best = __builtin_amdgcn_readfirstlane(*LDS_AT(const volatile unsigned, lbase + FLG + 40));
        if (!best) __builtin_amdgcn_s_sleep(1);
      }
      asm volatile("" ::: "memory");
    }
    const int idx2 = 2047 - (int)(best & 0x7ffu);
    if (held) {
#pragma unroll
      for (int p = 0; p < 2; ++p) {
        const int r = row + 8 * p;
        __builtin_amdgcn_raw_buffer_store_b128(xres[p], rsu, (r == (int)fr1 || r == fr2) && r != idx2 ? (r * D_ + col) * 2 : S_ * D_ * 2, 0, OAUX);
      }
    }
    if (__builtin_expect(idx2 != (int)fr1 && idx2 != fr2, 0) && !none((row | 8) == (idx2 | 8))) {
#pragma unroll
      for (int p = 0; p < 2; ++p)
        __builtin_amdgcn_raw_buffer_store_b128(u32x4{pack2(xf[p][0][0], xf[p][0][1]), pack2(xf[p][1][0], xf[p][1][1]), pack2(xf[p][2][0], xf[p][2][1]), pack2(xf[p][3][0], xf[p][3][1])},
            __builtin_amdgcn_make_buffer_rsrc(xout, 0, 3 * D_ * 2, 0x00020000), row + 8 * p == idx2 ? (2 * D_ + col) * 2 : S_ * D_ * 2, 0, 16);
      asm volatile("s_waitcnt vmcnt(0)" ::: "memory");
      if (lane == 0) st_ag(gran + GR_F + oni * 32, tag);
    }
  }
  }
}
struct Args2 { Args l[2]; };
extern "C" __global__ __launch_bounds__(768, 3) void layers_kernel(const Args2 args) {
  __shared__ __attribute__((aligned(128))) char smem[163840];
  const unsigned long long t_entry = 0;
  const int tid = threadIdx.x, bid = blockIdx.x;
  int wid = __builtin_amdgcn_readfirstlane(tid >> 6);
  asm volatile("" : "+s"(wid));
  const ArgP ap = (ArgP)__builtin_amdgcn_kernarg_segment_ptr();
  layer_body(wid, bid, (unsigned)(uintptr_t)smem, ap, t_entry);
  LAYER_HERE;
  if (!LAY2) {
    asm volatile(AGAIN :: "{s[0:1]}"(ap + 1), "{s2}"(bid), "{v0}"(wid * 64) : "s90", "s91", "scc", "memory");
    __builtin_unreachable();
  }
}

extern "C" __global__ void end_marker_kernel() {}

extern "C" void kernel_launch(void* const* d_in, const int* in_sizes, int n_in, void* d_out, int out_size,
                              void* d_ws, size_t ws_size, hipStream_t stream) {
  const u16* x0 = (const u16*)d_in[0];
  const u16* wqkv = (const u16*)d_in[1];
  const u16* wo = (const u16*)d_in[2];
  const u16* w_r = (const u16*)d_in[4];
  const u16* wg = (const u16*)d_in[5];
  const u16* wu = (const u16*)d_in[6];
  const u16* wd = (const u16*)d_in[7];
  u16* out = (u16*)d_out;

  char* ws = (char*)d_ws;
  const size_t MB4 = (size_t)S_ * D_ * 2;
  u16* kv = (u16*)(ws);
  u16* o = (u16*)(ws + 2 * MB4);
  u16* x1 = (u16*)(ws + 3 * MB4);
  unsigned* gran = (unsigned*)(ws + 4 * MB4);
  float* csf = (float*)(gran + GR_F + 16 * 32);
  double* stats = (double*)(csf + 8192);
  unsigned* hand = (unsigned*)(stats + 2 * STAT_N);
  static_assert((GR_F + 16 * 32) % 32 == 0 && (size_t)(GR_F + 16 * 32 + 8192 + 4 * STAT_N + 2 * HAND_N + PS_N) * 4 <= 6u << 20, "");
  u16* x3 = (u16*)(hand + 2 * HAND_N) - 3 * D_;
  u16* wc = (u16*)((((uintptr_t)ws + 4 * MB4 + (6u << 20) + 2047) & ~(uintptr_t)2047) + (((uintptr_t)wqkv + 128) & 2047));
  Args2 a;
  for (int l = 0; l < 2; ++l) {
    Args& r = a.l[l];
    r.lay = l; r.a = l ? x1 : x0; r.kv = kv; r.o = o; r.xo = l ? x3 : x1; r.so = l ? stats + STAT_N : stats;
    r.W = wqkv; r.wc = wc; r.wg = wg; r.wu = wu; r.wd = wd; r.wo = wo; r.w_r = w_r; r.gran = gran; r.csf = csf; r.stats = stats; r.hand = hand; r.out = out;
  }
  layers_kernel<<<256, 768, 0, stream>>>(a);
}
```

```cpp
#include <hip/hip_runtime.h>
#include <hip/hip_bf16.h>
#include <stdint.h>
#include <type_traits>
#include <utility>

typedef unsigned short u16;
typedef short s16x4 __attribute__((ext_vector_type(4)));
typedef short s16x8 __attribute__((ext_vector_type(8)));
typedef float f32x16 __attribute__((ext_vector_type(16)));
typedef float f32x4 __attribute__((ext_vector_type(4)));
typedef unsigned int u32x4 __attribute__((ext_vector_type(4)));
typedef unsigned int u32x2 __attribute__((ext_vector_type(2)));
typedef __bf16 bf16x8 __attribute__((ext_vector_type(8)));
typedef __bf16 bf16x16 __attribute__((ext_vector_type(16)));

__device__ __forceinline__ int fresh_lane() {
  int l;
  asm volatile("v_mbcnt_lo_u32_b32 %0, -1, 0\n\tv_mbcnt_hi_u32_b32 %0, -1, %0" : "=v"(l));
  return l;
}
#define STAMP(k, i)
#define STAMPV(k, i, v)
#define LOGDEF
#define LOG()
#define LOGAT(k)
#define lk_set(k)
#define LOGOUT(slot)
#define TE(grp, k)
#define TEARG
#define TEPASS
constexpr int S_ = 2048, D_ = 1024, H_ = 32, F_ = 3584;

__device__ __forceinline__ int kpos(int r) { return (r & ~12) | ((r & 4) << 1) | ((r & 8) >> 1); }
__device__ __forceinline__ float bf2f(u16 v) { return __uint_as_float(((unsigned)v) << 16); }
__device__ __forceinline__ u16 f2bf(float f) {
  __hip_bfloat16 b = __float2bfloat16(f);
  return *reinterpret_cast<u16*>(&b);
}
typedef float f32x2 __attribute__((ext_vector_type(2)));
typedef __bf16 bf16x2 __attribute__((ext_vector_type(2)));
__device__ __forceinline__ unsigned pack2(float lo, float hi) {
  f32x2 v = {lo, hi};
  return __builtin_bit_cast(unsigned, __builtin_convertvector(v, bf16x2));
}
__device__ __forceinline__ float rbf(float f) { return __uint_as_float(pack2(f, f) << 16); }
__device__ __forceinline__ void rbf2(float& a, float& b) {
  const unsigned p = pack2(a, b);
  a = __uint_as_float(p << 16); b = __uint_as_float(p & 0xffff0000u);
}

__device__ __forceinline__ float fadd(float a, float b) { float r = a + b; asm("" : "+v"(r)); return r; }
template <int CTRL, int RMASK>
__device__ __forceinline__ float dpp_add(float v) {
  const int t = __builtin_amdgcn_update_dpp(0, __float_as_int(v), CTRL, RMASK, 0xf, false);
  return v + __int_as_float(t);
}
__device__ __forceinline__ float wave_sum(float v) {
  v = dpp_add<0x111, 0xf>(v);
  v = dpp_add<0x112, 0xf>(v);
  v = dpp_add<0x114, 0xf>(v);
  v = dpp_add<0x118, 0xf>(v);
  v = dpp_add<0x142, 0xa>(v);
  v = dpp_add<0x143, 0xc>(v);
  return __int_as_float(__builtin_amdgcn_readlane(__float_as_int(v), 63));
}

template <int CTRL, int RMASK>
__device__ __forceinline__ unsigned dpp_umax(unsigned v) {
  const unsigned t = (unsigned)__builtin_amdgcn_update_dpp(0, (int)v, CTRL, RMASK, 0xf, false);
  return v > t ? v : t;
}
__device__ __forceinline__ unsigned wave_umax(unsigned v) {
  v = dpp_umax<0x111, 0xf>(v); v = dpp_umax<0x112, 0xf>(v); v = dpp_umax<0x114, 0xf>(v); v = dpp_umax<0x118, 0xf>(v);
  v = dpp_umax<0x142, 0xa>(v); v = dpp_umax<0x143, 0xc>(v);
  return (unsigned)__builtin_amdgcn_readlane((int)v, 63);
}

#define NREP 32
constexpr unsigned SENT = 0xFFFFFFFFu;
constexpr int GRAN_N = 256 * NREP;
constexpr int Y_N = F_ / 16 * D_;
constexpr int X2_N = D_ / 2;
constexpr int XS_N = 128;
constexpr int P4_N = 3 * D_ * 16;
constexpr int PA_N = H_ * 8 * 64;
constexpr int HAND_N = GRAN_N + Y_N + X2_N + XS_N + P4_N + PA_N;
constexpr int MD_O = 16 * 16 * 128 * 4;
constexpr int PK_O = MD_O;
constexpr int PP_O = PK_O + 128 * NREP;
constexpr int Y3_O = PP_O + H_ * S_;
constexpr int PS_N = Y3_O + Y_N;
constexpr int GR_ROW = 64, GR_COL = GR_ROW + 4 * S_, GR_KV = GR_COL + 16 * 16 * 192;
#define RO 8
#define RK 16
#define RX 1
constexpr int GR_O = GR_KV + 2 * 16 * 32 * RK, GR_X = GR_O + 2 * 16 * 64 * RO, GR_N = GR_X + 16 * 128 * RX;
constexpr unsigned QG = 30u << 20;
constexpr int GR_F = GR_N + 256 * 64;
constexpr int STAT_N = 3 * S_;

__device__ __forceinline__ void st_ag(unsigned* p, unsigned v) { __hip_atomic_store(p, v, __ATOMIC_RELAXED, __HIP_MEMORY_SCOPE_AGENT); }
__device__ __forceinline__ unsigned ld_ag(const unsigned* p) { return __hip_atomic_load(p, __ATOMIC_RELAXED, __HIP_MEMORY_SCOPE_AGENT); }
__device__ __forceinline__ double ld_ag(const double* p) { return __hip_atomic_load(p, __ATOMIC_RELAXED, __HIP_MEMORY_SCOPE_AGENT); }
__device__ __forceinline__ unsigned f2word(float f) { const unsigned u = __float_as_uint(f); return u < 0xFFFFFFFEu ? u : 0xFFFFFFFEu; }
constexpr int SPIN_MAX = 1 << 18;
__device__ __forceinline__ unsigned umax(unsigned a, unsigned b) { return a > b ? a : b; }
__device__ __forceinline__ bool none(bool p) { return __builtin_amdgcn_ballot_w64(p) == 0ull; }

__device__ __forceinline__ void ln_from_sums(double s1, double s2, float& mean, float& var, float& mb, float& den) {
  const double md = s1 * (1.0 / 1024.0);
  const double vd = fma(-md, md, s2 * (1.0 / 1024.0));
  mean = (float)md; var = fmaxf((float)vd, 0.f);
  mb = rbf(mean);
  den = rbf(__builtin_amdgcn_sqrtf(rbf(rbf(var) + 1e-5f)));
}
__device__ __forceinline__ float frcp(float x) { return __builtin_amdgcn_rcpf(x); }
__device__ __forceinline__ void ln_apply2(float& a, float& b, float mb, float den, float rin) {
  a -= mb; b -= mb;
  rbf2(a, b);
  float qa = a * rin, qb = b * rin;
  qa = fmaf(fmaf(-qa, den, a), rin, qa);
  qb = fmaf(fmaf(-qb, den, b), rin, qb);
  rbf2(qa, qb);
  a = qa; b = qb;
}

#define CODE_ADDR(SYM, out64)                                                                                          \
  do {                                                                                                                 \
    unsigned lo_, hi_;                                                                                                 \
    asm volatile("s_getpc_b64 s[90:91]\n\ts_add_u32 s90, s90, " SYM "@rel32@lo+4\n\ts_addc_u32 s91, s91, " SYM          \
                 "@rel32@hi+12\n\ts_mov_b32 %0, s90\n\ts_mov_b32 %1, s91"                                             \
                 : "=s"(lo_), "=s"(hi_) :: "s90", "s91", "scc");                                                        \
    out64 = ((unsigned long long)hi_ << 32) | lo_;                                                                     \
  } while (0)

#define QNX 4
constexpr int QMX = 32 / QNX, QNG = 16 / QNX, QMG = 8 / QNG;

__device__ __forceinline__ unsigned touch(const void* p) { return *reinterpret_cast<const unsigned*>(p); }

__device__ __forceinline__ unsigned touch_code(unsigned long long lo, unsigned long long hi, int pc, int lane) {
  const unsigned long long p = lo + (unsigned)(pc * 8192 + lane * 128);
  return p + 4 <= hi ? *reinterpret_cast<__attribute__((address_space(1))) const unsigned*>(p) : 0u;
}

template <bool SC = false>
__device__ __forceinline__ void glds16s(const void* sbase, unsigned voff, unsigned lds_dst) {
  unsigned keep;
  if (SC)
    asm volatile("s_mov_b32 %0, m0\n\ts_mov_b32 m0, %3\n\ts_nop 2\n\tglobal_load_lds_dwordx4 %1, %2 sc1\n\ts_mov_b32 m0, %0"
                 : "=&s"(keep) : "v"(voff), "s"(sbase), "s"(lds_dst) : "memory");
  else
  asm volatile("s_mov_b32 %0, m0\n\ts_mov_b32 m0, %3\n\ts_nop 2\n\tglobal_load_lds_dwordx4 %1, %2\n\ts_mov_b32 m0, %0"
               : "=&s"(keep) : "v"(voff), "s"(sbase), "s"(lds_dst) : "memory");
}
template <bool SC>
__device__ __forceinline__ void glds16x4(const void* sbase, unsigned v0, unsigned v1, unsigned v2, unsigned v3, unsigned lds_dst) {
  unsigned keep;
  if (SC)
    asm volatile("s_mov_b32 %0, m0\n\ts_mov_b32 m0, %6\n\ts_nop 2\n\tglobal_load_lds_dwordx4 %1, %5 sc1\n\tglobal_load_lds_dwordx4 %2, %5 offset:1024 sc1\n\t"
                 "global_load_lds_dwordx4 %3, %5 offset:2048 sc1\n\tglobal_load_lds_dwordx4 %4, %5 offset:3072 sc1\n\ts_mov_b32 m0, %0"
                 : "=&s"(keep) : "v"(v0), "v"(v1), "v"(v2), "v"(v3), "s"(sbase), "s"(__builtin_amdgcn_readfirstlane(lds_dst)) : "memory");
  else
    asm volatile("s_mov_b32 %0, m0\n\ts_mov_b32 m0, %6\n\ts_nop 2\n\tglobal_load_lds_dwordx4 %1, %5\n\tglobal_load_lds_dwordx4 %2, %5 offset:1024\n\t"
                 "global_load_lds_dwordx4 %3, %5 offset:2048\n\tglobal_load_lds_dwordx4 %4, %5 offset:3072\n\ts_mov_b32 m0, %0"
                 : "=&s"(keep) : "v"(v0), "v"(v1), "v"(v2), "v"(v3), "s"(sbase), "s"(__builtin_amdgcn_readfirstlane(lds_dst)) : "memory");
}
constexpr float QSCALE = 0.17677669529663687f * 1.4426950408889634f;

__device__ __forceinline__ unsigned min4(u32x4 v) { const unsigned a = v[0] < v[1] ? v[0] : v[1], b = v[2] < v[3] ? v[2] : v[3]; return a < b ? a : b; }
#define LDS_AT(T, a) ((__attribute__((address_space(3))) T*)(uintptr_t)(a))
__device__ __forceinline__ u32x4 lds_rd4(unsigned a) { return *LDS_AT(const volatile u32x4, a); }
__device__ __forceinline__ void lds_wr(unsigned a, unsigned v) { *LDS_AT(volatile unsigned, a) = v; }
__device__ __forceinline__ void split3(float v, unsigned& p0, unsigned& p1, unsigned& p2) {
  p0 = pack2(v, v) & 0xffffu; v -= __uint_as_float(p0 << 16);
  p1 = pack2(v, v) & 0xffffu; v -= __uint_as_float(p1 << 16);
  p2 = pack2(v, v) & 0xffffu;
}

#define PIN() __builtin_amdgcn_sched_barrier(0)
template <int B, class F, int... I>
__device__ __forceinline__ void static_for_(F&& f, std::integer_sequence<int, I...>) { (f(std::integral_constant<int, B + I>{}), ...); }
template <int B, int E, class F>
__device__ __forceinline__ void static_for(F&& f) { static_for_<B>(f, std::make_integer_sequence<int, E - B>{}); }

#define TPRE 60
#define TPSL 0
#define FCPRE 44
#define FCSLP 2
#define SIGAT 24
#define XPRIO 1
#define ZPRIO 3
#define OPD 8
#define LAUX 16
#define CSLP 2
#define EPRIO 3
#define OPRIO 1
#define XSLP 1
#define CSL1 8
#define XAUX 16
__device__ __forceinline__ void oproj_tail(const bool RES2, const int tid, const unsigned lbase, const unsigned flg, const unsigned fcm, const int tmi, const int tni, const int idx,
                                           unsigned* osig, unsigned* xsig, const unsigned tag,
                                           const u16* A, u16* out,
                                           const u16* res, const u16* __restrict__ w_r, double* stats,
                                           const double* stats_in, unsigned* hand, u32x4 (&xres)[2] TEARG) {
  constexpr int ldw = D_, PD = OPD;
  const int lane = tid & 63, wid = __builtin_amdgcn_readfirstlane(tid >> 6);
  const int m0 = tmi * 128, n0 = tni * 64;
  const int dk = RES2 ? 7 : 3;
  if (wid == 0) STAMP(dk, 0);
  TE(1, 3);
  asm volatile("" : "+s"(A) :: "memory");
  PIN();
  const int row = m0 + (wid & 3) * 32 + (wid >> 2) * 16 + (lane >> 3), col = n0 + (lane & 7) * 8;
  const unsigned tb = lbase + 147456;
  typedef __attribute__((address_space(1))) double* gdp;
  typedef __attribute__((address_space(1))) unsigned* gup;
  unsigned tr = tb + (row - m0) * 128 + (((lane ^ (lane >> 3)) & 7) << 4), oad;
  {
    oad = lane == 0 ? flg + 24 : flg + 64 + lane * 4;
    gup ps = (gup)(osig + lane * 64);
    asm volatile("" : "+v"(oad), "+v"(ps));
    asm volatile("s_waitcnt vmcnt(1)" ::: "memory");
    TE(2, 1);
    const unsigned old = __hip_atomic_fetch_add(LDS_AT(unsigned, oad), wid < 4 ? 0x101u : 0x10001u, __ATOMIC_RELAXED, __HIP_MEMORY_SCOPE_WORKGROUP);
    if (((__builtin_amdgcn_readfirstlane(old) >> (wid < 4 ? 8 : 16)) & 255u) == 3u && lane < RO) __hip_atomic_store(ps, tag, __ATOMIC_RELAXED, __HIP_MEMORY_SCOPE_AGENT);
    PIN();
    TE(2, 2);
    if (wid == 0) STAMP(dk, 12);
  }
  auto announce = [&]() __attribute__((always_inline)) {
    asm volatile("" ::: "memory");
    PIN();
    __builtin_amdgcn_s_barrier();
    PIN();
    if (wid == 0) STAMP(dk, 1);
    TE(2, 3); TE(4, 0); TE(7, 1);
  };
  if (wid < 4) {
    const __amdgpu_buffer_rsrc_t rsa = __builtin_amdgcn_make_buffer_rsrc(const_cast<u16*>(A), 0, S_ * D_ * 2, 0x00020000);
    int sa0 = (tmi * 4 + wid) * 65536, va0 = (lane >> 4) * 512 + (lane & 15) * 16;
    asm volatile("" : "+v"(va0));
    asm volatile("" : "+s"(sa0));
    auto lda = [&](int i, int h) { return __builtin_amdgcn_raw_buffer_load_b128(rsa, va0 + (i & 1) * 2048 + h * 256, sa0 + (i >> 1) * 4096, LAUX); };
    unsigned wad[4], tw[4];
    f32x4 acc[2][4];
    const int k = (lane >> 5) * 16 + ((lane >> 2) & 7);
#pragma unroll
    for (int j = 0; j < 4; ++j) {
      wad[j] = lbase + k * 128 + (((2 * j) ^ (k & 6)) << 4) + 8 * (lane & 3);
      tw[j] = tb + wid * 4096 + (lane & 15) * 128 + (((2 * j + (lane >> 5)) ^ (lane & 7)) << 4) + ((lane >> 4) & 1) * 8;
      asm volatile("" : "+v"(wad[j]), "+v"(tw[j]));
    }
    auto ldw_ = [&](int i, int j) {
      const unsigned a = wad[j] + (i & 3) * 4096;
      s16x4 lo = __builtin_amdgcn_ds_read_tr16_b64_v4i16(LDS_AT(s16x4, a));
      s16x4 h4 = __builtin_amdgcn_ds_read_tr16_b64_v4i16(LDS_AT(s16x4, a + 1024));
      return (s16x8)__builtin_shufflevector(lo, h4, 0, 1, 2, 3, 4, 5, 6, 7);
    };
    u32x4 id[2];
#pragma unroll
    for (int j = 0; j < 2; ++j) {
#pragma unroll
      for (int e = 0; e < 4; ++e) id[j][e] = (lane >> 4) == 2 * j + ((lane >> 3) & 1) && ((lane & 7) >> 1) == e ? ((lane & 1) ? 0x3f800000u : 0x3f80u) : 0u;
      asm volatile("" : "+v"(id[j]));
    }
    unsigned rad = tb + wid * 4096 + (lane & 15) * 128 + (((lane >> 4) ^ (lane & 7)) << 4), rfa = flg + 36 + wid;
    asm volatile("" : "+v"(rad), "+v"(rfa));
    u32x4 rb[2][2];
    u32x4 ow[2];
#pragma unroll
    for (int j = 0; j < 4; ++j) {
      const u32x2 v = *reinterpret_cast<const u32x2*>(w_r + n0 + j * 16 + (lane >> 4) * 4);
#pragma unroll
      for (int e = 0; e < 2; ++e) ow[j >> 1][(j & 1) * 2 + e] = (lane & 8) ? v[e] : 0x3f803f80u;
    }
    gdp pst = (gdp)(stats + (lane >> 4) % 3 * S_ + m0 + wid * 32 + (lane & 15));
    gup pps = (gup)(hand + 2 * HAND_N + ((tmi * 16 + tni) * 128 + wid * 32 + (lane & 15)) * 4 + (lane >> 4));
    int xo = (row * ldw + col) * 2;
    asm volatile("" : "+v"(tr), "+v"(pst), "+v"(pps), "+v"(xo), "+v"(ow[0]), "+v"(ow[1]));
    announce();
    u32x4 af[PD][2];
    s16x8 wf[2][4];
#pragma unroll
    for (int i = 0; i < PD; i += 2) { af[i][0] = lda(i, 0); af[i + 1][0] = lda(i + 1, 0); af[i][1] = lda(i, 1); af[i + 1][1] = lda(i + 1, 1); }
    PIN();
    __builtin_amdgcn_s_setprio(OPRIO);
#pragma unroll
    for (int q = 0; q < 4; ++q) rb[q >> 1][q & 1] = lds_rd4((rad ^ ((q & 1) * 64)) + (q >> 1) * 2048);
#pragma unroll
    for (int j = 0; j < 4; ++j) wf[j & 1][j >> 1] = ldw_(j & 1, j >> 1);
    PIN();
#pragma unroll
    for (int j = 0; j < 4; ++j)
#pragma unroll
      for (int h = 0; h < 2; ++h) { asm volatile("v_mfma_f32_16x16x32_bf16 %0, %1, %2, 0" : "=&v"(acc[h][j]) : "v"(id[j & 1]), "v"(rb[h][j >> 1])); PIN(); }
#pragma unroll 1
    for (int spin = 0; spin < SPIN_MAX; ++spin)
      if (__builtin_amdgcn_readfirstlane(*LDS_AT(const volatile unsigned, flg)) >= 40u) break;
    asm volatile("" : "+v"(va0) :: "memory");
    PIN();
    s16x4 wlo;
    static_for<0, 16>([&](auto ic) __attribute__((always_inline)) {
      constexpr int i = 2 * decltype(ic)::value;
      if (i == SIGAT) { __hip_atomic_fetch_add(LDS_AT(unsigned, oad + 4), 1u, __ATOMIC_RELAXED, __HIP_MEMORY_SCOPE_WORKGROUP); PIN(); }
#pragma unroll
      for (int g = 0; g < 16; ++g) {
        const int e = g & 1, j = g >> 2, h = (g >> 1) & 1, jn = (j + 2) & 3, in = j < 2 ? i : i + 2;
        asm volatile("v_mfma_f32_16x16x32_bf16 %0, %1, %2, %0" : "+v"(acc[h][j]) : "v"(wf[e][j]), "v"(af[(i + e) % PD][h]));
        PIN();
        if (in == 16 && !(g & 3)) asm volatile("v_add_u32 %0, 0x10000, %0" : "+v"(wad[jn]));
        if (in < 32) {
          const s16x4 v = __builtin_amdgcn_ds_read_tr16_b64_v4i16(LDS_AT(s16x4, wad[jn] + ((in + h) & 15) * 4096 + e * 1024));
          if (e) wf[h][jn] = (s16x8)__builtin_shufflevector(wlo, v, 0, 1, 2, 3, 4, 5, 6, 7);
          else wlo = v;
          PIN();
        }
        if ((g & 3) == 1 && i > 0 && i + PD - 2 < 32) { af[(i - 2 + (j & 1)) % PD][j >> 1] = lda(i + PD - 2 + (j & 1), j >> 1); PIN(); }
        if ((g & 3) == 3 && in < 32 && i != SIGAT) { __builtin_amdgcn_s_waitcnt(0xc47f); PIN(); }
      }
    });
    __builtin_amdgcn_s_setprio(ZPRIO);
    asm volatile("s_nop 2" : "+v"(acc[0][0]), "+v"(acc[1][0]));
    TE(4, 1); TE(7, 2);
    if (wid == 0) STAMP(dk, 17);
    if (wid == 1 && !RES2) STAMP(4, 0);
    u32x4 pk[2][2];
    float sv[2];
    auto sums = [&]() __attribute__((always_inline)) {
      f32x4 d[2];
      float q[2];
#pragma unroll
      for (int h = 0; h < 2; ++h) {
        d[h] = __builtin_amdgcn_mfma_f32_16x16x32_bf16(__builtin_bit_cast(s16x8, ow[0]), __builtin_bit_cast(s16x8, pk[h][0]), f32x4{0.f, 0.f, 0.f, 0.f}, 0, 0, 0);
        d[h] = __builtin_amdgcn_mfma_f32_16x16x32_bf16(__builtin_bit_cast(s16x8, ow[1]), __builtin_bit_cast(s16x8, pk[h][1]), d[h], 0, 0, 0);
        q[h] = 0.f;
      }
#pragma unroll
      for (int e = 0; e < 16; ++e) {
        const bf16x2 x = __builtin_bit_cast(bf16x2, (unsigned)pk[e & 1][e >> 3][(e >> 1) & 3]);
        q[e & 1] = __builtin_amdgcn_fdot2_f32_bf16(x, x, q[e & 1], false);
      }
      const auto a = __builtin_amdgcn_permlane32_swap(__float_as_uint(q[0]), __float_as_uint(q[1]), false, false);
      const unsigned b = __float_as_uint(__uint_as_float(a[0]) + __uint_as_float(a[1]));
      const auto c = __builtin_amdgcn_permlane16_swap(b, b, false, false);
      const unsigned e = __float_as_uint(__uint_as_float(c[0]) + __uint_as_float(c[1]));
      const auto f = __builtin_amdgcn_permlane32_swap(e, e, false, false);
#pragma unroll
      for (int h = 0; h < 2; ++h) sv[h] = (lane & 16) ? __uint_as_float(f[h]) : d[h][0];
    };
#pragma unroll
    for (int j = 0; j < 4; ++j)
#pragma unroll
      for (int h = 0; h < 2; ++h) {
        pk[h][j >> 1][(j & 1) * 2] = pack2(acc[h][j][0], acc[h][j][1]); pk[h][j >> 1][(j & 1) * 2 + 1] = pack2(acc[h][j][2], acc[h][j][3]);
        *LDS_AT(u32x2, tw[j] + h * 2048) = u32x2{pk[h][j >> 1][(j & 1) * 2], pk[h][j >> 1][(j & 1) * 2 + 1]};
        PIN();
      }
    asm volatile("" ::: "memory");
    *LDS_AT(volatile unsigned char, rfa) = (unsigned char)2;
#pragma unroll
    for (int p = 0; p < 2; ++p) xres[p] = lds_rd4(tr + p * 1024);
    PIN();
    if (!RES2) {
      const __amdgpu_buffer_rsrc_t rso = __builtin_amdgcn_make_buffer_rsrc(out, 0, S_ * D_ * 2, 0x00020000);
      const __amdgpu_buffer_rsrc_t rsf = __builtin_amdgcn_make_buffer_rsrc(xsig + wid, 0, RX * 512, 0x00020000);
      const u32x4 x2 = lds_rd4(tr + 2048), x3 = lds_rd4(tr + 3072);
      PIN();
#pragma unroll
      for (int p = 0; p < 4; ++p) { __builtin_amdgcn_raw_buffer_store_b128(p < 2 ? xres[p] : (p < 3 ? x2 : x3), rso, xo + p * (8 * ldw * 2), 0, XAUX); PIN(); }
      TE(4, 2); TE(5, 0);
      sums();
      if (lane < 48) { __builtin_amdgcn_global_atomic_fadd_f64(pst, (double)sv[0]); __builtin_amdgcn_global_atomic_fadd_f64(pst + 16, (double)sv[1]); }
      PIN();
      TE(4, 3); TE(5, 1);
      asm volatile("s_waitcnt vmcnt(2)" ::: "memory");
      __builtin_amdgcn_raw_buffer_store_b32(tag, rsf, lane * 512 + 256, 0, 16);
      *LDS_AT(volatile unsigned char, rfa) = (unsigned char)3;
      PIN();
      TE(5, 2);
      asm volatile("s_waitcnt vmcnt(1)" ::: "memory");
      __builtin_amdgcn_raw_buffer_store_b32(tag, rsf, lane * 512, 0, 16);
      PIN();
      TE(5, 3);
      __builtin_amdgcn_s_setprio(0);
      return;
    }
    TE(4, 2); TE(5, 0);
    sums();
    __hip_atomic_store(pps, f2word(sv[0]), __ATOMIC_RELAXED, __HIP_MEMORY_SCOPE_AGENT);
    __hip_atomic_store(pps + 64, f2word(sv[1]), __ATOMIC_RELAXED, __HIP_MEMORY_SCOPE_AGENT);
    if (wid == 0) STAMP(dk, 14);
  } else {
    u32x4 rres[4];
    double is1[4], is2[4];
#pragma unroll
    for (int p = 0; p < 4; ++p)
      rres[p] = __builtin_amdgcn_raw_buffer_load_b128(__builtin_amdgcn_make_buffer_rsrc(const_cast<u16*>(res), 0, S_ * D_ * 2, 0x00020000), ((row - 16 + 8 * p) * ldw + col) * 2, 0, 16);
#pragma unroll
    for (int p = 0; p < 4; ++p) { is1[p] = ld_ag(stats_in + row - 16 + 8 * p); is2[p] = ld_ag(stats_in + S_ + row - 16 + 8 * p); }
    PIN();
    if (RES2) {
#pragma unroll
      for (int p = 0; p < 4; ++p) {
        float mean, var, mb, den;
        ln_from_sums(is1[p], is2[p], mean, var, mb, den);
        const float rin = frcp(den);
#pragma unroll
        for (int e = 0; e < 4; ++e) {
          const f32x2 x = {__uint_as_float(rres[p][e] << 16), __uint_as_float(rres[p][e] & 0xffff0000u)};
          const f32x2 h = (x - f32x2{mb, mb}) * f32x2{rin, rin} + x;
          rres[p][e] = pack2(h[0], h[1]);
        }
        asm volatile("" : "+v"(rres[p]));
      }
      PIN();
    }
    if ((idx >> 5) == tmi * 4 + wid - 4) {
      const int dr = idx - (row - 16);
      u32x4 qq;
#pragma unroll 1
      for (int i = 0; i < SPIN_MAX; ++i) {
        qq = __builtin_amdgcn_raw_buffer_load_b128(__builtin_amdgcn_make_buffer_rsrc(hand, 0, HAND_N * 4, 0x00020000), (GRAN_N + Y_N) * 4 + col * 2, 0, 16);
        if (none(umax(umax(qq[0], qq[1]), umax(qq[2], qq[3])) == SENT)) break;
        asm volatile("" ::: "memory");
      }
#pragma unroll
      for (int p = 0; p < 4; ++p)
#pragma unroll
        for (int e = 0; e < 4; ++e) rres[p][e] = dr == 8 * p ? qq[e] : rres[p][e];
    }
#pragma unroll 1
    for (int i = 0; i < 4 * SPIN_MAX; ++i)
      if (__builtin_amdgcn_readfirstlane(*LDS_AT(const volatile unsigned, flg + 16)) >= 12u) break;
#pragma unroll
    for (int p = 0; p < 4; ++p) *LDS_AT(volatile u32x4, tr - 2048 + p * 1024) = rres[p];
    asm volatile("s_waitcnt lgkmcnt(0)" ::: "memory");
    announce();
    TE(4, 1); TE(7, 2);
    asm volatile("s_waitcnt vmcnt(0)" ::: "memory");
    if (!RES2) return;
    __builtin_amdgcn_s_sleep(TPRE);
    unsigned fm = SENT;
#pragma unroll 1
    for (int i = 0; i < 4 * SPIN_MAX; ++i) {
      fm = __builtin_amdgcn_readfirstlane(*LDS_AT(const volatile unsigned, fcm));
      if (fm != SENT) break;
      __builtin_amdgcn_s_sleep(1);
    }
    const unsigned r = (lane & 8) ? fm >> 16 : fm & 0xffffu;
    unsigned fra = tb + (r & 127u) * 128 + (((r ^ lane) & 7u) << 4);
    int fro = (r >> 5) == (unsigned)(tmi * 4 + wid - 4) && lane < 16 ? ((lane >> 3) * ldw + col) * 2 : S_ * D_ * 2;
    asm volatile("" : "+v"(fra), "+v"(fro));
#pragma unroll 1
    for (int i = 0; i < 4 * SPIN_MAX; ++i) {
      if (__builtin_amdgcn_readfirstlane(*LDS_AT(const volatile unsigned char, flg + 32 + wid)) == 2u) break;
      __builtin_amdgcn_s_sleep(TPSL);
    }
    asm volatile("" ::: "memory");
    __builtin_amdgcn_s_setprio(XPRIO);
    PIN();
    TE(4, 2); TE(5, 0);
    const u32x4 xr = lds_rd4(fra);
#pragma unroll
    for (int p = 0; p < 2; ++p) xres[p] = lds_rd4(tr + p * 1024);
    PIN();
    __builtin_amdgcn_raw_buffer_store_b128(xr, __builtin_amdgcn_make_buffer_rsrc(out, 0, 3 * D_ * 2, 0x00020000), fro, 0, XAUX);
  }
  TE(4, 3); TE(7, 3);
  PIN();
  __builtin_amdgcn_s_setprio(0);
  __hip_atomic_fetch_add(LDS_AT(unsigned, oad), 1u, __ATOMIC_RELAXED, __HIP_MEMORY_SCOPE_WORKGROUP);
  TE(5, 1); TE(5, 2);
  PIN();
}

#define MFMA32(a, b, c) __builtin_amdgcn_mfma_f32_32x32x16_bf16(a, b, c, 0, 0, 0)
#define MFMA16(a, b, c) __builtin_amdgcn_mfma_f32_16x16x32_bf16(a, b, c, 0, 0, 0)

#define PLO(x) __builtin_bit_cast(bf16x16, __builtin_shufflevector(x, x, 0, 1, 2, 3, 4, 5, 6, 7))
#define PHI(x) __builtin_bit_cast(bf16x16, __builtin_shufflevector(x, x, 8, 9, 10, 11, 12, 13, 14, 15))
#define SMF32(a, b, c) __builtin_amdgcn_smfmac_f32_32x32x32_bf16(__builtin_bit_cast(bf16x8, a), b, c, sidx, 0, 0)
#define SMF16(a, b, c) __builtin_amdgcn_smfmac_f32_16x16x64_bf16(__builtin_bit_cast(bf16x8, a), b, c, sidx, 0, 0)

__device__ __forceinline__ void patch_kv(unsigned tab, int r, s16x8& k0, s16x8& k1, s16x8& v0, s16x8& v1, bool on = true) {
  const int lane = fresh_lane(), hi = lane >> 5;
  if (on && (lane & 31) == kpos(r)) {
    k0 = *LDS_AT(const s16x8, tab + hi * 16); k1 = *LDS_AT(const s16x8, tab + 32 + hi * 16);
  }
  const short vv = *LDS_AT(const short, tab + 128 + (lane & 31) * 2);
  const int js = (on && hi == ((r >> 2) & 1)) ? (r >> 4) * 8 + ((r >> 3) & 1) * 4 + (r & 3) : -1;
#pragma unroll
  for (int e = 0; e < 8; ++e) { v0[e] = js == e ? vv : v0[e]; v1[e] = js == 8 + e ? vv : v1[e]; }
}

__device__ __forceinline__ void attn_safe(const u16* KV, int tile0, s16x8 q0, s16x8 q1,
                                       f32x16& o, float& l_out, int ptile, int prow, unsigned ptab) {
  const int lane = fresh_lane();
  float m = -INFINITY, l = 0.f;
  const int sidx = 0xDDDDDDDD;
  const __amdgpu_buffer_rsrc_t rs = __builtin_amdgcn_make_buffer_rsrc(const_cast<u16*>(KV), 0, 2 * S_ * D_ * 2, 0x00020000);
#pragma unroll
  for (int e = 0; e < 16; ++e) o[e] = 0.f;
  for (int tt = 0; tt < 64; ++tt) {
    const int t = tile0 + tt, off = t * 2048 + lane * 16;
    s16x8 k0 = __builtin_bit_cast(s16x8, __builtin_amdgcn_raw_buffer_load_b128(rs, off, 0, 16));
    s16x8 k1 = __builtin_bit_cast(s16x8, __builtin_amdgcn_raw_buffer_load_b128(rs, off + 1024, 0, 16));
    s16x8 v0 = __builtin_bit_cast(s16x8, __builtin_amdgcn_raw_buffer_load_b128(rs, off + S_ * D_ * 2, 0, 16));
    s16x8 v1 = __builtin_bit_cast(s16x8, __builtin_amdgcn_raw_buffer_load_b128(rs, off + S_ * D_ * 2 + 1024, 0, 16));
    if (t == ptile) patch_kv(ptab, prow, k0, k1, v0, v1);
    f32x16 sc;
#pragma unroll
    for (int e = 0; e < 16; ++e) sc[e] = 0.f;
    sc = MFMA32(k0, q0, sc); sc = MFMA32(k1, q1, sc);
    float mt = sc[0];
#pragma unroll
    for (int e = 1; e < 16; ++e) mt = fmaxf(mt, sc[e]);
    mt = fmaxf(mt, __shfl_xor(mt, 32, 64));
    const float mn = fmaxf(m, mt);
    const float alpha = __builtin_amdgcn_exp2f(m - mn);
    m = mn;
#pragma unroll
    for (int e = 0; e < 16; ++e) sc[e] = __builtin_amdgcn_exp2f(sc[e] - mn);
    float ls = 0.f;
#pragma unroll
    for (int e = 0; e < 16; ++e) ls += __uint_as_float(__float_as_uint(sc[e]) & 0xffff0000u);
    l = l * alpha + ls;
#pragma unroll
    for (int e = 0; e < 16; ++e) o[e] *= alpha;
    o = SMF32(v0, PLO(sc), o); o = SMF32(v1, PHI(sc), o);
  }
  l_out = l + __shfl_xor(l, 32, 64);
}

__device__ __forceinline__ float wave_fmax(float v) {
  asm volatile("s_nop 1\n\tv_max_f32_dpp %0, %0, %0 row_shr:1 row_mask:0xf bank_mask:0xf\n\ts_nop 1\n\tv_max_f32_dpp %0, %0, %0 row_shr:2 row_mask:0xf bank_mask:0xf\n\t"
               "s_nop 1\n\tv_max_f32_dpp %0, %0, %0 row_shr:4 row_mask:0xf bank_mask:0xf\n\ts_nop 1\n\tv_max_f32_dpp %0, %0, %0 row_shr:8 row_mask:0xf bank_mask:0xf\n\t"
               "s_nop 1\n\tv_max_f32_dpp %0, %0, %0 row_bcast:15 row_mask:0xa bank_mask:0xf\n\ts_nop 1\n\tv_max_f32_dpp %0, %0, %0 row_bcast:31 row_mask:0xc bank_mask:0xf\n\ts_nop 0"
               : "+v"(v));
  return __int_as_float(__builtin_amdgcn_readlane(__float_as_int(v), 63));
}

#define PSLP 8
#define WSLP 3
#define PRE1 8
#define PRE5 8
#define W3SLP 4
#define FSTART 10
#define OAUX 16
#define OMX 4
#define T0 2u
#define TW 10u
#define HW 2
#define HP1 1
#define POLLAT 11
#define X2AT 2
#define HPRIO 3
#define SSLP 1
#define SPRE 6
#define WGAPS {0, 1, 7, 8, 9, 10, 11, 12}
#define KVS 4
#define PNS 12
#define G0 3u
#define GS 2u
#define IAT 1u
#define YSLP 2
#define ISLP 8
#define KSLP 2
#define UAT 13u
#define UHW 0
#define PPSLP 2
#define XRSLP 2
#define MPRE 20
#define YD1 125
#define YD2 150
#define KDT 200
#define XDT 45
#define LNPRIO 1
#define MSLP 4
#define BARG 0
struct Args {
  long long lay;
  const u16* a; u16* kv; u16* o; u16* xo; double* so;
  const u16* W; u16* wc;
  const u16 *wg, *wu, *wd, *wo, *w_r;
  unsigned* gran; float* csf; double* stats; unsigned* hand; u16* out;
};
typedef __attribute__((address_space(4))) const Args* ArgP;
#define WCAT 3
#define CAP0 1
#define PRE4 50
#define PRE7 20
#define UARR 12u
#define GF0 10
#define XGATE 8
#define ASLP 1
#define B15 3
#define KO 5
#define PRO 2
#define HS 2
#define XD2 50
#define TFL 4u
#define QPRE 10
#define QSLP 1
#define GW 1u
#define G1 8u
#define GFD 2
#define YD3 90
#define WMAP 0x32100333
#define SMAP 0
#define STCH 6
#define KPOLL 10
#define TPRIO 2
#define TSLP 9
__device__ __forceinline__ bool slow_cols(const u16* W, int tni) { return ((((unsigned)(uintptr_t)W >> 7) + tni) & 7) == 3; }
#define ARGS_HERE                                                                                                      \
  ArgP ap_ = ap; asm volatile("" : "+s"(ap_));                                                                          \
  const u16* A = ap_->a; const u16* __restrict__ W = ap_->W; u16* KV = ap_->kv; u16* Of = ap_->o;        \
  const u16* __restrict__ wg = ap_->wg; const u16* __restrict__ wu = ap_->wu; const u16* __restrict__ wd = ap_->wd;       \
  const u16* __restrict__ wo = ap_->wo; const u16* __restrict__ w_r = ap_->w_r; unsigned* gran = ap_->gran;              \
  float* csf = ap_->csf; const double* stats = ap_->stats; double* stats_out = ap_->so; unsigned* hand = ap_->hand; \
  u16* xout = ap_->xo; u16* __restrict__ out = ap_->out
#define AGAIN "s_getpc_b64 s[90:91]\n\ts_add_u32 s90, s90, layers_kernel@rel32@lo+4\n\ts_addc_u32 s91, s91, layers_kernel@rel32@hi+12\n\ts_setpc_b64 s[90:91]"
#define KEPT(c)                                                                                                        \
  c "{v[104:107]}"(gu[0]), c "{v[108:111]}"(gu[1]), c "{v[112:115]}"(gu[2]), c "{v[116:119]}"(gu[3]), c "{v[120:123]}"(gu[4]),      \
  c "{v[124:127]}"(gu[5]), c "{v[128:131]}"(gu[6]), c "{v[132:135]}"(gu[7]), c "{v[136:139]}"(gu[8]), c "{v[140:143]}"(gu[9]),      \
  c "{v[144:147]}"(gu[10]), c "{v[148:151]}"(gu[11]), c "{v[152:155]}"(gu[12]), c "{v[156:159]}"(gu[13]), c "{v[160:163]}"(gu[14]), \
  c "{v[164:167]}"(gu[15])
#define LAYER_HERE ArgP apl_ = ap; asm volatile("" : "+s"(apl_)); const bool LAY2 = apl_->lay != 0
__device__ __forceinline__ void layer_body(const int wid, const int bid, const unsigned lbase, const ArgP ap, const unsigned long long t_entry) {
  constexpr int K = 1024, NKT = 16, NST = 3, ABYTES = 16384, WBYTES = 24576, STB = ABYTES + WBYTES, ldw = 3 * D_;
  constexpr int TABA = NST * STB, ALPHA = TABA + 2048, CRED = ALPHA + 1024;
  constexpr int KSB = 8192, ASB = 16384, FLG = 8 * ASB + 256;
  constexpr int DUM = FLG + 64, TAB = DUM + 256, RED = TAB + 512, SACT = RED + 4096, XML = SACT + 3584, UGL = XML + 4096, SPM = UGL + 1024, QOFF = 9 * ASB;
  constexpr int TABW = RED;
  static_assert(ALPHA % 1024 == 0 && RK == 16, "");
  static_assert(SPM + 2048 <= QOFF && CRED <= 8 * ASB && RED + 8192 <= QOFF && RED % 16 == 0, "lds");
  const int xcd = (bid & 1) * 4 + ((bid >> 1) & 3), loc = bid >> 3;
  const int tmi = (xcd / QNG) * QMX + loc / QNX, tni = (xcd % QNG) * QNX + loc % QNX;
  const int m0 = tmi * 128, n0 = tni * 64;
  const int omi = (xcd / (OMX / 2)) * OMX + loc / (32 / OMX), oni = (xcd % (OMX / 2)) * (32 / OMX) + loc % (32 / OMX);
  constexpr unsigned VOFF = (unsigned)S_ * D_ * 2;
  auto tile_at = [&](unsigned n) __attribute__((always_inline)) {
#pragma unroll 1
    for (int i = 0; i < 4 * SPIN_MAX; ++i) {
      if (__builtin_amdgcn_readfirstlane(*LDS_AT(const volatile unsigned, lbase + FLG + 36)) == n * 0x01010101u) break;
      __builtin_amdgcn_s_sleep(1);
    }
    asm volatile("" ::: "memory");
    PIN();
  };
  auto all_ge = [&](unsigned w, unsigned n) { return ((w + (128u - n) * 0x01010101u) & 0x80808080u) == 0x80808080u; };
  if (wid >= 8) {
    u32x4 gu[16];
    LAYER_HERE;
    constexpr int dk = 6;
    const int hw = wid - 8, b = bid;
    int nb = 0;
    constexpr int lim = NKT - 1;
    auto tick_nw = [&]() {
      PIN();
      if (nb < lim) { __builtin_amdgcn_s_barrier(); ++nb; } else __builtin_amdgcn_s_sleep(2);
      asm volatile("" ::: "memory");
      PIN();
    };
    auto tick = [&]() {
      asm volatile("s_waitcnt lgkmcnt(0)" ::: "memory");
      tick_nw();
    };
    auto fold16 = [&](f32x2 y0, f32x2 y1) __attribute__((always_inline)) {
      const auto q0 = __builtin_amdgcn_permlane32_swap(__float_as_uint(y0[0]), __float_as_uint(y1[0]), false, false);
      const auto q1 = __builtin_amdgcn_permlane32_swap(__float_as_uint(y0[1]), __float_as_uint(y1[1]), false, false);
      const auto q = __builtin_amdgcn_permlane16_swap(__float_as_uint(__uint_as_float(q0[0]) + __uint_as_float(q0[1])),
                                                      __float_as_uint(__uint_as_float(q1[0]) + __uint_as_float(q1[1])), false, false);
      return __uint_as_float(q[0]) + __uint_as_float(q[1]);
    };
    unsigned best, tag, rmd;
    float alpha, m, wsum;
    u32x4 tw;
    u32x2 xq;
    s16x8 sl[8];
    const int kcap = CAP0 + (tmi & 7);
    if (!LAY2) {
      ARGS_HERE;
      const int lane = fresh_lane(), t = hw * 64 + lane;
      const unsigned ep = ld_ag(gran);
      const int srow = m0 + tni * 8 + hw * 2;
      u32x4 xa[2][2], wv[8];
      const int wr_ = (3 - ((unsigned)(uintptr_t)W >> 7) - (tni & ~3)) & 7;
      const bool cpy = hw == 3 && wr_ < 4 && wr_ != (tni & 3), wld = hw < 3 ? wr_ != (tni & 3) : cpy;
      const int wcn = cpy ? (tni & ~3) + wr_ : tni, wcp = cpy ? (tni - wr_ - 1) & 3 : hw;
      const int wof = ((tmi * 64 + (lane >> 3)) * ldw + wcp * D_ + wcn * 64 + (lane & 7) * 8) * 2;
      auto ld_w = [&]() {
        const __amdgpu_buffer_rsrc_t rw = __builtin_amdgcn_make_buffer_rsrc(const_cast<u16*>(W), 0, wld ? D_ * ldw * 2 : 0, 0x00020000);
#pragma unroll
        for (int r = 0; r < 8; ++r) wv[r] = __builtin_amdgcn_raw_buffer_load_b128(rw, wof, r * 8 * ldw * 2, 0);
        PIN();
      };
      auto wcopy = [&]() __attribute__((always_inline)) {
        if (cpy) {
          const __amdgpu_buffer_rsrc_t rw = __builtin_amdgcn_make_buffer_rsrc(ap_->wc, 0, D_ * ldw * 2, 0x00020000);
#pragma unroll
          for (int r = 0; r < 8; ++r) __builtin_amdgcn_raw_buffer_store_b128(wv[r], rw, wof + r * 8 * ldw * 2, 0, 16);
        }
        PIN();
      };
      const int wat = ((hw == 3 ? SMAP : WMAP) >> (4 * (tmi & 7))) & 3;
      unsigned ps = 0u;
      auto stouch = [&](int at) __attribute__((always_inline)) {
        if (at == STCH && cpy) ps = __builtin_amdgcn_raw_buffer_load_b32(__builtin_amdgcn_make_buffer_rsrc(const_cast<u16*>(W), 0, D_ * ldw * 2, 0x00020000),
                                                                        (((tmi ^ 8) * 64 + lane) * ldw + wcp * D_ + wcn * 64) * 2, 0, 0);
        PIN();
      };
      auto ld_x = [&](int h) {
#pragma unroll
        for (int r = 0; r < 2; ++r) xa[r][h] = *reinterpret_cast<const u32x4*>(A + (size_t)(srow + r) * D_ + (h ^ (tmi >> 3)) * 512 + lane * 8);
        PIN();
      };
      ld_x(0);
      if (X2AT == 0) ld_x(1);
      if (wat == 0) ld_w();
      unsigned pc = 0u;
      if (hw == 3) {
        unsigned long long a0, a1;
        CODE_ADDR("layers_kernel", a0);
        CODE_ADDR("end_marker_kernel", a1);
        pc = touch_code(a0, a1, loc, lane);
      }
      if (hw == 1) pc = ld_ag(gran + ((lane & 1) ? GR_ROW + (m0 + tni * 8) * 4 : GR_KV + (tni * RK + tmi % RK) * 32));
      if (hw == 2) st_ag(gran + GR_N + bid * 64 + lane, lane + 1u);
      {
        const int gi = bid * 256 + t;
        if (gi < GRAN_N / 4) __builtin_amdgcn_raw_buffer_store_b128(u32x4{SENT, SENT, SENT, SENT}, __builtin_amdgcn_make_buffer_rsrc(hand, 0, GRAN_N * 4, 0x00020000), gi * 16, 0, 16);
        if (t < 24) __hip_atomic_store(stats_out + (t >> 3) * S_ + m0 + tni * 8 + (t & 7), 0.0, __ATOMIC_RELAXED, __HIP_MEMORY_SCOPE_AGENT);
      }
      tick();
      if (wat == 1) ld_w();
      if (X2AT == 1) ld_x(1);
      tick();
      if (wat == 2) ld_w();
      if (WCAT == 2) wcopy();
      if (X2AT == 2) ld_x(1);
      asm volatile("" :: "v"(ep));
      tick();
      if (wat == 3) ld_w();
      if (WCAT == 3) wcopy();
      if (X2AT == 3) ld_x(1);
      stouch(3);
#pragma unroll
      for (int i = 0; i < HW; ++i) { tick(); stouch(4 + i); }
      __builtin_amdgcn_s_setprio(HP1);
      PIN();
      tag = ep * 1664525u + 1013904223u;
      const __amdgpu_buffer_rsrc_t rsc = __builtin_amdgcn_make_buffer_rsrc(gran, 0, GR_N * 4, 0x00020000);
      {
        u32x4 od;
#pragma unroll
        for (int q = 0; q < 4; ++q) od[q] = ((lane & 7) >> 1) == q ? ((lane & 1) ? 0x3f800000u : 0x00003f80u) : 0u;
        f32x4 d[2] = {{0.f, 0.f, 0.f, 0.f}, {0.f, 0.f, 0.f, 0.f}};
#pragma unroll
        for (int r = 0; r < 8; ++r) d[r & 1] = __builtin_amdgcn_mfma_f32_16x16x32_bf16(__builtin_bit_cast(s16x8, od), __builtin_bit_cast(s16x8, wv[r]), d[r & 1], 0, 0, 0);
        u32x4 gr;
#pragma unroll
        for (int q = 0; q < 4; ++q) gr[q] = ((__float_as_uint(dpp_add<0x128, 0xf>(fadd(d[0][q], d[1][q]))) + 0x80u) & ~0xffu) | ((tag >> (8 * q)) & 0xffu);
        asm volatile("s_waitcnt vmcnt(0)" ::: "memory");
        __builtin_amdgcn_raw_buffer_store_b128(gr, rsc, (lane & 40) || !wld ? GR_N * 4 : (GR_COL + (wcn * 16 + tmi) * 192 + wcp * 64 + (lane & 7) * 8 + (lane >> 4) * 4) * 4, 0, 16);
      }
      asm volatile("" :: "v"(pc));
      tick_nw();
      stouch(6);
      {
        float xv[2][16], mean[2];
#pragma unroll
        for (int r = 0; r < 2; ++r) {
          float sm = 0.f;
#pragma unroll
          for (int i = 0; i < 4; ++i) {
            xv[r][2 * i] = __uint_as_float(xa[r][0][i] << 16); xv[r][2 * i + 1] = __uint_as_float(xa[r][0][i] & 0xffff0000u);
            xv[r][8 + 2 * i] = __uint_as_float(xa[r][1][i] << 16); xv[r][8 + 2 * i + 1] = __uint_as_float(xa[r][1][i] & 0xffff0000u);
          }
#pragma unroll
          for (int i = 0; i < 16; ++i) sm += xv[r][i];
          mean[r] = wave_sum(sm) * (1.0f / 1024.0f);
          tick();
          stouch(7 + r);
        }
        float vr[2];
#pragma unroll
        for (int r = 0; r < 2; ++r) {
          float v = 0.f;
#pragma unroll
          for (int i = 0; i < 16; ++i) { const float d = xv[r][i] - mean[r]; v += d * d; }
          vr[r] = wave_sum(v) * (1.0f / 1024.0f);
          if (r == 0) tick();
        }
        const float vv = lane & 2 ? vr[1] : vr[0], mm = lane & 2 ? mean[1] : mean[0];
        const float rin = 1.0f / rbf(sqrtf(rbf(rbf(vv) + 1e-5f)));
        if (lane < 4) __builtin_amdgcn_raw_buffer_store_b64(u32x2{__float_as_uint((lane & 1) ? rbf(mm) : rin), tag}, rsc, (GR_ROW + srow * 4 + lane * 2) * 4, 0, 16);
      }
      tick();
      float cs;
      while (nb < POLLAT) tick();
      asm volatile("" :: "v"(ps));
#pragma unroll 1
      for (int spin = 0;; ++spin) {
        u32x4 csv[4];
        const __amdgpu_buffer_rsrc_t rsw = __builtin_amdgcn_make_buffer_rsrc(gran, 0, hw < 3 ? GR_N * 4 : 0, 0x00020000);
#pragma unroll
        for (int i = 0; i < 4; ++i) csv[i] = __builtin_amdgcn_raw_buffer_load_b128(rsw, (GR_COL + (tni * 16 + (lane >> 4)) * 192 + hw * 64 + (lane & 15) * 4) * 4, i * (4 * 192 * 4), 16);
        const u32x4 rp = __builtin_amdgcn_raw_buffer_load_b128(rsc, (GR_ROW + (hw < 2 ? m0 + t : (m0 ^ 128) + tni * 8 + (t & 7)) * 4) * 4, 0, 16);
        PIN();
        tick(); tick();
        unsigned bad = 0u;
        float z[4];
#pragma unroll
        for (int q = 0; q < 4; ++q) {
          const unsigned tq = tag >> (8 * q);
          bad |= ((csv[0][q] ^ tq) | (csv[1][q] ^ tq)) | ((csv[2][q] ^ tq) | (csv[3][q] ^ tq));
          z[q] = fadd(fadd(__uint_as_float(csv[0][q] & ~0xffu), __uint_as_float(csv[1][q] & ~0xffu)), fadd(__uint_as_float(csv[2][q] & ~0xffu), __uint_as_float(csv[3][q] & ~0xffu)));
        }
        cs = fold16(f32x2{z[0], z[1]}, f32x2{z[2], z[3]});
        bad = (hw < 3 ? bad & 0xffu : 0u) | (rp[1] ^ tag) | (rp[3] ^ tag);
        alpha = __uint_as_float(rp[0]); m = __uint_as_float(rp[2]);
        if (none(bad != 0u) || spin >= SPIN_MAX) break;
      }
      if (tmi == 0 && hw < 3) st_ag(reinterpret_cast<unsigned*>(csf) + hw * 1024 + n0 + (lane & 15) * 4 + (lane >> 4), __float_as_uint(cs));
      unsigned c0, c1, c2;
      split3(cs, c0, c1, c2);
      tw = u32x4{c0 | (c1 << 16), c0 | (c2 << 16), c1 | (c0 << 16), 0u};
      best = rmd = 0u;
      wsum = 0.f;
      asm volatile("" : "=v"(xq), KEPT("="));
#pragma unroll
      for (int i = 0; i < 8; ++i) asm volatile("" : "=v"(sl[i]));
    } else {
      asm volatile("; kept registers" : KEPT("="));
      PIN();
      ARGS_HERE;
      const int lane = fresh_lane(), t = hw * 64 + lane;
      best = rmd = 0u;
      tag = __builtin_amdgcn_readfirstlane(*LDS_AT(const volatile unsigned, lbase + FLG + 20));
#pragma unroll
      for (int i = 0; i < 8; ++i) asm volatile("" : "=v"(sl[i]));
      unsigned sad;
      auto tickc = [&]() __attribute__((always_inline)) {
        tick();
        if (nb == kcap + 1) {
#pragma unroll
          for (int i = 0; i < 8; ++i) {
            const unsigned a = (sad ^ ((i >> 2) * 64) ^ (((i >> 1) & 1) * 16)) + (i & 1) * 12288;
            sl[i] = __builtin_shufflevector(__builtin_amdgcn_ds_read_tr16_b64_v4i16(LDS_AT(s16x4, a)), __builtin_amdgcn_ds_read_tr16_b64_v4i16(LDS_AT(s16x4, a + 1536)), 0, 1, 2, 3, 4, 5, 6, 7);
          }
        }
        PIN();
      };
      static_assert(CAP0 + 8 <= KPOLL, "the stage is taken before that");
      if (hw == 1) STAMP(4, 4);
      if (hw == 0) {
        __builtin_amdgcn_s_sleep(TPRE - 10);
        tile_at(3u);
        __builtin_amdgcn_s_sleep(XGATE);
        STAMP(4, 3);
#pragma unroll 1
        for (int spin = 0; spin < SPIN_MAX; ++spin) {
          if (none(ld_ag(gran + GR_X + (tmi * RX + tni % RX) * 128 + 64 + lane) != tag)) break;
          __builtin_amdgcn_s_sleep(1);
        }
      }
      static_assert(UHW == 0, "");
      if (hw == 0) asm volatile("s_waitcnt vmcnt(0)" ::: "memory");
      if (hw == 0) STAMP(4, 5);
      asm volatile("" ::: "memory");
      PIN();
      __builtin_amdgcn_s_barrier();
      PIN();
      if (hw == 1) STAMP(4, 6);
      {
        const int ln = fresh_lane(), g4 = ln >> 4, tp = ln & 3, k = 8 * g4 + ((ln & 15) >> 2);
        sad = lbase + (kcap % NST) * STB + ABYTES + k * 384 + (((2 * (tp >> 1)) ^ (4 * ((k >> 1) & 1)) ^ (g4 & 1)) << 4) + 8 * (tp & 1) + (hw % 3) * 128;
      }
      wsum = __uint_as_float(__builtin_amdgcn_readfirstlane(*LDS_AT(const volatile unsigned, lbase + FLG + 60)));
      asm volatile("" : "+v"(sad), "=v"(tw), "=v"(alpha), "=v"(m));
      PIN();
      const int row = m0 + (t & 127);
      const __amdgpu_buffer_rsrc_t rsg = __builtin_amdgcn_make_buffer_rsrc(hand, 0, GRAN_N * 4, 0x00020000);
      if (hw < 2 || tmi == tni) {
      while (nb < HS) tickc();
#pragma unroll 1
      for (int spin = 0; spin < SPIN_MAX; ++spin) {
        const unsigned v = ld_ag(gran + GR_X + (tmi * RX + tni % RX) * 128 + lane);
        tickc();
        if (none(v != tag)) break;
      }
      asm volatile("" ::: "memory");
      PIN();
      const double s1 = ld_ag(stats + row), s2 = ld_ag(stats + S_ + row), sw = ld_ag(stats + 2 * S_ + row);
      tickc();
      float mean, var, mb, den;
      ln_from_sums(s1, s2, mean, var, mb, den);
      const float rin = frcp(den), g1 = 1.0f + rin;
      const float mb2 = rbf(mean + (mean - mb) * rin);
      const float den2 = rbf(__builtin_amdgcn_sqrtf(rbf(rbf(var * g1 * g1) + 1e-5f)));
      alpha = g1 * frcp(den2); m = (mb * rin + mb2) * frcp(g1);
      if (tmi == tni && hw >= 2) {
        const float r = (float)fma(-(double)mb, (double)wsum, sw) * rin;
        const unsigned rb = __float_as_uint(r) + 0x8000u;
        unsigned rkey = (rb ^ ((unsigned)((int)rb >> 31) | 0x80000000u)) & 0xfffff800u;
        if (r != r) rkey = 0u;
        rkey = wave_umax(rkey | (unsigned)(2047 - row));
        const unsigned md = __builtin_amdgcn_readlane(f2word(__uint_as_float((__float_as_uint(mb) >> 16) | (__float_as_uint(den) & 0xffff0000u))), (2047 - (int)(rkey & 0xffffu)) & 63);
        __builtin_amdgcn_raw_buffer_store_b64(u32x2{rkey, md}, rsg, lane < NREP ? lane * 256 + (tmi * 2 + hw - 2) * 8 : GRAN_N * 4, 0, 16);
      }
      }
      while (nb < KPOLL) tickc();
#pragma unroll 1
      for (int spin = 0; spin < SPIN_MAX; ++spin) {
        const u32x2 v = __builtin_amdgcn_raw_buffer_load_b64(rsg, (bid % NREP) * 256 + (lane & 31) * 8, 0, 16);
        tick(); tick();
        if (none(umax(v[0], v[1]) == SENT)) {
          best = wave_umax(v[0]);
          rmd = __builtin_amdgcn_readlane(v[1], ((2047 - (int)(best & 0xffffu)) >> 6) & 31);
          break;
        }
      }
      const int ix = (2047 - (int)(best & 0xffffu)) & 2047;
      if (row == ix) alpha = 0.f;
      xq = __builtin_amdgcn_raw_buffer_load_b64(__builtin_amdgcn_make_buffer_rsrc(const_cast<u16*>(A), 0, S_ * D_ * 2, 0x00020000), (ix * D_ + hw * 256 + (lane & 3) * 64 + (lane >> 2) * 4) * 2, 0, 16);
      PIN();
    }
    tag = __builtin_amdgcn_readfirstlane(tag);
    wsum = __uint_as_float(__builtin_amdgcn_readfirstlane(__float_as_uint(wsum)));
    const int lane = fresh_lane(), t = hw * 64 + lane;
    {
      unsigned a0, a1, a2;
      split3(-m, a0, a1, a2);
      u32x4 ta = {a0 | (a0 << 16), a1 | (a0 << 16), a1 | (a2 << 16), 0u};
      asm volatile("" : "+v"(tw), "+v"(ta), "+v"(alpha));
      __builtin_amdgcn_s_setprio(TPRIO);
      if (nb < lim) {
        while (nb < lim) tick();
        __builtin_amdgcn_s_sleep(TSLP);
      }
      if (!LAY2 && hw < 3) *LDS_AT(u32x4, lbase + TABW + ((hw ^ (hw < 2 ? ((lane >> 3) ^ tmi) & 1 : 0)) * 64 + (lane & 15) * 4 + (lane >> 4)) * 16) = tw;
      if (t < 128) {
        *LDS_AT(u32x4, lbase + TABA + t * 16) = ta;
        *LDS_AT(float, lbase + ALPHA + t * 4) = alpha;
        *LDS_AT(float, lbase + ALPHA + 512 + t * 4) = alpha * QSCALE;
      }
    }
    if (t >= 192 && t < 196) *LDS_AT(u32x4, lbase + CRED + ((t >> 1) & 1) * 512 + (t & 1) * 128) = u32x4{0u, 0u, 0u, 0u};
    asm volatile("s_waitcnt lgkmcnt(0)" ::: "memory");
    PIN();
    __builtin_amdgcn_s_barrier();
    PIN();
    *LDS_AT(u32x4, lbase + XML + t * 16) = u32x4{SENT, SENT, SENT, SENT};
    *LDS_AT(u32x4, lbase + SPM + (t & 127) * 16) = u32x4{SENT, SENT, SENT, SENT};
    if (hw == 0) {
      unsigned v;
      asm volatile("v_mov_b32 %0, 0\n\tv_writelane_b32 %0, 2, 0\n\tv_writelane_b32 %0, %1, 5\n\tv_writelane_b32 %0, %2, 7"
                   : "=&v"(v) : "s"(tag), "s"(best));
      lds_wr(lbase + FLG + lane * 4, v);
    }
    const int idx = LAY2 ? (2047 - (int)(best & 0xffffu)) & 2047 : -64;
    const bool p1blk = (oni & 7) != 7, fast = tmi < 8, own = fast && (loc & 1);
    const int fg = xcd * 28 + (p1blk ? loc - (loc >> 3) : 0), ow = (xcd & 3) * 16 + (loc >> 1);
    static_assert(OMX == 4, "oni % 8 = loc % 8");
#define GLD(a, b) "global_load_dwordx4 v[" #a ":" #b "], %0, off\n\tv_lshl_add_u64 %0, %0, 0, %1\n\t"
#define GLD4(...) asm volatile(__VA_ARGS__ : "+v"(p) : "s"((unsigned long long)(F_ * 2)), KEPT() : "memory")
    auto gptr = [&](const u16* wg, const u16* wu) {
      const int ln = fresh_lane();
      return ((ln & 2) ? wu : wg) + (size_t)(hw * 256 + (ln >> 2) * 4) * F_ + fg * 16 + (ln & 1) * 8;
    };
    auto gather = [&](const u16* p, int st) __attribute__((always_inline)) {
      const int e = (st + fg) & 3;
      p += e * (64 * F_);
      if (e == 0) GLD4(GLD(104, 107) GLD(108, 111) GLD(112, 115) GLD(116, 119));
      else if (e == 1) GLD4(GLD(120, 123) GLD(124, 127) GLD(128, 131) GLD(132, 135));
      else if (e == 2) GLD4(GLD(136, 139) GLD(140, 143) GLD(144, 147) GLD(148, 151));
      else GLD4(GLD(152, 155) GLD(156, 159) GLD(160, 163) GLD(164, 167));
    };
    auto dmask = [&](int q) { const int ln = fresh_lane(); return ((ln >> 1) & 1) == q ? ((ln & 1) ? 0xffff0000u : 0x0000ffffu) : 0u; };
    unsigned dm0, dm1, dad;
    auto gu_setup = [&]() __attribute__((always_inline)) {
      const int ln = fresh_lane();
      dm0 = dmask(0); dm1 = dmask(1);
      dad = lbase + SACT + ((ln & 3) * 8 + (ln >> 5) * 4 + ((ln >> 4) & 1)) * 16 + hw * 4;
      asm volatile("" : "+v"(dm0), "+v"(dm1), "+v"(dad));
    };
    auto gate_up = [&](u32x2 x) __attribute__((always_inline)) {
      asm volatile("s_getpc_b64 s[90:91]\n\ts_add_u32 s90, s90, gate_up_code@rel32@lo+4\n\ts_addc_u32 s91, s91, gate_up_code@rel32@hi+12\n\ts_swappc_b64 s[92:93], s[90:91]"
                   : "+{v[100:101]}"(x) : "{v99}"(dad), "{v102}"(dm0), "{v103}"(dm1), KEPT()
                   : "v84", "v85", "v86", "v87", "v88", "v89", "v90", "v91", "v92", "v93", "v94", "v95", "v96", "v97", "s88", "s89", "s90", "s91", "s92", "s93", "scc", "memory");
    };
    auto sums2 = [&](int so) __attribute__((always_inline)) {
      const int ln = fresh_lane();
      const f32x4 g4 = *LDS_AT(const volatile f32x4, lbase + SACT + so + (ln >> 2) * 16), u4 = *LDS_AT(const volatile f32x4, lbase + SACT + so + 256 + (ln >> 2) * 16);
      return f32x2{fadd(fadd(g4[0], g4[1]), fadd(g4[2], g4[3])), fadd(fadd(u4[0], u4[1]), fadd(u4[2], u4[3]))};
    };
    auto act_diag = [&](int so, unsigned md, const f32x2& cgu) __attribute__((always_inline)) {
      const f32x2 gu2 = sums2(so);
      const float mb = __uint_as_float(md << 16), rin = frcp(__uint_as_float(md & 0xffff0000u));
      float gv = fmaf(-mb, cgu[0], gu2[0]) * rin, uv = fmaf(-mb, cgu[1], gu2[1]) * rin;
      asm("" : "+v"(gv));
      rbf2(gv, uv);
      const float av = rbf(gv * frcp(1.0f + __expf(-gv))) * uv;
      const unsigned ap2 = pack2(av, av);
      return __builtin_bit_cast(s16x4, u32x2{ap2 & dm0, ap2 & dm1});
    };
    auto down = [&](const s16x4& a, const u32x2 (&w)[16]) __attribute__((always_inline)) {
      f32x4 d[2] = {{0.f, 0.f, 0.f, 0.f}, {0.f, 0.f, 0.f, 0.f}};
      static_for<0, 16>([&](auto fc) __attribute__((always_inline)) {
        constexpr int f = decltype(fc)::value;
        d[f & 1] = __builtin_amdgcn_mfma_f32_4x4x4bf16_1k(a, __builtin_bit_cast(s16x4, w[f]), d[f & 1], 4, f, 0);
      });
      f32x4 z;
#pragma unroll
      for (int e = 0; e < 4; ++e) { z[e] = d[0][e] + d[1][e]; asm volatile("" : "+v"(z[e])); }
      return u32x4{f2word(z[0]), f2word(z[1]), f2word(z[2]), f2word(z[3])};
    };
    auto fold8 = [&](f32x2 y0, f32x2 y1) __attribute__((always_inline)) { return dpp_add<0x128, 0xf>(fold16(y0, y1)); };
    auto fold4 = [&](f32x2 y0, f32x2 y1) __attribute__((always_inline)) { return dpp_add<0x124, 0xf>(fold8(y0, y1)); };
    auto y_sum = [&](const __amdgpu_buffer_rsrc_t rs, auto nbc) __attribute__((always_inline)) {
      constexpr int NB = decltype(nbc)::value, N = 14 / NB;
      const int ln = fresh_lane(), src = (GRAN_N + (ln >> 2) * D_ + ow * 16 + (ln & 3) * 4) * 4;
      f32x2 y0 = {0.f, 0.f}, y1 = {0.f, 0.f};
#pragma unroll 1
      for (int bt = 0; bt < NB; ++bt) {
        f32x2 z0, z1;
#pragma unroll 1
        for (int spin = 0; spin < SPIN_MAX; ++spin) {
          u32x4 v[N];
#pragma unroll
          for (int i = 0; i < N; ++i) v[i] = __builtin_amdgcn_raw_buffer_load_b128(rs, src, (bt * N + i) * (16 * D_ * 4), 16);
          z0 = y0; z1 = y1;
#pragma unroll
          for (int i = 0; i < N; ++i) {
            z0 = f32x2{fadd(z0[0], __uint_as_float(v[i][0])), fadd(z0[1], __uint_as_float(v[i][1]))};
            z1 = f32x2{fadd(z1[0], __uint_as_float(v[i][2])), fadd(z1[1], __uint_as_float(v[i][3]))};
          }
          const float chk = fadd(fadd(z0[0], z0[1]), fadd(z1[0], z1[1]));
          if (none(chk != chk)) break;
          unsigned mx = 0u;
#pragma unroll
          for (int i = 0; i < N; ++i) mx = umax(umax(umax(mx, v[i][0]), v[i][1]), umax(v[i][2], v[i][3]));
          if (none(mx == SENT)) break;
          __builtin_amdgcn_s_sleep(YSLP);
          asm volatile("" ::: "memory");
        }
        y0 = z0; y1 = z1;
      }
      return fold4(y0, y1);
    };
    [&]() __attribute__((always_inline)) {
    ARGS_HERE;
    const unsigned l16 = lane * 16;
    unsigned* kvflag = gran + GR_KV + ((LAY2 ? 16 : 0) + tni) * (32 * RK);
    auto wait_sig = [&](int sh, unsigned n) {
#pragma unroll 1
      for (int i = 0; i < 4 * SPIN_MAX; ++i) {
        if (((__builtin_amdgcn_readfirstlane(*LDS_AT(const volatile unsigned, lbase + FLG + 32)) >> sh) & 7u) >= n) break;
        __builtin_amdgcn_s_sleep(SSLP);
      }
      asm volatile("" ::: "memory");
      PIN();
    };
    PIN();
    asm volatile("s_waitcnt lgkmcnt(0)" ::: "memory");
    if (!LAY2) asm volatile("s_waitcnt vmcnt(0)" ::: "memory");
    __builtin_amdgcn_s_barrier();
    PIN();
    __builtin_amdgcn_s_setprio(HPRIO);
    if (hw < 3) {
      __builtin_amdgcn_s_sleep(SPRE);
      wait_sig((hw & 1) * 8, 2u);
      __builtin_amdgcn_raw_buffer_store_b32(tag, __builtin_amdgcn_make_buffer_rsrc(kvflag, 0, hw < 2 ? RK * 128 : 0, 0x00020000), lane * 256 + ((tmi ^ hw) & 1) * 128 + tmi * 4, 0, 16);
      if (hw == 0) STAMP(LAY2 ? 5 : 1, 16);
    }
    if (hw == 3) {
      __builtin_amdgcn_s_sleep(KVS);
      const unsigned kv0 = ld_ag(kvflag + (tmi % RK) * 32 + (lane & 15));
      PIN();
      const char* cnt = reinterpret_cast<const char*>(gran + GR_N + bid * 64 + 2);
      unsigned vk[4], vp = lbase + FLG + 8 + (lane & 7), pw = 0u;
#pragma unroll
      for (int j = 0; j < 4; ++j) { vk[j] = l16 + (j >> 1) * 4096u + (j & 1) * (VOFF - 2048u); asm volatile("" : "+v"(vk[j])); }
#define G4(a, b, c, d, sc) "s_nop 0\n\tglobal_load_lds_dwordx4 %" #a ", %4" sc "\n\tglobal_load_lds_dwordx4 %" #b ", %4 offset:1024" sc "\n\t"             \
                       "global_load_lds_dwordx4 %" #c ", %4 offset:2048" sc "\n\tglobal_load_lds_dwordx4 %" #d ", %4 offset:3072" sc "\n\t"
#define M4 "s_addk_i32 m0, 0x1000\n\t"
      auto stage = [&](const char* sp, unsigned dst) __attribute__((always_inline)) {
        asm volatile("s_mov_b32 m0, %5\n\t" G4(0, 0, 1, 1, " sc1") M4 G4(2, 2, 3, 3, " sc1")
                     :: "v"(vk[0]), "v"(vk[1]), "v"(vk[2]), "v"(vk[3]), "s"(sp), "s"(dst) : "m0", "scc", "memory");
      };
      auto count = [&]() __attribute__((always_inline)) {
        asm volatile("s_mov_b32 m0, %2\n\ts_nop 0\n\tglobal_load_lds_dword %0, %1 sc1" :: "v"(0u), "s"(cnt), "s"(lbase + FLG - 252) : "m0", "memory");
        cnt += 8;
      };
      auto freed = [&](unsigned n) __attribute__((always_inline)) {
        if (!none(pw < n)) {
#pragma unroll 1
          for (int i = 0; i < 4 * SPIN_MAX; ++i) {
            pw = *LDS_AT(const volatile unsigned char, vp);
            if (none(pw < n)) break;
            __builtin_amdgcn_s_sleep(W3SLP);
          }
        }
        asm volatile("" ::: "memory");
      };
      unsigned long long ord = (0xfedcba9875316420ull >> 4 & ~((1ull << 2 * (tmi & 6)) - 1ull)) | (0xfedcba9875316420ull & ((1ull << 2 * (tmi & 6)) - 1ull));
      ord ^= (tmi & 9) * 0x1111111111111111ull;
      unsigned seen;
      const char* kvb = reinterpret_cast<const char*>(KV) + (size_t)(tni * 2 + (tmi & 1)) * (64 * 2048);
      auto panel = [&]() __attribute__((always_inline)) {
        const unsigned pn = (unsigned)ord & 15u;
        ord >>= 4;
        if (__builtin_expect(!((seen >> pn) & 1u), 0)) {
#pragma unroll 1
          for (int i = 0; i < SPIN_MAX && !((seen >> pn) & 1u); ++i) seen = (unsigned)__ballot(ld_ag(kvflag + (tmi % RK) * 32 + (lane & 15)) == tag);
        }
        asm volatile("" ::: "memory");
        return kvb + pn * 8192u;
      };
      asm volatile("" : "+s"(ord), "+s"(kvb), "+s"(cnt));
      asm volatile("" : "+v"(vp), "+v"(pw));
      PIN();
      seen = (unsigned)__ballot(kv0 == tag);
      const char* sp = panel();
      wait_sig(16, 4u);
      stage(sp, lbase + 2 * KSB);
      count();
      const int ksp = LAY2 ? PNS / 2 : -1, ksg = LAY2 || !p1blk ? 64 : GF0;
      const u16* gp = gptr(wg, wu);
#pragma unroll 1
      for (int k = 1; k < 15; ++k) {
        if (__builtin_expect(k == ksp, 0)) {
          gu_setup();
          gate_up(xq);
          PIN();
          *LDS_AT(volatile unsigned char, lbase + FLG + 7) = (unsigned char)0x7f;
        }
        freed((unsigned)(0xba9876543211000ull >> 4 * k) & 15u);
        stage(sp + 2048, lbase + ((2 * k + 1) & 7) * KSB);
        if (__builtin_expect(k == 1, 0)) { cnt -= 4; count(); cnt -= 4; }
        sp = panel();
        stage(sp, lbase + ((2 * k + 2) & 7) * KSB);
        count();
        if (__builtin_expect(k == ksg || (unsigned)(k - ksg - GFD) < 3u, 0)) gather(gp, k == ksg ? 0 : k - ksg - GFD + 1);
      }
      freed(12u);
      glds16s<true>(csf + 4096 + tni * 128, l16, lbase + UGL);
      stage(sp + 2048, lbase + 7 * KSB);
      count();
      const unsigned vwo = (unsigned)((lane >> 3) * D_ + oni * 64 + ((lane & 7) ^ ((lane >> 3) & 6)) * 8) * 2u;
      constexpr unsigned vs = 8u * D_ * 2u - 1024u;
#pragma unroll 1
      for (int i = 0; i < 6; ++i) {
        const int ns = (i + 5) & 7;
        freed(i < 3 ? 12u : 10u + i);
        const char* wp = reinterpret_cast<const char*>(wo) + (size_t)ns * (128 * D_ * 2);
#pragma unroll
        for (int j = 0; j < 4; ++j)
          glds16x4<false>(wp + j * (32u * D_ * 2u), vwo, vwo + vs, vwo + 2 * vs, vwo + 3 * vs, lbase + ns * ASB + j * 4096);
      }
      asm volatile("s_waitcnt vmcnt(0)" ::: "memory");
      return;
    }
    if (!LAY2) {
      auto until = [&](unsigned n) {
        while ((__builtin_amdgcn_readfirstlane(*LDS_AT(const volatile unsigned, lbase + FLG + 8)) & 255u) < n) __builtin_amdgcn_s_sleep(16);
        asm volatile("" ::: "memory");
        PIN();
      };
      if (hw != 1) {
        constexpr int N2 = HAND_N + Y_N, X3N = 3 * D_ / 2, N0 = X3N + PS_N;
        const __amdgpu_buffer_rsrc_t rsi = __builtin_amdgcn_make_buffer_rsrc(hand + (hw ? GRAN_N : 2 * HAND_N - X3N), 0, (hw ? N2 : N0) * 4, 0x00020000);
        until(IAT);
#pragma unroll 1
        for (int i = 0; i < ((hw ? N2 : N0) / 4 + 16383) / 16384; ++i) {
          __builtin_amdgcn_raw_buffer_store_b128(u32x4{SENT, SENT, SENT, SENT}, rsi, ((i * 256 + bid) * 64 + lane) * 16, 0, 16);
          __builtin_amdgcn_s_sleep(ISLP);
        }
      }
      if (hw != 1) {
        const __amdgpu_buffer_rsrc_t rq = __builtin_amdgcn_make_buffer_rsrc(reinterpret_cast<char*>(KV) + QG + ((256 + bid) * 2 + (hw >> 1)) * 4096, 0, 4096, 0x00020000);
#pragma unroll
        for (int i = 0; i < 4; ++i) __builtin_amdgcn_raw_buffer_store_b128(u32x4{SENT, SENT, SENT, SENT}, rq, lane * 16 + i * 1024, 0, 16);
      }
      const u16* p = gptr(wg, wu);
#pragma unroll 1
      for (unsigned n = T0; n < T0 + 13u; ++n) {
        until(n);
        if (hw == 2 && n == TFL) {
          const int ln = fresh_lane() & 15;
          const unsigned p1 = ld_ag(gran + (ln < 4 ? GR_O + (((ln >> 1) * 16 + omi) * RO + oni % RO) * 64 + (ln & 1) * 32 :
                                            (ln < 8 ? GR_X + (tmi * RX + tni % RX) * 128 + (ln & 3) * 32 : GR_KV + ((16 + tni) * RK + tmi % RK) * 32)));
          asm volatile("" :: "v"(p1));
        }
        if (hw == 2 && n == TW) {
          const int ln = fresh_lane();
          unsigned long long a0, a1;
          CODE_ADDR("layers_kernel", a0); CODE_ADDR("end_marker_kernel", a1);
          const unsigned p1 = touch_code(a0, a1, 3 + loc, ln), p0 = touch(wo + (size_t)(384 + (loc / (32 / OMX)) * (256 / OMX) + ln) * D_ + oni * 64);
          asm volatile("" :: "v"(p0), "v"(p1));
        }
        {
          const int st = n == G0 + GW * ((hw + 2) % 3) ? 0 : (n >= G1 && (n - G1) % GS == 0u && (n - G1) / GS < 3u ? 1 + (n - G1) / GS : -1);
          if (st >= 0 && p1blk) gather(p, st);
        }
        if (n == UAT && hw == UHW) {
          u32x4 a[4][2], c[2];
#pragma unroll
          for (int i = 0; i < 4; ++i)
#pragma unroll
            for (int h = 0; h < 2; ++h) a[i][h] = *reinterpret_cast<const u32x4*>(wo + (size_t)(tni * 64 + tmi * 4 + i) * D_ + h * 512 + lane * 8);
#pragma unroll
          for (int h = 0; h < 2; ++h) c[h] = *reinterpret_cast<const u32x4*>(w_r + h * 512 + lane * 8);
          auto dot = [&](unsigned x, unsigned y, float z) { return __builtin_amdgcn_fdot2_f32_bf16(__builtin_bit_cast(bf16x2, x), __builtin_bit_cast(bf16x2, y), z, false); };
          PIN();
          __builtin_amdgcn_s_setprio(0);
#pragma unroll 1
          for (int i = 0; i < SPIN_MAX; ++i) {
            if (__builtin_amdgcn_readfirstlane(*LDS_AT(const volatile unsigned, lbase + FLG + 16)) >= UARR) break;
            __builtin_amdgcn_s_sleep(4);
          }
          asm volatile("s_waitcnt vmcnt(0)" ::: "memory");
          PIN();
          float wm = 0.f;
#pragma unroll
          for (int e = 0; e < 8; ++e) wm = dot(c[e >> 2][e & 3], 0x3f803f80u, wm);
          wm = wave_sum(wm);
          lds_wr(lbase + FLG + 60, __float_as_uint(wm));
          wm *= -1.0f / 1024.0f;
          f32x2 u[2], g[2];
#pragma unroll
          for (int i = 0; i < 4; ++i) {
            float p1 = 0.f, pw = 0.f, p2 = 0.f;
#pragma unroll
            for (int e = 0; e < 8; ++e) {
              const unsigned x = a[i][e >> 2][e & 3];
              p1 = dot(x, 0x3f803f80u, p1); pw = dot(x, c[e >> 2][e & 3], pw); p2 = dot(x, x, p2);
            }
            u[i >> 1][i & 1] = fmaf(p1, wm, pw); g[i >> 1][i & 1] = p2;
          }
          float us = fold4(u[0], u[1]), gs = fold4(g[0], g[1]);
          us = dpp_add<0x121, 0xf>(dpp_add<0x122, 0xf>(us)); gs = dpp_add<0x121, 0xf>(dpp_add<0x122, 0xf>(gs));
          __builtin_amdgcn_raw_buffer_store_b32(__float_as_uint((lane & 1) ? __builtin_amdgcn_sqrtf(gs) * (1.0f / 32.0f) : us),
              __builtin_amdgcn_make_buffer_rsrc(csf + 4096 + tni * 128, 0, 512, 0x00020000), (lane & 14) ? 512 : ((lane & 1) * 64 + tmi * 4 + (lane >> 4)) * 4, 0, 16);
        }
      }
      if (hw == 2 && bid == 0) {
        const __amdgpu_buffer_rsrc_t rsc = __builtin_amdgcn_make_buffer_rsrc(gran, 0, GR_N * 4, 0x00020000);
#pragma unroll 1
        for (int spin = 0; spin < SPIN_MAX; ++spin) {
          bool ok = true;
#pragma unroll
          for (int i = 0; i < 4; ++i) ok &= __builtin_amdgcn_raw_buffer_load_b32(rsc, (GR_ROW + (i * 64 + lane) * 32 + 1) * 4, 0, 16) == tag;
          if (__all(ok)) break;
          __builtin_amdgcn_s_sleep(8);
          asm volatile("" ::: "memory");
        }
        if (lane == 0) st_ag(gran, tag);
      }
      return;
    }
    unsigned* X2 = hand + GRAN_N + Y_N;
    unsigned* XS = X2 + X2_N;
    unsigned* P4 = XS + XS_N;
    unsigned* PA = P4 + P4_N;
    const __amdgpu_buffer_rsrc_t rsh = __builtin_amdgcn_make_buffer_rsrc(hand, 0, HAND_N * 4, 0x00020000);
    auto signal = [&](unsigned ph) {
      asm volatile("" ::: "memory");
      PIN();
      *LDS_AT(volatile unsigned char, lbase + FLG + 4 + hw) = (unsigned char)ph;
    };
    auto wait_all = [&](unsigned ph) {
#pragma unroll 1
      for (int i = 0; i < SPIN_MAX; ++i) {
        if (all_ge(__builtin_amdgcn_readfirstlane(*LDS_AT(const volatile unsigned, lbase + FLG + 4)), ph)) break;
        __builtin_amdgcn_s_sleep(WSLP);
      }
      asm volatile("" ::: "memory");
      PIN();
    };
    gu_setup();
    gate_up(xq);
    signal(hw == 2 ? 2u : 0x7fu);
    if (wid == 8) STAMP(dk, 2);
    if (hw < 2) {
#pragma unroll 1
      for (int i = 0; i < SPIN_MAX; ++i) {
        if (__builtin_amdgcn_readfirstlane(*LDS_AT(const volatile unsigned, lbase + FLG)) >= FSTART) break;
        __builtin_amdgcn_s_sleep(WSLP);
      }
      asm volatile("" ::: "memory");
      PIN();
      if (wid == 8) STAMP(dk, 10);
      u32x2 w[16];
      const __amdgpu_buffer_rsrc_t rd_ = __builtin_amdgcn_make_buffer_rsrc(const_cast<u16*>(wd), 0, D_ * F_ * 2, 0x00020000);
      auto ldw = [&](int c) __attribute__((always_inline)) {
#pragma unroll
        for (int f = 0; f < 16; ++f) w[f] = __builtin_amdgcn_raw_buffer_load_b64(rd_, lane * 8 + c * 512 + (f & 1) * 2048, (fg * 16 + (f & ~1)) * (D_ * 2) + hw * 1024, 0);
        PIN();
      };
      ldw(0);
      const unsigned pt = __builtin_amdgcn_raw_buffer_load_b32(rd_, (lane >> 2) * (D_ * 2) + (lane & 3) * 128 + 512, fg * 16 * (D_ * 2) + hw * 1024, 0);
      int yo = p1blk ? (GRAN_N + fg * D_ + hw * 512) * 4 + lane * 16 : HAND_N * 4;
      asm volatile("" : "+v"(yo));
      wait_all(2u);
      if (wid == 8) STAMP(dk, 3);
      const s16x4 a = act_diag(0, rmd, sums2(1024));
      const u32x4 y0 = down(a, w);
      PIN();
      ldw(1);
      __builtin_amdgcn_raw_buffer_store_b128(y0, rsh, yo, 0, 16);
      PIN();
      __builtin_amdgcn_raw_buffer_store_b128(down(a, w), rsh, yo + 1024, 0, 16);
      asm volatile("" :: "v"(pt));
      if (wid == 8) STAMP(dk, 4);
    }
    PIN();
    [&]() __attribute__((always_inline)) {
    const int lane = fresh_lane();
    const float rmb = __uint_as_float(rmd << 16);
    auto first_words = [&](int src, auto nc) __attribute__((always_inline)) {
#pragma unroll 1
      for (int spin = 0; spin < SPIN_MAX; ++spin) {
        unsigned mx = 0u;
#pragma unroll
        for (int i = 0; i < decltype(nc)::value; ++i) mx = umax(mx, __builtin_amdgcn_raw_buffer_load_b32(rsh, src, i * (56 * D_ * 4), 16));
        if (none(mx == SENT)) break;
        __builtin_amdgcn_s_sleep(PSLP);
        asm volatile("" ::: "memory");
      }
    };
    if (hw == 2) {
      wait_all(2u);
      if (own) {
        const float rden = __uint_as_float(rmd & 0xffff0000u), rrin = frcp(rden);
        const int col = ow * 16 + (lane & 3) * 4 + (lane >> 4);
        const unsigned xo2 = ld_ag(reinterpret_cast<const unsigned*>(A + (size_t)idx * D_ + (col & ~1)));
        __builtin_amdgcn_s_sleep(PRE1);
        first_words((GRAN_N + (lane < 56 ? lane : 0) * D_ + ow * 16) * 4, std::integral_constant<int, 4>{});
        const float y = y_sum(rsh, std::integral_constant<int, 2>{});
        const unsigned rk = best >> 16;
        const float rv = __uint_as_float(((rk & 0x8000u) ? (rk & 0x7fffu) : (~rk & 0xffffu)) << 16);
        const float rs = rbf(frcp(1.0f + __expf(-rv)));
        const float x1v = __uint_as_float((col & 1) ? xo2 & 0xffff0000u : xo2 << 16);
        float h0 = x1v, h1 = x1v;
        ln_apply2(h0, h1, rmb, rden, rrin);
        const float x2v = rbf(x1v + rbf(h0 + rbf(rbf(y) * rs)));
        const auto pr = __builtin_amdgcn_permlane16_swap(__float_as_uint(x2v), __float_as_uint(x2v), false, false);
        __builtin_amdgcn_raw_buffer_store_b32(f2word(__uint_as_float((pr[0] >> 16) | pr[1])), rsh, (lane & 28) ? HAND_N * 4 : (int)(X2 - hand) * 4 + col * 2, 0, 16);
        const float d = x2v - rmb;
        const float sd = wave_sum(d), sq = wave_sum(d * d);
        if (lane < 2) st_ag(XS + ow * 2 + lane, f2word(lane ? sq : sd));
        STAMP(dk, 5);
      }
    }
    {
      const int ks = kcap ^ (tmi & 8), g4 = lane >> 4;
      const int src = (int)(X2 - hand) * 4 + ks * 128 + g4 * 16;
      int dst = (lane & 12) ? HAND_N * 4 : (int)((P4 - hand) + (tni * 16 + ks) * 192 + (hw ^ (hw < 2 ? ((lane >> 1) ^ tmi) & 1 : 0)) * 64 + (lane & 2) * 16 + (lane & 1) * 8 + (g4 >> 1) * 16 + (g4 & 1) * 4) * 4;
      asm volatile("" : "+v"(dst));
      u32x4 x0, x1;
      if (!(hw == 2 && own)) __builtin_amdgcn_s_sleep(PRE4);
#pragma unroll 1
      for (int spin = 0; spin < SPIN_MAX; ++spin) {
        x0 = __builtin_amdgcn_raw_buffer_load_b128(rsh, src, 0, 16); x1 = __builtin_amdgcn_raw_buffer_load_b128(rsh, src + 64, 0, 16);
        if (none(umax(umax(umax(x0[0], x0[1]), umax(x0[2], x0[3])), umax(umax(x1[0], x1[1]), umax(x1[2], x1[3]))) == SENT)) break;
        __builtin_amdgcn_s_sleep(PSLP);
        asm volatile("" ::: "memory");
      }
      f32x4 d[4];
#pragma unroll
      for (int i = 0; i < 4; ++i) d[i] = MFMA16(sl[2 * i], __builtin_bit_cast(s16x8, x0), (f32x4{0.f, 0.f, 0.f, 0.f}));
#pragma unroll
      for (int i = 0; i < 4; ++i) d[i] = MFMA16(sl[2 * i + 1], __builtin_bit_cast(s16x8, x1), d[i]);
      u32x4 o;
#pragma unroll
      for (int e = 0; e < 4; ++e) o[e] = f2word((lane & 2) ? ((lane & 1) ? d[3][e] : d[2][e]) : ((lane & 1) ? d[1][e] : d[0][e]));
      __builtin_amdgcn_raw_buffer_store_b128(o, rsh, dst, 0, 16);
      PIN();
      if (wid == 8) STAMP(dk, 6);
    }
    if (hw == 2) {
      const int col = (lane & 15) * 4 + (lane >> 4), src = (int)((P4 - hand) + (tni * 16 + (lane >> 4)) * 192 + (lane & 15) * 4) * 4;
      const __amdgpu_buffer_rsrc_t rsq = __builtin_amdgcn_make_buffer_rsrc(hand, 0, fast ? HAND_N * 4 : 0, 0x00020000);
      if (own) {
#pragma unroll
        for (int i = 0; i < 14; ++i)
          __builtin_amdgcn_raw_buffer_store_b128(u32x4{SENT, SENT, SENT, SENT}, rsh, (GRAN_N + (i * 16 + (lane >> 2)) * D_ + ow * 16 + (lane & 3) * 4) * 4, 0, 16);
      }
      float cs[3], val[3];
#pragma unroll
      for (int p = 0; p < 3; ++p) cs[p] = __uint_as_float(ld_ag(reinterpret_cast<const unsigned*>(csf) + p * 1024 + n0 + col));
      u32x2 xs;
      __builtin_amdgcn_s_sleep(PRE7);
      first_words((int)((P4 - hand) + (tni * 16 + (lane & 15)) * 192 + ((lane >> 4) % 3) * 64) * 4, std::integral_constant<int, 1>{});
#pragma unroll 1
      for (int spin = 0; spin < SPIN_MAX; ++spin) {
        u32x4 v[3][4];
        xs = __builtin_amdgcn_raw_buffer_load_b64(rsh, (int)((XS - hand) + lane * 2) * 4, 0, 16);
#pragma unroll
        for (int p = 2; p >= 0; --p)
#pragma unroll
          for (int i = 0; i < 4; ++i) v[p][i] = __builtin_amdgcn_raw_buffer_load_b128(p ? rsh : rsq, src + p * 256, i * (4 * 192 * 4), 16);
        auto part = [&](int p) __attribute__((always_inline)) {
          float z[4];
#pragma unroll
          for (int e = 0; e < 4; ++e) {
            z[e] = fadd(fadd(__uint_as_float(v[p][0][e]), __uint_as_float(v[p][1][e])), fadd(__uint_as_float(v[p][2][e]), __uint_as_float(v[p][3][e])));
          }
          val[p] = fold16(f32x2{z[0], z[1]}, f32x2{z[2], z[3]});
        };
        part(2); part(1);
        val[0] = 0.f;
        if (fast) part(0);
        const float chk = fadd(fadd(fadd(val[0], val[1]), fadd(val[2], __uint_as_float(xs[0]))), __uint_as_float(xs[1]));
        if (none(chk != chk)) break;
        unsigned mx = umax(xs[0], xs[1]);
#pragma unroll
        for (int i = 0; i < 12; ++i) mx = umax(umax(umax(mx, v[i >> 2][i & 3][0]), v[i >> 2][i & 3][1]), umax(v[i >> 2][i & 3][2], v[i >> 2][i & 3][3]));
        if (none(mx == SENT)) break;
        __builtin_amdgcn_s_sleep(PSLP);
        asm volatile("" ::: "memory");
      }
      const auto sw = __builtin_amdgcn_permlane32_swap(xs[0], xs[1], false, false);
      float sm = __uint_as_float(sw[0]) + __uint_as_float(sw[1]);
      sm = dpp_add<0x142, 0xa>(dpp_add<0x118, 0xf>(dpp_add<0x114, 0xf>(dpp_add<0x112, 0xf>(dpp_add<0x111, 0xf>(sm)))));
      const float dm = __int_as_float(__builtin_amdgcn_readlane(__float_as_int(sm), 31)) * (0.25f / 1024.0f);
      const float var = fmaxf(__int_as_float(__builtin_amdgcn_readlane(__float_as_int(sm), 63)) * (0.25f / 1024.0f) - dm * dm, 0.f);
      const float mb = rbf(rmb + dm);
      const float rin = frcp(rbf(__builtin_amdgcn_sqrtf(rbf(rbf(var) + 1e-5f))));
      const unsigned kv = pack2((val[1] - mb * cs[1]) * rin, (val[2] - mb * cs[2]) * rin);
      *LDS_AT(u16, lbase + TAB + 128 + kpos(col) * 2) = (u16)kv;
      *LDS_AT(u16, lbase + TAB + 256 + col * 2) = (u16)(kv >> 16);
      if (fast) *LDS_AT(u16, lbase + TAB + kpos(col) * 2) = f2bf((val[0] - mb * cs[0]) * rin * QSCALE);
      signal(7u);
      STAMP(dk, 7);
      return;
    }
    if (!fast) return;
    const int head = tni * 2 + hw;
    const unsigned tb = lbase + TAB + hw * 64;
    {
      const __amdgpu_buffer_rsrc_t rs = __builtin_amdgcn_make_buffer_rsrc(KV, 0, 2 * S_ * D_ * 2, 0x00020000);
      const int src = (head * 64 + tmi * 4) * 2048 + lane * 16;
      auto ld = [&](int i, unsigned off) { return __builtin_bit_cast(s16x8, __builtin_amdgcn_raw_buffer_load_b128(rs, src + (i & 3) * 1024, (i >> 3) * 65536 + ((i >> 2) & 1) * 4096 + off, 16)); };
#pragma unroll 1
      for (int i = 0; i < SPIN_MAX; ++i)
        if (none(ld_ag(kvflag + ((tmi & ~1) + hw) * 32 + tmi + (lane & 8)) != tag)) break;
      asm volatile("" ::: "memory");
      s16x8 f[16];
#pragma unroll
      for (int i = 0; i < 16; ++i) f[i] = ld(i, 0u);
      PIN();
      const bool on = ((lane ^ (lane >> 4)) & 1) == 0;
      const unsigned pl = lbase + RED + hw * 1024;
      unsigned pw = pl + (lane >> 4) * 64 + (lane & 15) * 2, pr = pl + (on ? (lane >> 5) * 16 : 512), qr = on ? tb + (lane >> 5) * 16 : pl + 512;
      const int gone = (((idx >> 7) & 7) == tmi) ? ((idx >> 10) * 2 + (kpos(idx & 31) >> 4)) * 64 + ((idx >> 5) & 3) * 16 + (kpos(idx & 31) & 15) : -1;
      asm volatile("" : "+v"(pw), "+v"(pr), "+v"(qr));
      *LDS_AT(u32x2, pl + 512 + lane * 8) = u32x2{0u, 0u};
      wait_all(7u);
      u32x4 q[2];
#pragma unroll
      for (int sx = 0; sx < 2; ++sx) {
        q[sx] = *LDS_AT(const u32x4, qr + sx * 32);
      }
      float sc[4];
#pragma unroll
      for (int i = 0; i < 8; ++i) {
        f32x4 d = MFMA16(__builtin_bit_cast(s16x8, q[0]), f[2 * i], (f32x4{0.f, 0.f, 0.f, 0.f}));
        d = MFMA16(__builtin_bit_cast(s16x8, q[1]), f[2 * i + 1], d);
        PIN();
        f[2 * i] = ld(2 * i, VOFF); f[2 * i + 1] = ld(2 * i + 1, VOFF);
        PIN();
#pragma unroll
        for (int r = 0; r < 2; ++r) sc[(i >> 2) * 2 + r] = (i & 3) == 0 || (lane >> 4) == (i & 3) ? d[r] : sc[(i >> 2) * 2 + r];
      }
      if (gone >= 0) {
#pragma unroll
        for (int r = 0; r < 4; ++r) sc[r] = r * 64 + lane == gone ? -INFINITY : sc[r];
      }
      const float mx = wave_fmax(fmaxf(fmaxf(sc[0], sc[1]), fmaxf(sc[2], sc[3])));
      const unsigned p0 = pack2(__builtin_amdgcn_exp2f(sc[0] - mx), __builtin_amdgcn_exp2f(sc[1] - mx)), p1 = pack2(__builtin_amdgcn_exp2f(sc[2] - mx), __builtin_amdgcn_exp2f(sc[3] - mx));
      *LDS_AT(u16, pw) = (u16)p0; *LDS_AT(u16, pw + 32) = (u16)(p0 >> 16);
      *LDS_AT(u16, pw + 256) = (u16)p1; *LDS_AT(u16, pw + 288) = (u16)(p1 >> 16);
      asm volatile("" ::: "memory");
      const float ls = wave_sum((__uint_as_float(p0 << 16) + __uint_as_float(p0 & 0xffff0000u)) + (__uint_as_float(p1 << 16) + __uint_as_float(p1 & 0xffff0000u)));
      f32x4 o = {0.f, 0.f, 0.f, 0.f};
#pragma unroll
      for (int i = 0; i < 16; ++i) o = MFMA16(*LDS_AT(const s16x8, pr + i * 32), f[i], o);
      int pv = __float_as_int((lane & 16) ? o[1] : o[0]);
      asm volatile("s_nop 0\n\tv_writelane_b32 %0, %1, 32\n\tv_writelane_b32 %0, %2, 33" : "+v"(pv) : "s"(ls), "s"(mx));
      __builtin_amdgcn_raw_buffer_store_b32(f2word(__int_as_float(pv)), rsh, lane < 34 ? (int)((PA - hand) + (head * 8 + tmi) * 64 + lane) * 4 : HAND_N * 4, 0, 16);
    }
    if (wid == 8) STAMP(dk, 8);
    if (tmi != ((idx >> 7) & 7)) return;
    {
      const int src = (int)((PA - hand) + head * 512 + (lane < 34 ? lane : 0)) * 4;
      const unsigned ta = tb + (lane & 31) * 2;
      const float so = wave_sum(lane < 32 ? bf2f(*LDS_AT(const u16, ta)) * bf2f(*LDS_AT(const u16, ta + 128)) : 0.f);
      const float vo = lane < 32 ? bf2f(*LDS_AT(const u16, ta + 256)) : 1.0f;
      unsigned v[8];
      __builtin_amdgcn_s_sleep(PRE5);
#pragma unroll 1
      for (int spin = 0; spin < SPIN_MAX; ++spin) {
        unsigned mx = 0u;
#pragma unroll
        for (int i = 0; i < 8; ++i) v[i] = __builtin_amdgcn_raw_buffer_load_b32(rsh, src + i * 256, 0, 16);
#pragma unroll
        for (int i = 0; i < 8; ++i) mx = umax(mx, v[i]);
        if (none(mx == SENT)) break;
        __builtin_amdgcn_s_sleep(PSLP);
        asm volatile("" ::: "memory");
      }
      float m8[8], mx = so;
#pragma unroll
      for (int i = 0; i < 8; ++i) { m8[i] = __int_as_float(__builtin_amdgcn_readlane((int)v[i], 33)); mx = fmaxf(mx, m8[i]); }
      float acc = __builtin_amdgcn_exp2f(so - mx) * vo;
#pragma unroll
      for (int i = 0; i < 8; ++i) acc += __builtin_amdgcn_exp2f(m8[i] - mx) * __uint_as_float(v[i]);
      const float l = __int_as_float(__builtin_amdgcn_readlane(__float_as_int(acc), 32));
      const float ov = acc / l;
      const float o1 = __int_as_float(__builtin_amdgcn_update_dpp(0, __float_as_int(ov), 0x55, 0xf, 0xf, false));
      const float o2 = __int_as_float(__builtin_amdgcn_update_dpp(0, __float_as_int(ov), 0xaa, 0xf, 0xf, false));
      const float o3 = __int_as_float(__builtin_amdgcn_update_dpp(0, __float_as_int(ov), 0xff, 0xf, 0xf, false));
      const __amdgpu_buffer_rsrc_t rso = __builtin_amdgcn_make_buffer_rsrc(Of, 0, S_ * D_ * 2, 0x00020000);
      if (lane < 32 && (lane & 3) == 0)
        __builtin_amdgcn_raw_buffer_store_b64(u32x2{pack2(ov, o1), pack2(o2, o3)}, rso,
            (((idx >> 5) * 32 + head) * 1024 + (lane >> 4) * 512 + (((lane >> 2) & 1) * 32 + (idx & 31)) * 8 + ((lane >> 3) & 1) * 4) * 2, 0, 16);
      asm volatile("s_waitcnt vmcnt(0)" ::: "memory");
      if (lane < RO) st_ag(gran + GR_O + ((16 + (idx >> 7)) * RO + lane) * 64 + 32 + head, tag);
      STAMP(dk, 9);
    }
    }();
    }();
    __builtin_amdgcn_s_setprio(0);
    if (LAY2 || hw != UHW) asm volatile("s_waitcnt vmcnt(0)" ::: "memory");
    PIN();
    ARGS_HERE;
    u32x2 w[16];
    auto ld_wd = [&]() __attribute__((always_inline)) {
      const __amdgpu_buffer_rsrc_t rd_ = __builtin_amdgcn_make_buffer_rsrc(const_cast<u16*>(wd), 0, D_ * F_ * 2, 0x00020000);
#pragma unroll
      for (int f = 0; f < 16; ++f) w[f] = __builtin_amdgcn_raw_buffer_load_b64(rd_, t * 8 + (f & 1) * 2048, (fg * 16 + (f & ~1)) * (D_ * 2), 0);
      PIN();
    };
    float fnum = 0.f, fvar = 0.f;
    u32x2 wq = {0u, 0u};
    const int rrow = omi * 128 + (oni >> 3) * 64 + (hw & 1) * 32;
    if (LAY2) {
      ld_wd();
      wq = *reinterpret_cast<const u32x2*>(w_r + hw * 256 + (lane & 3) * 64 + (lane >> 2) * 4);
      if (!p1blk) {
        const int fr = rrow + (lane & 7) * 4 + (lane >> 4);
        const double s1 = ld_ag(stats + fr), s2 = ld_ag(stats + S_ + fr), sw = ld_ag(stats + 2 * S_ + fr);
        lds_wr(lbase + SPM + 132, __float_as_uint(wsum));
        float mean, var, mb, den;
        ln_from_sums(s1, s2, mean, var, mb, den);
        const float g1 = 1.0f + frcp(den);
        fnum = fr == idx ? __uint_as_float(0x7fc00000u) : fmaf(-mean, wsum, (float)sw) * g1;
        fvar = var * g1 * g1;
      }
      asm volatile("" : "+v"(fnum), "+v"(fvar));
      PIN();
    }
    if (hw == 3) {
      const int nfl = (LAY2 && (idx >> 7) == omi) ? 64 : 32;
      auto pfl = (__attribute__((address_space(1))) const unsigned*)(gran + GR_O + (((LAY2 ? 16 : 0) + omi) * RO + oni % RO) * 64 + (lane < nfl ? lane : 0));
      constexpr unsigned vs = 8u * D_ * 2u - 1024u;
      unsigned vw0 = (unsigned)((lane >> 3) * D_ + oni * 64 + ((lane & 7) ^ ((lane >> 3) & 6)) * 8) * 2u, vw1 = vw0 + vs, vw2 = vw0 + 2 * vs, vw3 = vw0 + 3 * vs;
      asm volatile("" : "+v"(pfl), "+v"(vw0), "+v"(vw1), "+v"(vw2), "+v"(vw3));
      auto wait_arr = [&](unsigned n) {
#pragma unroll 1
        for (int i = 0; i < 4 * SPIN_MAX; ++i) {
          if (__builtin_amdgcn_readfirstlane(*LDS_AT(const volatile unsigned, lbase + FLG + 16)) >= n) break;
          __builtin_amdgcn_s_sleep(1);
        }
        asm volatile("" ::: "memory");
        PIN();
      };
      wait_arr(12u);
      unsigned fl;
      asm volatile("global_load_dword %0, %1, off sc1" : "=v"(fl) : "v"(pfl) : "memory");
#pragma unroll 1
      for (int i = 0; i < 4 * SPIN_MAX; ++i) {
        if (__builtin_amdgcn_readfirstlane(*LDS_AT(const volatile unsigned char, lbase + FLG + 24)) >= 8u) break;
        __builtin_amdgcn_s_sleep(1);
      }
      asm volatile("" ::: "memory");
      PIN();
#pragma unroll
      for (int j = 12; j < 20; ++j)
        glds16x4<false>(reinterpret_cast<const char*>(wo) + j * (32u * D_ * 2u), vw0, vw1, vw2, vw3, lbase + j * 4096);
      unsigned keep;
      asm volatile("s_mov_b32 %0, m0\n\ts_mov_b32 m0, %3\n\ts_nop 2\n\tglobal_load_lds_dword %1, %2 sc1\n\ts_mov_b32 m0, %0"
                   : "=&s"(keep) : "v"(0u), "s"(reinterpret_cast<const char*>(gran + GR_N + bid * 64 + 39)), "s"(lbase + FLG - 252) : "memory");
      asm volatile("s_waitcnt vmcnt(33)" : "+v"(fl) :: "memory");
      if (__builtin_amdgcn_ballot_w64(fl != tag)) {
        __builtin_amdgcn_s_sleep(CSL1);
#pragma unroll 1
        for (int spin = 0; spin < SPIN_MAX; ++spin) {
          if (!__builtin_amdgcn_ballot_w64(__hip_atomic_load(pfl, __ATOMIC_RELAXED, __HIP_MEMORY_SCOPE_AGENT) != tag)) break;
          __builtin_amdgcn_s_sleep(CSLP);
        }
      }
      asm volatile("" ::: "memory");
      PIN();
    }
    u32x4 pv[4];
    asm volatile("" : "=v"(pv[0]), "=v"(pv[1]), "=v"(pv[2]), "=v"(pv[3]));
    if (LAY2 && !p1blk && hw < 2) {
      const __amdgpu_buffer_rsrc_t rsp = __builtin_amdgcn_make_buffer_rsrc(hand + 2 * HAND_N + PP_O, 0, H_ * S_ * 4, 0x00020000);
      const int src = ((lane >> 3) * S_ + rrow + (lane & 7) * 4) * 4;
#pragma unroll 1
      for (int i = 0; i < 4 * SPIN_MAX; ++i) {
        if (__builtin_amdgcn_readfirstlane(*LDS_AT(const volatile unsigned char, lbase + FLG + 24)) >= 8u) break;
        __builtin_amdgcn_s_sleep(2);
      }
#pragma unroll 1
      for (int spin = 0; spin < SPIN_MAX; ++spin) {
        asm volatile("" ::: "memory");
        unsigned mx = 0u;
#pragma unroll
        for (int i = 0; i < 4; ++i) pv[i] = __builtin_amdgcn_raw_buffer_load_b128(rsp, src, i * (8 * S_ * 4), 16);
#pragma unroll
        for (int i = 0; i < 4; ++i) mx = umax(umax(umax(mx, pv[i][0]), pv[i][1]), umax(pv[i][2], pv[i][3]));
        if (none(mx == SENT)) break;
        __builtin_amdgcn_s_sleep(PPSLP);
      }
    }
    asm volatile("s_waitcnt lgkmcnt(0)" ::: "memory");
    __builtin_amdgcn_s_barrier();
    if (!LAY2) {
      PIN();
      gu_setup();
      dad += 1024;
      gate_up(u32x2{0x3f803f80u, 0x3f803f80u});
      PIN();
      asm volatile(AGAIN "\ngate_up_code:\n\t"
                 "s_mov_b32 s88, 0x01000100\n\ts_mov_b32 s89, 0x03020302\n\tv_perm_b32 v92, v100, v100, s88\n\tv_perm_b32 v93, v100, v100, s89\n\t"
                 "v_perm_b32 v94, v101, v101, s88\n\tv_perm_b32 v95, v101, v101, s89\n\ts_nop 0\n\tv_and_b32_dpp v96, v92, v102 quad_perm:[0,0,0,0] row_mask:0xf bank_mask:0xf\n\t"
                 "v_and_b32_dpp v97, v92, v103 quad_perm:[0,0,0,0] row_mask:0xf bank_mask:0xf\n\t"
                 "v_and_b32_dpp v100, v93, v102 quad_perm:[0,0,0,0] row_mask:0xf bank_mask:0xf\n\t"
                 "v_and_b32_dpp v101, v93, v103 quad_perm:[0,0,0,0] row_mask:0xf bank_mask:0xf\n\tv_mfma_f32_4x4x4_16b_bf16 v[84:87], v[96:97], v[104:105], 0\n\t"
                 "v_mfma_f32_4x4x4_16b_bf16 v[88:91], v[96:97], v[106:107], 0\n\tv_and_b32_dpp v96, v94, v102 quad_perm:[0,0,0,0] row_mask:0xf bank_mask:0xf\n\t"
                 "v_and_b32_dpp v97, v94, v103 quad_perm:[0,0,0,0] row_mask:0xf bank_mask:0xf\n\t"
                 "v_mfma_f32_4x4x4_16b_bf16 v[84:87], v[100:101], v[108:109], v[84:87]\n\tv_mfma_f32_4x4x4_16b_bf16 v[88:91], v[100:101], v[110:111], v[88:91]\n\t"
                 "v_and_b32_dpp v100, v95, v102 quad_perm:[0,0,0,0] row_mask:0xf bank_mask:0xf\n\t"
                 "v_and_b32_dpp v101, v95, v103 quad_perm:[0,0,0,0] row_mask:0xf bank_mask:0xf\n\tv_mfma_f32_4x4x4_16b_bf16 v[84:87], v[96:97], v[112:113], v[84:87]\n\t"
                 "v_mfma_f32_4x4x4_16b_bf16 v[88:91], v[96:97], v[114:115], v[88:91]\n\tv_and_b32_dpp v96, v92, v102 quad_perm:[1,1,1,1] row_mask:0xf bank_mask:0xf\n\t"
                 "v_and_b32_dpp v97, v92, v103 quad_perm:[1,1,1,1] row_mask:0xf bank_mask:0xf\n\t"
                 "v_mfma_f32_4x4x4_16b_bf16 v[84:87], v[100:101], v[116:117], v[84:87]\n\tv_mfma_f32_4x4x4_16b_bf16 v[88:91], v[100:101], v[118:119], v[88:91]\n\t"
                 "v_and_b32_dpp v100, v93, v102 quad_perm:[1,1,1,1] row_mask:0xf bank_mask:0xf\n\t"
                 "v_and_b32_dpp v101, v93, v103 quad_perm:[1,1,1,1] row_mask:0xf bank_mask:0xf\n\tv_mfma_f32_4x4x4_16b_bf16 v[84:87], v[96:97], v[120:121], v[84:87]\n\t"
                 "v_mfma_f32_4x4x4_16b_bf16 v[88:91], v[96:97], v[122:123], v[88:91]\n\tv_and_b32_dpp v96, v94, v102 quad_perm:[1,1,1,1] row_mask:0xf bank_mask:0xf\n\t"
                 "v_and_b32_dpp v97, v94, v103 quad_perm:[1,1,1,1] row_mask:0xf bank_mask:0xf\n\t"
                 "v_mfma_f32_4x4x4_16b_bf16 v[84:87], v[100:101], v[124:125], v[84:87]\n\tv_mfma_f32_4x4x4_16b_bf16 v[88:91], v[100:101], v[126:127], v[88:91]\n\t"
                 "v_and_b32_dpp v100, v95, v102 quad_perm:[1,1,1,1] row_mask:0xf bank_mask:0xf\n\t"
                 "v_and_b32_dpp v101, v95, v103 quad_perm:[1,1,1,1] row_mask:0xf bank_mask:0xf\n\tv_mfma_f32_4x4x4_16b_bf16 v[84:87], v[96:97], v[128:129], v[84:87]\n\t"
                 "v_mfma_f32_4x4x4_16b_bf16 v[88:91], v[96:97], v[130:131], v[88:91]\n\tv_and_b32_dpp v96, v92, v102 quad_perm:[2,2,2,2] row_mask:0xf bank_mask:0xf\n\t"
                 "v_and_b32_dpp v97, v92, v103 quad_perm:[2,2,2,2] row_mask:0xf bank_mask:0xf\n\t"
                 "v_mfma_f32_4x4x4_16b_bf16 v[84:87], v[100:101], v[132:133], v[84:87]\n\tv_mfma_f32_4x4x4_16b_bf16 v[88:91], v[100:101], v[134:135], v[88:91]\n\t"
                 "v_and_b32_dpp v100, v93, v102 quad_perm:[2,2,2,2] row_mask:0xf bank_mask:0xf\n\t"
                 "v_and_b32_dpp v101, v93, v103 quad_perm:[2,2,2,2] row_mask:0xf bank_mask:0xf\n\tv_mfma_f32_4x4x4_16b_bf16 v[84:87], v[96:97], v[136:137], v[84:87]\n\t"
                 "v_mfma_f32_4x4x4_16b_bf16 v[88:91], v[96:97], v[138:139], v[88:91]\n\tv_and_b32_dpp v96, v94, v102 quad_perm:[2,2,2,2] row_mask:0xf bank_mask:0xf\n\t"
                 "v_and_b32_dpp v97, v94, v103 quad_perm:[2,2,2,2] row_mask:0xf bank_mask:0xf\n\t"
                 "v_mfma_f32_4x4x4_16b_bf16 v[84:87], v[100:101], v[140:141], v[84:87]\n\tv_mfma_f32_4x4x4_16b_bf16 v[88:91], v[100:101], v[142:143], v[88:91]\n\t"
                 "v_and_b32_dpp v100, v95, v102 quad_perm:[2,2,2,2] row_mask:0xf bank_mask:0xf\n\t"
                 "v_and_b32_dpp v101, v95, v103 quad_perm:[2,2,2,2] row_mask:0xf bank_mask:0xf\n\tv_mfma_f32_4x4x4_16b_bf16 v[84:87], v[96:97], v[144:145], v[84:87]\n\t"
                 "v_mfma_f32_4x4x4_16b_bf16 v[88:91], v[96:97], v[146:147], v[88:91]\n\tv_and_b32_dpp v96, v92, v102 quad_perm:[3,3,3,3] row_mask:0xf bank_mask:0xf\n\t"
                 "v_and_b32_dpp v97, v92, v103 quad_perm:[3,3,3,3] row_mask:0xf bank_mask:0xf\n\t"
                 "v_mfma_f32_4x4x4_16b_bf16 v[84:87], v[100:101], v[148:149], v[84:87]\n\tv_mfma_f32_4x4x4_16b_bf16 v[88:91], v[100:101], v[150:151], v[88:91]\n\t"
                 "v_and_b32_dpp v100, v93, v102 quad_perm:[3,3,3,3] row_mask:0xf bank_mask:0xf\n\t"
                 "v_and_b32_dpp v101, v93, v103 quad_perm:[3,3,3,3] row_mask:0xf bank_mask:0xf\n\tv_mfma_f32_4x4x4_16b_bf16 v[84:87], v[96:97], v[152:153], v[84:87]\n\t"
                 "v_mfma_f32_4x4x4_16b_bf16 v[88:91], v[96:97], v[154:155], v[88:91]\n\tv_and_b32_dpp v96, v94, v102 quad_perm:[3,3,3,3] row_mask:0xf bank_mask:0xf\n\t"
                 "v_and_b32_dpp v97, v94, v103 quad_perm:[3,3,3,3] row_mask:0xf bank_mask:0xf\n\t"
                 "v_mfma_f32_4x4x4_16b_bf16 v[84:87], v[100:101], v[156:157], v[84:87]\n\tv_mfma_f32_4x4x4_16b_bf16 v[88:91], v[100:101], v[158:159], v[88:91]\n\t"
                 "v_and_b32_dpp v100, v95, v102 quad_perm:[3,3,3,3] row_mask:0xf bank_mask:0xf\n\t"
                 "v_and_b32_dpp v101, v95, v103 quad_perm:[3,3,3,3] row_mask:0xf bank_mask:0xf\n\tv_mfma_f32_4x4x4_16b_bf16 v[84:87], v[96:97], v[160:161], v[84:87]\n\t"
                 "v_mfma_f32_4x4x4_16b_bf16 v[88:91], v[96:97], v[162:163], v[88:91]\n\ts_nop 1\n\t"
                 "v_mfma_f32_4x4x4_16b_bf16 v[84:87], v[100:101], v[164:165], v[84:87]\n\tv_mfma_f32_4x4x4_16b_bf16 v[88:91], v[100:101], v[166:167], v[88:91]\n\t"
                 "s_nop 4\n\tv_permlane32_swap_b32 v84, v88\n\tv_permlane32_swap_b32 v85, v89\n\tv_permlane32_swap_b32 v86, v90\n\tv_permlane32_swap_b32 v87, v91\n\t"
                 "v_add_f32 v84, v84, v88\n\tv_add_f32 v85, v85, v89\n\tv_add_f32 v86, v86, v90\n\tv_add_f32 v87, v87, v91\n\ts_nop 0\n\t"
                 "v_permlane16_swap_b32 v84, v85\n\tv_permlane16_swap_b32 v86, v87\n\ts_nop 0\n\tv_add_f32 v84, v84, v85\n\tv_add_f32 v86, v86, v87\n\ts_nop 1\n\t"
                 "v_add_f32_dpp v84, v84, v84 row_ror:8 row_mask:0xf bank_mask:0xf\n\tv_add_f32_dpp v86, v86, v86 row_ror:8 row_mask:0xf bank_mask:0xf\n\ts_nop 0\n\t"
                 "v_add_f32_dpp v84, v84, v84 row_ror:4 row_mask:0xf bank_mask:0xf\n\tv_add_f32_dpp v86, v86, v86 row_ror:4 row_mask:0xf bank_mask:0xf\n\t"
                 "ds_write2_b32 v99, v84, v86 offset1:8\n\ts_setpc_b64 s[92:93]\n\t"
                   :: "{s[0:1]}"(ap + 1), "{s2}"(bid), "{v0}"(wid * 64), KEPT() : "s90", "s91", "scc", "memory");
      __builtin_unreachable();
    }
    {
    constexpr int dk = 8;
    constexpr int BEST = FLG + 40, MEET = FLG + 48;
    __builtin_amdgcn_s_setprio(HPRIO);
    if (hw == 0) STAMP(dk, 0);
    const unsigned tb1 = (unsigned)__builtin_amdgcn_s_memrealtime();
    unsigned* hand2 = hand + HAND_N;
    const __amdgpu_buffer_rsrc_t rsh = __builtin_amdgcn_make_buffer_rsrc(hand2, 0, (HAND_N + PS_N) * 4, 0x00020000);
    const __amdgpu_buffer_rsrc_t rsx = __builtin_amdgcn_make_buffer_rsrc(xout, 0, 3 * D_ * 2, 0x00020000);
    constexpr int OOR = (HAND_N + PS_N) * 4;
    if (!p1blk && hw < 2) {
      f32x2 a[2], c[2];
#pragma unroll
      for (int e = 0; e < 4; ++e) {
        a[e >> 1][e & 1] = (__uint_as_float(pv[0][e] << 16) + __uint_as_float(pv[1][e] << 16)) + (__uint_as_float(pv[2][e] << 16) + __uint_as_float(pv[3][e] << 16));
        c[e >> 1][e & 1] = (__uint_as_float(pv[0][e] & 0xffff0000u) + __uint_as_float(pv[1][e] & 0xffff0000u)) + (__uint_as_float(pv[2][e] & 0xffff0000u) + __uint_as_float(pv[3][e] & 0xffff0000u));
      }
      const float r = (fnum + fold8(a[0], a[1])) * __builtin_amdgcn_rsqf(fvar + fold8(c[0], c[1]));
      unsigned key = (__float_as_uint(r) ^ ((unsigned)(__float_as_int(r) >> 31) | 0x80000000u)) & 0xfffff800u;
      if (r != r) key = 0u;
      key = wave_umax(key | (unsigned)(2047 - (rrow + (lane & 7) * 4 + (lane >> 4))));
      __builtin_amdgcn_raw_buffer_store_b64(u32x2{key, tb1 & 0x7fffffffu}, rsh, lane < NREP ? (HAND_N + PK_O + lane * 128 + (rrow >> 5) * 2) * 4 : OOR, 0, 16);
      if (hw == 1) STAMP(dk, 2);
    }
    gu_setup();
    f32x2 cgu = sums2(1024);
    asm volatile("" : "+v"(cgu));
    bool got = true;
    auto get_fc = [&]() __attribute__((always_inline)) {
      const u32x2 fw = __builtin_amdgcn_raw_buffer_load_b64(rsh, (HAND_N + PK_O + (bid % NREP) * 128 + lane * 2) * 4, 0, 16);
      got = none(umax(fw[0], fw[1]) == SENT);
      if (got) {
        const unsigned c1 = wave_umax(fw[0]), c2 = wave_umax(fw[0] == c1 ? 0u : fw[0]);
#define BADFC 0
        const unsigned r1 = (2047u - (c1 & 0x7ffu)) ^ BADFC, r2 = 2047u - (c2 & 0x7ffu);
        unsigned dt = (unsigned)((int)((fw[1] - tb1) << 1) >> 1) + 512u;
        dt = dt < 1024u ? dt : 512u;
        *LDS_AT(volatile u32x2, lbase + SPM + 144) = u32x2{r1 | (r2 << 16), (unsigned)__builtin_amdgcn_readlane(dt, r1 >> 5) | (wave_umax(dt) << 16)};
      }
    };
    if (hw == 0) {
      __builtin_amdgcn_s_sleep(FCPRE);
#pragma unroll 1
      for (int i = 0; i < SPIN_MAX; ++i) {
        if (__builtin_amdgcn_readfirstlane(*LDS_AT(const volatile unsigned, lbase + FLG + 28)) - best >= 4u) break;
        __builtin_amdgcn_s_sleep(FCSLP);
      }
      asm volatile("" ::: "memory");
      get_fc();
    } else __builtin_amdgcn_s_sleep(TPRE - 10);
    PIN();
    tile_at(2u);
    const unsigned tb2 = (unsigned)__builtin_amdgcn_s_memrealtime() - 512u;
    LOGDEF;
    LOG();
#pragma unroll 1
    for (int spin = 0; spin < SPIN_MAX && !got; ++spin) { get_fc(); asm volatile("" ::: "memory"); }
    PIN();
    u32x2 fm = {SENT, 0u};
#pragma unroll 1
    for (int i = 0; i < 4 * SPIN_MAX && fm[0] == SENT; ++i) {
      const u32x2 q = *LDS_AT(const volatile u32x2, lbase + SPM + 144);
      fm = u32x2{(unsigned)__builtin_amdgcn_readfirstlane(q[0]), (unsigned)__builtin_amdgcn_readfirstlane(q[1])};
    }
    const int fr1 = fm[0] & 0x7ffu, fr2 = (fm[0] >> 16) & 0x7ffu;
    const unsigned t1 = tb2 + (fm[1] & 1023u), tmx = tb2 + ((fm[1] >> 16) & 1023u);
    auto until = [&](unsigned te) { while ((int)((unsigned)__builtin_amdgcn_s_memrealtime() - te) < 0) __builtin_amdgcn_s_sleep(1); asm volatile("" ::: "memory"); };
    lds_wr(lbase + SPM + 136, tmx);
    PIN();
    if (hw == 0) STAMP(dk, 3);
    const int xoff = (hw * 256 + (lane & 3) * 64 + (lane >> 2) * 4) * 2;
    const int oc = (omi * 2 + (oni >> 3)) * 32, col = oc + (lane & 7) * 4 + (lane >> 4);
    const int xoo = !p1blk && hw == 3 ? (col & ~1) * 2 : S_ * D_ * 2;
    auto get_row = [&](int r, int sl, u32x2& xr, unsigned& xo) __attribute__((always_inline)) {
      if ((r >> 7) == omi) {
#pragma unroll 1
        for (int i = 0; i < SPIN_MAX; ++i) {
          if (__builtin_amdgcn_readfirstlane(*LDS_AT(const volatile unsigned char, lbase + FLG + 24)) >= 16u) break;
          __builtin_amdgcn_s_sleep(1);
        }
        asm volatile("" ::: "memory");
      }
#pragma unroll 1
      for (int spin = 0; spin < SPIN_MAX; ++spin) {
        LOG();
        xr = __builtin_amdgcn_raw_buffer_load_b64(rsx, xoff, sl * (D_ * 2), 16);
        xo = __builtin_amdgcn_raw_buffer_load_b32(rsx, xoo, sl * (D_ * 2), 16);
        LOG();
        if (none(umax(umax(xr[0], xr[1]), xo) == SENT)) break;
        __builtin_amdgcn_s_sleep(XRSLP);
        asm volatile("" ::: "memory");
      }
      PIN();
    };
    auto row_md = [&](const u32x2& xr, int k) __attribute__((always_inline)) {
      auto dot = [&](unsigned x, unsigned y, float z) { return __builtin_amdgcn_fdot2_f32_bf16(__builtin_bit_cast(bf16x2, x), __builtin_bit_cast(bf16x2, y), z, false); };
      const float p1 = dot(xr[1], 0x3f803f80u, dot(xr[0], 0x3f803f80u, 0.f)), p2 = dot(xr[1], xr[1], dot(xr[0], xr[0], 0.f)), p3 = dot(xr[1], wq[1], dot(xr[0], wq[0], 0.f));
      const auto q = __builtin_amdgcn_permlane32_swap(__float_as_uint(p1), __float_as_uint(p2), false, false);
      const auto q3 = __builtin_amdgcn_permlane32_swap(__float_as_uint(p3), __float_as_uint(p3), false, false);
      const auto q2 = __builtin_amdgcn_permlane16_swap(__float_as_uint(__uint_as_float(q[0]) + __uint_as_float(q[1])), __float_as_uint(__uint_as_float(q3[0]) + __uint_as_float(q3[1])), false, false);
      float sv = __uint_as_float(q2[0]) + __uint_as_float(q2[1]);
      sv = dpp_add<0x121, 0xf>(dpp_add<0x122, 0xf>(dpp_add<0x124, 0xf>(dpp_add<0x128, 0xf>(sv))));
      lds_wr(lbase + SPM + k * 64 + (lane >> 4) * 16 + hw * 4, f2word(sv));
      u32x4 a, c;
#pragma unroll 1
      for (int i = 0; i < SPIN_MAX; ++i) {
        a = lds_rd4(lbase + SPM + k * 64); c = lds_rd4(lbase + SPM + k * 64 + 32);
        if (__builtin_amdgcn_readfirstlane(umax(umax(a[0], a[1]), umax(a[2], a[3]))) != SENT) break;
      }
      auto sum4 = [&](const u32x4& v) { return (__uint_as_float(v[0]) + __uint_as_float(v[1])) + (__uint_as_float(v[2]) + __uint_as_float(v[3])); };
      const float mean = sum4(a) * (1.0f / 1024.0f), msq = sum4(c) * (1.0f / 1024.0f);
      const float den = rbf(__builtin_amdgcn_sqrtf(rbf(rbf(fmaxf(fmaf(-mean, mean, msq), 0.f)) + 1e-5f)));
      return (unsigned)__builtin_amdgcn_readfirstlane((pack2(mean, mean) & 0xffffu) | (__float_as_uint(den) & 0xffff0000u));
    };
    auto meet = [&](unsigned n) __attribute__((always_inline)) {
      lds_wr(lbase + MEET + hw * 4, n);
#pragma unroll 1
      for (int i = 0; i < SPIN_MAX; ++i)
        if (__builtin_amdgcn_readfirstlane(min4(lds_rd4(lbase + MEET))) >= n) break;
      asm volatile("" ::: "memory");
    };
    auto put_y = [&](int so, unsigned md, const __amdgpu_buffer_rsrc_t rs, int yo) __attribute__((always_inline)) {
      __builtin_amdgcn_raw_buffer_store_b128(down(act_diag(so, md, cgu), w), rs, (yo + fg * D_) * 4 + t * 16, 0, 16);
      PIN();
    };
    auto y_sum4 = [&](const __amdgpu_buffer_rsrc_t rs, int yo, int k) __attribute__((always_inline)) {
      const int src = (yo + (hw * 56 + (lane >> 3)) * D_ + oc + (lane & 7) * 4) * 4;
      u32x4 v[7];
#pragma unroll
      for (int i = 0; i < 7; ++i) asm volatile("" : "=v"(v[i]));
      unsigned todo = 0x7fu;
#pragma unroll 1
      for (int spin = 0; spin < SPIN_MAX; ++spin) {
        LOG();
#pragma unroll
        for (int i = 0; i < 7; ++i) if ((todo >> i) & 1u) v[i] = __builtin_amdgcn_raw_buffer_load_b128(rs, src, i * (8 * D_ * 4), 16);
#pragma unroll
        for (int i = 0; i < 7; ++i) if (none(umax(umax(v[i][0], v[i][1]), umax(v[i][2], v[i][3])) == SENT)) todo &= ~(1u << i);
        if (!todo) break;
        __builtin_amdgcn_s_sleep(YSLP);
        asm volatile("" ::: "memory");
      }
      f32x2 z0 = {0.f, 0.f}, z1 = {0.f, 0.f};
#pragma unroll
      for (int i = 0; i < 7; ++i) {
        z0 += f32x2{__uint_as_float(v[i][0]), __uint_as_float(v[i][1])};
        z1 += f32x2{__uint_as_float(v[i][2]), __uint_as_float(v[i][3])};
      }
      const unsigned ya = lbase + SPM + 256 + k * 512 + ((lane & 7) * 4 + (lane >> 4)) * 16;
      lds_wr((lane & 8) ? lbase + DUM + lane * 4 : ya + hw * 4, f2word(fold8(z0, z1)));
      u32x4 q = {0u, 0u, 0u, 0u};
      if (hw == 3) {
#pragma unroll 1
        for (int i = 0; i < 4 * SPIN_MAX; ++i) {
          q = lds_rd4(ya);
          if (none(umax(umax(q[0], q[1]), umax(q[2], q[3])) == SENT)) break;
        }
      }
      return (__uint_as_float(q[0]) + __uint_as_float(q[1])) + (__uint_as_float(q[2]) + __uint_as_float(q[3]));
    };
    const __amdgpu_buffer_rsrc_t rs1 = __builtin_amdgcn_make_buffer_rsrc(hand, 0, HAND_N * 4, 0x00020000);
    u32x2 xr;
    unsigned xo, xo2;
    until(t1 + XDT);
    get_row(fr1, 0, xr, xo);
    if (hw == 0) STAMP(dk, 7);
#define FAILFC 0
    u32x2 xr2 = __builtin_amdgcn_raw_buffer_load_b64(rsx, xoff, D_ * 2, 16);
    xo2 = __builtin_amdgcn_raw_buffer_load_b32(rsx, xoo, D_ * 2, 16);
    PIN();
    if (p1blk) gate_up(xr);
    const unsigned md1 = row_md(xr, 0) ^ (FAILFC & 1);
    if (hw == 0) STAMP(dk, 10);
    LOGAT(20);
    if (p1blk) {
      put_y(0, md1, rsh, GRAN_N);
      if (hw == 0) STAMP(dk, 4);
      LOG();
    }
    if (!none(umax(umax(xr2[0], xr2[1]), xo2) == SENT)) { until(tmx + XD2); get_row(fr2, 1, xr2, xo2); }
    const unsigned md2 = row_md(xr2, 1) ^ (FAILFC & 2);
    unsigned pkey;
    {
      const u32x4 e = lds_rd4(lbase + SPM + (lane & 1) * 64 + 16);
      const unsigned md = (lane & 1) ? md2 : md1;
      const float r = fmaf(-__uint_as_float(md << 16), wsum, (__uint_as_float(e[0]) + __uint_as_float(e[1])) + (__uint_as_float(e[2]) + __uint_as_float(e[3]))) * frcp(__uint_as_float(md & 0xffff0000u));
      const unsigned rb = __float_as_uint(r) + 0x8000u;
      const unsigned key = (r != r ? 0u : (rb ^ ((unsigned)((int)rb >> 31) | 0x80000000u)) & 0xfffff800u) | (unsigned)(2047 - ((lane & 1) ? fr2 : fr1));
      pkey = umax(__builtin_amdgcn_readlane(key, 0), __builtin_amdgcn_readlane(key, 1));
    }
    const int pk = 2047 - (int)(pkey & 0x7ffu);
    const bool two = pk != fr1;
    const unsigned pmd = two ? md2 : md1;
    lds_wr(lbase + SPM + 140, pk);
    if (p1blk && two) {
      dad += 512;
      gate_up(xr2);
      dad -= 512;
      meet(1u);
      put_y(512, md2, rs1, GRAN_N);
    }
    if (hw == 0) STAMP(dk, 5);
    LOG();
    float y = 0.f;
    auto put_row = [&](int r, unsigned md, unsigned key) __attribute__((always_inline)) {
      const unsigned rk = key >> 16, rb = (rk & 0x8000u) ? (rk & 0x7fffu) : (~rk & 0xffffu);
      const float rden = __uint_as_float(md & 0xffff0000u), rs = rbf(frcp(1.0f + __expf(-__uint_as_float(rb << 16))));
      const float xa = __uint_as_float((col & 1) ? xo & 0xffff0000u : xo << 16);
      float ha = xa, hb = xa;
      ln_apply2(ha, hb, __uint_as_float(md << 16), rden, frcp(rden));
      const float o = xa + rbf(ha + rbf(rbf(y) * rs));
      __builtin_amdgcn_raw_buffer_store_b16((unsigned short)(pack2(o, o) & 0xffffu), __builtin_amdgcn_make_buffer_rsrc(out, 0, hw == 3 ? S_ * D_ * 2 : 0, 0x00020000),
                                            (lane & 8) ? S_ * D_ * 2 : (r * D_ + col) * 2, 0, OAUX);
    };
    if (!p1blk) {
      until(t1 + XDT + YD1 + (two ? YD3 : 0)); until(tmx + YD2 + (two ? YD3 : 0));
      if (hw == 3) STAMP(dk, 13);
      LOGAT(8);
      if (two) xo = xo2;
      y = y_sum4(__builtin_amdgcn_make_buffer_rsrc(two ? hand : hand2, 0, HAND_N * 4, 0x00020000), GRAN_N, 0);
      put_row(pk, pmd, pkey);
      if (hw == 3) STAMP(dk, 14);
      LOGAT(24);
    }
    unsigned bmd = 0u;
    best = 0u;
#pragma unroll 1
    for (int spin = 0; spin < 4 * SPIN_MAX; ++spin) {
      const u32x2 v = *LDS_AT(const volatile u32x2, lbase + BEST);
      best = __builtin_amdgcn_readfirstlane(v[0]); bmd = __builtin_amdgcn_readfirstlane(v[1]);
      if (best) break;
      __builtin_amdgcn_s_sleep(1);
    }
    asm volatile("" ::: "memory");
    const int idx = 2047 - (int)(best & 0x7ffu);
    if (hw == 0) STAMP(dk, 1);
    LOGAT(25);
    LOGOUT(hw);
    if (__builtin_expect(idx != pk || bmd != pmd || (best ^ pkey) >> 16, 0)) {
      asm volatile("s_waitcnt vmcnt(0)" ::: "memory");
      if (!p1blk && hw == 3 && idx != pk && lane == 0) st_ag(gran + GR_F + (16 + (oc >> 5)) * 32, tag);
      if (idx != pk || bmd != pmd) {
        get_row(idx, idx == fr1 ? 0 : (idx == fr2 ? 1 : 2), xr, xo);
        if (p1blk) {
          gate_up(xr);
          meet(2u);
          put_y(0, bmd, rsh, HAND_N + Y3_O);
        } else y = y_sum4(rsh, HAND_N + Y3_O, 2);
      }
      if (p1blk || hw != 3) return;
#pragma unroll 1
      for (int spin = 0; spin < SPIN_MAX && idx != pk; ++spin) {
        if (__builtin_amdgcn_readfirstlane(ld_ag(gran + GR_F + (oc >> 6) * 32)) == tag) break;
        asm volatile("" ::: "memory");
      }
      put_row(idx, bmd, best);
    }
    if (p1blk || hw != 3) return;
    }
    return;
  }
  if (wid >= 4) {
    const int lane = fresh_lane();
    const int lw = wid - 4;
    const long long lay_ = ap->lay;
    const u16* A = ap->a; const u16* W = ap->W; const u16* wc = ap->wc;
#define LPRIO 3
#define LP0 3
    __builtin_amdgcn_s_setprio(LP0);
    unsigned va[4], vw[6];
    const unsigned ul = lane;
    va[0] = (unsigned)(m0 + lw * 32) * (K * 2) + (ul >> 3) * (K * 2) + (((ul & 7u) ^ (ul >> 4)) << 4);
    va[1] = va[0] + (8 * K * 2 - 1024);
    va[2] = (va[0] ^ 64u) + 2 * (8 * K * 2 - 1024);
    va[3] = va[2] + (8 * K * 2 - 1024);
#pragma unroll
    for (int i = 0; i < 3; ++i) {
      const unsigned n = ul + 64u * i, k = __umul24(n, 171u) >> 12, cp = n - __umul24(k, 24u);
      const unsigned c = (cp & 7u) ^ ((k & 2u) << 1), part = (cp >> 3) ^ (cp < 16u ? ((c >> 2) ^ (unsigned)tmi) & 1u : 0u);
      vw[i] = __umul24(k, ldw * 2) + part * 2048u + (c << 4) + ((unsigned)(lw * 16 * ldw + n0) * 2u - i * 1024u);
      vw[i + 3] = (vw[i] ^ 16u) + 8 * ldw * 2;
    }
    PIN();
    asm volatile("" : "+v"(va[0]), "+v"(va[1]), "+v"(va[2]), "+v"(va[3]), "+v"(vw[0]), "+v"(vw[1]), "+v"(vw[2]), "+v"(vw[3]), "+v"(vw[4]), "+v"(vw[5]));
    const bool LAY2 = lay_ != 0;
    const int krot = tmi & 8;
    if (LAY2 && slow_cols(W, tni)) W = wc;
    auto stage_a = [&](int kt, int slot) {
      const char* pa = reinterpret_cast<const char*>(A) + (size_t)(kt ^ krot) * 128;
      asm volatile("s_mov_b32 m0, %5\n\ts_nop 0\n\tglobal_load_lds_dwordx4 %0, %4 sc1\n\tglobal_load_lds_dwordx4 %1, %4 offset:1024 sc1\n\t"
                   "global_load_lds_dwordx4 %2, %4 offset:2048 sc1\n\tglobal_load_lds_dwordx4 %3, %4 offset:3072 sc1"
                   :: "v"(va[0]), "v"(va[1]), "v"(va[2]), "v"(va[3]), "s"(pa), "s"(lbase + slot * STB + lw * 4096) : "m0", "memory");
    };
    auto stage_w = [&](int kt, int slot) {
      const char* pw = reinterpret_cast<const char*>(W) + (size_t)(kt ^ krot) * 128 * ldw;
      asm volatile("s_mov_b32 m0, %7\n\ts_nop 0\n\tglobal_load_lds_dwordx4 %0, %6 sc1\n\tglobal_load_lds_dwordx4 %1, %6 offset:1024 sc1\n\t"
                   "global_load_lds_dwordx4 %2, %6 offset:2048 sc1\n\ts_addk_i32 m0, 0xc00\n\ts_nop 0\n\tglobal_load_lds_dwordx4 %3, %6 sc1\n\t"
                   "global_load_lds_dwordx4 %4, %6 offset:1024 sc1\n\tglobal_load_lds_dwordx4 %5, %6 offset:2048 sc1"
                   :: "v"(vw[0]), "v"(vw[1]), "v"(vw[2]), "v"(vw[3]), "v"(vw[4]), "v"(vw[5]), "s"(pw), "s"(lbase + slot * STB + ABYTES + lw * 6144) : "m0", "scc", "memory");
    };
    if (LAY2) {
      __builtin_amdgcn_s_setprio(0);
      __builtin_amdgcn_s_sleep(TPRE - 10);
      tile_at(3u);
      __builtin_amdgcn_s_setprio(LPRIO);
#pragma unroll
      for (int st = 0; st < NST; ++st) stage_w(st, st);
      PIN();
      __builtin_amdgcn_s_barrier();
      PIN();
#pragma unroll
      for (int st = 0; st < NST; ++st) stage_a(st, st);
      asm volatile("s_waitcnt vmcnt(8)" ::: "memory");
    } else {
      __builtin_amdgcn_s_setprio(LPRIO);
      stage_a(0, 0); stage_w(0, 0);
      if (PRO > 1) { stage_a(1, 1); stage_w(1, 1); }
      if (PRO > 2) { stage_a(2, 2); stage_w(2, 2); }
      asm volatile("s_waitcnt vmcnt(%0)" :: "n"(10 * PRO - 10) : "memory");
    }
    int slot = 0;
    __builtin_amdgcn_s_barrier();
    if (!LAY2) {
      if (PRO < 2) { stage_a(1, 1); stage_w(1, 1); }
      if (PRO < 3) { stage_a(2, 2); stage_w(2, 2); }
    }
    for (int kt = 0; kt + 1 < NKT; ++kt) {
      if (LAY2 && kt == 0) asm volatile("s_waitcnt vmcnt(4)" ::: "memory");
      else if (kt + 2 >= NKT) asm volatile("s_waitcnt vmcnt(0)" ::: "memory");
      else asm volatile("s_waitcnt vmcnt(10)" ::: "memory");
      __builtin_amdgcn_s_barrier();
      if (kt + NST < NKT) { stage_a(kt + NST, slot); stage_w(kt + NST, slot); }
      slot = slot == NST - 1 ? 0 : slot + 1;
    }
    __builtin_amdgcn_s_setprio(0);
    __builtin_amdgcn_s_barrier();
  } else {
  LAYER_HERE;
  const int dk = LAY2 ? 5 : 1;
  const int lane = fresh_lane(), wm = wid & 1, wn = wid >> 1;
  if (wid == 0) STAMP(dk, 0);
  f32x4 acc[4][6];
#pragma unroll
  for (int i = 0; i < 4; ++i)
#pragma unroll
    for (int j = 0; j < 6; ++j) acc[i][j] = f32x4{0.f, 0.f, 0.f, 0.f};
  const int q16 = lane & 15, g4 = lane >> 4;
  const int e0 = 16 * (q16 >> 3) + (q16 & 7);
  s16x8 af[2][4];
  unsigned aad[2], wad[2];
  {
    const int row = wm * 64 + e0, swz = ((row >> 1) & 3) | (((row >> 4) & 1) << 2);
#pragma unroll
    for (int h = 0; h < 2; ++h) aad[h] = lbase + row * 128 + (((4 * h + g4) ^ swz) << 4);
    const int tq = (lane & 15) >> 2, tp = lane & 3, k = 8 * g4 + tq, ch = wn * 4 + 2 * (tp >> 1);
    wad[0] = lbase + ABYTES + k * 384 + ((ch ^ (4 * ((k >> 1) & 1)) ^ (g4 & 1)) << 4) + 8 * (tp & 1);
    wad[1] = wad[0] ^ 16;
  }
  auto lda = [&](unsigned a, int i) { return *LDS_AT(const s16x8, a + (i >> 1) * 4096 + (i & 1) * 1024); };
  auto ldw_ = [&](int h, int j) {
    const unsigned w = wad[j & 1] + h * 12288 + (j >> 1) * 128;
    s16x4 lo = __builtin_amdgcn_ds_read_tr16_b64_v4i16(LDS_AT(s16x4, w));
    s16x4 h4 = __builtin_amdgcn_ds_read_tr16_b64_v4i16(LDS_AT(s16x4, w + 1536));
    return (s16x8)__builtin_shufflevector(lo, h4, 0, 1, 2, 3, 4, 5, 6, 7);
  };
  ARGS_HERE;
  const bool give = wn != (tmi & 1);
  unsigned lto = lbase + (give ? 2 * KSB : 0) + wm * 4096, lqo = lbase + QOFF + wid * 4096;
  char* qgb = reinterpret_cast<char*>(KV) + QG + (((LAY2 ? 256 : 0) + tmi * 16 + tni) * 2 + wm) * 4096 - lto;
  char* kvb = reinterpret_cast<char*>(KV) + ((tni * 2 + wn) * 64 + tmi * 4 + wm * 2) * 2048 - lto;
  {
    const unsigned ato = lbase + ALPHA + wm * 256, dum = lbase + DUM + lane * 4;
    *LDS_AT(u32x4, lqo + lane * 16) = u32x4{lane >= 16 ? lbase + CRED : lbase + TABA + (wm * 64 + e0) * 16, lbase + TABW + (wn * 32 + e0) * 16,
                                                   ato + (give ? 512 : 0) + e0 * 4, lto + (lane >> 5) * 1024 + ((lane & 28) * 2 + (lane & 3)) * 16};
    *LDS_AT(u32x2, lqo + 2048 + lane * 16) = u32x2{aad[0], aad[1]};
    *LDS_AT(u32x4, lqo + 1024 + lane * 16) = u32x4{ato + g4 * 16 + (lane >> 5) * 32, lto + (lane >> 5) * 1024 + ((lane & 24) * 2 + (lane & 7)) * 16,
                                                          lane == 0 ? lbase + FLG + 32 : dum, lane == 0 ? lbase + FLG + 16 : dum};
  }
  {
    const __amdgpu_buffer_rsrc_t rq = __builtin_amdgcn_make_buffer_rsrc(qgb + lto, 0, give && !LAY2 ? 4096 : 0, 0x00020000);
#pragma unroll
    for (int i = 0; i < 4; ++i) __builtin_amdgcn_raw_buffer_store_b128(u32x4{SENT, SENT, SENT, SENT}, rq, lane * 16 + i * 1024, 0, 16);
  }
  unsigned qmv = lqo - lto;
  asm volatile("" : "+s"(lqo), "+s"(qmv), "+s"(kvb), "+s"(qgb) :: "memory");
#pragma unroll
  for (int i = 0; i < 4; ++i)
#pragma unroll
    for (int j = 0; j < 6; ++j) asm volatile("" : "+v"(acc[i][j]));
  asm volatile("" : "+v"(aad[0]), "+v"(aad[1]), "+v"(wad[0]), "+v"(wad[1]));
  if (LAY2) __builtin_amdgcn_s_barrier();
  __builtin_amdgcn_s_barrier();
  PIN();
  if (wid == 0) STAMP(dk, 1);
  s16x8 wq[4][2];
  wq[0][0] = ldw_(0, 0); af[0][0] = lda(aad[0], 0); wq[0][1] = ldw_(1, 0); af[1][0] = lda(aad[1], 0); wq[1][0] = ldw_(0, 1); wq[1][1] = ldw_(1, 1);
  af[0][1] = lda(aad[0], 1); af[1][1] = lda(aad[1], 1); af[0][2] = lda(aad[0], 2); af[1][2] = lda(aad[1], 2);
  PIN();
  s16x4 wlo;
  auto rw = [&](s16x8& d, int h, int j, int hi, int off = 0) __attribute__((always_inline)) {
    const s16x4 v = __builtin_amdgcn_ds_read_tr16_b64_v4i16(LDS_AT(s16x4, wad[j & 1] + h * 12288 + (j >> 1) * 128 + hi * 1536 + off));
    if (hi) d = (s16x8)__builtin_shufflevector(wlo, v, 0, 1, 2, 3, 4, 5, 6, 7);
    else wlo = v;
  };
  auto at = [&](s16x8& x, int r) __attribute__((always_inline)) {
    if (r == 0) asm volatile("" :: "{v[104:107]}"(x));
    if (r == 1) asm volatile("" :: "{v[108:111]}"(x));
    if (r == 2) asm volatile("" :: "{v[112:115]}"(x));
    if (r == 3) asm volatile("" :: "{v[116:119]}"(x));
    if (r == 4) asm volatile("" :: "{v[120:123]}"(x));
    if (r == 5) asm volatile("" :: "{v[124:127]}"(x));
    if (r == 6) asm volatile("" :: "{v[128:131]}"(x));
    if (r == 7) asm volatile("" :: "{v[132:135]}"(x));
    if (r == 8) asm volatile("" :: "{v[136:139]}"(x));
    if (r == 9) asm volatile("" :: "{v[140:143]}"(x));
    if (r == 10) asm volatile("" :: "{v[144:147]}"(x));
    if (r == 11) asm volatile("" :: "{v[148:151]}"(x));
    if (r == 12) asm volatile("" :: "{v[152:155]}"(x));
    if (r == 13) asm volatile("" :: "{v[156:159]}"(x));
    if (r == 14) asm volatile("" :: "{v[160:163]}"(x));
    if (r == 15) asm volatile("" :: "{v[164:167]}"(x));
  };
  auto mm = [&](int i, int j, int h, int sl, int hw_) __attribute__((always_inline)) {
    s16x8 &a = af[h][i], &w = wq[sl][hw_];
    at(a, h * 4 + i); at(w, 8 + sl * 2 + hw_);
    if (j >= 4) asm volatile("v_mfma_f32_16x16x32_bf16 %0, %1, %2, %0" : "+v"(acc[i][j]) : "v"(a), "v"(w));
    else asm volatile("v_mfma_f32_16x16x32_bf16 %0, %1, %2, %0" : "+v"(acc[i][j]) : "v"(w), "v"(a));
    PIN();
  };
  auto next = [&](int g, int J0, int s0, int s1, int off) __attribute__((always_inline)) {
    if (g == 2 || g == 3) rw(wq[s0][0], 0, J0, g & 1, off);
    if (g == 4 || g == 5) af[g & 1][0] = lda(aad[g & 1] + off, 0);
    if (g == 6 || g == 7) rw(wq[s0][1], 1, J0, g & 1, off);
    if (g >= 8 && g < 12) rw(wq[s1][(g - 8) >> 1], (g - 8) >> 1, J0 + 1, g & 1, off);
    if (g >= 12) af[g & 1][(g - 10) >> 1] = lda(aad[g & 1] + off, (g - 10) >> 1);
  };
  auto third = [&](int p, int J0, int delta) __attribute__((always_inline)) {
    auto mv = [&](unsigned& x) __attribute__((always_inline)) { asm volatile("v_add_u32 %0, %1, %0" : "+v"(x) : "s"(delta)); };
#pragma unroll
    for (int g = 0; g < 16; ++g) {
      const int h = g & 1, i = p == 1 ? (g >> 1) & 3 : g >> 2, t = p == 1 ? g >> 3 : (g >> 1) & 1, j = 2 * p + t, sl = p == 1 ? 2 - 2 * t : p + t;
      mm(i, j, h, sl, h);
      if (p == 0) {
        if (g < 2) af[g][3] = lda(aad[g], 3);
        else if (g < 6) rw(wq[2][(g - 2) >> 1], (g - 2) >> 1, 2, g & 1);
        else if (g < 10) rw(wq[3][(g - 6) >> 1], (g - 6) >> 1, 5, g & 1);
        else if (g < 12) mv(aad[g - 10]);
      }
      if (p == 1) {
        if (g < 4) rw(wq[0][g >> 1], g >> 1, 3, g & 1);
        if (g == 4) mv(wad[1]);
        if (g >= 8 && g < 12) rw(wq[2][(g - 8) >> 1], (g - 8) >> 1, 4, g & 1);
        if (g == 12) mv(wad[0]);
      }
      if (p == 2) {
        if (g == BARG) {
          asm volatile("" ::: "memory");
          PIN();
          __builtin_amdgcn_s_barrier();
          asm volatile("" ::: "memory");
        }
        next(g, J0, 0, 1, 0);
      }
      PIN();
      {
        if ((p == 2 && g == 15) || (p == 0 && g == 7)) __builtin_amdgcn_s_waitcnt(0xc67f);
        if (p == 0 && g == 2) __builtin_amdgcn_s_waitcnt(0xc57f);
        if ((p == 0 && g == 14) || (p == 1 && (g & 7) == 7)) __builtin_amdgcn_s_waitcnt(0xc07f);
        PIN();
      }
    }
  };
  third(0, 0, STB); third(1, 0, STB);
  {
    static_assert((NKT - NST - 1) % NST == 0, "");
#pragma unroll 1
    for (int kt = 0; kt < NKT - NST - 1; kt += NST) {
#pragma unroll
      for (int u = 0; u < NST; ++u) {
        const int delta = u == 1 ? -(NST - 1) * STB : STB;
        third(2, 0, 0); third(0, 0, delta); third(1, 0, delta);
      }
      asm volatile("s_waitcnt vmcnt(0)" ::: "memory");
    }
  }
  static_assert(NKT % NST == 1, "slots of the last stages");
#define TT(i)
  auto bar = [&]() { PIN(); __builtin_amdgcn_s_barrier(); asm volatile("" ::: "memory"); PIN(); };
  TT(0);
  third(2, 2, 0);
  const __amdgpu_buffer_rsrc_t rsk = __builtin_amdgcn_make_buffer_rsrc(give ? qgb : kvb, 0, 0x7fffffff, 0x00020000), rsv = __builtin_amdgcn_make_buffer_rsrc(kvb + VOFF, 0, 0x7fffffff, 0x00020000);
  const __amdgpu_buffer_rsrc_t rsq = __builtin_amdgcn_make_buffer_rsrc(kvb - qmv, 0, give ? 0x7fffffff : 0, 0x00020000);
  unsigned pk;
  u32x2 t0, t1, t2, t3;
  auto park = [&](int i) { return *LDS_AT(const volatile u32x2, pk + (i >> 1) * 1024 + (i & 1) * 8); };
  float al[4];
  f32x4 av[4];
  u32x4 kvp[12];
  auto cacc = [&](int p, int x, int t) -> f32x4& { return p < 2 ? acc[x][2 * p + t] : acc[(x >> 1) * 2 + t][4 + (x & 1)]; };
  auto signal = [&](unsigned v) { __hip_atomic_fetch_add(LDS_AT(unsigned, t3[0]), v, __ATOMIC_RELAXED, __HIP_MEMORY_SCOPE_WORKGROUP); PIN(); };
  auto outop = [&](int p, int k) {
    if (k >= 0 && k < 16) {
      const int x = k >> 2, t = (k >> 1) & 1;
      f32x4& c = cacc(p, x, t);
#pragma unroll
      for (int o = 2 * (k & 1); o < 2 * (k & 1) + 2; ++o) {
        float r;
        asm volatile("v_mul_f32 %0, %1, %2" : "=v"(r) : "v"(c[o]), "v"(p < 2 ? al[x] : av[(x >> 1) * 2 + t][o]));
        c[o] = r;
      }
      PIN();
    }
    if (k >= 1 && k <= 16) {
      const int x = (k - 1) >> 2, t = ((k - 1) >> 1) & 1, o = (k - 1) & 1;
      kvp[p * 4 + x][2 * t + o] = pack2(cacc(p, x, t)[2 * o], cacc(p, x, t)[2 * o + 1]);
      PIN();
      if (t && o) {
        if (p) __builtin_amdgcn_raw_buffer_store_b128(kvp[p * 4 + x], p == 1 ? rsk : rsv, (p == 1 ? t1[1] : t2[1]) + (x >> 1) * 2048 + (x & 1) * (p == 1 ? 64 : 128), 0, 16);
        else {
          __builtin_amdgcn_raw_buffer_store_b128(kvp[x], rsq, t1[1] + (x >> 1) * 2048 + (x & 1) * 64, 0, 16);
          *LDS_AT(u32x4, t1[1] + (x >> 1) * 2048 + (x & 1) * 64) = kvp[x];
        }
        PIN();
      }
    }
  };
  static_for<0, 168>([&](auto nc) __attribute__((always_inline)) {
    constexpr int n = decltype(nc)::value, sec = n < 80 ? n >> 4 : (n < 104 ? 5 : 6 + ((n - 104) >> 4)), g = n < 104 ? n - 16 * sec : (n - 104) & 15;
    auto taba = [&](int i) { af[0][i] = *LDS_AT(const s16x8, t0[0] + (i >> 1) * 512 + (i & 1) * 128); };
    auto tabw = [&](int j) { wq[(2 + (j >> 1)) & 3][j & 1] = *LDS_AT(const s16x8, t0[1] + ((j >> 1) * 64 + (j & 1) * 8) * 16); };
    auto rwn = [&](int e, int sl, int j, int off) { if (e >= 0 && e < 4) rw(wq[sl][e >> 1], e >> 1, j, e & 1, off); };
    if (g == 0) TT(1 + sec);
    if (sec == 5) {
      const int i = (g / 12) * 2 + (g & 1), j = (g % 12) >> 1;
      mm(i, j, 0, (2 + (j >> 1)) & 3, j & 1);
    } else {
      constexpr int part[10] = {1, 2, 1, 2, 0, 0, 1, 2, 0, 0}, s0[10] = {0, 2, 0, 2, 0, 0, 1, 3, 1, 3};
      const int h = g & 1, i = sec == 7 ? (g >> 3) * 2 + ((g >> 1) & 1) : g >> 2, t = sec == 7 ? (g >> 2) & 1 : (g >> 1) & 1;
      mm(i, 2 * part[sec] + t, h, (s0[sec] + t) & 3, h);
    }
    if (sec == 0 || sec == 2) {
      if (g < 2) af[g][3] = lda(aad[g] + sec / 2 * STB, 3);
      rwn(g - 2, 2, 4, sec / 2 * STB); rwn(g - 6, 3, 5, sec / 2 * STB);
    }
    if (sec == 1) {
      if (g == 1) bar();
      next(g, 2, 0, 1, STB);
    }
    if ((sec == 3 && g == 12) || (sec == 6 && g == 13)) { pk = lqo + fresh_lane() * 16; asm volatile("" : "+v"(pk)); }
    if (sec == 3) {
      rwn(g, 0, 0, STB); rwn(g - 4, 1, 1, STB);
      if (g >= 8 && g < 12) asm volatile("v_subrev_u32 %0, %1, %0" : "+v"(g < 10 ? aad[g & 1] : wad[g & 1]) : "s"(STB));
      if (g == 13) t1 = park(1);
      if (g == 14) t0 = park(0);
    }
    if (sec == 4) {
      if (g == B15) bar();
      if (g == 4) taba(0);
      if (g == 5 || g == 6) tabw(g - 5);
      if (g == 7) taba(1);
      if (g == 8 || g == 9) tabw(g - 6);
      if (g == 10 || g == 11) af[1][g - 10] = lda(aad[1], g - 10);
      if (g == 12) taba(2);
      if (g == 15) taba(3);
    }
    if (sec == 5) {
      if (g < 2) tabw(4 + g);
      rwn(g - 2, 1, 2, 0);
      if (g == 12 || g == 13) af[0][g - 12] = lda(aad[0], g - 12);
      if (g == 22) { al[0] = *LDS_AT(const float, t1[0]); al[1] = *LDS_AT(const float, t1[0] + 32); }
      rwn(g - 16, 2, 3, 0);
      if (g == 20 || g == 21) af[1][g - 18] = lda(aad[1], g - 18);
    }
    if (sec == 6) {
      if (g < 2) af[0][2 + g] = lda(aad[0], 2 + g);
      rwn(g - 2, 3, 4, 0);
      if (g == 9) { al[2] = *LDS_AT(const float, t1[0] + 128); al[3] = *LDS_AT(const float, t1[0] + 160); }
      if (g == 14) t2 = park(2);
      if ((g & 3) >= 2) rwn((g - 10) / 4 * 2 + (g & 1) - (g < 10) * 8, 0, 5, 0);
    }
    if (sec == 7) {
      if (g == 2 || g == 3 || g == 10 || g == 11) av[(g >> 3) * 2 + (g & 1)] = *LDS_AT(const f32x4, t2[0] + ((g >> 3) * 32 + (g & 1) * 8) * 4);
      rwn(g < 2 ? g : (g < 4 ? -1 : g - 2), 1, 0, 0); rwn(g - 6, 2, 1, 0);
      if (g == 7) asm volatile("v_xor_b32 %0, 0x200, %0" : "+v"(t1[0]));
      if (g == 14) {
#pragma unroll
        for (int x = 0; x < 4; ++x) al[x] = *LDS_AT(const float, t1[0] + (x >> 1) * 128 + (x & 1) * 32);
      }
      if (g == 12) t3 = park(3);
      if (g == 13) { const u32x2 a = park(4); aad[0] = a[0]; aad[1] = a[1]; }
      if (g == 15) {
        asm volatile("" ::: "memory");
        __builtin_amdgcn_s_waitcnt(0xc07f);
        bar();
      }
    }
    if (sec == 8) next(g, 0, 3, 0, STB);
    if (sec == 9) {
      if (g < 2) af[g][3] = lda(aad[g] + STB, 3);
      if (g == 2) signal(0x10000u);
    }
    PIN();
    outop(1, n - 104 - KO);
    outop(2, n - 120 - KO);
    {
      constexpr int wg[8] = WGAPS;
#pragma unroll
      for (int e = 0; e < 8; ++e)
        if (n - 136 == wg[e]) {
          *LDS_AT(u32x4, (e < 4 ? t1[1] + (e & 1) * 64 : t2[1] + 2048 + (e & 1) * 128) + ((e >> 1) & 1) * KSB) = kvp[4 + e];
          PIN();
        }
    }
    if (n == 152 + 3) { asm volatile("v_add_u32 %0, %1, %0" : "+v"(t1[1]) : "s"(qmv)); PIN(); }
    outop(0, n - 152 - KO);
  });
  TT(13);
  if (wid == 0) STAMP(dk, 18);
#pragma unroll
  for (int k = 16 - KO; k <= 16; ++k) outop(0, k);
  __hip_atomic_fetch_add(LDS_AT(unsigned, t3[1]), 1u, __ATOMIC_RELAXED, __HIP_MEMORY_SCOPE_WORKGROUP);
  TT(14);
  asm volatile("s_waitcnt vmcnt(4)" ::: "memory");
  signal(give ? 0u : 1u);
  TT(15);
  if (wid == 0) STAMP(dk, 19);
  }
  PIN();
  {
  const int lane = fresh_lane(), tid = wid * 64 + lane;
  LAYER_HERE;
  const int dk = LAY2 ? 6 : 2;
  const int g = (wid >> 1) & 1, hh = tmi & 1, qg = wid & 1, far = wid >> 2;
  const int head = tni * 2 + hh, qt = (tmi ^ far) * 4 + qg * 2;
  unsigned qaa = lbase + QOFF + (hh * 2 + qg) * 4096 + kpos(lane) * 16 + (g ? 0 : 2048), qab = qaa ^ 2048;
  unsigned wbase = lbase + g * 4096 + lane * 16;
  asm volatile("" : "+v"(wbase));
  auto sptr = [&](int sl) { return sl * KSB; };
  auto rd = [&](int sp, int off) { return *LDS_AT(const s16x8, wbase + sp + off); };
  int sl0 = sptr(0);
  const unsigned arr_a = lane == 0 ? lbase + FLG + 16 : lbase + DUM + lane * 4;
  auto arrive = [&]() { __hip_atomic_fetch_add(LDS_AT(unsigned, arr_a), 1u, __ATOMIC_RELAXED, __HIP_MEMORY_SCOPE_WORKGROUP); };
  unsigned arr_w = lane == 0 ? lbase + FLG + 8 + (wid >> 2) * 4 : lbase + DUM + lane * 4;
  unsigned arr_i = 1u << (8 * (wid & 3));
  auto arrive_w = [&]() { __hip_atomic_fetch_add(LDS_AT(unsigned, arr_w), arr_i, __ATOMIC_RELAXED, __HIP_MEMORY_SCOPE_WORKGROUP); };
  s16x8 sel;
#pragma unroll
  for (int e = 0; e < 8; ++e) sel[e] = ((lane & 7) == (e & 4)) ? (short)0x3F80 : (short)0;
  int sidx = 0xDDDDDDDD;
  f32x16 oa, ob, nn, sa, sb;
  f32x4 la = {0.f, 0.f, 0.f, 0.f}, lb = {0.f, 0.f, 0.f, 0.f};
#pragma unroll
  for (int e = 0; e < 16; ++e) { oa[e] = 0.f; ob[e] = 0.f; sa[e] = 0.f; sb[e] = 0.f; }
  asm volatile("" : "+v"(sidx), "+v"(sel), "+v"(oa), "+v"(ob), "+v"(la), "+v"(lb), "+v"(qaa), "+v"(qab), "+v"(arr_w), "+v"(arr_i));
  PIN();
  s16x8 qa0, qa1, qb0, qb1, k00, k01, v00, v01, v10, v11, k10, k11;
  asm volatile("" : "=v"(qa0), "=v"(qa1), "=v"(qb0), "=v"(qb1));
  u32x4 fw;
  auto first = [&]() {
    fw = lds_rd4(lbase + FLG + 16);
    asm volatile("" ::: "memory");
    k00 = rd(sl0, 0); k01 = rd(sl0, 1024);
    if (!far) { qa0 = *LDS_AT(const s16x8, qaa); qb0 = *LDS_AT(const s16x8, qab); qa1 = *LDS_AT(const s16x8, qaa + 1024); qb1 = *LDS_AT(const s16x8, qab + 1024); }
    v00 = rd(sl0, 2048); v01 = rd(sl0, 3072);
    PIN();
  };
  if (far) {
    ArgP ap_ = ap; asm volatile("" : "+s"(ap_));
    const __amdgpu_buffer_rsrc_t rq = __builtin_amdgcn_make_buffer_rsrc(reinterpret_cast<char*>(ap_->kv) + QG + (((LAY2 ? 256 : 0) + (tmi ^ 1) * 16 + tni) * 2 + qg) * 4096, 0, 4096, 0x00020000);
    const int o = kpos(lane) * 16 + (g ? 0 : 2048);
    __builtin_amdgcn_s_sleep(QPRE);
#pragma unroll 1
    for (int i = 0; i < SPIN_MAX; ++i) {
      const u32x4 a0 = __builtin_amdgcn_raw_buffer_load_b128(rq, o, 0, 16), b0 = __builtin_amdgcn_raw_buffer_load_b128(rq, o ^ 2048, 0, 16);
      const u32x4 a1 = __builtin_amdgcn_raw_buffer_load_b128(rq, o + 1024, 0, 16), b1 = __builtin_amdgcn_raw_buffer_load_b128(rq, (o ^ 2048) + 1024, 0, 16);
      qa0 = __builtin_bit_cast(s16x8, a0); qb0 = __builtin_bit_cast(s16x8, b0); qa1 = __builtin_bit_cast(s16x8, a1); qb1 = __builtin_bit_cast(s16x8, b1);
      unsigned mx = 0u;
#pragma unroll
      for (int e = 0; e < 4; ++e) mx = umax(umax(umax(mx, a0[e]), b0[e]), umax(a1[e], b1[e]));
      if (none(mx == SENT)) break;
      __builtin_amdgcn_s_sleep(QSLP);
      asm volatile("" ::: "memory");
    }
  }
  first();
  if (__builtin_amdgcn_readfirstlane(fw[0]) < 4u) {
#pragma unroll 1
    for (int i = 0; i < SPIN_MAX; ++i) {
      if (__builtin_amdgcn_readfirstlane(*LDS_AT(const volatile unsigned, lbase + FLG + 16)) >= 4u) break;
      __builtin_amdgcn_s_sleep(ASLP);
    }
    asm volatile("" ::: "memory");
    first();
  }
  const unsigned tag = __builtin_amdgcn_readfirstlane(fw[1]);
  const int idx = LAY2 ? (2047 - (int)(__builtin_amdgcn_readfirstlane(fw[3]) & 0xffffu)) & 2047 : -64;
  float ma;
  const float zoff = __uint_as_float(__builtin_amdgcn_readfirstlane(LAY2 && g == ((idx >> 6) & 1) ? 0u : 0xff800000u));
  {
    sa = MFMA32(k00, qa0, sa); sb = MFMA32(k00, qb0, sb);
    sa = MFMA32(k01, qa1, sa); sb = MFMA32(k01, qb1, sb);
    float mb = sb[0];
    ma = sa[0];
#pragma unroll
    for (int e = 1; e < 16; ++e) { ma = fmaxf(ma, sa[e]); mb = fmaxf(mb, sb[e]); }
    ma = fmaxf(ma, mb);
    const auto sw = __builtin_amdgcn_permlane32_swap(__float_as_uint(ma), __float_as_uint(ma), false, false);
    ma = fmaxf(__uint_as_float(sw[0]), __uint_as_float(sw[1]));
    f32x2 n2 = {-ma, -ma};
    asm volatile("" : "+v"(n2));
#pragma unroll
    for (int e = 0; e < 16; ++e) nn[e] = n2[e & 1];
    sa = MFMA32(k00, qa0, nn); sa = MFMA32(k01, qa1, sa);
    la[0] = lb[0] = -__uint_as_float(__float_as_uint(__builtin_amdgcn_exp2f(zoff - ma)) & 0xffff0000u);
  }
#define SB() __builtin_amdgcn_sched_barrier(0)
#define EX(i) sc[i] = __builtin_amdgcn_exp2f(sc[i])
  auto unit = [&](f32x16& sc, f32x16& sn, const s16x8& kn0, const s16x8& kn1, const s16x8& qn0, const s16x8& qn1,
                  f32x16& op, f32x4& lp, const s16x8& vp,
                  f32x16& oc, f32x4& lc, const s16x8& vc, auto&& mid) {
    lp = SMF16(sel, PHI(sn), lp); SB();
    op = SMF32(vp, PHI(sn), op); SB();
    EX(0); EX(1); EX(2); EX(3); SB();
    sn = MFMA32(kn0, qn0, nn); SB();
    EX(4); EX(5); EX(6); EX(7); SB();
    sn = MFMA32(kn1, qn1, sn); SB();
    mid();
    EX(8); EX(9); EX(10); EX(11); SB();
    lc = SMF16(sel, PLO(sc), lc); SB();
    oc = SMF32(vc, PLO(sc), oc); SB();
    EX(12); EX(13); EX(14); EX(15); SB();
    asm volatile("" : "+v"(sc));
  };
  sl0 = sptr(1);
  unsigned f0 = 0u;
  auto check = [&](unsigned need) {
    if (__builtin_expect(__builtin_amdgcn_readfirstlane(f0) < need, 0)) {
#pragma unroll 1
      for (int i = 0; i < SPIN_MAX; ++i) {
        if (__builtin_amdgcn_readfirstlane(*LDS_AT(const volatile unsigned, lbase + FLG)) >= need) break;
        __builtin_amdgcn_s_sleep(1);
      }
    }
    asm volatile("" ::: "memory");
  };
#define PAT 0x22221110
#define SAW8(i) do { if (((PAT >> (28 - 4 * (i))) & 3) != ((PAT >> ((32 - 4 * (i)) & 31)) & 3)) { __builtin_amdgcn_s_setprio((PAT >> (28 - 4 * (i))) & 3); SB(); } } while (0)
#define SAW(i) do { if (odd) SAW8(4 + (i)); else SAW8(i); } while (0)
  auto three = [&](int q, bool odd) __attribute__((always_inline)) {
    sl0 = sptr((2 * q + 1) & 7);
    SAW(0);
    unit(sa, sb, k00, k01, qb0, qb1, ob, lb, v11, oa, la, v00, [&]() { k10 = rd(sl0, 0); k11 = rd(sl0, 1024); SB(); });
    SAW(1);
    unit(sb, sa, k10, k11, qa0, qa1, oa, la, v01, ob, lb, v00, [&]() { v10 = rd(sl0, 2048); v11 = rd(sl0, 3072); SB(); });
    sl0 = sptr((2 * q + 2) & 7);
    SAW(2);
    unit(sa, sb, k10, k11, qb0, qb1, ob, lb, v01, oa, la, v10, [&]() { k00 = rd(sl0, 0); k01 = rd(sl0, 1024); SB(); });
  };
  auto fourth = [&](int st, int q, bool odd) __attribute__((always_inline)) {
    sl0 = sptr((2 * q + 2) & 7);
    SAW(3);
    unit(sb, sa, k00, k01, qa0, qa1, oa, la, v11, ob, lb, v10, [&]() {
      v00 = rd(sl0, 2048); v01 = rd(sl0, 3072);
      f0 = *LDS_AT(const volatile unsigned, lbase + FLG);
      SB();
      arrive_w();
      SB(); });
    check(st >= 14 ? 32u : 2u * st + 5u);
    SB();
  };
  SB();
  SAW8(0);
  {
    f32x16& sc = sa;
    sb = MFMA32(k00, qb0, nn); SB();
    k10 = rd(sl0, 0); k11 = rd(sl0, 1024); SB();
    sb = MFMA32(k01, qb1, sb); SB();
    EX(0); EX(1); EX(2); EX(3); EX(4); EX(5); EX(6); EX(7); EX(8); SB();
    oa = SMF32(v00, PLO(sa), oa); SB();
    EX(9); EX(10); EX(11); SB();
    la = SMF16(sel, PLO(sa), la); SB();
    EX(12); EX(13); EX(14); EX(15); SB();
    asm volatile("" : "+v"(sa));
  }
  auto given = [&]() {
    if (!far && g != hh) {
      asm volatile("s_waitcnt vmcnt(0)" ::: "memory");
      __hip_atomic_fetch_add(LDS_AT(unsigned, arr_w + 24), 256u, __ATOMIC_RELAXED, __HIP_MEMORY_SCOPE_WORKGROUP);
    }
    SB();
  };
  given();
  SAW8(1);
  unit(sb, sa, k10, k11, qa0, qa1, oa, la, v01, ob, lb, v00, [&]() { v10 = rd(sl0, 2048); v11 = rd(sl0, 3072); SB(); });
  sl0 = sptr(2);
  SB();
  SAW8(2);
  unit(sa, sb, k10, k11, qb0, qb1, ob, lb, v01, oa, la, v10, [&]() {});
  {
    f32x16& sc = sb;
    SAW8(3);
    la = SMF16(sel, PHI(sa), la); SB();
    oa = SMF32(v11, PHI(sa), oa); SB();
    EX(0); EX(1); EX(2); EX(3); EX(4); EX(5); EX(6); EX(7); EX(8); asm volatile("" : "+v"(sc)); SB();
    lb = SMF16(sel, PLO(sb), lb); SB();
    ob = SMF32(v10, PLO(sb), ob); SB();
    f0 = *LDS_AT(const volatile unsigned, lbase + FLG);
    asm volatile("" ::: "memory");
    k00 = rd(sl0, 0); k01 = rd(sl0, 1024); SB();
    EX(9); EX(10); EX(11); EX(12); EX(13); EX(14); EX(15); asm volatile("" : "+v"(sc)); SB();
    if (__builtin_amdgcn_readfirstlane(f0) < 3u) { check(3u); k00 = rd(sl0, 0); k01 = rd(sl0, 1024); }
    SB();
    v00 = rd(sl0, 2048); v01 = rd(sl0, 3072); SB(); arrive_w(); SB();
    sa = MFMA32(k00, qa0, nn); sa = MFMA32(k01, qa1, sa); asm volatile("" : "+v"(sa)); SB();
  }
  sl0 = sptr(3);
  SB();
  SAW8(4);
  unit(sa, sb, k00, k01, qb0, qb1, ob, lb, v11, oa, la, v00, [&]() {
    f0 = *LDS_AT(const volatile unsigned, lbase + FLG);
    asm volatile("" ::: "memory");
    k10 = rd(sl0, 0); k11 = rd(sl0, 1024); SB(); });
  if (__builtin_amdgcn_readfirstlane(f0) < 4u) { check(4u); k10 = rd(sl0, 0); k11 = rd(sl0, 1024); }
  SB();
  SAW8(5);
  unit(sb, sa, k10, k11, qa0, qa1, oa, la, v01, ob, lb, v00, [&]() { v10 = rd(sl0, 2048); v11 = rd(sl0, 3072); f0 = *LDS_AT(const volatile unsigned, lbase + FLG); SB(); });
  check(5u);
  sl0 = sptr(4);
  SB();
  SAW8(6);
  unit(sa, sb, k10, k11, qb0, qb1, ob, lb, v01, oa, la, v10, [&]() { k00 = rd(sl0, 0); k01 = rd(sl0, 1024); SB(); });
#pragma unroll 1
  for (int st = 2; st < 14; st += 4) {
    fourth(st - 1, 1, true); three(2, false); fourth(st, 2, false); three(3, true);
    fourth(st + 1, 3, true); three(0, false); fourth(st + 2, 0, false); three(1, true);
  }
  fourth(13, 1, true); three(2, false); fourth(14, 2, false); three(3, true);
  SAW8(7);
  unsigned vr, tk, tv;
  unsigned f4;
  auto table = [&]() {
    f4 = *LDS_AT(const volatile unsigned, lbase + FLG + 4);
    k00 = *LDS_AT(const volatile s16x8, tk); k01 = *LDS_AT(const volatile s16x8, tk + 32);
    vr = *LDS_AT(const volatile u16, tv);
  };
  unsigned xw, xm;
  u32x2 ml;
  u32x4 o2[4];
  auto take = [&]() {
    ml = *LDS_AT(const volatile u32x2, xm);
#pragma unroll
    for (int e = 0; e < 4; ++e) o2[e] = lds_rd4(xw + e * 1024);
  };
  float pzt;
  int dso;
  f32x16& sc = sb;
#define EXP() do { asm volatile("" : "+v"(sc)); SB(); } while (0)
  oa = SMF32(v11, PHI(sa), oa); SB();
  tk = lbase + TAB + 128 + hh * 64 + (lane >> 5) * 16; tv = lbase + TAB + 256 + hh * 64 + (lane & 31) * 2;
  table();
  SB();
  EX(0); EX(1); EXP();
  la = SMF16(sel, PHI(sa), la); SB();
  EX(2); EX(3); EX(4); EXP();
  xw = wbase + (qg ? QOFF : 4 * ASB) + far * 8192; xm = lbase + XML + wid * 512 + lane * 8;
  pzt = __builtin_amdgcn_exp2f(nn[0] + (zoff - 8.f));
  asm volatile("" : "+v"(xw), "+v"(xm), "+v"(pzt), "+v"(f4));
  SB();
  {
    const unsigned ph = LAY2 ? 0x79797979u : 0x80808080u;
    auto ready = [&]() { return ((__builtin_amdgcn_readfirstlane(f4) + ph) & 0x80808080u) == 0x80808080u; };
    if (__builtin_expect(!ready(), 0)) {
#pragma unroll 1
      for (int i = 0; i < SPIN_MAX; ++i) {
        __builtin_amdgcn_s_sleep(1);
        table();
        if (ready()) break;
      }
    }
    asm volatile("" ::: "memory");
  }
  SB();
  sa = MFMA32(k00, qb0, nn); asm volatile("" : "+v"(sa)); SB();
#pragma unroll
  for (int e = 0; e < 4; ++e) *LDS_AT(volatile f32x4, xw + e * 1024) = f32x4{oa[4 * e], oa[4 * e + 1], oa[4 * e + 2], oa[4 * e + 3]};
  *LDS_AT(volatile u32x2, xm) = u32x2{__float_as_uint(ma), f2word(pzt <= la[0] ? la[0] : 0.f)};
  SB();
  EX(5); EX(6); EX(7); EXP();
  sa = MFMA32(k01, qb1, sa); asm volatile("" : "+v"(sa), "+v"(vr)); SB();
  ARGS_HERE;
  EX(8); EX(9); EX(10); EXP();
  ob = SMF32(v10, PLO(sb), ob); SB();
  EX(11); EX(12); EX(13); EXP();
  lb = SMF16(sel, PLO(sb), lb); SB();
  const bool used = !LAY2 || (qt + g) * 32 + (lane & 31) != idx;
  dso = used ? (((qt + g) * 32 + head) * 1024 + lane * 8) * 2 : S_ * D_ * 2;
  xw += g ? -4096 : 4096; xm += g ? -1024 : 1024;
  asm volatile("" : "+v"(dso), "+v"(xw), "+v"(xm));
  SB();
  EX(14); EX(15); EXP();
  take();
  SB();
  lb = SMF16(sel, PHI(sb), lb); SB();
  ob = SMF32(v11, PHI(sb), ob); SB();
  __builtin_amdgcn_s_setprio(EPRIO);
  TE(0, 0); TE(1, 0); TE(2, 0); TE(3, 0); TE(7, 0);
  float lsb = lb[0];
  if (LAY2) {
    sa[0] = __builtin_amdgcn_exp2f(sa[0]);
    ob = SMF32((u32x4{lane < 32 ? vr : 0u, 0u, 0u, 0u}), PLO(sa), ob);
    lsb += __uint_as_float(__float_as_uint(sa[0]) & 0xffff0000u);
    lsb = pzt <= lsb ? lsb : 0.f;
  }
  const __amdgpu_buffer_rsrc_t rso = __builtin_amdgcn_make_buffer_rsrc(Of, 0, S_ * D_ * 2, 0x00020000);
  TE(0, 1);
  if (__builtin_amdgcn_ballot_w64(ml[1] == SENT) != 0ull) {
#pragma unroll 1
    for (int i = 0; i < SPIN_MAX; ++i) {
      __builtin_amdgcn_s_sleep(XSLP);
      if (!__builtin_amdgcn_ballot_w64(*LDS_AT(const volatile unsigned, xm + 4) == SENT)) break;
    }
    asm volatile("s_waitcnt lgkmcnt(0)" ::: "memory");
    take();
  }
  arrive();
  TE(0, 2);
  TE(0, 3); TE(1, 1);
  {
    const float m2 = __uint_as_float(ml[0]), l2 = __uint_as_float(ml[1]);
    const float mx = fmaxf(ma, m2);
    const float f1 = __builtin_amdgcn_exp2f(ma - mx), f2 = __builtin_amdgcn_exp2f(m2 - mx);
    const float inv = frcp(lsb * f1 + l2 * f2);
    const float c1 = f1 * inv, c2 = f2 * inv;
    f32x2 r[8];
#pragma unroll
    for (int e = 0; e < 8; ++e) {
      const unsigned x0 = o2[e >> 1][(e & 1) * 2], x1 = o2[e >> 1][(e & 1) * 2 + 1];
      r[e] = f32x2{__uint_as_float(x0) * c2, __uint_as_float(x1) * c2};
    }
#pragma unroll
    for (int e = 0; e < 8; ++e) r[e] = f32x2{ob[2 * e], ob[2 * e + 1]} * f32x2{c1, c1} + r[e];
    auto put = [&]() {
#pragma unroll
      for (int sx = 0; sx < 2; ++sx)
        __builtin_amdgcn_raw_buffer_store_b128(u32x4{pack2(r[4 * sx][0], r[4 * sx][1]), pack2(r[4 * sx + 1][0], r[4 * sx + 1][1]),
            pack2(r[4 * sx + 2][0], r[4 * sx + 2][1]), pack2(r[4 * sx + 3][0], r[4 * sx + 3][1])}, rso, dso + sx * 1024, 0, 16);
    };
    put();
    PIN();
    __builtin_amdgcn_s_setprio(0);
    PIN();
    f32x2 chk = {lsb - __builtin_amdgcn_fmed3f(lsb, 8.6736174e-19f, 1.2676506e30f), l2 - __builtin_amdgcn_fmed3f(l2, 8.6736174e-19f, 1.2676506e30f)};
#pragma unroll
    for (int e = 0; e < 8; ++e) chk = r[e] * f32x2{0.f, 0.f} + chk;
    if (__builtin_expect(__builtin_amdgcn_ballot_w64(used && !(chk[0] + chk[1] == 0.f)) != 0ull, 0)) {
      float l;
      attn_safe(KV, head * 64, qb0, qb1, ob, l, LAY2 ? head * 64 + (idx >> 5) : -1, idx & 31, lbase + TAB + 128 + hh * 64);
      const float inv2 = 1.0f / l;
#pragma unroll
      for (int e = 0; e < 8; ++e) r[e] = f32x2{ob[2 * e] * inv2, ob[2 * e + 1] * inv2};
      asm volatile("s_waitcnt vmcnt(0)" ::: "memory");
      put();
    }
    unsigned pp = 0u;
    if (LAY2) {
      const unsigned ua = lbase + UGL + hh * 128 + (lane >> 5) * 16;
      f32x2 du = {0.f, 0.f}, dn = {0.f, 0.f};
#pragma unroll
      for (int q = 0; q < 4; ++q) {
        const f32x4 uu = *LDS_AT(const f32x4, ua + q * 32), gg = *LDS_AT(const f32x4, ua + 256 + q * 32);
        du = r[2 * q] * f32x2{uu[0], uu[1]} + du; du = r[2 * q + 1] * f32x2{uu[2], uu[3]} + du;
        const f32x2 t0 = r[2 * q] * f32x2{gg[0], gg[1]}, t1 = r[2 * q + 1] * f32x2{gg[2], gg[3]};
        dn = t0 * t0 + dn; dn = t1 * t1 + dn;
      }
      const auto a = __builtin_amdgcn_permlane32_swap(__float_as_uint(du[0] + du[1]), __float_as_uint(du[0] + du[1]), false, false);
      const auto c = __builtin_amdgcn_permlane32_swap(__float_as_uint(dn[0] + dn[1]), __float_as_uint(dn[0] + dn[1]), false, false);
      pp = f2word(__uint_as_float(pack2(__uint_as_float(a[0]) + __uint_as_float(a[1]), __uint_as_float(c[0]) + __uint_as_float(c[1]))));
    }
    __builtin_amdgcn_raw_buffer_store_b32(pp, __builtin_amdgcn_make_buffer_rsrc(hand + 2 * HAND_N + PP_O, 0, LAY2 ? H_ * S_ * 4 : 0, 0x00020000),
                                          lane < 32 ? (head * S_ + (qt + g) * 32 + lane) * 4 : H_ * S_ * 4, 0, 16);
    PIN();
  }
  TE(1, 2);
  u32x4 xres[2];
  oproj_tail(LAY2, tid, lbase, lbase + FLG, lbase + SPM + 144, omi, oni, idx, gran + GR_O + ((LAY2 ? 16 : 0) + (tmi ^ far)) * (64 * RO) + head,
             gran + GR_X + omi * (128 * RX) + oni * 4, tag, Of, xout, A, w_r, stats_out, stats, hand, xres TEPASS);
  if (LAY2) {
    const int row = omi * 128 + (wid & 3) * 32 + (wid >> 2) * 16 + (lane >> 3), col = oni * 64 + (lane & 7) * 8;
    unsigned md[2], best = 0u;
    const __amdgpu_buffer_rsrc_t rsk = __builtin_amdgcn_make_buffer_rsrc(hand + HAND_N, 0, (HAND_N + PS_N) * 4, 0x00020000);
    f32x2 xf[2][4];
#pragma unroll
    for (int p = 0; p < 2; ++p)
#pragma unroll
      for (int e = 0; e < 4; ++e) {
        xf[p][e] = f32x2{__uint_as_float(xres[p][e] << 16), __uint_as_float(xres[p][e] & 0xffff0000u)};
        asm volatile("" : "+v"(xf[p][e]));
      }
    LOGDEF;
    LOG();
    const bool rtr = (oni & 7) == 7;
    {
      const int rl = (wid & 3) * 32 + (wid >> 2) * 16 + (lane & 15), rr = omi * 128 + rl;
      int src = (HAND_N + ((omi * 16 + (lane >> 4) * 4) * 128 + rl) * 4) * 4;
      int ko = rtr && (wid >> 2) == (oni >> 3) && lane < NREP ? lane * 1024 + (rr >> 4) * 8 : (HAND_N + PS_N) * 4;
      int bp = (lane >> 3) * 4;
      const float wsum = *LDS_AT(const volatile float, lbase + SPM + 132);
      asm volatile("" : "+v"(src), "+v"(ko), "+v"(bp));
      u32x4 v[4];
      __builtin_amdgcn_s_sleep(MPRE);
#pragma unroll 1
      for (int spin = 0; spin < SPIN_MAX; ++spin) {
        LOG();
#pragma unroll
        for (int i = 0; i < 4; ++i) v[i] = __builtin_amdgcn_raw_buffer_load_b128(rsk, src + (i & 1) * 2048, (i >> 1) * 4096, 16);
        unsigned mx = 0u;
#pragma unroll
        for (int i = 0; i < 4; ++i) mx = umax(umax(umax(mx, v[i][0]), v[i][1]), v[i][2]);
        if (none(mx == SENT)) break;
        __builtin_amdgcn_s_sleep(MSLP);
        asm volatile("" ::: "memory");
      }
      LOGAT(12);
      if (rtr) __builtin_amdgcn_s_setprio(3); else __builtin_amdgcn_s_setprio(LNPRIO);
      double sm[3];
#pragma unroll
      for (int c = 0; c < 3; ++c) {
        const double a = ((double)__uint_as_float(v[0][c]) + (double)__uint_as_float(v[1][c])) + ((double)__uint_as_float(v[2][c]) + (double)__uint_as_float(v[3][c]));
        u32x2 q = __builtin_bit_cast(u32x2, a);
        const auto lo = __builtin_amdgcn_permlane32_swap(q[0], q[0], false, false), hi = __builtin_amdgcn_permlane32_swap(q[1], q[1], false, false);
        q = __builtin_bit_cast(u32x2, __builtin_bit_cast(double, u32x2{lo[0], hi[0]}) + __builtin_bit_cast(double, u32x2{lo[1], hi[1]}));
        const auto l2 = __builtin_amdgcn_permlane16_swap(q[0], q[0], false, false), h2 = __builtin_amdgcn_permlane16_swap(q[1], q[1], false, false);
        sm[c] = __builtin_bit_cast(double, u32x2{l2[0], h2[0]}) + __builtin_bit_cast(double, u32x2{l2[1], h2[1]});
      }
      float mean, var, mb, den;
      ln_from_sums(sm[0], sm[1], mean, var, mb, den);
      const unsigned mdr = f2word(__uint_as_float((__float_as_uint(mb) >> 16) | (__float_as_uint(den) & 0xffff0000u)));
      md[0] = __builtin_amdgcn_ds_bpermute(bp, mdr); md[1] = __builtin_amdgcn_ds_bpermute(bp + 32, mdr);
      if (rtr) {
        const float r = (float)fma(-(double)mb, (double)wsum, sm[2]) * frcp(den);
        const unsigned rb = __float_as_uint(r) + 0x8000u;
        unsigned rkey = (rb ^ ((unsigned)((int)rb >> 31) | 0x80000000u)) & 0xfffff800u;
        if (r != r) rkey = 0u;
        const unsigned top = wave_umax(rkey | (unsigned)(2047 - rr));
        const unsigned mdt = __builtin_amdgcn_readlane(mdr, (2047 - (int)(top & 0xffffu)) & 15);
        __builtin_amdgcn_raw_buffer_store_b64(u32x2{top, mdt}, rsk, ko, 0, 16);
        PIN();
        __builtin_amdgcn_s_setprio(LNPRIO);
        __hip_atomic_fetch_add(LDS_AT(unsigned, lane == 0 ? lbase + SPM + 128 : lbase + DUM + lane * 4), 1u, __ATOMIC_RELAXED, __HIP_MEMORY_SCOPE_WORKGROUP);
        if (wid == 2 || wid == 5) STAMP(8, 6);
      }
      LOGAT(13);
    }
    auto keys = [&](const u32x4& v) __attribute__((always_inline)) {
      if (none(umax(umax(v[0], v[1]), umax(v[2], v[3])) == SENT)) {
        best = wave_umax(umax(v[0], v[2])) | 0x8000u;
        *LDS_AT(volatile u32x2, lbase + FLG + 40) = u32x2{best, (unsigned)__builtin_amdgcn_readlane(v[0] > v[2] ? v[1] : v[3], ((2047 - (int)(best & 0x7ffu)) >> 5) & 63)};
        STAMP(8, 11);
      }
    };
    auto ln_rows = [&]() __attribute__((always_inline)) {
#pragma unroll
      for (int p = 0; p < 2; ++p) {
        const float mb_ = __uint_as_float(md[p] << 16), rin = frcp(__uint_as_float(md[p] & 0xffff0000u));
#pragma unroll
        for (int e = 0; e < 4; ++e) {
          const f32x2 h = (xf[p][e] - f32x2{mb_, mb_}) * f32x2{rin, rin} + xf[p][e];
          xres[p][e] = pack2(h[0], h[1]);
        }
      }
      asm volatile("" : "+v"(xres[0]), "+v"(xres[1]));
    };
    if (wid == 0) STAMP(8, 12);
    ln_rows();
    LOGAT(14);
    const __amdgpu_buffer_rsrc_t rsu = __builtin_amdgcn_make_buffer_rsrc(out, 0, S_ * D_ * 2, 0x00020000);
    unsigned fr1 = SENT;
#pragma unroll 1
    for (int i = 0; i < 4 * SPIN_MAX && fr1 == SENT; ++i) fr1 = __builtin_amdgcn_readfirstlane(*LDS_AT(const volatile unsigned, lbase + SPM + 144));
    const int fr2 = (fr1 >> 16) & 0x7ffu;
    fr1 &= 0x7ffu;
    unsigned tmx = __builtin_amdgcn_readfirstlane(*LDS_AT(const volatile unsigned, lbase + SPM + 136)) + KDT;
    if (tmx - (unsigned)__builtin_amdgcn_s_memrealtime() > 1024u) tmx = 0u;
    auto due = [&]() { return tmx == 0u || (int)((unsigned)__builtin_amdgcn_s_memrealtime() - tmx) >= 0; };
    lk_set(15);
    const bool held = !none((row | 8) == ((int)fr1 | 8) || (row | 8) == (fr2 | 8));
#pragma unroll
    for (int p = 0; p < 2; ++p) __builtin_amdgcn_raw_buffer_store_b128(xres[p], rsu, row + 8 * p != (int)fr1 && row + 8 * p != fr2 ? ((row + 8 * p) * D_ + col) * 2 : S_ * D_ * 2, 0, OAUX);
    PIN();
    unsigned pk = SENT;
    auto pick = [&]() __attribute__((always_inline)) {
#pragma unroll 1
      for (int i = 0; i < 4 * SPIN_MAX && pk == SENT; ++i) pk = __builtin_amdgcn_readfirstlane(*LDS_AT(const volatile unsigned, lbase + SPM + 140));
    };
    auto put_one = [&](int r) __attribute__((always_inline)) {
#pragma unroll
      for (int p = 0; p < 2; ++p) __builtin_amdgcn_raw_buffer_store_b128(xres[p], rsu, row + 8 * p == r ? (r * D_ + col) * 2 : S_ * D_ * 2, 0, OAUX);
    };
    if (held) { pick(); put_one(pk == fr1 ? (fr2 != (int)fr1 ? fr2 : -1) : (int)fr1); }
    if (wid == 6) {
      if (rtr) {
#pragma unroll 1
        for (int i = 0; i < SPIN_MAX; ++i) {
          if (__builtin_amdgcn_readfirstlane(*LDS_AT(const volatile unsigned, lbase + SPM + 128)) == 7u) break;
          __builtin_amdgcn_s_sleep(1);
        }
        asm volatile("" ::: "memory");
      }
      while (!due()) __builtin_amdgcn_s_sleep(1);
    }
#pragma unroll 1
    for (int i = 0; i < 4 * SPIN_MAX && !best; ++i) {
      if (wid == 6) {
        LOG();
        keys(__builtin_amdgcn_raw_buffer_load_b128(rsk, (bid % NREP) * 1024 + lane * 16, 0, 16));
        if (!best) __builtin_amdgcn_s_sleep(KSLP);
      } else {
        best = __builtin_amdgcn_readfirstlane(*LDS_AT(const volatile unsigned, lbase + FLG + 40));
        if (!best) __builtin_amdgcn_s_sleep(1);
      }
      asm volatile("" ::: "memory");
    }
    const int idx2 = 2047 - (int)(best & 0x7ffu);
    if (__builtin_expect(idx2 != (int)fr1 || held, 0)) {
      pick();
      if (idx2 != (int)pk) {
        if (!none((row | 8) == (idx2 | 8))) {
#pragma unroll
          for (int p = 0; p < 2; ++p)
            __builtin_amdgcn_raw_buffer_store_b128(u32x4{pack2(xf[p][0][0], xf[p][0][1]), pack2(xf[p][1][0], xf[p][1][1]), pack2(xf[p][2][0], xf[p][2][1]), pack2(xf[p][3][0], xf[p][3][1])},
                __builtin_amdgcn_make_buffer_rsrc(xout, 0, 3 * D_ * 2, 0x00020000), row + 8 * p == idx2 ? (2 * D_ + col) * 2 : S_ * D_ * 2, 0, 16);
          asm volatile("s_waitcnt vmcnt(0)" ::: "memory");
          if (lane == 0) st_ag(gran + GR_F + oni * 32, tag);
        }
        if (!none((row | 8) == ((int)pk | 8))) {
#pragma unroll 1
          for (int spin = 0; spin < SPIN_MAX; ++spin) {
            if (none(ld_ag(gran + GR_F + (16 + oni * 2 + (lane & 1)) * 32) != tag)) break;
            asm volatile("" ::: "memory");
          }
          put_one(pk);
        }
      }
    }
  }
  }
}
struct Args2 { Args l[2]; };
extern "C" __global__ __launch_bounds__(768, 3) void layers_kernel(const Args2 args) {
  __shared__ __attribute__((aligned(128))) char smem[163840];
  const unsigned long long t_entry = 0;
  const int tid = threadIdx.x, bid = blockIdx.x;
  int wid = __builtin_amdgcn_readfirstlane(tid >> 6);
  asm volatile("" : "+s"(wid));
  const ArgP ap = (ArgP)__builtin_amdgcn_kernarg_segment_ptr();
  layer_body(wid, bid, (unsigned)(uintptr_t)smem, ap, t_entry);
  LAYER_HERE;
  if (!LAY2) {
    asm volatile(AGAIN :: "{s[0:1]}"(ap + 1), "{s2}"(bid), "{v0}"(wid * 64) : "s90", "s91", "scc", "memory");
    __builtin_unreachable();
  }
}

extern "C" __global__ void end_marker_kernel() {}

extern "C" void kernel_launch(void* const* d_in, const int* in_sizes, int n_in, void* d_out, int out_size,
                              void* d_ws, size_t ws_size, hipStream_t stream) {
  const u16* x0 = (const u16*)d_in[0];
  const u16* wqkv = (const u16*)d_in[1];
  const u16* wo = (const u16*)d_in[2];
  const u16* w_r = (const u16*)d_in[4];
  const u16* wg = (const u16*)d_in[5];
  const u16* wu = (const u16*)d_in[6];
  const u16* wd = (const u16*)d_in[7];
  u16* out = (u16*)d_out;

  char* ws = (char*)d_ws;
  const size_t MB4 = (size_t)S_ * D_ * 2;
  u16* kv = (u16*)(ws);
  u16* o = (u16*)(ws + 2 * MB4);
  u16* x1 = (u16*)(ws + 3 * MB4);
  unsigned* gran = (unsigned*)(ws + 4 * MB4);
  float* csf = (float*)(gran + GR_F + 48 * 32);
  double* stats = (double*)(csf + 8192);
  unsigned* hand = (unsigned*)(stats + 2 * STAT_N);
  static_assert((GR_F + 48 * 32) % 32 == 0 && (size_t)(GR_F + 48 * 32 + 8192 + 4 * STAT_N + 2 * HAND_N + PS_N) * 4 <= 6u << 20, "");
  u16* x3 = (u16*)(hand + 2 * HAND_N) - 3 * D_;
  u16* wc = (u16*)((((uintptr_t)ws + 4 * MB4 + (6u << 20) + 2047) & ~(uintptr_t)2047) + (((uintptr_t)wqkv + 128) & 2047));
  Args2 a;
  for (int l = 0; l < 2; ++l) {
    Args& r = a.l[l];
    r.lay = l; r.a = l ? x1 : x0; r.kv = kv; r.o = o; r.xo = l ? x3 : x1; r.so = l ? stats + STAT_N : stats;
    r.W = wqkv; r.wc = wc; r.wg = wg; r.wu = wu; r.wd = wd; r.wo = wo; r.w_r = w_r; r.gran = gran; r.csf = csf; r.stats = stats; r.hand = hand; r.out = out;
  }
  layers_kernel<<<256, 768, 0, stream>>>(a);
}
```
